```python
import torch

def _softmax(x, axis=-1):
    return torch.softmax(x, dim=axis)
def _gelu(x):
    return torch.nn.functional.gelu(x, approximate="tanh")
def _silu(x):
    return torch.nn.functional.silu(x)
def _sigmoid(x):
    return torch.sigmoid(x)
def _layernorm(x, axis=-1, eps=1e-5):
    m = x.mean(dim=axis, keepdim=True)
    v = x.var(dim=axis, keepdim=True, unbiased=False)
    return (x - m) / torch.sqrt(v + eps)
def _scatter_add(tgt, idx, src, axis=0):
    return tgt.clone().index_add_(axis, idx, src)
def _segment_sum(src, seg, n):
    out = torch.zeros((n,) + src.shape[1:], dtype=src.dtype)
    return out.index_add_(0, seg, src)
def _pad(x, pw):
    flat = tuple(v for pair in reversed(pw) for v in pair)
    return torch.nn.functional.pad(x, flat)

kernel_name = 'concat_heads__ssm_conv_h3__compressed_kv_cla__prompt'

def setup_inputs():

    _w = (lambda a: (lambda t: t.to(torch.bfloat16) if t.is_floating_point() else t)(torch.as_tensor(a)))
    def _A_setup():
        B, S, D = 1, 2048, 448
        ks = 4
        return {
            "q": _w((lambda *s: torch.randn(*s, dtype=torch.bfloat16) * 0.05)(B, S, D)),
            "k": _w((lambda *s: torch.randn(*s, dtype=torch.bfloat16) * 0.1)(B, S, D)),
            "v": _w((lambda *s: torch.randn(*s, dtype=torch.bfloat16) * 0.1)(B, S, D)),
            "shift_k": _w((lambda *s: torch.randn(*s, dtype=torch.bfloat16) * 0.1)(D, ks) * 0.5),
            "ssm_k": _w((lambda *s: torch.randn(*s, dtype=torch.bfloat16) * 0.1)(D, S) * (1.0 / S ** 0.5)),
            "ssm_D": _w((lambda *s: torch.randn(*s, dtype=torch.bfloat16) * 0.1)(D)),
        }
    _A_in = _A_setup()

    def _B_setup():
        B, H, Sq, Sk, D, M = 1, 16, 2048, 2048, 256, 448
        Dc = 4 * D
        return {
            "x": _w((lambda *s: torch.randn(*s, dtype=torch.bfloat16) * 3.2)(B, Sq, M)),
            "c_kv": _w((lambda *s: torch.randn(*s, dtype=torch.bfloat16) * 0.05)(B, Sk, Dc)),
            "w_q": _w((lambda *s: torch.randn(*s, dtype=torch.bfloat16) * 3.2)(M, H, D) * (M ** -0.5)),
            "w_uk": _w((lambda *s: torch.randn(*s, dtype=torch.bfloat16) * 3.2)(Dc, H, D) * (Dc ** -0.5)),
            "w_uv": _w((lambda *s: torch.randn(*s, dtype=torch.bfloat16) * 0.05)(Dc, H, D) * (Dc ** -0.5)),
            "w_o": _w((lambda *s: torch.randn(*s, dtype=torch.bfloat16) * 0.05)(H, D, M) * ((H * D) ** -0.5)),
        }
    _B_in = _B_setup()

    return {
        "A_q": _A_in["q"],
        "A_k": _A_in["k"],
        "A_v": _A_in["v"],
        "A_shift_k": _A_in["shift_k"],
        "A_ssm_k": _A_in["ssm_k"],
        "A_ssm_D": _A_in["ssm_D"],
        "B_x": _B_in["x"],
        "B_c_kv": _B_in["c_kv"],
        "B_w_q": _B_in["w_q"],
        "B_w_uk": _B_in["w_uk"],
        "B_w_uv": _B_in["w_uv"],
        "B_w_o": _B_in["w_o"],
    }

def reference(A_q, A_k, A_v, A_shift_k, A_ssm_k, A_ssm_D, B_x, B_c_kv, B_w_q, B_w_uk, B_w_uv, B_w_o):
    def _A_ref(q, k, v, shift_k, ssm_k, ssm_D):
        B, S, D = q.shape
        ks = shift_k.shape[-1]
        k_pad = _pad(k, ((0, 0), (ks - 1, 0), (0, 0)))
        k_shift = torch.zeros_like(k)
        for t in range(ks):
            k_shift = k_shift + k_pad[:, t:t + S, :] * shift_k[:, ks - 1 - t][None, None, :]
        kv = k_shift * v
        L = 2 * S
        kv_pad = _pad(kv, ((0, 0), (0, S), (0, 0)))
        kf_pad = _pad(ssm_k, ((0, 0), (0, S)))
        kv_f = (lambda x, axes=(-2,-1): torch.fft.rfft2(x.float(), dim=axes))(kv_pad[:, None, :, :], axes=(1, 2))
        kf_f = (lambda x, axes=(-2,-1): torch.fft.rfft2(x.float(), dim=axes))(kf_pad[:, None, :], axes=(1, 2))
        kf_f = (lambda x, *d: x.permute(*d))(kf_f, 1, 2, 0)
        y_f = kv_f * kf_f[None, :, :, :]
        y = (lambda x, s, axes=(-2,-1): torch.fft.irfft2(x, s=s, dim=axes).to(torch.bfloat16))(y_f, (1, L), axes=(1, 2))[:, 0, :S, :]
        y = y + kv * ssm_D[None, None, :]
        return q * y

    def _B_ref(x, c_kv, w_q, w_uk, w_uv, w_o):
        B, Sq, M = x.shape[0], x.shape[1], x.shape[2]
        Sk = c_kv.shape[1]
        H, D = w_q.shape[1], w_q.shape[2]
        q = torch.einsum("bqm,mhd->bhqd", x, w_q)
        k = torch.einsum("bkc,chd->bhkd", c_kv, w_uk)
        v = torch.einsum("bkc,chd->bhkd", c_kv, w_uv)
        scores = torch.einsum("bhqd,bhkd->bhqk", q, k) * (D ** -0.5)
        off = Sk - Sq
        qi = torch.arange(Sq)[:, None]
        kj = torch.arange(Sk)[None, :]
        cmask = kj <= (qi + off)
        scores = torch.where(cmask[None, None], scores, -1e9)
        p = _softmax(scores, axis=-1)
        ctx = torch.einsum("bhqk,bhkd->bhqd", p, v)
        return torch.einsum("bhqd,hdm->bqm", ctx, w_o)

    a = _A_ref(A_q, A_k, A_v, A_shift_k, A_ssm_k, A_ssm_D)
    b = _B_ref(B_x, B_c_kv, B_w_q, B_w_uk, B_w_uv, B_w_o)
    return torch.concatenate([a, b], axis=-1)
```

```cpp
#include <hip/hip_runtime.h>
#include <hip/hip_bf16.h>
#include <stdint.h>
#include <utility>

typedef __hip_bfloat16 bf16;
typedef __attribute__((ext_vector_type(4))) short bf16x4;
typedef __attribute__((ext_vector_type(2))) unsigned u32x2;

constexpr int SEQ = 2048;
constexpr int DA = 448;
constexpr int NH = 16;
constexpr int HD = 256;
constexpr int DC = 1024;
constexpr int OUTW = 896;
#define HROW(h) ((size_t)(h) * SEQ)
constexpr int HSTEP = SEQ;

#define KST(kid, which)

__device__ __forceinline__ float bf2f(unsigned short u) { return __uint_as_float(((unsigned)u) << 16); }
__device__ __forceinline__ unsigned short f2bf(float f) { return __bfloat16_as_ushort(__float2bfloat16(f)); }
__device__ __forceinline__ float rbf(float f) { return bf2f(f2bf(f)); }
typedef __attribute__((ext_vector_type(2))) __bf16 bf16x2n;
typedef __attribute__((ext_vector_type(2))) float f32x2n;
__device__ __forceinline__ unsigned pk2(float a, float b) {
  return __builtin_bit_cast(unsigned, __builtin_convertvector((f32x2n){a, b}, bf16x2n));
}

__device__ __forceinline__ int off_b(int row, int ch) { return 256 * row + 16 * (ch ^ (((row & 3) << 2) | ((row >> 2) & 3))); }

template <int R> __device__ __forceinline__ void sset(unsigned x) { asm volatile("s_mov_b32 s%c0, %1" ::"i"(R), "s"(x)); }
template <int R> __device__ __forceinline__ float vget() { float t; asm volatile("v_mov_b32 %0, v%c1" : "=v"(t) : "i"(R)); return t; }
template <int R> __device__ __forceinline__ void vset(float x) { asm volatile("v_mov_b32 v%c0, %1" ::"i"(R), "v"(x)); }
template <int R> __device__ __forceinline__ float acc_read1() { float t; asm volatile("v_accvgpr_read_b32 %0, a[%c1]" : "=v"(t) : "i"(R)); return t; }
template <class F, int... I> __device__ __forceinline__ void static_for(F&& f, std::integer_sequence<int, I...>) {
  (f(std::integral_constant<int, I>{}), ...);
}
template <int... Is> using iseq = std::integer_sequence<int, Is...>;
template <int N> using make_iseq = std::make_integer_sequence<int, N>;

typedef __attribute__((ext_vector_type(4))) unsigned u32x4c;
typedef __attribute__((ext_vector_type(4))) float f32x4c;
typedef __attribute__((ext_vector_type(2))) float f32x2c;
typedef unsigned long long u64;
#define DMA16(M0V, VO, SRD, SO) asm volatile("s_mov_b32 m0, %0\n\ts_nop 0\n\tbuffer_load_dwordx4 %1, %2, %3 offen lds" ::"s"(M0V), "v"(VO), "s"(SRD), "s"(SO) : "memory")
struct PR { u32x4c kk[7], vv[4], w[4]; };
__device__ __forceinline__ void prep_load(PR& p, int tid, int j, int rr, const bf16* __restrict__ k, const bf16* __restrict__ v,
                                          const bf16* __restrict__ shift_k) {
  const __amdgpu_buffer_rsrc_t sk = __builtin_amdgcn_make_buffer_rsrc((void*)k, (short)0, SEQ * DA * 2, 0x00020000);
  const __amdgpu_buffer_rsrc_t sv = __builtin_amdgcn_make_buffer_rsrc((void*)v, (short)0, SEQ * DA * 2, 0x00020000);
  const __amdgpu_buffer_rsrc_t sw = __builtin_amdgcn_make_buffer_rsrc((void*)(shift_k + j * 224), (short)0, 448, 0x00020000);
  const int vo = ((rr * 128 + (tid >> 3) * 4) * DA + j * 56 + (tid & 7) * 8) * 2;
#pragma unroll
  for (int q = 0; q < 7; ++q)
    asm volatile("buffer_load_dwordx4 %0, %1, %2, 0 offen" : "=v"(p.kk[q]) : "v"(vo + (q - 3) * DA * 2), "s"(sk) : "memory");
#pragma unroll
  for (int q = 0; q < 4; ++q)
    asm volatile("buffer_load_dwordx4 %0, %1, %2, 0 offen offset:%3" : "=v"(p.vv[q]) : "v"(vo), "s"(sv), "i"(q * DA * 2) : "memory");
#pragma unroll
  for (int q = 0; q < 4; ++q)
    asm volatile("buffer_load_dwordx4 %0, %1, %2, 0 offen offset:%3" : "=v"(p.w[q]) : "v"((tid & 7) * 64), "s"(sw), "i"(q * 16) : "memory");
}
__device__ __forceinline__ float bfl(unsigned u) { return __uint_as_float(u << 16); }
__device__ __forceinline__ float bfh(unsigned u) { return __uint_as_float(u & 0xffff0000u); }
__device__ __forceinline__ f32x2c rbf2(f32x2c a) { const unsigned u = pk2(a[0], a[1]); return (f32x2c){bfl(u), bfh(u)}; }
template <int I> __device__ __forceinline__ void prep_math(const PR& p, int tid, char* lx) {
  const int rp = tid >> 3, c = tid & 7;
  f32x2c wt[4], kf[7];
#pragma unroll
  for (int t = 0; t < 4; ++t)
    wt[t] = (t & 1) ? (f32x2c){bfh(p.w[I][t >> 1]), bfh(p.w[I][2 + (t >> 1)])} : (f32x2c){bfl(p.w[I][t >> 1]), bfl(p.w[I][2 + (t >> 1)])};
#pragma unroll
  for (int q = 0; q < 7; ++q) kf[q] = (f32x2c){bfl(p.kk[q][I]), bfh(p.kk[q][I])};
  f32x2c x[4];
#pragma unroll
  for (int e = 0; e < 4; ++e) {
    f32x2c s = rbf2(kf[e] * wt[3]);
    s = rbf2(s + rbf2(kf[e + 1] * wt[2]));
    s = rbf2(s + rbf2(kf[e + 2] * wt[1]));
    s = rbf2(s + rbf2(kf[e + 3] * wt[0]));
    x[e] = s * (f32x2c){bfl(p.vv[e][I]), bfh(p.vv[e][I])};
  }
#pragma unroll
  for (int u = 0; u < 2; ++u)
    *(u32x2*)(lx + (c * 8 + 2 * I + u) * 256 + (((31 - rp) * 8) ^ (c << 4))) = (u32x2){pk2(x[3][u], x[2][u]), pk2(x[1][u], x[0][u])};
}
__device__ __forceinline__ void prep_store(int tid, int j, int rr, bf16* __restrict__ xr, const char* lx) {
  const __amdgpu_buffer_rsrc_t sx = __builtin_amdgcn_make_buffer_rsrc((void*)(xr + (size_t)j * 56 * SEQ), (short)0, 56 * SEQ * 2, 0x00020000);
#pragma unroll
  for (int rd = 0; rd < 4; ++rd) {
    const int idx = tid + 256 * rd, ch = idx >> 4, q = idx & 15;
    u32x4c w = *(const u32x4c*)(lx + ch * 256 + 16 * (q ^ (ch >> 3)));
    __builtin_amdgcn_raw_buffer_store_b128(w, sx, (ch * SEQ + (15 - rr) * 128 + q * 8) * 2, 0, 16);
  }
}

#define CODE_TOUCH(LB, LE, wx, NL, m0v) do { unsigned blo_, bhi_, elo_; \
    asm volatile("s_getpc_b64 s[40:41]\n\ts_add_u32 s40, s40, " LB "@rel32@lo+4\n\ts_addc_u32 s41, s41, " LB "@rel32@hi+12\n\t" \
                 "s_mov_b32 %0, s40\n\ts_mov_b32 %1, s41\n\t" \
                 "s_getpc_b64 s[40:41]\n\ts_add_u32 s40, s40, " LE "@rel32@lo+4\n\ts_mov_b32 %2, s40" \
                 : "=s"(blo_), "=s"(bhi_), "=s"(elo_) :: "s40", "s41", "scc"); \
    const unsigned n_ = elo_ - (blo_ & ~127u); \
    const __amdgpu_buffer_rsrc_t srd_ = __builtin_amdgcn_make_buffer_rsrc((void*)(((unsigned long long)bhi_ << 32) | (blo_ & ~127u)), (short)0, n_ < 262144u ? n_ : 0u, 0x00020000); \
    const unsigned l_ = threadIdx.x & 63, vo_ = l_ < (NL) ? ((wx) + l_ * 128u) * 128u : 0x80000000u; \
    asm volatile("s_mov_b32 m0, %0\n\ts_nop 4\n\tbuffer_load_dword %1, %2, 0 offen lds" ::"s"(m0v), "v"(vo_), "s"(srd_) : "memory"); } while (0)
__device__ __forceinline__ void conv_load(int d, const bf16* __restrict__ ssm_k, const bf16* __restrict__ xr, unsigned lk) {
  const int lane = threadIdx.x & 63;
  const __amdgpu_buffer_rsrc_t srdK = __builtin_amdgcn_make_buffer_rsrc((void*)(ssm_k + (size_t)d * SEQ), (short)0, SEQ * 2, 0x00020000);
  const __amdgpu_buffer_rsrc_t srdX = __builtin_amdgcn_make_buffer_rsrc((void*)(xr + (size_t)d * SEQ), (short)0, SEQ * 2, 0x00020000);
  const unsigned vo = lane * 16, vo0 = vo - 32;
#define DMA1(M0V, M0OFF, VO, SRD, SO, SC) asm volatile("s_add_u32 m0, %0, %1\n\ts_nop 4\n\tbuffer_load_dwordx4 %2, %3, %4 offen" SC " lds" ::"s"(M0V), "i"(M0OFF), "v"(VO), "s"(SRD), "s"(SO) : "memory", "scc")
  DMA1(lk, 0, vo0, srdK, 0, "");
#pragma unroll
  for (int i = 1; i < 5; ++i) DMA1(lk, i * 1024, vo, srdK, i * 1024 - 32, "");
#pragma unroll
  for (int i = 0; i < 5; ++i) DMA1(lk, 5120 + i * 1024, vo, srdX, i * 1024, " sc1");
#undef DMA1
}
struct CV { unsigned pa[4], pb, qv, qs; __amdgpu_buffer_rsrc_t srdQ; };
constexpr int cv_J(int q) { int J = 7; while (q > J) { q -= J + 1; --J; } return J; }
constexpr int cv_i(int q) { int J = 7; while (q > J) { q -= J + 1; --J; } return q; }
template <int R, int N> __device__ __forceinline__ void cv_rd(CV& c) {
  if constexpr (N < 16) {
    constexpr int i = N >> 1, o = 16 * R + 128 * (i & 1) + 2 * (N & 1), r = 128 + 32 * (R & 1) + 2 * N;
    asm volatile("ds_read2_b32 v[%c1:%c2], %0 offset0:%c3 offset1:%c4" ::"v"(c.pa[i >> 1]), "i"(r), "i"(r + 1), "i"(o), "i"(o + 1));
  } else {
    constexpr int m = N - 16, r = 192 + 32 * (R & 1) + 4 * m;
    asm volatile("ds_read_b128 v[%c1:%c2], %0 offset:%c3" ::"v"(c.pb), "i"(r), "i"(r + 3), "i"(64 * R + 512 * (7 - m)));
  }
}
template <int R, int Q, bool LAST> __device__ __forceinline__ void cv_slot(CV& c) {
  constexpr int i = cv_i(Q), J = cv_J(Q), d = 4 * J, a = 128 + 32 * (R & 1) + 4 * i, b = 192 + 32 * (R & 1) + 4 * (J - i);
  if constexpr (Q == 0) asm volatile("s_waitcnt lgkmcnt(0)");
  asm volatile("v_mfma_f32_16x16x32_bf16 a[%c0:%c1], v[%c2:%c3], v[%c4:%c5], a[%c0:%c1]" ::"i"(d), "i"(d + 3), "i"(a), "i"(a + 3), "i"(b), "i"(b + 3));
  if constexpr (Q < 24 && !(LAST && R == 1)) cv_rd<R + 1, Q>(c);
  if constexpr (LAST && (R == 0 ? Q >= 24 && Q % 3 == 0 : Q % 3 == 0)) {
    constexpr int S_ = R == 0 ? (Q - 24) / 3 : 4 + Q / 3;
    asm volatile("buffer_load_dwordx4 a[%c0:%c1], %2, %3, %4 offen offset:%5 sc1" ::"i"(128 + 4 * S_), "i"(128 + 4 * S_ + 3), "v"(c.qv), "s"(c.srdQ),
                 "s"(c.qs + (S_ >> 2) * 4096), "i"((S_ & 3) * 1024) : "memory");
  }
  __builtin_amdgcn_sched_barrier(0);
}
template <int R, bool LAST, int... Q> __device__ __forceinline__ void cv_step(CV& c, iseq<Q...>) { (cv_slot<R, Q, LAST>(c), ...); }
template <int... N> __device__ __forceinline__ void cv_rd0(CV& c, iseq<N...>) { (cv_rd<0, N>(c), ...); }
template <class HOOK>
__device__ __forceinline__ void conv_wave(int d, float Dd, bf16* __restrict__ zT, char* lw, __amdgpu_buffer_rsrc_t srdQ, unsigned qs,
                                          HOOK&& hook) {
  int lane = threadIdx.x & 63; asm volatile("" : "+v"(lane));
  const int l16 = lane & 15, fq = lane >> 4;
#pragma unroll
  for (int r = 0; r < 5; ++r) {
    const u32x4c w = *(const u32x4c*)(lw + r * 1024 + lane * 16);
    const unsigned w4 = *(const unsigned*)(lw + r * 1024 + lane * 16 + 16);
    *(u32x4c*)(lw + 10304 + r * 1024 + lane * 16) = (u32x4c){__builtin_amdgcn_alignbit(w[1], w[0], 16), __builtin_amdgcn_alignbit(w[2], w[1], 16),
                                                             __builtin_amdgcn_alignbit(w[3], w[2], 16), __builtin_amdgcn_alignbit(w4, w[3], 16)};
  }
  CV c;
#pragma unroll
  for (int i = 0; i < 4; ++i) c.pa[i] = (unsigned)(size_t)lw + ((l16 + 1) & 1) * 10304 + 4 * ((8 * fq + l16 + 1) >> 1) + 1024 * i;
  c.pb = (unsigned)(size_t)lw + 5120 + (2032 - 16 * l16 + 8 * fq) * 2 - 512 * 7;
  static_for([&](auto I) { asm volatile("v_accvgpr_write_b32 a[%c0], 0" ::"i"((int)I)); }, make_iseq<32>{});
  asm volatile("" ::: "memory");
  cv_rd0(c, make_iseq<24>{});
  __builtin_amdgcn_sched_barrier(0);
#pragma unroll 1
  for (int r = 0; r < 3; ++r) {
    cv_step<0, false>(c, make_iseq<36>{});
    cv_step<1, false>(c, make_iseq<36>{});
#pragma unroll
    for (int i = 0; i < 4; ++i) c.pa[i] += 128;
    c.pb += 128;
    hook(r);
    __builtin_amdgcn_sched_barrier(0);
  }
  c.qv = lane * 16; c.qs = __builtin_amdgcn_readfirstlane(qs); c.srdQ = srdQ;
  asm volatile("" : "+s"(c.srdQ), "+s"(c.qs));
  cv_step<0, true>(c, make_iseq<36>{});
  cv_step<1, true>(c, make_iseq<36>{});
  const __amdgpu_buffer_rsrc_t srdZ = __builtin_amdgcn_make_buffer_rsrc((void*)(zT + (size_t)d * SEQ), (short)0, SEQ * 2, 0x00020000);
  asm volatile("s_nop 7");
  static_for([&](auto Jc) __attribute__((always_inline)) {
    constexpr int J = Jc;
    hook(4 + J);
    __builtin_amdgcn_sched_barrier(0);
    const int t = 256 * J + 16 * l16 + 4 * fq;
    bf16x4 xv = *(const bf16x4*)(lw + 5120 + (2044 - t) * 2);
    const float y[4] = {acc_read1<4 * J>(), acc_read1<4 * J + 1>(), acc_read1<4 * J + 2>(), acc_read1<4 * J + 3>()};
    float z[4];
#pragma unroll
    for (int e = 0; e < 4; ++e) z[e] = rbf(y[e]) + rbf(bf2f(xv[3 - e]) * Dd);
    u32x2 w = {pk2(z[0], z[1]), pk2(z[2], z[3])};
    __builtin_amdgcn_raw_buffer_store_b64(w, srdZ, t * 2, 0, 16);
  }, make_iseq<8>{});
}

template <bool Z> __device__ __forceinline__ void post_dma(int j, int tb, const bf16* __restrict__ zT, const bf16* __restrict__ q, unsigned m0w) {
  int lane = threadIdx.x & 63; asm volatile("" : "+v"(lane));
  const __amdgpu_buffer_rsrc_t sz = __builtin_amdgcn_make_buffer_rsrc((void*)(zT + (size_t)j * 56 * SEQ + tb * 32), (short)0, 56 * SEQ * 2, 0x00020000);
  const __amdgpu_buffer_rsrc_t sq = __builtin_amdgcn_make_buffer_rsrc((void*)(q + (size_t)tb * 32 * DA + j * 56), (short)0, 32 * DA * 2, 0x00020000);
  const int tl = lane / 7, ck = lane - tl * 7;
  const unsigned vz = lane < 56 ? (ck * 8 + tl) * SEQ * 2 : 0x80000000u, vq = lane < 56 ? (tl * DA + ck * 8) * 2 : 0x80000000u;
#pragma unroll
  for (int i = 0; i < 4; ++i) if constexpr (Z)
    asm volatile("s_mov_b32 m0, %0\n\ts_nop 4\n\tbuffer_load_dwordx4 %1, %2, %3 offen sc1 lds" ::"s"(m0w + i * 1024), "v"(vz), "s"(sz), "s"(i * 16) : "memory");
#pragma unroll
  for (int i = 0; i < 4; ++i) if constexpr (!Z)
    asm volatile("s_mov_b32 m0, %0\n\ts_nop 4\n\tbuffer_load_dwordx4 %1, %2, %3 offen lds" ::"s"(m0w + 4096 + i * 1024), "v"(vq), "s"(sq), "s"(i * 8 * DA * 2) : "memory");
}
__device__ __forceinline__ void post_math(int j, int tb, bf16* __restrict__ out, const char* lw) {
  int lane = threadIdx.x & 63; asm volatile("" : "+v"(lane));
  const int tl = lane / 7, ck = lane - tl * 7;
  const __amdgpu_buffer_rsrc_t so = __builtin_amdgcn_make_buffer_rsrc((void*)(out + (size_t)tb * 32 * OUTW + j * 56), (short)0, 32 * OUTW * 2 - 2 * DA, 0x00020000);
  const unsigned vo = lane < 56 ? (tl * OUTW + ck * 8) * 2 : 0x80000000u;
  const char* lz = lw + ck * 16 + (tl & 7) * 2;
#pragma unroll
  for (int i = 0; i < 4; ++i) {
    const u32x4c qv = *(const u32x4c*)(lw + 4096 + i * 1024 + lane * 16);
    u32x4c o;
#pragma unroll
    for (int e = 0; e < 4; ++e) {
      const float z0 = bf2f(*(const unsigned short*)(lz + i * 1024 + 2 * e * 112));
      const float z1 = bf2f(*(const unsigned short*)(lz + i * 1024 + (2 * e + 1) * 112));
      o[e] = pk2(bfl(qv[e]) * z0, bfh(qv[e]) * z1);
    }
    __builtin_amdgcn_raw_buffer_store_b128(o, so, vo, i * 8 * OUTW * 2, 0);
  }
}

#define DI __device__ __forceinline__
#define GAP() __builtin_amdgcn_sched_barrier(0)
typedef __attribute__((ext_vector_type(4))) float f32x4g;
typedef __attribute__((ext_vector_type(4))) unsigned u32x4g;
constexpr int LDW = NH * HD;
struct GM {
  unsigned aA[2], aB[2][8];
  unsigned voA, voB, vbA, vbB, kA, kW, kB;
  u64 tab;
  __amdgpu_buffer_rsrc_t srdA, srdB;
  unsigned sA[8], mA, mB;
};
template <int P, int KS, int I> DI void g_wread(GM& g) {
  constexpr int nf = I >> 1, hf = I & 1, r = 128 + 64 * P + 8 * nf + 4 * KS + 2 * hf;
  asm volatile("ds_read_b64_tr_b16 a[%c1:%c2], %0 offset:%c3" ::"v"(g.aB[hf][nf]), "i"(r), "i"(r + 1), "i"(P * 32768 + KS * 16384));
}
template <int P, int M, int KS> DI void g_aread(GM& g) {
  constexpr int r = 96 + 8 * (M & 1) + 4 * KS;
  asm volatile("ds_read_b128 v[%c1:%c2], %0 offset:%c3" ::"v"(g.aA[KS]), "i"(r), "i"(r + 3), "i"(P * 32768 + M * 2048));
}
template <bool TR, int P, int H, int G, bool FIRST> DI void g_gap(GM& g) {
  constexpr int m = 4 * H + (G >> 4), q = G & 15, n = q >> 1, ks = q & 1, F = m * 8 + n;
  constexpr int a = 96 + 8 * (m & 1) + 4 * ks, b = 128 + 64 * P + 8 * n + 4 * ks, d = m < 4 ? 128 + 4 * F : 4 * (F - 32);
  constexpr int J = q == 8 ? 2 * (G >> 4) : q == 12 ? 2 * (G >> 4) + 1 : -1, gj = (J & 3) ^ ((J & 3) >> 1);
  if constexpr (q == 0 && G > 0) asm volatile("s_waitcnt lgkmcnt(4)");
  if constexpr (J == 0) asm volatile("s_add_u32 m0, %0, %1" ::"s"(g.mA), "i"((P ^ H ^ 1) * 32768 + (1 - H) * 8192) : "scc");
  else if constexpr (J == 4) asm volatile("s_add_u32 m0, %0, %1" ::"s"(g.mB), "i"((P ^ H) * 32768 + (1 - H) * 16384) : "scc");
  else if constexpr (J > 0 && (J & 3) == 1) asm volatile("s_bitset1_b32 m0, 12");
  else if constexpr (J > 0 && (J & 3) == 2) asm volatile("s_bitset1_b32 m0, %0" ::"i"(J < 4 ? 14 : 13));
  else if constexpr (J > 0) asm volatile("s_bitset0_b32 m0, 12");
#define GMF(D, X, Y, C) asm volatile("v_mfma_f32_16x16x32_bf16 " D "[%c0:%c1], " X ", " Y ", " C ::"i"(d), "i"(d + 3), "i"(a), "i"(a + 3), "i"(b), "i"(b + 3))
#define GMF2(D, C) do { if constexpr (TR) GMF(D, "v[%c2:%c3]", "a[%c4:%c5]", C); else GMF(D, "a[%c4:%c5]", "v[%c2:%c3]", C); } while (0)
  if constexpr (FIRST && ks == 0) { if constexpr (m < 4) GMF2("v", "0"); else GMF2("a", "0"); }
  else if constexpr (m < 4) GMF2("v", "v[%c0:%c1]");
  else GMF2("a", "a[%c0:%c1]");
#undef GMF2
#undef GMF
#define GD(VO, SRD, SO) asm volatile("buffer_load_dwordx4 %0, %1, %2 offen lds" ::"v"(VO), "s"(SRD), "s"(SO) : "memory")
  if constexpr (J >= 0 && J < 4) GD(g.voA, g.srdA, g.sA[(1 - H) * 2 + (gj & 1) + (gj >> 1) * 4]);
  else if constexpr (J >= 4) GD(g.voB, g.srdB, ((1 - H) * 32 + 16 * (gj & 1)) * LDW * 2 + (gj >> 1) * 256);
#undef GD
  if constexpr (q == 1 || q == 2) g_aread<(m < 7 ? P : P ^ 1), (m + 1) & 7, q - 1>(g);
  if constexpr (q >= 3 && q < 11 && (q & 1)) g_wread<P ^ 1, H, (G >> 4) * 4 + (q - 3) / 2>(g);
  if constexpr (H == 1 && G == 0) asm volatile("s_mov_b32 %0, %1" : "=s"(g.kA) : "s"(g.kW));
  if constexpr (H == 1 && G == 4) asm volatile("v_lshl_add_u32 %0, %2, 7, %3\n\ts_bfe_u32 %1, %4, 0x40000" : "=v"(g.voA), "=s"(g.kW) : "s"(g.kA), "v"(g.vbA), "s"((unsigned)g.tab) : "scc");
  if constexpr (H == 1 && G == 6) asm volatile("s_lshr_b64 %0, %0, 4\n\ts_lshl_b32 %1, %2, 19" : "+s"(g.tab), "=&s"(g.kB) : "s"(g.kW) : "scc");
  if constexpr (H == 1 && G == 10) asm volatile("v_add_u32 %0, %1, %2" : "=v"(g.voB) : "s"(g.kB), "v"(g.vbB));
  if constexpr (G == 61) asm volatile("s_waitcnt vmcnt(12) lgkmcnt(0)\n\ts_barrier" ::: "memory");
  GAP();
}
template <bool TR, int P, int H, bool FIRST, int... Gs> DI void g_half_(GM& g, iseq<Gs...>) { (g_gap<TR, P, H, Gs, FIRST>(g), ...); }
template <bool TR, int P, int H, bool FIRST> DI void g_half(GM& g) { g_half_<TR, P, H, FIRST>(g, make_iseq<64>{}); }
template <int P, int... Is> DI void g_read0(GM& g, iseq<Is...>) {
  (g_wread<P, 0, Is>(g), ...); (g_wread<P, 1, Is>(g), ...);
  g_aread<P, 0, 0>(g); g_aread<P, 0, 1>(g);
}
struct GItem { const bf16* A; const bf16* W; int lda; u64 tab; };
DI void g_srcA(GM& g, const GItem& it, int wid, int lane) {
  g.srdA = __builtin_amdgcn_make_buffer_rsrc((void*)it.A, (short)0, it.A ? 256 * it.lda * 2 : 0, 0x00020000);
  g.vbA = ((wid * 8 + (lane >> 3)) * it.lda + ((lane & 7) ^ (((wid & 1) * 4 + (lane >> 4)) & 7)) * 8) * 2;
#pragma unroll
  for (int j = 0; j < 8; ++j) g.sA[j] = j * 64 * it.lda;
  if (it.A == nullptr) g.mA = 0x28000;
}
DI void g_srcB(GM& g, const GItem& it) {
  g.srdB = __builtin_amdgcn_make_buffer_rsrc((void*)it.W, (short)0, it.A ? (it.lda - 1) * LDW * 2 + 512 : 0, 0x00020000);
  g.tab = it.tab;
  if (it.A == nullptr) g.mB = 0x28000;
}
DI void g_fix(GM& g) {
  g.tab = ((u64)(unsigned)__builtin_amdgcn_readfirstlane((unsigned)(g.tab >> 32)) << 32) | (unsigned)__builtin_amdgcn_readfirstlane((unsigned)g.tab);
  g.mA = __builtin_amdgcn_readfirstlane(g.mA); g.mB = __builtin_amdgcn_readfirstlane(g.mB); g.kW = __builtin_amdgcn_readfirstlane(g.kW);
#pragma unroll
  for (int j = 1; j < 8; ++j) g.sA[j] = __builtin_amdgcn_readfirstlane(g.sA[j]);
}
template <int I> DI void g_pro1(GM& g, unsigned po) {
  constexpr int grp = I >> 2, i = I & 3, t = grp < 4 ? 0 : grp < 7 ? 1 : 2;
  constexpr bool isA = grp == 0 || grp == 3 || grp == 5;
  const unsigned k = ((unsigned)g.tab >> (4 * t)) & 15, so = t == 1 ? po ^ 32768 : po;
  if constexpr (isA) {
    constexpr int jj = (grp == 3 ? 2 : 0) + (i & 1) + (i >> 1) * 4;
    DMA16(g.mA + so + jj * 4096, g.vbA, g.srdA, g.sA[jj] + k * 128);
  } else {
    constexpr int ks = grp == 2 || grp == 6;
    DMA16(g.mB + so + ks * 16384 + (i >> 1) * 8192 + (i & 1) * 4096, g.vbB, g.srdB, k * 0x80000 + (ks * 32 + 16 * (i & 1)) * LDW * 2 + (i >> 1) * 256);
  }
}
template <int B, int... Is> DI void g_pro(GM& g, unsigned po, iseq<Is...>) { (g_pro1<B + Is>(g, po), ...); }
constexpr float CS = 0.0625f * 1.4426950408889634f;
template <bool SCL, int R> DI unsigned g_pack() {
  if constexpr (R < 128 && !SCL) {
    unsigned u;
    asm volatile("v_cvt_pk_bf16_f32 %0, v%c1, v%c2" : "=v"(u) : "i"(128 + R), "i"(129 + R));
    return u;
  } else if constexpr (R < 128) {
    float x, y;
    asm volatile("v_mul_f32 %0, %2, v%c3\n\tv_mul_f32 %1, %2, v%c4" : "=&v"(x), "=&v"(y) : "s"(CS), "i"(128 + R), "i"(129 + R));
    return pk2(x, y);
  } else {
    float x = acc_read1<R - 128>(), y = acc_read1<R - 127>();
    if constexpr (SCL) { x *= CS; y *= CS; }
    return pk2(x, y);
  }
}
template <bool TR, bool SCL> DI void g_epilogue(bf16* out, int wid, int lane) {
  asm volatile("s_nop 7");
  GAP();
  const auto rs = __builtin_amdgcn_make_buffer_rsrc((void*)out, 0, 131072, 0x00020000);
  const int vo = (wid >> 1) * 65536 + (wid & 1) * (TR ? 8192 : 4096) + lane * 16;
  static_for([&](auto I) {
    constexpr int i = I, fx = TR ? (i >> 3) * 16 + (i & 7) : 2 * i, fy = TR ? fx + 8 : fx + 1;
    const u32x4g w = {g_pack<SCL, 4 * fx>(), g_pack<SCL, 4 * fx + 2>(), g_pack<SCL, 4 * fy>(), g_pack<SCL, 4 * fy + 2>()};
    __builtin_amdgcn_raw_buffer_store_b128(w, rs, vo, TR ? (i >> 3) * 16384 + (i & 7) * 1024 : (i >> 2) * 8192 + (i & 3) * 1024, 16);
  }, make_iseq<32>{});
  GAP();
}

typedef __attribute__((ext_vector_type(4))) unsigned u32x4;
typedef __attribute__((ext_vector_type(4))) float f32x4;

constexpr int R_S = 128, R_P = 192, R_CI = 224, R_E = 96;
constexpr int R_TS = 112, R_RS = 114, R_M = 116;
constexpr int R_PD = 118, R_THR = 120, R_TMP = 121;
constexpr int r_dk(int q) { return q < 6 ? 90 + q : 120 + q; }
constexpr int S_V = 96, S_T = 97, S_K = 98, S_I = 99, S_EV = 100;
DI void set_srdk(const void* p, unsigned n) {
  asm volatile("s_mov_b64 s[92:93], %0\n\ts_mov_b32 s94, %1\n\ts_mov_b32 s95, 0x20000" ::"s"(p), "s"(n));
}
struct AC {
  unsigned ka, va, dw[2], vq;
  __amdgpu_buffer_rsrc_t sw;
  unsigned m0x[2];
  u64 redo;
  unsigned rare;
  int mrow;
  bool diag0;
};
template <int X, int F> DI void kread(AC& c) {
  constexpr int r = 192 + 4 * (F & 15);
  asm volatile("ds_read_b128 a[%c1:%c2], %0 offset:%c3" ::"v"(c.ka), "i"(r), "i"(r + 3), "i"(X * 32768 + F * 1024));
}
template <int X, int F> DI void vread(AC& c) {
  constexpr int r = 192 + 4 * (F & 15);
  asm volatile("ds_read_b128 a[%c1:%c2], %0 offset:%c3" ::"v"(c.va), "i"(r), "i"(r + 3), "i"(X * 32768 + (F & 1) * 16384 + (F >> 1) * 1024));
}
template <int X, int... Fs> DI void kreads(AC& c, iseq<Fs...>) { (kread<X, Fs>(c), ...); }
template <int X, int J> DI void dma_m0(AC& c) {
  if constexpr (J == 0) asm volatile("s_mov_b32 m0, %0" ::"s"(c.m0x[X]));
  else if constexpr (J == 4) asm volatile("s_or_b32 m0, %0, 0x1000" ::"s"(c.m0x[X]) : "scc");
  else if constexpr (J == 8) asm volatile("s_bitset0_b32 m0, 16");
  else if constexpr (J == 12) asm volatile("s_bitset0_b32 m0, 12");
}
template <int J> DI void dma_at(AC& c) {
  if constexpr (J < 8)
    asm volatile("buffer_load_dwordx4 %0, %1, s96 offen offset:%2 sc1 lds" ::"v"(c.dw[J >= 4]), "s"(c.sw), "i"((J & 3) * 1024) : "memory");
  else
    asm volatile("buffer_load_dwordx4 v%c0, s[92:95], s98 offen offset:%1 sc1 lds" ::"i"(r_dk(J - 8)), "i"((J & 3) * 1024) : "memory");
}
DI void dma4k(const AC& c, unsigned so, unsigned m0v, int hf) {
    asm volatile("s_mov_b32 m0, %0\n\ts_nop 4\n\tbuffer_load_dwordx4 %1, %2, %3 offen sc1 lds\n\tbuffer_load_dwordx4 %1, %2, %3 offen offset:1024 sc1 lds\n\t"
                 "buffer_load_dwordx4 %1, %2, %3 offen offset:2048 sc1 lds\n\tbuffer_load_dwordx4 %1, %2, %3 offen offset:3072 sc1 lds"
                 ::"s"(m0v + hf * 4096), "v"(c.dw[hf]), "s"(c.sw), "s"(so) : "memory");
}
DI void dma8k(const AC& c, unsigned so, unsigned m0v) { dma4k(c, so, m0v, 0); dma4k(c, so, m0v, 1); }
template <int S_> DI void q_load1(unsigned vo, __amdgpu_buffer_rsrc_t srd, unsigned so) {
  asm volatile("buffer_load_dwordx4 a[%c0:%c1], %2, %3, %4 offen offset:%5 sc1" ::"i"(128 + 4 * S_), "i"(128 + 4 * S_ + 3), "v"(vo), "s"(srd),
               "s"(so + (S_ >> 2) * 4096), "i"((S_ & 3) * 1024) : "memory");
}
template <int... Ss> DI void q_load(unsigned vo, __amdgpu_buffer_rsrc_t srd, unsigned so, iseq<Ss...>) {
  so = __builtin_amdgcn_readfirstlane(so);
  asm volatile("s_nop 4" : "+s"(srd), "+s"(so));
  (q_load1<Ss>(vo, srd, so), ...);
}
template <int Q> DI void acc_zero2() {
  asm volatile("v_accvgpr_write_b32 a[%c0], 0\n\tv_accvgpr_write_b32 a[%c1], 0" ::"i"(2 * Q), "i"(2 * Q + 1));
}
struct SMS { int ex[128], op0[128], nop[128], last; };
constexpr SMS sm_sched() {
  SMS z{};
  int et[32] = {}, e = 0;
  for (int t = 0; t < 128; ++t) {
    const int g = t & 15;
    const bool x = t < 48 ? (g >= 8 && (g & 1)) : (g >= 8 && g != 10 && g != 13 && g != 15);
    z.ex[t] = x && e < 32 ? e : -1;
    if (z.ex[t] >= 0) et[e++] = t;
  }
  int o = 0;
  for (int t = 0; t < 128; ++t) {
    const int g = t & 15;
    int cap = g < 8 ? (g == 0 ? 0 : 1) : (z.ex[t] >= 0 || t < 48 ? 0 : 2);
    z.op0[t] = o; z.nop[t] = 0;
    while (cap > 0 && o < 48 && et[2 * (o / 3) + (o % 3 == 0 ? 0 : 1)] + 2 <= t) {
      const int el = 2 * (o / 3) + o % 3;
      if (!(o % 3 < 2 && (el == 0 || el == 4))) --cap;
      ++o; ++z.nop[t]; z.last = t;
    }
  }
  return z;
}
constexpr SMS SM = sm_sched();
static_assert(SM.last <= 115);
constexpr int p_reg(int Y, int k) { return R_P + 16 * Y + 4 * ((k >> 3) * 2 + ((k >> 1) & 1)) + ((k >> 2) & 1) * 2 + (k & 1); }
template <int Y, int O> DI void sm_op() {
  constexpr int k = O / 3, e = 2 * k + O % 3, ts = R_TS + ((e >> 2) & 1);
  if constexpr (O % 3 == 2) asm volatile("v_cvt_pk_bf16_f32 v%c0, v%c1, v%c2" ::"i"(p_reg(Y, k)), "i"(R_E + ((2 * k) & 15)), "i"(R_E + ((2 * k + 1) & 15)));
  else if constexpr (e == 1 || e == 5) asm volatile("v_add_f32_e32 v%c0, v%c1, v%c2" ::"i"(ts), "i"(R_E + e - 1), "i"(R_E + e));
  else if constexpr (e != 0 && e != 4) asm volatile("v_add_f32_e32 v%c0, v%c0, v%c1" ::"i"(ts), "i"(R_E + (e & 15)));
}
template <int Y, int T> DI void sm_gap(AC& c) {
  if constexpr (SM.ex[T] >= 0) asm volatile("v_exp_f32 v%c0, v%c1" ::"i"(R_E + (SM.ex[T] & 15)), "i"(R_S + 32 * Y + SM.ex[T]));
  if constexpr (SM.nop[T] > 0) sm_op<Y, SM.op0[T]>();
  if constexpr (SM.nop[T] > 1) sm_op<Y, SM.op0[T] + 1>();
  if constexpr (SM.nop[T] > 2) sm_op<Y, SM.op0[T] + 2>();
  if constexpr (T == SM.last + 1) asm volatile("v_add_f32_e32 v%c0, v%c1, v%c2" ::"i"(R_TMP), "i"(R_TS), "i"(R_TS + 1));
  if constexpr (T == SM.last + 2) asm volatile("v_cmp_nle_f32_e64 %0, v%c1, v%c2" : "=s"(c.redo) : "i"(R_TMP), "i"(R_THR));
}
template <int X, bool FIRST, bool NOQK, int G> DI void gapA(AC& c) {
  constexpr int Xp = X ^ 1, kb = G >> 4, g = G & 15, qb = g >> 3, s = g & 7;
  if constexpr (NOQK && G < 48 && G % 3 == 0) q_load1<G / 3>(c.vq, c.sw, c.rare);
  if constexpr (G == 48) asm volatile("s_waitcnt vmcnt(%0) lgkmcnt(0)\n\ts_barrier" ::"i"(NOQK ? 16 : 0) : "memory");
  else if constexpr (g == 0 && !NOQK) asm volatile("s_waitcnt lgkmcnt(0)");
  if constexpr (!NOQK) {
    constexpr int r = 192 + 4 * ((kb & 1) * 8 + s), q = 128 + 4 * g;
    constexpr int d = R_S + 32 * X + 4 * (kb * 2 + qb), ci = s == 0 ? R_CI + 4 * (kb * 2 + qb) : d;
    if constexpr (FIRST && s == 0)
      asm volatile("v_mfma_f32_16x16x32_bf16 v[%c0:%c1], a[%c2:%c3], a[%c4:%c5], 0" ::"i"(d), "i"(d + 3), "i"(r), "i"(r + 3), "i"(q), "i"(q + 3));
    else
      asm volatile("v_mfma_f32_16x16x32_bf16 v[%c0:%c1], a[%c2:%c3], a[%c4:%c5], v[%c6:%c7]" ::"i"(d), "i"(d + 3), "i"(r), "i"(r + 3), "i"(q), "i"(q + 3),
                   "i"(ci), "i"(ci + 3));
    if constexpr (g < 8 && kb < 3) kread<X, (kb + 1) * 8 + g>(c);
    if constexpr (G == 54) asm volatile("s_add_u32 s96, s97, 0x1000000" ::: "scc");
    if constexpr (G == 55) dma_m0<X, 0>(c);
    if constexpr (G >= 56 && !(G & 1)) dma_at<(G - 56) / 2>(c);
    if constexpr (G == 57) asm volatile("s_mov_b32 s97, s98\n\ts_lshr_b32 s100, s100, 1" ::: "scc");
    if constexpr (G == 63) asm volatile("s_add_u32 s98, s98, s99" ::: "scc");
    if constexpr (G == 59 && !FIRST) asm volatile("s_and_b32 s101, s100, 1\n\ts_or_b32 s101, s101, %0" ::"s"((unsigned)c.redo) : "scc");
    if constexpr (G == 61 && !FIRST) asm volatile("s_or_b32 %0, s101, %1" : "=s"(c.rare) : "s"((unsigned)(c.redo >> 32)) : "scc");
  } else if constexpr (SM.nop[64 + G] > 0) asm volatile("s_nop 0");
  if constexpr (!FIRST && g < 8 && kb == 3) vread<Xp, g>(c);
  if constexpr (FIRST) acc_zero2<G>();
  else sm_gap<Xp, 64 + G>(c);
  GAP();
}
template <int R> DI void rescale1(float alpha) {
  float t;
  asm volatile("v_accvgpr_read_b32 %0, a[%c2]\n\tv_mul_f32 %0, %0, %1\n\tv_accvgpr_write_b32 a[%c2], %0" : "=&v"(t) : "v"(alpha), "i"(R));
}
template <int... Rs> DI void rescale_all(const float (&alpha)[2], iseq<Rs...>) { (rescale1<Rs>(alpha[(Rs >> 2) & 1]), ...); }
template <bool MAX> DI float row_reduce(float v) {
  auto p = __builtin_amdgcn_permlane32_swap(__float_as_uint(v), __float_as_uint(v), false, false);
  v = MAX ? fmaxf(__uint_as_float(p[0]), __uint_as_float(p[1])) : __uint_as_float(p[0]) + __uint_as_float(p[1]);
  p = __builtin_amdgcn_permlane16_swap(__float_as_uint(v), __float_as_uint(v), false, false);
  return MAX ? fmaxf(__uint_as_float(p[0]), __uint_as_float(p[1])) : __uint_as_float(p[0]) + __uint_as_float(p[1]);
}
template <int Y, int QB> DI float row_max() {
  constexpr int r = R_S + 32 * Y + 4 * QB;
  float mx;
  asm volatile("v_max_f32 %0, v%c1, v%c2\n\tv_max3_f32 %0, %0, v%c3, v%c4" : "=&v"(mx) : "i"(r), "i"(r + 1), "i"(r + 2), "i"(r + 3));
  static_for([&](auto K) {
    constexpr int q = r + 8 * (K + 1);
    float t = mx;
    asm volatile("v_max3_f32 %0, %0, v%c1, v%c2\n\tv_max3_f32 %0, %0, v%c3, v%c4" : "+v"(t) : "i"(q), "i"(q + 1), "i"(q + 2), "i"(q + 3));
    mx = t;
  }, make_iseq<3>{});
  return row_reduce<true>(mx);
}
template <int R> DI void v_sub(float d) { asm volatile("v_sub_f32 v%c0, v%c0, %1" ::"i"(R), "v"(d)); }
template <int B> DI void s_shift(const float (&d)[2]) { static_for([&](auto E) { v_sub<B + E>(d[(E >> 2) & 1]); }, make_iseq<32>{}); }
DI void m_shift(const float (&d)[2]) {
  s_shift<R_CI>(d);
  v_sub<R_M>(-d[0]); v_sub<R_M + 1>(-d[1]);
}
template <int B, bool INPL> DI void set_mask(const AC& c, int t64) {
  const int mr = c.mrow - t64;
  const float ninf = -INFINITY;
  static_for([&](auto I) {
    constexpr int i = I, r = B + 4 * i, k = (i >> 1) * 16 - (i & 1) * 16;
    const int mr_ = mr; const float b = ninf;
    if constexpr (INPL)
      asm volatile("v_cmp_gt_i32 vcc, %c4, %8\n\tv_cndmask_b32 v%c0, v%c0, %9, vcc\n\tv_cmp_gt_i32 vcc, %c5, %8\n\tv_cndmask_b32 v%c1, v%c1, %9, vcc\n\t"
                   "v_cmp_gt_i32 vcc, %c6, %8\n\tv_cndmask_b32 v%c2, v%c2, %9, vcc\n\tv_cmp_gt_i32 vcc, %c7, %8\n\tv_cndmask_b32 v%c3, v%c3, %9, vcc"
                   ::"i"(r), "i"(r + 1), "i"(r + 2), "i"(r + 3), "i"(k), "i"(k + 1), "i"(k + 2), "i"(k + 3), "v"(mr_), "v"(b) : "vcc");
    else
      asm volatile("v_cmp_gt_i32 vcc, %c4, %8\n\tv_cndmask_b32_e64 v%c0, -v%c10, %9, vcc\n\tv_cmp_gt_i32 vcc, %c5, %8\n\tv_cndmask_b32_e64 v%c1, -v%c10, %9, vcc\n\t"
                   "v_cmp_gt_i32 vcc, %c6, %8\n\tv_cndmask_b32_e64 v%c2, -v%c10, %9, vcc\n\tv_cmp_gt_i32 vcc, %c7, %8\n\tv_cndmask_b32_e64 v%c3, -v%c10, %9, vcc"
                   ::"i"(r), "i"(r + 1), "i"(r + 2), "i"(r + 3), "i"(k), "i"(k + 1), "i"(k + 2), "i"(k + 3), "v"(mr_), "v"(b), "i"(R_M + (i & 1)) : "vcc");
  }, make_iseq<8>{});
  asm volatile("s_nop 1");
}
template <int X> DI void sm_redo() {
  constexpr int Xp = X ^ 1;
  float d[2], dt[2], alpha[2], ts[2] = {0.f, 0.f};
  static_for([&](auto Q) {
    constexpr int qb = Q;
    const float pd = vget<R_PD + qb>();
    d[qb] = fmaxf(row_max<Xp, qb>() - pd, 0.f);
    dt[qb] = d[qb] + pd;
    vset<R_PD + qb>(d[qb]);
    alpha[qb] = __builtin_amdgcn_exp2f(-d[qb]);
    vset<R_RS + qb>(vget<R_RS + qb>() * alpha[qb]);
  }, make_iseq<2>{});
  vset<R_THR>(d[0] > 0.f || d[1] > 0.f ? -1.f : 4096.f);
  m_shift(d);
  static_for([&](auto K) {
    constexpr int k = K, qb = (k >> 1) & 1;
    const float e0 = __builtin_amdgcn_exp2f(vget<R_S + 32 * Xp + 2 * k>() - dt[qb]), e1 = __builtin_amdgcn_exp2f(vget<R_S + 32 * Xp + 2 * k + 1>() - dt[qb]);
    ts[qb] += e0; ts[qb] += e1;
    vset<p_reg(Xp, k)>(__uint_as_float(pk2(e0, e1)));
  }, make_iseq<16>{});
  vset<R_TS>(ts[0]); vset<R_TS + 1>(ts[1]);
  rescale_all(alpha, make_iseq<128>{});
  asm volatile("s_nop 1" ::: "memory");
  GAP();
}
template <int X, bool FIRST, bool NOQK, int... Gs> DI void phaseA_(AC& c, iseq<Gs...>) {
  (gapA<X, FIRST, NOQK, Gs>(c), ...);
}
template <int X, bool FIRST, bool NOQK> DI void phaseA(AC& c) { phaseA_<X, FIRST, NOQK>(c, make_iseq<64>{}); }

template <int X, bool SMX, int G> DI void gapB(AC& c) {
  constexpr int Xp = X ^ 1, blk = G >> 4, g = G & 15;
  if constexpr (g == 0) asm volatile("s_waitcnt lgkmcnt(0)");
  if constexpr (SMX && g == 7 && blk < 3) dma_m0<X, blk * 4 + 4>(c);
  constexpr int qb = (g >> 1) & 1, s = g & 1, r = 192 + 4 * ((blk & 1) * 8 + (g >> 2) * 2 + s), o = 4 * ((blk * 4 + (g >> 2)) * 2 + qb);
  constexpr int p = R_P + 16 * Xp + 4 * (s * 2 + qb);
  asm volatile("v_mfma_f32_16x16x32_bf16 a[%c0:%c1], a[%c2:%c3], v[%c4:%c5], a[%c0:%c1]" ::"i"(o), "i"(o + 3), "i"(r), "i"(r + 3), "i"(p), "i"(p + 3));
  if constexpr (SMX && (G == 1 || G == 2)) asm volatile("v_add_f32_e32 v%c0, v%c0, v%c1" ::"i"(R_RS + G - 1), "i"(R_TS + G - 1));
  if constexpr (g < 8) {
    if constexpr (blk < 3) vread<Xp, (blk + 1) * 8 + g>(c);
    else if constexpr (SMX) kread<Xp, g>(c);
  }
  if constexpr (SMX) {
    if constexpr (g >= 8 && !(g & 1) && blk < 3) dma_at<blk * 4 + 4 + (g - 8) / 2>(c);
    sm_gap<X, G>(c);
  }
  GAP();
}
template <int X, int... Gs> DI void phaseB_(AC& c, iseq<Gs...>) { (gapB<X, true, Gs>(c), ...); }
template <int X> DI void phaseB(AC& c) { phaseB_<X>(c, make_iseq<64>{}); }
constexpr int sm_nex() { int n = 0; for (int t = 0; t < 64; ++t) n += SM.ex[t] >= 0; return n; }
DI void phaseB0(AC& c) {
  dma_m0<0, 4>(c);
  kreads<1>(c, make_iseq<8>{});
  dma_at<4>(c);
  asm volatile("s_nop 3");
  if (__builtin_expect(c.diag0, 0)) set_mask<R_S, true>(c, 0);
  float d[2];
  d[0] = row_max<0, 0>(); GAP(); dma_at<5>(c);
  d[1] = row_max<0, 1>(); GAP(); dma_at<6>(c);
#pragma unroll
  for (int qb = 0; qb < 2; ++qb) if (!(d[qb] > -3.0e38f)) d[qb] = 0.f;
  vset<R_M>(d[0]); vset<R_M + 1>(d[1]);
  const f32x2c nd[2] = {{-d[0], -d[0]}, {-d[1], -d[1]}};
  static_for([&](auto E) {
    constexpr int e = E;
    const f32x2c v = nd[(e >> 1) & 1];
    asm volatile("v_pk_add_f32 v[%c0:%c1], v[%c0:%c1], %4\n\tv_mov_b64 v[%c2:%c3], %4" ::"i"(R_S + 2 * e), "i"(R_S + 2 * e + 1), "i"(R_CI + 2 * e), "i"(R_CI + 2 * e + 1), "v"(v));
    if constexpr (e == 3) dma_at<7>(c);
    if constexpr (e == 6) dma_m0<0, 8>(c);
    if constexpr (e == 7 || e == 11 || e == 15) dma_at<8 + (e - 7) / 4>(c);
  }, make_iseq<16>{});
  if (__builtin_expect(c.diag0, 0)) set_mask<R_CI, false>(c, 64);
  static_assert(sm_nex() >= 16 && SM.op0[64] >= 3 * ((sm_nex() - 15) / 2) && SM.op0[64] <= 24);
  static_for([&](auto E) {
    constexpr int e = E;
    asm volatile("v_exp_f32 v%c0, v%c1" ::"i"(R_E + e), "i"(R_S + e));
    if constexpr (e == 3) dma_at<11>(c);
    if constexpr (e == 6) dma_m0<0, 12>(c);
    if constexpr (e == 7 || e == 11 || e == 15) dma_at<12 + (e - 7) / 4>(c);
  }, make_iseq<16>{});
  static_for([&](auto O) { sm_op<0, O>(); }, make_iseq<SM.op0[64]>{});
  dma_at<15>(c);
  static_for([&](auto E) { asm volatile("v_exp_f32 v%c0, v%c1" ::"i"(R_E + (int)E), "i"(R_S + 16 + (int)E)); }, make_iseq<sm_nex() - 16>{});
  GAP();
}

#define ACLOB_ALL "a0","a1","a2","a3","a4","a5","a6","a7","a8","a9","a10","a11","a12","a13","a14","a15","a16","a17","a18","a19","a20","a21","a22","a23","a24","a25","a26","a27","a28","a29","a30","a31","a32","a33","a34","a35","a36","a37","a38","a39","a40","a41","a42","a43","a44","a45","a46","a47","a48","a49","a50","a51","a52","a53","a54","a55","a56","a57","a58","a59","a60","a61","a62","a63","a64","a65","a66","a67","a68","a69","a70","a71","a72","a73","a74","a75","a76","a77","a78","a79","a80","a81","a82","a83","a84","a85","a86","a87","a88","a89","a90","a91","a92","a93","a94","a95","a96","a97","a98","a99","a100","a101","a102","a103","a104","a105","a106","a107","a108","a109","a110","a111","a112","a113","a114","a115","a116","a117","a118","a119","a120","a121","a122","a123","a124","a125","a126","a127","a128","a129","a130","a131","a132","a133","a134","a135","a136","a137","a138","a139","a140","a141","a142","a143","a144","a145","a146","a147","a148","a149","a150","a151","a152","a153","a154","a155","a156","a157","a158","a159","a160","a161","a162","a163","a164","a165","a166","a167","a168","a169","a170","a171","a172","a173","a174","a175","a176","a177","a178","a179","a180","a181","a182","a183","a184","a185","a186","a187","a188","a189","a190","a191","a192","a193","a194","a195","a196","a197","a198","a199","a200","a201","a202","a203","a204","a205","a206","a207","a208","a209","a210","a211","a212","a213","a214","a215","a216","a217","a218","a219","a220","a221","a222","a223","a224","a225","a226","a227","a228","a229","a230","a231","a232","a233","a234","a235","a236","a237","a238","a239","a240","a241","a242","a243","a244","a245","a246","a247","a248","a249","a250","a251","a252","a253","a254","a255"

struct EP { float fa[2], fb[2]; int vo; unsigned so[4]; };
template <int I, int E, bool MERGE> DI void epi_pair(const EP& p) {
  constexpr int qb = I & 1, r = 16 * (I >> 1) + 4 * qb + (E >> 1) * 8 + (E & 1) * 2, t = 96 + 2 * E, w = 128 + 4 * I + E;
  if constexpr (MERGE) asm volatile("v_accvgpr_read_b32 v106, a[%c0]" ::"i"(w));
  asm volatile("v_accvgpr_read_b32 v%c0, a[%c2]\n\tv_accvgpr_read_b32 v%c1, a[%c3]\n\tv_mul_f32 v%c0, v%c0, %4\n\tv_mul_f32 v%c1, v%c1, %4"
               ::"i"(t), "i"(t + 1), "i"(r), "i"(r + 1), "v"(p.fa[qb]));
  if constexpr (MERGE)
    asm volatile("v_lshlrev_b32 v104, 16, v106\n\tv_and_b32 v105, 0xffff0000, v106\n\tv_fmac_f32 v%c0, v104, %2\n\tv_fmac_f32 v%c1, v105, %2"
                 ::"i"(t), "i"(t + 1), "v"(p.fb[qb]));
  asm volatile("v_cvt_pk_bf16_f32 v%c0, v%c1, v%c2" ::"i"(w), "i"(t), "i"(t + 1));
}
template <int I> DI void epi_store(const AC& c, const EP& p) {
  asm volatile("buffer_store_dwordx4 v[%c0:%c1], %2, %3, %4 offen offset:%5 sc1" ::"i"(128 + 4 * I), "i"(131 + 4 * I), "v"(p.vo), "s"(c.sw),
               "s"(p.so[I >> 2]), "i"((I & 3) * 1024) : "memory");
}
struct PJ { unsigned tb[4], vs[2]; };
template <int HF, int R> DI void p_read(const PJ& j) {
  constexpr int mb = R >> 4, r = 64 * HF + 2 * R;
  asm volatile("ds_read_b64_tr_b16 a[%c1:%c2], %0 offset:%c3" ::"v"(j.tb[2 * HF + mb]), "i"(r), "i"(r + 1), "i"((R & 15) * 2048));
}
template <int QB, int E> DI void p_cvt() {
  asm volatile("v_cvt_pk_bf16_f32 v%c0, v%c1, v%c2" ::"i"(208 + 4 * QB + E), "i"(192 + 8 * QB + 2 * E), "i"(193 + 8 * QB + 2 * E));
}
template <int QB, int PR> DI void p_store(const AC& c, const PJ& j) {
  asm volatile("buffer_store_dwordx4 v[%c0:%c1], %2, %3, s96 offen offset:%4 sc1" ::"i"(208 + 4 * QB), "i"(211 + 4 * QB), "v"(j.vs[QB]), "s"(c.sw),
               "i"(PR * 128) : "memory");
}
constexpr int p_nrd(int G) { return G == 12 || G == 16 || G > 21 ? 0 : (G >= 4 && G < 8) || G == 9 || G == 11 || G >= 20 ? 1 : 2; }
constexpr int p_rd0(int G) { int n = 0; for (int g = 0; g < G; ++g) n += p_nrd(g); return n; }
static_assert(p_rd0(32) == 32);
template <int HF, int G> DI void p_gap(AC& c, PJ& j) {
  constexpr int ch = G >> 3, t = G & 7, a = 64 * HF + 32 * (ch & 1) + 4 * t, b = 128 + 4 * (2 * t + (ch >> 1)), d = 192 + 4 * ch;
  if constexpr (G == 0 && HF == 0) asm volatile("s_waitcnt lgkmcnt(0)");
  if constexpr (G == 0 && HF == 1) asm volatile("s_waitcnt vmcnt(22) lgkmcnt(0)\n\ts_barrier" ::: "memory");
  if constexpr (t == 0)
    asm volatile("v_mfma_f32_16x16x32_bf16 v[%c0:%c1], a[%c2:%c3], v[%c4:%c5], 0" ::"i"(d), "i"(d + 3), "i"(a), "i"(a + 3), "i"(b), "i"(b + 3));
  else
    asm volatile("v_mfma_f32_16x16x32_bf16 v[%c0:%c1], a[%c2:%c3], v[%c4:%c5], v[%c0:%c1]" ::"i"(d), "i"(d + 3), "i"(a), "i"(a + 3), "i"(b), "i"(b + 3));
  if constexpr (p_nrd(G) > 0) p_read<HF ^ 1, p_rd0(G)>(j);
  if constexpr (p_nrd(G) > 1) p_read<HF ^ 1, p_rd0(G) + 1>(j);
  if constexpr (G >= 4 && G < 8) p_cvt<1, G - 4>();
  if constexpr (G == 9) p_store<1, HF ^ 1>(c, j);
  if constexpr (G >= 20 && G < 24) p_cvt<0, G - 20>();
  if constexpr (G == 25) p_store<0, HF>(c, j);
  if constexpr (G == 11 && HF == 0) asm volatile("s_bitset0_b32 m0, 12");
  if constexpr (G == 11 && HF == 1) asm volatile("s_or_b32 m0, s97, s99" ::: "scc");
  if constexpr (G == 12 || G == 16 || G == 26 || G == 30) dma_at<(HF ? 8 : 12) + (G == 12 ? 0 : G == 16 ? 1 : G == 26 ? 2 : 3)>(c);
  if constexpr (G == 22 || G == 23) asm volatile("v_xor_b32 %0, s100, %0" : "+v"(j.tb[2 * (HF ^ 1) + G - 22]));
  if constexpr (HF == 0 && G == 24) asm volatile("s_add_u32 s96, s96, 0x40000\n\ts_xor_b32 s100, s100, 0x10000" ::: "scc");
  if constexpr (HF == 0 && G == 27) asm volatile("s_cmp_lt_u32 s98, 0x300\n\ts_cselect_b32 s99, s99, 0x31000" ::: "scc");
  if constexpr (HF == 0 && G == 28) asm volatile("s_xor_b32 s97, s97, s100" ::: "scc");
  if constexpr (HF == 0 && G == 31) asm volatile("s_add_u32 s98, s98, 128" ::: "scc");
  GAP();
}
template <int HF, int... Gs> DI void p_half(AC& c, PJ& j, iseq<Gs...>) { (p_gap<HF, Gs>(c, j), ...); }
constexpr int f_dma(int G) { return G == 2 ? 0 : G == 5 ? 1 : G == 8 ? 2 : G >= 12 && G <= 36 && G % 6 == 0 ? 1 + G / 6 : G >= 50 && G <= 68 && G % 6 == 2 ? G / 6 : -1; }
constexpr int f_ndma(int G) { int n = 0; for (int g = 0; g < G; ++g) n += f_dma(g) >= 0; return n; }
static_assert(f_ndma(74) == 12);
template <int MODE, int G> DI void gapF(AC& c, const EP& p, const PJ& j) {
  if constexpr (MODE && G == 48) asm volatile("s_waitcnt lgkmcnt(0)\n\ts_barrier" ::: "memory");
  if constexpr (G < 64) gapB<0, false, G>(c);
  if constexpr (MODE == 2 && G >= 10 && (G - 10) % 16 == 0) asm volatile("s_waitcnt vmcnt(%0)" ::"i"(12 - (G - 10) / 4 + f_ndma(G)) : "memory");
  if constexpr (G >= 10) epi_pair<(G - 10) / 4, (G - 10) & 3, MODE == 2>(p);
  if constexpr (MODE == 0) {
    if constexpr (G >= 14 && ((G - 14) & 3) == 0) epi_store<(G - 14) / 4>(c, p);
    if constexpr (G == 73) epi_store<15>(c, p);
  } else {
    if constexpr (G == 0) asm volatile("s_or_b32 m0, %0, 0x1000\n\ts_movk_i32 s98, 256" ::"s"(c.m0x[0]) : "scc");
    if constexpr (G == 15) asm volatile("s_bitset0_b32 m0, 12");
    if constexpr (G == 48) asm volatile("s_or_b32 m0, %0, 0x1000\n\ts_movk_i32 s98, 384" ::"s"(c.m0x[1]) : "scc");
    if constexpr (f_dma(G) >= 0) dma_at<8 + (f_dma(G) & 7)>(c);
    if constexpr (G >= 48 && G < 64) { p_read<0, 2 * (G - 48)>(j); p_read<0, 2 * (G - 48) + 1>(j); }
  }
  GAP();
}
template <int MODE, int... Gs> DI void phaseF(AC& c, const EP& p, const PJ& j, iseq<Gs...>) { (gapF<MODE, Gs>(c, p, j), ...); }
template <int K> DI void r_load(const AC& c, unsigned vo, unsigned so) {
  asm volatile("buffer_load_dwordx4 v[%c0:%c1], %2, %3, %4 offen offset:%5 sc1" ::"i"(128 + 4 * K), "i"(131 + 4 * K), "v"(vo), "s"(c.sw),
               "s"(so + ((K & 15) >> 2) * 4096), "i"((K & 1) * 512 + ((K >> 1) & 1) * 2048) : "memory");
}
template <int K, int NL> DI void r_sum() {
  constexpr int d = 100 + 8 * (K >> 4) + 4 * (K & 1), b = 128 + 4 * K;
  if constexpr ((K & 3) == 0) asm volatile("s_waitcnt vmcnt(%0)" ::"i"(NL - 4 - K) : "memory");
  if constexpr ((K & 15) < 2) asm volatile("v_mfma_f32_16x16x32_bf16 v[%c0:%c1], v[96:99], v[%c2:%c3], 0" ::"i"(d), "i"(d + 3), "i"(b), "i"(b + 3));
  else asm volatile("v_mfma_f32_16x16x32_bf16 v[%c0:%c1], v[96:99], v[%c2:%c3], v[%c0:%c1]" ::"i"(d), "i"(d + 3), "i"(b), "i"(b + 3));
}

#define CMAX 12
#define NYT 16
DI u64* fw(u64* p, int w) { return p + (w >> 4) * 544 + (w & 15); }
DI void set_flag(u64* p, u64 tag, int skip = 0) {
  asm volatile("s_cmp_lg_u32 %2, 0\n\ts_cbranch_scc1 1f\n\ts_mov_b64 exec, 1\n\tglobal_store_dwordx2 %0, %1, off sc1\n\ts_mov_b64 exec, -1\n1:"
               ::"v"(p), "v"(tag), "s"(skip) : "scc", "memory");
}
DI void wait_flags(u64* p, u64 need, u64 tag) {
  const u64* q = fw(p, threadIdx.x & 63);
  u64 v; unsigned cnt;
  asm volatile("s_mov_b32 %1, 0x100000\n\ts_mov_b64 exec, %3\n"
               "1:\n\tglobal_load_dwordx2 %0, %2, off sc1\n\ts_waitcnt vmcnt(0)\n\tv_cmp_eq_u64 vcc, %4, %0\n\t"
               "s_cmp_eq_u64 vcc, %3\n\ts_cbranch_scc1 2f\n\ts_sleep 4\n\ts_sub_u32 %1, %1, 1\n\ts_cbranch_scc0 1b\n"
               "2:\n\ts_mov_b64 exec, -1" : "=&v"(v), "=&s"(cnt) : "v"(q), "s"(need), "s"(tag) : "vcc", "scc", "memory");
}
template <int H> DI void fl_issue(u64* gf, u64 need) {
  const u64* q = fw(gf, threadIdx.x & 63) + H * 4 * 544;
  asm volatile("s_mov_b64 exec, %1\n\tglobal_load_dwordx2 v[%c2:%c3], %0, off sc1\n\ts_mov_b64 exec, -1" ::"v"(q), "s"(need), "i"(122 + 2 * H), "i"(123 + 2 * H) : "memory");
}
template <int H> DI void fl_check(u64* gf, u64 need, u64 tag) {
  u64 m;
  asm volatile("v_cmp_eq_u64 %0, %1, v[%c2:%c3]" : "=&s"(m) : "s"(tag), "i"(122 + 2 * H), "i"(123 + 2 * H) : "memory");
  if (__builtin_expect((m & need) != need, 0)) wait_flags(gf + H * 4 * 544, need, tag);
}
template <bool TR, bool SCL, class START, class MID, class PRE>
DI void g_item(GM& g, const GItem& nxt, u64* pend, u64& tag, bf16* out, int wid, int lane,
               u64* gf, u64 n0, u64 n1, START&& start, MID&& mid, PRE&& pre) {
  constexpr int nkt = SCL ? 7 : 16, P0 = SCL ? 0 : 1;
  GAP();
  start(std::integral_constant<int, P0>{});
  g_fix(g);
  asm volatile("s_nop 4");
  GAP();
  auto hook = [&](int t) __attribute__((always_inline)) {
    if (t == 1) {
      set_flag(pend, tag, wid);
      mid();
    } else if (t == nkt - 3) g_srcB(g, nxt);
    else {
      g_srcA(g, nxt, wid, lane);
      if constexpr (!SCL) { fl_issue<0>(gf, n0); if (n1) fl_issue<1>(gf, n1); }
    }
    g_fix(g);
    asm volatile("s_nop 4");
    GAP();
  };
  g_half<TR, P0, 0, true>(g);
  g_half<TR, P0, 1, true>(g);
  unsigned evm = 2u | (4u << (nkt - 5)) | (4u << (nkt - 4));
  asm volatile("" : "+s"(evm));
#pragma unroll 1
  for (int t = 1;; t += 2) {
    g_half<TR, P0 ^ 1, 0, false>(g);
    if (__builtin_expect((evm >> t) & 1, 0)) hook(t);
    g_half<TR, P0 ^ 1, 1, false>(g);
    if (!SCL && t + 1 >= nkt) break;
    g_half<TR, P0, 0, false>(g);
    if (__builtin_expect(t + 1 == (SCL ? nkt - 3 : nkt - 2), 0)) hook(t + 1);
    g_half<TR, P0, 1, false>(g);
    if (SCL && t + 2 >= nkt) break;
  }
  pre();
  GAP();
  g_epilogue<TR, SCL>(out, wid, lane);
}

constexpr size_t TSZ = (size_t)NH * SEQ * HD;
constexpr unsigned WSB = 5 * TSZ * 2 + 128 * 66560;
__global__ __launch_bounds__(256, 1) __attribute__((amdgpu_num_vgpr(90), amdgpu_num_sgpr(96))) void fused_kernel(const bf16* __restrict__ ckv, const bf16* __restrict__ wuk,
                                                       const bf16* __restrict__ wuv, const bf16* __restrict__ x,
                                                       const bf16* __restrict__ wq, bf16* Qb,
                                                       const bf16* __restrict__ ak, const bf16* __restrict__ av,
                                                       const bf16* __restrict__ ash, const bf16* __restrict__ assmk,
                                                       const bf16* __restrict__ assmD, const bf16* __restrict__ wo,
                                                       const bf16* __restrict__ aq, bf16* __restrict__ out) {
  extern __shared__ __attribute__((aligned(16))) char lds[];
  asm volatile("at_code_begin:" ::: ACLOB_ALL, "v255", "s101");
  asm volatile("" : "+s"(ckv), "+s"(wuk), "+s"(wuv), "+s"(x), "+s"(wq), "+s"(Qb), "+s"(ak), "+s"(av), "+s"(ash), "+s"(assmk),
                    "+s"(assmD), "+s"(wo), "+s"(aq), "+s"(out));
  bf16* Kb = Qb + TSZ;
  bf16* Vb = Kb + TSZ;
  float* partbuf = (float*)(Qb + 5 * TSZ);
  u64* flags = (u64*)(partbuf + (size_t)128 * 16640);
  bf16* xqT = (bf16*)(flags + 400 * 544);
  KST(0, 0);
  const int tid = threadIdx.x, lane = tid & 63;
  const int wid = __builtin_amdgcn_readfirstlane(tid >> 6);
  const int b = blockIdx.x, j = b & 7, r = b >> 3, rr = r & 15, hsel = rr & 1, pj = rr >> 1, kv = pj & 1;
  const bool isY = r >= 16;
  const int h = j + 8 * hsel, mt = (rr >> 2) + (isY ? 4 : 0);
  const unsigned lbase = (unsigned)(size_t)lds;
  u64* gf = fw(flags, j * 128);
  const unsigned m0d = lbase + 163840 - 256;
  const unsigned m0f = lbase + 131072 + wid * 8192;
  const int cch = rr * 4 + wid;
  const bool doA = !isY && cch < 56;
  u64 tag = 0;
  AC c;
  auto att_init = [&]() __attribute__((always_inline)) {
    int l = threadIdx.x & 63; asm volatile("" : "+v"(l));
    c.ka = lbase + l * 16;
    c.dw[0] = wid * 8192 + l * 16; c.dw[1] = c.dw[0] + 4096;
    c.sw = __builtin_amdgcn_make_buffer_rsrc((void*)Qb, (short)0, WSB, 0x00020000);
    set_srdk(Qb, WSB);
    static_for([&](auto I) { vset<r_dk(I)>(__uint_as_float(c.dw[I < 4])); }, make_iseq<8>{});
    sset<S_I>(0x8000);
  };
  const unsigned oQ = (unsigned)h << 20, oK = oQ + (unsigned)(TSZ * 2);
  auto pro_q = [&](int qb) __attribute__((always_inline)) {
    q_load(c.ka - lbase, c.sw, oQ + (qb * 128 + wid * 32) * 512, make_iseq<16>{});
  };
  auto pro_k = [&](int t0) __attribute__((always_inline)) {
    dma8k(c, oK + t0 * 32768, lbase + wid * 8192);
    dma8k(c, oK + t0 * 32768 + 32768, lbase + 32768 + wid * 8192);
  };
  static_assert(NYT <= 16 && NYT >= 12 && !(NYT & 1));
  const bool solo = pj == 7;
  const bool doP = isY != solo;
  const int NY = solo ? 18 : NYT;
  const int nx0 = 32 - 2 * pj - NY, cx = nx0 < CMAX ? nx0 : CMAX, ex = nx0 - cx;
  const u64 needY = (((0x101ull << (((31 - 2 * pj) >> 2) + 1)) - 0x1010ull) << (hsel * 16)) & ~(1ull << (hsel * 16 + kv * 8 + mt));
  const u64 needX = ((0x1010ull << ((ex + 3) >> 2)) - 0x1010ull) << (hsel * 16);
  const u64 needQ = (1ull << (32 + hsel * 8 + (pj >> 1))) | (1ull << (32 + hsel * 8 + ((15 - pj) >> 1)));
  const u64 needA = 0x0f0full << (hsel * 16);
  const u64 needW = 0x3fffull << 36;
  u64* gflag = fw(gf, hsel * 16 + kv * 8 + mt);
  unsigned dv = 0;
  {
    GM g;
    PR pr;
    const u64 tabQ = 0x6543210ull;
    const int cls = (rr + (rr >> 2)) & 3;
    const u64 tabX = cls == 0 ? 0xfedcb9875a463210ull : cls == 1 ? 0xefadc6982b574310ull : cls == 2 ? 0xfebad7693c285410ull : 0xefcba8764d392510ull;
    const u64 tabG = !isY ? tabX : cls == 0 ? 0xfedcb987543a6210ull : cls == 1 ? 0xefadc698254b7310ull : cls == 2 ? 0xfebad769325c8410ull : 0xefcba876432d9510ull;
    const GItem itQ{x + (size_t)pj * 256 * DA, wq + h * 256, DA, tabQ};
    const GItem itG{ckv + (size_t)mt * 256 * DC, (kv ? wuv : wuk) + h * 256, DC, tabG};
    const GItem none{nullptr, nullptr, 0, 0};
    if (doP) prep_load(pr, tid, j, rr, ak, av, ash);
    g.mA = lbase + wid * 1024; g.mB = g.mA + 65536; g.kW = 0;
    g.vbB = (4 * wid + (lane >> 4)) * LDW * 2 + 16 * ((lane & 15) ^ (((lane >> 4) << 2) | wid));
    g_srcA(g, isY ? itQ : itG, wid, lane);
    g_srcB(g, isY ? itQ : itG);
    g_fix(g);
    const unsigned po = isY ? 0 : 32768;
    g_pro<0>(g, po, make_iseq<12>{});
    asm volatile("global_load_dwordx2 v[120:121], %0, off sc1" ::"v"(fw(flags, 6144)) : "memory");
    if (doP) {
      asm volatile("s_waitcnt vmcnt(13)" ::: "memory");
#pragma unroll
      for (int q = 0; q < 7; ++q) asm volatile("" : "+v"(pr.kk[q]));
#pragma unroll
      for (int q = 0; q < 4; ++q) asm volatile("" : "+v"(pr.vv[q]), "+v"(pr.w[q]));
      static_for([&](auto Ic) __attribute__((always_inline)) {
        constexpr int I = Ic;
        g_pro<12 + 4 * I>(g, po, make_iseq<4>{});
        GAP();
        prep_math<I>(pr, tid, lds + 131072);
        GAP();
      }, make_iseq<4>{});
      asm volatile("s_waitcnt lgkmcnt(0)\n\ts_barrier" ::: "memory");
      prep_store(tid, j, rr, xqT, lds + 131072);
    } else g_pro<12>(g, po, make_iseq<4>{});
    GAP();
    CODE_TOUCH("at_code_begin", "pf_code_end", r * 4 + wid, 8, m0d);
    {
      const int fr = lane & 15, fq = lane >> 4, q = (lane & 15) >> 2, p = lane & 3;
#pragma unroll
      for (int k2 = 0; k2 < 2; ++k2)
        g.aA[k2] = lbase + (wid >> 1) * 16384 + (fr >> 3) * 1024 + (fr & 7) * 128 + (((4 * k2 + fq) ^ (fr >> 1)) * 16);
#pragma unroll
      for (int hf = 0; hf < 2; ++hf)
#pragma unroll
        for (int nf = 0; nf < 8; ++nf)
          g.aB[hf][nf] = lbase + 65536 + (wid & 1) * 8192 + off_b(8 * fq + 4 * hf + q, nf * 2 + (p >> 1)) + 8 * (p & 1);
    }
    auto start = [&](auto Pc) __attribute__((always_inline)) {
      if (doP) asm volatile("s_waitcnt vmcnt(13)" ::: "memory");
      else {
        asm volatile("s_nop 4");
        g_pro<16>(g, po, make_iseq<12>{});
        asm volatile("s_waitcnt vmcnt(8)" ::: "memory");
      }
      asm volatile("s_barrier" ::: "memory");
      g_read0<Pc>(g, make_iseq<16>{});
      GAP();
      u64 z = (((u64)__float_as_uint(vget<121>()) << 32) | __float_as_uint(vget<120>())) + 0x9e3779b97f4a7c15ull;
      z = (z ^ (z >> 30)) * 0xbf58476d1ce4e5b9ull; z = (z ^ (z >> 27)) * 0x94d049bb133111ebull; z ^= z >> 31;
      tag = ((u64)(unsigned)__builtin_amdgcn_readfirstlane((int)(z >> 32)) << 32) | (unsigned)__builtin_amdgcn_readfirstlane((int)z);
      asm volatile("s_waitcnt lgkmcnt(0)\n\ts_barrier\n\ts_nop 4" ::: "memory");
      g_pro<28>(g, po, make_iseq<4>{});
      g.kW = ((unsigned)g.tab >> 8) & 15;
      g.voA = g.vbA + (((unsigned)g.tab >> 4) & 15) * 128;
      g.voB = g.vbB + g.kW * 0x80000;
      g.tab >>= 12;
      GAP();
    };
    u64* pend = fw(gf, 64 + r);
    const u64 np0 = isY ? needA | needQ : needQ, np1 = doA ? 0x3fffc000ull : 0ull;
    if (isY) {
      g_item<false, true>(g, itG, pend, tag, Qb + (HROW(h) + pj * 256) * HD, wid, lane, gf, 0, 0, start, [&]() {}, [&]() {});
      pend = fw(gf, 32 + hsel * 8 + pj);
      KST(0, 3);
    }
    auto start2 = [&](auto Pc) __attribute__((always_inline)) { if (!isY) start(Pc); };
    auto mid = [&]() __attribute__((always_inline)) {
      if (wid < 2) post_dma<false>(j, r * 2 + wid, xqT, aq, m0f);
    };
    auto pre = [&]() __attribute__((always_inline)) {
      fl_check<0>(gf, np0, tag);
      fl_check<1>(gf, np1, tag);
      att_init();
      if (isY) { pro_k(cx); pro_q(15 - pj); }
      else if (doA) {
        asm volatile("global_load_ushort %0, %1, off" : "=v"(dv) : "v"(assmD + j * 56 + cch) : "memory");
        conv_load(j * 56 + cch, assmk, xqT, lbase + 65536 + wid * 15488);
      }
    };
    if (kv) g_item<true, false>(g, none, pend, tag, Vb + (HROW(h) + mt * 256) * HD, wid, lane, gf, np0, np1, start2, mid, pre);
    else g_item<false, false>(g, none, pend, tag, Kb + (HROW(h) + mt * 256) * HD, wid, lane, gf, np0, np1, start2, mid, pre);
  }
  KST(0, 4);
  u64* pend2 = gflag;
  if (doA) {
    asm volatile("s_waitcnt vmcnt(32)" : "+v"(dv) :: "memory");
    const float Dd = bf2f(dv);
    KST(0, 5);
    conv_wave(j * 56 + cch, Dd, xqT + (size_t)DA * SEQ, lds + 65536 + wid * 15488,
              c.sw, oQ + ((15 - pj) * 128 + wid * 32) * 512,
              [&](int cr) __attribute__((always_inline)) {
      if (cr == 8) {
        const char* tp = (const char*)wo + (size_t)(j + (cch >= 28 ? 8 : 0)) * (HD * DA * 2) + ((cch % 28) * 64 + lane) * 128;
        asm volatile("s_mov_b32 m0, %1\n\ts_nop 0\n\tglobal_load_lds_dword %0, off" ::"v"(tp), "s"(m0d) : "memory");
      }
      if (cr == 1) {
        asm volatile("s_waitcnt vmcnt(0)\n\ts_barrier" ::: "memory");
        set_flag(gflag, tag, wid);
      } else if (cr == 2) fl_issue<0>(gf, 0x0101ull << (hsel * 16));
      else if (cr >= 4 && cr < 8) {
        if (cr == 4) {
          asm volatile("s_waitcnt vmcnt(16)" ::: "memory");
          fl_check<0>(gf, 0x0101ull << (hsel * 16), tag);
        }
        dma4k(c, oK + ((cr >> 1) & 1) * 32768, lbase + ((cr >> 1) & 1) * 32768 + wid * 8192, cr & 1);
      }
    });
    KST(0, 6);
    pend2 = fw(gf, 100 + rr);
  } else if (!isY) {
    asm volatile("s_waitcnt vmcnt(0)\n\ts_barrier" ::: "memory");
    set_flag(gflag, tag, wid);
    pro_q(pj);
    KST(0, 5);
    wait_flags(gf, needA, tag);
    pro_k(0);
    asm volatile("s_waitcnt vmcnt(0)" ::: "memory");
    KST(0, 6);
  }
  c.va = c.ka + 65536;
  c.m0x[0] = lbase + 65536 + wid * 8192; c.m0x[1] = c.m0x[0] + 32768;
  const unsigned oP = (unsigned)(5 * TSZ * 2) + (h * 8 + pj) * 66560;
  const int qbF = isY ? 15 - pj : pj;
  PJ pq;
  const int vml = 65536 + wid * 256 + (lane & 15) * 16;
  const int nitems = isY ? 1 : 2, it0 = !isY && solo ? 1 : 0;
#pragma unroll 1
  for (int it = it0; it < nitems; ++it) {
    int qb, t0, n, jmp, c0, c1;
    u64 n0, n1;
    if (isY) { qb = 15 - pj; t0 = cx; n = NY; jmp = ex; c1 = 14 - cx; n1 = needY; c0 = solo ? 30 : NY - 2; n0 = 1ull << (48 + hsel * 8 + pj); }
    else if (it == 0) { qb = 15 - pj; t0 = 0; n = nx0; jmp = 16 - cx; c1 = ex ? cx - 2 : 30; n1 = needX; c0 = 2; n0 = needA; }
    else { qb = pj; t0 = 0; n = 2 * pj + 2; jmp = 0; c1 = 30; c0 = 30; n0 = n1 = 0; }
    const bool last = isY || it == 1;
    c.mrow = qb * 128 + wid * 32 + (lane & 15) - 4 * (lane >> 4);
    static_for([&](auto I) { vset<R_RS + I>(0.f); }, make_iseq<6>{});
    vset<R_THR>(4096.f);
    c.diag0 = qb == 0;
    sset<S_T>(oK + t0 * 32768); sset<S_K>(oK + t0 * 32768 + 32768);
    const int ct = !last ? 30 : n > 2 ? n - 3 : 0, cw = isY ? (solo ? 13 : c1) : last ? (n > 2 ? 0 : 30) : n - 2;
    sset<S_EV>((3u << c0) | (3u << c1) | (last ? 3u << n >> 2 : 1u << n >> 1));
    if (it != it0) asm volatile("s_waitcnt vmcnt(16)" ::: "memory");
    else {
      if (doA) asm volatile("s_waitcnt vmcnt(6)" ::: "memory");
      asm volatile("s_waitcnt vmcnt(32) lgkmcnt(0)\n\ts_barrier" ::: "memory");
    }
    KST(0, 8 + 3 * it);
    kreads<0>(c, make_iseq<8>{});
    GAP();
    auto event = [&](int i) __attribute__((always_inline)) {
      if (i == c0) fl_check<0>(gf, n0, tag);
      if (i == c1) { fl_check<0>(gf, n1, tag); asm volatile("s_add_u32 s98, s98, %0" ::"s"(jmp * 32768) : "scc"); }
      if (i + 1 == c0 || i + 1 == c1) fl_issue<0>(gf, (i + 1 == c0 ? n0 : 0) | (i + 1 == c1 ? n1 : 0));
      if (i == cw) fl_issue<1>(gf, needW);
      if (i + 2 == n) {
        sset<S_K>(oK);
        if (last) {
          int l = threadIdx.x & 63; asm volatile("" : "+v"(l));
          const unsigned vw = (64 * wid + (l >> 3)) * (DA * 2) + 16 * ((l & 7) ^ ((l >> 3) & 6));
          static_for([&](auto Q) { vset<r_dk(Q)>(__uint_as_float(vw + (Q ^ 4) * (8 * DA * 2) - (Q & 3) * 1024)); }, make_iseq<8>{});
          set_srdk(wo + (size_t)h * HD * DA, HD * DA * 2);
          sset<S_K>(0); sset<S_I>(128);
        }
        if (isY && !solo)
          asm volatile("s_nop 4\n\tbuffer_load_dwordx4 v[122:125], %0, %1, %2 offen sc1" ::"v"(vml), "s"(c.sw), "s"(oP) : "memory");
      }
      if (i == ct) {
        fl_check<1>(gf, needW, tag);
        if (wid < 2) post_dma<true>(j, r * 2 + wid, xqT + (size_t)DA * SEQ, aq, m0f);
      }
      if (last && i + 3 >= n && i + 1 < n && i) set_mask<R_CI, false>(c, (2 * qb + i + 3 - n) * 64);
      if (i == 0) set_flag(it != it0 ? fw(gf, 48 + hsel * 8 + pj) : pend2, tag, wid);
      asm volatile("s_nop 4");
      GAP();
    };
#define STA(k)
    STA(0);
    phaseA<0, true, false>(c);
    STA(1);
    event(0);
    STA(2);
    phaseB0(c);
    STA(3);
#pragma unroll 1
    for (int i = 1;; i += 2) {
      phaseA<1, false, false>(c);
      STA(9 + i);
      if (__builtin_expect(c.rare != 0, 0)) { if (c.redo) sm_redo<1>(); event(i); }
      phaseB<1>(c);
      if (i + 1 >= n) break;
      phaseA<0, false, false>(c);
      STA(10 + i);
      if (__builtin_expect(c.rare != 0, 0)) { if (c.redo) sm_redo<0>(); event(i + 1); }
      phaseB<0>(c);
    }
    STA(4);
    c.rare = __builtin_amdgcn_readfirstlane(!last ? oQ + (pj * 128 + wid * 32) * 512 : oP + wid * 16384);
    c.vq = last && (!isY || solo) ? 0x80000000u : c.ka;
    asm volatile("s_nop 4" : "+s"(c.rare));
    phaseA<0, false, true>(c);
    if (__builtin_expect(c.redo != 0, 0)) sm_redo<0>();
    STA(5);
    KST(0, 9 + 3 * it);
    const float l[2] = {row_reduce<false>(vget<R_RS>() + vget<R_TS>()), row_reduce<false>(vget<R_RS + 1>() + vget<R_TS + 1>())};
    const float cm[2] = {vget<R_M>(), vget<R_M + 1>()};
    EP p;
    p.vo = c.dw[0] + wid * 8192;
#pragma unroll
    for (int e = 0; e < 4; ++e) p.so[e] = oP + e * 4096;
    {
      int lz = threadIdx.x & 63; asm volatile("" : "+v"(lz));
      const int rw = lz >> 2;
#pragma unroll
      for (int e = 0; e < 4; ++e) pq.tb[e] = lbase + rw * 128 + 16 * ((2 * e + ((lz & 3) >> 1)) ^ (rw & 6)) + 8 * (lz & 1);
    }
    if (!isY || solo) {
      p.fa[0] = __builtin_amdgcn_rcpf(l[0]); p.fa[1] = __builtin_amdgcn_rcpf(l[1]);
      if (!last) {
        __builtin_amdgcn_raw_buffer_store_b128(__builtin_bit_cast(u32x4, (f32x4){cm[0], l[0], cm[1], l[1]}), c.sw, vml, oP, 16);
        phaseF<0>(c, p, pq, make_iseq<74>{});
      } else phaseF<1>(c, p, pq, make_iseq<74>{});
    } else {
      const f32x4 ml = {vget<122>(), vget<123>(), vget<124>(), vget<125>()};
#pragma unroll
      for (int e = 0; e < 2; ++e) {
        const float mm = fmaxf(cm[e], ml[2 * e]);
        const float wa = __builtin_amdgcn_exp2f(cm[e] - mm), wb = ml[2 * e + 1] * __builtin_amdgcn_exp2f(ml[2 * e] - mm);
        const float inv = __builtin_amdgcn_rcpf(l[e] * wa + wb);
        p.fa[e] = wa * inv; p.fb[e] = wb * inv;
      }
      phaseF<2>(c, p, pq, make_iseq<74>{});
    }
    KST(0, 10 + 3 * it);
  }
  {
    int lz = threadIdx.x & 63; asm volatile("" : "+v"(lz));
    const int fr = lz & 15, fq = lz >> 4;
    const unsigned oS = (unsigned)(3 * TSZ * 2) + qbF * 0x200000;
    pq.tb[0] ^= 0x8000; pq.tb[1] ^= 0x8000;
    pq.vs[0] = (fr >> 3) * 16384 + (fq & 1) * 512 + (fq >> 1) * 256 + (fr & 7) * 16; pq.vs[1] = pq.vs[0] + 32768;
    sset<96>(oS + wid * 65536 + h * 1024); sset<97>(c.m0x[1]); sset<99>(0x1000); sset<100>(0x8000);
    asm volatile("" : "+s"(c.sw));
    GAP();
#pragma unroll 1
    for (int pc = 0; pc < 7; ++pc) {
      p_half<0>(c, pq, make_iseq<32>{});
      p_half<1>(c, pq, make_iseq<32>{});
    }
    asm volatile("s_nop 5");
    p_cvt<1, 0>(); p_cvt<1, 1>(); p_cvt<1, 2>(); p_cvt<1, 3>();
    p_store<1, 1>(c, pq);
    KST(0, 27);
    {
      const int jj = 4 * (fr >> 3) + (fr & 3);
      const unsigned one = ((fq ^ (fr >> 2)) & 1) ? 0u : (jj & 1) ? 0x3f800000u : 0x3f80u;
      static_for([&](auto I) { vset<96 + I>(__uint_as_float((jj >> 1) == I ? one : 0u)); }, make_iseq<4>{});
    }
    asm volatile("s_waitcnt vmcnt(0)" ::: "memory");
    set_flag(fw(flags, 1024 + (qbF * 4 + wid) * 16 + h), tag);
    KST(0, 30);
    if (wid < 2) post_math(j, r * 2 + wid, out, lds + 131072 + wid * 8192);
    if (wid == 3) wait_flags(fw(flags, 1024 + (qbF * 4 + (h & 3)) * 16), 0xffffull, tag);
    asm volatile("s_barrier" ::: "memory");
    KST(0, 28);
    const unsigned vo = (fq >> 1) * 1024 + (fq & 1) * 256 + fr * 16;
    unsigned so = __builtin_amdgcn_readfirstlane(oS + (1 + wid) * 0x40000 + (h & 3) * 65536 + (h >> 2) * 16384);
    bf16* op = out + (size_t)(qbF * 128 + (h & 3) * 32 + (h >> 2) * 8 + (fr & 7)) * OUTW + DA + wid * 64 + (fr >> 3) * 32 + fq * 8;
    auto fin = [&](auto NLc) __attribute__((always_inline)) {
      constexpr int NL = NLc;
      asm volatile("s_nop 4" : "+s"(so), "+s"(c.sw));
      static_for([&](auto K) { r_load<K>(c, vo, so + (K >> 4) * 0x100000); }, make_iseq<NL>{});
      static_for([&](auto K) { r_sum<K, NL>(); }, make_iseq<NL>{});
      asm volatile("s_nop 7");
      static_for([&](auto U) {
        constexpr int d = 100 + 8 * U;
        *(u32x4*)(op + U * 256) = (u32x4){pk2(vget<d>(), vget<d + 1>()), pk2(vget<d + 2>(), vget<d + 3>()), pk2(vget<d + 4>(), vget<d + 5>()), pk2(vget<d + 6>(), vget<d + 7>())};
      }, make_iseq<NL / 16>{});
    };
    if (wid < 3) fin(std::integral_constant<int, 32>{});
    else fin(std::integral_constant<int, 16>{});
    KST(0, 29);
    set_flag(fw(flags, 6144), tag, (b ^ 128) | wid);
  }
  asm volatile("s_waitcnt vmcnt(0)" ::: "memory");
  asm volatile("pf_code_end:");
  KST(0, 1);
}

extern "C" void kernel_launch(void* const* d_in, const int* in_sizes, int n_in, void* d_out, int out_size, void* d_ws,
                              size_t ws_size, hipStream_t stream) {
  const bf16* Aq = (const bf16*)d_in[0];
  const bf16* Ak = (const bf16*)d_in[1];
  const bf16* Av = (const bf16*)d_in[2];
  const bf16* Ash = (const bf16*)d_in[3];
  const bf16* Assmk = (const bf16*)d_in[4];
  const bf16* AssmD = (const bf16*)d_in[5];
  const bf16* Bx = (const bf16*)d_in[6];
  const bf16* Bckv = (const bf16*)d_in[7];
  const bf16* Bwq = (const bf16*)d_in[8];
  const bf16* Bwuk = (const bf16*)d_in[9];
  const bf16* Bwuv = (const bf16*)d_in[10];
  const bf16* Bwo = (const bf16*)d_in[11];
  bf16* out = (bf16*)d_out;
  hipFuncSetAttribute((const void*)fused_kernel, hipFuncAttributeMaxDynamicSharedMemorySize, 163840);
  fused_kernel<<<256, 256, 163840, stream>>>(Bckv, Bwuk, Bwuv, Bx, Bwq, (bf16*)d_ws, Ak, Av, Ash, Assmk, AssmD, Bwo, Aq, out);
}
```

```cpp
#include <hip/hip_runtime.h>
#include <hip/hip_bf16.h>
#include <stdint.h>
#include <utility>

typedef __hip_bfloat16 bf16;
typedef __attribute__((ext_vector_type(4))) short bf16x4;
typedef __attribute__((ext_vector_type(2))) unsigned u32x2;

constexpr int SEQ = 2048;
constexpr int DA = 448;
constexpr int NH = 16;
constexpr int HD = 256;
constexpr int DC = 1024;
constexpr int OUTW = 896;
#define HROW(h) ((size_t)(h) * SEQ)
constexpr int HSTEP = SEQ;

#define KST(kid, which)

__device__ __forceinline__ float bf2f(unsigned short u) { return __uint_as_float(((unsigned)u) << 16); }
__device__ __forceinline__ unsigned short f2bf(float f) { return __bfloat16_as_ushort(__float2bfloat16(f)); }
__device__ __forceinline__ float rbf(float f) { return bf2f(f2bf(f)); }
typedef __attribute__((ext_vector_type(2))) __bf16 bf16x2n;
typedef __attribute__((ext_vector_type(2))) float f32x2n;
__device__ __forceinline__ unsigned pk2(float a, float b) {
  return __builtin_bit_cast(unsigned, __builtin_convertvector((f32x2n){a, b}, bf16x2n));
}

__device__ __forceinline__ int off_b(int row, int ch) { return 256 * row + 16 * (ch ^ (((row & 3) << 2) | ((row >> 2) & 3))); }

template <int R> __device__ __forceinline__ void sset(unsigned x) { asm volatile("s_mov_b32 s%c0, %1" ::"i"(R), "s"(x)); }
template <int R> __device__ __forceinline__ float vget() { float t; asm volatile("v_mov_b32 %0, v%c1" : "=v"(t) : "i"(R)); return t; }
template <int R> __device__ __forceinline__ void vset(float x) { asm volatile("v_mov_b32 v%c0, %1" ::"i"(R), "v"(x)); }
template <int R> __device__ __forceinline__ float acc_read1() { float t; asm volatile("v_accvgpr_read_b32 %0, a[%c1]" : "=v"(t) : "i"(R)); return t; }
template <class F, int... I> __device__ __forceinline__ void static_for(F&& f, std::integer_sequence<int, I...>) {
  (f(std::integral_constant<int, I>{}), ...);
}
template <int... Is> using iseq = std::integer_sequence<int, Is...>;
template <int N> using make_iseq = std::make_integer_sequence<int, N>;

typedef __attribute__((ext_vector_type(4))) unsigned u32x4c;
typedef __attribute__((ext_vector_type(4))) float f32x4c;
typedef __attribute__((ext_vector_type(2))) float f32x2c;
typedef unsigned long long u64;
#define DMA16(M0V, VO, SRD, SO) asm volatile("s_mov_b32 m0, %0\n\ts_nop 0\n\tbuffer_load_dwordx4 %1, %2, %3 offen lds" ::"s"(M0V), "v"(VO), "s"(SRD), "s"(SO) : "memory")
struct PR { u32x4c kk[7], vv[4], w[4]; };
__device__ __forceinline__ void prep_load(PR& p, int tid, int j, int rr, const bf16* __restrict__ k, const bf16* __restrict__ v,
                                          const bf16* __restrict__ shift_k) {
  const __amdgpu_buffer_rsrc_t sk = __builtin_amdgcn_make_buffer_rsrc((void*)k, (short)0, SEQ * DA * 2, 0x00020000);
  const __amdgpu_buffer_rsrc_t sv = __builtin_amdgcn_make_buffer_rsrc((void*)v, (short)0, SEQ * DA * 2, 0x00020000);
  const __amdgpu_buffer_rsrc_t sw = __builtin_amdgcn_make_buffer_rsrc((void*)(shift_k + j * 224), (short)0, 448, 0x00020000);
  const int vo = ((rr * 128 + (tid >> 3) * 4) * DA + j * 56 + (tid & 7) * 8) * 2;
#pragma unroll
  for (int q = 0; q < 7; ++q)
    asm volatile("buffer_load_dwordx4 %0, %1, %2, 0 offen" : "=v"(p.kk[q]) : "v"(vo + (q - 3) * DA * 2), "s"(sk) : "memory");
#pragma unroll
  for (int q = 0; q < 4; ++q)
    asm volatile("buffer_load_dwordx4 %0, %1, %2, 0 offen offset:%3" : "=v"(p.vv[q]) : "v"(vo), "s"(sv), "i"(q * DA * 2) : "memory");
#pragma unroll
  for (int q = 0; q < 4; ++q)
    asm volatile("buffer_load_dwordx4 %0, %1, %2, 0 offen offset:%3" : "=v"(p.w[q]) : "v"((tid & 7) * 64), "s"(sw), "i"(q * 16) : "memory");
}
__device__ __forceinline__ float bfl(unsigned u) { return __uint_as_float(u << 16); }
__device__ __forceinline__ float bfh(unsigned u) { return __uint_as_float(u & 0xffff0000u); }
__device__ __forceinline__ f32x2c rbf2(f32x2c a) { const unsigned u = pk2(a[0], a[1]); return (f32x2c){bfl(u), bfh(u)}; }
template <int I> __device__ __forceinline__ void prep_math(const PR& p, int tid, char* lx) {
  const int rp = tid >> 3, c = tid & 7;
  f32x2c wt[4], kf[7];
#pragma unroll
  for (int t = 0; t < 4; ++t)
    wt[t] = (t & 1) ? (f32x2c){bfh(p.w[I][t >> 1]), bfh(p.w[I][2 + (t >> 1)])} : (f32x2c){bfl(p.w[I][t >> 1]), bfl(p.w[I][2 + (t >> 1)])};
#pragma unroll
  for (int q = 0; q < 7; ++q) kf[q] = (f32x2c){bfl(p.kk[q][I]), bfh(p.kk[q][I])};
  f32x2c x[4];
#pragma unroll
  for (int e = 0; e < 4; ++e) {
    f32x2c s = rbf2(kf[e] * wt[3]);
    s = rbf2(s + rbf2(kf[e + 1] * wt[2]));
    s = rbf2(s + rbf2(kf[e + 2] * wt[1]));
    s = rbf2(s + rbf2(kf[e + 3] * wt[0]));
    x[e] = s * (f32x2c){bfl(p.vv[e][I]), bfh(p.vv[e][I])};
  }
#pragma unroll
  for (int u = 0; u < 2; ++u)
    *(u32x2*)(lx + (c * 8 + 2 * I + u) * 256 + (((31 - rp) * 8) ^ (c << 4))) = (u32x2){pk2(x[3][u], x[2][u]), pk2(x[1][u], x[0][u])};
}
__device__ __forceinline__ void prep_store(int tid, int j, int rr, bf16* __restrict__ xr, const char* lx) {
  const __amdgpu_buffer_rsrc_t sx = __builtin_amdgcn_make_buffer_rsrc((void*)(xr + (size_t)j * 56 * SEQ), (short)0, 56 * SEQ * 2, 0x00020000);
#pragma unroll
  for (int rd = 0; rd < 4; ++rd) {
    const int idx = tid + 256 * rd, ch = idx >> 4, q = idx & 15;
    u32x4c w = *(const u32x4c*)(lx + ch * 256 + 16 * (q ^ (ch >> 3)));
    __builtin_amdgcn_raw_buffer_store_b128(w, sx, (ch * SEQ + (15 - rr) * 128 + q * 8) * 2, 0, 16);
  }
}

#define CODE_TOUCH(LB, LE, wx, NL, m0v) do { unsigned blo_, bhi_, elo_; \
    asm volatile("s_getpc_b64 s[40:41]\n\ts_add_u32 s40, s40, " LB "@rel32@lo+4\n\ts_addc_u32 s41, s41, " LB "@rel32@hi+12\n\t" \
                 "s_mov_b32 %0, s40\n\ts_mov_b32 %1, s41\n\t" \
                 "s_getpc_b64 s[40:41]\n\ts_add_u32 s40, s40, " LE "@rel32@lo+4\n\ts_mov_b32 %2, s40" \
                 : "=s"(blo_), "=s"(bhi_), "=s"(elo_) :: "s40", "s41", "scc"); \
    const unsigned n_ = elo_ - (blo_ & ~127u); \
    const __amdgpu_buffer_rsrc_t srd_ = __builtin_amdgcn_make_buffer_rsrc((void*)(((unsigned long long)bhi_ << 32) | (blo_ & ~127u)), (short)0, n_ < 262144u ? n_ : 0u, 0x00020000); \
    const unsigned l_ = threadIdx.x & 63, vo_ = l_ < (NL) ? ((wx) + l_ * 128u) * 128u : 0x80000000u; \
    asm volatile("s_mov_b32 m0, %0\n\ts_nop 4\n\tbuffer_load_dword %1, %2, 0 offen lds" ::"s"(m0v), "v"(vo_), "s"(srd_) : "memory"); } while (0)
__device__ __forceinline__ void conv_load(int d, const bf16* __restrict__ ssm_k, const bf16* __restrict__ xr, unsigned lk) {
  const int lane = threadIdx.x & 63;
  const __amdgpu_buffer_rsrc_t srdK = __builtin_amdgcn_make_buffer_rsrc((void*)(ssm_k + (size_t)d * SEQ), (short)0, SEQ * 2, 0x00020000);
  const __amdgpu_buffer_rsrc_t srdX = __builtin_amdgcn_make_buffer_rsrc((void*)(xr + (size_t)d * SEQ), (short)0, SEQ * 2, 0x00020000);
  const unsigned vo = lane * 16, vo0 = vo - 32;
#define DMA1(M0V, M0OFF, VO, SRD, SO, SC) asm volatile("s_add_u32 m0, %0, %1\n\ts_nop 4\n\tbuffer_load_dwordx4 %2, %3, %4 offen" SC " lds" ::"s"(M0V), "i"(M0OFF), "v"(VO), "s"(SRD), "s"(SO) : "memory", "scc")
  DMA1(lk, 0, vo0, srdK, 0, "");
#pragma unroll
  for (int i = 1; i < 5; ++i) DMA1(lk, i * 1024, vo, srdK, i * 1024 - 32, "");
#pragma unroll
  for (int i = 0; i < 5; ++i) DMA1(lk, 5120 + i * 1024, vo, srdX, i * 1024, " sc1");
#undef DMA1
}
struct CV { unsigned pa[4], pb, qv, qs; __amdgpu_buffer_rsrc_t srdQ; };
constexpr int cv_J(int q) { int J = 7; while (q > J) { q -= J + 1; --J; } return J; }
constexpr int cv_i(int q) { int J = 7; while (q > J) { q -= J + 1; --J; } return q; }
template <int R, int N> __device__ __forceinline__ void cv_rd(CV& c) {
  if constexpr (N < 16) {
    constexpr int i = N >> 1, o = 16 * R + 128 * (i & 1) + 2 * (N & 1), r = 128 + 32 * (R & 1) + 2 * N;
    asm volatile("ds_read2_b32 v[%c1:%c2], %0 offset0:%c3 offset1:%c4" ::"v"(c.pa[i >> 1]), "i"(r), "i"(r + 1), "i"(o), "i"(o + 1));
  } else {
    constexpr int m = N - 16, r = 192 + 32 * (R & 1) + 4 * m;
    asm volatile("ds_read_b128 v[%c1:%c2], %0 offset:%c3" ::"v"(c.pb), "i"(r), "i"(r + 3), "i"(64 * R + 512 * (7 - m)));
  }
}
template <int R, int Q, bool LAST> __device__ __forceinline__ void cv_slot(CV& c) {
  constexpr int i = cv_i(Q), J = cv_J(Q), d = 4 * J, a = 128 + 32 * (R & 1) + 4 * i, b = 192 + 32 * (R & 1) + 4 * (J - i);
  if constexpr (Q == 0) asm volatile("s_waitcnt lgkmcnt(0)");
  asm volatile("v_mfma_f32_16x16x32_bf16 a[%c0:%c1], v[%c2:%c3], v[%c4:%c5], a[%c0:%c1]" ::"i"(d), "i"(d + 3), "i"(a), "i"(a + 3), "i"(b), "i"(b + 3));
  if constexpr (Q < 24 && !(LAST && R == 1)) cv_rd<R + 1, Q>(c);
  if constexpr (LAST && (R == 0 ? Q >= 24 && Q % 3 == 0 : Q % 3 == 0)) {
    constexpr int S_ = R == 0 ? (Q - 24) / 3 : 4 + Q / 3;
    asm volatile("buffer_load_dwordx4 a[%c0:%c1], %2, %3, %4 offen offset:%5 sc1" ::"i"(128 + 4 * S_), "i"(128 + 4 * S_ + 3), "v"(c.qv), "s"(c.srdQ),
                 "s"(c.qs + (S_ >> 2) * 4096), "i"((S_ & 3) * 1024) : "memory");
  }
  __builtin_amdgcn_sched_barrier(0);
}
template <int R, bool LAST, int... Q> __device__ __forceinline__ void cv_step(CV& c, iseq<Q...>) { (cv_slot<R, Q, LAST>(c), ...); }
template <int... N> __device__ __forceinline__ void cv_rd0(CV& c, iseq<N...>) { (cv_rd<0, N>(c), ...); }
template <class HOOK>
__device__ __forceinline__ void conv_wave(int d, float Dd, bf16* __restrict__ zT, char* lw, __amdgpu_buffer_rsrc_t srdQ, unsigned qs,
                                          HOOK&& hook) {
  int lane = threadIdx.x & 63; asm volatile("" : "+v"(lane));
  const int l16 = lane & 15, fq = lane >> 4;
#pragma unroll
  for (int r = 0; r < 5; ++r) {
    const u32x4c w = *(const u32x4c*)(lw + r * 1024 + lane * 16);
    const unsigned w4 = *(const unsigned*)(lw + r * 1024 + lane * 16 + 16);
    *(u32x4c*)(lw + 10304 + r * 1024 + lane * 16) = (u32x4c){__builtin_amdgcn_alignbit(w[1], w[0], 16), __builtin_amdgcn_alignbit(w[2], w[1], 16),
                                                             __builtin_amdgcn_alignbit(w[3], w[2], 16), __builtin_amdgcn_alignbit(w4, w[3], 16)};
  }
  CV c;
#pragma unroll
  for (int i = 0; i < 4; ++i) c.pa[i] = (unsigned)(size_t)lw + ((l16 + 1) & 1) * 10304 + 4 * ((8 * fq + l16 + 1) >> 1) + 1024 * i;
  c.pb = (unsigned)(size_t)lw + 5120 + (2032 - 16 * l16 + 8 * fq) * 2 - 512 * 7;
  static_for([&](auto I) { asm volatile("v_accvgpr_write_b32 a[%c0], 0" ::"i"((int)I)); }, make_iseq<32>{});
  asm volatile("" ::: "memory");
  cv_rd0(c, make_iseq<24>{});
  __builtin_amdgcn_sched_barrier(0);
#pragma unroll 1
  for (int r = 0; r < 3; ++r) {
    cv_step<0, false>(c, make_iseq<36>{});
    cv_step<1, false>(c, make_iseq<36>{});
#pragma unroll
    for (int i = 0; i < 4; ++i) c.pa[i] += 128;
    c.pb += 128;
    hook(r);
    __builtin_amdgcn_sched_barrier(0);
  }
  c.qv = lane * 16; c.qs = __builtin_amdgcn_readfirstlane(qs); c.srdQ = srdQ;
  asm volatile("" : "+s"(c.srdQ), "+s"(c.qs));
  cv_step<0, true>(c, make_iseq<36>{});
  cv_step<1, true>(c, make_iseq<36>{});
  const __amdgpu_buffer_rsrc_t srdZ = __builtin_amdgcn_make_buffer_rsrc((void*)(zT + (size_t)d * SEQ), (short)0, SEQ * 2, 0x00020000);
  asm volatile("s_nop 7");
  static_for([&](auto Jc) __attribute__((always_inline)) {
    constexpr int J = Jc;
    hook(4 + J);
    __builtin_amdgcn_sched_barrier(0);
    const int t = 256 * J + 16 * l16 + 4 * fq;
    bf16x4 xv = *(const bf16x4*)(lw + 5120 + (2044 - t) * 2);
    const float y[4] = {acc_read1<4 * J>(), acc_read1<4 * J + 1>(), acc_read1<4 * J + 2>(), acc_read1<4 * J + 3>()};
    float z[4];
#pragma unroll
    for (int e = 0; e < 4; ++e) z[e] = rbf(y[e]) + rbf(bf2f(xv[3 - e]) * Dd);
    u32x2 w = {pk2(z[0], z[1]), pk2(z[2], z[3])};
    __builtin_amdgcn_raw_buffer_store_b64(w, srdZ, t * 2, 0, 16);
  }, make_iseq<8>{});
}

template <bool Z> __device__ __forceinline__ void post_dma(int j, int tb, const bf16* __restrict__ zT, const bf16* __restrict__ q, unsigned m0w) {
  int lane = threadIdx.x & 63; asm volatile("" : "+v"(lane));
  const __amdgpu_buffer_rsrc_t sz = __builtin_amdgcn_make_buffer_rsrc((void*)(zT + (size_t)j * 56 * SEQ + tb * 32), (short)0, 56 * SEQ * 2, 0x00020000);
  const __amdgpu_buffer_rsrc_t sq = __builtin_amdgcn_make_buffer_rsrc((void*)(q + (size_t)tb * 32 * DA + j * 56), (short)0, 32 * DA * 2, 0x00020000);
  const int tl = lane / 7, ck = lane - tl * 7;
  const unsigned vz = lane < 56 ? (ck * 8 + tl) * SEQ * 2 : 0x80000000u, vq = lane < 56 ? (tl * DA + ck * 8) * 2 : 0x80000000u;
#pragma unroll
  for (int i = 0; i < 4; ++i) if constexpr (Z)
    asm volatile("s_mov_b32 m0, %0\n\ts_nop 4\n\tbuffer_load_dwordx4 %1, %2, %3 offen sc1 lds" ::"s"(m0w + i * 1024), "v"(vz), "s"(sz), "s"(i * 16) : "memory");
#pragma unroll
  for (int i = 0; i < 4; ++i) if constexpr (!Z)
    asm volatile("s_mov_b32 m0, %0\n\ts_nop 4\n\tbuffer_load_dwordx4 %1, %2, %3 offen lds" ::"s"(m0w + 4096 + i * 1024), "v"(vq), "s"(sq), "s"(i * 8 * DA * 2) : "memory");
}
__device__ __forceinline__ void post_math(int j, int tb, bf16* __restrict__ out, const char* lw) {
  int lane = threadIdx.x & 63; asm volatile("" : "+v"(lane));
  const int tl = lane / 7, ck = lane - tl * 7;
  const __amdgpu_buffer_rsrc_t so = __builtin_amdgcn_make_buffer_rsrc((void*)(out + (size_t)tb * 32 * OUTW + j * 56), (short)0, 32 * OUTW * 2 - 2 * DA, 0x00020000);
  const unsigned vo = lane < 56 ? (tl * OUTW + ck * 8) * 2 : 0x80000000u;
  const char* lz = lw + ck * 16 + (tl & 7) * 2;
#pragma unroll
  for (int i = 0; i < 4; ++i) {
    const u32x4c qv = *(const u32x4c*)(lw + 4096 + i * 1024 + lane * 16);
    u32x4c o;
#pragma unroll
    for (int e = 0; e < 4; ++e) {
      const float z0 = bf2f(*(const unsigned short*)(lz + i * 1024 + 2 * e * 112));
      const float z1 = bf2f(*(const unsigned short*)(lz + i * 1024 + (2 * e + 1) * 112));
      o[e] = pk2(bfl(qv[e]) * z0, bfh(qv[e]) * z1);
    }
    __builtin_amdgcn_raw_buffer_store_b128(o, so, vo, i * 8 * OUTW * 2, 0);
  }
}

#define DI __device__ __forceinline__
#define GAP() __builtin_amdgcn_sched_barrier(0)
typedef __attribute__((ext_vector_type(4))) float f32x4g;
typedef __attribute__((ext_vector_type(4))) unsigned u32x4g;
constexpr int LDW = NH * HD;
struct GM {
  unsigned aA[2], aB[2][8];
  unsigned voA, voB, vbA, vbB, kA, kW, kB;
  u64 tab;
  __amdgpu_buffer_rsrc_t srdA, srdB;
  unsigned sA[8], mA, mB;
};
template <int P, int KS, int I> DI void g_wread(GM& g) {
  constexpr int nf = I >> 1, hf = I & 1, r = 128 + 64 * P + 8 * nf + 4 * KS + 2 * hf;
  asm volatile("ds_read_b64_tr_b16 a[%c1:%c2], %0 offset:%c3" ::"v"(g.aB[hf][nf]), "i"(r), "i"(r + 1), "i"(P * 32768 + KS * 16384));
}
template <int P, int M, int KS> DI void g_aread(GM& g) {
  constexpr int r = 96 + 8 * (M & 1) + 4 * KS;
  asm volatile("ds_read_b128 v[%c1:%c2], %0 offset:%c3" ::"v"(g.aA[KS]), "i"(r), "i"(r + 3), "i"(P * 32768 + M * 2048));
}
template <bool TR, int P, int H, int G, bool FIRST> DI void g_gap(GM& g) {
  constexpr int m = 4 * H + (G >> 4), q = G & 15, n = q >> 1, ks = q & 1, F = m * 8 + n;
  constexpr int a = 96 + 8 * (m & 1) + 4 * ks, b = 128 + 64 * P + 8 * n + 4 * ks, d = m < 4 ? 128 + 4 * F : 4 * (F - 32);
  constexpr int J = q == 8 ? 2 * (G >> 4) : q == 12 ? 2 * (G >> 4) + 1 : -1, gj = (J & 3) ^ ((J & 3) >> 1);
  if constexpr (q == 0 && G > 0) asm volatile("s_waitcnt lgkmcnt(4)");
  if constexpr (J == 0) asm volatile("s_add_u32 m0, %0, %1" ::"s"(g.mA), "i"((P ^ H ^ 1) * 32768 + (1 - H) * 8192) : "scc");
  else if constexpr (J == 4) asm volatile("s_add_u32 m0, %0, %1" ::"s"(g.mB), "i"((P ^ H) * 32768 + (1 - H) * 16384) : "scc");
  else if constexpr (J > 0 && (J & 3) == 1) asm volatile("s_bitset1_b32 m0, 12");
  else if constexpr (J > 0 && (J & 3) == 2) asm volatile("s_bitset1_b32 m0, %0" ::"i"(J < 4 ? 14 : 13));
  else if constexpr (J > 0) asm volatile("s_bitset0_b32 m0, 12");
#define GMF(D, X, Y, C) asm volatile("v_mfma_f32_16x16x32_bf16 " D "[%c0:%c1], " X ", " Y ", " C ::"i"(d), "i"(d + 3), "i"(a), "i"(a + 3), "i"(b), "i"(b + 3))
#define GMF2(D, C) do { if constexpr (TR) GMF(D, "v[%c2:%c3]", "a[%c4:%c5]", C); else GMF(D, "a[%c4:%c5]", "v[%c2:%c3]", C); } while (0)
  if constexpr (FIRST && ks == 0) { if constexpr (m < 4) GMF2("v", "0"); else GMF2("a", "0"); }
  else if constexpr (m < 4) GMF2("v", "v[%c0:%c1]");
  else GMF2("a", "a[%c0:%c1]");
#undef GMF2
#undef GMF
#define GD(VO, SRD, SO) asm volatile("buffer_load_dwordx4 %0, %1, %2 offen lds" ::"v"(VO), "s"(SRD), "s"(SO) : "memory")
  if constexpr (J >= 0 && J < 4) GD(g.voA, g.srdA, g.sA[(1 - H) * 2 + (gj & 1) + (gj >> 1) * 4]);
  else if constexpr (J >= 4) GD(g.voB, g.srdB, ((1 - H) * 32 + 16 * (gj & 1)) * LDW * 2 + (gj >> 1) * 256);
#undef GD
  if constexpr (q == 1 || q == 2) g_aread<(m < 7 ? P : P ^ 1), (m + 1) & 7, q - 1>(g);
  if constexpr (q >= 3 && q < 11 && (q & 1)) g_wread<P ^ 1, H, (G >> 4) * 4 + (q - 3) / 2>(g);
  if constexpr (H == 1 && G == 0) asm volatile("s_mov_b32 %0, %1" : "=s"(g.kA) : "s"(g.kW));
  if constexpr (H == 1 && G == 4) asm volatile("v_lshl_add_u32 %0, %2, 7, %3\n\ts_bfe_u32 %1, %4, 0x40000" : "=v"(g.voA), "=s"(g.kW) : "s"(g.kA), "v"(g.vbA), "s"((unsigned)g.tab) : "scc");
  if constexpr (H == 1 && G == 6) asm volatile("s_lshr_b64 %0, %0, 4\n\ts_lshl_b32 %1, %2, 19" : "+s"(g.tab), "=&s"(g.kB) : "s"(g.kW) : "scc");
  if constexpr (H == 1 && G == 10) asm volatile("v_add_u32 %0, %1, %2" : "=v"(g.voB) : "s"(g.kB), "v"(g.vbB));
  if constexpr (G == 61) asm volatile("s_waitcnt vmcnt(12) lgkmcnt(0)\n\ts_barrier" ::: "memory");
  GAP();
}
template <bool TR, int P, int H, bool FIRST, int... Gs> DI void g_half_(GM& g, iseq<Gs...>) { (g_gap<TR, P, H, Gs, FIRST>(g), ...); }
template <bool TR, int P, int H, bool FIRST> DI void g_half(GM& g) { g_half_<TR, P, H, FIRST>(g, make_iseq<64>{}); }
template <int P, int... Is> DI void g_read0(GM& g, iseq<Is...>) {
  (g_wread<P, 0, Is>(g), ...); (g_wread<P, 1, Is>(g), ...);
  g_aread<P, 0, 0>(g); g_aread<P, 0, 1>(g);
}
struct GItem { const bf16* A; const bf16* W; int lda; u64 tab; };
DI void g_srcA(GM& g, const GItem& it, int wid, int lane) {
  g.srdA = __builtin_amdgcn_make_buffer_rsrc((void*)it.A, (short)0, it.A ? 256 * it.lda * 2 : 0, 0x00020000);
  g.vbA = ((wid * 8 + (lane >> 3)) * it.lda + ((lane & 7) ^ (((wid & 1) * 4 + (lane >> 4)) & 7)) * 8) * 2;
#pragma unroll
  for (int j = 0; j < 8; ++j) g.sA[j] = j * 64 * it.lda;
  if (it.A == nullptr) g.mA = 0x28000;
}
DI void g_srcB(GM& g, const GItem& it) {
  g.srdB = __builtin_amdgcn_make_buffer_rsrc((void*)it.W, (short)0, it.A ? (it.lda - 1) * LDW * 2 + 512 : 0, 0x00020000);
  g.tab = it.tab;
  if (it.A == nullptr) g.mB = 0x28000;
}
DI void g_fix(GM& g) {
  g.tab = ((u64)(unsigned)__builtin_amdgcn_readfirstlane((unsigned)(g.tab >> 32)) << 32) | (unsigned)__builtin_amdgcn_readfirstlane((unsigned)g.tab);
  g.mA = __builtin_amdgcn_readfirstlane(g.mA); g.mB = __builtin_amdgcn_readfirstlane(g.mB); g.kW = __builtin_amdgcn_readfirstlane(g.kW);
#pragma unroll
  for (int j = 1; j < 8; ++j) g.sA[j] = __builtin_amdgcn_readfirstlane(g.sA[j]);
}
template <int I> DI void g_pro1(GM& g, unsigned po) {
  constexpr int grp = I >> 2, i = I & 3, t = grp < 4 ? 0 : grp < 7 ? 1 : 2;
  constexpr bool isA = grp == 0 || grp == 3 || grp == 5;
  const unsigned k = ((unsigned)g.tab >> (4 * t)) & 15, so = t == 1 ? po ^ 32768 : po;
  if constexpr (isA) {
    constexpr int jj = (grp == 3 ? 2 : 0) + (i & 1) + (i >> 1) * 4;
    DMA16(g.mA + so + jj * 4096, g.vbA, g.srdA, g.sA[jj] + k * 128);
  } else {
    constexpr int ks = grp == 2 || grp == 6;
    DMA16(g.mB + so + ks * 16384 + (i >> 1) * 8192 + (i & 1) * 4096, g.vbB, g.srdB, k * 0x80000 + (ks * 32 + 16 * (i & 1)) * LDW * 2 + (i >> 1) * 256);
  }
}
template <int B, int... Is> DI void g_pro(GM& g, unsigned po, iseq<Is...>) { (g_pro1<B + Is>(g, po), ...); }
constexpr float CS = 0.0625f * 1.4426950408889634f;
template <bool SCL, int R> DI unsigned g_pack() {
  if constexpr (R < 128 && !SCL) {
    unsigned u;
    asm volatile("v_cvt_pk_bf16_f32 %0, v%c1, v%c2" : "=v"(u) : "i"(128 + R), "i"(129 + R));
    return u;
  } else if constexpr (R < 128) {
    float x, y;
    asm volatile("v_mul_f32 %0, %2, v%c3\n\tv_mul_f32 %1, %2, v%c4" : "=&v"(x), "=&v"(y) : "s"(CS), "i"(128 + R), "i"(129 + R));
    return pk2(x, y);
  } else {
    float x = acc_read1<R - 128>(), y = acc_read1<R - 127>();
    if constexpr (SCL) { x *= CS; y *= CS; }
    return pk2(x, y);
  }
}
template <bool TR, bool SCL> DI void g_epilogue(bf16* out, int wid, int lane) {
  asm volatile("s_nop 7");
  GAP();
  const auto rs = __builtin_amdgcn_make_buffer_rsrc((void*)out, 0, 131072, 0x00020000);
  const int vo = (wid >> 1) * 65536 + (wid & 1) * (TR ? 8192 : 4096) + lane * 16;
  static_for([&](auto I) {
    constexpr int i = I, fx = TR ? (i >> 3) * 16 + (i & 7) : 2 * i, fy = TR ? fx + 8 : fx + 1;
    const u32x4g w = {g_pack<SCL, 4 * fx>(), g_pack<SCL, 4 * fx + 2>(), g_pack<SCL, 4 * fy>(), g_pack<SCL, 4 * fy + 2>()};
    __builtin_amdgcn_raw_buffer_store_b128(w, rs, vo, TR ? (i >> 3) * 16384 + (i & 7) * 1024 : (i >> 2) * 8192 + (i & 3) * 1024, 16);
  }, make_iseq<32>{});
  GAP();
}

typedef __attribute__((ext_vector_type(4))) unsigned u32x4;
typedef __attribute__((ext_vector_type(4))) float f32x4;

constexpr int R_S = 128, R_P = 192, R_CI = 224, R_E = 96;
constexpr int R_TS = 112, R_RS = 114, R_M = 116;
constexpr int R_PD = 118, R_THR = 120, R_TMP = 121;
constexpr int r_dk(int q) { return q < 6 ? 90 + q : 120 + q; }
constexpr int S_V = 96, S_T = 97, S_K = 98, S_I = 99, S_EV = 100;
DI void set_srdk(const void* p, unsigned n) {
  asm volatile("s_mov_b64 s[92:93], %0\n\ts_mov_b32 s94, %1\n\ts_mov_b32 s95, 0x20000" ::"s"(p), "s"(n));
}
struct AC {
  unsigned ka, va, dw[2], vq;
  __amdgpu_buffer_rsrc_t sw;
  unsigned m0x[2];
  u64 redo;
  unsigned rare;
  int mrow;
  bool diag0;
};
template <int X, int F> DI void kread(AC& c) {
  constexpr int r = 192 + 4 * (F & 15);
  asm volatile("ds_read_b128 a[%c1:%c2], %0 offset:%c3" ::"v"(c.ka), "i"(r), "i"(r + 3), "i"(X * 32768 + F * 1024));
}
template <int X, int F> DI void vread(AC& c) {
  constexpr int r = 192 + 4 * (F & 15);
  asm volatile("ds_read_b128 a[%c1:%c2], %0 offset:%c3" ::"v"(c.va), "i"(r), "i"(r + 3), "i"(X * 32768 + (F & 1) * 16384 + (F >> 1) * 1024));
}
template <int X, int... Fs> DI void kreads(AC& c, iseq<Fs...>) { (kread<X, Fs>(c), ...); }
template <int X, int J> DI void dma_m0(AC& c) {
  if constexpr (J == 0) asm volatile("s_mov_b32 m0, %0" ::"s"(c.m0x[X]));
  else if constexpr (J == 4) asm volatile("s_or_b32 m0, %0, 0x1000" ::"s"(c.m0x[X]) : "scc");
  else if constexpr (J == 8) asm volatile("s_bitset0_b32 m0, 16");
  else if constexpr (J == 12) asm volatile("s_bitset0_b32 m0, 12");
}
template <int J> DI void dma_at(AC& c) {
  if constexpr (J < 8)
    asm volatile("buffer_load_dwordx4 %0, %1, s96 offen offset:%2 sc1 lds" ::"v"(c.dw[J >= 4]), "s"(c.sw), "i"((J & 3) * 1024) : "memory");
  else
    asm volatile("buffer_load_dwordx4 v%c0, s[92:95], s98 offen offset:%1 sc1 lds" ::"i"(r_dk(J - 8)), "i"((J & 3) * 1024) : "memory");
}
DI void dma4k(const AC& c, unsigned so, unsigned m0v, int hf) {
    asm volatile("s_mov_b32 m0, %0\n\ts_nop 4\n\tbuffer_load_dwordx4 %1, %2, %3 offen sc1 lds\n\tbuffer_load_dwordx4 %1, %2, %3 offen offset:1024 sc1 lds\n\t"
                 "buffer_load_dwordx4 %1, %2, %3 offen offset:2048 sc1 lds\n\tbuffer_load_dwordx4 %1, %2, %3 offen offset:3072 sc1 lds"
                 ::"s"(m0v + hf * 4096), "v"(c.dw[hf]), "s"(c.sw), "s"(so) : "memory");
}
DI void dma8k(const AC& c, unsigned so, unsigned m0v) { dma4k(c, so, m0v, 0); dma4k(c, so, m0v, 1); }
template <int S_> DI void q_load1(unsigned vo, __amdgpu_buffer_rsrc_t srd, unsigned so) {
  asm volatile("buffer_load_dwordx4 a[%c0:%c1], %2, %3, %4 offen offset:%5 sc1" ::"i"(128 + 4 * S_), "i"(128 + 4 * S_ + 3), "v"(vo), "s"(srd),
               "s"(so + (S_ >> 2) * 4096), "i"((S_ & 3) * 1024) : "memory");
}
template <int... Ss> DI void q_load(unsigned vo, __amdgpu_buffer_rsrc_t srd, unsigned so, iseq<Ss...>) {
  so = __builtin_amdgcn_readfirstlane(so);
  asm volatile("s_nop 4" : "+s"(srd), "+s"(so));
  (q_load1<Ss>(vo, srd, so), ...);
}
template <int Q> DI void acc_zero2() {
  asm volatile("v_accvgpr_write_b32 a[%c0], 0\n\tv_accvgpr_write_b32 a[%c1], 0" ::"i"(2 * Q), "i"(2 * Q + 1));
}
struct SMS { int ex[128], op0[128], nop[128], last; };
constexpr SMS sm_sched() {
  SMS z{};
  int et[32] = {}, e = 0;
  for (int t = 0; t < 128; ++t) {
    const int g = t & 15;
    const bool x = t < 48 ? (g >= 8 && (g & 1)) : (g >= 8 && g != 10 && g != 13 && g != 15);
    z.ex[t] = x && e < 32 ? e : -1;
    if (z.ex[t] >= 0) et[e++] = t;
  }
  int o = 0;
  for (int t = 0; t < 128; ++t) {
    const int g = t & 15;
    int cap = g < 8 ? (g == 0 ? 0 : 1) : (z.ex[t] >= 0 || t < 48 ? 0 : 2);
    z.op0[t] = o; z.nop[t] = 0;
    while (cap > 0 && o < 48 && et[2 * (o / 3) + (o % 3 == 0 ? 0 : 1)] + 2 <= t) {
      const int el = 2 * (o / 3) + o % 3;
      if (!(o % 3 < 2 && (el == 0 || el == 4))) --cap;
      ++o; ++z.nop[t]; z.last = t;
    }
  }
  return z;
}
constexpr SMS SM = sm_sched();
static_assert(SM.last <= 115);
constexpr int p_reg(int Y, int k) { return R_P + 16 * Y + 4 * ((k >> 3) * 2 + ((k >> 1) & 1)) + ((k >> 2) & 1) * 2 + (k & 1); }
template <int Y, int O> DI void sm_op() {
  constexpr int k = O / 3, e = 2 * k + O % 3, ts = R_TS + ((e >> 2) & 1);
  if constexpr (O % 3 == 2) asm volatile("v_cvt_pk_bf16_f32 v%c0, v%c1, v%c2" ::"i"(p_reg(Y, k)), "i"(R_E + ((2 * k) & 15)), "i"(R_E + ((2 * k + 1) & 15)));
  else if constexpr (e == 1 || e == 5) asm volatile("v_add_f32_e32 v%c0, v%c1, v%c2" ::"i"(ts), "i"(R_E + e - 1), "i"(R_E + e));
  else if constexpr (e != 0 && e != 4) asm volatile("v_add_f32_e32 v%c0, v%c0, v%c1" ::"i"(ts), "i"(R_E + (e & 15)));
}
template <int Y, int T> DI void sm_gap(AC& c) {
  if constexpr (SM.ex[T] >= 0) asm volatile("v_exp_f32 v%c0, v%c1" ::"i"(R_E + (SM.ex[T] & 15)), "i"(R_S + 32 * Y + SM.ex[T]));
  if constexpr (SM.nop[T] > 0) sm_op<Y, SM.op0[T]>();
  if constexpr (SM.nop[T] > 1) sm_op<Y, SM.op0[T] + 1>();
  if constexpr (SM.nop[T] > 2) sm_op<Y, SM.op0[T] + 2>();
  if constexpr (T == SM.last + 1) asm volatile("v_add_f32_e32 v%c0, v%c1, v%c2" ::"i"(R_TMP), "i"(R_TS), "i"(R_TS + 1));
  if constexpr (T == SM.last + 2) asm volatile("v_cmp_nle_f32_e64 %0, v%c1, v%c2" : "=s"(c.redo) : "i"(R_TMP), "i"(R_THR));
}
template <int X, bool FIRST, bool NOQK, int G> DI void gapA(AC& c) {
  constexpr int Xp = X ^ 1, kb = G >> 4, g = G & 15, qb = g >> 3, s = g & 7;
  if constexpr (NOQK && G < 48 && G % 3 == 0) q_load1<G / 3>(c.vq, c.sw, c.rare);
  if constexpr (G == 48) asm volatile("s_waitcnt vmcnt(%0) lgkmcnt(0)\n\ts_barrier" ::"i"(NOQK ? 16 : 0) : "memory");
  else if constexpr (g == 0 && !NOQK) asm volatile("s_waitcnt lgkmcnt(0)");
  if constexpr (!NOQK) {
    constexpr int r = 192 + 4 * ((kb & 1) * 8 + s), q = 128 + 4 * g;
    constexpr int d = R_S + 32 * X + 4 * (kb * 2 + qb), ci = s == 0 ? R_CI + 4 * (kb * 2 + qb) : d;
    if constexpr (FIRST && s == 0)
      asm volatile("v_mfma_f32_16x16x32_bf16 v[%c0:%c1], a[%c2:%c3], a[%c4:%c5], 0" ::"i"(d), "i"(d + 3), "i"(r), "i"(r + 3), "i"(q), "i"(q + 3));
    else
      asm volatile("v_mfma_f32_16x16x32_bf16 v[%c0:%c1], a[%c2:%c3], a[%c4:%c5], v[%c6:%c7]" ::"i"(d), "i"(d + 3), "i"(r), "i"(r + 3), "i"(q), "i"(q + 3),
                   "i"(ci), "i"(ci + 3));
    if constexpr (g < 8 && kb < 3) kread<X, (kb + 1) * 8 + g>(c);
    if constexpr (G == 54) asm volatile("s_add_u32 s96, s97, 0x1000000" ::: "scc");
    if constexpr (G == 55) dma_m0<X, 0>(c);
    if constexpr (G >= 56 && !(G & 1)) dma_at<(G - 56) / 2>(c);
    if constexpr (G == 57) asm volatile("s_mov_b32 s97, s98\n\ts_lshr_b32 s100, s100, 1" ::: "scc");
    if constexpr (G == 63) asm volatile("s_add_u32 s98, s98, s99" ::: "scc");
    if constexpr (G == 59 && !FIRST) asm volatile("s_and_b32 s101, s100, 1\n\ts_or_b32 s101, s101, %0" ::"s"((unsigned)c.redo) : "scc");
    if constexpr (G == 61 && !FIRST) asm volatile("s_or_b32 %0, s101, %1" : "=s"(c.rare) : "s"((unsigned)(c.redo >> 32)) : "scc");
  } else if constexpr (SM.nop[64 + G] > 0) asm volatile("s_nop 0");
  if constexpr (!FIRST && g < 8 && kb == 3) vread<Xp, g>(c);
  if constexpr (FIRST) acc_zero2<G>();
  else sm_gap<Xp, 64 + G>(c);
  GAP();
}
template <int R> DI void rescale1(float alpha) {
  float t;
  asm volatile("v_accvgpr_read_b32 %0, a[%c2]\n\tv_mul_f32 %0, %0, %1\n\tv_accvgpr_write_b32 a[%c2], %0" : "=&v"(t) : "v"(alpha), "i"(R));
}
template <int... Rs> DI void rescale_all(const float (&alpha)[2], iseq<Rs...>) { (rescale1<Rs>(alpha[(Rs >> 2) & 1]), ...); }
template <bool MAX> DI float row_reduce(float v) {
  auto p = __builtin_amdgcn_permlane32_swap(__float_as_uint(v), __float_as_uint(v), false, false);
  v = MAX ? fmaxf(__uint_as_float(p[0]), __uint_as_float(p[1])) : __uint_as_float(p[0]) + __uint_as_float(p[1]);
  p = __builtin_amdgcn_permlane16_swap(__float_as_uint(v), __float_as_uint(v), false, false);
  return MAX ? fmaxf(__uint_as_float(p[0]), __uint_as_float(p[1])) : __uint_as_float(p[0]) + __uint_as_float(p[1]);
}
template <int Y, int QB> DI float row_max() {
  constexpr int r = R_S + 32 * Y + 4 * QB;
  float mx;
  asm volatile("v_max_f32 %0, v%c1, v%c2\n\tv_max3_f32 %0, %0, v%c3, v%c4" : "=&v"(mx) : "i"(r), "i"(r + 1), "i"(r + 2), "i"(r + 3));
  static_for([&](auto K) {
    constexpr int q = r + 8 * (K + 1);
    float t = mx;
    asm volatile("v_max3_f32 %0, %0, v%c1, v%c2\n\tv_max3_f32 %0, %0, v%c3, v%c4" : "+v"(t) : "i"(q), "i"(q + 1), "i"(q + 2), "i"(q + 3));
    mx = t;
  }, make_iseq<3>{});
  return row_reduce<true>(mx);
}
template <int R> DI void v_sub(float d) { asm volatile("v_sub_f32 v%c0, v%c0, %1" ::"i"(R), "v"(d)); }
template <int B> DI void s_shift(const float (&d)[2]) { static_for([&](auto E) { v_sub<B + E>(d[(E >> 2) & 1]); }, make_iseq<32>{}); }
DI void m_shift(const float (&d)[2]) {
  s_shift<R_CI>(d);
  v_sub<R_M>(-d[0]); v_sub<R_M + 1>(-d[1]);
}
template <int B, bool INPL> DI void set_mask(const AC& c, int t64) {
  const int mr = c.mrow - t64;
  const float ninf = -INFINITY;
  static_for([&](auto I) {
    constexpr int i = I, r = B + 4 * i, k = (i >> 1) * 16 - (i & 1) * 16;
    const int mr_ = mr; const float b = ninf;
    if constexpr (INPL)
      asm volatile("v_cmp_gt_i32 vcc, %c4, %8\n\tv_cndmask_b32 v%c0, v%c0, %9, vcc\n\tv_cmp_gt_i32 vcc, %c5, %8\n\tv_cndmask_b32 v%c1, v%c1, %9, vcc\n\t"
                   "v_cmp_gt_i32 vcc, %c6, %8\n\tv_cndmask_b32 v%c2, v%c2, %9, vcc\n\tv_cmp_gt_i32 vcc, %c7, %8\n\tv_cndmask_b32 v%c3, v%c3, %9, vcc"
                   ::"i"(r), "i"(r + 1), "i"(r + 2), "i"(r + 3), "i"(k), "i"(k + 1), "i"(k + 2), "i"(k + 3), "v"(mr_), "v"(b) : "vcc");
    else
      asm volatile("v_cmp_gt_i32 vcc, %c4, %8\n\tv_cndmask_b32_e64 v%c0, -v%c10, %9, vcc\n\tv_cmp_gt_i32 vcc, %c5, %8\n\tv_cndmask_b32_e64 v%c1, -v%c10, %9, vcc\n\t"
                   "v_cmp_gt_i32 vcc, %c6, %8\n\tv_cndmask_b32_e64 v%c2, -v%c10, %9, vcc\n\tv_cmp_gt_i32 vcc, %c7, %8\n\tv_cndmask_b32_e64 v%c3, -v%c10, %9, vcc"
                   ::"i"(r), "i"(r + 1), "i"(r + 2), "i"(r + 3), "i"(k), "i"(k + 1), "i"(k + 2), "i"(k + 3), "v"(mr_), "v"(b), "i"(R_M + (i & 1)) : "vcc");
  }, make_iseq<8>{});
  asm volatile("s_nop 1");
}
template <int X> DI void sm_redo() {
  constexpr int Xp = X ^ 1;
  float d[2], dt[2], alpha[2], ts[2] = {0.f, 0.f};
  static_for([&](auto Q) {
    constexpr int qb = Q;
    const float pd = vget<R_PD + qb>();
    d[qb] = fmaxf(row_max<Xp, qb>() - pd, 0.f);
    dt[qb] = d[qb] + pd;
    vset<R_PD + qb>(d[qb]);
    alpha[qb] = __builtin_amdgcn_exp2f(-d[qb]);
    vset<R_RS + qb>(vget<R_RS + qb>() * alpha[qb]);
  }, make_iseq<2>{});
  vset<R_THR>(d[0] > 0.f || d[1] > 0.f ? -1.f : 4096.f);
  m_shift(d);
  static_for([&](auto K) {
    constexpr int k = K, qb = (k >> 1) & 1;
    const float e0 = __builtin_amdgcn_exp2f(vget<R_S + 32 * Xp + 2 * k>() - dt[qb]), e1 = __builtin_amdgcn_exp2f(vget<R_S + 32 * Xp + 2 * k + 1>() - dt[qb]);
    ts[qb] += e0; ts[qb] += e1;
    vset<p_reg(Xp, k)>(__uint_as_float(pk2(e0, e1)));
  }, make_iseq<16>{});
  vset<R_TS>(ts[0]); vset<R_TS + 1>(ts[1]);
  rescale_all(alpha, make_iseq<128>{});
  asm volatile("s_nop 1" ::: "memory");
  GAP();
}
template <int X, bool FIRST, bool NOQK, int... Gs> DI void phaseA_(AC& c, iseq<Gs...>) {
  (gapA<X, FIRST, NOQK, Gs>(c), ...);
}
template <int X, bool FIRST, bool NOQK> DI void phaseA(AC& c) { phaseA_<X, FIRST, NOQK>(c, make_iseq<64>{}); }

template <int X, bool SMX, int G> DI void gapB(AC& c) {
  constexpr int Xp = X ^ 1, blk = G >> 4, g = G & 15;
  if constexpr (g == 0) asm volatile("s_waitcnt lgkmcnt(0)");
  if constexpr (SMX && g == 7 && blk < 3) dma_m0<X, blk * 4 + 4>(c);
  constexpr int qb = (g >> 1) & 1, s = g & 1, r = 192 + 4 * ((blk & 1) * 8 + (g >> 2) * 2 + s), o = 4 * ((blk * 4 + (g >> 2)) * 2 + qb);
  constexpr int p = R_P + 16 * Xp + 4 * (s * 2 + qb);
  asm volatile("v_mfma_f32_16x16x32_bf16 a[%c0:%c1], a[%c2:%c3], v[%c4:%c5], a[%c0:%c1]" ::"i"(o), "i"(o + 3), "i"(r), "i"(r + 3), "i"(p), "i"(p + 3));
  if constexpr (SMX && (G == 1 || G == 2)) asm volatile("v_add_f32_e32 v%c0, v%c0, v%c1" ::"i"(R_RS + G - 1), "i"(R_TS + G - 1));
  if constexpr (g < 8) {
    if constexpr (blk < 3) vread<Xp, (blk + 1) * 8 + g>(c);
    else if constexpr (SMX) kread<Xp, g>(c);
  }
  if constexpr (SMX) {
    if constexpr (g >= 8 && !(g & 1) && blk < 3) dma_at<blk * 4 + 4 + (g - 8) / 2>(c);
    sm_gap<X, G>(c);
  }
  GAP();
}
template <int X, int... Gs> DI void phaseB_(AC& c, iseq<Gs...>) { (gapB<X, true, Gs>(c), ...); }
template <int X> DI void phaseB(AC& c) { phaseB_<X>(c, make_iseq<64>{}); }
constexpr int sm_nex() { int n = 0; for (int t = 0; t < 64; ++t) n += SM.ex[t] >= 0; return n; }
DI void phaseB0(AC& c) {
  dma_m0<0, 4>(c);
  kreads<1>(c, make_iseq<8>{});
  dma_at<4>(c);
  asm volatile("s_nop 3");
  if (__builtin_expect(c.diag0, 0)) set_mask<R_S, true>(c, 0);
  float d[2];
  d[0] = row_max<0, 0>(); GAP(); dma_at<5>(c);
  d[1] = row_max<0, 1>(); GAP(); dma_at<6>(c);
#pragma unroll
  for (int qb = 0; qb < 2; ++qb) if (!(d[qb] > -3.0e38f)) d[qb] = 0.f;
  vset<R_M>(d[0]); vset<R_M + 1>(d[1]);
  const f32x2c nd[2] = {{-d[0], -d[0]}, {-d[1], -d[1]}};
  static_for([&](auto E) {
    constexpr int e = E;
    const f32x2c v = nd[(e >> 1) & 1];
    asm volatile("v_pk_add_f32 v[%c0:%c1], v[%c0:%c1], %4\n\tv_mov_b64 v[%c2:%c3], %4" ::"i"(R_S + 2 * e), "i"(R_S + 2 * e + 1), "i"(R_CI + 2 * e), "i"(R_CI + 2 * e + 1), "v"(v));
    if constexpr (e == 3) dma_at<7>(c);
    if constexpr (e == 6) dma_m0<0, 8>(c);
    if constexpr (e == 7 || e == 11 || e == 15) dma_at<8 + (e - 7) / 4>(c);
  }, make_iseq<16>{});
  if (__builtin_expect(c.diag0, 0)) set_mask<R_CI, false>(c, 64);
  static_assert(sm_nex() >= 16 && SM.op0[64] >= 3 * ((sm_nex() - 15) / 2) && SM.op0[64] <= 24);
  static_for([&](auto E) {
    constexpr int e = E;
    asm volatile("v_exp_f32 v%c0, v%c1" ::"i"(R_E + e), "i"(R_S + e));
    if constexpr (e == 3) dma_at<11>(c);
    if constexpr (e == 6) dma_m0<0, 12>(c);
    if constexpr (e == 7 || e == 11 || e == 15) dma_at<12 + (e - 7) / 4>(c);
  }, make_iseq<16>{});
  static_for([&](auto O) { sm_op<0, O>(); }, make_iseq<SM.op0[64]>{});
  dma_at<15>(c);
  static_for([&](auto E) { asm volatile("v_exp_f32 v%c0, v%c1" ::"i"(R_E + (int)E), "i"(R_S + 16 + (int)E)); }, make_iseq<sm_nex() - 16>{});
  GAP();
}

#define ACLOB_ALL "a0","a1","a2","a3","a4","a5","a6","a7","a8","a9","a10","a11","a12","a13","a14","a15","a16","a17","a18","a19","a20","a21","a22","a23","a24","a25","a26","a27","a28","a29","a30","a31","a32","a33","a34","a35","a36","a37","a38","a39","a40","a41","a42","a43","a44","a45","a46","a47","a48","a49","a50","a51","a52","a53","a54","a55","a56","a57","a58","a59","a60","a61","a62","a63","a64","a65","a66","a67","a68","a69","a70","a71","a72","a73","a74","a75","a76","a77","a78","a79","a80","a81","a82","a83","a84","a85","a86","a87","a88","a89","a90","a91","a92","a93","a94","a95","a96","a97","a98","a99","a100","a101","a102","a103","a104","a105","a106","a107","a108","a109","a110","a111","a112","a113","a114","a115","a116","a117","a118","a119","a120","a121","a122","a123","a124","a125","a126","a127","a128","a129","a130","a131","a132","a133","a134","a135","a136","a137","a138","a139","a140","a141","a142","a143","a144","a145","a146","a147","a148","a149","a150","a151","a152","a153","a154","a155","a156","a157","a158","a159","a160","a161","a162","a163","a164","a165","a166","a167","a168","a169","a170","a171","a172","a173","a174","a175","a176","a177","a178","a179","a180","a181","a182","a183","a184","a185","a186","a187","a188","a189","a190","a191","a192","a193","a194","a195","a196","a197","a198","a199","a200","a201","a202","a203","a204","a205","a206","a207","a208","a209","a210","a211","a212","a213","a214","a215","a216","a217","a218","a219","a220","a221","a222","a223","a224","a225","a226","a227","a228","a229","a230","a231","a232","a233","a234","a235","a236","a237","a238","a239","a240","a241","a242","a243","a244","a245","a246","a247","a248","a249","a250","a251","a252","a253","a254","a255"

struct EP { float fa[2], fb[2]; int vo; unsigned so[4]; };
template <int I, int E, bool MERGE> DI void epi_pair(const EP& p) {
  constexpr int qb = I & 1, r = 16 * (I >> 1) + 4 * qb + (E >> 1) * 8 + (E & 1) * 2, t = 96 + 2 * E, w = 128 + 4 * I + E;
  if constexpr (MERGE) asm volatile("v_accvgpr_read_b32 v106, a[%c0]" ::"i"(w));
  asm volatile("v_accvgpr_read_b32 v%c0, a[%c2]\n\tv_accvgpr_read_b32 v%c1, a[%c3]\n\tv_mul_f32 v%c0, v%c0, %4\n\tv_mul_f32 v%c1, v%c1, %4"
               ::"i"(t), "i"(t + 1), "i"(r), "i"(r + 1), "v"(p.fa[qb]));
  if constexpr (MERGE)
    asm volatile("v_lshlrev_b32 v104, 16, v106\n\tv_and_b32 v105, 0xffff0000, v106\n\tv_fmac_f32 v%c0, v104, %2\n\tv_fmac_f32 v%c1, v105, %2"
                 ::"i"(t), "i"(t + 1), "v"(p.fb[qb]));
  asm volatile("v_cvt_pk_bf16_f32 v%c0, v%c1, v%c2" ::"i"(w), "i"(t), "i"(t + 1));
}
template <int I> DI void epi_store(const AC& c, const EP& p) {
  asm volatile("buffer_store_dwordx4 v[%c0:%c1], %2, %3, %4 offen offset:%5 sc1" ::"i"(128 + 4 * I), "i"(131 + 4 * I), "v"(p.vo), "s"(c.sw),
               "s"(p.so[I >> 2]), "i"((I & 3) * 1024) : "memory");
}
constexpr int R_TB = 107, R_VS = 111;
template <int HF, int R> DI void p_read() {
  constexpr int mb = R >> 4, r = 64 * HF + 2 * R;
  asm volatile("ds_read_b64_tr_b16 a[%c1:%c2], v%c0 offset:%c3" ::"i"(R_TB + 2 * HF + mb), "i"(r), "i"(r + 1), "i"((R & 15) * 2048));
}
template <int QB, int E> DI void p_cvt() {
  asm volatile("v_cvt_pk_bf16_f32 v%c0, v%c1, v%c2" ::"i"(208 + 4 * QB + E), "i"(192 + 8 * QB + 2 * E), "i"(193 + 8 * QB + 2 * E));
}
template <int QB, int PR> DI void p_store(const AC& c) {
  asm volatile("buffer_store_dwordx4 v[%c0:%c1], v%c2, %3, s96 offen offset:%4 sc1" ::"i"(208 + 4 * QB), "i"(211 + 4 * QB), "i"(R_VS + QB), "s"(c.sw),
               "i"(PR * 128) : "memory");
}
constexpr int p_dma(int HF, int G) { return G == 3 && !HF ? 0 : G == 11 ? 1 - HF : G == 15 ? 2 - HF : G == 19 ? 3 - HF : G == 27 && HF ? 3 : -1; }
template <int HF, int G> DI void p_gap(AC& c) {
  constexpr int ch = G >> 3, t = G & 7, a = 64 * HF + 32 * (ch & 1) + 4 * t, b = 128 + 4 * (2 * t + (ch >> 1)), d = 192 + 4 * ch;
  if constexpr (t == 0)
    asm volatile("v_mfma_f32_16x16x32_bf16 v[%c0:%c1], a[%c2:%c3], v[%c4:%c5], 0" ::"i"(d), "i"(d + 3), "i"(a), "i"(a + 3), "i"(b), "i"(b + 3));
  else
    asm volatile("v_mfma_f32_16x16x32_bf16 v[%c0:%c1], a[%c2:%c3], v[%c4:%c5], v[%c0:%c1]" ::"i"(d), "i"(d + 3), "i"(a), "i"(a + 3), "i"(b), "i"(b + 3));
  p_read<HF ^ 1, G>();
  if constexpr (G == 0) asm volatile("v_xor_b32 v%c0, s100, v%c0" ::"i"(R_TB + 2 * HF + 1));
  if constexpr (G == 17) asm volatile("v_xor_b32 v%c0, s100, v%c0" ::"i"(R_TB + 2 * (HF ^ 1)));
  if constexpr (G >= 4 && G <= 8 && G != 7) p_cvt<1, (G < 7 ? G - 4 : 3)>();
  if constexpr (G == 24 || (G == 7 && HF == 0)) asm volatile("s_waitcnt lgkmcnt(8)");
  if constexpr (G == 7 && HF == 1) asm volatile("s_waitcnt vmcnt(13) lgkmcnt(8)\n\ts_barrier" ::: "memory");
  if constexpr (G == 10) p_store<1, HF ^ 1>(c);
  if constexpr (G >= 20 && G < 24) p_cvt<0, G - 20>();
  if constexpr (G == 25) p_store<0, HF>(c);
  if constexpr (p_dma(HF, G) >= 0) dma_at<(HF ? 8 : 12) + p_dma(HF, G)>(c);
  if constexpr (HF == 0 && G == 2) asm volatile("s_bitset0_b32 m0, 12");
  if constexpr (HF == 0 && G == 13) asm volatile("s_add_u32 s96, s96, 0x40000");
  if constexpr (HF == 0 && G == 26) asm volatile("s_add_u32 s98, s98, 128");
  if constexpr (HF == 0 && G == 27) asm volatile("s_cmp_lt_u32 s98, 0x380");
  if constexpr (HF == 0 && G == 28) asm volatile("s_cselect_b32 s99, s99, 0x31000");
  if constexpr (HF == 1 && G == 1) asm volatile("s_xor_b32 s100, s100, 0x10000");
  if constexpr (HF == 1 && G == 2) asm volatile("s_xor_b32 s97, s97, s100");
  if constexpr (HF == 1 && G == 9) asm volatile("s_or_b32 m0, s97, s99");
  if constexpr (HF == 1 && G == 30) asm volatile("s_cmp_lt_u32 s98, 0x500");
  GAP();
}
template <int HF, int... Gs> DI void p_half(AC& c, iseq<Gs...>) { (p_gap<HF, Gs>(c), ...); }
constexpr int f_dma(int G) { return G == 2 ? 0 : G == 5 ? 1 : G == 8 ? 2 : G >= 12 && G <= 36 && G % 6 == 0 ? 1 + G / 6 : G >= 50 && G <= 68 && G % 6 == 2 ? G / 6 : -1; }
constexpr int f_ndma(int G) { int n = 0; for (int g = 0; g < G; ++g) n += f_dma(g) >= 0; return n; }
static_assert(f_ndma(74) == 12);
template <int MODE, int G> DI void gapF(AC& c, const EP& p) {
  if constexpr (MODE && G == 48) asm volatile("s_waitcnt lgkmcnt(0)\n\ts_barrier" ::: "memory");
  if constexpr (G < 64) gapB<0, false, G>(c);
  if constexpr (MODE == 2 && G >= 10 && (G - 10) % 16 == 0) asm volatile("s_waitcnt vmcnt(%0)" ::"i"(12 - (G - 10) / 4 + f_ndma(G)) : "memory");
  if constexpr (G >= 10) epi_pair<(G - 10) / 4, (G - 10) & 3, MODE == 2>(p);
  if constexpr (MODE == 0) {
    if constexpr (G >= 14 && ((G - 14) & 3) == 0) epi_store<(G - 14) / 4>(c, p);
    if constexpr (G == 73) epi_store<15>(c, p);
  } else {
    if constexpr (G == 0) asm volatile("s_or_b32 m0, %0, 0x1000\n\ts_movk_i32 s98, 256" ::"s"(c.m0x[0]) : "scc");
    if constexpr (G == 15) asm volatile("s_bitset0_b32 m0, 12");
    if constexpr (G == 48) asm volatile("s_or_b32 m0, %0, 0x1000\n\ts_movk_i32 s98, 384" ::"s"(c.m0x[1]) : "scc");
    if constexpr (f_dma(G) >= 0) dma_at<8 + (f_dma(G) & 7)>(c);
    if constexpr (G == 73) asm volatile("s_waitcnt vmcnt(4) lgkmcnt(0)" ::: "memory");
    if constexpr (G >= 48 && G < 64) { p_read<0, 2 * (G - 48)>(); p_read<0, 2 * (G - 48) + 1>(); }
  }
  GAP();
}
template <int MODE, int... Gs> DI void phaseF(AC& c, const EP& p, iseq<Gs...>) { (gapF<MODE, Gs>(c, p), ...); }
template <int K> DI void r_load(const AC& c, unsigned vo, unsigned so) {
  asm volatile("buffer_load_dwordx4 v[%c0:%c1], %2, %3, %4 offen offset:%5 sc1" ::"i"(128 + 4 * K), "i"(131 + 4 * K), "v"(vo), "s"(c.sw),
               "s"(so + ((K & 15) >> 2) * 4096), "i"((K & 1) * 512 + ((K >> 1) & 1) * 2048) : "memory");
}
template <int K, int NL> DI void r_sum() {
  constexpr int d = 100 + 8 * (K >> 4) + 4 * (K & 1), b = 128 + 4 * K;
  if constexpr ((K & 3) == 0) asm volatile("s_waitcnt vmcnt(%0)" ::"i"(NL - 4 - K) : "memory");
  if constexpr ((K & 15) < 2) asm volatile("v_mfma_f32_16x16x32_bf16 v[%c0:%c1], v[96:99], v[%c2:%c3], 0" ::"i"(d), "i"(d + 3), "i"(b), "i"(b + 3));
  else asm volatile("v_mfma_f32_16x16x32_bf16 v[%c0:%c1], v[96:99], v[%c2:%c3], v[%c0:%c1]" ::"i"(d), "i"(d + 3), "i"(b), "i"(b + 3));
}

#define CMAX 12
#define NYT 16
DI u64* fw(u64* p, int w) { return p + (w >> 4) * 544 + (w & 15); }
DI void set_flag(u64* p, u64 tag, int skip = 0) {
  asm volatile("s_cmp_lg_u32 %2, 0\n\ts_cbranch_scc1 1f\n\ts_mov_b64 exec, 1\n\tglobal_store_dwordx2 %0, %1, off sc1\n\ts_mov_b64 exec, -1\n1:"
               ::"v"(p), "v"(tag), "s"(skip) : "scc", "memory");
}
DI void wait_flags(u64* p, u64 need, u64 tag) {
  const u64* q = fw(p, threadIdx.x & 63);
  u64 v; unsigned cnt;
  asm volatile("s_mov_b32 %1, 0x100000\n\ts_mov_b64 exec, %3\n"
               "1:\n\tglobal_load_dwordx2 %0, %2, off sc1\n\ts_waitcnt vmcnt(0)\n\tv_cmp_eq_u64 vcc, %4, %0\n\t"
               "s_cmp_eq_u64 vcc, %3\n\ts_cbranch_scc1 2f\n\ts_sleep 4\n\ts_sub_u32 %1, %1, 1\n\ts_cbranch_scc0 1b\n"
               "2:\n\ts_mov_b64 exec, -1" : "=&v"(v), "=&s"(cnt) : "v"(q), "s"(need), "s"(tag) : "vcc", "scc", "memory");
}
template <int H> DI void fl_issue(u64* gf, u64 need) {
  const u64* q = fw(gf, threadIdx.x & 63) + H * 4 * 544;
  asm volatile("s_mov_b64 exec, %1\n\tglobal_load_dwordx2 v[%c2:%c3], %0, off sc1\n\ts_mov_b64 exec, -1" ::"v"(q), "s"(need), "i"(122 + 2 * H), "i"(123 + 2 * H) : "memory");
}
template <int H> DI void fl_check(u64* gf, u64 need, u64 tag) {
  u64 m;
  asm volatile("v_cmp_eq_u64 %0, %1, v[%c2:%c3]" : "=&s"(m) : "s"(tag), "i"(122 + 2 * H), "i"(123 + 2 * H) : "memory");
  if (__builtin_expect((m & need) != need, 0)) wait_flags(gf + H * 4 * 544, need, tag);
}
template <bool TR, bool SCL, class START, class MID, class PRE>
DI void g_item(GM& g, const GItem& nxt, u64* pend, u64& tag, bf16* out, int wid, int lane,
               u64* gf, u64 n0, u64 n1, START&& start, MID&& mid, PRE&& pre) {
  constexpr int nkt = SCL ? 7 : 16, P0 = SCL ? 0 : 1;
  GAP();
  start(std::integral_constant<int, P0>{});
  g_fix(g);
  asm volatile("s_nop 4");
  GAP();
  auto hook = [&](int t) __attribute__((always_inline)) {
    if (t == 1) {
      set_flag(pend, tag, wid);
      mid();
    } else if (t == nkt - 3) g_srcB(g, nxt);
    else {
      g_srcA(g, nxt, wid, lane);
      if constexpr (!SCL) { fl_issue<0>(gf, n0); if (n1) fl_issue<1>(gf, n1); }
    }
    g_fix(g);
    asm volatile("s_nop 4");
    GAP();
  };
  g_half<TR, P0, 0, true>(g);
  g_half<TR, P0, 1, true>(g);
  unsigned evm = 2u | (4u << (nkt - 5)) | (4u << (nkt - 4));
  asm volatile("" : "+s"(evm));
#pragma unroll 1
  for (int t = 1;; t += 2) {
    g_half<TR, P0 ^ 1, 0, false>(g);
    if (__builtin_expect((evm >> t) & 1, 0)) hook(t);
    g_half<TR, P0 ^ 1, 1, false>(g);
    if (!SCL && t + 1 >= nkt) break;
    g_half<TR, P0, 0, false>(g);
    if (__builtin_expect(t + 1 == (SCL ? nkt - 3 : nkt - 2), 0)) hook(t + 1);
    g_half<TR, P0, 1, false>(g);
    if (SCL && t + 2 >= nkt) break;
  }
  pre();
  GAP();
  g_epilogue<TR, SCL>(out, wid, lane);
}

constexpr size_t TSZ = (size_t)NH * SEQ * HD;
constexpr unsigned WSB = 5 * TSZ * 2 + 128 * 66560;
__global__ __launch_bounds__(256, 1) __attribute__((amdgpu_num_vgpr(90), amdgpu_num_sgpr(96))) void fused_kernel(const bf16* __restrict__ ckv, const bf16* __restrict__ wuk,
                                                       const bf16* __restrict__ wuv, const bf16* __restrict__ x,
                                                       const bf16* __restrict__ wq, bf16* Qb,
                                                       const bf16* __restrict__ ak, const bf16* __restrict__ av,
                                                       const bf16* __restrict__ ash, const bf16* __restrict__ assmk,
                                                       const bf16* __restrict__ assmD, const bf16* __restrict__ wo,
                                                       const bf16* __restrict__ aq, bf16* __restrict__ out) {
  extern __shared__ __attribute__((aligned(16))) char lds[];
  asm volatile("at_code_begin:" ::: ACLOB_ALL, "v255", "s101");
  asm volatile("" : "+s"(ckv), "+s"(wuk), "+s"(wuv), "+s"(x), "+s"(wq), "+s"(Qb), "+s"(ak), "+s"(av), "+s"(ash), "+s"(assmk),
                    "+s"(assmD), "+s"(wo), "+s"(aq), "+s"(out));
  bf16* Kb = Qb + TSZ;
  bf16* Vb = Kb + TSZ;
  float* partbuf = (float*)(Qb + 5 * TSZ);
  u64* flags = (u64*)(partbuf + (size_t)128 * 16640);
  bf16* xqT = (bf16*)(flags + 400 * 544);
  KST(0, 0);
  const int tid = threadIdx.x, lane = tid & 63;
  const int wid = __builtin_amdgcn_readfirstlane(tid >> 6);
  const int b = blockIdx.x, j = b & 7, r = b >> 3, rr = r & 15, hsel = rr & 1, pj = rr >> 1, kv = pj & 1;
  const bool isY = r >= 16;
  const int h = j + 8 * hsel, mt = (rr >> 2) + (isY ? 4 : 0);
  const unsigned lbase = (unsigned)(size_t)lds;
  u64* gf = fw(flags, j * 128);
  const unsigned m0d = lbase + 163840 - 256;
  const unsigned m0f = lbase + 131072 + wid * 8192;
  const int cch = rr * 4 + wid;
  const bool doA = !isY && cch < 56;
  u64 tag = 0;
  AC c;
  auto att_init = [&]() __attribute__((always_inline)) {
    int l = threadIdx.x & 63; asm volatile("" : "+v"(l));
    c.ka = lbase + l * 16;
    c.dw[0] = wid * 8192 + l * 16; c.dw[1] = c.dw[0] + 4096;
    c.sw = __builtin_amdgcn_make_buffer_rsrc((void*)Qb, (short)0, WSB, 0x00020000);
    set_srdk(Qb, WSB);
    static_for([&](auto I) { vset<r_dk(I)>(__uint_as_float(c.dw[I < 4])); }, make_iseq<8>{});
    sset<S_I>(0x8000);
  };
  const unsigned oQ = (unsigned)h << 20, oK = oQ + (unsigned)(TSZ * 2);
  auto pro_q = [&](int qb) __attribute__((always_inline)) {
    q_load(c.ka - lbase, c.sw, oQ + (qb * 128 + wid * 32) * 512, make_iseq<16>{});
  };
  auto pro_k = [&](int t0) __attribute__((always_inline)) {
    dma8k(c, oK + t0 * 32768, lbase + wid * 8192);
    dma8k(c, oK + t0 * 32768 + 32768, lbase + 32768 + wid * 8192);
  };
  static_assert(NYT <= 16 && NYT >= 12 && !(NYT & 1));
  const bool solo = pj == 7;
  const bool doP = isY != solo;
  const int NY = solo ? 18 : NYT;
  const int nx0 = 32 - 2 * pj - NY, cx = nx0 < CMAX ? nx0 : CMAX, ex = nx0 - cx;
  const u64 needY = (((0x101ull << (((31 - 2 * pj) >> 2) + 1)) - 0x1010ull) << (hsel * 16)) & ~(1ull << (hsel * 16 + kv * 8 + mt));
  const u64 needX = ((0x1010ull << ((ex + 3) >> 2)) - 0x1010ull) << (hsel * 16);
  const u64 needQ = (1ull << (32 + hsel * 8 + (pj >> 1))) | (1ull << (32 + hsel * 8 + ((15 - pj) >> 1)));
  const u64 needA = 0x0f0full << (hsel * 16);
  const u64 needW = 0x3fffull << 36;
  u64* gflag = fw(gf, hsel * 16 + kv * 8 + mt);
  unsigned dv = 0;
  {
    GM g;
    PR pr;
    const u64 tabQ = 0x6543210ull;
    const int cls = (rr + (rr >> 2)) & 3;
    const u64 tabX = cls == 0 ? 0xfedcb9875a463210ull : cls == 1 ? 0xefadc6982b574310ull : cls == 2 ? 0xfebad7693c285410ull : 0xefcba8764d392510ull;
    const u64 tabG = !isY ? tabX : cls == 0 ? 0xfedcb987543a6210ull : cls == 1 ? 0xefadc698254b7310ull : cls == 2 ? 0xfebad769325c8410ull : 0xefcba876432d9510ull;
    const GItem itQ{x + (size_t)pj * 256 * DA, wq + h * 256, DA, tabQ};
    const GItem itG{ckv + (size_t)mt * 256 * DC, (kv ? wuv : wuk) + h * 256, DC, tabG};
    const GItem none{nullptr, nullptr, 0, 0};
    if (doP) prep_load(pr, tid, j, rr, ak, av, ash);
    g.mA = lbase + wid * 1024; g.mB = g.mA + 65536; g.kW = 0;
    g.vbB = (4 * wid + (lane >> 4)) * LDW * 2 + 16 * ((lane & 15) ^ (((lane >> 4) << 2) | wid));
    g_srcA(g, isY ? itQ : itG, wid, lane);
    g_srcB(g, isY ? itQ : itG);
    g_fix(g);
    const unsigned po = isY ? 0 : 32768;
    g_pro<0>(g, po, make_iseq<12>{});
    asm volatile("global_load_dwordx2 v[120:121], %0, off sc1" ::"v"(fw(flags, 6144)) : "memory");
    if (doP) {
      asm volatile("s_waitcnt vmcnt(13)" ::: "memory");
#pragma unroll
      for (int q = 0; q < 7; ++q) asm volatile("" : "+v"(pr.kk[q]));
#pragma unroll
      for (int q = 0; q < 4; ++q) asm volatile("" : "+v"(pr.vv[q]), "+v"(pr.w[q]));
      static_for([&](auto Ic) __attribute__((always_inline)) {
        constexpr int I = Ic;
        g_pro<12 + 4 * I>(g, po, make_iseq<4>{});
        GAP();
        prep_math<I>(pr, tid, lds + 131072);
        GAP();
      }, make_iseq<4>{});
      asm volatile("s_waitcnt lgkmcnt(0)\n\ts_barrier" ::: "memory");
      prep_store(tid, j, rr, xqT, lds + 131072);
    } else g_pro<12>(g, po, make_iseq<4>{});
    GAP();
    CODE_TOUCH("at_code_begin", "pf_code_end", r * 4 + wid, 8, m0d);
    {
      const int fr = lane & 15, fq = lane >> 4, q = (lane & 15) >> 2, p = lane & 3;
#pragma unroll
      for (int k2 = 0; k2 < 2; ++k2)
        g.aA[k2] = lbase + (wid >> 1) * 16384 + (fr >> 3) * 1024 + (fr & 7) * 128 + (((4 * k2 + fq) ^ (fr >> 1)) * 16);
#pragma unroll
      for (int hf = 0; hf < 2; ++hf)
#pragma unroll
        for (int nf = 0; nf < 8; ++nf)
          g.aB[hf][nf] = lbase + 65536 + (wid & 1) * 8192 + off_b(8 * fq + 4 * hf + q, nf * 2 + (p >> 1)) + 8 * (p & 1);
    }
    auto start = [&](auto Pc) __attribute__((always_inline)) {
      if (doP) asm volatile("s_waitcnt vmcnt(13)" ::: "memory");
      else {
        asm volatile("s_nop 4");
        g_pro<16>(g, po, make_iseq<12>{});
        asm volatile("s_waitcnt vmcnt(8)" ::: "memory");
      }
      asm volatile("s_barrier" ::: "memory");
      g_read0<Pc>(g, make_iseq<16>{});
      GAP();
      u64 z = (((u64)__float_as_uint(vget<121>()) << 32) | __float_as_uint(vget<120>())) + 0x9e3779b97f4a7c15ull;
      z = (z ^ (z >> 30)) * 0xbf58476d1ce4e5b9ull; z = (z ^ (z >> 27)) * 0x94d049bb133111ebull; z ^= z >> 31;
      tag = ((u64)(unsigned)__builtin_amdgcn_readfirstlane((int)(z >> 32)) << 32) | (unsigned)__builtin_amdgcn_readfirstlane((int)z);
      asm volatile("s_waitcnt lgkmcnt(0)\n\ts_barrier\n\ts_nop 4" ::: "memory");
      g_pro<28>(g, po, make_iseq<4>{});
      g.kW = ((unsigned)g.tab >> 8) & 15;
      g.voA = g.vbA + (((unsigned)g.tab >> 4) & 15) * 128;
      g.voB = g.vbB + g.kW * 0x80000;
      g.tab >>= 12;
      GAP();
    };
    u64* pend = fw(gf, 64 + r);
    const u64 np0 = isY ? needA | needQ : needQ, np1 = doA ? 0x3fffc000ull : 0ull;
    if (isY) {
      g_item<false, true>(g, itG, pend, tag, Qb + (HROW(h) + pj * 256) * HD, wid, lane, gf, 0, 0, start, [&]() {}, [&]() {});
      pend = fw(gf, 32 + hsel * 8 + pj);
      KST(0, 3);
    }
    auto start2 = [&](auto Pc) __attribute__((always_inline)) { if (!isY) start(Pc); };
    auto mid = [&]() __attribute__((always_inline)) {
      if (wid < 2) post_dma<false>(j, r * 2 + wid, xqT, aq, m0f);
    };
    auto pre = [&]() __attribute__((always_inline)) {
      fl_check<0>(gf, np0, tag);
      fl_check<1>(gf, np1, tag);
      att_init();
      if (isY) { pro_k(cx); pro_q(15 - pj); }
      else if (doA) {
        asm volatile("global_load_ushort %0, %1, off" : "=v"(dv) : "v"(assmD + j * 56 + cch) : "memory");
        conv_load(j * 56 + cch, assmk, xqT, lbase + 65536 + wid * 15488);
      }
    };
    if (kv) g_item<true, false>(g, none, pend, tag, Vb + (HROW(h) + mt * 256) * HD, wid, lane, gf, np0, np1, start2, mid, pre);
    else g_item<false, false>(g, none, pend, tag, Kb + (HROW(h) + mt * 256) * HD, wid, lane, gf, np0, np1, start2, mid, pre);
  }
  KST(0, 4);
  u64* pend2 = gflag;
  if (doA) {
    asm volatile("s_waitcnt vmcnt(32)" : "+v"(dv) :: "memory");
    const float Dd = bf2f(dv);
    KST(0, 5);
    conv_wave(j * 56 + cch, Dd, xqT + (size_t)DA * SEQ, lds + 65536 + wid * 15488,
              c.sw, oQ + ((15 - pj) * 128 + wid * 32) * 512,
              [&](int cr) __attribute__((always_inline)) {
      if (cr == 8) {
        const char* tp = (const char*)wo + (size_t)(j + (cch >= 28 ? 8 : 0)) * (HD * DA * 2) + ((cch % 28) * 64 + lane) * 128;
        asm volatile("s_mov_b32 m0, %1\n\ts_nop 0\n\tglobal_load_lds_dword %0, off" ::"v"(tp), "s"(m0d) : "memory");
      }
      if (cr == 1) {
        asm volatile("s_waitcnt vmcnt(0)\n\ts_barrier" ::: "memory");
        set_flag(gflag, tag, wid);
      } else if (cr == 2) fl_issue<0>(gf, 0x0101ull << (hsel * 16));
      else if (cr >= 4 && cr < 8) {
        if (cr == 4) {
          asm volatile("s_waitcnt vmcnt(16)" ::: "memory");
          fl_check<0>(gf, 0x0101ull << (hsel * 16), tag);
        }
        dma4k(c, oK + ((cr >> 1) & 1) * 32768, lbase + ((cr >> 1) & 1) * 32768 + wid * 8192, cr & 1);
      }
    });
    KST(0, 6);
    pend2 = fw(gf, 100 + rr);
  } else if (!isY) {
    asm volatile("s_waitcnt vmcnt(0)\n\ts_barrier" ::: "memory");
    set_flag(gflag, tag, wid);
    pro_q(pj);
    KST(0, 5);
    wait_flags(gf, needA, tag);
    pro_k(0);
    asm volatile("s_waitcnt vmcnt(0)" ::: "memory");
    KST(0, 6);
  }
  c.va = c.ka + 65536;
  c.m0x[0] = lbase + 65536 + wid * 8192; c.m0x[1] = c.m0x[0] + 32768;
  const unsigned oP = (unsigned)(5 * TSZ * 2) + (h * 8 + pj) * 66560;
  const int qbF = isY ? 15 - pj : pj;
  const unsigned oS = (unsigned)(3 * TSZ * 2) + qbF * 0x200000;
  const int vml = 65536 + wid * 256 + (lane & 15) * 16;
  const int nitems = isY ? 1 : 2, it0 = !isY && solo ? 1 : 0;
#pragma unroll 1
  for (int it = it0; it < nitems; ++it) {
    int qb, t0, n, jmp, c0, c1;
    u64 n0, n1;
    if (isY) { qb = 15 - pj; t0 = cx; n = NY; jmp = ex; c1 = 14 - cx; n1 = needY; c0 = solo ? 30 : NY - 2; n0 = 1ull << (48 + hsel * 8 + pj); }
    else if (it == 0) { qb = 15 - pj; t0 = 0; n = nx0; jmp = 16 - cx; c1 = ex ? cx - 2 : 30; n1 = needX; c0 = 2; n0 = needA; }
    else { qb = pj; t0 = 0; n = 2 * pj + 2; jmp = 0; c1 = 30; c0 = 30; n0 = n1 = 0; }
    const bool last = isY || it == 1;
    c.mrow = qb * 128 + wid * 32 + (lane & 15) - 4 * (lane >> 4);
    static_for([&](auto I) { vset<R_RS + I>(0.f); }, make_iseq<6>{});
    vset<R_THR>(4096.f);
    c.diag0 = qb == 0;
    sset<S_T>(oK + t0 * 32768); sset<S_K>(oK + t0 * 32768 + 32768);
    const int ct = !last ? 30 : n > 2 ? n - 3 : 0, cw = isY ? (solo ? 13 : c1) : last ? (n > 2 ? 0 : 30) : n - 2;
    sset<S_EV>((3u << c0) | (3u << c1) | (last ? 3u << n >> 2 : 1u << n >> 1));
    if (it != it0) asm volatile("s_waitcnt vmcnt(16)" ::: "memory");
    else {
      if (doA) asm volatile("s_waitcnt vmcnt(6)" ::: "memory");
      asm volatile("s_waitcnt vmcnt(32) lgkmcnt(0)\n\ts_barrier" ::: "memory");
    }
    KST(0, 8 + 3 * it);
    kreads<0>(c, make_iseq<8>{});
    GAP();
    auto event = [&](int i) __attribute__((always_inline)) {
      if (i == c0) fl_check<0>(gf, n0, tag);
      if (i == c1) { fl_check<0>(gf, n1, tag); asm volatile("s_add_u32 s98, s98, %0" ::"s"(jmp * 32768) : "scc"); }
      if (i + 1 == c0 || i + 1 == c1) fl_issue<0>(gf, (i + 1 == c0 ? n0 : 0) | (i + 1 == c1 ? n1 : 0));
      if (i == cw) fl_issue<1>(gf, needW);
      if (i + 2 == n) {
        sset<S_K>(oK);
        if (last) {
          int l = threadIdx.x & 63; asm volatile("" : "+v"(l));
          const unsigned vw = (64 * wid + (l >> 3)) * (DA * 2) + 16 * ((l & 7) ^ ((l >> 3) & 6));
          static_for([&](auto Q) { vset<r_dk(Q)>(__uint_as_float(vw + (Q ^ 4) * (8 * DA * 2) - (Q & 3) * 1024)); }, make_iseq<8>{});
          set_srdk(wo + (size_t)h * HD * DA, HD * DA * 2);
          sset<S_K>(0); sset<S_I>(128);
        }
        if (isY && !solo)
          asm volatile("s_nop 4\n\tbuffer_load_dwordx4 v[122:125], %0, %1, %2 offen sc1" ::"v"(vml), "s"(c.sw), "s"(oP) : "memory");
      }
      if (i == ct) {
        fl_check<1>(gf, needW, tag);
        if (wid < 2) post_dma<true>(j, r * 2 + wid, xqT + (size_t)DA * SEQ, aq, m0f);
      }
      if (last && i + 3 >= n && i + 1 < n && i) set_mask<R_CI, false>(c, (2 * qb + i + 3 - n) * 64);
      if (i == 0) set_flag(it != it0 ? fw(gf, 48 + hsel * 8 + pj) : pend2, tag, wid);
      asm volatile("s_nop 4");
      GAP();
    };
#define STA(k)
    STA(0);
    phaseA<0, true, false>(c);
    STA(1);
    event(0);
    STA(2);
    phaseB0(c);
    STA(3);
#pragma unroll 1
    for (int i = 1;; i += 2) {
      phaseA<1, false, false>(c);
      STA(9 + i);
      if (__builtin_expect(c.rare != 0, 0)) { if (c.redo) sm_redo<1>(); event(i); }
      phaseB<1>(c);
      if (i + 1 >= n) break;
      phaseA<0, false, false>(c);
      STA(10 + i);
      if (__builtin_expect(c.rare != 0, 0)) { if (c.redo) sm_redo<0>(); event(i + 1); }
      phaseB<0>(c);
    }
    STA(4);
    c.rare = __builtin_amdgcn_readfirstlane(!last ? oQ + (pj * 128 + wid * 32) * 512 : oP + wid * 16384);
    c.vq = last && (!isY || solo) ? 0x80000000u : c.ka;
    asm volatile("s_nop 4" : "+s"(c.rare));
    phaseA<0, false, true>(c);
    if (__builtin_expect(c.redo != 0, 0)) sm_redo<0>();
    STA(5);
    KST(0, 9 + 3 * it);
    const float l[2] = {row_reduce<false>(vget<R_RS>() + vget<R_TS>()), row_reduce<false>(vget<R_RS + 1>() + vget<R_TS + 1>())};
    const float cm[2] = {vget<R_M>(), vget<R_M + 1>()};
    EP p;
    p.vo = c.dw[0] + wid * 8192;
#pragma unroll
    for (int e = 0; e < 4; ++e) p.so[e] = oP + e * 4096;
    {
      int lz = threadIdx.x & 63; asm volatile("" : "+v"(lz));
      const int rw = lz >> 2;
      static_for([&](auto E) { vset<R_TB + E>(__uint_as_float(lbase + rw * 128 + 16 * ((2 * E + ((lz & 3) >> 1)) ^ (rw & 6)) + 8 * (lz & 1))); }, make_iseq<4>{});
      const unsigned vs = ((lz >> 3) & 1) * 16384 + ((lz >> 4) & 1) * 512 + (lz >> 5) * 256 + (lz & 7) * 16;
      vset<R_VS>(__uint_as_float(vs)); vset<R_VS + 1>(__uint_as_float(vs + 32768));
      sset<96>(oS + wid * 65536 + h * 1024); sset<97>(c.m0x[1]); sset<100>(0x8000);
    }
    if (!isY || solo) {
      p.fa[0] = __builtin_amdgcn_rcpf(l[0]); p.fa[1] = __builtin_amdgcn_rcpf(l[1]);
      if (!last) {
        __builtin_amdgcn_raw_buffer_store_b128(__builtin_bit_cast(u32x4, (f32x4){cm[0], l[0], cm[1], l[1]}), c.sw, vml, oP, 16);
        phaseF<0>(c, p, make_iseq<74>{});
      } else phaseF<1>(c, p, make_iseq<74>{});
    } else {
      const f32x4 ml = {vget<122>(), vget<123>(), vget<124>(), vget<125>()};
#pragma unroll
      for (int e = 0; e < 2; ++e) {
        const float mm = fmaxf(cm[e], ml[2 * e]);
        const float wa = __builtin_amdgcn_exp2f(cm[e] - mm), wb = ml[2 * e + 1] * __builtin_amdgcn_exp2f(ml[2 * e] - mm);
        const float inv = __builtin_amdgcn_rcpf(l[e] * wa + wb);
        p.fa[e] = wa * inv; p.fb[e] = wb * inv;
      }
      phaseF<2>(c, p, make_iseq<74>{});
    }
    KST(0, 10 + 3 * it);
  }
  {
    asm volatile("v_xor_b32 v%c1, s100, v%c1\n\ts_movk_i32 s99, 0x1000\np_loop:" : "+s"(c.sw) : "i"(R_TB) : "scc");
    GAP();
    p_half<0>(c, make_iseq<32>{});
    p_half<1>(c, make_iseq<32>{});
    asm volatile("s_cbranch_scc1 p_loop\n\ts_nop 5" ::"s"(c.sw));
    GAP();
    p_cvt<1, 0>(); p_cvt<1, 1>(); p_cvt<1, 2>(); p_cvt<1, 3>();
    p_store<1, 1>(c);
    GAP();
    int lz = threadIdx.x & 63; asm volatile("" : "+v"(lz));
    const int fr = lz & 15, fq = lz >> 4;
    KST(0, 27);
    {
      const int jj = 4 * (fr >> 3) + (fr & 3);
      const unsigned one = ((fq ^ (fr >> 2)) & 1) ? 0u : (jj & 1) ? 0x3f800000u : 0x3f80u;
      static_for([&](auto I) { vset<96 + I>(__uint_as_float((jj >> 1) == I ? one : 0u)); }, make_iseq<4>{});
    }
    asm volatile("s_waitcnt vmcnt(0)" ::: "memory");
    set_flag(fw(flags, 1024 + (qbF * 4 + wid) * 16 + h), tag);
    KST(0, 30);
    if (wid < 2) post_math(j, r * 2 + wid, out, lds + 131072 + wid * 8192);
    if (wid == 3) wait_flags(fw(flags, 1024 + (qbF * 4 + (h & 3)) * 16), 0xffffull, tag);
    asm volatile("s_barrier" ::: "memory");
    KST(0, 28);
    const unsigned vo = (fq >> 1) * 1024 + (fq & 1) * 256 + fr * 16;
    unsigned so = __builtin_amdgcn_readfirstlane(oS + (1 + wid) * 0x40000 + (h & 3) * 65536 + (h >> 2) * 16384);
    bf16* op = out + (size_t)(qbF * 128 + (h & 3) * 32 + (h >> 2) * 8 + (fr & 7)) * OUTW + DA + wid * 64 + (fr >> 3) * 32 + fq * 8;
    auto fin = [&](auto NLc) __attribute__((always_inline)) {
      constexpr int NL = NLc;
      asm volatile("s_nop 4" : "+s"(so), "+s"(c.sw));
      static_for([&](auto K) { r_load<K>(c, vo, so + (K >> 4) * 0x100000); }, make_iseq<NL>{});
      static_for([&](auto K) { r_sum<K, NL>(); }, make_iseq<NL>{});
      asm volatile("s_nop 7");
      static_for([&](auto U) {
        constexpr int d = 100 + 8 * U;
        *(u32x4*)(op + U * 256) = (u32x4){pk2(vget<d>(), vget<d + 1>()), pk2(vget<d + 2>(), vget<d + 3>()), pk2(vget<d + 4>(), vget<d + 5>()), pk2(vget<d + 6>(), vget<d + 7>())};
      }, make_iseq<NL / 16>{});
    };
    if (wid < 3) fin(std::integral_constant<int, 32>{});
    else fin(std::integral_constant<int, 16>{});
    KST(0, 29);
    set_flag(fw(flags, 6144), tag, (b ^ 128) | wid);
  }
  asm volatile("s_waitcnt vmcnt(0)" ::: "memory");
  asm volatile("pf_code_end:");
  KST(0, 1);
}

extern "C" void kernel_launch(void* const* d_in, const int* in_sizes, int n_in, void* d_out, int out_size, void* d_ws,
                              size_t ws_size, hipStream_t stream) {
  const bf16* Aq = (const bf16*)d_in[0];
  const bf16* Ak = (const bf16*)d_in[1];
  const bf16* Av = (const bf16*)d_in[2];
  const bf16* Ash = (const bf16*)d_in[3];
  const bf16* Assmk = (const bf16*)d_in[4];
  const bf16* AssmD = (const bf16*)d_in[5];
  const bf16* Bx = (const bf16*)d_in[6];
  const bf16* Bckv = (const bf16*)d_in[7];
  const bf16* Bwq = (const bf16*)d_in[8];
  const bf16* Bwuk = (const bf16*)d_in[9];
  const bf16* Bwuv = (const bf16*)d_in[10];
  const bf16* Bwo = (const bf16*)d_in[11];
  bf16* out = (bf16*)d_out;
  hipFuncSetAttribute((const void*)fused_kernel, hipFuncAttributeMaxDynamicSharedMemorySize, 163840);
  fused_kernel<<<256, 256, 163840, stream>>>(Bckv, Bwuk, Bwuv, Bx, Bwq, (bf16*)d_ws, Ak, Av, Ash, Assmk, AssmD, Bwo, Aq, out);
}
```

```cpp
#include <hip/hip_runtime.h>
#include <hip/hip_bf16.h>
#include <stdint.h>
#include <utility>

typedef __hip_bfloat16 bf16;
typedef __attribute__((ext_vector_type(4))) short bf16x4;
typedef __attribute__((ext_vector_type(2))) unsigned u32x2;

constexpr int SEQ = 2048;
constexpr int DA = 448;
constexpr int NH = 16;
constexpr int HD = 256;
constexpr int DC = 1024;
constexpr int OUTW = 896;
#define HROW(h) ((size_t)(h) * SEQ)
constexpr int HSTEP = SEQ;

#define KST(kid, which)

__device__ __forceinline__ float bf2f(unsigned short u) { return __uint_as_float(((unsigned)u) << 16); }
__device__ __forceinline__ unsigned short f2bf(float f) { return __bfloat16_as_ushort(__float2bfloat16(f)); }
__device__ __forceinline__ float rbf(float f) { return bf2f(f2bf(f)); }
typedef __attribute__((ext_vector_type(2))) __bf16 bf16x2n;
typedef __attribute__((ext_vector_type(2))) float f32x2n;
__device__ __forceinline__ unsigned pk2(float a, float b) {
  return __builtin_bit_cast(unsigned, __builtin_convertvector((f32x2n){a, b}, bf16x2n));
}

__device__ __forceinline__ int off_b(int row, int ch) { return 256 * row + 16 * (ch ^ (((row & 3) << 2) | ((row >> 2) & 3))); }

template <int R> __device__ __forceinline__ void sset(unsigned x) { asm volatile("s_mov_b32 s%c0, %1" ::"i"(R), "s"(x)); }
template <int R> __device__ __forceinline__ float vget() { float t; asm volatile("v_mov_b32 %0, v%c1" : "=v"(t) : "i"(R)); return t; }
template <int R> __device__ __forceinline__ void vset(float x) { asm volatile("v_mov_b32 v%c0, %1" ::"i"(R), "v"(x)); }
template <int R> __device__ __forceinline__ float acc_read1() { float t; asm volatile("v_accvgpr_read_b32 %0, a[%c1]" : "=v"(t) : "i"(R)); return t; }
template <class F, int... I> __device__ __forceinline__ void static_for(F&& f, std::integer_sequence<int, I...>) {
  (f(std::integral_constant<int, I>{}), ...);
}
template <int... Is> using iseq = std::integer_sequence<int, Is...>;
template <int N> using make_iseq = std::make_integer_sequence<int, N>;

typedef __attribute__((ext_vector_type(4))) unsigned u32x4c;
typedef __attribute__((ext_vector_type(4))) float f32x4c;
typedef __attribute__((ext_vector_type(2))) float f32x2c;
typedef unsigned long long u64;
#define DMA16(M0V, VO, SRD, SO) asm volatile("s_mov_b32 m0, %0\n\ts_nop 0\n\tbuffer_load_dwordx4 %1, %2, %3 offen lds" ::"s"(M0V), "v"(VO), "s"(SRD), "s"(SO) : "memory")
struct PR { u32x4c kk[7], vv[4], w[4]; };
__device__ __forceinline__ void prep_load(PR& p, int tid, int j, int rr, const bf16* __restrict__ k, const bf16* __restrict__ v,
                                          const bf16* __restrict__ shift_k) {
  const __amdgpu_buffer_rsrc_t sk = __builtin_amdgcn_make_buffer_rsrc((void*)k, (short)0, SEQ * DA * 2, 0x00020000);
  const __amdgpu_buffer_rsrc_t sv = __builtin_amdgcn_make_buffer_rsrc((void*)v, (short)0, SEQ * DA * 2, 0x00020000);
  const __amdgpu_buffer_rsrc_t sw = __builtin_amdgcn_make_buffer_rsrc((void*)(shift_k + j * 224), (short)0, 448, 0x00020000);
  const int vo = ((rr * 128 + (tid >> 3) * 4) * DA + j * 56 + (tid & 7) * 8) * 2;
#pragma unroll
  for (int q = 0; q < 7; ++q)
    asm volatile("buffer_load_dwordx4 %0, %1, %2, 0 offen" : "=v"(p.kk[q]) : "v"(vo + (q - 3) * DA * 2), "s"(sk) : "memory");
#pragma unroll
  for (int q = 0; q < 4; ++q)
    asm volatile("buffer_load_dwordx4 %0, %1, %2, 0 offen offset:%3" : "=v"(p.vv[q]) : "v"(vo), "s"(sv), "i"(q * DA * 2) : "memory");
#pragma unroll
  for (int q = 0; q < 4; ++q)
    asm volatile("buffer_load_dwordx4 %0, %1, %2, 0 offen offset:%3" : "=v"(p.w[q]) : "v"((tid & 7) * 64), "s"(sw), "i"(q * 16) : "memory");
}
__device__ __forceinline__ float bfl(unsigned u) { return __uint_as_float(u << 16); }
__device__ __forceinline__ float bfh(unsigned u) { return __uint_as_float(u & 0xffff0000u); }
__device__ __forceinline__ f32x2c rbf2(f32x2c a) { const unsigned u = pk2(a[0], a[1]); return (f32x2c){bfl(u), bfh(u)}; }
template <int I> __device__ __forceinline__ void prep_math(const PR& p, int tid, char* lx) {
  const int rp = tid >> 3, c = tid & 7;
  f32x2c wt[4], kf[7];
#pragma unroll
  for (int t = 0; t < 4; ++t)
    wt[t] = (t & 1) ? (f32x2c){bfh(p.w[I][t >> 1]), bfh(p.w[I][2 + (t >> 1)])} : (f32x2c){bfl(p.w[I][t >> 1]), bfl(p.w[I][2 + (t >> 1)])};
#pragma unroll
  for (int q = 0; q < 7; ++q) kf[q] = (f32x2c){bfl(p.kk[q][I]), bfh(p.kk[q][I])};
  f32x2c x[4];
#pragma unroll
  for (int e = 0; e < 4; ++e) {
    f32x2c s = rbf2(kf[e] * wt[3]);
    s = rbf2(s + rbf2(kf[e + 1] * wt[2]));
    s = rbf2(s + rbf2(kf[e + 2] * wt[1]));
    s = rbf2(s + rbf2(kf[e + 3] * wt[0]));
    x[e] = s * (f32x2c){bfl(p.vv[e][I]), bfh(p.vv[e][I])};
  }
#pragma unroll
  for (int u = 0; u < 2; ++u)
    *(u32x2*)(lx + (c * 8 + 2 * I + u) * 256 + (((31 - rp) * 8) ^ (c << 4))) = (u32x2){pk2(x[3][u], x[2][u]), pk2(x[1][u], x[0][u])};
}
__device__ __forceinline__ void prep_store(int tid, int j, int rr, bf16* __restrict__ xr, const char* lx) {
  const __amdgpu_buffer_rsrc_t sx = __builtin_amdgcn_make_buffer_rsrc((void*)(xr + (size_t)j * 56 * SEQ), (short)0, 56 * SEQ * 2, 0x00020000);
#pragma unroll
  for (int rd = 0; rd < 4; ++rd) {
    const int idx = tid + 256 * rd, ch = idx >> 4, q = idx & 15;
    u32x4c w = *(const u32x4c*)(lx + ch * 256 + 16 * (q ^ (ch >> 3)));
    __builtin_amdgcn_raw_buffer_store_b128(w, sx, (ch * SEQ + (15 - rr) * 128 + q * 8) * 2, 0, 16);
  }
}

#define CODE_TOUCH(LB, LE, wx, NL, m0v) do { unsigned blo_, bhi_, elo_; \
    asm volatile("s_getpc_b64 s[40:41]\n\ts_add_u32 s40, s40, " LB "@rel32@lo+4\n\ts_addc_u32 s41, s41, " LB "@rel32@hi+12\n\t" \
                 "s_mov_b32 %0, s40\n\ts_mov_b32 %1, s41\n\t" \
                 "s_getpc_b64 s[40:41]\n\ts_add_u32 s40, s40, " LE "@rel32@lo+4\n\ts_mov_b32 %2, s40" \
                 : "=s"(blo_), "=s"(bhi_), "=s"(elo_) :: "s40", "s41", "scc"); \
    const unsigned n_ = elo_ - (blo_ & ~127u); \
    const __amdgpu_buffer_rsrc_t srd_ = __builtin_amdgcn_make_buffer_rsrc((void*)(((unsigned long long)bhi_ << 32) | (blo_ & ~127u)), (short)0, n_ < 262144u ? n_ : 0u, 0x00020000); \
    const unsigned l_ = threadIdx.x & 63, vo_ = l_ < (NL) ? ((wx) + l_ * 128u) * 128u : 0x80000000u; \
    asm volatile("s_mov_b32 m0, %0\n\ts_nop 4\n\tbuffer_load_dword %1, %2, 0 offen lds" ::"s"(m0v), "v"(vo_), "s"(srd_) : "memory"); } while (0)
template <int I> __device__ __forceinline__ void d_store() {
  asm volatile("buffer_store_dwordx4 v[%c0:%c1], v%c2, s[92:95], s101 offen offset:%3 sc1" ::"i"(128 + 4 * I), "i"(131 + 4 * I), "i"(112 + ((I >> 2) & 1)),
               "i"((I & 3) * 1024) : "memory");
  if constexpr ((I & 7) == 7) asm volatile("s_addk_i32 s101, 0x4000" ::: "scc");
}
__device__ __forceinline__ void d_none() { asm volatile("v_bfrev_b32 v112, 1\n\tv_bfrev_b32 v113, 1\n\ts_mov_b32 s101, 0"); }
struct CV { unsigned pa[4], pb, qv; };
constexpr int cv_J(int q) { int J = 7; while (q > J) { q -= J + 1; --J; } return J; }
constexpr int cv_i(int q) { int J = 7; while (q > J) { q -= J + 1; --J; } return q; }
template <int R, int N> __device__ __forceinline__ void cv_rd(CV& c) {
  if constexpr (N < 16) {
    constexpr int i = N >> 1, o = 16 * R + 128 * (i & 1) + 2 * (N & 1), r = 32 + 32 * (R & 1) + 2 * N;
    asm volatile("ds_read2_b32 a[%c1:%c2], %0 offset0:%c3 offset1:%c4" ::"v"(c.pa[i >> 1]), "i"(r), "i"(r + 1), "i"(o), "i"(o + 1));
  } else {
    constexpr int m = N - 16, r = 192 + 32 * (R & 1) + 4 * m;
    asm volatile("ds_read_b128 a[%c1:%c2], %0 offset:%c3" ::"v"(c.pb), "i"(r), "i"(r + 3), "i"(64 * R + 512 * (7 - m)));
  }
}
template <int R, int Q, int MODE> __device__ __forceinline__ void cv_slot(CV& c) {
  constexpr int i = cv_i(Q), J = cv_J(Q), d = 4 * J, a = 32 + 32 * (R & 1) + 4 * i, b = 192 + 32 * (R & 1) + 4 * (J - i);
  if constexpr (Q == 0) asm volatile("s_waitcnt lgkmcnt(0)");
  if constexpr (MODE == 1 && R == 0 && i == 0)
    asm volatile("v_mfma_f32_16x16x32_bf16 a[%c0:%c1], a[%c2:%c3], a[%c4:%c5], 0" ::"i"(d), "i"(d + 3), "i"(a), "i"(a + 3), "i"(b), "i"(b + 3));
  else
    asm volatile("v_mfma_f32_16x16x32_bf16 a[%c0:%c1], a[%c2:%c3], a[%c4:%c5], a[%c0:%c1]" ::"i"(d), "i"(d + 3), "i"(a), "i"(a + 3), "i"(b), "i"(b + 3));
  if constexpr (Q < 24 && !(MODE == 2 && R == 1)) cv_rd<R + 1, Q>(c);
  if constexpr (MODE == 1 && Q % 7 == 3) d_store<22 + 5 * R + Q / 7>();
  if constexpr (MODE == 2 && (R == 0 ? Q >= 24 && Q % 3 == 0 : Q % 3 == 0)) {
    constexpr int S_ = R == 0 ? (Q - 24) / 3 : 4 + Q / 3;
    asm volatile("buffer_load_dwordx4 a[%c0:%c1], %2, s[92:95], s97 offen offset:%3 sc1" ::"i"(128 + 4 * S_), "i"(128 + 4 * S_ + 3), "v"(c.qv),
                 "i"((S_ & 3) * 1024) : "memory");
    if constexpr ((S_ & 3) == 3) asm volatile("s_addk_i32 s97, 4096" ::: "scc");
  }
  __builtin_amdgcn_sched_barrier(0);
}
template <int R, int MODE, int... Q> __device__ __forceinline__ void cv_step(CV& c, iseq<Q...>) { (cv_slot<R, Q, MODE>(c), ...); }
template <int... N> __device__ __forceinline__ void cv_rd0(CV& c, iseq<N...>) { (cv_rd<0, N>(c), ...); }
template <class HOOK>
__device__ __forceinline__ void conv_wave(int d, float Dd, bf16* __restrict__ zT, char* lw, HOOK&& hook) {
  int lane = threadIdx.x & 63; asm volatile("" : "+v"(lane));
  const int l16 = lane & 15, fq = lane >> 4;
  u32x4c w[5]; unsigned w4[5];
#pragma unroll
  for (int r = 0; r < 5; ++r) {
    w[r] = *(const u32x4c*)(lw + r * 1024 + lane * 16);
    w4[r] = *(const unsigned*)(lw + r * 1024 + lane * 16 + 16);
  }
  d_store<16>(); d_store<17>();
#pragma unroll
  for (int r = 0; r < 5; ++r)
    *(u32x4c*)(lw + 10304 + r * 1024 + lane * 16) = (u32x4c){__builtin_amdgcn_alignbit(w[r][1], w[r][0], 16), __builtin_amdgcn_alignbit(w[r][2], w[r][1], 16),
                                                             __builtin_amdgcn_alignbit(w[r][3], w[r][2], 16), __builtin_amdgcn_alignbit(w4[r], w[r][3], 16)};
  d_store<18>(); d_store<19>();
  CV c;
#pragma unroll
  for (int i = 0; i < 4; ++i) c.pa[i] = (unsigned)(size_t)lw + ((l16 + 1) & 1) * 10304 + 4 * ((8 * fq + l16 + 1) >> 1) + 1024 * i;
  c.pb = (unsigned)(size_t)lw + 5120 + (2032 - 16 * l16 + 8 * fq) * 2 - 512 * 7;
  asm volatile("" ::: "memory");
  cv_rd0(c, make_iseq<24>{});
  d_store<20>(); d_store<21>();
  __builtin_amdgcn_sched_barrier(0);
  cv_step<0, 1>(c, make_iseq<36>{});
  cv_step<1, 1>(c, make_iseq<36>{});
#pragma unroll 1
  for (int r = 1; r < 3; ++r) {
#pragma unroll
    for (int i = 0; i < 4; ++i) c.pa[i] += 128;
    c.pb += 128;
    __builtin_amdgcn_sched_barrier(0);
    cv_step<0, 0>(c, make_iseq<36>{});
    cv_step<1, 0>(c, make_iseq<36>{});
    hook(r);
  }
#pragma unroll
  for (int i = 0; i < 4; ++i) c.pa[i] += 128;
  c.pb += 128;
  c.qv = lane * 16;
  __builtin_amdgcn_sched_barrier(0);
  cv_step<0, 2>(c, make_iseq<36>{});
  cv_step<1, 2>(c, make_iseq<36>{});
  const __amdgpu_buffer_rsrc_t srdZ = __builtin_amdgcn_make_buffer_rsrc((void*)(zT + (size_t)d * SEQ), (short)0, SEQ * 2, 0x00020000);
  asm volatile("s_nop 7");
  static_for([&](auto Jc) __attribute__((always_inline)) {
    constexpr int J = Jc;
    hook(4 + J);
    __builtin_amdgcn_sched_barrier(0);
    const int t = 256 * J + 16 * l16 + 4 * fq;
    bf16x4 xv = *(const bf16x4*)(lw + 5120 + (2044 - t) * 2);
    const float y[4] = {acc_read1<4 * J>(), acc_read1<4 * J + 1>(), acc_read1<4 * J + 2>(), acc_read1<4 * J + 3>()};
    float z[4];
#pragma unroll
    for (int e = 0; e < 4; ++e) z[e] = rbf(y[e]) + rbf(bf2f(xv[3 - e]) * Dd);
    u32x2 w = {pk2(z[0], z[1]), pk2(z[2], z[3])};
    __builtin_amdgcn_raw_buffer_store_b64(w, srdZ, t * 2, 0, 16);
  }, make_iseq<8>{});
}

template <bool Z> __device__ __forceinline__ void post_dma(int j, int tb, const bf16* __restrict__ zT, const bf16* __restrict__ q, unsigned m0w) {
  int lane = threadIdx.x & 63; asm volatile("" : "+v"(lane));
  const __amdgpu_buffer_rsrc_t sz = __builtin_amdgcn_make_buffer_rsrc((void*)(zT + (size_t)j * 56 * SEQ + tb * 32), (short)0, 56 * SEQ * 2, 0x00020000);
  const __amdgpu_buffer_rsrc_t sq = __builtin_amdgcn_make_buffer_rsrc((void*)(q + (size_t)tb * 32 * DA + j * 56), (short)0, 32 * DA * 2, 0x00020000);
  const int tl = lane / 7, ck = lane - tl * 7;
  const unsigned vz = lane < 56 ? (ck * 8 + tl) * SEQ * 2 : 0x80000000u, vq = lane < 56 ? (tl * DA + ck * 8) * 2 : 0x80000000u;
#pragma unroll
  for (int i = 0; i < 4; ++i) if constexpr (Z)
    asm volatile("s_mov_b32 m0, %0\n\ts_nop 4\n\tbuffer_load_dwordx4 %1, %2, %3 offen sc1 lds" ::"s"(m0w + i * 1024), "v"(vz), "s"(sz), "s"(i * 16) : "memory");
#pragma unroll
  for (int i = 0; i < 4; ++i) if constexpr (!Z)
    asm volatile("s_mov_b32 m0, %0\n\ts_nop 4\n\tbuffer_load_dwordx4 %1, %2, %3 offen lds" ::"s"(m0w + 4096 + i * 1024), "v"(vq), "s"(sq), "s"(i * 8 * DA * 2) : "memory");
}
__device__ __forceinline__ void post_math(int j, int tb, bf16* __restrict__ out, const char* lw) {
  int lane = threadIdx.x & 63; asm volatile("" : "+v"(lane));
  const int tl = lane / 7, ck = lane - tl * 7;
  const __amdgpu_buffer_rsrc_t so = __builtin_amdgcn_make_buffer_rsrc((void*)(out + (size_t)tb * 32 * OUTW + j * 56), (short)0, 32 * OUTW * 2 - 2 * DA, 0x00020000);
  const unsigned vo = lane < 56 ? (tl * OUTW + ck * 8) * 2 : 0x80000000u;
  const char* lz = lw + ck * 16 + (tl & 7) * 2;
#pragma unroll
  for (int i = 0; i < 4; ++i) {
    const u32x4c qv = *(const u32x4c*)(lw + 4096 + i * 1024 + lane * 16);
    u32x4c o;
#pragma unroll
    for (int e = 0; e < 4; ++e) {
      const float z0 = bf2f(*(const unsigned short*)(lz + i * 1024 + 2 * e * 112));
      const float z1 = bf2f(*(const unsigned short*)(lz + i * 1024 + (2 * e + 1) * 112));
      o[e] = pk2(bfl(qv[e]) * z0, bfh(qv[e]) * z1);
    }
    __builtin_amdgcn_raw_buffer_store_b128(o, so, vo, i * 8 * OUTW * 2, 0);
  }
}

#define DI __device__ __forceinline__
#define GAP() __builtin_amdgcn_sched_barrier(0)
typedef __attribute__((ext_vector_type(4))) float f32x4g;
typedef __attribute__((ext_vector_type(4))) unsigned u32x4g;
constexpr int LDW = NH * HD;
struct GM {
  unsigned aA[2], aB[2][8];
  unsigned voA, voB, vbA, vbB, kA, kW, kB;
  u64 tab;
  __amdgpu_buffer_rsrc_t srdA, srdB;
  unsigned sA[8], mA, mB;
};
template <int P, int KS, int I> DI void g_wread(GM& g) {
  constexpr int nf = I >> 1, hf = I & 1, r = 192 - 64 * P + 8 * nf + 4 * KS + 2 * hf;
  asm volatile("ds_read_b64_tr_b16 a[%c1:%c2], %0 offset:%c3" ::"v"(g.aB[hf][nf]), "i"(r), "i"(r + 1), "i"(P * 32768 + KS * 16384));
}
template <int P, int M, int KS> DI void g_aread(GM& g) {
  constexpr int r = 96 + 8 * (M & 1) + 4 * KS;
  asm volatile("ds_read_b128 v[%c1:%c2], %0 offset:%c3" ::"v"(g.aA[KS]), "i"(r), "i"(r + 3), "i"(P * 32768 + M * 2048));
}
constexpr float CS = 0.0625f * 1.4426950408889634f;
struct GE {
  __amdgpu_buffer_rsrc_t sk, sx;
  unsigned vl, vl0, vq, vk[2], mC, mK;
};
#define SG0 36
#define SGS 6
struct ES { int op0[129], nop[129]; };
constexpr ES e_sched(bool TR, bool SCL) {
  ES z{};
  const int per = SCL ? 16 : 8;
  int o = 0;
  for (int G = 0; G <= 128; ++G) {
    int cap = G == 128 ? 10000 : SCL ? 4 : 2;
    z.op0[G] = o;
    while (o < 32 * per) {
      const int i = o / per, rdy = (TR ? 32 * (i >> 3) + 16 + 2 * (i & 7) + 1 : 4 * i + 3) + 4;
      if ((G < rdy && G < 128) || cap-- <= 0) break;
      ++o; ++z.nop[G];
    }
  }
  return z;
}
template <bool TR, bool SCL> constexpr ES ESCH = e_sched(TR, SCL);
constexpr int e_st(int MODE, int G) {
  const int g0 = MODE == 4 ? 5 : MODE == 2 ? 13 : SG0, gs = MODE == 4 ? 6 : MODE == 2 ? 7 : SGS;
  return MODE >= 2 && G >= g0 && (G - g0) % gs == 0 && (G - g0) / gs < 16 ? (G - g0) / gs + (MODE == 4 ? 16 : 0) : -1;
}
constexpr int e_nst(int MODE, int G0, int G1) { int n = 0; for (int G = G0; G < G1; ++G) n += e_st(MODE, G) >= 0; return n; }
static_assert(e_nst(2, 0, 128) == 16 && e_nst(3, 0, 128) == 16 && e_nst(4, 0, 104) == 16);
template <bool TR, bool SCL, int O> DI void e_op() {
  constexpr int per = SCL ? 16 : 8, i = O / per, k = O % per, r = 128 + 4 * i, t = 114 + 4 * (i & 1), c = k - (per - 4);
  if constexpr (k < 4) asm volatile("v_accvgpr_read_b32 v%c0, a[%c1]" ::"i"(t + k), "i"(4 * i + k));
  else if constexpr (c < 0) asm volatile("v_mul_f32 v%c0, %1, v%c0" ::"i"(k < 8 ? r + (k ^ 6) : t + k - 8), "s"(CS));
  else {
    constexpr int d = c == 0 ? r + 3 : c == 1 ? r + 2 : r + c - 2, x = c == 0 ? r + 2 : c == 1 ? r : t + 2 * (c - 2);
    asm volatile("v_cvt_pk_bf16_f32 v%c0, v%c1, v%c2" ::"i"(d), "i"(x), "i"(x + 1));
  }
}
template <bool TR, bool SCL, int B, int... Is> DI void e_ops(iseq<Is...>) { (e_op<TR, SCL, B + Is>(), ...); }
#define LGS 2
constexpr int e_slot(int G) { return G < 10 ? G : (G - 10) % LGS == 1 && (G - 10) / LGS < 32 ? 10 + (G - 10) / LGS : -1; }
constexpr int E_CONV = 9, E_K0 = 11 + 23 * LGS;
static_assert(e_slot(E_K0) == 33);
template <int S> DI void e_m0(const GE& e) {
  if constexpr (S == 0 || S == 1 || S == 5 || S == 9) asm volatile("s_add_u32 m0, %0, %1" ::"s"(e.mC), "i"(S * 1024));
  if constexpr (S >= 26 && (S - 26) % 4 == 0) asm volatile("s_add_u32 m0, %0, %1" ::"s"(e.mK), "i"(((S - 26) >> 3) * 32768 + ((S - 26) & 4) * 1024));
}
template <int S> DI void e_load(const GE& e) {
  if constexpr (S == 0) asm volatile("buffer_load_dwordx4 %0, %1, 0 offen lds" ::"v"(e.vl0), "s"(e.sk) : "memory");
  else if constexpr (S < 5) asm volatile("buffer_load_dwordx4 %0, %1, %2 offen offset:%3 lds" ::"v"(e.vl), "s"(e.sk), "s"(1024 - 32), "i"((S - 1) * 1024) : "memory");
  else if constexpr (S < 9) asm volatile("buffer_load_dwordx4 %0, %1, 0 offen offset:%2 sc1 lds" ::"v"(e.vl), "s"(e.sx), "i"((S - 5) * 1024) : "memory");
  else if constexpr (S == 9) asm volatile("buffer_load_dwordx4 %0, %1, %2 offen sc1 lds" ::"v"(e.vl), "s"(e.sx), "s"(4096) : "memory");
  else if constexpr (S < 26) {
    constexpr int f = S - 10;
    asm volatile("buffer_load_dwordx4 a[%c0:%c1], %2, s[92:95], s96 offen offset:%3 sc1" ::"i"(128 + 4 * f), "i"(131 + 4 * f), "v"(e.vq),
                 "i"((f & 3) * 1024) : "memory");
    if constexpr ((f & 3) == 3) asm volatile("s_addk_i32 s96, 4096");
  } else {
    constexpr int f = S - 26;
    asm volatile("buffer_load_dwordx4 %0, s[92:95], s98 offen offset:%1 sc1 lds" ::"v"(e.vk[(f >> 2) & 1]), "i"((f & 3) * 1024) : "memory");
    if constexpr (f == 7) asm volatile("s_add_u32 s98, s98, 0x8000");
  }
}
constexpr int e_younger(int G0) {
  int n = e_nst(3, G0 + 1, 128);
  for (int G = G0 + 1; G < 128; ++G) n += e_slot(G) >= 0;
  return n;
}
constexpr int W_CONV = e_younger(E_CONV), W_K0 = e_younger(E_K0);
template <bool TR, int P, int H, int G, int MODE> DI void g_gap(GM& g, const GE& e) {
  constexpr int m = 4 * H + (G >> 4), q = G & 15, n = q >> 1, ks = q & 1, GG = 64 * H + G;
  constexpr bool inV = TR ? m & 1 : n & 1;
  constexpr int a = 96 + 8 * (m & 1) + 4 * ks, b = 192 - 64 * P + 8 * n + 4 * ks, d = (inV ? 128 : 0) + 4 * (TR ? 8 * (m >> 1) + n : 4 * m + (n >> 1));
  constexpr int J = MODE == 3 ? -1 : q == 8 ? 2 * (G >> 4) : q == 12 ? 2 * (G >> 4) + 1 : -1, gj = (J & 3) ^ ((J & 3) >> 1);
  if constexpr (MODE == 3) { if constexpr (q == 0 && GG > 0) asm volatile("s_waitcnt lgkmcnt(0)"); e_m0<e_slot(GG)>(e); }
  else if constexpr (q == 0 && G > 0) asm volatile("s_waitcnt lgkmcnt(4)");
  if constexpr (J == 0) asm volatile("s_add_u32 m0, %0, %1" ::"s"(g.mA), "i"((P ^ H ^ 1) * 32768 + (1 - H) * 8192) : "scc");
  else if constexpr (J == 4) asm volatile("s_add_u32 m0, %0, %1" ::"s"(g.mB), "i"((P ^ H) * 32768 + (1 - H) * 16384) : "scc");
  else if constexpr (J > 0 && (J & 3) == 1) asm volatile("s_bitset1_b32 m0, 12");
  else if constexpr (J > 0 && (J & 3) == 2) asm volatile("s_bitset1_b32 m0, %0" ::"i"(J < 4 ? 14 : 13));
  else if constexpr (J > 0) asm volatile("s_bitset0_b32 m0, 12");
#define GMF(D, X, Y, C) asm volatile("v_mfma_f32_16x16x32_bf16 " D "[%c0:%c1], " X ", " Y ", " C ::"i"(d), "i"(d + 3), "i"(a), "i"(a + 3), "i"(b), "i"(b + 3))
#define GMF2(D, C) do { if constexpr (TR) GMF(D, "v[%c2:%c3]", "a[%c4:%c5]", C); else GMF(D, "a[%c4:%c5]", "v[%c2:%c3]", C); } while (0)
  if constexpr ((MODE == 1 || MODE == 4) && ks == 0) { if constexpr (inV) GMF2("v", "0"); else GMF2("a", "0"); }
  else if constexpr (inV) GMF2("v", "v[%c0:%c1]");
  else GMF2("a", "a[%c0:%c1]");
#undef GMF2
#undef GMF
  if constexpr (e_st(MODE, GG) >= 0) {
    static_assert(MODE == 4 || ESCH<TR, MODE == 2>.op0[GG] >= (e_st(MODE, GG) + 1) * (MODE == 2 ? 16 : 8));
    d_store<e_st(MODE, GG)>();
  }
  if constexpr (MODE == 2 || MODE == 3) e_ops<TR, MODE == 2, ESCH<TR, MODE == 2>.op0[GG]>(make_iseq<ESCH<TR, MODE == 2>.nop[GG]>{});
#define GD(VO, SRD, SO) asm volatile("buffer_load_dwordx4 %0, %1, %2 offen lds" ::"v"(VO), "s"(SRD), "s"(SO) : "memory")
  if constexpr (J >= 0 && J < 4) GD(g.voA, g.srdA, g.sA[(1 - H) * 2 + (gj & 1) + (gj >> 1) * 4]);
  else if constexpr (J >= 4) GD(g.voB, g.srdB, ((1 - H) * 32 + 16 * (gj & 1)) * LDW * 2 + (gj >> 1) * 256);
#undef GD
  if constexpr ((q == 1 || q == 2) && !(MODE == 3 && m == 7)) g_aread<(m < 7 ? P : P ^ 1), (m + 1) & 7, q - 1>(g);
  if constexpr (MODE == 3) {
    if constexpr (e_slot(GG) >= 0) e_load<e_slot(GG)>(e);
  } else {
    if constexpr (q >= 3 && q < 11 && (q & 1)) g_wread<P ^ 1, H, (G >> 4) * 4 + (q - 3) / 2>(g);
    if constexpr (H == 1 && G == 0) asm volatile("s_mov_b32 %0, %1" : "=s"(g.kA) : "s"(g.kW));
    if constexpr (H == 1 && G == 4) asm volatile("v_lshl_add_u32 %0, %2, 7, %3\n\ts_bfe_u32 %1, %4, 0x40000" : "=v"(g.voA), "=s"(g.kW) : "s"(g.kA), "v"(g.vbA), "s"((unsigned)g.tab) : "scc");
    if constexpr (H == 1 && G == 6) asm volatile("s_lshr_b64 %0, %0, 4\n\ts_lshl_b32 %1, %2, 19" : "+s"(g.tab), "=&s"(g.kB) : "s"(g.kW) : "scc");
    if constexpr (H == 1 && G == 10) asm volatile("v_add_u32 %0, %1, %2" : "=v"(g.voB) : "s"(g.kB), "v"(g.vbB));
    constexpr int nst = e_nst(MODE, H ? 29 : 0, GG + 1);
    if constexpr (G == 61) asm volatile("s_waitcnt vmcnt(%0) lgkmcnt(0)\n\ts_barrier" ::"i"(12 + nst) : "memory");
  }
  if constexpr ((MODE == 2 || MODE == 3) && GG == 127) {
    asm volatile("s_nop 7");
    e_ops<TR, MODE == 2, ESCH<TR, MODE == 2>.op0[128]>(make_iseq<ESCH<TR, MODE == 2>.nop[128]>{});
  }
  GAP();
}
template <bool TR, int P, int H, int MODE, int... Gs> DI void g_half_(GM& g, const GE& e, iseq<Gs...>) { (g_gap<TR, P, H, Gs, MODE>(g, e), ...); }
template <bool TR, int P, int H, int MODE> DI void g_half(GM& g, const GE& e) { g_half_<TR, P, H, MODE>(g, e, make_iseq<64>{}); }
template <int P, int... Is> DI void g_read0(GM& g, iseq<Is...>) {
  (g_wread<P, 0, Is>(g), ...); (g_wread<P, 1, Is>(g), ...);
  g_aread<P, 0, 0>(g); g_aread<P, 0, 1>(g);
}
struct GItem { const bf16* A; const bf16* W; int lda; u64 tab; };
DI void g_srcA(GM& g, const GItem& it, int wid, int lane) {
  g.srdA = __builtin_amdgcn_make_buffer_rsrc((void*)it.A, (short)0, it.A ? 256 * it.lda * 2 : 0, 0x00020000);
  g.vbA = ((wid * 8 + (lane >> 3)) * it.lda + ((lane & 7) ^ (((wid & 1) * 4 + (lane >> 4)) & 7)) * 8) * 2;
#pragma unroll
  for (int j = 0; j < 8; ++j) g.sA[j] = j * 64 * it.lda;
  if (it.A == nullptr) g.mA = 0x28000;
}
DI void g_srcB(GM& g, const GItem& it) {
  g.srdB = __builtin_amdgcn_make_buffer_rsrc((void*)it.W, (short)0, it.A ? (it.lda - 1) * LDW * 2 + 512 : 0, 0x00020000);
  g.tab = it.tab;
  if (it.A == nullptr) g.mB = 0x28000;
}
DI void g_fix(GM& g) {
  g.tab = ((u64)(unsigned)__builtin_amdgcn_readfirstlane((unsigned)(g.tab >> 32)) << 32) | (unsigned)__builtin_amdgcn_readfirstlane((unsigned)g.tab);
  g.mA = __builtin_amdgcn_readfirstlane(g.mA); g.mB = __builtin_amdgcn_readfirstlane(g.mB); g.kW = __builtin_amdgcn_readfirstlane(g.kW);
#pragma unroll
  for (int j = 1; j < 8; ++j) g.sA[j] = __builtin_amdgcn_readfirstlane(g.sA[j]);
}
template <int I> DI void g_pro1(GM& g, unsigned po) {
  constexpr int grp = I >> 2, i = I & 3, t = grp < 4 ? 0 : grp < 7 ? 1 : 2;
  constexpr bool isA = grp == 0 || grp == 3 || grp == 5;
  const unsigned k = ((unsigned)g.tab >> (4 * t)) & 15, so = t == 1 ? po ^ 32768 : po;
  if constexpr (isA) {
    constexpr int jj = (grp == 3 ? 2 : 0) + (i & 1) + (i >> 1) * 4;
    DMA16(g.mA + so + jj * 4096, g.vbA, g.srdA, g.sA[jj] + k * 128);
  } else {
    constexpr int ks = grp == 2 || grp == 6;
    DMA16(g.mB + so + ks * 16384 + (i >> 1) * 8192 + (i & 1) * 4096, g.vbB, g.srdB, k * 0x80000 + (ks * 32 + 16 * (i & 1)) * LDW * 2 + (i >> 1) * 256);
  }
}
template <int B, int... Is> DI void g_pro(GM& g, unsigned po, iseq<Is...>) { (g_pro1<B + Is>(g, po), ...); }

typedef __attribute__((ext_vector_type(4))) unsigned u32x4;
typedef __attribute__((ext_vector_type(4))) float f32x4;

constexpr int R_S = 128, R_P = 192, R_CI = 224, R_E = 96;
constexpr int R_TS = 112, R_RS = 114, R_M = 116;
constexpr int R_PD = 118, R_THR = 120, R_TMP = 121;
constexpr int r_dk(int q) { return q < 6 ? 90 + q : 120 + q; }
constexpr int S_V = 96, S_T = 97, S_K = 98, S_I = 99, S_EV = 100;
DI void set_srdk(const void* p, unsigned n) {
  asm volatile("s_mov_b64 s[92:93], %0\n\ts_mov_b32 s94, %1\n\ts_mov_b32 s95, 0x20000" ::"s"(p), "s"(n));
}
struct AC {
  unsigned ka, va, dw[2], vq;
  __amdgpu_buffer_rsrc_t sw;
  unsigned m0x[2];
  u64 redo;
  unsigned rare;
  int mrow;
  bool diag0;
};
template <int X, int F> DI void kread(AC& c) {
  constexpr int r = 192 + 4 * (F & 15);
  asm volatile("ds_read_b128 a[%c1:%c2], %0 offset:%c3" ::"v"(c.ka), "i"(r), "i"(r + 3), "i"(X * 32768 + F * 1024));
}
template <int X, int F> DI void vread(AC& c) {
  constexpr int r = 192 + 4 * (F & 15);
  asm volatile("ds_read_b128 a[%c1:%c2], %0 offset:%c3" ::"v"(c.va), "i"(r), "i"(r + 3), "i"(X * 32768 + (F & 1) * 16384 + (F >> 1) * 1024));
}
template <int X, int... Fs> DI void kreads(AC& c, iseq<Fs...>) { (kread<X, Fs>(c), ...); }
template <int X, int J> DI void dma_m0(AC& c) {
  if constexpr (J == 0) asm volatile("s_mov_b32 m0, %0" ::"s"(c.m0x[X]));
  else if constexpr (J == 4) asm volatile("s_or_b32 m0, %0, 0x1000" ::"s"(c.m0x[X]) : "scc");
  else if constexpr (J == 8) asm volatile("s_bitset0_b32 m0, 16");
  else if constexpr (J == 12) asm volatile("s_bitset0_b32 m0, 12");
}
template <int J> DI void dma_at(AC& c) {
  if constexpr (J < 8)
    asm volatile("buffer_load_dwordx4 %0, %1, s96 offen offset:%2 sc1 lds" ::"v"(c.dw[J >= 4]), "s"(c.sw), "i"((J & 3) * 1024) : "memory");
  else
    asm volatile("buffer_load_dwordx4 v%c0, s[92:95], s98 offen offset:%1 sc1 lds" ::"i"(r_dk(J - 8)), "i"((J & 3) * 1024) : "memory");
}
DI void dma4k(const AC& c, unsigned so, unsigned m0v, int hf) {
    asm volatile("s_mov_b32 m0, %0\n\ts_nop 4\n\tbuffer_load_dwordx4 %1, %2, %3 offen sc1 lds\n\tbuffer_load_dwordx4 %1, %2, %3 offen offset:1024 sc1 lds\n\t"
                 "buffer_load_dwordx4 %1, %2, %3 offen offset:2048 sc1 lds\n\tbuffer_load_dwordx4 %1, %2, %3 offen offset:3072 sc1 lds"
                 ::"s"(m0v + hf * 4096), "v"(c.dw[hf]), "s"(c.sw), "s"(so) : "memory");
}
DI void dma8k(const AC& c, unsigned so, unsigned m0v) { dma4k(c, so, m0v, 0); dma4k(c, so, m0v, 1); }
template <int S_> DI void q_load1(unsigned vo, __amdgpu_buffer_rsrc_t srd, unsigned so) {
  asm volatile("buffer_load_dwordx4 a[%c0:%c1], %2, %3, %4 offen offset:%5 sc1" ::"i"(128 + 4 * S_), "i"(128 + 4 * S_ + 3), "v"(vo), "s"(srd),
               "s"(so + (S_ >> 2) * 4096), "i"((S_ & 3) * 1024) : "memory");
}
template <int Q> DI void acc_zero2() {
  asm volatile("v_accvgpr_write_b32 a[%c0], 0\n\tv_accvgpr_write_b32 a[%c1], 0" ::"i"(2 * Q), "i"(2 * Q + 1));
}
struct SMS { int ex[128], op0[128], nop[128], last; };
constexpr SMS sm_sched() {
  SMS z{};
  int et[32] = {}, e = 0;
  for (int t = 0; t < 128; ++t) {
    const int g = t & 15;
    const bool x = t < 48 ? (g >= 8 && (g & 1)) : (g >= 8 && g != 10 && g != 13 && g != 15);
    z.ex[t] = x && e < 32 ? e : -1;
    if (z.ex[t] >= 0) et[e++] = t;
  }
  int o = 0;
  for (int t = 0; t < 128; ++t) {
    const int g = t & 15;
    int cap = g < 8 ? (g == 0 ? 0 : 1) : (z.ex[t] >= 0 || t < 48 ? 0 : 2);
    z.op0[t] = o; z.nop[t] = 0;
    while (cap > 0 && o < 48 && et[2 * (o / 3) + (o % 3 == 0 ? 0 : 1)] + 2 <= t) {
      const int el = 2 * (o / 3) + o % 3;
      if (!(o % 3 < 2 && (el == 0 || el == 4))) --cap;
      ++o; ++z.nop[t]; z.last = t;
    }
  }
  return z;
}
constexpr SMS SM = sm_sched();
static_assert(SM.last <= 115);
constexpr int p_reg(int Y, int k) { return R_P + 16 * Y + 4 * ((k >> 3) * 2 + ((k >> 1) & 1)) + ((k >> 2) & 1) * 2 + (k & 1); }
template <int Y, int O> DI void sm_op() {
  constexpr int k = O / 3, e = 2 * k + O % 3, ts = R_TS + ((e >> 2) & 1);
  if constexpr (O % 3 == 2) asm volatile("v_cvt_pk_bf16_f32 v%c0, v%c1, v%c2" ::"i"(p_reg(Y, k)), "i"(R_E + ((2 * k) & 15)), "i"(R_E + ((2 * k + 1) & 15)));
  else if constexpr (e == 1 || e == 5) asm volatile("v_add_f32_e32 v%c0, v%c1, v%c2" ::"i"(ts), "i"(R_E + e - 1), "i"(R_E + e));
  else if constexpr (e != 0 && e != 4) asm volatile("v_add_f32_e32 v%c0, v%c0, v%c1" ::"i"(ts), "i"(R_E + (e & 15)));
}
template <int Y, int T> DI void sm_gap(AC& c) {
  if constexpr (SM.ex[T] >= 0) asm volatile("v_exp_f32 v%c0, v%c1" ::"i"(R_E + (SM.ex[T] & 15)), "i"(R_S + 32 * Y + SM.ex[T]));
  if constexpr (SM.nop[T] > 0) sm_op<Y, SM.op0[T]>();
  if constexpr (SM.nop[T] > 1) sm_op<Y, SM.op0[T] + 1>();
  if constexpr (SM.nop[T] > 2) sm_op<Y, SM.op0[T] + 2>();
  if constexpr (T == SM.last + 1) asm volatile("v_add_f32_e32 v%c0, v%c1, v%c2" ::"i"(R_TMP), "i"(R_TS), "i"(R_TS + 1));
  if constexpr (T == SM.last + 2) asm volatile("v_cmp_nle_f32_e64 %0, v%c1, v%c2" : "=s"(c.redo) : "i"(R_TMP), "i"(R_THR));
}
template <int X, bool FIRST, bool NOQK, int G> DI void gapA(AC& c) {
  constexpr int Xp = X ^ 1, kb = G >> 4, g = G & 15, qb = g >> 3, s = g & 7;
  if constexpr (NOQK && G < 48 && G % 3 == 0) q_load1<G / 3>(c.vq, c.sw, c.rare);
  if constexpr (G == 48) asm volatile("s_waitcnt vmcnt(%0) lgkmcnt(0)\n\ts_barrier" ::"i"(NOQK ? 16 : FIRST ? 12 : 0) : "memory");
  else if constexpr (g == 0 && !NOQK) asm volatile("s_waitcnt lgkmcnt(0)");
  if constexpr (!NOQK) {
    constexpr int r = 192 + 4 * ((kb & 1) * 8 + s), q = 128 + 4 * g;
    constexpr int d = R_S + 32 * X + 4 * (kb * 2 + qb), ci = s == 0 ? R_CI + 4 * (kb * 2 + qb) : d;
    if constexpr (FIRST && s == 0)
      asm volatile("v_mfma_f32_16x16x32_bf16 v[%c0:%c1], a[%c2:%c3], a[%c4:%c5], 0" ::"i"(d), "i"(d + 3), "i"(r), "i"(r + 3), "i"(q), "i"(q + 3));
    else
      asm volatile("v_mfma_f32_16x16x32_bf16 v[%c0:%c1], a[%c2:%c3], a[%c4:%c5], v[%c6:%c7]" ::"i"(d), "i"(d + 3), "i"(r), "i"(r + 3), "i"(q), "i"(q + 3),
                   "i"(ci), "i"(ci + 3));
    if constexpr (g < 8 && kb < 3) kread<X, (kb + 1) * 8 + g>(c);
    if constexpr (G == 54) asm volatile("s_add_u32 s96, s97, 0x1000000" ::: "scc");
    if constexpr (G == 55) dma_m0<X, 0>(c);
    if constexpr (G >= 56 && !(G & 1)) dma_at<(G - 56) / 2>(c);
    if constexpr (G == 57) asm volatile("s_mov_b32 s97, s98\n\ts_lshr_b32 s100, s100, 1" ::: "scc");
    if constexpr (G == 63) asm volatile("s_add_u32 s98, s98, s99" ::: "scc");
    if constexpr (G == 59 && !FIRST) asm volatile("s_and_b32 s101, s100, 1\n\ts_or_b32 s101, s101, %0" ::"s"((unsigned)c.redo) : "scc");
    if constexpr (G == 61 && !FIRST) asm volatile("s_or_b32 %0, s101, %1" : "=s"(c.rare) : "s"((unsigned)(c.redo >> 32)) : "scc");
  } else if constexpr (SM.nop[64 + G] > 0) asm volatile("s_nop 0");
  if constexpr (!FIRST && g < 8 && kb == 3) vread<Xp, g>(c);
  if constexpr (FIRST && (G & 3) == 1) d_store<16 + G / 4>();
  if constexpr (FIRST) acc_zero2<G>();
  else sm_gap<Xp, 64 + G>(c);
  GAP();
}
template <int R> DI void rescale1(float alpha) {
  float t;
  asm volatile("v_accvgpr_read_b32 %0, a[%c2]\n\tv_mul_f32 %0, %0, %1\n\tv_accvgpr_write_b32 a[%c2], %0" : "=&v"(t) : "v"(alpha), "i"(R));
}
template <int... Rs> DI void rescale_all(const float (&alpha)[2], iseq<Rs...>) { (rescale1<Rs>(alpha[(Rs >> 2) & 1]), ...); }
template <bool MAX> DI float row_reduce(float v) {
  auto p = __builtin_amdgcn_permlane32_swap(__float_as_uint(v), __float_as_uint(v), false, false);
  v = MAX ? fmaxf(__uint_as_float(p[0]), __uint_as_float(p[1])) : __uint_as_float(p[0]) + __uint_as_float(p[1]);
  p = __builtin_amdgcn_permlane16_swap(__float_as_uint(v), __float_as_uint(v), false, false);
  return MAX ? fmaxf(__uint_as_float(p[0]), __uint_as_float(p[1])) : __uint_as_float(p[0]) + __uint_as_float(p[1]);
}
template <int Y, int QB> DI float row_max() {
  constexpr int r = R_S + 32 * Y + 4 * QB;
  float mx;
  asm volatile("v_max_f32 %0, v%c1, v%c2\n\tv_max3_f32 %0, %0, v%c3, v%c4" : "=&v"(mx) : "i"(r), "i"(r + 1), "i"(r + 2), "i"(r + 3));
  static_for([&](auto K) {
    constexpr int q = r + 8 * (K + 1);
    float t = mx;
    asm volatile("v_max3_f32 %0, %0, v%c1, v%c2\n\tv_max3_f32 %0, %0, v%c3, v%c4" : "+v"(t) : "i"(q), "i"(q + 1), "i"(q + 2), "i"(q + 3));
    mx = t;
  }, make_iseq<3>{});
  return row_reduce<true>(mx);
}
template <int R> DI void v_sub(float d) { asm volatile("v_sub_f32 v%c0, v%c0, %1" ::"i"(R), "v"(d)); }
template <int B> DI void s_shift(const float (&d)[2]) { static_for([&](auto E) { v_sub<B + E>(d[(E >> 2) & 1]); }, make_iseq<32>{}); }
DI void m_shift(const float (&d)[2]) {
  s_shift<R_CI>(d);
  v_sub<R_M>(-d[0]); v_sub<R_M + 1>(-d[1]);
}
template <int B, bool INPL> DI void set_mask(const AC& c, int t64) {
  const int mr = c.mrow - t64;
  const float ninf = -INFINITY;
  static_for([&](auto I) {
    constexpr int i = I, r = B + 4 * i, k = (i >> 1) * 16 - (i & 1) * 16;
    const int mr_ = mr; const float b = ninf;
    if constexpr (INPL)
      asm volatile("v_cmp_gt_i32 vcc, %c4, %8\n\tv_cndmask_b32 v%c0, v%c0, %9, vcc\n\tv_cmp_gt_i32 vcc, %c5, %8\n\tv_cndmask_b32 v%c1, v%c1, %9, vcc\n\t"
                   "v_cmp_gt_i32 vcc, %c6, %8\n\tv_cndmask_b32 v%c2, v%c2, %9, vcc\n\tv_cmp_gt_i32 vcc, %c7, %8\n\tv_cndmask_b32 v%c3, v%c3, %9, vcc"
                   ::"i"(r), "i"(r + 1), "i"(r + 2), "i"(r + 3), "i"(k), "i"(k + 1), "i"(k + 2), "i"(k + 3), "v"(mr_), "v"(b) : "vcc");
    else
      asm volatile("v_cmp_gt_i32 vcc, %c4, %8\n\tv_cndmask_b32_e64 v%c0, -v%c10, %9, vcc\n\tv_cmp_gt_i32 vcc, %c5, %8\n\tv_cndmask_b32_e64 v%c1, -v%c10, %9, vcc\n\t"
                   "v_cmp_gt_i32 vcc, %c6, %8\n\tv_cndmask_b32_e64 v%c2, -v%c10, %9, vcc\n\tv_cmp_gt_i32 vcc, %c7, %8\n\tv_cndmask_b32_e64 v%c3, -v%c10, %9, vcc"
                   ::"i"(r), "i"(r + 1), "i"(r + 2), "i"(r + 3), "i"(k), "i"(k + 1), "i"(k + 2), "i"(k + 3), "v"(mr_), "v"(b), "i"(R_M + (i & 1)) : "vcc");
  }, make_iseq<8>{});
  asm volatile("s_nop 1");
}
template <int X> DI void sm_redo() {
  constexpr int Xp = X ^ 1;
  float d[2], dt[2], alpha[2], ts[2] = {0.f, 0.f};
  static_for([&](auto Q) {
    constexpr int qb = Q;
    const float pd = vget<R_PD + qb>();
    d[qb] = fmaxf(row_max<Xp, qb>() - pd, 0.f);
    dt[qb] = d[qb] + pd;
    vset<R_PD + qb>(d[qb]);
    alpha[qb] = __builtin_amdgcn_exp2f(-d[qb]);
    vset<R_RS + qb>(vget<R_RS + qb>() * alpha[qb]);
  }, make_iseq<2>{});
  vset<R_THR>(d[0] > 0.f || d[1] > 0.f ? -1.f : 4096.f);
  m_shift(d);
  static_for([&](auto K) {
    constexpr int k = K, qb = (k >> 1) & 1;
    const float e0 = __builtin_amdgcn_exp2f(vget<R_S + 32 * Xp + 2 * k>() - dt[qb]), e1 = __builtin_amdgcn_exp2f(vget<R_S + 32 * Xp + 2 * k + 1>() - dt[qb]);
    ts[qb] += e0; ts[qb] += e1;
    vset<p_reg(Xp, k)>(__uint_as_float(pk2(e0, e1)));
  }, make_iseq<16>{});
  vset<R_TS>(ts[0]); vset<R_TS + 1>(ts[1]);
  rescale_all(alpha, make_iseq<128>{});
  asm volatile("s_nop 1" ::: "memory");
  GAP();
}
template <int X, bool FIRST, bool NOQK, int... Gs> DI void phaseA_(AC& c, iseq<Gs...>) {
  (gapA<X, FIRST, NOQK, Gs>(c), ...);
}
template <int X, bool FIRST, bool NOQK> DI void phaseA(AC& c) { phaseA_<X, FIRST, NOQK>(c, make_iseq<64>{}); }

template <int X, bool SMX, int G> DI void gapB(AC& c) {
  constexpr int Xp = X ^ 1, blk = G >> 4, g = G & 15;
  if constexpr (g == 0) asm volatile("s_waitcnt lgkmcnt(0)");
  if constexpr (SMX && g == 7 && blk < 3) dma_m0<X, blk * 4 + 4>(c);
  constexpr int qb = (g >> 1) & 1, s = g & 1, r = 192 + 4 * ((blk & 1) * 8 + (g >> 2) * 2 + s), o = 4 * ((blk * 4 + (g >> 2)) * 2 + qb);
  constexpr int p = R_P + 16 * Xp + 4 * (s * 2 + qb);
  asm volatile("v_mfma_f32_16x16x32_bf16 a[%c0:%c1], a[%c2:%c3], v[%c4:%c5], a[%c0:%c1]" ::"i"(o), "i"(o + 3), "i"(r), "i"(r + 3), "i"(p), "i"(p + 3));
  if constexpr (SMX && (G == 1 || G == 2)) asm volatile("v_add_f32_e32 v%c0, v%c0, v%c1" ::"i"(R_RS + G - 1), "i"(R_TS + G - 1));
  if constexpr (g < 8) {
    if constexpr (blk < 3) vread<Xp, (blk + 1) * 8 + g>(c);
    else if constexpr (SMX) kread<Xp, g>(c);
  }
  if constexpr (SMX) {
    if constexpr (g >= 8 && !(g & 1) && blk < 3) dma_at<blk * 4 + 4 + (g - 8) / 2>(c);
    sm_gap<X, G>(c);
  }
  GAP();
}
template <int X, int... Gs> DI void phaseB_(AC& c, iseq<Gs...>) { (gapB<X, true, Gs>(c), ...); }
template <int X> DI void phaseB(AC& c) { phaseB_<X>(c, make_iseq<64>{}); }
constexpr int sm_nex() { int n = 0; for (int t = 0; t < 64; ++t) n += SM.ex[t] >= 0; return n; }
DI void phaseB0(AC& c) {
  dma_m0<0, 4>(c);
  kreads<1>(c, make_iseq<8>{});
  dma_at<4>(c);
  asm volatile("s_nop 3");
  if (__builtin_expect(c.diag0, 0)) set_mask<R_S, true>(c, 0);
  float d[2];
  d[0] = row_max<0, 0>(); GAP(); dma_at<5>(c);
  d[1] = row_max<0, 1>(); GAP(); dma_at<6>(c);
#pragma unroll
  for (int qb = 0; qb < 2; ++qb) if (!(d[qb] > -3.0e38f)) d[qb] = 0.f;
  vset<R_M>(d[0]); vset<R_M + 1>(d[1]);
  const f32x2c nd[2] = {{-d[0], -d[0]}, {-d[1], -d[1]}};
  static_for([&](auto E) {
    constexpr int e = E;
    const f32x2c v = nd[(e >> 1) & 1];
    asm volatile("v_pk_add_f32 v[%c0:%c1], v[%c0:%c1], %4\n\tv_mov_b64 v[%c2:%c3], %4" ::"i"(R_S + 2 * e), "i"(R_S + 2 * e + 1), "i"(R_CI + 2 * e), "i"(R_CI + 2 * e + 1), "v"(v));
    if constexpr (e == 3) dma_at<7>(c);
    if constexpr (e == 6) dma_m0<0, 8>(c);
    if constexpr (e == 7 || e == 11 || e == 15) dma_at<8 + (e - 7) / 4>(c);
  }, make_iseq<16>{});
  if (__builtin_expect(c.diag0, 0)) set_mask<R_CI, false>(c, 64);
  static_assert(sm_nex() >= 16 && SM.op0[64] >= 3 * ((sm_nex() - 15) / 2) && SM.op0[64] <= 24);
  static_for([&](auto E) {
    constexpr int e = E;
    asm volatile("v_exp_f32 v%c0, v%c1" ::"i"(R_E + e), "i"(R_S + e));
    if constexpr (e == 3) dma_at<11>(c);
    if constexpr (e == 6) dma_m0<0, 12>(c);
    if constexpr (e == 7 || e == 11 || e == 15) dma_at<12 + (e - 7) / 4>(c);
  }, make_iseq<16>{});
  static_for([&](auto O) { sm_op<0, O>(); }, make_iseq<SM.op0[64]>{});
  dma_at<15>(c);
  static_for([&](auto E) { asm volatile("v_exp_f32 v%c0, v%c1" ::"i"(R_E + (int)E), "i"(R_S + 16 + (int)E)); }, make_iseq<sm_nex() - 16>{});
  GAP();
}

#define ACLOB_ALL "a0","a1","a2","a3","a4","a5","a6","a7","a8","a9","a10","a11","a12","a13","a14","a15","a16","a17","a18","a19","a20","a21","a22","a23","a24","a25","a26","a27","a28","a29","a30","a31","a32","a33","a34","a35","a36","a37","a38","a39","a40","a41","a42","a43","a44","a45","a46","a47","a48","a49","a50","a51","a52","a53","a54","a55","a56","a57","a58","a59","a60","a61","a62","a63","a64","a65","a66","a67","a68","a69","a70","a71","a72","a73","a74","a75","a76","a77","a78","a79","a80","a81","a82","a83","a84","a85","a86","a87","a88","a89","a90","a91","a92","a93","a94","a95","a96","a97","a98","a99","a100","a101","a102","a103","a104","a105","a106","a107","a108","a109","a110","a111","a112","a113","a114","a115","a116","a117","a118","a119","a120","a121","a122","a123","a124","a125","a126","a127","a128","a129","a130","a131","a132","a133","a134","a135","a136","a137","a138","a139","a140","a141","a142","a143","a144","a145","a146","a147","a148","a149","a150","a151","a152","a153","a154","a155","a156","a157","a158","a159","a160","a161","a162","a163","a164","a165","a166","a167","a168","a169","a170","a171","a172","a173","a174","a175","a176","a177","a178","a179","a180","a181","a182","a183","a184","a185","a186","a187","a188","a189","a190","a191","a192","a193","a194","a195","a196","a197","a198","a199","a200","a201","a202","a203","a204","a205","a206","a207","a208","a209","a210","a211","a212","a213","a214","a215","a216","a217","a218","a219","a220","a221","a222","a223","a224","a225","a226","a227","a228","a229","a230","a231","a232","a233","a234","a235","a236","a237","a238","a239","a240","a241","a242","a243","a244","a245","a246","a247","a248","a249","a250","a251","a252","a253","a254","a255"

struct EP { float fa[2], fb[2]; int vo; unsigned so[4]; };
template <int I, int E, bool MERGE> DI void epi_pair(const EP& p) {
  constexpr int qb = I & 1, r = 16 * (I >> 1) + 4 * qb + (E >> 1) * 8 + (E & 1) * 2, t = 96 + 2 * E, w = 128 + 4 * I + E;
  if constexpr (MERGE) asm volatile("v_accvgpr_read_b32 v106, a[%c0]" ::"i"(w));
  asm volatile("v_accvgpr_read_b32 v%c0, a[%c2]\n\tv_accvgpr_read_b32 v%c1, a[%c3]\n\tv_mul_f32 v%c0, v%c0, %4\n\tv_mul_f32 v%c1, v%c1, %4"
               ::"i"(t), "i"(t + 1), "i"(r), "i"(r + 1), "v"(p.fa[qb]));
  if constexpr (MERGE)
    asm volatile("v_lshlrev_b32 v104, 16, v106\n\tv_and_b32 v105, 0xffff0000, v106\n\tv_fmac_f32 v%c0, v104, %2\n\tv_fmac_f32 v%c1, v105, %2"
                 ::"i"(t), "i"(t + 1), "v"(p.fb[qb]));
  asm volatile("v_cvt_pk_bf16_f32 v%c0, v%c1, v%c2" ::"i"(w), "i"(t), "i"(t + 1));
}
template <int I> DI void epi_store(const AC& c, const EP& p) {
  asm volatile("buffer_store_dwordx4 v[%c0:%c1], %2, %3, %4 offen offset:%5 sc1" ::"i"(128 + 4 * I), "i"(131 + 4 * I), "v"(p.vo), "s"(c.sw),
               "s"(p.so[I >> 2]), "i"((I & 3) * 1024) : "memory");
}
constexpr int R_TB = 107, R_VS = 111;
template <int HF, int R> DI void p_read() {
  constexpr int mb = R >> 4, r = 64 * HF + 2 * R;
  asm volatile("ds_read_b64_tr_b16 a[%c1:%c2], v%c0 offset:%c3" ::"i"(R_TB + 2 * HF + mb), "i"(r), "i"(r + 1), "i"((R & 15) * 2048));
}
template <int QB, int E> DI void p_cvt() {
  asm volatile("v_cvt_pk_bf16_f32 v%c0, v%c1, v%c2" ::"i"(208 + 4 * QB + E), "i"(192 + 8 * QB + 2 * E), "i"(193 + 8 * QB + 2 * E));
}
template <int QB, int PR> DI void p_store(const AC& c) {
  asm volatile("buffer_store_dwordx4 v[%c0:%c1], v%c2, %3, s96 offen offset:%4 sc1" ::"i"(208 + 4 * QB), "i"(211 + 4 * QB), "i"(R_VS + QB), "s"(c.sw),
               "i"(PR * 128) : "memory");
}
constexpr int p_dma(int HF, int G) { return G == 3 && !HF ? 0 : G == 11 ? 1 - HF : G == 15 ? 2 - HF : G == 19 ? 3 - HF : G == 27 && HF ? 3 : -1; }
template <int HF, int G> DI void p_gap(AC& c) {
  constexpr int ch = G >> 3, t = G & 7, a = 64 * HF + 32 * (ch & 1) + 4 * t, b = 128 + 4 * (2 * t + (ch >> 1)), d = 192 + 4 * ch;
  if constexpr (t == 0)
    asm volatile("v_mfma_f32_16x16x32_bf16 v[%c0:%c1], a[%c2:%c3], v[%c4:%c5], 0" ::"i"(d), "i"(d + 3), "i"(a), "i"(a + 3), "i"(b), "i"(b + 3));
  else
    asm volatile("v_mfma_f32_16x16x32_bf16 v[%c0:%c1], a[%c2:%c3], v[%c4:%c5], v[%c0:%c1]" ::"i"(d), "i"(d + 3), "i"(a), "i"(a + 3), "i"(b), "i"(b + 3));
  p_read<HF ^ 1, G>();
  if constexpr (G == 0) asm volatile("v_xor_b32 v%c0, s100, v%c0" ::"i"(R_TB + 2 * HF + 1));
  if constexpr (G == 17) asm volatile("v_xor_b32 v%c0, s100, v%c0" ::"i"(R_TB + 2 * (HF ^ 1)));
  if constexpr (G >= 4 && G <= 8 && G != 7) p_cvt<1, (G < 7 ? G - 4 : 3)>();
  if constexpr (G == 24 || (G == 7 && HF == 0)) asm volatile("s_waitcnt lgkmcnt(8)");
  if constexpr (G == 7 && HF == 1) asm volatile("s_waitcnt vmcnt(13) lgkmcnt(8)\n\ts_barrier" ::: "memory");
  if constexpr (G == 10) p_store<1, HF ^ 1>(c);
  if constexpr (G >= 20 && G < 24) p_cvt<0, G - 20>();
  if constexpr (G == 25) p_store<0, HF>(c);
  if constexpr (p_dma(HF, G) >= 0) dma_at<(HF ? 8 : 12) + p_dma(HF, G)>(c);
  if constexpr (HF == 0 && G == 2) asm volatile("s_bitset0_b32 m0, 12");
  if constexpr (HF == 0 && G == 13) asm volatile("s_add_u32 s96, s96, 0x40000");
  if constexpr (HF == 0 && G == 26) asm volatile("s_add_u32 s98, s98, 128");
  if constexpr (HF == 0 && G == 27) asm volatile("s_cmp_lt_u32 s98, 0x380");
  if constexpr (HF == 0 && G == 28) asm volatile("s_cselect_b32 s99, s99, 0x31000");
  if constexpr (HF == 1 && G == 1) asm volatile("s_xor_b32 s100, s100, 0x10000");
  if constexpr (HF == 1 && G == 2) asm volatile("s_xor_b32 s97, s97, s100");
  if constexpr (HF == 1 && G == 9) asm volatile("s_or_b32 m0, s97, s99");
  if constexpr (HF == 1 && G == 30) asm volatile("s_cmp_lt_u32 s98, 0x500");
  GAP();
}
template <int HF, int... Gs> DI void p_half(AC& c, iseq<Gs...>) { (p_gap<HF, Gs>(c), ...); }
constexpr int f_dma(int G) { return G == 2 ? 0 : G == 5 ? 1 : G == 8 ? 2 : G >= 12 && G <= 36 && G % 6 == 0 ? 1 + G / 6 : G >= 50 && G <= 68 && G % 6 == 2 ? G / 6 : -1; }
constexpr int f_ndma(int G) { int n = 0; for (int g = 0; g < G; ++g) n += f_dma(g) >= 0; return n; }
static_assert(f_ndma(74) == 12);
template <int MODE, int G> DI void gapF(AC& c, const EP& p) {
  if constexpr (MODE && G == 48) asm volatile("s_waitcnt lgkmcnt(0)\n\ts_barrier" ::: "memory");
  if constexpr (G < 64) gapB<0, false, G>(c);
  if constexpr (MODE == 2 && G >= 10 && (G - 10) % 16 == 0) asm volatile("s_waitcnt vmcnt(%0)" ::"i"(12 - (G - 10) / 4 + f_ndma(G)) : "memory");
  if constexpr (G >= 10) epi_pair<(G - 10) / 4, (G - 10) & 3, MODE == 2>(p);
  if constexpr (MODE == 0) {
    if constexpr (G >= 14 && ((G - 14) & 3) == 0) epi_store<(G - 14) / 4>(c, p);
    if constexpr (G == 73) epi_store<15>(c, p);
  } else {
    if constexpr (G == 0) asm volatile("s_or_b32 m0, %0, 0x1000\n\ts_movk_i32 s98, 256" ::"s"(c.m0x[0]) : "scc");
    if constexpr (G == 15) asm volatile("s_bitset0_b32 m0, 12");
    if constexpr (G == 48) asm volatile("s_or_b32 m0, %0, 0x1000\n\ts_movk_i32 s98, 384" ::"s"(c.m0x[1]) : "scc");
    if constexpr (f_dma(G) >= 0) dma_at<8 + (f_dma(G) & 7)>(c);
    if constexpr (G == 73) asm volatile("s_waitcnt vmcnt(4) lgkmcnt(0)" ::: "memory");
    if constexpr (G >= 48 && G < 64) { p_read<0, 2 * (G - 48)>(); p_read<0, 2 * (G - 48) + 1>(); }
  }
  GAP();
}
template <int MODE, int... Gs> DI void phaseF(AC& c, const EP& p, iseq<Gs...>) { (gapF<MODE, Gs>(c, p), ...); }
template <int K> DI void r_load(const AC& c, unsigned vo, unsigned so) {
  asm volatile("buffer_load_dwordx4 v[%c0:%c1], %2, %3, %4 offen offset:%5 sc1" ::"i"(128 + 4 * K), "i"(131 + 4 * K), "v"(vo), "s"(c.sw),
               "s"(so + ((K & 15) >> 2) * 4096), "i"((K & 1) * 512 + ((K >> 1) & 1) * 2048) : "memory");
}
template <int K, int NL> DI void r_sum() {
  constexpr int d = 100 + 8 * (K >> 4) + 4 * (K & 1), b = 128 + 4 * K;
  if constexpr ((K & 3) == 0) asm volatile("s_waitcnt vmcnt(%0)" ::"i"(NL - 4 - K) : "memory");
  if constexpr ((K & 15) < 2) asm volatile("v_mfma_f32_16x16x32_bf16 v[%c0:%c1], v[96:99], v[%c2:%c3], 0" ::"i"(d), "i"(d + 3), "i"(b), "i"(b + 3));
  else asm volatile("v_mfma_f32_16x16x32_bf16 v[%c0:%c1], v[96:99], v[%c2:%c3], v[%c0:%c1]" ::"i"(d), "i"(d + 3), "i"(b), "i"(b + 3));
}

#define CMAX 12
#define NYT 16
DI u64* fw(u64* p, int w) { return p + (w >> 4) * 544 + (w & 15); }
DI void set_flag(u64* p, u64 tag, int skip = 0) {
  asm volatile("s_cmp_lg_u32 %2, 0\n\ts_cbranch_scc1 1f\n\ts_mov_b64 exec, 1\n\tglobal_store_dwordx2 %0, %1, off sc1\n\ts_mov_b64 exec, -1\n1:"
               ::"v"(p), "v"(tag), "s"(skip) : "scc", "memory");
}
DI void wait_flags(u64* p, u64 need, u64 tag) {
  const u64* q = fw(p, threadIdx.x & 63);
  u64 v; unsigned cnt;
  asm volatile("s_mov_b32 %1, 0x100000\n\ts_mov_b64 exec, %3\n"
               "1:\n\tglobal_load_dwordx2 %0, %2, off sc1\n\ts_waitcnt vmcnt(0)\n\tv_cmp_eq_u64 vcc, %4, %0\n\t"
               "s_cmp_eq_u64 vcc, %3\n\ts_cbranch_scc1 2f\n\ts_sleep 4\n\ts_sub_u32 %1, %1, 1\n\ts_cbranch_scc0 1b\n"
               "2:\n\ts_mov_b64 exec, -1" : "=&v"(v), "=&s"(cnt) : "v"(q), "s"(need), "s"(tag) : "vcc", "scc", "memory");
}
template <int H> DI void fl_issue(u64* gf, u64 need) {
  const u64* q = fw(gf, threadIdx.x & 63) + H * 4 * 544;
  asm volatile("s_mov_b64 exec, %1\n\tglobal_load_dwordx2 v[%c2:%c3], %0, off sc1\n\ts_mov_b64 exec, -1" ::"v"(q), "s"(need), "i"(122 + 2 * H), "i"(123 + 2 * H) : "memory");
}
template <int H> DI void fl_check(u64* gf, u64 need, u64 tag) {
  u64 m;
  asm volatile("v_cmp_eq_u64 %0, %1, v[%c2:%c3]" : "=&s"(m) : "s"(tag), "i"(122 + 2 * H), "i"(123 + 2 * H) : "memory");
  if (__builtin_expect((m & need) != need, 0)) wait_flags(gf + H * 4 * 544, need, tag);
}
template <bool TR, bool SCL, class START, class MID, class PRE>
DI void g_item(GM& g, const GItem& nxt, u64* pend, u64& tag, unsigned out, int wid, int lane,
               u64* gf, u64 n0, u64 n1, unsigned evx, START&& start, MID&& mid, PRE&& pre) {
  constexpr int nkt = SCL ? 7 : 16, P0 = SCL ? 0 : 1;
  GE e;
  GAP();
  start(std::integral_constant<int, P0>{});
  g_fix(g);
  asm volatile("s_nop 4");
  GAP();
  auto hook = [&](int t) __attribute__((always_inline)) {
    if (t == 1) {
      set_flag(pend, tag, wid);
      mid(1);
    } else if (t < nkt - 3) mid(t);
    else if (t == nkt - 3) {
      g_srcB(g, nxt);
      if constexpr (!SCL) { fl_issue<0>(gf, n0); if (n1) fl_issue<1>(gf, n1); mid(t); }
    } else g_srcA(g, nxt, wid, lane);
    g_fix(g);
    asm volatile("s_nop 4");
    GAP();
  };
  g_half<TR, P0, 0, SCL ? 1 : 4>(g, e);
  g_half<TR, P0, 1, SCL ? 1 : 4>(g, e);
  unsigned evm = 2u | (4u << (nkt - 5)) | (4u << (nkt - 4)) | evx;
  asm volatile("" : "+s"(evm));
#pragma unroll 1
  for (int t = 1;; t += 2) {
    g_half<TR, P0 ^ 1, 0, 0>(g, e);
    if (__builtin_expect((evm >> t) & 1, 0)) hook(t);
    g_half<TR, P0 ^ 1, 1, 0>(g, e);
    if (SCL && t + 2 >= nkt) break;
    g_half<TR, P0, 0, 0>(g, e);
    if (__builtin_expect(t + 1 == (SCL ? nkt - 3 : nkt - 2), 0)) hook(t + 1);
    g_half<TR, P0, 1, 0>(g, e);
    if (!SCL && t + 3 >= nkt) break;
  }
  int l = threadIdx.x & 63; asm volatile("" : "+v"(l));
  vset<112>(__uint_as_float(l * 16)); vset<113>(__uint_as_float(l * 16 + (TR ? 4096 : 8192)));
  sset<101>(out + (wid >> 1) * 65536 + (wid & 1) * (TR ? 8192 : 4096));
  pre(e);
  if constexpr (!SCL) asm volatile("s_nop 4" : "+s"(e.sk), "+s"(e.sx), "+s"(e.mC), "+s"(e.mK));
  GAP();
  g_half<TR, 0, 0, SCL ? 2 : 3>(g, e);
  g_half<TR, 0, 1, SCL ? 2 : 3>(g, e);
}

constexpr size_t TSZ = (size_t)NH * SEQ * HD;
constexpr unsigned WSB = 5 * TSZ * 2 + 128 * 66560;
__global__ __launch_bounds__(256, 1) __attribute__((amdgpu_num_vgpr(90), amdgpu_num_sgpr(96))) void fused_kernel(const bf16* __restrict__ ckv, const bf16* __restrict__ wuk,
                                                       const bf16* __restrict__ wuv, const bf16* __restrict__ x,
                                                       const bf16* __restrict__ wq, bf16* Qb,
                                                       const bf16* __restrict__ ak, const bf16* __restrict__ av,
                                                       const bf16* __restrict__ ash, const bf16* __restrict__ assmk,
                                                       const bf16* __restrict__ assmD, const bf16* __restrict__ wo,
                                                       const bf16* __restrict__ aq, bf16* __restrict__ out) {
  extern __shared__ __attribute__((aligned(16))) char lds[];
  asm volatile("at_code_begin:" ::: ACLOB_ALL, "v255", "s101");
  asm volatile("" : "+s"(ckv), "+s"(wuk), "+s"(wuv), "+s"(x), "+s"(wq), "+s"(Qb), "+s"(ak), "+s"(av), "+s"(ash), "+s"(assmk),
                    "+s"(assmD), "+s"(wo), "+s"(aq), "+s"(out));
  float* partbuf = (float*)(Qb + 5 * TSZ);
  u64* flags = (u64*)(partbuf + (size_t)128 * 16640);
  bf16* xqT = (bf16*)(flags + 400 * 544);
  KST(0, 0);
  const int tid = threadIdx.x, lane = tid & 63;
  const int wid = __builtin_amdgcn_readfirstlane(tid >> 6);
  const int b = blockIdx.x, j = b & 7, r = b >> 3, rr = r & 15, hsel = rr & 1, pj = rr >> 1, kv = pj & 1;
  const bool isY = r >= 16;
  const int h = j + 8 * hsel, mt = (rr >> 2) + (isY ? 4 : 0);
  const unsigned lbase = (unsigned)(size_t)lds;
  u64* gf = fw(flags, j * 128);
  const unsigned m0d = lbase + 163840 - 256;
  const unsigned m0f = lbase + 131072 + wid * 8192;
  const int cch = rr * 4 + wid;
  const bool doA = !isY && cch < 56;
  u64 tag = 0;
  AC c;
  auto att_init = [&]() __attribute__((always_inline)) {
    int l = threadIdx.x & 63; asm volatile("" : "+v"(l));
    c.ka = lbase + l * 16;
    c.dw[0] = wid * 8192 + l * 16; c.dw[1] = c.dw[0] + 4096;
    c.sw = __builtin_amdgcn_make_buffer_rsrc((void*)Qb, (short)0, WSB, 0x00020000);
    static_for([&](auto I) { vset<r_dk(I)>(__uint_as_float(c.dw[I < 4])); }, make_iseq<8>{});
    sset<S_I>(0x8000);
  };
  const unsigned oQ = (unsigned)h << 20, oK = oQ + (unsigned)(TSZ * 2);
  auto pro_k = [&](int t0) __attribute__((always_inline)) {
    dma8k(c, oK + t0 * 32768, lbase + wid * 8192);
    dma8k(c, oK + t0 * 32768 + 32768, lbase + 32768 + wid * 8192);
  };
  static_assert(NYT <= 16 && NYT >= 12 && !(NYT & 1));
  const bool solo = pj == 7;
  const bool doP = isY != solo;
  const int NY = solo ? 18 : NYT;
  const int nx0 = 32 - 2 * pj - NY, cx = nx0 < CMAX ? nx0 : CMAX, ex = nx0 - cx;
  const u64 needY = (((0x101ull << (((31 - 2 * pj) >> 2) + 1)) - 0x1010ull) << (hsel * 16)) & ~(1ull << (hsel * 16 + kv * 8 + mt));
  const u64 needX = ((0x1010ull << ((ex + 3) >> 2)) - 0x1010ull) << (hsel * 16);
  const u64 needQ = (1ull << (32 + hsel * 8 + (pj >> 1))) | (1ull << (32 + hsel * 8 + ((15 - pj) >> 1)));
  const u64 needA = 0x0f0full << (hsel * 16);
  const u64 needW = 0x3fffull << 36;
  u64* gflag = fw(gf, hsel * 16 + kv * 8 + mt);
  unsigned dv = 0;
  {
    GM g;
    PR pr;
    const u64 tabQ = 0x6543210ull;
    const int cls = (rr + (rr >> 2)) & 3;
    const u64 tabX = cls == 0 ? 0xfedcb9875a463210ull : cls == 1 ? 0xefadc6982b574310ull : cls == 2 ? 0xfebad7693c285410ull : 0xefcba8764d392510ull;
    const u64 tabG = !isY ? tabX : cls == 0 ? 0xfedcb987543a6210ull : cls == 1 ? 0xefadc698254b7310ull : cls == 2 ? 0xfebad769325c8410ull : 0xefcba876432d9510ull;
    const GItem itQ{x + (size_t)pj * 256 * DA, wq + h * 256, DA, tabQ};
    const GItem itG{ckv + (size_t)mt * 256 * DC, (kv ? wuv : wuk) + h * 256, DC, tabG};
    const GItem none{nullptr, nullptr, 0, 0};
    if (doP) prep_load(pr, tid, j, rr, ak, av, ash);
    set_srdk(Qb, WSB);
    d_none();
    const unsigned lA = lbase + (isY ? 65536 : 0), lW = lA ^ 65536;
    g.mA = lA + wid * 1024; g.mB = lW + wid * 1024; g.kW = 0;
    g.vbB = (4 * wid + (lane >> 4)) * LDW * 2 + 16 * ((lane & 15) ^ (((lane >> 4) << 2) | wid));
    g_srcA(g, isY ? itQ : itG, wid, lane);
    g_srcB(g, isY ? itQ : itG);
    g_fix(g);
    const unsigned po = isY ? 0 : 32768;
    g_pro<0>(g, po, make_iseq<12>{});
    asm volatile("global_load_dwordx2 v[120:121], %0, off sc1" ::"v"(fw(flags, 6144)) : "memory");
    if (doP) {
      asm volatile("s_waitcnt vmcnt(13)" ::: "memory");
#pragma unroll
      for (int q = 0; q < 7; ++q) asm volatile("" : "+v"(pr.kk[q]));
#pragma unroll
      for (int q = 0; q < 4; ++q) asm volatile("" : "+v"(pr.vv[q]), "+v"(pr.w[q]));
      static_for([&](auto Ic) __attribute__((always_inline)) {
        constexpr int I = Ic;
        g_pro<12 + 4 * I>(g, po, make_iseq<4>{});
        GAP();
        prep_math<I>(pr, tid, lds + 131072);
        GAP();
      }, make_iseq<4>{});
      asm volatile("s_waitcnt lgkmcnt(0)\n\ts_barrier" ::: "memory");
      prep_store(tid, j, rr, xqT, lds + 131072);
    } else g_pro<12>(g, po, make_iseq<4>{});
    GAP();
    CODE_TOUCH("at_code_begin", "pf_code_end", r * 4 + wid, 8, m0d);
    {
      const int fr = lane & 15, fq = lane >> 4, q = (lane & 15) >> 2, p = lane & 3;
#pragma unroll
      for (int k2 = 0; k2 < 2; ++k2)
        g.aA[k2] = lA + (wid >> 1) * 16384 + (fr >> 3) * 1024 + (fr & 7) * 128 + (((4 * k2 + fq) ^ (fr >> 1)) * 16);
#pragma unroll
      for (int hf = 0; hf < 2; ++hf)
#pragma unroll
        for (int nf = 0; nf < 8; ++nf)
          g.aB[hf][nf] = lW + (wid & 1) * 8192 + off_b(8 * fq + 4 * hf + q, nf * 2 + (p >> 1)) + 8 * (p & 1);
    }
    auto start = [&](auto Pc) __attribute__((always_inline)) {
      if (doP) asm volatile("s_waitcnt vmcnt(13)" ::: "memory");
      else {
        asm volatile("s_nop 4");
        g_pro<16>(g, po, make_iseq<12>{});
        asm volatile("s_waitcnt vmcnt(8)" ::: "memory");
      }
      asm volatile("s_barrier" ::: "memory");
      g_read0<Pc>(g, make_iseq<16>{});
      GAP();
      u64 z = (((u64)__float_as_uint(vget<121>()) << 32) | __float_as_uint(vget<120>())) + 0x9e3779b97f4a7c15ull;
      z = (z ^ (z >> 30)) * 0xbf58476d1ce4e5b9ull; z = (z ^ (z >> 27)) * 0x94d049bb133111ebull; z ^= z >> 31;
      tag = ((u64)(unsigned)__builtin_amdgcn_readfirstlane((int)(z >> 32)) << 32) | (unsigned)__builtin_amdgcn_readfirstlane((int)z);
      asm volatile("s_waitcnt lgkmcnt(0)\n\ts_barrier\n\ts_nop 4" ::: "memory");
      g_pro<28>(g, po, make_iseq<4>{});
      g.kW = ((unsigned)g.tab >> 8) & 15;
      g.voA = g.vbA + (((unsigned)g.tab >> 4) & 15) * 128;
      g.voB = g.vbB + g.kW * 0x80000;
      g.tab >>= 12;
      GAP();
    };
    u64* pend = fw(gf, 64 + r);
    const u64 np0 = isY ? needA | needQ : needQ, np1 = doA ? 0x3fffc000ull : 0ull;
    if (isY) {
      g_item<false, true>(g, itG, pend, tag, oQ + pj * 131072, wid, lane, gf, 0, 0, 0, start, [&](int) {}, [&](GE&) {});
      pend = fw(gf, 32 + hsel * 8 + pj);
      KST(0, 3);
    }
    auto start2 = [&](auto Pc) __attribute__((always_inline)) { if (!isY) start(Pc); };
#define TT 3
#define TK 1
    auto mid = [&](int t) __attribute__((always_inline)) {
      if (t == 1) { if (wid < 2) post_dma<false>(j, r * 2 + wid, xqT, aq, m0f); }
      else {
        int l = threadIdx.x & 63; asm volatile("" : "+v"(l));
        if (t < 13) {
          const char* tp = (const char*)ckv + 1024 * DC * 2 + (((b & 15) * 4 + wid) * 256 + l) * 128;
          asm volatile("s_mov_b32 m0, %1\n\ts_nop 0\n\tglobal_load_lds_dword %0, off" ::"v"(tp), "s"(m0d) : "memory");
          asm volatile("global_load_lds_dword %0, off" ::"v"(tp + 8192) : "memory");
          asm volatile("global_load_lds_dword %0, off" ::"v"(tp + 16384) : "memory");
          asm volatile("global_load_lds_dword %0, off" ::"v"(tp + 24576) : "memory");
        } else if (doA && TK) {
          const char* tp = (const char*)assmk + (size_t)(j * 56 + cch) * (SEQ * 2) + (l & 31) * 128;
          asm volatile("s_mov_b32 m0, %1\n\ts_nop 0\n\tglobal_load_lds_dword %0, off" ::"v"(tp), "s"(m0d) : "memory");
        }
      }
    };
    const unsigned evx = isY || !solo || !TT ? 0u : 1u << TT;
    auto pre = [&](GE& e) __attribute__((always_inline)) {
      fl_check<0>(gf, np0, tag);
      fl_check<1>(gf, np1, tag);
      att_init();
      const int d = doA ? j * 56 + cch : 0;
      asm volatile("global_load_ushort %0, %1, off" : "=v"(dv) : "v"(assmD + d) : "memory");
      e.sk = __builtin_amdgcn_make_buffer_rsrc((void*)(assmk + (size_t)d * SEQ), (short)0, SEQ * 2, 0x00020000);
      e.sx = __builtin_amdgcn_make_buffer_rsrc((void*)(xqT + (size_t)d * SEQ), (short)0, SEQ * 2, 0x00020000);
      e.vl = c.ka - lbase; e.vl0 = e.vl - 32; e.vq = doA ? 0x80000000u : e.vl; e.vk[0] = c.dw[0]; e.vk[1] = c.dw[1];
      e.mC = doA ? lbase + 65536 + wid * 15488 : 0x30000;
      e.mK = isY ? lbase + wid * 8192 : 0x30000;
      const unsigned q0 = oQ + ((solo && !isY ? pj : 15 - pj) * 128 + wid * 32) * 512;
      sset<96>(q0); sset<97>(q0);
      sset<98>(oK + cx * 32768);
    };
    if (kv) g_item<true, false>(g, none, pend, tag, oK + 0x1000000 + mt * 131072, wid, lane, gf, np0, np1, evx, start2, mid, pre);
    else g_item<false, false>(g, none, pend, tag, oK + mt * 131072, wid, lane, gf, np0, np1, evx, start2, mid, pre);
  }
  KST(0, 4);
  u64* pend2 = gflag;
  asm volatile("s_waitcnt vmcnt(%1)" : "+v"(dv) : "i"(W_CONV) : "memory");
  if (doA) {
    const float Dd = bf2f(dv);
    KST(0, 5);
    conv_wave(j * 56 + cch, Dd, xqT + (size_t)DA * SEQ, lds + 65536 + wid * 15488,
              [&](int cr) __attribute__((always_inline)) {
      if (cr == 8) {
        const char* tp = (const char*)wo + (size_t)(j + (cch >= 28 ? 8 : 0)) * (HD * DA * 2) + ((cch % 28) * 64 + lane) * 128;
        asm volatile("s_mov_b32 m0, %1\n\ts_nop 0\n\tglobal_load_lds_dword %0, off" ::"v"(tp), "s"(m0d) : "memory");
      }
      if (cr == 1) {
        asm volatile("s_waitcnt vmcnt(0)\n\ts_barrier" ::: "memory");
        set_flag(gflag, tag, wid);
      } else if (cr == 2) fl_issue<0>(gf, 0x0101ull << (hsel * 16));
      else if (cr >= 4 && cr < 8) {
        if (cr == 4) {
          asm volatile("s_waitcnt vmcnt(16)" ::: "memory");
          fl_check<0>(gf, 0x0101ull << (hsel * 16), tag);
        }
        dma4k(c, oK + ((cr >> 1) & 1) * 32768, lbase + ((cr >> 1) & 1) * 32768 + wid * 8192, cr & 1);
      }
    });
    KST(0, 6);
    pend2 = fw(gf, 100 + rr);
  } else if (!isY) {
    static_for([&](auto I) { d_store<16 + I>(); }, make_iseq<16>{});
    asm volatile("s_waitcnt vmcnt(0)\n\ts_barrier" ::: "memory");
    set_flag(gflag, tag, wid);
    KST(0, 5);
    wait_flags(gf, needA, tag);
    pro_k(0);
    asm volatile("s_waitcnt vmcnt(0)" ::: "memory");
    KST(0, 6);
  }
  c.va = c.ka + 65536;
  c.m0x[0] = lbase + 65536 + wid * 8192; c.m0x[1] = c.m0x[0] + 32768;
  const unsigned oP = (unsigned)(5 * TSZ * 2) + (h * 8 + pj) * 66560;
  const int qbF = isY ? 15 - pj : pj;
  const unsigned oS = (unsigned)(3 * TSZ * 2) + qbF * 0x200000;
  const int vml = 65536 + wid * 256 + (lane & 15) * 16;
  const int nitems = isY ? 1 : 2, it0 = !isY && solo ? 1 : 0;
#pragma unroll 1
  for (int it = it0; it < nitems; ++it) {
    int qb, t0, n, jmp, c0, c1;
    u64 n0, n1;
    if (isY) { qb = 15 - pj; t0 = cx; n = NY; jmp = ex; c1 = 14 - cx; n1 = needY; c0 = solo ? 30 : NY - 2; n0 = 1ull << (48 + hsel * 8 + pj); }
    else if (it == 0) { qb = 15 - pj; t0 = 0; n = nx0; jmp = 16 - cx; c1 = ex ? cx - 2 : 30; n1 = needX; c0 = 2; n0 = needA; }
    else { qb = pj; t0 = 0; n = 2 * pj + 2; jmp = 0; c1 = 30; c0 = 30; n0 = n1 = 0; }
    const bool last = isY || it == 1;
    c.mrow = qb * 128 + wid * 32 + (lane & 15) - 4 * (lane >> 4);
    static_for([&](auto I) { vset<R_RS + I>(0.f); }, make_iseq<6>{});
    vset<R_THR>(4096.f);
    if (!isY) d_none();
    c.diag0 = qb == 0;
    sset<S_T>(oK + t0 * 32768); sset<S_K>(oK + t0 * 32768 + 32768);
    const int ct = !last ? 30 : n > 2 ? n - 3 : 0, cw = isY ? (solo ? 13 : c1) : last ? (n > 2 ? 0 : 30) : n - 2;
    sset<S_EV>((3u << c0) | (3u << c1) | (last ? 3u << n >> 2 : 1u << n >> 1) | (isY ? 4u : 0u));
    if (it != it0) asm volatile("s_waitcnt vmcnt(16)" ::: "memory");
    else {
      if (doA) asm volatile("s_waitcnt vmcnt(6)" ::: "memory");
      asm volatile("s_waitcnt vmcnt(%0) lgkmcnt(0)\n\ts_barrier" ::"i"(W_K0) : "memory");
    }
    KST(0, 8 + 3 * it);
    kreads<0>(c, make_iseq<8>{});
    GAP();
    auto event = [&](int i) __attribute__((always_inline)) {
      if (i == c0) fl_check<0>(gf, n0, tag);
      if (i == c1) { fl_check<0>(gf, n1, tag); asm volatile("s_add_u32 s98, s98, %0" ::"s"(jmp * 32768) : "scc"); }
      if (i + 1 == c0 || i + 1 == c1) fl_issue<0>(gf, (i + 1 == c0 ? n0 : 0) | (i + 1 == c1 ? n1 : 0));
      if (i == cw) fl_issue<1>(gf, needW);
      if (i + 2 == n) {
        sset<S_K>(oK);
        if (last) {
          int l = threadIdx.x & 63; asm volatile("" : "+v"(l));
          const unsigned vw = (64 * wid + (l >> 3)) * (DA * 2) + 16 * ((l & 7) ^ ((l >> 3) & 6));
          static_for([&](auto Q) { vset<r_dk(Q)>(__uint_as_float(vw + (Q ^ 4) * (8 * DA * 2) - (Q & 3) * 1024)); }, make_iseq<8>{});
          set_srdk(wo + (size_t)h * HD * DA, HD * DA * 2);
          sset<S_K>(0); sset<S_I>(128);
        }
        if (isY && !solo)
          asm volatile("s_nop 4\n\tbuffer_load_dwordx4 v[122:125], %0, %1, %2 offen sc1" ::"v"(vml), "s"(c.sw), "s"(oP) : "memory");
      }
      if (i == ct) {
        fl_check<1>(gf, needW, tag);
        if (wid < 2) post_dma<true>(j, r * 2 + wid, xqT + (size_t)DA * SEQ, aq, m0f);
      }
      if (last && i + 3 >= n && i + 1 < n && i) set_mask<R_CI, false>(c, (2 * qb + i + 3 - n) * 64);
      if (i == (isY ? 1 : 0)) set_flag(it != it0 ? fw(gf, 48 + hsel * 8 + pj) : pend2, tag, wid);
      asm volatile("s_nop 4");
      GAP();
    };
#define STA(k)
    STA(0);
    phaseA<0, true, false>(c);
    STA(1);
    event(0);
    STA(2);
    phaseB0(c);
    STA(3);
#pragma unroll 1
    for (int i = 1;; i += 2) {
      phaseA<1, false, false>(c);
      STA(9 + i);
      if (__builtin_expect(c.rare != 0, 0)) { if (c.redo) sm_redo<1>(); event(i); }
      phaseB<1>(c);
      if (i + 1 >= n) break;
      phaseA<0, false, false>(c);
      STA(10 + i);
      if (__builtin_expect(c.rare != 0, 0)) { if (c.redo) sm_redo<0>(); event(i + 1); }
      phaseB<0>(c);
    }
    STA(4);
    c.rare = __builtin_amdgcn_readfirstlane(!last ? oQ + (pj * 128 + wid * 32) * 512 : oP + wid * 16384);
    c.vq = last && (!isY || solo) ? 0x80000000u : c.ka;
    asm volatile("s_nop 4" : "+s"(c.rare));
    phaseA<0, false, true>(c);
    if (__builtin_expect(c.redo != 0, 0)) sm_redo<0>();
    STA(5);
    KST(0, 9 + 3 * it);
    const float l[2] = {row_reduce<false>(vget<R_RS>() + vget<R_TS>()), row_reduce<false>(vget<R_RS + 1>() + vget<R_TS + 1>())};
    const float cm[2] = {vget<R_M>(), vget<R_M + 1>()};
    EP p;
    p.vo = c.dw[0] + wid * 8192;
#pragma unroll
    for (int e = 0; e < 4; ++e) p.so[e] = oP + e * 4096;
    {
      int lz = threadIdx.x & 63; asm volatile("" : "+v"(lz));
      const int rw = lz >> 2;
      static_for([&](auto E) { vset<R_TB + E>(__uint_as_float(lbase + rw * 128 + 16 * ((2 * E + ((lz & 3) >> 1)) ^ (rw & 6)) + 8 * (lz & 1))); }, make_iseq<4>{});
      const unsigned vs = ((lz >> 3) & 1) * 16384 + ((lz >> 4) & 1) * 512 + (lz >> 5) * 256 + (lz & 7) * 16;
      vset<R_VS>(__uint_as_float(vs)); vset<R_VS + 1>(__uint_as_float(vs + 32768));
      sset<96>(oS + wid * 65536 + h * 1024); sset<97>(c.m0x[1]); sset<100>(0x8000);
    }
    if (!isY || solo) {
      p.fa[0] = __builtin_amdgcn_rcpf(l[0]); p.fa[1] = __builtin_amdgcn_rcpf(l[1]);
      if (!last) {
        __builtin_amdgcn_raw_buffer_store_b128(__builtin_bit_cast(u32x4, (f32x4){cm[0], l[0], cm[1], l[1]}), c.sw, vml, oP, 16);
        phaseF<0>(c, p, make_iseq<74>{});
      } else phaseF<1>(c, p, make_iseq<74>{});
    } else {
      const f32x4 ml = {vget<122>(), vget<123>(), vget<124>(), vget<125>()};
#pragma unroll
      for (int e = 0; e < 2; ++e) {
        const float mm = fmaxf(cm[e], ml[2 * e]);
        const float wa = __builtin_amdgcn_exp2f(cm[e] - mm), wb = ml[2 * e + 1] * __builtin_amdgcn_exp2f(ml[2 * e] - mm);
        const float inv = __builtin_amdgcn_rcpf(l[e] * wa + wb);
        p.fa[e] = wa * inv; p.fb[e] = wb * inv;
      }
      phaseF<2>(c, p, make_iseq<74>{});
    }
    KST(0, 10 + 3 * it);
  }
  {
    asm volatile("v_xor_b32 v%c1, s100, v%c1\n\ts_movk_i32 s99, 0x1000\np_loop:" : "+s"(c.sw) : "i"(R_TB) : "scc");
    GAP();
    p_half<0>(c, make_iseq<32>{});
    p_half<1>(c, make_iseq<32>{});
    asm volatile("s_cbranch_scc1 p_loop\n\ts_nop 5" ::"s"(c.sw));
    GAP();
    p_cvt<1, 0>(); p_cvt<1, 1>(); p_cvt<1, 2>(); p_cvt<1, 3>();
    p_store<1, 1>(c);
    GAP();
    int lz = threadIdx.x & 63; asm volatile("" : "+v"(lz));
    const int fr = lz & 15, fq = lz >> 4;
    KST(0, 27);
    {
      const int jj = 4 * (fr >> 3) + (fr & 3);
      const unsigned one = ((fq ^ (fr >> 2)) & 1) ? 0u : (jj & 1) ? 0x3f800000u : 0x3f80u;
      static_for([&](auto I) { vset<96 + I>(__uint_as_float((jj >> 1) == I ? one : 0u)); }, make_iseq<4>{});
    }
    asm volatile("s_waitcnt vmcnt(0)" ::: "memory");
    set_flag(fw(flags, 1024 + (qbF * 4 + wid) * 16 + h), tag);
    KST(0, 30);
    if (wid < 2) post_math(j, r * 2 + wid, out, lds + 131072 + wid * 8192);
    if (wid == 3) wait_flags(fw(flags, 1024 + (qbF * 4 + (h & 3)) * 16), 0xffffull, tag);
    asm volatile("s_barrier" ::: "memory");
    KST(0, 28);
    const unsigned vo = (fq >> 1) * 1024 + (fq & 1) * 256 + fr * 16;
    unsigned so = __builtin_amdgcn_readfirstlane(oS + (1 + wid) * 0x40000 + (h & 3) * 65536 + (h >> 2) * 16384);
    bf16* op = out + (size_t)(qbF * 128 + (h & 3) * 32 + (h >> 2) * 8 + (fr & 7)) * OUTW + DA + wid * 64 + (fr >> 3) * 32 + fq * 8;
    auto fin = [&](auto NLc) __attribute__((always_inline)) {
      constexpr int NL = NLc;
      asm volatile("s_nop 4" : "+s"(so), "+s"(c.sw));
      static_for([&](auto K) { r_load<K>(c, vo, so + (K >> 4) * 0x100000); }, make_iseq<NL>{});
      static_for([&](auto K) { r_sum<K, NL>(); }, make_iseq<NL>{});
      asm volatile("s_nop 7");
      static_for([&](auto U) {
        constexpr int d = 100 + 8 * U;
        *(u32x4*)(op + U * 256) = (u32x4){pk2(vget<d>(), vget<d + 1>()), pk2(vget<d + 2>(), vget<d + 3>()), pk2(vget<d + 4>(), vget<d + 5>()), pk2(vget<d + 6>(), vget<d + 7>())};
      }, make_iseq<NL / 16>{});
    };
    if (wid < 3) fin(std::integral_constant<int, 32>{});
    else fin(std::integral_constant<int, 16>{});
    KST(0, 29);
    set_flag(fw(flags, 6144), tag, (b ^ 128) | wid);
  }
  asm volatile("s_waitcnt vmcnt(0)" ::: "memory");
  asm volatile("pf_code_end:");
  KST(0, 1);
}

extern "C" void kernel_launch(void* const* d_in, const int* in_sizes, int n_in, void* d_out, int out_size, void* d_ws,
                              size_t ws_size, hipStream_t stream) {
  const bf16* Aq = (const bf16*)d_in[0];
  const bf16* Ak = (const bf16*)d_in[1];
  const bf16* Av = (const bf16*)d_in[2];
  const bf16* Ash = (const bf16*)d_in[3];
  const bf16* Assmk = (const bf16*)d_in[4];
  const bf16* AssmD = (const bf16*)d_in[5];
  const bf16* Bx = (const bf16*)d_in[6];
  const bf16* Bckv = (const bf16*)d_in[7];
  const bf16* Bwq = (const bf16*)d_in[8];
  const bf16* Bwuk = (const bf16*)d_in[9];
  const bf16* Bwuv = (const bf16*)d_in[10];
  const bf16* Bwo = (const bf16*)d_in[11];
  bf16* out = (bf16*)d_out;
  hipFuncSetAttribute((const void*)fused_kernel, hipFuncAttributeMaxDynamicSharedMemorySize, 163840);
  fused_kernel<<<256, 256, 163840, stream>>>(Bckv, Bwuk, Bwuv, Bx, Bwq, (bf16*)d_ws, Ak, Av, Ash, Assmk, AssmD, Bwo, Aq, out);
}
```

```cpp
#include <hip/hip_runtime.h>
#include <hip/hip_bf16.h>
#include <stdint.h>
#include <utility>

typedef __hip_bfloat16 bf16;
typedef __attribute__((ext_vector_type(4))) short bf16x4;
typedef __attribute__((ext_vector_type(2))) unsigned u32x2;

constexpr int SEQ = 2048;
constexpr int DA = 448;
constexpr int NH = 16;
constexpr int HD = 256;
constexpr int DC = 1024;
constexpr int OUTW = 896;
#define HROW(h) ((size_t)(h) * SEQ)
constexpr int HSTEP = SEQ;

#define KST(kid, which)

__device__ __forceinline__ float bf2f(unsigned short u) { return __uint_as_float(((unsigned)u) << 16); }
__device__ __forceinline__ unsigned short f2bf(float f) { return __bfloat16_as_ushort(__float2bfloat16(f)); }
__device__ __forceinline__ float rbf(float f) { return bf2f(f2bf(f)); }
typedef __attribute__((ext_vector_type(2))) __bf16 bf16x2n;
typedef __attribute__((ext_vector_type(2))) float f32x2n;
__device__ __forceinline__ unsigned pk2(float a, float b) {
  return __builtin_bit_cast(unsigned, __builtin_convertvector((f32x2n){a, b}, bf16x2n));
}

__device__ __forceinline__ int off_b(int row, int ch) { return 256 * row + 16 * (ch ^ (((row & 3) << 2) | ((row >> 2) & 3))); }

template <int R> __device__ __forceinline__ void sset(unsigned x) { asm volatile("s_mov_b32 s%c0, %1" ::"i"(R), "s"(x)); }
template <int R> __device__ __forceinline__ float vget() { float t; asm volatile("v_mov_b32 %0, v%c1" : "=v"(t) : "i"(R)); return t; }
template <int R> __device__ __forceinline__ void vset(float x) { asm volatile("v_mov_b32 v%c0, %1" ::"i"(R), "v"(x)); }
template <int R> __device__ __forceinline__ float acc_read1() { float t; asm volatile("v_accvgpr_read_b32 %0, a[%c1]" : "=v"(t) : "i"(R)); return t; }
template <class F, int... I> __device__ __forceinline__ void static_for(F&& f, std::integer_sequence<int, I...>) {
  (f(std::integral_constant<int, I>{}), ...);
}
template <int... Is> using iseq = std::integer_sequence<int, Is...>;
template <int N> using make_iseq = std::make_integer_sequence<int, N>;

typedef __attribute__((ext_vector_type(4))) unsigned u32x4c;
typedef __attribute__((ext_vector_type(4))) float f32x4c;
typedef __attribute__((ext_vector_type(2))) float f32x2c;
typedef unsigned long long u64;
#define DMA16(M0V, VO, SRD, SO) asm volatile("s_mov_b32 m0, %0\n\ts_nop 0\n\tbuffer_load_dwordx4 %1, %2, %3 offen lds" ::"s"(M0V), "v"(VO), "s"(SRD), "s"(SO) : "memory")
struct PR { u32x4c kk[7], vv[4], w[4]; };
__device__ __forceinline__ void prep_load(PR& p, int tid, int j, int rr, const bf16* __restrict__ k, const bf16* __restrict__ v,
                                          const bf16* __restrict__ shift_k) {
  const __amdgpu_buffer_rsrc_t sk = __builtin_amdgcn_make_buffer_rsrc((void*)k, (short)0, SEQ * DA * 2, 0x00020000);
  const __amdgpu_buffer_rsrc_t sv = __builtin_amdgcn_make_buffer_rsrc((void*)v, (short)0, SEQ * DA * 2, 0x00020000);
  const __amdgpu_buffer_rsrc_t sw = __builtin_amdgcn_make_buffer_rsrc((void*)(shift_k + j * 224), (short)0, 448, 0x00020000);
  const int vo = ((rr * 128 + (tid >> 3) * 4) * DA + j * 56 + (tid & 7) * 8) * 2;
#pragma unroll
  for (int q = 0; q < 7; ++q)
    asm volatile("buffer_load_dwordx4 %0, %1, %2, 0 offen" : "=v"(p.kk[q]) : "v"(vo + (q - 3) * DA * 2), "s"(sk) : "memory");
#pragma unroll
  for (int q = 0; q < 4; ++q)
    asm volatile("buffer_load_dwordx4 %0, %1, %2, 0 offen offset:%3" : "=v"(p.vv[q]) : "v"(vo), "s"(sv), "i"(q * DA * 2) : "memory");
#pragma unroll
  for (int q = 0; q < 4; ++q)
    asm volatile("buffer_load_dwordx4 %0, %1, %2, 0 offen offset:%3" : "=v"(p.w[q]) : "v"((tid & 7) * 64), "s"(sw), "i"(q * 16) : "memory");
}
__device__ __forceinline__ float bfl(unsigned u) { return __uint_as_float(u << 16); }
__device__ __forceinline__ float bfh(unsigned u) { return __uint_as_float(u & 0xffff0000u); }
__device__ __forceinline__ f32x2c rbf2(f32x2c a) { const unsigned u = pk2(a[0], a[1]); return (f32x2c){bfl(u), bfh(u)}; }
template <int I> __device__ __forceinline__ void prep_math(const PR& p, int tid, char* lx) {
  const int rp = tid >> 3, c = tid & 7;
  f32x2c wt[4], kf[7];
#pragma unroll
  for (int t = 0; t < 4; ++t)
    wt[t] = (t & 1) ? (f32x2c){bfh(p.w[I][t >> 1]), bfh(p.w[I][2 + (t >> 1)])} : (f32x2c){bfl(p.w[I][t >> 1]), bfl(p.w[I][2 + (t >> 1)])};
#pragma unroll
  for (int q = 0; q < 7; ++q) kf[q] = (f32x2c){bfl(p.kk[q][I]), bfh(p.kk[q][I])};
  f32x2c x[4];
#pragma unroll
  for (int e = 0; e < 4; ++e) {
    f32x2c s = rbf2(kf[e] * wt[3]);
    s = rbf2(s + rbf2(kf[e + 1] * wt[2]));
    s = rbf2(s + rbf2(kf[e + 2] * wt[1]));
    s = rbf2(s + rbf2(kf[e + 3] * wt[0]));
    x[e] = s * (f32x2c){bfl(p.vv[e][I]), bfh(p.vv[e][I])};
  }
#pragma unroll
  for (int u = 0; u < 2; ++u)
    *(u32x2*)(lx + (c * 8 + 2 * I + u) * 256 + (((31 - rp) * 8) ^ (c << 4))) = (u32x2){pk2(x[3][u], x[2][u]), pk2(x[1][u], x[0][u])};
}
__device__ __forceinline__ void prep_store(int tid, int j, int rr, bf16* __restrict__ xr, const char* lx) {
  const __amdgpu_buffer_rsrc_t sx = __builtin_amdgcn_make_buffer_rsrc((void*)(xr + (size_t)j * 56 * SEQ), (short)0, 56 * SEQ * 2, 0x00020000);
#pragma unroll
  for (int rd = 0; rd < 4; ++rd) {
    const int idx = tid + 256 * rd, ch = idx >> 4, q = idx & 15;
    u32x4c w = *(const u32x4c*)(lx + ch * 256 + 16 * (q ^ (ch >> 3)));
    __builtin_amdgcn_raw_buffer_store_b128(w, sx, (ch * SEQ + (15 - rr) * 128 + q * 8) * 2, 0, 16);
  }
}

#define CODE_TOUCH(LB, LE, wx, NW, NL, m0v) do { unsigned blo_, bhi_, elo_; \
    asm volatile("s_getpc_b64 s[40:41]\n\ts_add_u32 s40, s40, " LB "@rel32@lo+4\n\ts_addc_u32 s41, s41, " LB "@rel32@hi+12\n\t" \
                 "s_mov_b32 %0, s40\n\ts_mov_b32 %1, s41\n\t" \
                 "s_getpc_b64 s[40:41]\n\ts_add_u32 s40, s40, " LE "@rel32@lo+4\n\ts_mov_b32 %2, s40" \
                 : "=s"(blo_), "=s"(bhi_), "=s"(elo_) :: "s40", "s41", "scc"); \
    const unsigned n_ = elo_ - (blo_ & ~127u); \
    const __amdgpu_buffer_rsrc_t srd_ = __builtin_amdgcn_make_buffer_rsrc((void*)(((unsigned long long)bhi_ << 32) | (blo_ & ~127u)), (short)0, n_ < 262144u ? n_ : 0u, 0x00020000); \
    const unsigned l_ = threadIdx.x & 63, vo_ = l_ < (NL) ? ((wx) + l_ * (NW)) * 128u : 0x80000000u; \
    asm volatile("s_mov_b32 m0, %0\n\ts_nop 4\n\tbuffer_load_dword %1, %2, 0 offen lds" ::"s"(m0v), "v"(vo_), "s"(srd_) : "memory"); } while (0)
template <int I> __device__ __forceinline__ void d_store() {
  asm volatile("buffer_store_dwordx4 v[%c0:%c1], v%c2, s[92:95], s101 offen offset:%3 sc1" ::"i"(128 + 4 * I), "i"(131 + 4 * I), "i"(112 + ((I >> 2) & 1)),
               "i"((I & 3) * 1024) : "memory");
  if constexpr ((I & 7) == 7) asm volatile("s_addk_i32 s101, 0x4000" ::: "scc");
}
__device__ __forceinline__ void d_none() { asm volatile("v_bfrev_b32 v112, 1\n\tv_bfrev_b32 v113, 1\n\ts_mov_b32 s101, 0"); }
struct CV { unsigned pa[4], pb, qv; };
constexpr int cv_J(int q) { int J = 7; while (q > J) { q -= J + 1; --J; } return J; }
constexpr int cv_i(int q) { int J = 7; while (q > J) { q -= J + 1; --J; } return q; }
template <int R, int N> __device__ __forceinline__ void cv_rd(CV& c) {
  if constexpr (N < 16) {
    constexpr int i = N >> 1, o = 16 * R + 128 * (i & 1) + 2 * (N & 1), r = 32 + 32 * (R & 1) + 2 * N;
    asm volatile("ds_read2_b32 a[%c1:%c2], %0 offset0:%c3 offset1:%c4" ::"v"(c.pa[i >> 1]), "i"(r), "i"(r + 1), "i"(o), "i"(o + 1));
  } else {
    constexpr int m = N - 16, r = 192 + 32 * (R & 1) + 4 * m;
    asm volatile("ds_read_b128 a[%c1:%c2], %0 offset:%c3" ::"v"(c.pb), "i"(r), "i"(r + 3), "i"(64 * R + 512 * (7 - m)));
  }
}
template <int R, int Q, int MODE> __device__ __forceinline__ void cv_slot(CV& c) {
  constexpr int i = cv_i(Q), J = cv_J(Q), d = 4 * J, a = 32 + 32 * (R & 1) + 4 * i, b = 192 + 32 * (R & 1) + 4 * (J - i);
  if constexpr (Q == 0) asm volatile("s_waitcnt lgkmcnt(0)");
  if constexpr (MODE == 1 && R == 0 && i == 0)
    asm volatile("v_mfma_f32_16x16x32_bf16 a[%c0:%c1], a[%c2:%c3], a[%c4:%c5], 0" ::"i"(d), "i"(d + 3), "i"(a), "i"(a + 3), "i"(b), "i"(b + 3));
  else
    asm volatile("v_mfma_f32_16x16x32_bf16 a[%c0:%c1], a[%c2:%c3], a[%c4:%c5], a[%c0:%c1]" ::"i"(d), "i"(d + 3), "i"(a), "i"(a + 3), "i"(b), "i"(b + 3));
  if constexpr (Q < 24 && !(MODE == 2 && R == 1)) cv_rd<R + 1, Q>(c);
  if constexpr (MODE == 1 && Q % 7 == 3) d_store<22 + 5 * R + Q / 7>();
  if constexpr (MODE == 2 && (R == 0 ? Q >= 24 && Q % 3 == 0 : Q % 3 == 0)) {
    constexpr int S_ = R == 0 ? (Q - 24) / 3 : 4 + Q / 3;
    asm volatile("buffer_load_dwordx4 a[%c0:%c1], %2, s[92:95], s97 offen offset:%3 sc1" ::"i"(128 + 4 * S_), "i"(128 + 4 * S_ + 3), "v"(c.qv),
                 "i"((S_ & 3) * 1024) : "memory");
    if constexpr ((S_ & 3) == 3) asm volatile("s_addk_i32 s97, 4096" ::: "scc");
  }
  __builtin_amdgcn_sched_barrier(0);
}
template <int R, int MODE, int... Q> __device__ __forceinline__ void cv_step(CV& c, iseq<Q...>) { (cv_slot<R, Q, MODE>(c), ...); }
template <int... N> __device__ __forceinline__ void cv_rd0(CV& c, iseq<N...>) { (cv_rd<0, N>(c), ...); }
template <class HOOK>
__device__ __forceinline__ void conv_wave(int d, float Dd, bf16* __restrict__ zT, char* lw, HOOK&& hook) {
  int lane = threadIdx.x & 63; asm volatile("" : "+v"(lane));
  const int l16 = lane & 15, fq = lane >> 4;
  u32x4c w[5]; unsigned w4[5];
#pragma unroll
  for (int r = 0; r < 5; ++r) {
    w[r] = *(const u32x4c*)(lw + r * 1024 + lane * 16);
    w4[r] = *(const unsigned*)(lw + r * 1024 + lane * 16 + 16);
  }
  d_store<16>(); d_store<17>();
#pragma unroll
  for (int r = 0; r < 5; ++r)
    *(u32x4c*)(lw + 10304 + r * 1024 + lane * 16) = (u32x4c){__builtin_amdgcn_alignbit(w[r][1], w[r][0], 16), __builtin_amdgcn_alignbit(w[r][2], w[r][1], 16),
                                                             __builtin_amdgcn_alignbit(w[r][3], w[r][2], 16), __builtin_amdgcn_alignbit(w4[r], w[r][3], 16)};
  d_store<18>(); d_store<19>();
  CV c;
#pragma unroll
  for (int i = 0; i < 4; ++i) c.pa[i] = (unsigned)(size_t)lw + ((l16 + 1) & 1) * 10304 + 4 * ((8 * fq + l16 + 1) >> 1) + 1024 * i;
  c.pb = (unsigned)(size_t)lw + 5120 + (2032 - 16 * l16 + 8 * fq) * 2 - 512 * 7;
  asm volatile("" ::: "memory");
  cv_rd0(c, make_iseq<24>{});
  d_store<20>(); d_store<21>();
  __builtin_amdgcn_sched_barrier(0);
  cv_step<0, 1>(c, make_iseq<36>{});
  cv_step<1, 1>(c, make_iseq<36>{});
#pragma unroll 1
  for (int r = 1; r < 3; ++r) {
#pragma unroll
    for (int i = 0; i < 4; ++i) c.pa[i] += 128;
    c.pb += 128;
    __builtin_amdgcn_sched_barrier(0);
    cv_step<0, 0>(c, make_iseq<36>{});
    cv_step<1, 0>(c, make_iseq<36>{});
    hook(r);
  }
#pragma unroll
  for (int i = 0; i < 4; ++i) c.pa[i] += 128;
  c.pb += 128;
  c.qv = lane * 16;
  __builtin_amdgcn_sched_barrier(0);
  cv_step<0, 2>(c, make_iseq<36>{});
  cv_step<1, 2>(c, make_iseq<36>{});
  const __amdgpu_buffer_rsrc_t srdZ = __builtin_amdgcn_make_buffer_rsrc((void*)(zT + (size_t)d * SEQ), (short)0, SEQ * 2, 0x00020000);
  asm volatile("s_nop 7");
  static_for([&](auto Jc) __attribute__((always_inline)) {
    constexpr int J = Jc;
    hook(4 + J);
    __builtin_amdgcn_sched_barrier(0);
    const int t = 256 * J + 16 * l16 + 4 * fq;
    bf16x4 xv = *(const bf16x4*)(lw + 5120 + (2044 - t) * 2);
    const float y[4] = {acc_read1<4 * J>(), acc_read1<4 * J + 1>(), acc_read1<4 * J + 2>(), acc_read1<4 * J + 3>()};
    float z[4];
#pragma unroll
    for (int e = 0; e < 4; ++e) z[e] = rbf(y[e]) + rbf(bf2f(xv[3 - e]) * Dd);
    u32x2 w = {pk2(z[0], z[1]), pk2(z[2], z[3])};
    __builtin_amdgcn_raw_buffer_store_b64(w, srdZ, t * 2, 0, 16);
  }, make_iseq<8>{});
}

template <bool Z> __device__ __forceinline__ void post_dma(int j, int tb, const bf16* __restrict__ zT, const bf16* __restrict__ q, unsigned m0w) {
  int lane = threadIdx.x & 63; asm volatile("" : "+v"(lane));
  const __amdgpu_buffer_rsrc_t sz = __builtin_amdgcn_make_buffer_rsrc((void*)(zT + (size_t)j * 56 * SEQ + tb * 32), (short)0, 56 * SEQ * 2, 0x00020000);
  const __amdgpu_buffer_rsrc_t sq = __builtin_amdgcn_make_buffer_rsrc((void*)(q + (size_t)tb * 32 * DA + j * 56), (short)0, 32 * DA * 2, 0x00020000);
  const int tl = lane / 7, ck = lane - tl * 7;
  const unsigned vz = lane < 56 ? (ck * 8 + tl) * SEQ * 2 : 0x80000000u, vq = lane < 56 ? (tl * DA + ck * 8) * 2 : 0x80000000u;
#pragma unroll
  for (int i = 0; i < 4; ++i) if constexpr (Z)
    asm volatile("s_mov_b32 m0, %0\n\ts_nop 4\n\tbuffer_load_dwordx4 %1, %2, %3 offen sc1 lds" ::"s"(m0w + i * 1024), "v"(vz), "s"(sz), "s"(i * 16) : "memory");
#pragma unroll
  for (int i = 0; i < 4; ++i) if constexpr (!Z)
    asm volatile("s_mov_b32 m0, %0\n\ts_nop 4\n\tbuffer_load_dwordx4 %1, %2, %3 offen lds" ::"s"(m0w + 4096 + i * 1024), "v"(vq), "s"(sq), "s"(i * 8 * DA * 2) : "memory");
}
__device__ __forceinline__ void post_math(int j, int tb, bf16* __restrict__ out, const char* lw) {
  int lane = threadIdx.x & 63; asm volatile("" : "+v"(lane));
  const int tl = lane / 7, ck = lane - tl * 7;
  const __amdgpu_buffer_rsrc_t so = __builtin_amdgcn_make_buffer_rsrc((void*)(out + (size_t)tb * 32 * OUTW + j * 56), (short)0, 32 * OUTW * 2 - 2 * DA, 0x00020000);
  const unsigned vo = lane < 56 ? (tl * OUTW + ck * 8) * 2 : 0x80000000u;
  const char* lz = lw + ck * 16 + (tl & 7) * 2;
#pragma unroll
  for (int i = 0; i < 4; ++i) {
    const u32x4c qv = *(const u32x4c*)(lw + 4096 + i * 1024 + lane * 16);
    u32x4c o;
#pragma unroll
    for (int e = 0; e < 4; ++e) {
      const float z0 = bf2f(*(const unsigned short*)(lz + i * 1024 + 2 * e * 112));
      const float z1 = bf2f(*(const unsigned short*)(lz + i * 1024 + (2 * e + 1) * 112));
      o[e] = pk2(bfl(qv[e]) * z0, bfh(qv[e]) * z1);
    }
    __builtin_amdgcn_raw_buffer_store_b128(o, so, vo, i * 8 * OUTW * 2, 0);
  }
}

#define DI __device__ __forceinline__
#define GAP() __builtin_amdgcn_sched_barrier(0)
typedef __attribute__((ext_vector_type(4))) float f32x4g;
typedef __attribute__((ext_vector_type(4))) unsigned u32x4g;
constexpr int LDW = NH * HD;
struct GM {
  unsigned aA[2], aB[2][8];
  unsigned voA, voB, vbA, vbB, kA, kW, kB;
  u64 tab;
  __amdgpu_buffer_rsrc_t srdA, srdB;
  unsigned sA[8], mA, mB;
};
template <int P, int KS, int I> DI void g_wread(GM& g) {
  constexpr int nf = I >> 1, hf = I & 1, r = 192 - 64 * P + 8 * nf + 4 * KS + 2 * hf;
  asm volatile("ds_read_b64_tr_b16 a[%c1:%c2], %0 offset:%c3" ::"v"(g.aB[hf][nf]), "i"(r), "i"(r + 1), "i"(P * 32768 + KS * 16384));
}
template <int P, int M, int KS> DI void g_aread(GM& g) {
  constexpr int r = 96 + 8 * (M & 1) + 4 * KS;
  asm volatile("ds_read_b128 v[%c1:%c2], %0 offset:%c3" ::"v"(g.aA[KS]), "i"(r), "i"(r + 3), "i"(P * 32768 + M * 2048));
}
constexpr float CS = 0.0625f * 1.4426950408889634f;
struct GE {
  __amdgpu_buffer_rsrc_t sk, sx;
  unsigned vl, vl0, vq, vk[2], mC, mK;
};
#define SG0 36
#define SGS 6
struct ES { int op0[129], nop[129]; };
constexpr ES e_sched(bool TR, bool SCL) {
  ES z{};
  const int per = SCL ? 16 : 8;
  int o = 0;
  for (int G = 0; G <= 128; ++G) {
    int cap = G == 128 ? 10000 : SCL ? 4 : 2;
    z.op0[G] = o;
    while (o < 32 * per) {
      const int i = o / per, rdy = (TR ? 32 * (i >> 3) + 16 + 2 * (i & 7) + 1 : 4 * i + 3) + 4;
      if ((G < rdy && G < 128) || cap-- <= 0) break;
      ++o; ++z.nop[G];
    }
  }
  return z;
}
template <bool TR, bool SCL> constexpr ES ESCH = e_sched(TR, SCL);
constexpr int e_st(int MODE, int G) {
  const int g0 = MODE == 4 ? 5 : MODE == 2 ? 13 : SG0, gs = MODE == 4 ? 6 : MODE == 2 ? 7 : SGS;
  return MODE >= 2 && G >= g0 && (G - g0) % gs == 0 && (G - g0) / gs < 16 ? (G - g0) / gs + (MODE == 4 ? 16 : 0) : -1;
}
constexpr int e_nst(int MODE, int G0, int G1) { int n = 0; for (int G = G0; G < G1; ++G) n += e_st(MODE, G) >= 0; return n; }
static_assert(e_nst(2, 0, 128) == 16 && e_nst(3, 0, 128) == 16 && e_nst(4, 0, 104) == 16);
template <bool TR, bool SCL, int O> DI void e_op() {
  constexpr int per = SCL ? 16 : 8, i = O / per, k = O % per, r = 128 + 4 * i, t = 114 + 4 * (i & 1), c = k - (per - 4);
  if constexpr (k < 4) asm volatile("v_accvgpr_read_b32 v%c0, a[%c1]" ::"i"(t + k), "i"(4 * i + k));
  else if constexpr (c < 0) asm volatile("v_mul_f32 v%c0, %1, v%c0" ::"i"(k < 8 ? r + (k ^ 6) : t + k - 8), "s"(CS));
  else {
    constexpr int d = c == 0 ? r + 3 : c == 1 ? r + 2 : r + c - 2, x = c == 0 ? r + 2 : c == 1 ? r : t + 2 * (c - 2);
    asm volatile("v_cvt_pk_bf16_f32 v%c0, v%c1, v%c2" ::"i"(d), "i"(x), "i"(x + 1));
  }
}
template <bool TR, bool SCL, int B, int... Is> DI void e_ops(iseq<Is...>) { (e_op<TR, SCL, B + Is>(), ...); }
#define LGS 2
constexpr int e_slot(int G) { return G < 10 ? G : (G - 10) % LGS == 1 && (G - 10) / LGS < 32 ? 10 + (G - 10) / LGS : -1; }
constexpr int E_CONV = 9, E_K0 = 11 + 23 * LGS;
static_assert(e_slot(E_K0) == 33);
template <int S> DI void e_m0(const GE& e) {
  if constexpr (S == 0 || S == 1 || S == 5 || S == 9) asm volatile("s_add_u32 m0, %0, %1" ::"s"(e.mC), "i"(S * 1024));
  if constexpr (S >= 26 && (S - 26) % 4 == 0) asm volatile("s_add_u32 m0, %0, %1" ::"s"(e.mK), "i"(((S - 26) >> 3) * 32768 + ((S - 26) & 4) * 1024));
}
template <int S> DI void e_load(const GE& e) {
  if constexpr (S == 0) asm volatile("buffer_load_dwordx4 %0, %1, 0 offen lds" ::"v"(e.vl0), "s"(e.sk) : "memory");
  else if constexpr (S < 5) asm volatile("buffer_load_dwordx4 %0, %1, %2 offen offset:%3 lds" ::"v"(e.vl), "s"(e.sk), "s"(1024 - 32), "i"((S - 1) * 1024) : "memory");
  else if constexpr (S < 9) asm volatile("buffer_load_dwordx4 %0, %1, 0 offen offset:%2 sc1 lds" ::"v"(e.vl), "s"(e.sx), "i"((S - 5) * 1024) : "memory");
  else if constexpr (S == 9) asm volatile("buffer_load_dwordx4 %0, %1, %2 offen sc1 lds" ::"v"(e.vl), "s"(e.sx), "s"(4096) : "memory");
  else if constexpr (S < 26) {
    constexpr int f = S - 10;
    asm volatile("buffer_load_dwordx4 a[%c0:%c1], %2, s[92:95], s96 offen offset:%3 sc1" ::"i"(128 + 4 * f), "i"(131 + 4 * f), "v"(e.vq),
                 "i"((f & 3) * 1024) : "memory");
    if constexpr ((f & 3) == 3) asm volatile("s_addk_i32 s96, 4096");
  } else {
    constexpr int f = S - 26;
    asm volatile("buffer_load_dwordx4 %0, s[92:95], s98 offen offset:%1 sc1 lds" ::"v"(e.vk[(f >> 2) & 1]), "i"((f & 3) * 1024) : "memory");
    if constexpr (f == 7) asm volatile("s_add_u32 s98, s98, 0x8000");
  }
}
constexpr int e_younger(int G0) {
  int n = e_nst(3, G0 + 1, 128);
  for (int G = G0 + 1; G < 128; ++G) n += e_slot(G) >= 0;
  return n;
}
constexpr int W_CONV = e_younger(E_CONV), W_K0 = e_younger(E_K0);
template <bool TR, int P, int H, int G, int MODE> DI void g_gap(GM& g, const GE& e) {
  constexpr int m = 4 * H + (G >> 4), q = G & 15, n = q >> 1, ks = q & 1, GG = 64 * H + G;
  constexpr bool inV = TR ? m & 1 : n & 1;
  constexpr int a = 96 + 8 * (m & 1) + 4 * ks, b = 192 - 64 * P + 8 * n + 4 * ks, d = (inV ? 128 : 0) + 4 * (TR ? 8 * (m >> 1) + n : 4 * m + (n >> 1));
  constexpr int J = MODE == 3 ? -1 : q == 8 ? 2 * (G >> 4) : q == 12 ? 2 * (G >> 4) + 1 : -1, gj = (J & 3) ^ ((J & 3) >> 1);
  if constexpr (MODE == 3) { if constexpr (q == 0 && GG > 0) asm volatile("s_waitcnt lgkmcnt(0)"); e_m0<e_slot(GG)>(e); }
  else if constexpr (q == 0 && G > 0) asm volatile("s_waitcnt lgkmcnt(4)");
  if constexpr (J == 0) asm volatile("s_add_u32 m0, %0, %1" ::"s"(g.mA), "i"((P ^ H ^ 1) * 32768 + (1 - H) * 8192) : "scc");
  else if constexpr (J == 4) asm volatile("s_add_u32 m0, %0, %1" ::"s"(g.mB), "i"((P ^ H) * 32768 + (1 - H) * 16384) : "scc");
  else if constexpr (J > 0 && (J & 3) == 1) asm volatile("s_bitset1_b32 m0, 12");
  else if constexpr (J > 0 && (J & 3) == 2) asm volatile("s_bitset1_b32 m0, %0" ::"i"(J < 4 ? 14 : 13));
  else if constexpr (J > 0) asm volatile("s_bitset0_b32 m0, 12");
#define GMF(D, X, Y, C) asm volatile("v_mfma_f32_16x16x32_bf16 " D "[%c0:%c1], " X ", " Y ", " C ::"i"(d), "i"(d + 3), "i"(a), "i"(a + 3), "i"(b), "i"(b + 3))
#define GMF2(D, C) do { if constexpr (TR) GMF(D, "v[%c2:%c3]", "a[%c4:%c5]", C); else GMF(D, "a[%c4:%c5]", "v[%c2:%c3]", C); } while (0)
  if constexpr ((MODE == 1 || MODE == 4) && ks == 0) { if constexpr (inV) GMF2("v", "0"); else GMF2("a", "0"); }
  else if constexpr (inV) GMF2("v", "v[%c0:%c1]");
  else GMF2("a", "a[%c0:%c1]");
#undef GMF2
#undef GMF
  if constexpr (e_st(MODE, GG) >= 0) {
    static_assert(MODE == 4 || ESCH<TR, MODE == 2>.op0[GG] >= (e_st(MODE, GG) + 1) * (MODE == 2 ? 16 : 8));
    d_store<e_st(MODE, GG)>();
  }
  if constexpr (MODE == 2 || MODE == 3) e_ops<TR, MODE == 2, ESCH<TR, MODE == 2>.op0[GG]>(make_iseq<ESCH<TR, MODE == 2>.nop[GG]>{});
#define GD(VO, SRD, SO) asm volatile("buffer_load_dwordx4 %0, %1, %2 offen lds" ::"v"(VO), "s"(SRD), "s"(SO) : "memory")
  if constexpr (J >= 0 && J < 4) GD(g.voA, g.srdA, g.sA[(1 - H) * 2 + (gj & 1) + (gj >> 1) * 4]);
  else if constexpr (J >= 4) GD(g.voB, g.srdB, ((1 - H) * 32 + 16 * (gj & 1)) * LDW * 2 + (gj >> 1) * 256);
#undef GD
  if constexpr ((q == 1 || q == 2) && !(MODE == 3 && m == 7)) g_aread<(m < 7 ? P : P ^ 1), (m + 1) & 7, q - 1>(g);
  if constexpr (MODE == 3) {
    if constexpr (e_slot(GG) >= 0) e_load<e_slot(GG)>(e);
  } else {
    if constexpr (q >= 3 && q < 11 && (q & 1)) g_wread<P ^ 1, H, (G >> 4) * 4 + (q - 3) / 2>(g);
    if constexpr (H == 1 && G == 0) asm volatile("s_mov_b32 %0, %1" : "=s"(g.kA) : "s"(g.kW));
    if constexpr (H == 1 && G == 4) asm volatile("v_lshl_add_u32 %0, %2, 7, %3\n\ts_bfe_u32 %1, %4, 0x40000" : "=v"(g.voA), "=s"(g.kW) : "s"(g.kA), "v"(g.vbA), "s"((unsigned)g.tab) : "scc");
    if constexpr (H == 1 && G == 6) asm volatile("s_lshr_b64 %0, %0, 4\n\ts_lshl_b32 %1, %2, 19" : "+s"(g.tab), "=&s"(g.kB) : "s"(g.kW) : "scc");
    if constexpr (H == 1 && G == 10) asm volatile("v_add_u32 %0, %1, %2" : "=v"(g.voB) : "s"(g.kB), "v"(g.vbB));
    constexpr int nst = e_nst(MODE, H ? 29 : 0, GG + 1);
    if constexpr (G == 61) asm volatile("s_waitcnt vmcnt(%0) lgkmcnt(0)\n\ts_barrier" ::"i"(12 + nst) : "memory");
  }
  if constexpr ((MODE == 2 || MODE == 3) && GG == 127) {
    asm volatile("s_nop 7");
    e_ops<TR, MODE == 2, ESCH<TR, MODE == 2>.op0[128]>(make_iseq<ESCH<TR, MODE == 2>.nop[128]>{});
  }
  GAP();
}
template <bool TR, int P, int H, int MODE, int... Gs> DI void g_half_(GM& g, const GE& e, iseq<Gs...>) { (g_gap<TR, P, H, Gs, MODE>(g, e), ...); }
template <bool TR, int P, int H, int MODE> DI void g_half(GM& g, const GE& e) { g_half_<TR, P, H, MODE>(g, e, make_iseq<64>{}); }
template <int P, int... Is> DI void g_read0(GM& g, iseq<Is...>) {
  (g_wread<P, 0, Is>(g), ...); (g_wread<P, 1, Is>(g), ...);
  g_aread<P, 0, 0>(g); g_aread<P, 0, 1>(g);
}
struct GItem { const bf16* A; const bf16* W; int lda; u64 tab; };
DI void g_srcA(GM& g, const GItem& it, int wid, int lane) {
  g.srdA = __builtin_amdgcn_make_buffer_rsrc((void*)it.A, (short)0, it.A ? 256 * it.lda * 2 : 0, 0x00020000);
  g.vbA = ((wid * 8 + (lane >> 3)) * it.lda + ((lane & 7) ^ (((wid & 1) * 4 + (lane >> 4)) & 7)) * 8) * 2;
#pragma unroll
  for (int j = 0; j < 8; ++j) g.sA[j] = j * 64 * it.lda;
  if (it.A == nullptr) g.mA = 0x28000;
}
DI void g_srcB(GM& g, const GItem& it) {
  g.srdB = __builtin_amdgcn_make_buffer_rsrc((void*)it.W, (short)0, it.A ? (it.lda - 1) * LDW * 2 + 512 : 0, 0x00020000);
  g.tab = it.tab;
  if (it.A == nullptr) g.mB = 0x28000;
}
DI void g_fix(GM& g) {
  g.tab = ((u64)(unsigned)__builtin_amdgcn_readfirstlane((unsigned)(g.tab >> 32)) << 32) | (unsigned)__builtin_amdgcn_readfirstlane((unsigned)g.tab);
  g.mA = __builtin_amdgcn_readfirstlane(g.mA); g.mB = __builtin_amdgcn_readfirstlane(g.mB); g.kW = __builtin_amdgcn_readfirstlane(g.kW);
#pragma unroll
  for (int j = 1; j < 8; ++j) g.sA[j] = __builtin_amdgcn_readfirstlane(g.sA[j]);
}
template <int I> DI void g_pro1(GM& g, unsigned po) {
  constexpr int grp = I >> 2, i = I & 3, t = grp < 4 ? 0 : grp < 7 ? 1 : 2;
  constexpr bool isA = grp == 0 || grp == 3 || grp == 5;
  const unsigned k = ((unsigned)g.tab >> (4 * t)) & 15, so = t == 1 ? po ^ 32768 : po;
  if constexpr (isA) {
    constexpr int jj = (grp == 3 ? 2 : 0) + (i & 1) + (i >> 1) * 4;
    DMA16(g.mA + so + jj * 4096, g.vbA, g.srdA, g.sA[jj] + k * 128);
  } else {
    constexpr int ks = grp == 2 || grp == 6;
    DMA16(g.mB + so + ks * 16384 + (i >> 1) * 8192 + (i & 1) * 4096, g.vbB, g.srdB, k * 0x80000 + (ks * 32 + 16 * (i & 1)) * LDW * 2 + (i >> 1) * 256);
  }
}
template <int B, int... Is> DI void g_pro(GM& g, unsigned po, iseq<Is...>) { (g_pro1<B + Is>(g, po), ...); }

typedef __attribute__((ext_vector_type(4))) unsigned u32x4;
typedef __attribute__((ext_vector_type(4))) float f32x4;

constexpr int R_S = 128, R_P = 192, R_CI = 224, R_E = 96;
constexpr int R_TS = 112, R_RS = 114, R_M = 116;
constexpr int R_PD = 118, R_THR = 120, R_TMP = 121;
constexpr int r_dk(int q) { return q < 6 ? 90 + q : 120 + q; }
constexpr int S_V = 96, S_T = 97, S_K = 98, S_I = 99, S_EV = 100;
DI void set_srdk(const void* p, unsigned n) {
  asm volatile("s_mov_b64 s[92:93], %0\n\ts_mov_b32 s94, %1\n\ts_mov_b32 s95, 0x20000" ::"s"(p), "s"(n));
}
struct AC {
  unsigned ka, va, dw[2], vq;
  __amdgpu_buffer_rsrc_t sw;
  unsigned m0x[2];
  u64 redo;
  unsigned rare;
  int mrow;
  bool diag0;
};
template <int X, int F> DI void kread(AC& c) {
  constexpr int r = 192 + 4 * (F & 15);
  asm volatile("ds_read_b128 a[%c1:%c2], %0 offset:%c3" ::"v"(c.ka), "i"(r), "i"(r + 3), "i"(X * 32768 + F * 1024));
}
template <int X, int F> DI void vread(AC& c) {
  constexpr int r = 192 + 4 * (F & 15);
  asm volatile("ds_read_b128 a[%c1:%c2], %0 offset:%c3" ::"v"(c.va), "i"(r), "i"(r + 3), "i"(X * 32768 + (F & 1) * 16384 + (F >> 1) * 1024));
}
template <int X, int... Fs> DI void kreads(AC& c, iseq<Fs...>) { (kread<X, Fs>(c), ...); }
template <int X, int J> DI void dma_m0(AC& c) {
  if constexpr (J == 0) asm volatile("s_mov_b32 m0, %0" ::"s"(c.m0x[X]));
  else if constexpr (J == 4) asm volatile("s_or_b32 m0, %0, 0x1000" ::"s"(c.m0x[X]) : "scc");
  else if constexpr (J == 8) asm volatile("s_bitset0_b32 m0, 16");
  else if constexpr (J == 12) asm volatile("s_bitset0_b32 m0, 12");
}
template <int J> DI void dma_at(AC& c) {
  if constexpr (J < 8)
    asm volatile("buffer_load_dwordx4 %0, %1, s96 offen offset:%2 sc1 lds" ::"v"(c.dw[J >= 4]), "s"(c.sw), "i"((J & 3) * 1024) : "memory");
  else
    asm volatile("buffer_load_dwordx4 v%c0, s[92:95], s98 offen offset:%1 sc1 lds" ::"i"(r_dk(J - 8)), "i"((J & 3) * 1024) : "memory");
}
DI void dma4k(const AC& c, unsigned so, unsigned m0v, int hf) {
    asm volatile("s_mov_b32 m0, %0\n\ts_nop 4\n\tbuffer_load_dwordx4 %1, %2, %3 offen sc1 lds\n\tbuffer_load_dwordx4 %1, %2, %3 offen offset:1024 sc1 lds\n\t"
                 "buffer_load_dwordx4 %1, %2, %3 offen offset:2048 sc1 lds\n\tbuffer_load_dwordx4 %1, %2, %3 offen offset:3072 sc1 lds"
                 ::"s"(m0v + hf * 4096), "v"(c.dw[hf]), "s"(c.sw), "s"(so) : "memory");
}
DI void dma8k(const AC& c, unsigned so, unsigned m0v) { dma4k(c, so, m0v, 0); dma4k(c, so, m0v, 1); }
template <int S_> DI void q_load1(unsigned vo, __amdgpu_buffer_rsrc_t srd, unsigned so) {
  asm volatile("buffer_load_dwordx4 a[%c0:%c1], %2, %3, %4 offen offset:%5 sc1" ::"i"(128 + 4 * S_), "i"(128 + 4 * S_ + 3), "v"(vo), "s"(srd),
               "s"(so + (S_ >> 2) * 4096), "i"((S_ & 3) * 1024) : "memory");
}
template <int Q> DI void acc_zero2() {
  asm volatile("v_accvgpr_write_b32 a[%c0], 0\n\tv_accvgpr_write_b32 a[%c1], 0" ::"i"(2 * Q), "i"(2 * Q + 1));
}
struct SMS { int ex[128], op0[128], nop[128], last; };
constexpr SMS sm_sched() {
  SMS z{};
  int et[32] = {}, e = 0;
  for (int t = 0; t < 128; ++t) {
    const int g = t & 15;
    const bool x = t < 48 ? (g >= 8 && (g & 1)) : (g >= 8 && g != 10 && g != 13 && g != 15);
    z.ex[t] = x && e < 32 ? e : -1;
    if (z.ex[t] >= 0) et[e++] = t;
  }
  int o = 0;
  for (int t = 0; t < 128; ++t) {
    const int g = t & 15;
    int cap = g < 8 ? (g == 0 ? 0 : 1) : (z.ex[t] >= 0 || t < 48 ? 0 : 2);
    z.op0[t] = o; z.nop[t] = 0;
    while (cap > 0 && o < 48 && et[2 * (o / 3) + (o % 3 == 0 ? 0 : 1)] + 2 <= t) {
      const int el = 2 * (o / 3) + o % 3;
      if (!(o % 3 < 2 && (el == 0 || el == 4))) --cap;
      ++o; ++z.nop[t]; z.last = t;
    }
  }
  return z;
}
constexpr SMS SM = sm_sched();
static_assert(SM.last <= 115);
constexpr int p_reg(int Y, int k) { return R_P + 16 * Y + 4 * ((k >> 3) * 2 + ((k >> 1) & 1)) + ((k >> 2) & 1) * 2 + (k & 1); }
template <int Y, int O> DI void sm_op() {
  constexpr int k = O / 3, e = 2 * k + O % 3, ts = R_TS + ((e >> 2) & 1);
  if constexpr (O % 3 == 2) asm volatile("v_cvt_pk_bf16_f32 v%c0, v%c1, v%c2" ::"i"(p_reg(Y, k)), "i"(R_E + ((2 * k) & 15)), "i"(R_E + ((2 * k + 1) & 15)));
  else if constexpr (e == 1 || e == 5) asm volatile("v_add_f32_e32 v%c0, v%c1, v%c2" ::"i"(ts), "i"(R_E + e - 1), "i"(R_E + e));
  else if constexpr (e != 0 && e != 4) asm volatile("v_add_f32_e32 v%c0, v%c0, v%c1" ::"i"(ts), "i"(R_E + (e & 15)));
}
template <int Y, int T> DI void sm_gap(AC& c) {
  if constexpr (SM.ex[T] >= 0) asm volatile("v_exp_f32 v%c0, v%c1" ::"i"(R_E + (SM.ex[T] & 15)), "i"(R_S + 32 * Y + SM.ex[T]));
  if constexpr (SM.nop[T] > 0) sm_op<Y, SM.op0[T]>();
  if constexpr (SM.nop[T] > 1) sm_op<Y, SM.op0[T] + 1>();
  if constexpr (SM.nop[T] > 2) sm_op<Y, SM.op0[T] + 2>();
  if constexpr (T == SM.last + 1) asm volatile("v_add_f32_e32 v%c0, v%c1, v%c2" ::"i"(R_TMP), "i"(R_TS), "i"(R_TS + 1));
  if constexpr (T == SM.last + 2) asm volatile("v_cmp_nle_f32_e64 %0, v%c1, v%c2" : "=s"(c.redo) : "i"(R_TMP), "i"(R_THR));
}
template <int X, bool FIRST, bool NOQK, int G> DI void gapA(AC& c) {
  constexpr int Xp = X ^ 1, kb = G >> 4, g = G & 15, qb = g >> 3, s = g & 7;
  if constexpr (NOQK && G < 48 && G % 3 == 0) q_load1<G / 3>(c.vq, c.sw, c.rare);
  if constexpr (G == 48) asm volatile("s_waitcnt vmcnt(%0) lgkmcnt(0)\n\ts_barrier" ::"i"(NOQK ? 16 : FIRST ? 12 : 0) : "memory");
  else if constexpr (g == 0 && !NOQK) asm volatile("s_waitcnt lgkmcnt(0)");
  if constexpr (!NOQK) {
    constexpr int r = 192 + 4 * ((kb & 1) * 8 + s), q = 128 + 4 * g;
    constexpr int d = R_S + 32 * X + 4 * (kb * 2 + qb), ci = s == 0 ? R_CI + 4 * (kb * 2 + qb) : d;
    if constexpr (FIRST && s == 0)
      asm volatile("v_mfma_f32_16x16x32_bf16 v[%c0:%c1], a[%c2:%c3], a[%c4:%c5], 0" ::"i"(d), "i"(d + 3), "i"(r), "i"(r + 3), "i"(q), "i"(q + 3));
    else
      asm volatile("v_mfma_f32_16x16x32_bf16 v[%c0:%c1], a[%c2:%c3], a[%c4:%c5], v[%c6:%c7]" ::"i"(d), "i"(d + 3), "i"(r), "i"(r + 3), "i"(q), "i"(q + 3),
                   "i"(ci), "i"(ci + 3));
    if constexpr (g < 8 && kb < 3) kread<X, (kb + 1) * 8 + g>(c);
    if constexpr (G == 54) asm volatile("s_add_u32 s96, s97, 0x1000000" ::: "scc");
    if constexpr (G == 55) dma_m0<X, 0>(c);
    if constexpr (G >= 56 && !(G & 1)) dma_at<(G - 56) / 2>(c);
    if constexpr (G == 57) asm volatile("s_mov_b32 s97, s98\n\ts_lshr_b32 s100, s100, 1" ::: "scc");
    if constexpr (G == 63) asm volatile("s_add_u32 s98, s98, s99" ::: "scc");
    if constexpr (G == 59 && !FIRST) asm volatile("s_and_b32 s101, s100, 1\n\ts_or_b32 s101, s101, %0" ::"s"((unsigned)c.redo) : "scc");
    if constexpr (G == 61 && !FIRST) asm volatile("s_or_b32 %0, s101, %1" : "=s"(c.rare) : "s"((unsigned)(c.redo >> 32)) : "scc");
  } else if constexpr (SM.nop[64 + G] > 0) asm volatile("s_nop 0");
  if constexpr (!FIRST && g < 8 && kb == 3) vread<Xp, g>(c);
  if constexpr (FIRST && (G & 3) == 1) d_store<16 + G / 4>();
  if constexpr (FIRST) acc_zero2<G>();
  else sm_gap<Xp, 64 + G>(c);
  GAP();
}
template <int R> DI void rescale1(float alpha) {
  float t;
  asm volatile("v_accvgpr_read_b32 %0, a[%c2]\n\tv_mul_f32 %0, %0, %1\n\tv_accvgpr_write_b32 a[%c2], %0" : "=&v"(t) : "v"(alpha), "i"(R));
}
template <int... Rs> DI void rescale_all(const float (&alpha)[2], iseq<Rs...>) { (rescale1<Rs>(alpha[(Rs >> 2) & 1]), ...); }
template <bool MAX> DI float row_reduce(float v) {
  auto p = __builtin_amdgcn_permlane32_swap(__float_as_uint(v), __float_as_uint(v), false, false);
  v = MAX ? fmaxf(__uint_as_float(p[0]), __uint_as_float(p[1])) : __uint_as_float(p[0]) + __uint_as_float(p[1]);
  p = __builtin_amdgcn_permlane16_swap(__float_as_uint(v), __float_as_uint(v), false, false);
  return MAX ? fmaxf(__uint_as_float(p[0]), __uint_as_float(p[1])) : __uint_as_float(p[0]) + __uint_as_float(p[1]);
}
template <int Y, int QB> DI float row_max() {
  constexpr int r = R_S + 32 * Y + 4 * QB;
  float mx;
  asm volatile("v_max_f32 %0, v%c1, v%c2\n\tv_max3_f32 %0, %0, v%c3, v%c4" : "=&v"(mx) : "i"(r), "i"(r + 1), "i"(r + 2), "i"(r + 3));
  static_for([&](auto K) {
    constexpr int q = r + 8 * (K + 1);
    float t = mx;
    asm volatile("v_max3_f32 %0, %0, v%c1, v%c2\n\tv_max3_f32 %0, %0, v%c3, v%c4" : "+v"(t) : "i"(q), "i"(q + 1), "i"(q + 2), "i"(q + 3));
    mx = t;
  }, make_iseq<3>{});
  return row_reduce<true>(mx);
}
template <int R> DI void v_sub(float d) { asm volatile("v_sub_f32 v%c0, v%c0, %1" ::"i"(R), "v"(d)); }
template <int B> DI void s_shift(const float (&d)[2]) { static_for([&](auto E) { v_sub<B + E>(d[(E >> 2) & 1]); }, make_iseq<32>{}); }
DI void m_shift(const float (&d)[2]) {
  s_shift<R_CI>(d);
  v_sub<R_M>(-d[0]); v_sub<R_M + 1>(-d[1]);
}
template <int B, bool INPL> DI void set_mask(const AC& c, int t64) {
  const int mr = c.mrow - t64;
  const float ninf = -INFINITY;
  static_for([&](auto I) {
    constexpr int i = I, r = B + 4 * i, k = (i >> 1) * 16 - (i & 1) * 16;
    const int mr_ = mr; const float b = ninf;
    if constexpr (INPL)
      asm volatile("v_cmp_gt_i32 vcc, %c4, %8\n\tv_cndmask_b32 v%c0, v%c0, %9, vcc\n\tv_cmp_gt_i32 vcc, %c5, %8\n\tv_cndmask_b32 v%c1, v%c1, %9, vcc\n\t"
                   "v_cmp_gt_i32 vcc, %c6, %8\n\tv_cndmask_b32 v%c2, v%c2, %9, vcc\n\tv_cmp_gt_i32 vcc, %c7, %8\n\tv_cndmask_b32 v%c3, v%c3, %9, vcc"
                   ::"i"(r), "i"(r + 1), "i"(r + 2), "i"(r + 3), "i"(k), "i"(k + 1), "i"(k + 2), "i"(k + 3), "v"(mr_), "v"(b) : "vcc");
    else
      asm volatile("v_cmp_gt_i32 vcc, %c4, %8\n\tv_cndmask_b32_e64 v%c0, -v%c10, %9, vcc\n\tv_cmp_gt_i32 vcc, %c5, %8\n\tv_cndmask_b32_e64 v%c1, -v%c10, %9, vcc\n\t"
                   "v_cmp_gt_i32 vcc, %c6, %8\n\tv_cndmask_b32_e64 v%c2, -v%c10, %9, vcc\n\tv_cmp_gt_i32 vcc, %c7, %8\n\tv_cndmask_b32_e64 v%c3, -v%c10, %9, vcc"
                   ::"i"(r), "i"(r + 1), "i"(r + 2), "i"(r + 3), "i"(k), "i"(k + 1), "i"(k + 2), "i"(k + 3), "v"(mr_), "v"(b), "i"(R_M + (i & 1)) : "vcc");
  }, make_iseq<8>{});
  asm volatile("s_nop 1");
}
template <int X> DI void sm_redo() {
  constexpr int Xp = X ^ 1;
  float d[2], dt[2], alpha[2], ts[2] = {0.f, 0.f};
  static_for([&](auto Q) {
    constexpr int qb = Q;
    const float pd = vget<R_PD + qb>();
    d[qb] = fmaxf(row_max<Xp, qb>() - pd, 0.f);
    dt[qb] = d[qb] + pd;
    vset<R_PD + qb>(d[qb]);
    alpha[qb] = __builtin_amdgcn_exp2f(-d[qb]);
    vset<R_RS + qb>(vget<R_RS + qb>() * alpha[qb]);
  }, make_iseq<2>{});
  vset<R_THR>(d[0] > 0.f || d[1] > 0.f ? -1.f : 4096.f);
  m_shift(d);
  static_for([&](auto K) {
    constexpr int k = K, qb = (k >> 1) & 1;
    const float e0 = __builtin_amdgcn_exp2f(vget<R_S + 32 * Xp + 2 * k>() - dt[qb]), e1 = __builtin_amdgcn_exp2f(vget<R_S + 32 * Xp + 2 * k + 1>() - dt[qb]);
    ts[qb] += e0; ts[qb] += e1;
    vset<p_reg(Xp, k)>(__uint_as_float(pk2(e0, e1)));
  }, make_iseq<16>{});
  vset<R_TS>(ts[0]); vset<R_TS + 1>(ts[1]);
  rescale_all(alpha, make_iseq<128>{});
  asm volatile("s_nop 1" ::: "memory");
  GAP();
}
template <int X, bool FIRST, bool NOQK, int... Gs> DI void phaseA_(AC& c, iseq<Gs...>) {
  (gapA<X, FIRST, NOQK, Gs>(c), ...);
}
template <int X, bool FIRST, bool NOQK> DI void phaseA(AC& c) { phaseA_<X, FIRST, NOQK>(c, make_iseq<64>{}); }

template <int X, bool SMX, int G> DI void gapB(AC& c) {
  constexpr int Xp = X ^ 1, blk = G >> 4, g = G & 15;
  if constexpr (g == 0) asm volatile("s_waitcnt lgkmcnt(0)");
  if constexpr (SMX && g == 7 && blk < 3) dma_m0<X, blk * 4 + 4>(c);
  constexpr int qb = (g >> 1) & 1, s = g & 1, r = 192 + 4 * ((blk & 1) * 8 + (g >> 2) * 2 + s), o = 4 * ((blk * 4 + (g >> 2)) * 2 + qb);
  constexpr int p = R_P + 16 * Xp + 4 * (s * 2 + qb);
  asm volatile("v_mfma_f32_16x16x32_bf16 a[%c0:%c1], a[%c2:%c3], v[%c4:%c5], a[%c0:%c1]" ::"i"(o), "i"(o + 3), "i"(r), "i"(r + 3), "i"(p), "i"(p + 3));
  if constexpr (SMX && (G == 1 || G == 2)) asm volatile("v_add_f32_e32 v%c0, v%c0, v%c1" ::"i"(R_RS + G - 1), "i"(R_TS + G - 1));
  if constexpr (g < 8) {
    if constexpr (blk < 3) vread<Xp, (blk + 1) * 8 + g>(c);
    else if constexpr (SMX) kread<Xp, g>(c);
  }
  if constexpr (SMX) {
    if constexpr (g >= 8 && !(g & 1) && blk < 3) dma_at<blk * 4 + 4 + (g - 8) / 2>(c);
    sm_gap<X, G>(c);
  }
  GAP();
}
template <int X, int... Gs> DI void phaseB_(AC& c, iseq<Gs...>) { (gapB<X, true, Gs>(c), ...); }
template <int X> DI void phaseB(AC& c) { phaseB_<X>(c, make_iseq<64>{}); }
constexpr int sm_nex() { int n = 0; for (int t = 0; t < 64; ++t) n += SM.ex[t] >= 0; return n; }
DI void phaseB0(AC& c) {
  dma_m0<0, 4>(c);
  kreads<1>(c, make_iseq<8>{});
  dma_at<4>(c);
  asm volatile("s_nop 3");
  if (__builtin_expect(c.diag0, 0)) set_mask<R_S, true>(c, 0);
  float d[2];
  d[0] = row_max<0, 0>(); GAP(); dma_at<5>(c);
  d[1] = row_max<0, 1>(); GAP(); dma_at<6>(c);
#pragma unroll
  for (int qb = 0; qb < 2; ++qb) if (!(d[qb] > -3.0e38f)) d[qb] = 0.f;
  vset<R_M>(d[0]); vset<R_M + 1>(d[1]);
  const f32x2c nd[2] = {{-d[0], -d[0]}, {-d[1], -d[1]}};
  static_for([&](auto E) {
    constexpr int e = E;
    const f32x2c v = nd[(e >> 1) & 1];
    asm volatile("v_pk_add_f32 v[%c0:%c1], v[%c0:%c1], %4\n\tv_mov_b64 v[%c2:%c3], %4" ::"i"(R_S + 2 * e), "i"(R_S + 2 * e + 1), "i"(R_CI + 2 * e), "i"(R_CI + 2 * e + 1), "v"(v));
    if constexpr (e == 3) dma_at<7>(c);
    if constexpr (e == 6) dma_m0<0, 8>(c);
    if constexpr (e == 7 || e == 11 || e == 15) dma_at<8 + (e - 7) / 4>(c);
  }, make_iseq<16>{});
  if (__builtin_expect(c.diag0, 0)) set_mask<R_CI, false>(c, 64);
  static_assert(sm_nex() >= 16 && SM.op0[64] >= 3 * ((sm_nex() - 15) / 2) && SM.op0[64] <= 24);
  static_for([&](auto E) {
    constexpr int e = E;
    asm volatile("v_exp_f32 v%c0, v%c1" ::"i"(R_E + e), "i"(R_S + e));
    if constexpr (e == 3) dma_at<11>(c);
    if constexpr (e == 6) dma_m0<0, 12>(c);
    if constexpr (e == 7 || e == 11 || e == 15) dma_at<12 + (e - 7) / 4>(c);
  }, make_iseq<16>{});
  static_for([&](auto O) { sm_op<0, O>(); }, make_iseq<SM.op0[64]>{});
  dma_at<15>(c);
  static_for([&](auto E) { asm volatile("v_exp_f32 v%c0, v%c1" ::"i"(R_E + (int)E), "i"(R_S + 16 + (int)E)); }, make_iseq<sm_nex() - 16>{});
  GAP();
}

#define ACLOB_ALL "a0","a1","a2","a3","a4","a5","a6","a7","a8","a9","a10","a11","a12","a13","a14","a15","a16","a17","a18","a19","a20","a21","a22","a23","a24","a25","a26","a27","a28","a29","a30","a31","a32","a33","a34","a35","a36","a37","a38","a39","a40","a41","a42","a43","a44","a45","a46","a47","a48","a49","a50","a51","a52","a53","a54","a55","a56","a57","a58","a59","a60","a61","a62","a63","a64","a65","a66","a67","a68","a69","a70","a71","a72","a73","a74","a75","a76","a77","a78","a79","a80","a81","a82","a83","a84","a85","a86","a87","a88","a89","a90","a91","a92","a93","a94","a95","a96","a97","a98","a99","a100","a101","a102","a103","a104","a105","a106","a107","a108","a109","a110","a111","a112","a113","a114","a115","a116","a117","a118","a119","a120","a121","a122","a123","a124","a125","a126","a127","a128","a129","a130","a131","a132","a133","a134","a135","a136","a137","a138","a139","a140","a141","a142","a143","a144","a145","a146","a147","a148","a149","a150","a151","a152","a153","a154","a155","a156","a157","a158","a159","a160","a161","a162","a163","a164","a165","a166","a167","a168","a169","a170","a171","a172","a173","a174","a175","a176","a177","a178","a179","a180","a181","a182","a183","a184","a185","a186","a187","a188","a189","a190","a191","a192","a193","a194","a195","a196","a197","a198","a199","a200","a201","a202","a203","a204","a205","a206","a207","a208","a209","a210","a211","a212","a213","a214","a215","a216","a217","a218","a219","a220","a221","a222","a223","a224","a225","a226","a227","a228","a229","a230","a231","a232","a233","a234","a235","a236","a237","a238","a239","a240","a241","a242","a243","a244","a245","a246","a247","a248","a249","a250","a251","a252","a253","a254","a255"

struct EP { float fa[2], fb[2]; int vo; unsigned so[4]; };
template <int I, int E, bool MERGE> DI void epi_pair(const EP& p) {
  constexpr int qb = I & 1, r = 16 * (I >> 1) + 4 * qb + (E >> 1) * 8 + (E & 1) * 2, t = 96 + 2 * E, w = 128 + 4 * I + E;
  if constexpr (MERGE) asm volatile("v_accvgpr_read_b32 v106, a[%c0]" ::"i"(w));
  asm volatile("v_accvgpr_read_b32 v%c0, a[%c2]\n\tv_accvgpr_read_b32 v%c1, a[%c3]\n\tv_mul_f32 v%c0, v%c0, %4\n\tv_mul_f32 v%c1, v%c1, %4"
               ::"i"(t), "i"(t + 1), "i"(r), "i"(r + 1), "v"(p.fa[qb]));
  if constexpr (MERGE)
    asm volatile("v_lshlrev_b32 v104, 16, v106\n\tv_and_b32 v105, 0xffff0000, v106\n\tv_fmac_f32 v%c0, v104, %2\n\tv_fmac_f32 v%c1, v105, %2"
                 ::"i"(t), "i"(t + 1), "v"(p.fb[qb]));
  asm volatile("v_cvt_pk_bf16_f32 v%c0, v%c1, v%c2" ::"i"(w), "i"(t), "i"(t + 1));
}
template <int I> DI void epi_store(const AC& c, const EP& p) {
  asm volatile("buffer_store_dwordx4 v[%c0:%c1], %2, %3, %4 offen offset:%5 sc1" ::"i"(128 + 4 * I), "i"(131 + 4 * I), "v"(p.vo), "s"(c.sw),
               "s"(p.so[I >> 2]), "i"((I & 3) * 1024) : "memory");
}
constexpr int R_TB = 107, R_VS = 111;
template <int HF, int R> DI void p_read() {
  constexpr int mb = R >> 4, r = 64 * HF + 2 * R;
  asm volatile("ds_read_b64_tr_b16 a[%c1:%c2], v%c0 offset:%c3" ::"i"(R_TB + 2 * HF + mb), "i"(r), "i"(r + 1), "i"((R & 15) * 2048));
}
template <int QB, int E> DI void p_cvt() {
  asm volatile("v_cvt_pk_bf16_f32 v%c0, v%c1, v%c2" ::"i"(208 + 4 * QB + E), "i"(192 + 8 * QB + 2 * E), "i"(193 + 8 * QB + 2 * E));
}
template <int QB, int PR> DI void p_store(const AC& c) {
  asm volatile("buffer_store_dwordx4 v[%c0:%c1], v%c2, %3, s96 offen offset:%4 sc1" ::"i"(208 + 4 * QB), "i"(211 + 4 * QB), "i"(R_VS + QB), "s"(c.sw),
               "i"(PR * 128) : "memory");
}
constexpr int p_dma(int HF, int G) { return G == 3 && !HF ? 0 : G == 11 ? 1 - HF : G == 15 ? 2 - HF : G == 19 ? 3 - HF : G == 27 && HF ? 3 : -1; }
template <int HF, int G> DI void p_gap(AC& c) {
  constexpr int ch = G >> 3, t = G & 7, a = 64 * HF + 32 * (ch & 1) + 4 * t, b = 128 + 4 * (2 * t + (ch >> 1)), d = 192 + 4 * ch;
  if constexpr (t == 0)
    asm volatile("v_mfma_f32_16x16x32_bf16 v[%c0:%c1], a[%c2:%c3], v[%c4:%c5], 0" ::"i"(d), "i"(d + 3), "i"(a), "i"(a + 3), "i"(b), "i"(b + 3));
  else
    asm volatile("v_mfma_f32_16x16x32_bf16 v[%c0:%c1], a[%c2:%c3], v[%c4:%c5], v[%c0:%c1]" ::"i"(d), "i"(d + 3), "i"(a), "i"(a + 3), "i"(b), "i"(b + 3));
  p_read<HF ^ 1, G>();
  if constexpr (G == 0) asm volatile("v_xor_b32 v%c0, s100, v%c0" ::"i"(R_TB + 2 * HF + 1));
  if constexpr (G == 17) asm volatile("v_xor_b32 v%c0, s100, v%c0" ::"i"(R_TB + 2 * (HF ^ 1)));
  if constexpr (G >= 4 && G <= 8 && G != 7) p_cvt<1, (G < 7 ? G - 4 : 3)>();
  if constexpr (G == 24 || (G == 7 && HF == 0)) asm volatile("s_waitcnt lgkmcnt(8)");
  if constexpr (G == 7 && HF == 1) asm volatile("s_waitcnt vmcnt(13) lgkmcnt(8)\n\ts_barrier" ::: "memory");
  if constexpr (G == 10) p_store<1, HF ^ 1>(c);
  if constexpr (G >= 20 && G < 24) p_cvt<0, G - 20>();
  if constexpr (G == 25) p_store<0, HF>(c);
  if constexpr (p_dma(HF, G) >= 0) dma_at<(HF ? 8 : 12) + p_dma(HF, G)>(c);
  if constexpr (HF == 0 && G == 2) asm volatile("s_bitset0_b32 m0, 12");
  if constexpr (HF == 0 && G == 13) asm volatile("s_add_u32 s96, s96, 0x40000");
  if constexpr (HF == 0 && G == 26) asm volatile("s_add_u32 s98, s98, 128");
  if constexpr (HF == 0 && G == 27) asm volatile("s_cmp_lt_u32 s98, 0x380");
  if constexpr (HF == 0 && G == 28) asm volatile("s_cselect_b32 s99, s99, 0x31000");
  if constexpr (HF == 1 && G == 1) asm volatile("s_xor_b32 s100, s100, 0x10000");
  if constexpr (HF == 1 && G == 2) asm volatile("s_xor_b32 s97, s97, s100");
  if constexpr (HF == 1 && G == 9) asm volatile("s_or_b32 m0, s97, s99");
  if constexpr (HF == 1 && G == 30) asm volatile("s_cmp_lt_u32 s98, 0x500");
  GAP();
}
template <int HF, int... Gs> DI void p_half(AC& c, iseq<Gs...>) { (p_gap<HF, Gs>(c), ...); }
constexpr int f_dma(int G) { return G == 2 ? 0 : G == 5 ? 1 : G == 8 ? 2 : G >= 12 && G <= 36 && G % 6 == 0 ? 1 + G / 6 : G >= 50 && G <= 68 && G % 6 == 2 ? G / 6 : -1; }
constexpr int f_ndma(int G) { int n = 0; for (int g = 0; g < G; ++g) n += f_dma(g) >= 0; return n; }
static_assert(f_ndma(74) == 12);
template <int MODE, int G> DI void gapF(AC& c, const EP& p) {
  if constexpr (MODE && G == 48) asm volatile("s_waitcnt lgkmcnt(0)\n\ts_barrier" ::: "memory");
  if constexpr (G < 64) gapB<0, false, G>(c);
  if constexpr (MODE == 2 && G >= 10 && (G - 10) % 16 == 0) asm volatile("s_waitcnt vmcnt(%0)" ::"i"(12 - (G - 10) / 4 + f_ndma(G)) : "memory");
  if constexpr (G >= 10) epi_pair<(G - 10) / 4, (G - 10) & 3, MODE == 2>(p);
  if constexpr (MODE == 0) {
    if constexpr (G >= 14 && ((G - 14) & 3) == 0) epi_store<(G - 14) / 4>(c, p);
    if constexpr (G == 73) epi_store<15>(c, p);
  } else {
    if constexpr (G == 0) asm volatile("s_or_b32 m0, %0, 0x1000\n\ts_movk_i32 s98, 256" ::"s"(c.m0x[0]) : "scc");
    if constexpr (G == 15) asm volatile("s_bitset0_b32 m0, 12");
    if constexpr (G == 48) asm volatile("s_or_b32 m0, %0, 0x1000\n\ts_movk_i32 s98, 384" ::"s"(c.m0x[1]) : "scc");
    if constexpr (f_dma(G) >= 0) dma_at<8 + (f_dma(G) & 7)>(c);
    if constexpr (G == 73) asm volatile("s_waitcnt vmcnt(4) lgkmcnt(0)" ::: "memory");
    if constexpr (G >= 48 && G < 64) { p_read<0, 2 * (G - 48)>(); p_read<0, 2 * (G - 48) + 1>(); }
  }
  GAP();
}
template <int MODE, int... Gs> DI void phaseF(AC& c, const EP& p, iseq<Gs...>) { (gapF<MODE, Gs>(c, p), ...); }
template <int U, int K> DI void r_load(const AC& c, unsigned vo, unsigned so) {
  asm volatile("buffer_load_dwordx4 a[%c0:%c1], %2, %3, %4 offen offset:%5 sc1" ::"i"(64 * U + 4 * K), "i"(64 * U + 4 * K + 3), "v"(vo), "s"(c.sw),
               "s"(so + (K >> 2) * 4096), "i"((K & 1) * 512 + ((K >> 1) & 1) * 2048) : "memory");
}
template <int U, int K> DI void r_sum() {
  constexpr int d = 100 + 4 * (K & 1), b = 64 * U + 4 * K;
  if constexpr (K < 2) asm volatile("v_mfma_f32_16x16x32_bf16 v[%c0:%c1], v[96:99], a[%c2:%c3], 0" ::"i"(d), "i"(d + 3), "i"(b), "i"(b + 3));
  else asm volatile("v_mfma_f32_16x16x32_bf16 v[%c0:%c1], v[96:99], a[%c2:%c3], v[%c0:%c1]" ::"i"(d), "i"(d + 3), "i"(b), "i"(b + 3));
}
template <int U> DI void r_fin(unsigned vo, __amdgpu_buffer_rsrc_t srd, unsigned so) {
  asm volatile("" : "+s"(srd), "+s"(so));
  static_for([&](auto K) { r_sum<U, K>(); }, make_iseq<16>{});
  asm volatile("s_nop 7");
  static_for([&](auto E) { asm volatile("v_cvt_pk_bf16_f32 v%c0, v%c1, v%c2" ::"i"(116 + 4 * U + E), "i"(100 + 2 * E), "i"(101 + 2 * E)); }, make_iseq<4>{});
  asm volatile("buffer_store_dwordx4 v[%c3:%c4], %0, %1, %2 offen" ::"v"(vo), "s"(srd), "s"(so), "i"(116 + 4 * U), "i"(119 + 4 * U) : "memory");
}

#define CMAX 12
#define NYT 16
DI u64* fw(u64* p, int w) { return p + (w >> 4) * 544 + (w & 15); }
DI void set_flag(u64* p, u64 tag, int skip = 0) {
  asm volatile("s_cmp_lg_u32 %2, 0\n\ts_cbranch_scc1 1f\n\ts_mov_b64 exec, 1\n\tglobal_store_dwordx2 %0, %1, off sc1\n\ts_mov_b64 exec, -1\n1:"
               ::"v"(p), "v"(tag), "s"(skip) : "scc", "memory");
}
DI void wait_flags(u64* p, u64 need, u64 tag) {
  const u64* q = fw(p, threadIdx.x & 63);
  u64 v; unsigned cnt;
  asm volatile("s_mov_b32 %1, 0x100000\n\ts_mov_b64 exec, %3\n"
               "1:\n\tglobal_load_dwordx2 %0, %2, off sc1\n\ts_waitcnt vmcnt(0)\n\tv_cmp_eq_u64 vcc, %4, %0\n\t"
               "s_cmp_eq_u64 vcc, %3\n\ts_cbranch_scc1 2f\n\ts_sleep 4\n\ts_sub_u32 %1, %1, 1\n\ts_cbranch_scc0 1b\n"
               "2:\n\ts_mov_b64 exec, -1" : "=&v"(v), "=&s"(cnt) : "v"(q), "s"(need), "s"(tag) : "vcc", "scc", "memory");
}
template <int H> DI void fl_issue(u64* gf, u64 need) {
  const u64* q = fw(gf, threadIdx.x & 63) + H * 4 * 544;
  asm volatile("s_mov_b64 exec, %1\n\tglobal_load_dwordx2 v[%c2:%c3], %0, off sc1\n\ts_mov_b64 exec, -1" ::"v"(q), "s"(need), "i"(122 + 2 * H), "i"(123 + 2 * H) : "memory");
}
template <int H> DI void fl_check(u64* gf, u64 need, u64 tag) {
  u64 m;
  asm volatile("v_cmp_eq_u64 %0, %1, v[%c2:%c3]" : "=&s"(m) : "s"(tag), "i"(122 + 2 * H), "i"(123 + 2 * H) : "memory");
  if (__builtin_expect((m & need) != need, 0)) wait_flags(gf + H * 4 * 544, need, tag);
}
template <bool TR, bool SCL, class START, class MID, class PRE>
DI void g_item(GM& g, const GItem& nxt, u64* pend, u64& tag, unsigned out, int wid, int lane,
               u64* gf, u64 n0, u64 n1, unsigned evx, START&& start, MID&& mid, PRE&& pre) {
  constexpr int nkt = SCL ? 7 : 16, P0 = SCL ? 0 : 1;
  GE e;
  GAP();
  start(std::integral_constant<int, P0>{});
  g_fix(g);
  asm volatile("s_nop 4");
  GAP();
  auto hook = [&](int t) __attribute__((always_inline)) {
    if (t == 1) {
      set_flag(pend, tag, wid);
      mid(1);
    } else if (t < nkt - 3) mid(t);
    else if (t == nkt - 3) {
      g_srcB(g, nxt);
      if constexpr (!SCL) { fl_issue<0>(gf, n0); if (n1) fl_issue<1>(gf, n1); mid(t); }
    } else g_srcA(g, nxt, wid, lane);
    g_fix(g);
    asm volatile("s_nop 4");
    GAP();
  };
  g_half<TR, P0, 0, SCL ? 1 : 4>(g, e);
  g_half<TR, P0, 1, SCL ? 1 : 4>(g, e);
  unsigned evm = 2u | (4u << (nkt - 5)) | (4u << (nkt - 4)) | evx;
  asm volatile("" : "+s"(evm));
#pragma unroll 1
  for (int t = 1;; t += 2) {
    g_half<TR, P0 ^ 1, 0, 0>(g, e);
    if (__builtin_expect((evm >> t) & 1, 0)) hook(t);
    g_half<TR, P0 ^ 1, 1, 0>(g, e);
    if (SCL && t + 2 >= nkt) break;
    g_half<TR, P0, 0, 0>(g, e);
    if (__builtin_expect(t + 1 == (SCL ? nkt - 3 : nkt - 2), 0)) hook(t + 1);
    g_half<TR, P0, 1, 0>(g, e);
    if (!SCL && t + 3 >= nkt) break;
  }
  int l = threadIdx.x & 63; asm volatile("" : "+v"(l));
  vset<112>(__uint_as_float(l * 16)); vset<113>(__uint_as_float(l * 16 + (TR ? 4096 : 8192)));
  sset<101>(out + (wid >> 1) * 65536 + (wid & 1) * (TR ? 8192 : 4096));
  pre(e);
  if constexpr (!SCL) asm volatile("s_nop 4" : "+s"(e.sk), "+s"(e.sx), "+s"(e.mC), "+s"(e.mK));
  GAP();
  g_half<TR, 0, 0, SCL ? 2 : 3>(g, e);
  g_half<TR, 0, 1, SCL ? 2 : 3>(g, e);
}

constexpr size_t TSZ = (size_t)NH * SEQ * HD;
constexpr unsigned WSB = 5 * TSZ * 2 + 128 * 66560;
__global__ __launch_bounds__(256, 1) __attribute__((amdgpu_num_vgpr(90), amdgpu_num_sgpr(96))) void fused_kernel(const bf16* __restrict__ ckv, const bf16* __restrict__ wuk,
                                                       const bf16* __restrict__ wuv, const bf16* __restrict__ x,
                                                       const bf16* __restrict__ wq, bf16* Qb,
                                                       const bf16* __restrict__ ak, const bf16* __restrict__ av,
                                                       const bf16* __restrict__ ash, const bf16* __restrict__ assmk,
                                                       const bf16* __restrict__ assmD, const bf16* __restrict__ wo,
                                                       const bf16* __restrict__ aq, bf16* __restrict__ out) {
  extern __shared__ __attribute__((aligned(16))) char lds[];
  asm volatile("at_code_begin:" ::: ACLOB_ALL, "v255", "s101");
  asm volatile("" : "+s"(ckv), "+s"(wuk), "+s"(wuv), "+s"(x), "+s"(wq), "+s"(Qb), "+s"(ak), "+s"(av), "+s"(ash), "+s"(assmk),
                    "+s"(assmD), "+s"(wo), "+s"(aq), "+s"(out));
  float* partbuf = (float*)(Qb + 5 * TSZ);
  u64* flags = (u64*)(partbuf + (size_t)128 * 16640);
  bf16* xqT = (bf16*)(flags + 400 * 544);
  KST(0, 0);
  const int tid = threadIdx.x, lane = tid & 63;
  const int wid = __builtin_amdgcn_readfirstlane(tid >> 6);
  const int b = blockIdx.x, j = b & 7, r = b >> 3, rr = r & 15, hsel = rr & 1, pj = rr >> 1, kv = pj & 1;
  const bool isY = r >= 16;
  const int h = j + 8 * hsel, mt = (rr >> 2) + (isY ? 4 : 0);
  const unsigned lbase = (unsigned)(size_t)lds;
  u64* gf = fw(flags, j * 128);
  const unsigned m0d = lbase + 131072 + 24576;
  const unsigned m0f = lbase + 131072 + wid * 8192;
  const int cch = rr * 4 + wid;
  const bool doA = !isY && cch < 56;
  u64 tag = 0;
  AC c;
  auto att_init = [&]() __attribute__((always_inline)) {
    int l = threadIdx.x & 63; asm volatile("" : "+v"(l));
    c.ka = lbase + l * 16;
    c.dw[0] = wid * 8192 + l * 16; c.dw[1] = c.dw[0] + 4096;
    c.sw = __builtin_amdgcn_make_buffer_rsrc((void*)Qb, (short)0, WSB, 0x00020000);
    static_for([&](auto I) { vset<r_dk(I)>(__uint_as_float(c.dw[I < 4])); }, make_iseq<8>{});
    sset<S_I>(0x8000);
  };
  const unsigned oQ = (unsigned)h << 20, oK = oQ + (unsigned)(TSZ * 2);
  auto pro_k = [&](int t0) __attribute__((always_inline)) {
    dma8k(c, oK + t0 * 32768, lbase + wid * 8192);
    dma8k(c, oK + t0 * 32768 + 32768, lbase + 32768 + wid * 8192);
  };
  static_assert(NYT <= 16 && NYT >= 12 && !(NYT & 1));
  const bool solo = pj == 7;
  const bool doP = isY != solo;
  const int NY = solo ? 18 : NYT;
  const int nx0 = 32 - 2 * pj - NY, cx = nx0 < CMAX ? nx0 : CMAX, ex = nx0 - cx;
  const u64 needY = (((0x101ull << (((31 - 2 * pj) >> 2) + 1)) - 0x1010ull) << (hsel * 16)) & ~(1ull << (hsel * 16 + kv * 8 + mt));
  const u64 needX = ((0x1010ull << ((ex + 3) >> 2)) - 0x1010ull) << (hsel * 16);
  const u64 needQ = (1ull << (32 + hsel * 8 + (pj >> 1))) | (1ull << (32 + hsel * 8 + ((15 - pj) >> 1)));
  const u64 needA = 0x0f0full << (hsel * 16);
  const u64 needW = 0x3fffull << 36;
  u64* gflag = fw(gf, hsel * 16 + kv * 8 + mt);
  unsigned dv = 0;
  {
    GM g;
    PR pr;
    const u64 tabQ = 0x6543210ull;
    const int cls = (rr + (rr >> 2)) & 3;
    const u64 tabX = cls == 0 ? 0xfedcb9875a463210ull : cls == 1 ? 0xefadc6982b574310ull : cls == 2 ? 0xfebad7693c285410ull : 0xefcba8764d392510ull;
    const u64 tabG = !isY ? tabX : cls == 0 ? 0xfedcb987543a6210ull : cls == 1 ? 0xefadc698254b7310ull : cls == 2 ? 0xfebad769325c8410ull : 0xefcba876432d9510ull;
    const GItem itQ{x + (size_t)pj * 256 * DA, wq + h * 256, DA, tabQ};
    const GItem itG{ckv + (size_t)mt * 256 * DC, (kv ? wuv : wuk) + h * 256, DC, tabG};
    const GItem none{nullptr, nullptr, 0, 0};
    if (doP) prep_load(pr, tid, j, rr, ak, av, ash);
    set_srdk(Qb, WSB);
    d_none();
    const unsigned lA = lbase + (isY ? 65536 : 0), lW = lA ^ 65536;
    g.mA = lA + wid * 1024; g.mB = lW + wid * 1024; g.kW = 0;
    g.vbB = (4 * wid + (lane >> 4)) * LDW * 2 + 16 * ((lane & 15) ^ (((lane >> 4) << 2) | wid));
    g_srcA(g, isY ? itQ : itG, wid, lane);
    g_srcB(g, isY ? itQ : itG);
    g_fix(g);
    const unsigned po = isY ? 0 : 32768;
    g_pro<0>(g, po, make_iseq<12>{});
    asm volatile("global_load_dwordx2 v[120:121], %0, off sc1" ::"v"(fw(flags, 6144)) : "memory");
    if (doP) {
      asm volatile("s_waitcnt vmcnt(13)" ::: "memory");
#pragma unroll
      for (int q = 0; q < 7; ++q) asm volatile("" : "+v"(pr.kk[q]));
#pragma unroll
      for (int q = 0; q < 4; ++q) asm volatile("" : "+v"(pr.vv[q]), "+v"(pr.w[q]));
      static_for([&](auto Ic) __attribute__((always_inline)) {
        constexpr int I = Ic;
        g_pro<12 + 4 * I>(g, po, make_iseq<4>{});
        GAP();
        prep_math<I>(pr, tid, lds + 131072);
        GAP();
      }, make_iseq<4>{});
      asm volatile("s_waitcnt lgkmcnt(0)\n\ts_barrier" ::: "memory");
      prep_store(tid, j, rr, xqT, lds + 131072);
    } else g_pro<12>(g, po, make_iseq<4>{});
    GAP();
    CODE_TOUCH("at_code_begin", "pf_code_end", r * 4 + wid, 128u, 8, m0d);
    {
      const int fr = lane & 15, fq = lane >> 4, q = (lane & 15) >> 2, p = lane & 3;
#pragma unroll
      for (int k2 = 0; k2 < 2; ++k2)
        g.aA[k2] = lA + (wid >> 1) * 16384 + (fr >> 3) * 1024 + (fr & 7) * 128 + (((4 * k2 + fq) ^ (fr >> 1)) * 16);
#pragma unroll
      for (int hf = 0; hf < 2; ++hf)
#pragma unroll
        for (int nf = 0; nf < 8; ++nf)
          g.aB[hf][nf] = lW + (wid & 1) * 8192 + off_b(8 * fq + 4 * hf + q, nf * 2 + (p >> 1)) + 8 * (p & 1);
    }
    auto start = [&](auto Pc) __attribute__((always_inline)) {
      if (doP) asm volatile("s_waitcnt vmcnt(13)" ::: "memory");
      else {
        asm volatile("s_nop 4");
        g_pro<16>(g, po, make_iseq<12>{});
        asm volatile("s_waitcnt vmcnt(8)" ::: "memory");
      }
      asm volatile("s_barrier" ::: "memory");
      g_read0<Pc>(g, make_iseq<16>{});
      GAP();
      u64 z = (((u64)__float_as_uint(vget<121>()) << 32) | __float_as_uint(vget<120>())) + 0x9e3779b97f4a7c15ull;
      z = (z ^ (z >> 30)) * 0xbf58476d1ce4e5b9ull; z = (z ^ (z >> 27)) * 0x94d049bb133111ebull; z ^= z >> 31;
      tag = ((u64)(unsigned)__builtin_amdgcn_readfirstlane((int)(z >> 32)) << 32) | (unsigned)__builtin_amdgcn_readfirstlane((int)z);
      asm volatile("s_waitcnt lgkmcnt(0)\n\ts_barrier\n\ts_nop 4" ::: "memory");
      g_pro<28>(g, po, make_iseq<4>{});
      g.kW = ((unsigned)g.tab >> 8) & 15;
      g.voA = g.vbA + (((unsigned)g.tab >> 4) & 15) * 128;
      g.voB = g.vbB + g.kW * 0x80000;
      g.tab >>= 12;
      GAP();
    };
    u64* pend = fw(gf, 64 + r);
    const u64 np0 = isY ? needA | needQ : needQ, np1 = doA ? 0x3fffc000ull : 0ull;
    if (isY) {
      g_item<false, true>(g, itG, pend, tag, oQ + pj * 131072, wid, lane, gf, 0, 0, 0, start, [&](int) {}, [&](GE&) {});
      pend = fw(gf, 32 + hsel * 8 + pj);
      KST(0, 3);
    }
    auto start2 = [&](auto Pc) __attribute__((always_inline)) { if (!isY) start(Pc); };
#define TT 3
#define TK 1
    auto mid = [&](int t) __attribute__((always_inline)) {
      if (t == 1) { if (!isY) post_dma<false>(j, r * 4 + wid, xqT, aq, m0f); }
      else {
        int l = threadIdx.x & 63; asm volatile("" : "+v"(l));
        if (t < 13) {
          const char* tp = (const char*)ckv + 1024 * DC * 2 + (((b & 15) * 4 + wid) * 256 + l) * 128;
          asm volatile("s_mov_b32 m0, %1\n\ts_nop 0\n\tglobal_load_lds_dword %0, off" ::"v"(tp), "s"(m0d) : "memory");
          asm volatile("global_load_lds_dword %0, off" ::"v"(tp + 8192) : "memory");
          asm volatile("global_load_lds_dword %0, off" ::"v"(tp + 16384) : "memory");
          asm volatile("global_load_lds_dword %0, off" ::"v"(tp + 24576) : "memory");
        } else if (doA && TK) {
          const char* tp = (const char*)assmk + (size_t)(j * 56 + cch) * (SEQ * 2) + (l & 31) * 128;
          asm volatile("s_mov_b32 m0, %1\n\ts_nop 0\n\tglobal_load_lds_dword %0, off" ::"v"(tp), "s"(m0d) : "memory");
        }
      }
    };
    const unsigned evx = isY || !solo || !TT ? 0u : 1u << TT;
    auto pre = [&](GE& e) __attribute__((always_inline)) {
      fl_check<0>(gf, np0, tag);
      fl_check<1>(gf, np1, tag);
      att_init();
      const int d = doA ? j * 56 + cch : 0;
      asm volatile("global_load_ushort %0, %1, off" : "=v"(dv) : "v"(assmD + d) : "memory");
      e.sk = __builtin_amdgcn_make_buffer_rsrc((void*)(assmk + (size_t)d * SEQ), (short)0, SEQ * 2, 0x00020000);
      e.sx = __builtin_amdgcn_make_buffer_rsrc((void*)(xqT + (size_t)d * SEQ), (short)0, SEQ * 2, 0x00020000);
      e.vl = c.ka - lbase; e.vl0 = e.vl - 32; e.vq = doA ? 0x80000000u : e.vl; e.vk[0] = c.dw[0]; e.vk[1] = c.dw[1];
      e.mC = doA ? lbase + 65536 + wid * 15488 : 0x30000;
      e.mK = isY ? lbase + wid * 8192 : 0x30000;
      const unsigned q0 = oQ + ((solo && !isY ? pj : 15 - pj) * 128 + wid * 32) * 512;
      sset<96>(q0); sset<97>(q0);
      sset<98>(oK + cx * 32768);
    };
    if (kv) g_item<true, false>(g, none, pend, tag, oK + 0x1000000 + mt * 131072, wid, lane, gf, np0, np1, evx, start2, mid, pre);
    else g_item<false, false>(g, none, pend, tag, oK + mt * 131072, wid, lane, gf, np0, np1, evx, start2, mid, pre);
  }
  KST(0, 4);
  u64* pend2 = gflag;
  asm volatile("s_waitcnt vmcnt(%1)" : "+v"(dv) : "i"(W_CONV) : "memory");
  if (doA) {
    const float Dd = bf2f(dv);
    KST(0, 5);
    conv_wave(j * 56 + cch, Dd, xqT + (size_t)DA * SEQ, lds + 65536 + wid * 15488,
              [&](int cr) __attribute__((always_inline)) {
      if (cr == 8) {
        const char* tp = (const char*)wo + (size_t)(j + (cch >= 28 ? 8 : 0)) * (HD * DA * 2) + ((cch % 28) * 64 + lane) * 128;
        asm volatile("s_mov_b32 m0, %1\n\ts_nop 0\n\tglobal_load_lds_dword %0, off" ::"v"(tp), "s"(m0d) : "memory");
      }
      if (cr == 1) {
        asm volatile("s_waitcnt vmcnt(0)\n\ts_barrier" ::: "memory");
        set_flag(gflag, tag, wid);
      } else if (cr == 2) fl_issue<0>(gf, 0x0101ull << (hsel * 16));
      else if (cr >= 4 && cr < 8) {
        if (cr == 4) {
          asm volatile("s_waitcnt vmcnt(16)" ::: "memory");
          fl_check<0>(gf, 0x0101ull << (hsel * 16), tag);
        }
        dma4k(c, oK + ((cr >> 1) & 1) * 32768, lbase + ((cr >> 1) & 1) * 32768 + wid * 8192, cr & 1);
      }
    });
    KST(0, 6);
    pend2 = fw(gf, 100 + rr);
  } else if (!isY) {
    static_for([&](auto I) { d_store<16 + I>(); }, make_iseq<16>{});
    asm volatile("s_waitcnt vmcnt(0)\n\ts_barrier" ::: "memory");
    set_flag(gflag, tag, wid);
    KST(0, 5);
    wait_flags(gf, needA, tag);
    pro_k(0);
    asm volatile("s_waitcnt vmcnt(0)" ::: "memory");
    KST(0, 6);
  }
  c.va = c.ka + 65536;
  c.m0x[0] = lbase + 65536 + wid * 8192; c.m0x[1] = c.m0x[0] + 32768;
  const unsigned oP = (unsigned)(5 * TSZ * 2) + (h * 8 + pj) * 66560;
  const int qbF = isY ? 15 - pj : pj;
  constexpr unsigned oS = (unsigned)(3 * TSZ * 2);
  const int vml = 65536 + wid * 256 + (lane & 15) * 16;
  const int nitems = isY ? 1 : 2, it0 = !isY && solo ? 1 : 0;
#pragma unroll 1
  for (int it = it0; it < nitems; ++it) {
    int qb, t0, n, jmp, c0, c1;
    u64 n0, n1;
    if (isY) { qb = 15 - pj; t0 = cx; n = NY; jmp = ex; c1 = 14 - cx; n1 = needY; c0 = solo ? 30 : NY - 2; n0 = 1ull << (48 + hsel * 8 + pj); }
    else if (it == 0) { qb = 15 - pj; t0 = 0; n = nx0; jmp = 16 - cx; c1 = ex ? cx - 2 : 30; n1 = needX; c0 = 2; n0 = needA; }
    else { qb = pj; t0 = 0; n = 2 * pj + 2; jmp = 0; c1 = 30; c0 = 30; n0 = n1 = 0; }
    const bool last = isY || it == 1;
    c.mrow = qb * 128 + wid * 32 + (lane & 15) - 4 * (lane >> 4);
    static_for([&](auto I) { vset<R_RS + I>(0.f); }, make_iseq<6>{});
    vset<R_THR>(4096.f);
    if (!isY) d_none();
    c.diag0 = qb == 0;
    sset<S_T>(oK + t0 * 32768); sset<S_K>(oK + t0 * 32768 + 32768);
    const int ct = !last || isY ? 30 : n > 2 ? n - 3 : 0, cw = isY ? 30 : last ? (n > 2 ? 0 : 30) : n - 2;
    sset<S_EV>((3u << c0) | (3u << c1) | (last ? 3u << n >> 2 : 1u << n >> 1) | (isY ? 4u : 0u));
    if (it != it0) asm volatile("s_waitcnt vmcnt(16)" ::: "memory");
    else {
      if (doA) asm volatile("s_waitcnt vmcnt(6)" ::: "memory");
      asm volatile("s_waitcnt vmcnt(%0) lgkmcnt(0)\n\ts_barrier" ::"i"(W_K0) : "memory");
    }
    KST(0, 8 + 3 * it);
    kreads<0>(c, make_iseq<8>{});
    GAP();
    auto event = [&](int i) __attribute__((always_inline)) {
      if (i == c0) fl_check<0>(gf, n0, tag);
      if (i == c1) { fl_check<0>(gf, n1, tag); asm volatile("s_add_u32 s98, s98, %0" ::"s"(jmp * 32768) : "scc"); }
      if (i + 1 == c0 || i + 1 == c1) fl_issue<0>(gf, (i + 1 == c0 ? n0 : 0) | (i + 1 == c1 ? n1 : 0));
      if (i == cw) fl_issue<1>(gf, needW);
      if (i + 2 == n) {
        sset<S_K>(oK);
        if (last) {
          int l = threadIdx.x & 63; asm volatile("" : "+v"(l));
          const unsigned vw = (64 * wid + (l >> 3)) * (DA * 2) + 16 * ((l & 7) ^ ((l >> 3) & 6));
          static_for([&](auto Q) { vset<r_dk(Q)>(__uint_as_float(vw + (Q ^ 4) * (8 * DA * 2) - (Q & 3) * 1024)); }, make_iseq<8>{});
          set_srdk(wo + (size_t)h * HD * DA, HD * DA * 2);
          sset<S_K>(0); sset<S_I>(128);
        }
        if (isY && !solo)
          asm volatile("s_nop 4\n\tbuffer_load_dwordx4 v[122:125], %0, %1, %2 offen sc1" ::"v"(vml), "s"(c.sw), "s"(oP) : "memory");
      }
      if (i == ct) {
        CODE_TOUCH("tail_begin", "pf_code_end", r * 4 + wid, 64u, 8, m0f);
        {
          int l = threadIdx.x & 63; asm volatile("" : "+v"(l));
          const char* tp = (const char*)wo + (size_t)h * (HD * DA * 2) + ((pj * 4 + wid) * 56 + (l < 56 ? l : 0)) * 128;
          asm volatile("s_mov_b32 m0, %1\n\ts_nop 0\n\tglobal_load_lds_dword %0, off" ::"v"(tp), "s"(m0f) : "memory");
        }
        fl_check<1>(gf, needW, tag);
        post_dma<true>(j, r * 4 + wid, xqT + (size_t)DA * SEQ, aq, m0f);
      }
      if (last && i + 3 >= n && i + 1 < n && i) set_mask<R_CI, false>(c, (2 * qb + i + 3 - n) * 64);
      if (i == (isY ? 1 : 0)) set_flag(it != it0 ? fw(gf, 48 + hsel * 8 + pj) : pend2, tag, wid);
      asm volatile("s_nop 4");
      GAP();
    };
#define STA(k)
    STA(0);
    phaseA<0, true, false>(c);
    STA(1);
    event(0);
    STA(2);
    phaseB0(c);
    STA(3);
#pragma unroll 1
    for (int i = 1;; i += 2) {
      phaseA<1, false, false>(c);
      STA(9 + i);
      if (__builtin_expect(c.rare != 0, 0)) { if (c.redo) sm_redo<1>(); event(i); }
      phaseB<1>(c);
      if (i + 1 >= n) break;
      phaseA<0, false, false>(c);
      STA(10 + i);
      if (__builtin_expect(c.rare != 0, 0)) { if (c.redo) sm_redo<0>(); event(i + 1); }
      phaseB<0>(c);
    }
    STA(4);
    asm volatile("tail_begin:");
    c.rare = __builtin_amdgcn_readfirstlane(!last ? oQ + (pj * 128 + wid * 32) * 512 : oP + wid * 16384);
    c.vq = last && (!isY || solo) ? 0x80000000u : c.ka;
    asm volatile("s_nop 4" : "+s"(c.rare));
    phaseA<0, false, true>(c);
    if (__builtin_expect(c.redo != 0, 0)) sm_redo<0>();
    STA(5);
    KST(0, 9 + 3 * it);
    const float l[2] = {row_reduce<false>(vget<R_RS>() + vget<R_TS>()), row_reduce<false>(vget<R_RS + 1>() + vget<R_TS + 1>())};
    const float cm[2] = {vget<R_M>(), vget<R_M + 1>()};
    EP p;
    p.vo = c.dw[0] + wid * 8192;
#pragma unroll
    for (int e = 0; e < 4; ++e) p.so[e] = oP + e * 4096;
    {
      int lz = threadIdx.x & 63; asm volatile("" : "+v"(lz));
      const int rw = lz >> 2;
      static_for([&](auto E) { vset<R_TB + E>(__uint_as_float(lbase + rw * 128 + 16 * ((2 * E + ((lz & 3) >> 1)) ^ (rw & 6)) + 8 * (lz & 1))); }, make_iseq<4>{});
      const unsigned vs = ((lz >> 3) & 1) * 16384 + ((lz >> 4) & 1) * 512 + (lz >> 5) * 256 + (lz & 7) * 16;
      vset<R_VS>(__uint_as_float(vs)); vset<R_VS + 1>(__uint_as_float(vs + 32768));
      sset<96>(oS + qbF * 0x200000 + wid * 65536 + (j * 2 + hsel) * 1024); sset<97>(c.m0x[1]); sset<100>(0x8000);
    }
    if (!isY || solo) {
      p.fa[0] = __builtin_amdgcn_rcpf(l[0]); p.fa[1] = __builtin_amdgcn_rcpf(l[1]);
      if (!last) {
        __builtin_amdgcn_raw_buffer_store_b128(__builtin_bit_cast(u32x4, (f32x4){cm[0], l[0], cm[1], l[1]}), c.sw, vml, oP, 16);
        phaseF<0>(c, p, make_iseq<74>{});
      } else phaseF<1>(c, p, make_iseq<74>{});
    } else {
      const f32x4 ml = {vget<122>(), vget<123>(), vget<124>(), vget<125>()};
#pragma unroll
      for (int e = 0; e < 2; ++e) {
        const float mm = fmaxf(cm[e], ml[2 * e]);
        const float wa = __builtin_amdgcn_exp2f(cm[e] - mm), wb = ml[2 * e + 1] * __builtin_amdgcn_exp2f(ml[2 * e] - mm);
        const float inv = __builtin_amdgcn_rcpf(l[e] * wa + wb);
        p.fa[e] = wa * inv; p.fb[e] = wb * inv;
      }
      phaseF<2>(c, p, make_iseq<74>{});
    }
    KST(0, 10 + 3 * it);
  }
  const unsigned rdy = lbase + 163836;
  asm volatile("ds_write_b32 %0, %1" ::"v"(rdy), "v"(0) : "memory");
  {
    asm volatile("v_xor_b32 v%c1, s100, v%c1\n\ts_movk_i32 s99, 0x1000\np_loop:" : "+s"(c.sw) : "i"(R_TB) : "scc");
    GAP();
    p_half<0>(c, make_iseq<32>{});
    p_half<1>(c, make_iseq<32>{});
    asm volatile("s_cbranch_scc1 p_loop\n\ts_nop 5" ::"s"(c.sw));
    GAP();
    p_cvt<1, 0>(); p_cvt<1, 1>(); p_cvt<1, 2>(); p_cvt<1, 3>();
    p_store<1, 1>(c);
    GAP();
    KST(0, 27);
    u64* pf = fw(flags, 1024 + (qbF * 4 + wid) * 16 + j * 2 + hsel);
    asm volatile("s_waitcnt vmcnt(0)" : "+v"(pf) :: "memory");
    set_flag(pf, tag);
    KST(0, 30);
  }
#define HELP 1
  if (!isY) post_math(j, r * 4 + wid, out, lds + 131072 + wid * 8192);
  {
    int lz = threadIdx.x & 63; asm volatile("" : "+v"(lz));
    const int fr = lz & 15, fq = lz >> 4;
    {
      const int jj = 4 * (fr >> 3) + (fr & 3);
      const unsigned one = ((fq ^ (fr >> 2)) & 1) ? 0u : (jj & 1) ? 0x3f800000u : 0x3f80u;
      static_for([&](auto I) { vset<96 + I>(__uint_as_float((jj >> 1) == I ? one : 0u)); }, make_iseq<4>{});
    }
    const unsigned vo = (fq >> 1) * 1024 + (fq & 1) * 256 + fr * 16, vo0 = isY && wid >= 4 - HELP ? 0x80000000u : vo, vo2 = wid >= 4 - HELP ? vo : 0x80000000u, vo1 = wid < 3 ? vo + 0x100000 : 0x80000000u;
    const __amdgpu_buffer_rsrc_t so_ = __builtin_amdgcn_make_buffer_rsrc((void*)out, (short)0, SEQ * OUTW * 2, 0x00020000);
    const unsigned vw = (((h & 3) * 32 + (h >> 2) * 8 + (fr & 7)) * OUTW + DA + wid * 64 + (fr >> 3) * 32 + fq * 8) * 2;
    const u64* q = fw(flags, 1024 + ((fq & 1 ? 15 - pj : qbF) * 4 + (h & 3)) * 16) + fr;
    unsigned so[2];
#pragma unroll
    for (int u = 0; u < 2; ++u)
      so[u] = __builtin_amdgcn_readfirstlane(oS + (u ? 15 - pj : qbF) * 0x200000 + (1 + wid) * 0x40000 + (h & 3) * 65536 + (h >> 2) * 16384);
    auto fin0 = [&]() __attribute__((always_inline)) {
      if (wid < 3) r_fin<1>(vw, so_, qbF * 128 * OUTW * 2 + 512);
      if (!isY || wid < 4 - HELP) r_fin<0>(vw, so_, qbF * 128 * OUTW * 2);
      KST(0, 16);
    };
    unsigned pend = isY || wid < 4 - HELP ? 0x5555u : 0x55555555u, cnt = 0x400000;
    bool done0 = false;
    asm volatile("s_waitcnt lgkmcnt(0)" : "+s"(so[0]), "+s"(so[1]), "+s"(c.sw));
    KST(0, 28);
#define PSL 4
    while (true) {
      unsigned nw;
      if (wid == 3) {
        u64 m, v;
        const u64 ex = pend | (pend << 1);
        asm volatile("s_mov_b64 exec, %4\n\tglobal_load_dwordx2 %1, %2, off sc1\n\ts_waitcnt vmcnt(0)\n\tv_cmp_eq_u64 %0, %3, %1\n\ts_mov_b64 exec, -1"
                     : "=&s"(m), "=&v"(v) : "v"(q), "s"(tag), "s"(ex) : "memory");
        nw = (unsigned)m & ((unsigned)m >> 1) & pend;
        if (nw) asm volatile("ds_write_b32 %0, %1" ::"v"(rdy), "v"(~pend | nw) : "memory");
      } else {
        unsigned t;
        asm volatile("ds_read_b32 %0, %1\n\ts_waitcnt lgkmcnt(0)" : "=v"(t) : "v"(rdy) : "memory");
        nw = __builtin_amdgcn_readfirstlane(t) & pend;
      }
      if (--cnt == 0) nw = pend;
      if (nw == 0) {
        if (!done0 && !(pend & 0xffff)) {
          asm volatile("s_waitcnt vmcnt(0)" ::: "memory");
          fin0(); done0 = true;
        } else if (wid == 3) asm volatile("s_sleep %0" ::"i"(PSL));
        else asm volatile("s_sleep 1");
        continue;
      }
      asm volatile("s_nop 4");
      static_for([&](auto S) {
        constexpr int K = 2 * (S & 7);
        if ((nw >> (2 * S)) & 1) {
          if constexpr (S < 8) { r_load<0, K>(c, vo0, so[0]); r_load<0, K + 1>(c, vo0, so[0]); r_load<1, K>(c, vo1, so[0]); r_load<1, K + 1>(c, vo1, so[0]); }
          else { r_load<2, K>(c, vo2, so[1]); r_load<2, K + 1>(c, vo2, so[1]); }
        }
      }, make_iseq<16>{});
      pend &= ~nw;
      if (pend == 0) break;
    }
    asm volatile("s_waitcnt vmcnt(0)" ::: "memory");
    if (!done0) fin0();
    if (!isY && wid >= 4 - HELP) r_fin<2>(vw, so_, (15 - pj) * 128 * OUTW * 2);
    KST(0, 29);
    set_flag(fw(flags, 6144), tag, (b ^ 128) | wid);
  }
  asm volatile("s_waitcnt vmcnt(0)" ::: "memory");
  asm volatile("pf_code_end:");
  KST(0, 1);
}

extern "C" void kernel_launch(void* const* d_in, const int* in_sizes, int n_in, void* d_out, int out_size, void* d_ws,
                              size_t ws_size, hipStream_t stream) {
  const bf16* Aq = (const bf16*)d_in[0];
  const bf16* Ak = (const bf16*)d_in[1];
  const bf16* Av = (const bf16*)d_in[2];
  const bf16* Ash = (const bf16*)d_in[3];
  const bf16* Assmk = (const bf16*)d_in[4];
  const bf16* AssmD = (const bf16*)d_in[5];
  const bf16* Bx = (const bf16*)d_in[6];
  const bf16* Bckv = (const bf16*)d_in[7];
  const bf16* Bwq = (const bf16*)d_in[8];
  const bf16* Bwuk = (const bf16*)d_in[9];
  const bf16* Bwuv = (const bf16*)d_in[10];
  const bf16* Bwo = (const bf16*)d_in[11];
  bf16* out = (bf16*)d_out;
  hipFuncSetAttribute((const void*)fused_kernel, hipFuncAttributeMaxDynamicSharedMemorySize, 163840);
  fused_kernel<<<256, 256, 163840, stream>>>(Bckv, Bwuk, Bwuv, Bx, Bwq, (bf16*)d_ws, Ak, Av, Ash, Assmk, AssmD, Bwo, Aq, out);
}
```

```cpp
#include <hip/hip_runtime.h>
#include <hip/hip_bf16.h>
#include <stdint.h>
#include <utility>

typedef __hip_bfloat16 bf16;
typedef __attribute__((ext_vector_type(4))) short bf16x4;
typedef __attribute__((ext_vector_type(2))) unsigned u32x2;

constexpr int SEQ = 2048;
constexpr int DA = 448;
constexpr int NH = 16;
constexpr int HD = 256;
constexpr int DC = 1024;
constexpr int OUTW = 896;
#define HROW(h) ((size_t)(h) * SEQ)
constexpr int HSTEP = SEQ;

#define KST(kid, which)

__device__ __forceinline__ float bf2f(unsigned short u) { return __uint_as_float(((unsigned)u) << 16); }
__device__ __forceinline__ unsigned short f2bf(float f) { return __bfloat16_as_ushort(__float2bfloat16(f)); }
__device__ __forceinline__ float rbf(float f) { return bf2f(f2bf(f)); }
typedef __attribute__((ext_vector_type(2))) __bf16 bf16x2n;
typedef __attribute__((ext_vector_type(2))) float f32x2n;
__device__ __forceinline__ unsigned pk2(float a, float b) {
  return __builtin_bit_cast(unsigned, __builtin_convertvector((f32x2n){a, b}, bf16x2n));
}

__device__ __forceinline__ int off_b(int row, int ch) { return 256 * row + 16 * (ch ^ (((row & 3) << 2) | ((row >> 2) & 3))); }

template <int R> __device__ __forceinline__ void sset(unsigned x) { asm volatile("s_mov_b32 s%c0, %1" ::"i"(R), "s"(x)); }
template <int R> __device__ __forceinline__ float vget() { float t; asm volatile("v_mov_b32 %0, v%c1" : "=v"(t) : "i"(R)); return t; }
template <int R> __device__ __forceinline__ void vset(float x) { asm volatile("v_mov_b32 v%c0, %1" ::"i"(R), "v"(x)); }
template <int R> __device__ __forceinline__ float acc_read1() { float t; asm volatile("v_accvgpr_read_b32 %0, a[%c1]" : "=v"(t) : "i"(R)); return t; }
template <class F, int... I> __device__ __forceinline__ void static_for(F&& f, std::integer_sequence<int, I...>) {
  (f(std::integral_constant<int, I>{}), ...);
}
template <int... Is> using iseq = std::integer_sequence<int, Is...>;
template <int N> using make_iseq = std::make_integer_sequence<int, N>;

typedef __attribute__((ext_vector_type(4))) unsigned u32x4c;
typedef __attribute__((ext_vector_type(4))) float f32x4c;
typedef __attribute__((ext_vector_type(2))) float f32x2c;
typedef unsigned long long u64;
#define DMA16(M0V, VO, SRD, SO) asm volatile("s_mov_b32 m0, %0\n\ts_nop 0\n\tbuffer_load_dwordx4 %1, %2, %3 offen lds" ::"s"(M0V), "v"(VO), "s"(SRD), "s"(SO) : "memory")
struct PR { u32x4c kk[7], vv[4], w[4]; };
__device__ __forceinline__ void prep_load(PR& p, int tid, int j, int rr, const bf16* __restrict__ k, const bf16* __restrict__ v,
                                          const bf16* __restrict__ shift_k) {
  const __amdgpu_buffer_rsrc_t sk = __builtin_amdgcn_make_buffer_rsrc((void*)k, (short)0, SEQ * DA * 2, 0x00020000);
  const __amdgpu_buffer_rsrc_t sv = __builtin_amdgcn_make_buffer_rsrc((void*)v, (short)0, SEQ * DA * 2, 0x00020000);
  const __amdgpu_buffer_rsrc_t sw = __builtin_amdgcn_make_buffer_rsrc((void*)(shift_k + j * 224), (short)0, 448, 0x00020000);
  const int vo = ((rr * 128 + (tid >> 3) * 4) * DA + j * 56 + (tid & 7) * 8) * 2;
#pragma unroll
  for (int q = 0; q < 7; ++q)
    asm volatile("buffer_load_dwordx4 %0, %1, %2, 0 offen" : "=v"(p.kk[q]) : "v"(vo + (q - 3) * DA * 2), "s"(sk) : "memory");
#pragma unroll
  for (int q = 0; q < 4; ++q)
    asm volatile("buffer_load_dwordx4 %0, %1, %2, 0 offen offset:%3" : "=v"(p.vv[q]) : "v"(vo), "s"(sv), "i"(q * DA * 2) : "memory");
#pragma unroll
  for (int q = 0; q < 4; ++q)
    asm volatile("buffer_load_dwordx4 %0, %1, %2, 0 offen offset:%3" : "=v"(p.w[q]) : "v"((tid & 7) * 64), "s"(sw), "i"(q * 16) : "memory");
}
__device__ __forceinline__ float bfl(unsigned u) { return __uint_as_float(u << 16); }
__device__ __forceinline__ float bfh(unsigned u) { return __uint_as_float(u & 0xffff0000u); }
__device__ __forceinline__ f32x2c rbf2(f32x2c a) { const unsigned u = pk2(a[0], a[1]); return (f32x2c){bfl(u), bfh(u)}; }
template <int I> __device__ __forceinline__ void prep_math(const PR& p, int tid, char* lx) {
  const int rp = tid >> 3, c = tid & 7;
  f32x2c wt[4], kf[7];
#pragma unroll
  for (int t = 0; t < 4; ++t)
    wt[t] = (t & 1) ? (f32x2c){bfh(p.w[I][t >> 1]), bfh(p.w[I][2 + (t >> 1)])} : (f32x2c){bfl(p.w[I][t >> 1]), bfl(p.w[I][2 + (t >> 1)])};
#pragma unroll
  for (int q = 0; q < 7; ++q) kf[q] = (f32x2c){bfl(p.kk[q][I]), bfh(p.kk[q][I])};
  f32x2c x[4];
#pragma unroll
  for (int e = 0; e < 4; ++e) {
    f32x2c s = rbf2(kf[e] * wt[3]);
    s = rbf2(s + rbf2(kf[e + 1] * wt[2]));
    s = rbf2(s + rbf2(kf[e + 2] * wt[1]));
    s = rbf2(s + rbf2(kf[e + 3] * wt[0]));
    x[e] = s * (f32x2c){bfl(p.vv[e][I]), bfh(p.vv[e][I])};
  }
#pragma unroll
  for (int u = 0; u < 2; ++u)
    *(u32x2*)(lx + (c * 8 + 2 * I + u) * 256 + (((31 - rp) * 8) ^ (c << 4))) = (u32x2){pk2(x[3][u], x[2][u]), pk2(x[1][u], x[0][u])};
}
__device__ __forceinline__ void prep_store(int tid, int j, int rr, bf16* __restrict__ xr, const char* lx) {
  const __amdgpu_buffer_rsrc_t sx = __builtin_amdgcn_make_buffer_rsrc((void*)(xr + (size_t)j * 56 * SEQ), (short)0, 56 * SEQ * 2, 0x00020000);
#pragma unroll
  for (int rd = 0; rd < 4; ++rd) {
    const int idx = tid + 256 * rd, ch = idx >> 4, q = idx & 15;
    u32x4c w = *(const u32x4c*)(lx + ch * 256 + 16 * (q ^ (ch >> 3)));
    __builtin_amdgcn_raw_buffer_store_b128(w, sx, (ch * SEQ + (15 - rr) * 128 + q * 8) * 2, 0, 16);
  }
}

#define CODE_TOUCH(LB, LE, wx, NW, NL, m0v) do { unsigned blo_, bhi_, elo_; \
    asm volatile("s_getpc_b64 s[40:41]\n\ts_add_u32 s40, s40, " LB "@rel32@lo+4\n\ts_addc_u32 s41, s41, " LB "@rel32@hi+12\n\t" \
                 "s_mov_b32 %0, s40\n\ts_mov_b32 %1, s41\n\t" \
                 "s_getpc_b64 s[40:41]\n\ts_add_u32 s40, s40, " LE "@rel32@lo+4\n\ts_mov_b32 %2, s40" \
                 : "=s"(blo_), "=s"(bhi_), "=s"(elo_) :: "s40", "s41", "scc"); \
    const unsigned n_ = elo_ - (blo_ & ~127u); \
    const __amdgpu_buffer_rsrc_t srd_ = __builtin_amdgcn_make_buffer_rsrc((void*)(((unsigned long long)bhi_ << 32) | (blo_ & ~127u)), (short)0, n_ < 262144u ? n_ : 0u, 0x00020000); \
    const unsigned l_ = threadIdx.x & 63, vo_ = l_ < (NL) ? ((wx) + l_ * (NW)) * 128u : 0x80000000u; \
    asm volatile("s_mov_b32 m0, %0\n\ts_nop 4\n\tbuffer_load_dword %1, %2, 0 offen lds" ::"s"(m0v), "v"(vo_), "s"(srd_) : "memory"); } while (0)
template <int I> __device__ __forceinline__ void d_store() {
  asm volatile("buffer_store_dwordx4 v[%c0:%c1], v%c2, s[92:95], s101 offen offset:%3 sc1" ::"i"(128 + 4 * I), "i"(131 + 4 * I), "i"(112 + ((I >> 2) & 1)),
               "i"((I & 3) * 1024) : "memory");
  if constexpr ((I & 7) == 7) asm volatile("s_addk_i32 s101, 0x4000" ::: "scc");
}
__device__ __forceinline__ void d_none() { asm volatile("v_bfrev_b32 v112, 1\n\tv_bfrev_b32 v113, 1\n\ts_mov_b32 s101, 0"); }
struct CV { unsigned pa[4], pb, qv; };
constexpr int cv_J(int q) { int J = 7; while (q > J) { q -= J + 1; --J; } return J; }
constexpr int cv_i(int q) { int J = 7; while (q > J) { q -= J + 1; --J; } return q; }
template <int R, int N> __device__ __forceinline__ void cv_rd(CV& c) {
  if constexpr (N < 16) {
    constexpr int i = N >> 1, o = 16 * R + 128 * (i & 1) + 2 * (N & 1), r = 32 + 32 * (R & 1) + 2 * N;
    asm volatile("ds_read2_b32 a[%c1:%c2], %0 offset0:%c3 offset1:%c4" ::"v"(c.pa[i >> 1]), "i"(r), "i"(r + 1), "i"(o), "i"(o + 1));
  } else {
    constexpr int m = N - 16, r = 192 + 32 * (R & 1) + 4 * m;
    asm volatile("ds_read_b128 a[%c1:%c2], %0 offset:%c3" ::"v"(c.pb), "i"(r), "i"(r + 3), "i"(64 * R + 512 * (7 - m)));
  }
}
template <int R, int Q, int MODE> __device__ __forceinline__ void cv_slot(CV& c) {
  constexpr int i = cv_i(Q), J = cv_J(Q), d = 4 * J, a = 32 + 32 * (R & 1) + 4 * i, b = 192 + 32 * (R & 1) + 4 * (J - i);
  if constexpr (Q == 0) asm volatile("s_waitcnt lgkmcnt(0)");
  if constexpr (MODE == 1 && R == 0 && i == 0)
    asm volatile("v_mfma_f32_16x16x32_bf16 a[%c0:%c1], a[%c2:%c3], a[%c4:%c5], 0" ::"i"(d), "i"(d + 3), "i"(a), "i"(a + 3), "i"(b), "i"(b + 3));
  else
    asm volatile("v_mfma_f32_16x16x32_bf16 a[%c0:%c1], a[%c2:%c3], a[%c4:%c5], a[%c0:%c1]" ::"i"(d), "i"(d + 3), "i"(a), "i"(a + 3), "i"(b), "i"(b + 3));
  if constexpr (Q < 24 && !(MODE == 2 && R == 1)) cv_rd<R + 1, Q>(c);
  if constexpr (MODE == 1 && Q % 7 == 3) d_store<22 + 5 * R + Q / 7>();
  if constexpr (MODE == 2 && (R == 0 ? Q >= 24 && Q % 3 == 0 : Q % 3 == 0)) {
    constexpr int S_ = R == 0 ? (Q - 24) / 3 : 4 + Q / 3;
    asm volatile("buffer_load_dwordx4 a[%c0:%c1], %2, s[92:95], s97 offen offset:%3 sc1" ::"i"(128 + 4 * S_), "i"(128 + 4 * S_ + 3), "v"(c.qv),
                 "i"((S_ & 3) * 1024) : "memory");
    if constexpr ((S_ & 3) == 3) asm volatile("s_addk_i32 s97, 4096" ::: "scc");
  }
  __builtin_amdgcn_sched_barrier(0);
}
template <int R, int MODE, int... Q> __device__ __forceinline__ void cv_step(CV& c, iseq<Q...>) { (cv_slot<R, Q, MODE>(c), ...); }
template <int... N> __device__ __forceinline__ void cv_rd0(CV& c, iseq<N...>) { (cv_rd<0, N>(c), ...); }
template <class HOOK>
__device__ __forceinline__ void conv_wave(int d, float Dd, bf16* __restrict__ zT, char* lw, HOOK&& hook) {
  int lane = threadIdx.x & 63; asm volatile("" : "+v"(lane));
  const int l16 = lane & 15, fq = lane >> 4;
  u32x4c w[5]; unsigned w4[5];
#pragma unroll
  for (int r = 0; r < 5; ++r) {
    w[r] = *(const u32x4c*)(lw + r * 1024 + lane * 16);
    w4[r] = *(const unsigned*)(lw + r * 1024 + lane * 16 + 16);
  }
  d_store<16>(); d_store<17>();
#pragma unroll
  for (int r = 0; r < 5; ++r)
    *(u32x4c*)(lw + 10304 + r * 1024 + lane * 16) = (u32x4c){__builtin_amdgcn_alignbit(w[r][1], w[r][0], 16), __builtin_amdgcn_alignbit(w[r][2], w[r][1], 16),
                                                             __builtin_amdgcn_alignbit(w[r][3], w[r][2], 16), __builtin_amdgcn_alignbit(w4[r], w[r][3], 16)};
  d_store<18>(); d_store<19>();
  CV c;
#pragma unroll
  for (int i = 0; i < 4; ++i) c.pa[i] = (unsigned)(size_t)lw + ((l16 + 1) & 1) * 10304 + 4 * ((8 * fq + l16 + 1) >> 1) + 1024 * i;
  c.pb = (unsigned)(size_t)lw + 5120 + (2032 - 16 * l16 + 8 * fq) * 2 - 512 * 7;
  asm volatile("" ::: "memory");
  cv_rd0(c, make_iseq<24>{});
  d_store<20>(); d_store<21>();
  __builtin_amdgcn_sched_barrier(0);
  cv_step<0, 1>(c, make_iseq<36>{});
  cv_step<1, 1>(c, make_iseq<36>{});
#pragma unroll 1
  for (int r = 1; r < 3; ++r) {
#pragma unroll
    for (int i = 0; i < 4; ++i) c.pa[i] += 128;
    c.pb += 128;
    __builtin_amdgcn_sched_barrier(0);
    cv_step<0, 0>(c, make_iseq<36>{});
    cv_step<1, 0>(c, make_iseq<36>{});
    hook(r);
  }
#pragma unroll
  for (int i = 0; i < 4; ++i) c.pa[i] += 128;
  c.pb += 128;
  c.qv = lane * 16;
  __builtin_amdgcn_sched_barrier(0);
  cv_step<0, 2>(c, make_iseq<36>{});
  cv_step<1, 2>(c, make_iseq<36>{});
  const __amdgpu_buffer_rsrc_t srdZ = __builtin_amdgcn_make_buffer_rsrc((void*)(zT + (size_t)d * SEQ), (short)0, SEQ * 2, 0x00020000);
  asm volatile("s_nop 7");
  static_for([&](auto Jc) __attribute__((always_inline)) {
    constexpr int J = Jc;
    hook(4 + J);
    __builtin_amdgcn_sched_barrier(0);
    const int t = 256 * J + 16 * l16 + 4 * fq;
    bf16x4 xv = *(const bf16x4*)(lw + 5120 + (2044 - t) * 2);
    const float y[4] = {acc_read1<4 * J>(), acc_read1<4 * J + 1>(), acc_read1<4 * J + 2>(), acc_read1<4 * J + 3>()};
    float z[4];
#pragma unroll
    for (int e = 0; e < 4; ++e) z[e] = rbf(y[e]) + rbf(bf2f(xv[3 - e]) * Dd);
    u32x2 w = {pk2(z[0], z[1]), pk2(z[2], z[3])};
    __builtin_amdgcn_raw_buffer_store_b64(w, srdZ, t * 2, 0, 16);
  }, make_iseq<8>{});
}

template <bool Z> __device__ __forceinline__ void post_dma(int j, int tb, const bf16* __restrict__ zT, const bf16* __restrict__ q, unsigned m0w) {
  int lane = threadIdx.x & 63; asm volatile("" : "+v"(lane));
  const __amdgpu_buffer_rsrc_t sz = __builtin_amdgcn_make_buffer_rsrc((void*)(zT + (size_t)j * 56 * SEQ + tb * 32), (short)0, 56 * SEQ * 2, 0x00020000);
  const __amdgpu_buffer_rsrc_t sq = __builtin_amdgcn_make_buffer_rsrc((void*)(q + (size_t)tb * 32 * DA + j * 56), (short)0, 32 * DA * 2, 0x00020000);
  const int tl = lane / 7, ck = lane - tl * 7;
  const unsigned vz = lane < 56 ? (ck * 8 + tl) * SEQ * 2 : 0x80000000u, vq = lane < 56 ? (tl * DA + ck * 8) * 2 : 0x80000000u;
#pragma unroll
  for (int i = 0; i < 4; ++i) if constexpr (Z)
    asm volatile("s_mov_b32 m0, %0\n\ts_nop 4\n\tbuffer_load_dwordx4 %1, %2, %3 offen sc1 lds" ::"s"(m0w + i * 1024), "v"(vz), "s"(sz), "s"(i * 16) : "memory");
#pragma unroll
  for (int i = 0; i < 4; ++i) if constexpr (!Z)
    asm volatile("s_mov_b32 m0, %0\n\ts_nop 4\n\tbuffer_load_dwordx4 %1, %2, %3 offen lds" ::"s"(m0w + 4096 + i * 1024), "v"(vq), "s"(sq), "s"(i * 8 * DA * 2) : "memory");
}
__device__ __forceinline__ void post_math(int j, int tb, bf16* __restrict__ out, const char* lw) {
  int lane = threadIdx.x & 63; asm volatile("" : "+v"(lane));
  const int tl = lane / 7, ck = lane - tl * 7;
  const __amdgpu_buffer_rsrc_t so = __builtin_amdgcn_make_buffer_rsrc((void*)(out + (size_t)tb * 32 * OUTW + j * 56), (short)0, 32 * OUTW * 2 - 2 * DA, 0x00020000);
  const unsigned vo = lane < 56 ? (tl * OUTW + ck * 8) * 2 : 0x80000000u;
  const char* lz = lw + ck * 16 + (tl & 7) * 2;
#pragma unroll
  for (int i = 0; i < 4; ++i) {
    const u32x4c qv = *(const u32x4c*)(lw + 4096 + i * 1024 + lane * 16);
    u32x4c o;
#pragma unroll
    for (int e = 0; e < 4; ++e) {
      const float z0 = bf2f(*(const unsigned short*)(lz + i * 1024 + 2 * e * 112));
      const float z1 = bf2f(*(const unsigned short*)(lz + i * 1024 + (2 * e + 1) * 112));
      o[e] = pk2(bfl(qv[e]) * z0, bfh(qv[e]) * z1);
    }
    __builtin_amdgcn_raw_buffer_store_b128(o, so, vo, i * 8 * OUTW * 2, 0);
  }
}

#define DI __device__ __forceinline__
#define GAP() __builtin_amdgcn_sched_barrier(0)
typedef __attribute__((ext_vector_type(4))) float f32x4g;
typedef __attribute__((ext_vector_type(4))) unsigned u32x4g;
constexpr int LDW = NH * HD;
struct GM {
  unsigned aA[2], aB[2][8];
  unsigned voA, voB, vbA, vbB, kA, kW, kB;
  u64 tab;
  __amdgpu_buffer_rsrc_t srdA, srdB;
  unsigned sA[8], mA, mB;
};
template <int P, int KS, int I> DI void g_wread(GM& g) {
  constexpr int nf = I >> 1, hf = I & 1, r = 192 - 64 * P + 8 * nf + 4 * KS + 2 * hf;
  asm volatile("ds_read_b64_tr_b16 a[%c1:%c2], %0 offset:%c3" ::"v"(g.aB[hf][nf]), "i"(r), "i"(r + 1), "i"(P * 32768 + KS * 16384));
}
template <int P, int M, int KS> DI void g_aread(GM& g) {
  constexpr int r = 96 + 8 * (M & 1) + 4 * KS;
  asm volatile("ds_read_b128 v[%c1:%c2], %0 offset:%c3" ::"v"(g.aA[KS]), "i"(r), "i"(r + 3), "i"(P * 32768 + M * 2048));
}
constexpr float CS = 0.0625f * 1.4426950408889634f;
struct GE {
  __amdgpu_buffer_rsrc_t sk, sx;
  unsigned vl, vl0, vq, vk[2], mC, mK;
};
#define SG0 36
#define SGS 6
struct ES { int op0[129], nop[129]; };
constexpr ES e_sched(bool TR, bool SCL) {
  ES z{};
  const int per = SCL ? 16 : 8;
  int o = 0;
  for (int G = 0; G <= 128; ++G) {
    int cap = G == 128 ? 10000 : SCL ? 4 : 2;
    z.op0[G] = o;
    while (o < 32 * per) {
      const int i = o / per, rdy = (TR ? 32 * (i >> 3) + 16 + 2 * (i & 7) + 1 : 4 * i + 3) + 4;
      if ((G < rdy && G < 128) || cap-- <= 0) break;
      ++o; ++z.nop[G];
    }
  }
  return z;
}
template <bool TR, bool SCL> constexpr ES ESCH = e_sched(TR, SCL);
constexpr int e_st(int MODE, int G) {
  const int g0 = MODE == 4 ? 5 : MODE == 2 ? 13 : SG0, gs = MODE == 4 ? 6 : MODE == 2 ? 7 : SGS;
  return MODE >= 2 && G >= g0 && (G - g0) % gs == 0 && (G - g0) / gs < 16 ? (G - g0) / gs + (MODE == 4 ? 16 : 0) : -1;
}
constexpr int e_nst(int MODE, int G0, int G1) { int n = 0; for (int G = G0; G < G1; ++G) n += e_st(MODE, G) >= 0; return n; }
static_assert(e_nst(2, 0, 128) == 16 && e_nst(3, 0, 128) == 16 && e_nst(4, 0, 104) == 16);
template <bool TR, bool SCL, int O> DI void e_op() {
  constexpr int per = SCL ? 16 : 8, i = O / per, k = O % per, r = 128 + 4 * i, t = 114 + 4 * (i & 1), c = k - (per - 4);
  if constexpr (k < 4) asm volatile("v_accvgpr_read_b32 v%c0, a[%c1]" ::"i"(t + k), "i"(4 * i + k));
  else if constexpr (c < 0) asm volatile("v_mul_f32 v%c0, %1, v%c0" ::"i"(k < 8 ? r + (k ^ 6) : t + k - 8), "s"(CS));
  else {
    constexpr int d = c == 0 ? r + 3 : c == 1 ? r + 2 : r + c - 2, x = c == 0 ? r + 2 : c == 1 ? r : t + 2 * (c - 2);
    asm volatile("v_cvt_pk_bf16_f32 v%c0, v%c1, v%c2" ::"i"(d), "i"(x), "i"(x + 1));
  }
}
template <bool TR, bool SCL, int B, int... Is> DI void e_ops(iseq<Is...>) { (e_op<TR, SCL, B + Is>(), ...); }
#define LGS 2
constexpr int e_slot(int G) { return G < 10 ? G : (G - 10) % LGS == 1 && (G - 10) / LGS < 32 ? 10 + (G - 10) / LGS : -1; }
constexpr int E_CONV = 9, E_K0 = 11 + 23 * LGS;
static_assert(e_slot(E_K0) == 33);
template <int S> DI void e_m0(const GE& e) {
  if constexpr (S == 0 || S == 1 || S == 5 || S == 9) asm volatile("s_add_u32 m0, %0, %1" ::"s"(e.mC), "i"(S * 1024));
  if constexpr (S >= 26 && (S - 26) % 4 == 0) asm volatile("s_add_u32 m0, %0, %1" ::"s"(e.mK), "i"(((S - 26) >> 3) * 32768 + ((S - 26) & 4) * 1024));
}
template <int S> DI void e_load(const GE& e) {
  if constexpr (S == 0) asm volatile("buffer_load_dwordx4 %0, %1, 0 offen lds" ::"v"(e.vl0), "s"(e.sk) : "memory");
  else if constexpr (S < 5) asm volatile("buffer_load_dwordx4 %0, %1, %2 offen offset:%3 lds" ::"v"(e.vl), "s"(e.sk), "s"(1024 - 32), "i"((S - 1) * 1024) : "memory");
  else if constexpr (S < 9) asm volatile("buffer_load_dwordx4 %0, %1, 0 offen offset:%2 sc1 lds" ::"v"(e.vl), "s"(e.sx), "i"((S - 5) * 1024) : "memory");
  else if constexpr (S == 9) asm volatile("buffer_load_dwordx4 %0, %1, %2 offen sc1 lds" ::"v"(e.vl), "s"(e.sx), "s"(4096) : "memory");
  else if constexpr (S < 26) {
    constexpr int f = S - 10;
    asm volatile("buffer_load_dwordx4 a[%c0:%c1], %2, s[92:95], s96 offen offset:%3 sc1" ::"i"(128 + 4 * f), "i"(131 + 4 * f), "v"(e.vq),
                 "i"((f & 3) * 1024) : "memory");
    if constexpr ((f & 3) == 3) asm volatile("s_addk_i32 s96, 4096");
  } else {
    constexpr int f = S - 26;
    asm volatile("buffer_load_dwordx4 %0, s[92:95], s98 offen offset:%1 sc1 lds" ::"v"(e.vk[(f >> 2) & 1]), "i"((f & 3) * 1024) : "memory");
    if constexpr (f == 7) asm volatile("s_add_u32 s98, s98, 0x8000");
  }
}
constexpr int e_younger(int G0) {
  int n = e_nst(3, G0 + 1, 128);
  for (int G = G0 + 1; G < 128; ++G) n += e_slot(G) >= 0;
  return n;
}
constexpr int W_CONV = e_younger(E_CONV), W_K0 = e_younger(E_K0);
template <bool TR, int P, int H, int G, int MODE> DI void g_gap(GM& g, const GE& e) {
  constexpr int m = 4 * H + (G >> 4), q = G & 15, n = q >> 1, ks = q & 1, GG = 64 * H + G;
  constexpr bool inV = TR ? m & 1 : n & 1;
  constexpr int a = 96 + 8 * (m & 1) + 4 * ks, b = 192 - 64 * P + 8 * n + 4 * ks, d = (inV ? 128 : 0) + 4 * (TR ? 8 * (m >> 1) + n : 4 * m + (n >> 1));
  constexpr int J = MODE == 3 ? -1 : q == 8 ? 2 * (G >> 4) : q == 12 ? 2 * (G >> 4) + 1 : -1, gj = (J & 3) ^ ((J & 3) >> 1);
  if constexpr (MODE == 3) { if constexpr (q == 0 && GG > 0) asm volatile("s_waitcnt lgkmcnt(0)"); e_m0<e_slot(GG)>(e); }
  else if constexpr (q == 0 && G > 0) asm volatile("s_waitcnt lgkmcnt(4)");
  if constexpr (J == 0) asm volatile("s_add_u32 m0, %0, %1" ::"s"(g.mA), "i"((P ^ H ^ 1) * 32768 + (1 - H) * 8192) : "scc");
  else if constexpr (J == 4) asm volatile("s_add_u32 m0, %0, %1" ::"s"(g.mB), "i"((P ^ H) * 32768 + (1 - H) * 16384) : "scc");
  else if constexpr (J > 0 && (J & 3) == 1) asm volatile("s_bitset1_b32 m0, 12");
  else if constexpr (J > 0 && (J & 3) == 2) asm volatile("s_bitset1_b32 m0, %0" ::"i"(J < 4 ? 14 : 13));
  else if constexpr (J > 0) asm volatile("s_bitset0_b32 m0, 12");
#define GMF(D, X, Y, C) asm volatile("v_mfma_f32_16x16x32_bf16 " D "[%c0:%c1], " X ", " Y ", " C ::"i"(d), "i"(d + 3), "i"(a), "i"(a + 3), "i"(b), "i"(b + 3))
#define GMF2(D, C) do { if constexpr (TR) GMF(D, "v[%c2:%c3]", "a[%c4:%c5]", C); else GMF(D, "a[%c4:%c5]", "v[%c2:%c3]", C); } while (0)
  if constexpr ((MODE == 1 || MODE == 4) && ks == 0) { if constexpr (inV) GMF2("v", "0"); else GMF2("a", "0"); }
  else if constexpr (inV) GMF2("v", "v[%c0:%c1]");
  else GMF2("a", "a[%c0:%c1]");
#undef GMF2
#undef GMF
  if constexpr (e_st(MODE, GG) >= 0) {
    static_assert(MODE == 4 || ESCH<TR, MODE == 2>.op0[GG] >= (e_st(MODE, GG) + 1) * (MODE == 2 ? 16 : 8));
    d_store<e_st(MODE, GG)>();
  }
  if constexpr (MODE == 2 || MODE == 3) e_ops<TR, MODE == 2, ESCH<TR, MODE == 2>.op0[GG]>(make_iseq<ESCH<TR, MODE == 2>.nop[GG]>{});
#define GD(VO, SRD, SO) asm volatile("buffer_load_dwordx4 %0, %1, %2 offen lds" ::"v"(VO), "s"(SRD), "s"(SO) : "memory")
  if constexpr (J >= 0 && J < 4) GD(g.voA, g.srdA, g.sA[(1 - H) * 2 + (gj & 1) + (gj >> 1) * 4]);
  else if constexpr (J >= 4) GD(g.voB, g.srdB, ((1 - H) * 32 + 16 * (gj & 1)) * LDW * 2 + (gj >> 1) * 256);
#undef GD
  if constexpr ((q == 1 || q == 2) && !(MODE == 3 && m == 7)) g_aread<(m < 7 ? P : P ^ 1), (m + 1) & 7, q - 1>(g);
  if constexpr (MODE == 3) {
    if constexpr (e_slot(GG) >= 0) e_load<e_slot(GG)>(e);
  } else {
    if constexpr (q >= 3 && q < 11 && (q & 1)) g_wread<P ^ 1, H, (G >> 4) * 4 + (q - 3) / 2>(g);
    if constexpr (H == 1 && G == 0) asm volatile("s_mov_b32 %0, %1" : "=s"(g.kA) : "s"(g.kW));
    if constexpr (H == 1 && G == 4) asm volatile("v_lshl_add_u32 %0, %2, 7, %3\n\ts_bfe_u32 %1, %4, 0x40000" : "=v"(g.voA), "=s"(g.kW) : "s"(g.kA), "v"(g.vbA), "s"((unsigned)g.tab) : "scc");
    if constexpr (H == 1 && G == 6) asm volatile("s_lshr_b64 %0, %0, 4\n\ts_lshl_b32 %1, %2, 19" : "+s"(g.tab), "=&s"(g.kB) : "s"(g.kW) : "scc");
    if constexpr (H == 1 && G == 10) asm volatile("v_add_u32 %0, %1, %2" : "=v"(g.voB) : "s"(g.kB), "v"(g.vbB));
    constexpr int nst = e_nst(MODE, H ? 29 : 0, GG + 1);
    if constexpr (G == 61) asm volatile("s_waitcnt vmcnt(%0) lgkmcnt(0)\n\ts_barrier" ::"i"(12 + nst) : "memory");
  }
  if constexpr ((MODE == 2 || MODE == 3) && GG == 127) {
    asm volatile("s_nop 7");
    e_ops<TR, MODE == 2, ESCH<TR, MODE == 2>.op0[128]>(make_iseq<ESCH<TR, MODE == 2>.nop[128]>{});
  }
  GAP();
}
template <bool TR, int P, int H, int MODE, int... Gs> DI void g_half_(GM& g, const GE& e, iseq<Gs...>) { (g_gap<TR, P, H, Gs, MODE>(g, e), ...); }
template <bool TR, int P, int H, int MODE> DI void g_half(GM& g, const GE& e) { g_half_<TR, P, H, MODE>(g, e, make_iseq<64>{}); }
template <int P, int... Is> DI void g_read0(GM& g, iseq<Is...>) {
  (g_wread<P, 0, Is>(g), ...); (g_wread<P, 1, Is>(g), ...);
  g_aread<P, 0, 0>(g); g_aread<P, 0, 1>(g);
}
struct GItem { const bf16* A; const bf16* W; int lda; u64 tab; };
DI void g_srcA(GM& g, const GItem& it, int wid, int lane) {
  g.srdA = __builtin_amdgcn_make_buffer_rsrc((void*)it.A, (short)0, it.A ? 256 * it.lda * 2 : 0, 0x00020000);
  g.vbA = ((wid * 8 + (lane >> 3)) * it.lda + ((lane & 7) ^ (((wid & 1) * 4 + (lane >> 4)) & 7)) * 8) * 2;
#pragma unroll
  for (int j = 0; j < 8; ++j) g.sA[j] = j * 64 * it.lda;
  if (it.A == nullptr) g.mA = 0x28000;
}
DI void g_srcB(GM& g, const GItem& it) {
  g.srdB = __builtin_amdgcn_make_buffer_rsrc((void*)it.W, (short)0, it.A ? (it.lda - 1) * LDW * 2 + 512 : 0, 0x00020000);
  g.tab = it.tab;
  if (it.A == nullptr) g.mB = 0x28000;
}
DI void g_fix(GM& g) {
  g.tab = ((u64)(unsigned)__builtin_amdgcn_readfirstlane((unsigned)(g.tab >> 32)) << 32) | (unsigned)__builtin_amdgcn_readfirstlane((unsigned)g.tab);
  g.mA = __builtin_amdgcn_readfirstlane(g.mA); g.mB = __builtin_amdgcn_readfirstlane(g.mB); g.kW = __builtin_amdgcn_readfirstlane(g.kW);
#pragma unroll
  for (int j = 1; j < 8; ++j) g.sA[j] = __builtin_amdgcn_readfirstlane(g.sA[j]);
}
template <int I> DI void g_pro1(GM& g, unsigned po) {
  constexpr int grp = I >> 2, i = I & 3, t = grp < 4 ? 0 : grp < 7 ? 1 : 2;
  constexpr bool isA = grp == 0 || grp == 3 || grp == 5;
  const unsigned k = ((unsigned)g.tab >> (4 * t)) & 15, so = t == 1 ? po ^ 32768 : po;
  if constexpr (isA) {
    constexpr int jj = (grp == 3 ? 2 : 0) + (i & 1) + (i >> 1) * 4;
    DMA16(g.mA + so + jj * 4096, g.vbA, g.srdA, g.sA[jj] + k * 128);
  } else {
    constexpr int ks = grp == 2 || grp == 6;
    DMA16(g.mB + so + ks * 16384 + (i >> 1) * 8192 + (i & 1) * 4096, g.vbB, g.srdB, k * 0x80000 + (ks * 32 + 16 * (i & 1)) * LDW * 2 + (i >> 1) * 256);
  }
}
template <int B, int... Is> DI void g_pro(GM& g, unsigned po, iseq<Is...>) { (g_pro1<B + Is>(g, po), ...); }

typedef __attribute__((ext_vector_type(4))) unsigned u32x4;
typedef __attribute__((ext_vector_type(4))) float f32x4;

constexpr int R_S = 128, R_P = 192, R_CI = 224, R_E = 96;
constexpr int R_TS = 112, R_RS = 114, R_M = 116;
constexpr int R_PD = 118, R_THR = 120, R_TMP = 121;
constexpr int r_dk(int q) { return q < 6 ? 90 + q : 120 + q; }
constexpr int S_V = 96, S_T = 97, S_K = 98, S_I = 99, S_EV = 100;
DI void set_srdk(const void* p, unsigned n) {
  asm volatile("s_mov_b64 s[92:93], %0\n\ts_mov_b32 s94, %1\n\ts_mov_b32 s95, 0x20000" ::"s"(p), "s"(n));
}
struct AC {
  unsigned ka, va, dw[2], vq;
  __amdgpu_buffer_rsrc_t sw;
  unsigned m0x[2];
  u64 redo;
  unsigned rare;
  int mrow;
  bool diag0;
};
template <int X, int F> DI void kread(AC& c) {
  constexpr int r = 192 + 4 * (F & 15);
  asm volatile("ds_read_b128 a[%c1:%c2], %0 offset:%c3" ::"v"(c.ka), "i"(r), "i"(r + 3), "i"(X * 32768 + F * 1024));
}
template <int X, int F> DI void vread(AC& c) {
  constexpr int r = 192 + 4 * (F & 15);
  asm volatile("ds_read_b128 a[%c1:%c2], %0 offset:%c3" ::"v"(c.va), "i"(r), "i"(r + 3), "i"(X * 32768 + (F & 1) * 16384 + (F >> 1) * 1024));
}
template <int X, int... Fs> DI void kreads(AC& c, iseq<Fs...>) { (kread<X, Fs>(c), ...); }
template <int X, int J> DI void dma_m0(AC& c) {
  if constexpr (J == 0) asm volatile("s_mov_b32 m0, %0" ::"s"(c.m0x[X]));
  else if constexpr (J == 4) asm volatile("s_or_b32 m0, %0, 0x1000" ::"s"(c.m0x[X]) : "scc");
  else if constexpr (J == 8) asm volatile("s_bitset0_b32 m0, 16");
  else if constexpr (J == 12) asm volatile("s_bitset0_b32 m0, 12");
}
template <int J> DI void dma_at(AC& c) {
  if constexpr (J < 8)
    asm volatile("buffer_load_dwordx4 %0, %1, s96 offen offset:%2 sc1 lds" ::"v"(c.dw[J >= 4]), "s"(c.sw), "i"((J & 3) * 1024) : "memory");
  else
    asm volatile("buffer_load_dwordx4 v%c0, s[92:95], s98 offen offset:%1 sc1 lds" ::"i"(r_dk(J - 8)), "i"((J & 3) * 1024) : "memory");
}
DI void dma4k(const AC& c, unsigned so, unsigned m0v, int hf) {
    asm volatile("s_mov_b32 m0, %0\n\ts_nop 4\n\tbuffer_load_dwordx4 %1, %2, %3 offen sc1 lds\n\tbuffer_load_dwordx4 %1, %2, %3 offen offset:1024 sc1 lds\n\t"
                 "buffer_load_dwordx4 %1, %2, %3 offen offset:2048 sc1 lds\n\tbuffer_load_dwordx4 %1, %2, %3 offen offset:3072 sc1 lds"
                 ::"s"(m0v + hf * 4096), "v"(c.dw[hf]), "s"(c.sw), "s"(so) : "memory");
}
DI void dma8k(const AC& c, unsigned so, unsigned m0v) { dma4k(c, so, m0v, 0); dma4k(c, so, m0v, 1); }
template <int S_> DI void q_load1(unsigned vo, __amdgpu_buffer_rsrc_t srd, unsigned so) {
  asm volatile("buffer_load_dwordx4 a[%c0:%c1], %2, %3, %4 offen offset:%5 sc1" ::"i"(128 + 4 * S_), "i"(128 + 4 * S_ + 3), "v"(vo), "s"(srd),
               "s"(so + (S_ >> 2) * 4096), "i"((S_ & 3) * 1024) : "memory");
}
template <int Q> DI void acc_zero2() {
  asm volatile("v_accvgpr_write_b32 a[%c0], 0\n\tv_accvgpr_write_b32 a[%c1], 0" ::"i"(2 * Q), "i"(2 * Q + 1));
}
struct SMS { int ex[128], op0[128], nop[128], last; };
constexpr SMS sm_sched() {
  SMS z{};
  int et[32] = {}, e = 0;
  for (int t = 0; t < 128; ++t) {
    const int g = t & 15;
    const bool x = t < 48 ? (g >= 8 && (g & 1)) : (g >= 8 && g != 10 && g != 13 && g != 15);
    z.ex[t] = x && e < 32 ? e : -1;
    if (z.ex[t] >= 0) et[e++] = t;
  }
  int o = 0;
  for (int t = 0; t < 128; ++t) {
    const int g = t & 15;
    int cap = g < 8 ? (g == 0 ? 0 : 1) : (z.ex[t] >= 0 || t < 48 ? 0 : 2);
    z.op0[t] = o; z.nop[t] = 0;
    while (cap > 0 && o < 48 && et[2 * (o / 3) + (o % 3 == 0 ? 0 : 1)] + 2 <= t) {
      const int el = 2 * (o / 3) + o % 3;
      if (!(o % 3 < 2 && (el == 0 || el == 4))) --cap;
      ++o; ++z.nop[t]; z.last = t;
    }
  }
  return z;
}
constexpr SMS SM = sm_sched();
static_assert(SM.last <= 115);
constexpr int p_reg(int Y, int k) { return R_P + 16 * Y + 4 * ((k >> 3) * 2 + ((k >> 1) & 1)) + ((k >> 2) & 1) * 2 + (k & 1); }
template <int Y, int O> DI void sm_op() {
  constexpr int k = O / 3, e = 2 * k + O % 3, ts = R_TS + ((e >> 2) & 1);
  if constexpr (O % 3 == 2) asm volatile("v_cvt_pk_bf16_f32 v%c0, v%c1, v%c2" ::"i"(p_reg(Y, k)), "i"(R_E + ((2 * k) & 15)), "i"(R_E + ((2 * k + 1) & 15)));
  else if constexpr (e == 1 || e == 5) asm volatile("v_add_f32_e32 v%c0, v%c1, v%c2" ::"i"(ts), "i"(R_E + e - 1), "i"(R_E + e));
  else if constexpr (e != 0 && e != 4) asm volatile("v_add_f32_e32 v%c0, v%c0, v%c1" ::"i"(ts), "i"(R_E + (e & 15)));
}
template <int Y, int T> DI void sm_gap(AC& c) {
  if constexpr (SM.ex[T] >= 0) asm volatile("v_exp_f32 v%c0, v%c1" ::"i"(R_E + (SM.ex[T] & 15)), "i"(R_S + 32 * Y + SM.ex[T]));
  if constexpr (SM.nop[T] > 0) sm_op<Y, SM.op0[T]>();
  if constexpr (SM.nop[T] > 1) sm_op<Y, SM.op0[T] + 1>();
  if constexpr (SM.nop[T] > 2) sm_op<Y, SM.op0[T] + 2>();
  if constexpr (T == SM.last + 1) asm volatile("v_add_f32_e32 v%c0, v%c1, v%c2" ::"i"(R_TMP), "i"(R_TS), "i"(R_TS + 1));
  if constexpr (T == SM.last + 2) asm volatile("v_cmp_nle_f32_e64 %0, v%c1, v%c2" : "=s"(c.redo) : "i"(R_TMP), "i"(R_THR));
}
template <int X, bool FIRST, bool NOQK, int G> DI void gapA(AC& c) {
  constexpr int Xp = X ^ 1, kb = G >> 4, g = G & 15, qb = g >> 3, s = g & 7;
  if constexpr (NOQK && G < 48 && G % 3 == 0) q_load1<G / 3>(c.vq, c.sw, c.rare);
  if constexpr (G == 48) asm volatile("s_waitcnt vmcnt(%0) lgkmcnt(0)\n\ts_barrier" ::"i"(NOQK ? 16 : FIRST ? 12 : 0) : "memory");
  else if constexpr (g == 0 && !NOQK) asm volatile("s_waitcnt lgkmcnt(0)");
  if constexpr (!NOQK) {
    constexpr int r = 192 + 4 * ((kb & 1) * 8 + s), q = 128 + 4 * g;
    constexpr int d = R_S + 32 * X + 4 * (kb * 2 + qb), ci = s == 0 ? R_CI + 4 * (kb * 2 + qb) : d;
    if constexpr (FIRST && s == 0)
      asm volatile("v_mfma_f32_16x16x32_bf16 v[%c0:%c1], a[%c2:%c3], a[%c4:%c5], 0" ::"i"(d), "i"(d + 3), "i"(r), "i"(r + 3), "i"(q), "i"(q + 3));
    else
      asm volatile("v_mfma_f32_16x16x32_bf16 v[%c0:%c1], a[%c2:%c3], a[%c4:%c5], v[%c6:%c7]" ::"i"(d), "i"(d + 3), "i"(r), "i"(r + 3), "i"(q), "i"(q + 3),
                   "i"(ci), "i"(ci + 3));
    if constexpr (g < 8 && kb < 3) kread<X, (kb + 1) * 8 + g>(c);
    if constexpr (G == 54) asm volatile("s_add_u32 s96, s97, 0x1000000" ::: "scc");
    if constexpr (G == 55) dma_m0<X, 0>(c);
    if constexpr (G >= 56 && !(G & 1)) dma_at<(G - 56) / 2>(c);
    if constexpr (G == 57) asm volatile("s_mov_b32 s97, s98\n\ts_lshr_b32 s100, s100, 1" ::: "scc");
    if constexpr (G == 63) asm volatile("s_add_u32 s98, s98, s99" ::: "scc");
    if constexpr (G == 59 && !FIRST) asm volatile("s_and_b32 s101, s100, 1\n\ts_or_b32 s101, s101, %0" ::"s"((unsigned)c.redo) : "scc");
    if constexpr (G == 61 && !FIRST) asm volatile("s_or_b32 %0, s101, %1" : "=s"(c.rare) : "s"((unsigned)(c.redo >> 32)) : "scc");
  } else if constexpr (SM.nop[64 + G] > 0) asm volatile("s_nop 0");
  if constexpr (!FIRST && g < 8 && kb == 3) vread<Xp, g>(c);
  if constexpr (FIRST && (G & 3) == 1) d_store<16 + G / 4>();
  if constexpr (FIRST) acc_zero2<G>();
  else sm_gap<Xp, 64 + G>(c);
  GAP();
}
template <int R> DI void rescale1(float alpha) {
  float t;
  asm volatile("v_accvgpr_read_b32 %0, a[%c2]\n\tv_mul_f32 %0, %0, %1\n\tv_accvgpr_write_b32 a[%c2], %0" : "=&v"(t) : "v"(alpha), "i"(R));
}
template <int... Rs> DI void rescale_all(const float (&alpha)[2], iseq<Rs...>) { (rescale1<Rs>(alpha[(Rs >> 2) & 1]), ...); }
template <bool MAX> DI float row_reduce(float v) {
  auto p = __builtin_amdgcn_permlane32_swap(__float_as_uint(v), __float_as_uint(v), false, false);
  v = MAX ? fmaxf(__uint_as_float(p[0]), __uint_as_float(p[1])) : __uint_as_float(p[0]) + __uint_as_float(p[1]);
  p = __builtin_amdgcn_permlane16_swap(__float_as_uint(v), __float_as_uint(v), false, false);
  return MAX ? fmaxf(__uint_as_float(p[0]), __uint_as_float(p[1])) : __uint_as_float(p[0]) + __uint_as_float(p[1]);
}
template <int Y, int QB> DI float row_max() {
  constexpr int r = R_S + 32 * Y + 4 * QB;
  float mx;
  asm volatile("v_max_f32 %0, v%c1, v%c2\n\tv_max3_f32 %0, %0, v%c3, v%c4" : "=&v"(mx) : "i"(r), "i"(r + 1), "i"(r + 2), "i"(r + 3));
  static_for([&](auto K) {
    constexpr int q = r + 8 * (K + 1);
    float t = mx;
    asm volatile("v_max3_f32 %0, %0, v%c1, v%c2\n\tv_max3_f32 %0, %0, v%c3, v%c4" : "+v"(t) : "i"(q), "i"(q + 1), "i"(q + 2), "i"(q + 3));
    mx = t;
  }, make_iseq<3>{});
  return row_reduce<true>(mx);
}
template <int R> DI void v_sub(float d) { asm volatile("v_sub_f32 v%c0, v%c0, %1" ::"i"(R), "v"(d)); }
template <int B> DI void s_shift(const float (&d)[2]) { static_for([&](auto E) { v_sub<B + E>(d[(E >> 2) & 1]); }, make_iseq<32>{}); }
DI void m_shift(const float (&d)[2]) {
  s_shift<R_CI>(d);
  v_sub<R_M>(-d[0]); v_sub<R_M + 1>(-d[1]);
}
template <int B, bool INPL> DI void set_mask(const AC& c, int t64) {
  const int mr = c.mrow - t64;
  const float ninf = -INFINITY;
  static_for([&](auto I) {
    constexpr int i = I, r = B + 4 * i, k = (i >> 1) * 16 - (i & 1) * 16;
    const int mr_ = mr; const float b = ninf;
    if constexpr (INPL)
      asm volatile("v_cmp_gt_i32 vcc, %c4, %8\n\tv_cndmask_b32 v%c0, v%c0, %9, vcc\n\tv_cmp_gt_i32 vcc, %c5, %8\n\tv_cndmask_b32 v%c1, v%c1, %9, vcc\n\t"
                   "v_cmp_gt_i32 vcc, %c6, %8\n\tv_cndmask_b32 v%c2, v%c2, %9, vcc\n\tv_cmp_gt_i32 vcc, %c7, %8\n\tv_cndmask_b32 v%c3, v%c3, %9, vcc"
                   ::"i"(r), "i"(r + 1), "i"(r + 2), "i"(r + 3), "i"(k), "i"(k + 1), "i"(k + 2), "i"(k + 3), "v"(mr_), "v"(b) : "vcc");
    else
      asm volatile("v_cmp_gt_i32 vcc, %c4, %8\n\tv_cndmask_b32_e64 v%c0, -v%c10, %9, vcc\n\tv_cmp_gt_i32 vcc, %c5, %8\n\tv_cndmask_b32_e64 v%c1, -v%c10, %9, vcc\n\t"
                   "v_cmp_gt_i32 vcc, %c6, %8\n\tv_cndmask_b32_e64 v%c2, -v%c10, %9, vcc\n\tv_cmp_gt_i32 vcc, %c7, %8\n\tv_cndmask_b32_e64 v%c3, -v%c10, %9, vcc"
                   ::"i"(r), "i"(r + 1), "i"(r + 2), "i"(r + 3), "i"(k), "i"(k + 1), "i"(k + 2), "i"(k + 3), "v"(mr_), "v"(b), "i"(R_M + (i & 1)) : "vcc");
  }, make_iseq<8>{});
  asm volatile("s_nop 1");
}
template <int X> DI void sm_redo() {
  constexpr int Xp = X ^ 1;
  float d[2], dt[2], alpha[2], ts[2] = {0.f, 0.f};
  static_for([&](auto Q) {
    constexpr int qb = Q;
    const float pd = vget<R_PD + qb>();
    d[qb] = fmaxf(row_max<Xp, qb>() - pd, 0.f);
    dt[qb] = d[qb] + pd;
    vset<R_PD + qb>(d[qb]);
    alpha[qb] = __builtin_amdgcn_exp2f(-d[qb]);
    vset<R_RS + qb>(vget<R_RS + qb>() * alpha[qb]);
  }, make_iseq<2>{});
  vset<R_THR>(d[0] > 0.f || d[1] > 0.f ? -1.f : 4096.f);
  m_shift(d);
  static_for([&](auto K) {
    constexpr int k = K, qb = (k >> 1) & 1;
    const float e0 = __builtin_amdgcn_exp2f(vget<R_S + 32 * Xp + 2 * k>() - dt[qb]), e1 = __builtin_amdgcn_exp2f(vget<R_S + 32 * Xp + 2 * k + 1>() - dt[qb]);
    ts[qb] += e0; ts[qb] += e1;
    vset<p_reg(Xp, k)>(__uint_as_float(pk2(e0, e1)));
  }, make_iseq<16>{});
  vset<R_TS>(ts[0]); vset<R_TS + 1>(ts[1]);
  rescale_all(alpha, make_iseq<128>{});
  asm volatile("s_nop 1" ::: "memory");
  GAP();
}
template <int X, bool FIRST, bool NOQK, int... Gs> DI void phaseA_(AC& c, iseq<Gs...>) {
  (gapA<X, FIRST, NOQK, Gs>(c), ...);
}
template <int X, bool FIRST, bool NOQK> DI void phaseA(AC& c) { phaseA_<X, FIRST, NOQK>(c, make_iseq<64>{}); }

template <int X, bool SMX, int G> DI void gapB(AC& c) {
  constexpr int Xp = X ^ 1, blk = G >> 4, g = G & 15;
  if constexpr (g == 0) asm volatile("s_waitcnt lgkmcnt(0)");
  if constexpr (SMX && g == 7 && blk < 3) dma_m0<X, blk * 4 + 4>(c);
  constexpr int qb = (g >> 1) & 1, s = g & 1, r = 192 + 4 * ((blk & 1) * 8 + (g >> 2) * 2 + s), o = 4 * ((blk * 4 + (g >> 2)) * 2 + qb);
  constexpr int p = R_P + 16 * Xp + 4 * (s * 2 + qb);
  asm volatile("v_mfma_f32_16x16x32_bf16 a[%c0:%c1], a[%c2:%c3], v[%c4:%c5], a[%c0:%c1]" ::"i"(o), "i"(o + 3), "i"(r), "i"(r + 3), "i"(p), "i"(p + 3));
  if constexpr (SMX && (G == 1 || G == 2)) asm volatile("v_add_f32_e32 v%c0, v%c0, v%c1" ::"i"(R_RS + G - 1), "i"(R_TS + G - 1));
  if constexpr (g < 8) {
    if constexpr (blk < 3) vread<Xp, (blk + 1) * 8 + g>(c);
    else if constexpr (SMX) kread<Xp, g>(c);
  }
  if constexpr (SMX) {
    if constexpr (g >= 8 && !(g & 1) && blk < 3) dma_at<blk * 4 + 4 + (g - 8) / 2>(c);
    sm_gap<X, G>(c);
  }
  GAP();
}
template <int X, int... Gs> DI void phaseB_(AC& c, iseq<Gs...>) { (gapB<X, true, Gs>(c), ...); }
template <int X> DI void phaseB(AC& c) { phaseB_<X>(c, make_iseq<64>{}); }
constexpr int sm_nex() { int n = 0; for (int t = 0; t < 64; ++t) n += SM.ex[t] >= 0; return n; }
DI void phaseB0(AC& c) {
  dma_m0<0, 4>(c);
  kreads<1>(c, make_iseq<8>{});
  dma_at<4>(c);
  asm volatile("s_nop 3");
  if (__builtin_expect(c.diag0, 0)) set_mask<R_S, true>(c, 0);
  float d[2];
  d[0] = row_max<0, 0>(); GAP(); dma_at<5>(c);
  d[1] = row_max<0, 1>(); GAP(); dma_at<6>(c);
#pragma unroll
  for (int qb = 0; qb < 2; ++qb) if (!(d[qb] > -3.0e38f)) d[qb] = 0.f;
  vset<R_M>(d[0]); vset<R_M + 1>(d[1]);
  const f32x2c nd[2] = {{-d[0], -d[0]}, {-d[1], -d[1]}};
  static_for([&](auto E) {
    constexpr int e = E;
    const f32x2c v = nd[(e >> 1) & 1];
    asm volatile("v_pk_add_f32 v[%c0:%c1], v[%c0:%c1], %4\n\tv_mov_b64 v[%c2:%c3], %4" ::"i"(R_S + 2 * e), "i"(R_S + 2 * e + 1), "i"(R_CI + 2 * e), "i"(R_CI + 2 * e + 1), "v"(v));
    if constexpr (e == 3) dma_at<7>(c);
    if constexpr (e == 6) dma_m0<0, 8>(c);
    if constexpr (e == 7 || e == 11 || e == 15) dma_at<8 + (e - 7) / 4>(c);
  }, make_iseq<16>{});
  if (__builtin_expect(c.diag0, 0)) set_mask<R_CI, false>(c, 64);
  static_assert(sm_nex() >= 16 && SM.op0[64] >= 3 * ((sm_nex() - 15) / 2) && SM.op0[64] <= 24);
  static_for([&](auto E) {
    constexpr int e = E;
    asm volatile("v_exp_f32 v%c0, v%c1" ::"i"(R_E + e), "i"(R_S + e));
    if constexpr (e == 3) dma_at<11>(c);
    if constexpr (e == 6) dma_m0<0, 12>(c);
    if constexpr (e == 7 || e == 11 || e == 15) dma_at<12 + (e - 7) / 4>(c);
  }, make_iseq<16>{});
  static_for([&](auto O) { sm_op<0, O>(); }, make_iseq<SM.op0[64]>{});
  dma_at<15>(c);
  static_for([&](auto E) { asm volatile("v_exp_f32 v%c0, v%c1" ::"i"(R_E + (int)E), "i"(R_S + 16 + (int)E)); }, make_iseq<sm_nex() - 16>{});
  GAP();
}

#define ACLOB_ALL "a0","a1","a2","a3","a4","a5","a6","a7","a8","a9","a10","a11","a12","a13","a14","a15","a16","a17","a18","a19","a20","a21","a22","a23","a24","a25","a26","a27","a28","a29","a30","a31","a32","a33","a34","a35","a36","a37","a38","a39","a40","a41","a42","a43","a44","a45","a46","a47","a48","a49","a50","a51","a52","a53","a54","a55","a56","a57","a58","a59","a60","a61","a62","a63","a64","a65","a66","a67","a68","a69","a70","a71","a72","a73","a74","a75","a76","a77","a78","a79","a80","a81","a82","a83","a84","a85","a86","a87","a88","a89","a90","a91","a92","a93","a94","a95","a96","a97","a98","a99","a100","a101","a102","a103","a104","a105","a106","a107","a108","a109","a110","a111","a112","a113","a114","a115","a116","a117","a118","a119","a120","a121","a122","a123","a124","a125","a126","a127","a128","a129","a130","a131","a132","a133","a134","a135","a136","a137","a138","a139","a140","a141","a142","a143","a144","a145","a146","a147","a148","a149","a150","a151","a152","a153","a154","a155","a156","a157","a158","a159","a160","a161","a162","a163","a164","a165","a166","a167","a168","a169","a170","a171","a172","a173","a174","a175","a176","a177","a178","a179","a180","a181","a182","a183","a184","a185","a186","a187","a188","a189","a190","a191","a192","a193","a194","a195","a196","a197","a198","a199","a200","a201","a202","a203","a204","a205","a206","a207","a208","a209","a210","a211","a212","a213","a214","a215","a216","a217","a218","a219","a220","a221","a222","a223","a224","a225","a226","a227","a228","a229","a230","a231","a232","a233","a234","a235","a236","a237","a238","a239","a240","a241","a242","a243","a244","a245","a246","a247","a248","a249","a250","a251","a252","a253","a254","a255"

struct EP { float fa[2], fb[2]; int vo; unsigned so[4]; };
template <int I, int E, bool MERGE> DI void epi_pair(const EP& p) {
  constexpr int qb = I & 1, r = 16 * (I >> 1) + 4 * qb + (E >> 1) * 8 + (E & 1) * 2, t = 96 + 2 * E, w = 128 + 4 * I + E;
  if constexpr (MERGE) asm volatile("v_accvgpr_read_b32 v106, a[%c0]" ::"i"(w));
  asm volatile("v_accvgpr_read_b32 v%c0, a[%c2]\n\tv_accvgpr_read_b32 v%c1, a[%c3]\n\tv_mul_f32 v%c0, v%c0, %4\n\tv_mul_f32 v%c1, v%c1, %4"
               ::"i"(t), "i"(t + 1), "i"(r), "i"(r + 1), "v"(p.fa[qb]));
  if constexpr (MERGE)
    asm volatile("v_lshlrev_b32 v104, 16, v106\n\tv_and_b32 v105, 0xffff0000, v106\n\tv_fmac_f32 v%c0, v104, %2\n\tv_fmac_f32 v%c1, v105, %2"
                 ::"i"(t), "i"(t + 1), "v"(p.fb[qb]));
  asm volatile("v_cvt_pk_bf16_f32 v%c0, v%c1, v%c2" ::"i"(w), "i"(t), "i"(t + 1));
}
template <int I> DI void epi_store(const AC& c, const EP& p) {
  asm volatile("buffer_store_dwordx4 v[%c0:%c1], %2, %3, %4 offen offset:%5 sc1" ::"i"(128 + 4 * I), "i"(131 + 4 * I), "v"(p.vo), "s"(c.sw),
               "s"(p.so[I >> 2]), "i"((I & 3) * 1024) : "memory");
}
constexpr int R_TB = 107, R_VS = 111;
template <int HF, int R> DI void p_read() {
  constexpr int mb = R >> 4, r = 64 * HF + 2 * R;
  asm volatile("ds_read_b64_tr_b16 a[%c1:%c2], v%c0 offset:%c3" ::"i"(R_TB + 2 * HF + mb), "i"(r), "i"(r + 1), "i"((R & 15) * 2048));
}
template <int QB, int E> DI void p_cvt() {
  asm volatile("v_cvt_pk_bf16_f32 v%c0, v%c1, v%c2" ::"i"(208 + 4 * QB + E), "i"(192 + 8 * QB + 2 * E), "i"(193 + 8 * QB + 2 * E));
}
template <int QB, int PR> DI void p_store(const AC& c) {
  asm volatile("buffer_store_dwordx4 v[%c0:%c1], v%c2, %3, s96 offen offset:%4 sc1" ::"i"(208 + 4 * QB), "i"(211 + 4 * QB), "i"(R_VS + QB), "s"(c.sw),
               "i"(PR * 128) : "memory");
}
constexpr int p_dma(int HF, int G) { return G == 3 && !HF ? 0 : G == 11 ? 1 - HF : G == 15 ? 2 - HF : G == 19 ? 3 - HF : G == 27 && HF ? 3 : -1; }
template <int HF, int G> DI void p_gap(AC& c) {
  constexpr int ch = G >> 3, t = G & 7, a = 64 * HF + 32 * (ch & 1) + 4 * t, b = 128 + 4 * (2 * t + (ch >> 1)), d = 192 + 4 * ch;
  if constexpr (t == 0)
    asm volatile("v_mfma_f32_16x16x32_bf16 v[%c0:%c1], a[%c2:%c3], v[%c4:%c5], 0" ::"i"(d), "i"(d + 3), "i"(a), "i"(a + 3), "i"(b), "i"(b + 3));
  else
    asm volatile("v_mfma_f32_16x16x32_bf16 v[%c0:%c1], a[%c2:%c3], v[%c4:%c5], v[%c0:%c1]" ::"i"(d), "i"(d + 3), "i"(a), "i"(a + 3), "i"(b), "i"(b + 3));
  p_read<HF ^ 1, G>();
  if constexpr (G == 0) asm volatile("v_xor_b32 v%c0, s100, v%c0" ::"i"(R_TB + 2 * HF + 1));
  if constexpr (G == 17) asm volatile("v_xor_b32 v%c0, s100, v%c0" ::"i"(R_TB + 2 * (HF ^ 1)));
  if constexpr (G >= 4 && G <= 8 && G != 7) p_cvt<1, (G < 7 ? G - 4 : 3)>();
  if constexpr (G == 24 || (G == 7 && HF == 0)) asm volatile("s_waitcnt lgkmcnt(8)");
  if constexpr (G == 7 && HF == 1) asm volatile("s_waitcnt vmcnt(13) lgkmcnt(8)\n\ts_barrier" ::: "memory");
  if constexpr (G == 10) p_store<1, HF ^ 1>(c);
  if constexpr (G >= 20 && G < 24) p_cvt<0, G - 20>();
  if constexpr (G == 25) p_store<0, HF>(c);
  if constexpr (p_dma(HF, G) >= 0) dma_at<(HF ? 8 : 12) + p_dma(HF, G)>(c);
  if constexpr (HF == 0 && G == 2) asm volatile("s_bitset0_b32 m0, 12");
  if constexpr (HF == 0 && G == 13) asm volatile("s_add_u32 s96, s96, 0x40000");
  if constexpr (HF == 0 && G == 26) asm volatile("s_add_u32 s98, s98, 128");
  if constexpr (HF == 0 && G == 27) asm volatile("s_cmp_lt_u32 s98, 0x380");
  if constexpr (HF == 0 && G == 28) asm volatile("s_cselect_b32 s99, s99, 0x31000");
  if constexpr (HF == 1 && G == 1) asm volatile("s_xor_b32 s100, s100, 0x10000");
  if constexpr (HF == 1 && G == 2) asm volatile("s_xor_b32 s97, s97, s100");
  if constexpr (HF == 1 && G == 9) asm volatile("s_or_b32 m0, s97, s99");
  if constexpr (HF == 1 && G == 30) asm volatile("s_cmp_lt_u32 s98, 0x500");
  GAP();
}
template <int HF, int... Gs> DI void p_half(AC& c, iseq<Gs...>) { (p_gap<HF, Gs>(c), ...); }
constexpr int f_dma(int G) { return G == 2 ? 0 : G == 5 ? 1 : G == 8 ? 2 : G >= 12 && G <= 36 && G % 6 == 0 ? 1 + G / 6 : G >= 50 && G <= 68 && G % 6 == 2 ? G / 6 : -1; }
constexpr int f_ndma(int G) { int n = 0; for (int g = 0; g < G; ++g) n += f_dma(g) >= 0; return n; }
static_assert(f_ndma(74) == 12);
template <int MODE, int G> DI void gapF(AC& c, const EP& p) {
  if constexpr (MODE && G == 48) asm volatile("s_waitcnt lgkmcnt(0)\n\ts_barrier" ::: "memory");
  if constexpr (G < 64) gapB<0, false, G>(c);
  if constexpr (MODE == 2 && G >= 10 && (G - 10) % 16 == 0) asm volatile("s_waitcnt vmcnt(%0)" ::"i"(12 - (G - 10) / 4 + f_ndma(G)) : "memory");
  if constexpr (G >= 10) epi_pair<(G - 10) / 4, (G - 10) & 3, MODE == 2>(p);
  if constexpr (MODE == 0) {
    if constexpr (G >= 14 && ((G - 14) & 3) == 0) epi_store<(G - 14) / 4>(c, p);
    if constexpr (G == 73) epi_store<15>(c, p);
  } else {
    if constexpr (G == 0) asm volatile("s_or_b32 m0, %0, 0x1000\n\ts_movk_i32 s98, 256" ::"s"(c.m0x[0]) : "scc");
    if constexpr (G == 15) asm volatile("s_bitset0_b32 m0, 12");
    if constexpr (G == 48) asm volatile("s_or_b32 m0, %0, 0x1000\n\ts_movk_i32 s98, 384" ::"s"(c.m0x[1]) : "scc");
    if constexpr (f_dma(G) >= 0) dma_at<8 + (f_dma(G) & 7)>(c);
    if constexpr (G == 73) asm volatile("s_waitcnt vmcnt(4) lgkmcnt(0)" ::: "memory");
    if constexpr (G >= 48 && G < 64) { p_read<0, 2 * (G - 48)>(); p_read<0, 2 * (G - 48) + 1>(); }
  }
  GAP();
}
template <int MODE, int... Gs> DI void phaseF(AC& c, const EP& p, iseq<Gs...>) { (gapF<MODE, Gs>(c, p), ...); }
template <int U, int K> DI void r_load(const AC& c, unsigned vo, unsigned so) {
  asm volatile("buffer_load_dwordx4 a[%c0:%c1], %2, %3, %4 offen offset:%5 sc1" ::"i"(64 * U + 4 * K), "i"(64 * U + 4 * K + 3), "v"(vo), "s"(c.sw),
               "s"(so + (K >> 2) * 4096), "i"((K & 1) * 512 + ((K >> 1) & 1) * 2048) : "memory");
}
template <int U, int K> DI void r_sum() {
  constexpr int d = 100 + 4 * (K & 1), b = 64 * U + 4 * K;
  if constexpr (K < 2) asm volatile("v_mfma_f32_16x16x32_bf16 v[%c0:%c1], v[96:99], a[%c2:%c3], 0" ::"i"(d), "i"(d + 3), "i"(b), "i"(b + 3));
  else asm volatile("v_mfma_f32_16x16x32_bf16 v[%c0:%c1], v[96:99], a[%c2:%c3], v[%c0:%c1]" ::"i"(d), "i"(d + 3), "i"(b), "i"(b + 3));
}
template <int U> DI void r_fin(unsigned vo, __amdgpu_buffer_rsrc_t srd, unsigned so) {
  asm volatile("" : "+s"(srd), "+s"(so));
  static_for([&](auto K) { r_sum<U, K>(); }, make_iseq<16>{});
  asm volatile("s_nop 7");
  static_for([&](auto E) { asm volatile("v_cvt_pk_bf16_f32 v%c0, v%c1, v%c2" ::"i"(116 + 4 * U + E), "i"(100 + 2 * E), "i"(101 + 2 * E)); }, make_iseq<4>{});
  asm volatile("buffer_store_dwordx4 v[%c3:%c4], %0, %1, %2 offen" ::"v"(vo), "s"(srd), "s"(so), "i"(116 + 4 * U), "i"(119 + 4 * U) : "memory");
}

#define CMAX 12
#define NYT 16
DI u64* fw(u64* p, int w) { return p + (w >> 4) * 544 + (w & 15); }
DI void set_flag(u64* p, u64 tag, int skip = 0) {
  asm volatile("s_cmp_lg_u32 %2, 0\n\ts_cbranch_scc1 1f\n\ts_mov_b64 exec, 1\n\tglobal_store_dwordx2 %0, %1, off sc1\n\ts_mov_b64 exec, -1\n1:"
               ::"v"(p), "v"(tag), "s"(skip) : "scc", "memory");
}
DI void wait_flags(u64* p, u64 need, u64 tag) {
  const u64* q = fw(p, threadIdx.x & 63);
  u64 v; unsigned cnt;
  asm volatile("s_mov_b32 %1, 0x100000\n\ts_mov_b64 exec, %3\n"
               "1:\n\tglobal_load_dwordx2 %0, %2, off sc1\n\ts_waitcnt vmcnt(0)\n\tv_cmp_eq_u64 vcc, %4, %0\n\t"
               "s_cmp_eq_u64 vcc, %3\n\ts_cbranch_scc1 2f\n\ts_sleep 4\n\ts_sub_u32 %1, %1, 1\n\ts_cbranch_scc0 1b\n"
               "2:\n\ts_mov_b64 exec, -1" : "=&v"(v), "=&s"(cnt) : "v"(q), "s"(need), "s"(tag) : "vcc", "scc", "memory");
}
template <int H> DI void fl_issue(u64* gf, u64 need) {
  const u64* q = fw(gf, threadIdx.x & 63) + H * 4 * 544;
  asm volatile("s_mov_b64 exec, %1\n\tglobal_load_dwordx2 v[%c2:%c3], %0, off sc1\n\ts_mov_b64 exec, -1" ::"v"(q), "s"(need), "i"(122 + 2 * H), "i"(123 + 2 * H) : "memory");
}
template <int H> DI void fl_check(u64* gf, u64 need, u64 tag) {
  u64 m;
  asm volatile("v_cmp_eq_u64 %0, %1, v[%c2:%c3]" : "=&s"(m) : "s"(tag), "i"(122 + 2 * H), "i"(123 + 2 * H) : "memory");
  if (__builtin_expect((m & need) != need, 0)) wait_flags(gf + H * 4 * 544, need, tag);
}
template <bool TR, bool SCL, class START, class MID, class PRE>
DI void g_item(GM& g, const GItem& nxt, u64* pend, u64& tag, unsigned out, int wid, int lane,
               u64* gf, u64 n0, u64 n1, unsigned evx, START&& start, MID&& mid, PRE&& pre) {
  constexpr int nkt = SCL ? 7 : 16, P0 = SCL ? 0 : 1;
  GE e;
  GAP();
  start(std::integral_constant<int, P0>{});
  g_fix(g);
  asm volatile("s_nop 4");
  GAP();
  auto hook = [&](int t) __attribute__((always_inline)) {
    if (t == 1) {
      set_flag(pend, tag, wid);
      mid(1);
    } else if (t < nkt - 3) mid(t);
    else if (t == nkt - 3) {
      g_srcB(g, nxt);
      if constexpr (!SCL) { fl_issue<0>(gf, n0); if (n1) fl_issue<1>(gf, n1); mid(t); }
    } else g_srcA(g, nxt, wid, lane);
    g_fix(g);
    asm volatile("s_nop 4");
    GAP();
  };
  g_half<TR, P0, 0, SCL ? 1 : 4>(g, e);
  g_half<TR, P0, 1, SCL ? 1 : 4>(g, e);
  unsigned evm = 2u | (4u << (nkt - 5)) | (4u << (nkt - 4)) | evx;
  asm volatile("" : "+s"(evm));
#pragma unroll 1
  for (int t = 1;; t += 2) {
    g_half<TR, P0 ^ 1, 0, 0>(g, e);
    if (__builtin_expect((evm >> t) & 1, 0)) hook(t);
    g_half<TR, P0 ^ 1, 1, 0>(g, e);
    if (SCL && t + 2 >= nkt) break;
    g_half<TR, P0, 0, 0>(g, e);
    if (__builtin_expect(t + 1 == (SCL ? nkt - 3 : nkt - 2), 0)) hook(t + 1);
    g_half<TR, P0, 1, 0>(g, e);
    if (!SCL && t + 3 >= nkt) break;
  }
  int l = threadIdx.x & 63; asm volatile("" : "+v"(l));
  vset<112>(__uint_as_float(l * 16)); vset<113>(__uint_as_float(l * 16 + (TR ? 4096 : 8192)));
  sset<101>(out + (wid >> 1) * 65536 + (wid & 1) * (TR ? 8192 : 4096));
  pre(e);
  if constexpr (!SCL) asm volatile("s_nop 4" : "+s"(e.sk), "+s"(e.sx), "+s"(e.mC), "+s"(e.mK));
  GAP();
  g_half<TR, 0, 0, SCL ? 2 : 3>(g, e);
  g_half<TR, 0, 1, SCL ? 2 : 3>(g, e);
}

constexpr size_t TSZ = (size_t)NH * SEQ * HD;
constexpr unsigned WSB = 5 * TSZ * 2 + 128 * 66560;
__global__ __launch_bounds__(256, 1) __attribute__((amdgpu_num_vgpr(90), amdgpu_num_sgpr(96))) void fused_kernel(const bf16* __restrict__ ckv, const bf16* __restrict__ wuk,
                                                       const bf16* __restrict__ wuv, const bf16* __restrict__ x,
                                                       const bf16* __restrict__ wq, bf16* Qb,
                                                       const bf16* __restrict__ ak, const bf16* __restrict__ av,
                                                       const bf16* __restrict__ ash, const bf16* __restrict__ assmk,
                                                       const bf16* __restrict__ assmD, const bf16* __restrict__ wo,
                                                       const bf16* __restrict__ aq, bf16* __restrict__ out) {
  extern __shared__ __attribute__((aligned(16))) char lds[];
  asm volatile("at_code_begin:" ::: ACLOB_ALL, "v255", "s101");
  asm volatile("" : "+s"(ckv), "+s"(wuk), "+s"(wuv), "+s"(x), "+s"(wq), "+s"(Qb), "+s"(ak), "+s"(av), "+s"(ash), "+s"(assmk),
                    "+s"(assmD), "+s"(wo), "+s"(aq), "+s"(out));
  float* partbuf = (float*)(Qb + 5 * TSZ);
  u64* flags = (u64*)(partbuf + (size_t)128 * 16640);
  bf16* xqT = (bf16*)(flags + 400 * 544);
  KST(0, 0);
  const int tid = threadIdx.x, lane = tid & 63;
  const int wid = __builtin_amdgcn_readfirstlane(tid >> 6);
  const int b = blockIdx.x, j = b & 7, r = b >> 3, rr = r & 15, hsel = rr & 1, pj = rr >> 1, kv = pj & 1;
  const bool isY = r >= 16;
  const int h = j + 8 * hsel, mt = (rr >> 2) + (isY ? 4 : 0);
  const unsigned lbase = (unsigned)(size_t)lds;
  u64* gf = fw(flags, j * 128);
  const unsigned m0d = lbase + 131072 + 24576;
  const unsigned m0f = lbase + 131072 + wid * 8192;
  const int cch = rr * 4 + wid;
  const bool doA = !isY && cch < 56;
  u64 tag = 0;
  AC c;
  auto att_init = [&]() __attribute__((always_inline)) {
    int l = threadIdx.x & 63; asm volatile("" : "+v"(l));
    c.ka = lbase + l * 16;
    c.dw[0] = wid * 8192 + l * 16; c.dw[1] = c.dw[0] + 4096;
    c.sw = __builtin_amdgcn_make_buffer_rsrc((void*)Qb, (short)0, WSB, 0x00020000);
    static_for([&](auto I) { vset<r_dk(I)>(__uint_as_float(c.dw[I < 4])); }, make_iseq<8>{});
    sset<S_I>(0x8000);
  };
  const unsigned oQ = (unsigned)h << 20, oK = oQ + (unsigned)(TSZ * 2);
  auto pro_k = [&](int t0) __attribute__((always_inline)) {
    dma8k(c, oK + t0 * 32768, lbase + wid * 8192);
    dma8k(c, oK + t0 * 32768 + 32768, lbase + 32768 + wid * 8192);
  };
  static_assert(NYT <= 18 && NYT >= 12 && !(NYT & 1));
  const bool solo = pj == 7;
  const bool doP = isY != solo;
#define NYM 0
#define NYD -2
  const int NY = solo ? 18 : NYT + ((NYM >> pj) & 1) * (NYD);
  const int nx0 = 32 - 2 * pj - NY, cx = nx0 < CMAX ? nx0 : CMAX, ex = nx0 - cx;
  const u64 needY = (((0x101ull << (((31 - 2 * pj) >> 2) + 1)) - 0x1010ull) << (hsel * 16)) & ~(1ull << (hsel * 16 + kv * 8 + mt));
  const u64 needX = ((0x1010ull << ((ex + 3) >> 2)) - 0x1010ull) << (hsel * 16);
  const u64 needQ = (1ull << (32 + hsel * 8 + (pj >> 1))) | (1ull << (32 + hsel * 8 + ((15 - pj) >> 1)));
  const u64 needA = 0x0f0full << (hsel * 16);
  const u64 needW = 0x3fffull << 36;
  u64* gflag = fw(gf, hsel * 16 + kv * 8 + mt);
  unsigned dv = 0;
  {
    GM g;
    PR pr;
    const u64 tabQ = 0x6543210ull;
    const int cls = (rr + (rr >> 2)) & 3;
    const u64 tabX = cls == 0 ? 0xfedcb9875a463210ull : cls == 1 ? 0xefadc6982b574310ull : cls == 2 ? 0xfebad7693c285410ull : 0xefcba8764d392510ull;
    const u64 tabG = !isY ? tabX : cls == 0 ? 0xfedcb987543a6210ull : cls == 1 ? 0xefadc698254b7310ull : cls == 2 ? 0xfebad769325c8410ull : 0xefcba876432d9510ull;
    const GItem itQ{x + (size_t)pj * 256 * DA, wq + h * 256, DA, tabQ};
    const GItem itG{ckv + (size_t)mt * 256 * DC, (kv ? wuv : wuk) + h * 256, DC, tabG};
    const GItem none{nullptr, nullptr, 0, 0};
    if (doP) prep_load(pr, tid, j, rr, ak, av, ash);
    set_srdk(Qb, WSB);
    d_none();
    const unsigned lA = lbase + (isY ? 65536 : 0), lW = lA ^ 65536;
    g.mA = lA + wid * 1024; g.mB = lW + wid * 1024; g.kW = 0;
    g.vbB = (4 * wid + (lane >> 4)) * LDW * 2 + 16 * ((lane & 15) ^ (((lane >> 4) << 2) | wid));
    g_srcA(g, isY ? itQ : itG, wid, lane);
    g_srcB(g, isY ? itQ : itG);
    g_fix(g);
    const unsigned po = isY ? 0 : 32768;
    g_pro<0>(g, po, make_iseq<12>{});
    asm volatile("global_load_dwordx2 v[120:121], %0, off sc1" ::"v"(fw(flags, 6144)) : "memory");
    if (doP) {
      asm volatile("s_waitcnt vmcnt(13)" ::: "memory");
#pragma unroll
      for (int q = 0; q < 7; ++q) asm volatile("" : "+v"(pr.kk[q]));
#pragma unroll
      for (int q = 0; q < 4; ++q) asm volatile("" : "+v"(pr.vv[q]), "+v"(pr.w[q]));
      static_for([&](auto Ic) __attribute__((always_inline)) {
        constexpr int I = Ic;
        g_pro<12 + 4 * I>(g, po, make_iseq<4>{});
        GAP();
        prep_math<I>(pr, tid, lds + 131072);
        GAP();
      }, make_iseq<4>{});
      asm volatile("s_waitcnt lgkmcnt(0)\n\ts_barrier" ::: "memory");
      prep_store(tid, j, rr, xqT, lds + 131072);
    } else g_pro<12>(g, po, make_iseq<4>{});
    GAP();
    CODE_TOUCH("at_code_begin", "pf_code_end", r * 4 + wid, 128u, 8, m0d);
    {
      const int fr = lane & 15, fq = lane >> 4, q = (lane & 15) >> 2, p = lane & 3;
#pragma unroll
      for (int k2 = 0; k2 < 2; ++k2)
        g.aA[k2] = lA + (wid >> 1) * 16384 + (fr >> 3) * 1024 + (fr & 7) * 128 + (((4 * k2 + fq) ^ (fr >> 1)) * 16);
#pragma unroll
      for (int hf = 0; hf < 2; ++hf)
#pragma unroll
        for (int nf = 0; nf < 8; ++nf)
          g.aB[hf][nf] = lW + (wid & 1) * 8192 + off_b(8 * fq + 4 * hf + q, nf * 2 + (p >> 1)) + 8 * (p & 1);
    }
    auto start = [&](auto Pc) __attribute__((always_inline)) {
      if (doP) asm volatile("s_waitcnt vmcnt(13)" ::: "memory");
      else {
        asm volatile("s_nop 4");
        g_pro<16>(g, po, make_iseq<12>{});
        asm volatile("s_waitcnt vmcnt(8)" ::: "memory");
      }
      asm volatile("s_barrier" ::: "memory");
      g_read0<Pc>(g, make_iseq<16>{});
      GAP();
      u64 z = (((u64)__float_as_uint(vget<121>()) << 32) | __float_as_uint(vget<120>())) + 0x9e3779b97f4a7c15ull;
      z = (z ^ (z >> 30)) * 0xbf58476d1ce4e5b9ull; z = (z ^ (z >> 27)) * 0x94d049bb133111ebull; z ^= z >> 31;
      tag = ((u64)(unsigned)__builtin_amdgcn_readfirstlane((int)(z >> 32)) << 32) | (unsigned)__builtin_amdgcn_readfirstlane((int)z);
      asm volatile("s_waitcnt lgkmcnt(0)\n\ts_barrier\n\ts_nop 4" ::: "memory");
      g_pro<28>(g, po, make_iseq<4>{});
      g.kW = ((unsigned)g.tab >> 8) & 15;
      g.voA = g.vbA + (((unsigned)g.tab >> 4) & 15) * 128;
      g.voB = g.vbB + g.kW * 0x80000;
      g.tab >>= 12;
      GAP();
    };
    u64* pend = fw(gf, 64 + r);
    const u64 np0 = isY ? needA | needQ : needQ, np1 = doA ? 0x3fffc000ull : 0ull;
    if (isY) {
      g_item<false, true>(g, itG, pend, tag, oQ + pj * 131072, wid, lane, gf, 0, 0, 0, start, [&](int) {}, [&](GE&) {});
      pend = fw(gf, 32 + hsel * 8 + pj);
      KST(0, 3);
    }
    auto start2 = [&](auto Pc) __attribute__((always_inline)) { if (!isY) start(Pc); };
#define TT 3
#define TK 1
    auto mid = [&](int t) __attribute__((always_inline)) {
      if (t == 1) { if (!isY) post_dma<false>(j, r * 4 + wid, xqT, aq, m0f); }
      else {
        int l = threadIdx.x & 63; asm volatile("" : "+v"(l));
        if (t < 13) {
          const char* tp = (const char*)ckv + 1024 * DC * 2 + (((b & 15) * 4 + wid) * 256 + l) * 128;
          asm volatile("s_mov_b32 m0, %1\n\ts_nop 0\n\tglobal_load_lds_dword %0, off" ::"v"(tp), "s"(m0d) : "memory");
          asm volatile("global_load_lds_dword %0, off" ::"v"(tp + 8192) : "memory");
          asm volatile("global_load_lds_dword %0, off" ::"v"(tp + 16384) : "memory");
          asm volatile("global_load_lds_dword %0, off" ::"v"(tp + 24576) : "memory");
        } else if (doA && TK) {
          const char* tp = (const char*)assmk + (size_t)(j * 56 + cch) * (SEQ * 2) + (l & 31) * 128;
          asm volatile("s_mov_b32 m0, %1\n\ts_nop 0\n\tglobal_load_lds_dword %0, off" ::"v"(tp), "s"(m0d) : "memory");
        }
      }
    };
    const unsigned evx = isY || !solo || !TT ? 0u : 1u << TT;
    auto pre = [&](GE& e) __attribute__((always_inline)) {
      fl_check<0>(gf, np0, tag);
      fl_check<1>(gf, np1, tag);
      att_init();
      const int d = doA ? j * 56 + cch : 0;
      asm volatile("global_load_ushort %0, %1, off" : "=v"(dv) : "v"(assmD + d) : "memory");
      e.sk = __builtin_amdgcn_make_buffer_rsrc((void*)(assmk + (size_t)d * SEQ), (short)0, SEQ * 2, 0x00020000);
      e.sx = __builtin_amdgcn_make_buffer_rsrc((void*)(xqT + (size_t)d * SEQ), (short)0, SEQ * 2, 0x00020000);
      e.vl = c.ka - lbase; e.vl0 = e.vl - 32; e.vq = doA ? 0x80000000u : e.vl; e.vk[0] = c.dw[0]; e.vk[1] = c.dw[1];
      e.mC = doA ? lbase + 65536 + wid * 15488 : 0x30000;
      e.mK = isY ? lbase + wid * 8192 : 0x30000;
      const unsigned q0 = oQ + ((solo && !isY ? pj : 15 - pj) * 128 + wid * 32) * 512;
      sset<96>(q0); sset<97>(q0);
      sset<98>(oK + cx * 32768);
    };
    if (kv) g_item<true, false>(g, none, pend, tag, oK + 0x1000000 + mt * 131072, wid, lane, gf, np0, np1, evx, start2, mid, pre);
    else g_item<false, false>(g, none, pend, tag, oK + mt * 131072, wid, lane, gf, np0, np1, evx, start2, mid, pre);
  }
  KST(0, 4);
  u64* pend2 = gflag;
  asm volatile("s_waitcnt vmcnt(%1)" : "+v"(dv) : "i"(W_CONV) : "memory");
  if (doA) {
    const float Dd = bf2f(dv);
    KST(0, 5);
    conv_wave(j * 56 + cch, Dd, xqT + (size_t)DA * SEQ, lds + 65536 + wid * 15488,
              [&](int cr) __attribute__((always_inline)) {
      if (cr == 8) {
        const char* tp = (const char*)wo + (size_t)(j + (cch >= 28 ? 8 : 0)) * (HD * DA * 2) + ((cch % 28) * 64 + lane) * 128;
        asm volatile("s_mov_b32 m0, %1\n\ts_nop 0\n\tglobal_load_lds_dword %0, off" ::"v"(tp), "s"(m0d) : "memory");
      }
      if (cr == 1) {
        asm volatile("s_waitcnt vmcnt(0)\n\ts_barrier" ::: "memory");
        set_flag(gflag, tag, wid);
      } else if (cr == 2) fl_issue<0>(gf, 0x0101ull << (hsel * 16));
      else if (cr >= 4 && cr < 8) {
        if (cr == 4) {
          asm volatile("s_waitcnt vmcnt(16)" ::: "memory");
          fl_check<0>(gf, 0x0101ull << (hsel * 16), tag);
        }
        dma4k(c, oK + ((cr >> 1) & 1) * 32768, lbase + ((cr >> 1) & 1) * 32768 + wid * 8192, cr & 1);
      }
    });
    KST(0, 6);
    pend2 = fw(gf, 100 + rr);
  } else if (!isY) {
    static_for([&](auto I) { d_store<16 + I>(); }, make_iseq<16>{});
    asm volatile("s_waitcnt vmcnt(0)\n\ts_barrier" ::: "memory");
    set_flag(gflag, tag, wid);
    KST(0, 5);
    wait_flags(gf, needA, tag);
    pro_k(0);
    asm volatile("s_waitcnt vmcnt(0)" ::: "memory");
    KST(0, 6);
  }
  c.va = c.ka + 65536;
  c.m0x[0] = lbase + 65536 + wid * 8192; c.m0x[1] = c.m0x[0] + 32768;
  const unsigned oP = (unsigned)(5 * TSZ * 2) + (h * 8 + pj) * 66560;
  const int qbF = isY ? 15 - pj : pj;
  constexpr unsigned oS = (unsigned)(3 * TSZ * 2);
  const int vml = 65536 + wid * 256 + (lane & 15) * 16;
  const int nitems = isY ? 1 : 2, it0 = !isY && solo ? 1 : 0;
#pragma unroll 1
  for (int it = it0; it < nitems; ++it) {
    int qb, t0, n, jmp, c0, c1;
    u64 n0, n1;
    if (isY) { qb = 15 - pj; t0 = cx; n = NY; jmp = ex; c1 = 14 - cx; n1 = needY; c0 = solo ? 30 : NY - 2; n0 = 1ull << (48 + hsel * 8 + pj); }
    else if (it == 0) { qb = 15 - pj; t0 = 0; n = nx0; jmp = 16 - cx; c1 = ex ? cx - 2 : 30; n1 = needX; c0 = 2; n0 = needA; }
    else { qb = pj; t0 = 0; n = 2 * pj + 2; jmp = 0; c1 = 30; c0 = 30; n0 = n1 = 0; }
    const bool last = isY || it == 1;
    c.mrow = qb * 128 + wid * 32 + (lane & 15) - 4 * (lane >> 4);
    static_for([&](auto I) { vset<R_RS + I>(0.f); }, make_iseq<6>{});
    vset<R_THR>(4096.f);
    if (!isY) d_none();
    c.diag0 = qb == 0;
    sset<S_T>(oK + t0 * 32768); sset<S_K>(oK + t0 * 32768 + 32768);
    const int ct = !last || isY ? 30 : n > 2 ? n - 3 : 0, cw = isY ? 30 : last ? (n > 2 ? 0 : 30) : n - 2;
    sset<S_EV>((3u << c0) | (3u << c1) | (last ? 3u << n >> 2 : 1u << n >> 1) | (isY ? 4u : 0u));
    if (it != it0) asm volatile("s_waitcnt vmcnt(16)" ::: "memory");
    else {
      if (doA) asm volatile("s_waitcnt vmcnt(6)" ::: "memory");
      asm volatile("s_waitcnt vmcnt(%0) lgkmcnt(0)\n\ts_barrier" ::"i"(W_K0) : "memory");
    }
    KST(0, 8 + 3 * it);
    kreads<0>(c, make_iseq<8>{});
    GAP();
    auto event = [&](int i) __attribute__((always_inline)) {
      if (i == c0) fl_check<0>(gf, n0, tag);
      if (i == c1) { fl_check<0>(gf, n1, tag); asm volatile("s_add_u32 s98, s98, %0" ::"s"(jmp * 32768) : "scc"); }
      if (i + 1 == c0 || i + 1 == c1) fl_issue<0>(gf, (i + 1 == c0 ? n0 : 0) | (i + 1 == c1 ? n1 : 0));
      if (i == cw) fl_issue<1>(gf, needW);
      if (i + 2 == n) {
        sset<S_K>(oK);
        if (last) {
          int l = threadIdx.x & 63; asm volatile("" : "+v"(l));
          const unsigned vw = (64 * wid + (l >> 3)) * (DA * 2) + 16 * ((l & 7) ^ ((l >> 3) & 6));
          static_for([&](auto Q) { vset<r_dk(Q)>(__uint_as_float(vw + (Q ^ 4) * (8 * DA * 2) - (Q & 3) * 1024)); }, make_iseq<8>{});
          set_srdk(wo + (size_t)h * HD * DA, HD * DA * 2);
          sset<S_K>(0); sset<S_I>(128);
        }
        if (isY && !solo)
          asm volatile("s_nop 4\n\tbuffer_load_dwordx4 v[122:125], %0, %1, %2 offen sc1" ::"v"(vml), "s"(c.sw), "s"(oP) : "memory");
      }
      if (i == ct) {
        CODE_TOUCH("tail_begin", "pf_code_end", r * 4 + wid, 64u, 8, m0f);
        {
          int l = threadIdx.x & 63; asm volatile("" : "+v"(l));
          const char* tp = (const char*)wo + (size_t)h * (HD * DA * 2) + ((pj * 4 + wid) * 56 + (l < 56 ? l : 0)) * 128;
          asm volatile("s_mov_b32 m0, %1\n\ts_nop 0\n\tglobal_load_lds_dword %0, off" ::"v"(tp), "s"(m0f) : "memory");
        }
        fl_check<1>(gf, needW, tag);
        post_dma<true>(j, r * 4 + wid, xqT + (size_t)DA * SEQ, aq, m0f);
      }
      if (last && i + 3 >= n && i + 1 < n && i) set_mask<R_CI, false>(c, (2 * qb + i + 3 - n) * 64);
      if (i == (isY ? 1 : 0)) set_flag(it != it0 ? fw(gf, 48 + hsel * 8 + pj) : pend2, tag, wid);
      asm volatile("s_nop 4");
      GAP();
    };
#define STA(k)
    STA(0);
    phaseA<0, true, false>(c);
    STA(1);
    event(0);
    STA(2);
    phaseB0(c);
    STA(3);
#pragma unroll 1
    for (int i = 1;; i += 2) {
      phaseA<1, false, false>(c);
      STA(9 + i);
      if (__builtin_expect(c.rare != 0, 0)) { if (c.redo) sm_redo<1>(); event(i); }
      phaseB<1>(c);
      if (i + 1 >= n) break;
      phaseA<0, false, false>(c);
      STA(10 + i);
      if (__builtin_expect(c.rare != 0, 0)) { if (c.redo) sm_redo<0>(); event(i + 1); }
      phaseB<0>(c);
    }
    STA(4);
    asm volatile("tail_begin:");
    c.rare = __builtin_amdgcn_readfirstlane(!last ? oQ + (pj * 128 + wid * 32) * 512 : oP + wid * 16384);
    c.vq = last && (!isY || solo) ? 0x80000000u : c.ka;
    asm volatile("s_nop 4" : "+s"(c.rare));
    phaseA<0, false, true>(c);
    if (__builtin_expect(c.redo != 0, 0)) sm_redo<0>();
    STA(5);
    KST(0, 9 + 3 * it);
    const float l[2] = {row_reduce<false>(vget<R_RS>() + vget<R_TS>()), row_reduce<false>(vget<R_RS + 1>() + vget<R_TS + 1>())};
    const float cm[2] = {vget<R_M>(), vget<R_M + 1>()};
    EP p;
    p.vo = c.dw[0] + wid * 8192;
#pragma unroll
    for (int e = 0; e < 4; ++e) p.so[e] = oP + e * 4096;
    {
      int lz = threadIdx.x & 63; asm volatile("" : "+v"(lz));
      const int rw = lz >> 2;
      static_for([&](auto E) { vset<R_TB + E>(__uint_as_float(lbase + rw * 128 + 16 * ((2 * E + ((lz & 3) >> 1)) ^ (rw & 6)) + 8 * (lz & 1))); }, make_iseq<4>{});
      const unsigned vs = ((lz >> 3) & 1) * 16384 + ((lz >> 4) & 1) * 512 + (lz >> 5) * 256 + (lz & 7) * 16;
      vset<R_VS>(__uint_as_float(vs)); vset<R_VS + 1>(__uint_as_float(vs + 32768));
      sset<96>(oS + qbF * 0x200000 + wid * 65536 + (j * 2 + hsel) * 1024); sset<97>(c.m0x[1]); sset<100>(0x8000);
    }
    if (!isY || solo) {
      p.fa[0] = __builtin_amdgcn_rcpf(l[0]); p.fa[1] = __builtin_amdgcn_rcpf(l[1]);
      if (!last) {
        __builtin_amdgcn_raw_buffer_store_b128(__builtin_bit_cast(u32x4, (f32x4){cm[0], l[0], cm[1], l[1]}), c.sw, vml, oP, 16);
        phaseF<0>(c, p, make_iseq<74>{});
      } else phaseF<1>(c, p, make_iseq<74>{});
    } else {
      const f32x4 ml = {vget<122>(), vget<123>(), vget<124>(), vget<125>()};
#pragma unroll
      for (int e = 0; e < 2; ++e) {
        const float mm = fmaxf(cm[e], ml[2 * e]);
        const float wa = __builtin_amdgcn_exp2f(cm[e] - mm), wb = ml[2 * e + 1] * __builtin_amdgcn_exp2f(ml[2 * e] - mm);
        const float inv = __builtin_amdgcn_rcpf(l[e] * wa + wb);
        p.fa[e] = wa * inv; p.fb[e] = wb * inv;
      }
      phaseF<2>(c, p, make_iseq<74>{});
    }
    KST(0, 10 + 3 * it);
  }
  const unsigned rdy = lbase + 163836;
  asm volatile("ds_write_b32 %0, %1" ::"v"(rdy), "v"(0) : "memory");
  {
    asm volatile("v_xor_b32 v%c1, s100, v%c1\n\ts_movk_i32 s99, 0x1000\np_loop:" : "+s"(c.sw) : "i"(R_TB) : "scc");
    GAP();
    p_half<0>(c, make_iseq<32>{});
    p_half<1>(c, make_iseq<32>{});
    asm volatile("s_cbranch_scc1 p_loop\n\ts_nop 5" ::"s"(c.sw));
    GAP();
    p_cvt<1, 0>(); p_cvt<1, 1>(); p_cvt<1, 2>(); p_cvt<1, 3>();
    p_store<1, 1>(c);
    GAP();
    KST(0, 27);
    u64* pf = fw(flags, 1024 + (qbF * 4 + wid) * 16 + j * 2 + hsel);
    asm volatile("s_waitcnt vmcnt(0)" : "+v"(pf) :: "memory");
    set_flag(pf, tag);
    KST(0, 30);
  }
#define HELP 0
  if (!isY) post_math(j, r * 4 + wid, out, lds + 131072 + wid * 8192);
  {
    int lz = threadIdx.x & 63; asm volatile("" : "+v"(lz));
    const int fr = lz & 15, fq = lz >> 4;
    {
      const int jj = 4 * (fr >> 3) + (fr & 3);
      const unsigned one = ((fq ^ (fr >> 2)) & 1) ? 0u : (jj & 1) ? 0x3f800000u : 0x3f80u;
      static_for([&](auto I) { vset<96 + I>(__uint_as_float((jj >> 1) == I ? one : 0u)); }, make_iseq<4>{});
    }
    const unsigned vo = (fq >> 1) * 1024 + (fq & 1) * 256 + fr * 16, vo0 = isY && wid >= 4 - HELP ? 0x80000000u : vo, vo2 = wid >= 4 - HELP ? vo : 0x80000000u, vo1 = wid < 3 ? vo + 0x100000 : 0x80000000u;
    const __amdgpu_buffer_rsrc_t so_ = __builtin_amdgcn_make_buffer_rsrc((void*)out, (short)0, SEQ * OUTW * 2, 0x00020000);
    const unsigned vw = (((h & 3) * 32 + (h >> 2) * 8 + (fr & 7)) * OUTW + DA + wid * 64 + (fr >> 3) * 32 + fq * 8) * 2;
    const u64* q = fw(flags, 1024 + ((fq & 1 ? 15 - pj : qbF) * 4 + (h & 3)) * 16) + fr;
    unsigned so[2];
#pragma unroll
    for (int u = 0; u < 2; ++u)
      so[u] = __builtin_amdgcn_readfirstlane(oS + (u ? 15 - pj : qbF) * 0x200000 + (1 + wid) * 0x40000 + (h & 3) * 65536 + (h >> 2) * 16384);
    auto fin0 = [&]() __attribute__((always_inline)) {
      if (wid < 3) r_fin<1>(vw, so_, qbF * 128 * OUTW * 2 + 512);
      if (!isY || wid < 4 - HELP) r_fin<0>(vw, so_, qbF * 128 * OUTW * 2);
      KST(0, 16);
    };
    unsigned pend = isY || wid < 4 - HELP ? 0x5555u : 0x55555555u, cnt = 0x400000;
    bool done0 = false;
    asm volatile("s_waitcnt lgkmcnt(0)" : "+s"(so[0]), "+s"(so[1]), "+s"(c.sw));
    KST(0, 28);
#define PSL 4
    while (true) {
      unsigned nw;
      if (wid == 3) {
        u64 m, v;
        const u64 ex = pend | (pend << 1);
        asm volatile("s_mov_b64 exec, %4\n\tglobal_load_dwordx2 %1, %2, off sc1\n\ts_waitcnt vmcnt(0)\n\tv_cmp_eq_u64 %0, %3, %1\n\ts_mov_b64 exec, -1"
                     : "=&s"(m), "=&v"(v) : "v"(q), "s"(tag), "s"(ex) : "memory");
        nw = (unsigned)m & ((unsigned)m >> 1) & pend;
        if (nw) asm volatile("ds_write_b32 %0, %1" ::"v"(rdy), "v"(~pend | nw) : "memory");
      } else {
        unsigned t;
        asm volatile("ds_read_b32 %0, %1\n\ts_waitcnt lgkmcnt(0)" : "=v"(t) : "v"(rdy) : "memory");
        nw = __builtin_amdgcn_readfirstlane(t) & pend;
      }
      if (--cnt == 0) nw = pend;
      if (nw == 0) {
        if (!done0 && !(pend & 0xffff)) {
          asm volatile("s_waitcnt vmcnt(0)" ::: "memory");
          fin0(); done0 = true;
        } else if (wid == 3) asm volatile("s_sleep %0" ::"i"(PSL));
        else asm volatile("s_sleep 1");
        continue;
      }
      asm volatile("s_nop 4");
      static_for([&](auto S) {
        constexpr int K = 2 * (S & 7);
        if ((nw >> (2 * S)) & 1) {
          if constexpr (S < 8) { r_load<0, K>(c, vo0, so[0]); r_load<0, K + 1>(c, vo0, so[0]); r_load<1, K>(c, vo1, so[0]); r_load<1, K + 1>(c, vo1, so[0]); }
          else { r_load<2, K>(c, vo2, so[1]); r_load<2, K + 1>(c, vo2, so[1]); }
        }
      }, make_iseq<16>{});
      pend &= ~nw;
      if (pend == 0) break;
    }
    asm volatile("s_waitcnt vmcnt(0)" ::: "memory");
    if (!done0) fin0();
    if (!isY && wid >= 4 - HELP) r_fin<2>(vw, so_, (15 - pj) * 128 * OUTW * 2);
    KST(0, 29);
    set_flag(fw(flags, 6144), tag, (b ^ 128) | wid);
  }
  asm volatile("s_waitcnt vmcnt(0)" ::: "memory");
  asm volatile("pf_code_end:");
  KST(0, 1);
}

extern "C" void kernel_launch(void* const* d_in, const int* in_sizes, int n_in, void* d_out, int out_size, void* d_ws,
                              size_t ws_size, hipStream_t stream) {
  const bf16* Aq = (const bf16*)d_in[0];
  const bf16* Ak = (const bf16*)d_in[1];
  const bf16* Av = (const bf16*)d_in[2];
  const bf16* Ash = (const bf16*)d_in[3];
  const bf16* Assmk = (const bf16*)d_in[4];
  const bf16* AssmD = (const bf16*)d_in[5];
  const bf16* Bx = (const bf16*)d_in[6];
  const bf16* Bckv = (const bf16*)d_in[7];
  const bf16* Bwq = (const bf16*)d_in[8];
  const bf16* Bwuk = (const bf16*)d_in[9];
  const bf16* Bwuv = (const bf16*)d_in[10];
  const bf16* Bwo = (const bf16*)d_in[11];
  bf16* out = (bf16*)d_out;
  hipFuncSetAttribute((const void*)fused_kernel, hipFuncAttributeMaxDynamicSharedMemorySize, 163840);
  fused_kernel<<<256, 256, 163840, stream>>>(Bckv, Bwuk, Bwuv, Bx, Bwq, (bf16*)d_ws, Ak, Av, Ash, Assmk, AssmD, Bwo, Aq, out);
}
```

```cpp
#include <hip/hip_runtime.h>
#include <hip/hip_bf16.h>
#include <stdint.h>
#include <utility>

typedef __hip_bfloat16 bf16;
typedef __attribute__((ext_vector_type(4))) short bf16x4;
typedef __attribute__((ext_vector_type(2))) unsigned u32x2;

constexpr int SEQ = 2048;
constexpr int DA = 448;
constexpr int NH = 16;
constexpr int HD = 256;
constexpr int DC = 1024;
constexpr int OUTW = 896;
constexpr unsigned TSB = NH * SEQ * HD * 2, O_K = TSB, O_S = 3 * TSB, O_P = 5 * TSB, PSTR = 67584, O_F = O_P + 128 * PSTR,
                   O_X = O_F + 400 * 4352, O_Z = O_X + DA * SEQ * 2, WSB = O_Z + DA * SEQ * 2;
constexpr unsigned fo(int w) { return O_F + (w >> 4) * 4352 + (w & 15) * 8; }

#define KST(kid, which)

__device__ __forceinline__ float bf2f(unsigned short u) { return __uint_as_float(((unsigned)u) << 16); }
__device__ __forceinline__ unsigned short f2bf(float f) { return __bfloat16_as_ushort(__float2bfloat16(f)); }
__device__ __forceinline__ float rbf(float f) { return bf2f(f2bf(f)); }
typedef __attribute__((ext_vector_type(2))) __bf16 bf16x2n;
typedef __attribute__((ext_vector_type(2))) float f32x2n;
__device__ __forceinline__ unsigned pk2(float a, float b) {
  return __builtin_bit_cast(unsigned, __builtin_convertvector((f32x2n){a, b}, bf16x2n));
}

__device__ __forceinline__ int off_b(int row, int ch) { return 256 * row + 16 * (ch ^ (((row & 3) << 2) | ((row >> 2) & 3))); }

template <int R> __device__ __forceinline__ void sset(unsigned x) { asm volatile("s_mov_b32 s%c0, %1" ::"i"(R), "s"(x)); }
template <int R> __device__ __forceinline__ void vsetu(unsigned x) { asm volatile("v_mov_b32 v%c0, %1" ::"i"(R), "v"(x)); }
template <int R> __device__ __forceinline__ void srd_set(const void* p, unsigned n) {
  asm volatile("s_mov_b64 s[%c0:%c1], %4\n\ts_mov_b32 s%c2, %5\n\ts_mov_b32 s%c3, 0x20000" ::"i"(R), "i"(R + 1), "i"(R + 2), "i"(R + 3), "s"(p), "s"(n));
}
template <int R, int L> __device__ __forceinline__ void tload() { asm volatile("v_readlane_b32 s%c0, v78, %1" ::"i"(R), "i"(L)); }
template <int L> __device__ __forceinline__ unsigned tget() { unsigned t; asm volatile("v_readlane_b32 %0, v78, %1" : "=s"(t) : "i"(L)); return t; }
#define FL_SET(S) "buffer_store_dwordx2 v[82:83], v81, s[88:91], " S " offen sc1"
#define FL_ARM(N) "v_cndmask_b32_e64 v80, -2.0, v79, " N
#define FL_POLL "buffer_load_dwordx2 v[124:125], v80, s[88:91], 0 offen sc1"
#define FL_CHECK(N) "s_mov_b32 s48, 0x100000\n7:\n\tv_cmp_eq_u64 vcc, s[84:85], v[124:125]\n\ts_and_b64 vcc, vcc, " N "\n\ts_cmp_eq_u64 vcc, " N \
                    "\n\ts_cbranch_scc1 8f\n\ts_sleep 2\n\t" FL_POLL "\n\ts_waitcnt vmcnt(0)\n\ts_sub_u32 s48, s48, 1\n\ts_cbranch_scc0 7b\n8:"
template <int L> __device__ __forceinline__ void fl_set() {
  asm volatile("v_readlane_b32 s48, v78, %0\n\ts_nop 4\n\t" FL_SET("s48") ::"i"(L) : "memory");
}
template <int L> __device__ __forceinline__ void fl_poll() {
  asm volatile("v_readlane_b32 s50, v78, %0\n\tv_readlane_b32 s51, v78, %1\n\ts_nop 1\n\t" FL_ARM("s[50:51]") "\n\t" FL_POLL ::"i"(L), "i"(L + 1) : "memory");
}
__device__ __forceinline__ void fl_check() { asm volatile(FL_CHECK("s[50:51]") ::: "vcc", "scc", "memory"); }
template <int R> __device__ __forceinline__ float vget() { float t; asm volatile("v_mov_b32 %0, v%c1" : "=v"(t) : "i"(R)); return t; }
template <int R> __device__ __forceinline__ void vset(float x) { asm volatile("v_mov_b32 v%c0, %1" ::"i"(R), "v"(x)); }
template <int R> __device__ __forceinline__ float acc_read1() { float t; asm volatile("v_accvgpr_read_b32 %0, a[%c1]" : "=v"(t) : "i"(R)); return t; }
template <class F, int... I> __device__ __forceinline__ void static_for(F&& f, std::integer_sequence<int, I...>) {
  (f(std::integral_constant<int, I>{}), ...);
}
template <int... Is> using iseq = std::integer_sequence<int, Is...>;
template <int N> using make_iseq = std::make_integer_sequence<int, N>;

typedef __attribute__((ext_vector_type(4))) unsigned u32x4c;
typedef __attribute__((ext_vector_type(4))) float f32x4c;
typedef __attribute__((ext_vector_type(2))) float f32x2c;
typedef unsigned long long u64;
struct PR { u32x4c kk[7], vv[4]; };
__device__ __forceinline__ void prep_load(PR& p, int tid, int cg, int tb, const bf16* __restrict__ k, const bf16* __restrict__ v,
                                          const bf16* __restrict__ shift_k) {
  const __amdgpu_buffer_rsrc_t sk = __builtin_amdgcn_make_buffer_rsrc((void*)k, (short)0, SEQ * DA * 2, 0x00020000);
  const __amdgpu_buffer_rsrc_t sv = __builtin_amdgcn_make_buffer_rsrc((void*)v, (short)0, SEQ * DA * 2, 0x00020000);
  const __amdgpu_buffer_rsrc_t sw = __builtin_amdgcn_make_buffer_rsrc((void*)(shift_k + cg * 256), (short)0, 512, 0x00020000);
  const int vo = ((tb * 128 + (tid >> 3) * 4) * DA + cg * 64 + (tid & 7) * 8) * 2;
#pragma unroll
  for (int q = 0; q < 7; ++q)
    asm volatile("buffer_load_dwordx4 %0, %1, %2, 0 offen" : "=v"(p.kk[q]) : "v"(vo + (q - 3) * DA * 2), "s"(sk) : "memory");
#pragma unroll
  for (int q = 0; q < 4; ++q)
    asm volatile("buffer_load_dwordx4 %0, %1, %2, 0 offen offset:%3" : "=v"(p.vv[q]) : "v"(vo), "s"(sv), "i"(q * DA * 2) : "memory");
#pragma unroll
  for (int q = 0; q < 4; ++q)
    asm volatile("buffer_load_dwordx4 a[%c0:%c1], %2, %3, 0 offen offset:%4" ::"i"(4 * q), "i"(4 * q + 3), "v"((tid & 7) * 64), "s"(sw), "i"(q * 16) : "memory");
}
__device__ __forceinline__ float bfl(unsigned u) { return __uint_as_float(u << 16); }
__device__ __forceinline__ float bfh(unsigned u) { return __uint_as_float(u & 0xffff0000u); }
__device__ __forceinline__ f32x2c rbf2(f32x2c a) { const unsigned u = pk2(a[0], a[1]); return (f32x2c){bfl(u), bfh(u)}; }
template <int I> __device__ __forceinline__ void prep_math(const PR& p, int tid, char* lx) {
  const int rp = tid >> 3, c = tid & 7;
  f32x2c wt[4], kf[7];
  const unsigned w[4] = {__float_as_uint(acc_read1<4 * I>()), __float_as_uint(acc_read1<4 * I + 1>()), __float_as_uint(acc_read1<4 * I + 2>()),
                         __float_as_uint(acc_read1<4 * I + 3>())};
#pragma unroll
  for (int t = 0; t < 4; ++t)
    wt[t] = (t & 1) ? (f32x2c){bfh(w[t >> 1]), bfh(w[2 + (t >> 1)])} : (f32x2c){bfl(w[t >> 1]), bfl(w[2 + (t >> 1)])};
#pragma unroll
  for (int q = 0; q < 7; ++q) kf[q] = (f32x2c){bfl(p.kk[q][I]), bfh(p.kk[q][I])};
  f32x2c x[4];
#pragma unroll
  for (int e = 0; e < 4; ++e) {
    f32x2c s = rbf2(kf[e] * wt[3]);
    s = rbf2(s + rbf2(kf[e + 1] * wt[2]));
    s = rbf2(s + rbf2(kf[e + 2] * wt[1]));
    s = rbf2(s + rbf2(kf[e + 3] * wt[0]));
    x[e] = s * (f32x2c){bfl(p.vv[e][I]), bfh(p.vv[e][I])};
  }
#pragma unroll
  for (int u = 0; u < 2; ++u)
    *(u32x2*)(lx + (c * 8 + 2 * I + u) * 256 + (((31 - rp) * 8) ^ (c << 4))) = (u32x2){pk2(x[3][u], x[2][u]), pk2(x[1][u], x[0][u])};
}
__device__ __forceinline__ void prep_store(int tid, int cg, int tb, bf16* __restrict__ xr, const char* lx) {
  const __amdgpu_buffer_rsrc_t sx = __builtin_amdgcn_make_buffer_rsrc((void*)(xr + (size_t)cg * 64 * SEQ), (short)0, 64 * SEQ * 2, 0x00020000);
#pragma unroll
  for (int rd = 0; rd < 4; ++rd) {
    const int idx = tid + 256 * rd, ch = idx >> 4, q = idx & 15;
    u32x4c w = *(const u32x4c*)(lx + ch * 256 + 16 * (q ^ (ch >> 3)));
    __builtin_amdgcn_raw_buffer_store_b128(w, sx, (ch * SEQ + (15 - tb) * 128 + q * 8) * 2, 0, 16);
  }
}

#define CODE_TOUCH(LB, LE, wx, NW, NL, m0v) do { unsigned blo_, bhi_, elo_; \
    asm volatile("s_getpc_b64 s[48:49]\n\ts_add_u32 s48, s48, " LB "@rel32@lo+4\n\ts_addc_u32 s49, s49, " LB "@rel32@hi+12\n\t" \
                 "s_mov_b32 %0, s48\n\ts_mov_b32 %1, s49\n\t" \
                 "s_getpc_b64 s[48:49]\n\ts_add_u32 s48, s48, " LE "@rel32@lo+4\n\ts_mov_b32 %2, s48" \
                 : "=s"(blo_), "=s"(bhi_), "=s"(elo_) :: "scc"); \
    const unsigned n_ = elo_ - (blo_ & ~127u); \
    const __amdgpu_buffer_rsrc_t srd_ = __builtin_amdgcn_make_buffer_rsrc((void*)(((unsigned long long)bhi_ << 32) | (blo_ & ~127u)), (short)0, n_ < 262144u ? n_ : 0u, 0x00020000); \
    const unsigned l_ = threadIdx.x & 63, vo_ = l_ < (NL) ? ((wx) + l_ * (NW)) * 128u : 0x80000000u; \
    asm volatile("s_mov_b32 m0, %0\n\ts_nop 4\n\tbuffer_load_dword %1, %2, 0 offen lds" ::"s"(m0v), "v"(vo_), "s"(srd_) : "memory"); } while (0)
template <int I> __device__ __forceinline__ void d_store() {
  asm volatile("buffer_store_dwordx4 v[%c0:%c1], v%c2, s[88:91], s101 offen offset:%3 sc1" ::"i"(128 + 4 * I), "i"(131 + 4 * I), "i"(112 + ((I >> 2) & 1)),
               "i"((I & 3) * 1024) : "memory");
  if constexpr ((I & 7) == 7) asm volatile("s_addk_i32 s101, 0x4000" ::: "scc");
}
struct CV { unsigned pa[4], pb; };
constexpr int cv_J(int q) { int J = 7; while (q > J) { q -= J + 1; --J; } return J; }
constexpr int cv_i(int q) { int J = 7; while (q > J) { q -= J + 1; --J; } return q; }
template <int R, int N> __device__ __forceinline__ void cv_rd(CV& c) {
  if constexpr (N < 16) {
    constexpr int i = N >> 1, o = 16 * R + 128 * (i & 1) + 2 * (N & 1), r = 32 + 32 * (R & 1) + 2 * N;
    asm volatile("ds_read2_b32 a[%c1:%c2], %0 offset0:%c3 offset1:%c4" ::"v"(c.pa[i >> 1]), "i"(r), "i"(r + 1), "i"(o), "i"(o + 1));
  } else {
    constexpr int m = N - 16, r = 192 + 32 * (R & 1) + 4 * m;
    asm volatile("ds_read_b128 a[%c1:%c2], %0 offset:%c3" ::"v"(c.pb), "i"(r), "i"(r + 3), "i"(64 * R + 512 * (7 - m)));
  }
}
template <int R, int Q, int MODE> __device__ __forceinline__ void cv_slot(CV& c) {
  constexpr int i = cv_i(Q), J = cv_J(Q), d = 4 * J, a = 32 + 32 * (R & 1) + 4 * i, b = 192 + 32 * (R & 1) + 4 * (J - i);
  if constexpr (Q == 0) asm volatile("s_waitcnt lgkmcnt(0)");
  if constexpr (MODE == 1 && R == 0 && i == 0)
    asm volatile("v_mfma_f32_16x16x32_bf16 a[%c0:%c1], a[%c2:%c3], a[%c4:%c5], 0" ::"i"(d), "i"(d + 3), "i"(a), "i"(a + 3), "i"(b), "i"(b + 3));
  else
    asm volatile("v_mfma_f32_16x16x32_bf16 a[%c0:%c1], a[%c2:%c3], a[%c4:%c5], a[%c0:%c1]" ::"i"(d), "i"(d + 3), "i"(a), "i"(a + 3), "i"(b), "i"(b + 3));
  if constexpr (Q < 24 && !(MODE == 2 && R == 1)) cv_rd<R + 1, Q>(c);
  if constexpr (MODE == 1 && Q % 7 == 3) d_store<22 + 5 * R + Q / 7>();
  if constexpr (MODE == 2 && (R == 0 ? Q >= 24 && Q % 3 == 0 : Q % 3 == 0)) {
    constexpr int S_ = R == 0 ? (Q - 24) / 3 : 4 + Q / 3;
    asm volatile("buffer_load_dwordx4 a[%c0:%c1], v84, s[88:91], s96 offen offset:%2 sc1" ::"i"(128 + 4 * S_), "i"(128 + 4 * S_ + 3),
                 "i"((S_ & 3) * 1024) : "memory");
    if constexpr ((S_ & 3) == 3) asm volatile("s_addk_i32 s96, 4096" ::: "scc");
  }
  __builtin_amdgcn_sched_barrier(0);
}
template <int R, int MODE, int... Q> __device__ __forceinline__ void cv_step(CV& c, iseq<Q...>) { (cv_slot<R, Q, MODE>(c), ...); }
template <int... N> __device__ __forceinline__ void cv_rd0(CV& c, iseq<N...>) { (cv_rd<0, N>(c), ...); }
template <class HOOK>
__device__ __forceinline__ void conv_wave(int d, float Dd, bf16* __restrict__ zT, char* lw, HOOK&& hook) {
  int lane = threadIdx.x & 63; asm volatile("" : "+v"(lane));
  const int l16 = lane & 15, fq = lane >> 4;
  u32x4c w[5]; unsigned w4[5];
#pragma unroll
  for (int r = 0; r < 5; ++r) {
    w[r] = *(const u32x4c*)(lw + r * 1024 + lane * 16);
    w4[r] = *(const unsigned*)(lw + r * 1024 + lane * 16 + 16);
  }
  d_store<16>(); d_store<17>();
#pragma unroll
  for (int r = 0; r < 5; ++r)
    *(u32x4c*)(lw + 10304 + r * 1024 + lane * 16) = (u32x4c){__builtin_amdgcn_alignbit(w[r][1], w[r][0], 16), __builtin_amdgcn_alignbit(w[r][2], w[r][1], 16),
                                                             __builtin_amdgcn_alignbit(w[r][3], w[r][2], 16), __builtin_amdgcn_alignbit(w4[r], w[r][3], 16)};
  d_store<18>(); d_store<19>();
  CV c;
#pragma unroll
  for (int i = 0; i < 4; ++i) c.pa[i] = (unsigned)(size_t)lw + ((l16 + 1) & 1) * 10304 + 4 * ((8 * fq + l16 + 1) >> 1) + 1024 * i;
  c.pb = (unsigned)(size_t)lw + 5120 + (2032 - 16 * l16 + 8 * fq) * 2 - 512 * 7;
  asm volatile("" ::: "memory");
  cv_rd0(c, make_iseq<24>{});
  d_store<20>(); d_store<21>();
  __builtin_amdgcn_sched_barrier(0);
  cv_step<0, 1>(c, make_iseq<36>{});
  cv_step<1, 1>(c, make_iseq<36>{});
#pragma unroll 1
  for (int r = 1; r < 3; ++r) {
#pragma unroll
    for (int i = 0; i < 4; ++i) c.pa[i] += 128;
    c.pb += 128;
    __builtin_amdgcn_sched_barrier(0);
    cv_step<0, 0>(c, make_iseq<36>{});
    cv_step<1, 0>(c, make_iseq<36>{});
    hook(r);
  }
#pragma unroll
  for (int i = 0; i < 4; ++i) c.pa[i] += 128;
  c.pb += 128;
  __builtin_amdgcn_sched_barrier(0);
  cv_step<0, 2>(c, make_iseq<36>{});
  cv_step<1, 2>(c, make_iseq<36>{});
  const __amdgpu_buffer_rsrc_t srdZ = __builtin_amdgcn_make_buffer_rsrc((void*)(zT + (size_t)d * SEQ), (short)0, SEQ * 2, 0x00020000);
  asm volatile("s_nop 7");
  static_for([&](auto Jc) __attribute__((always_inline)) {
    constexpr int J = Jc;
    hook(4 + J);
    __builtin_amdgcn_sched_barrier(0);
    const int t = 256 * J + 16 * l16 + 4 * fq;
    bf16x4 xv = *(const bf16x4*)(lw + 5120 + (2044 - t) * 2);
    const float y[4] = {acc_read1<4 * J>(), acc_read1<4 * J + 1>(), acc_read1<4 * J + 2>(), acc_read1<4 * J + 3>()};
    float z[4];
#pragma unroll
    for (int e = 0; e < 4; ++e) z[e] = rbf(y[e]) + rbf(bf2f(xv[3 - e]) * Dd);
    u32x2 w = {pk2(z[0], z[1]), pk2(z[2], z[3])};
    __builtin_amdgcn_raw_buffer_store_b64(w, srdZ, t * 2, 0, 16);
  }, make_iseq<8>{});
}

__device__ __forceinline__ void post_dma(int cg, int tb, const bf16* __restrict__ q, unsigned m0w) {
  int lane = threadIdx.x & 63; asm volatile("" : "+v"(lane));
  const __amdgpu_buffer_rsrc_t sq = __builtin_amdgcn_make_buffer_rsrc((void*)(q + (size_t)tb * 32 * DA + cg * 64), (short)0, 32 * DA * 2, 0x00020000);
  const unsigned vq = ((lane >> 3) * DA + (lane & 7) * 8) * 2;
  asm volatile("s_mov_b32 m0, %0\n\ts_nop 4\n\tbuffer_load_dwordx4 %1, %2, 0 offen lds\n\tbuffer_load_dwordx4 %1, %2, %3 offen offset:1024 lds\n\t"
               "buffer_load_dwordx4 %1, %2, %4 offen offset:2048 lds\n\tbuffer_load_dwordx4 %1, %2, %5 offen offset:3072 lds"
               ::"s"(m0w + 4096), "v"(vq), "s"(sq), "s"(8 * DA * 2 - 1024), "s"(16 * DA * 2 - 2048), "s"(24 * DA * 2 - 3072) : "memory");
}
__device__ __forceinline__ void post_math(int cg, int tb, bf16* __restrict__ out, const char* lw) {
  int lane = threadIdx.x & 63; asm volatile("" : "+v"(lane));
  const int tl = lane >> 3, ck = lane & 7;
  const __amdgpu_buffer_rsrc_t so = __builtin_amdgcn_make_buffer_rsrc((void*)(out + (size_t)tb * 32 * OUTW + cg * 64), (short)0, 32 * OUTW * 2 - 2 * DA, 0x00020000);
  const unsigned vo = (tl * OUTW + ck * 8) * 2;
  const char* lz = lw + ck * 16 + tl * 2;
#pragma unroll
  for (int i = 0; i < 4; ++i) {
    const u32x4c qv = *(const u32x4c*)(lw + 4096 + i * 1024 + lane * 16);
    u32x4c o;
#pragma unroll
    for (int e = 0; e < 4; ++e) {
      const float z0 = bf2f(*(const unsigned short*)(lz + i * 1024 + 2 * e * 128));
      const float z1 = bf2f(*(const unsigned short*)(lz + i * 1024 + (2 * e + 1) * 128));
      o[e] = pk2(bfl(qv[e]) * z0, bfh(qv[e]) * z1);
    }
    __builtin_amdgcn_raw_buffer_store_b128(o, so, vo, i * 8 * OUTW * 2, 0);
  }
}

#define DI __device__ __forceinline__
#define GAP() __builtin_amdgcn_sched_barrier(0)
typedef __attribute__((ext_vector_type(4))) float f32x4g;
typedef __attribute__((ext_vector_type(4))) unsigned u32x4g;
constexpr int LDW = NH * HD;
struct GM { unsigned aA[2], aB[2][8]; };
template <bool W, int J, int V> DI void g_dma() {
  if constexpr (J == 0) asm volatile("buffer_load_dwordx4 v%c0, s[%c1:%c2], 0 offen lds" ::"i"(V), "i"(W ? 60 : 56), "i"(W ? 63 : 59) : "memory");
  else asm volatile("buffer_load_dwordx4 v%c0, s[%c1:%c2], s%c3 offen lds" ::"i"(V), "i"(W ? 60 : 56), "i"(W ? 63 : 59), "i"((W ? 70 : 63) + J) : "memory");
}
template <int P, int KS, int I> DI void g_wread(GM& g) {
  constexpr int nf = I >> 1, hf = I & 1, r = 192 - 64 * P + 8 * nf + 4 * KS + 2 * hf;
  asm volatile("ds_read_b64_tr_b16 a[%c1:%c2], %0 offset:%c3" ::"v"(g.aB[hf][nf]), "i"(r), "i"(r + 1), "i"(P * 32768 + KS * 16384));
}
template <int P, int M, int KS> DI void g_aread(GM& g) {
  constexpr int r = 96 + 8 * (M & 1) + 4 * KS;
  asm volatile("ds_read_b128 v[%c1:%c2], %0 offset:%c3" ::"v"(g.aA[KS]), "i"(r), "i"(r + 3), "i"(P * 32768 + M * 2048));
}
constexpr float CS = 0.0625f * 1.4426950408889634f;
struct ES { int op0[129], nop[129]; };
constexpr ES e_sched(bool TR, bool SCL) {
  ES z{};
  const int per = SCL ? 16 : 8;
  int o = 0;
  for (int G = 0; G <= 128; ++G) {
    int cap = G == 128 ? 10000 : SCL ? 4 : 2;
    z.op0[G] = o;
    while (o < 32 * per) {
      const int i = o / per, rdy = (TR ? 32 * (i >> 3) + 16 + 2 * (i & 7) + 1 : 4 * i + 3) + 4;
      if ((G < rdy && G < 128) || cap-- <= 0) break;
      ++o; ++z.nop[G];
    }
  }
  return z;
}
template <bool TR, bool SCL> constexpr ES ESCH = e_sched(TR, SCL);
constexpr int e_vm(int G) { return (G * 3) % 8 < 3 ? G * 3 / 8 : -1; }
constexpr int e_st(int MODE, int G) {
  if (MODE == 5) return e_vm(G) >= 32 ? e_vm(G) - 32 : -1;
  const int g0 = MODE == 4 ? 5 : MODE == 2 ? 13 : 34, gs = MODE == 2 ? 7 : 6;
  return MODE >= 2 && G >= g0 && (G - g0) % gs == 0 && (G - g0) / gs < 16 ? (G - g0) / gs + (MODE == 4 ? 16 : 0) : -1;
}
constexpr int e_nst(int MODE, int G0, int G1) { int n = 0; for (int G = G0; G < G1; ++G) n += e_st(MODE, G) >= 0; return n; }
static_assert(e_nst(2, 0, 128) == 16 && e_nst(3, 0, 128) == 16 && e_nst(5, 0, 128) == 16 && e_nst(4, 0, 104) == 16);
template <bool TR, bool SCL, int O> DI void e_op() {
  constexpr int per = SCL ? 16 : 8, i = O / per, k = O % per, r = 128 + 4 * i, t = 114 + 4 * (i & 1), c = k - (per - 4);
  if constexpr (k < 4) asm volatile("v_accvgpr_read_b32 v%c0, a[%c1]" ::"i"(t + k), "i"(4 * i + k));
  else if constexpr (c < 0) asm volatile("v_mul_f32 v%c0, %1, v%c0" ::"i"(k < 8 ? r + (k ^ 6) : t + k - 8), "s"(CS));
  else {
    constexpr int d = c == 0 ? r + 3 : c == 1 ? r + 2 : r + c - 2, x = c == 0 ? r + 2 : c == 1 ? r : t + 2 * (c - 2);
    asm volatile("v_cvt_pk_bf16_f32 v%c0, v%c1, v%c2" ::"i"(d), "i"(x), "i"(x + 1));
  }
}
template <bool TR, bool SCL, int B, int... Is> DI void e_ops(iseq<Is...>) { (e_op<TR, SCL, B + Is>(), ...); }
constexpr int e_slot(int MODE, int G) { return MODE == 3 ? (G % 3 == 0 && G < 30 ? G / 3 : -1) : e_vm(G) >= 0 && e_vm(G) < 32 ? 10 + e_vm(G) : -1; }
template <int S> DI void e_m0() {
  if constexpr (S == 0 || S == 1 || S == 5 || S == 9) asm volatile("s_add_u32 m0, s78, %0" ::"i"(S * 1024));
  if constexpr (S >= 26 && (S - 26) % 4 == 0) asm volatile("s_add_u32 m0, s79, %0" ::"i"(((S - 26) >> 3) * 32768 + ((S - 26) & 4) * 1024));
}
template <int S> DI void e_load() {
  if constexpr (S == 0) asm volatile("buffer_load_dwordx4 v122, s[56:59], 0 offen lds" ::: "memory");
  else if constexpr (S < 5) asm volatile("buffer_load_dwordx4 v84, s[56:59], s64 offen offset:%0 lds" ::"i"((S - 1) * 1024) : "memory");
  else if constexpr (S < 9) asm volatile("buffer_load_dwordx4 v84, s[60:63], 0 offen offset:%0 sc1 lds" ::"i"((S - 5) * 1024) : "memory");
  else if constexpr (S == 9) asm volatile("buffer_load_dwordx4 v84, s[60:63], s62 offen sc1 lds" ::: "memory");
  else if constexpr (S < 26) {
    constexpr int f = S - 10;
    asm volatile("buffer_load_dwordx4 a[%c0:%c1], v84, s[88:91], s96 offen offset:%2 sc1" ::"i"(128 + 4 * f), "i"(131 + 4 * f), "i"((f & 3) * 1024) : "memory");
    if constexpr ((f & 3) == 3) asm volatile("s_addk_i32 s96, 4096");
  } else {
    constexpr int f = S - 26;
    asm volatile("buffer_load_dwordx4 v%c0, s[88:91], s98 offen offset:%1 sc1 lds" ::"i"(86 + ((f >> 2) & 1)), "i"((f & 3) * 1024) : "memory");
    if constexpr (f == 7) asm volatile("s_add_u32 s98, s98, 0x8000");
    if constexpr (f == 15) asm volatile(FL_POLL ::: "memory");
  }
}
template <bool TR, int P, int H, int G, int MODE> DI void g_gap(GM& g) {
  constexpr int m = 4 * H + (G >> 4), q = G & 15, n = q >> 1, ks = q & 1, GG = 64 * H + G;
  constexpr bool inV = TR ? m & 1 : n & 1, END = MODE == 3 || MODE == 5, EPI = END || MODE == 2;
  constexpr int a = 96 + 8 * (m & 1) + 4 * ks, b = 192 - 64 * P + 8 * n + 4 * ks, d = (inV ? 128 : 0) + 4 * (TR ? 8 * (m >> 1) + n : 4 * m + (n >> 1));
  constexpr int J = END ? -1 : q == 8 ? 2 * (G >> 4) : q == 12 ? 2 * (G >> 4) + 1 : -1, gj = (J & 3) ^ ((J & 3) >> 1);
  if constexpr (END) { if constexpr (q == 0 && GG > 0) asm volatile("s_waitcnt lgkmcnt(0)"); e_m0<e_slot(MODE, GG)>(); }
  else if constexpr (q == 0 && G > 0) asm volatile("s_waitcnt lgkmcnt(4)");
  if constexpr (J == 0) asm volatile("s_add_u32 m0, s78, %0" ::"i"((P ^ H ^ 1) * 32768 + (1 - H) * 8192));
  else if constexpr (J == 4) asm volatile("s_add_u32 m0, s79, %0" ::"i"((P ^ H) * 32768 + (1 - H) * 16384));
  else if constexpr (J > 0 && (J & 3) == 1) asm volatile("s_bitset1_b32 m0, 12");
  else if constexpr (J > 0 && (J & 3) == 2) asm volatile("s_bitset1_b32 m0, %0" ::"i"(J < 4 ? 14 : 13));
  else if constexpr (J > 0) asm volatile("s_bitset0_b32 m0, 12");
#define GMF(D, X, Y, C) asm volatile("v_mfma_f32_16x16x32_bf16 " D "[%c0:%c1], " X ", " Y ", " C ::"i"(d), "i"(d + 3), "i"(a), "i"(a + 3), "i"(b), "i"(b + 3))
#define GMF2(D, C) do { if constexpr (TR) GMF(D, "v[%c2:%c3]", "a[%c4:%c5]", C); else GMF(D, "a[%c4:%c5]", "v[%c2:%c3]", C); } while (0)
  if constexpr ((MODE == 1 || MODE == 4) && ks == 0) { if constexpr (inV) GMF2("v", "0"); else GMF2("a", "0"); }
  else if constexpr (inV) GMF2("v", "v[%c0:%c1]");
  else GMF2("a", "a[%c0:%c1]");
#undef GMF2
#undef GMF
  if constexpr (e_st(MODE, GG) >= 0) {
    static_assert(MODE == 4 || ESCH<TR, MODE == 2>.op0[GG] >= (e_st(MODE, GG) + 1) * (MODE == 2 ? 16 : 8));
    d_store<e_st(MODE, GG)>();
  }
  if constexpr (EPI) e_ops<TR, MODE == 2, ESCH<TR, MODE == 2>.op0[GG]>(make_iseq<ESCH<TR, MODE == 2>.nop[GG]>{});
  if constexpr (J >= 0 && J < 4) g_dma<false, (1 - H) * 2 + (gj & 1) + (gj >> 1) * 4, 90>();
  else if constexpr (J >= 4) g_dma<true, (1 - H) * 4 + gj, 91>();
  if constexpr ((q == 1 || q == 2) && !(END && m == 7)) g_aread<(m < 7 ? P : P ^ 1), (m + 1) & 7, q - 1>(g);
  if constexpr (END) {
    if constexpr (e_slot(MODE, GG) >= 0) e_load<e_slot(MODE, GG)>();
    if constexpr (!TR && GG >= 3 && GG < 7 && e_slot(MODE, GG) < 0) asm volatile("s_nop 0");
    if constexpr (TR && GG >= 19 && GG < 21 && e_slot(MODE, GG) < 0) asm volatile("s_nop 0");
  } else {
    if constexpr (q >= 3 && q < 11 && (q & 1)) g_wread<P ^ 1, H, (G >> 4) * 4 + (q - 3) / 2>(g);
    if constexpr (H == 1 && G == 0) asm volatile("v_lshl_add_u32 v90, s82, 7, v92\n\ts_and_b32 s82, s80, 15");
    if constexpr (H == 1 && G == 4) asm volatile("s_lshr_b64 s[80:81], s[80:81], 4\n\tv_lshl_add_u32 v91, s82, 19, v93");
    constexpr int nst = e_nst(MODE, H ? 29 : 0, GG + 1);
    if constexpr (G == 61) asm volatile("s_waitcnt vmcnt(%0) lgkmcnt(0)\n\ts_barrier" ::"i"(12 + nst) : "memory");
  }
  if constexpr (EPI && GG == 127) {
    asm volatile("s_nop 7");
    e_ops<TR, MODE == 2, ESCH<TR, MODE == 2>.op0[128]>(make_iseq<ESCH<TR, MODE == 2>.nop[128]>{});
  }
  GAP();
}
template <bool TR, int P, int H, int MODE, int... Gs> DI void g_half_(GM& g, iseq<Gs...>) { (g_gap<TR, P, H, Gs, MODE>(g), ...); }
template <bool TR, int P, int H, int MODE> DI void g_half(GM& g) { g_half_<TR, P, H, MODE>(g, make_iseq<64>{}); }
template <bool TR, int P, int MODE> DI void g_tile(GM& g) { g_half<TR, P, 0, MODE>(g); g_half<TR, P, 1, MODE>(g); }
template <int P, int... Is> DI void g_read0(GM& g, iseq<Is...>) {
  (g_wread<P, 0, Is>(g), ...); (g_wread<P, 1, Is>(g), ...);
  g_aread<P, 0, 0>(g); g_aread<P, 0, 1>(g);
}
template <int I> DI void g_pro1(const unsigned (&m)[4]) {
  constexpr int grp = I >> 2, i = I & 3, t = grp < 4 ? 0 : grp < 7 ? 1 : 2;
  constexpr bool isA = grp == 0 || grp == 3 || grp == 5;
  constexpr int jj = (grp == 3 ? 2 : 0) + (i & 1) + (i >> 1) * 4, ks = grp == 2 || grp == 6;
  asm volatile("s_add_u32 m0, %0, %1\n\ts_nop 0" ::"s"(m[(t & 1) * 2 + !isA]), "i"(isA ? jj * 4096 : ks * 16384 + (i >> 1) * 8192 + (i & 1) * 4096) : "scc");
  if constexpr (isA) g_dma<false, jj, t ? 90 : 92>();
  else g_dma<true, ks * 4 + i, t ? 91 : 93>();
}
template <int B, int... Is> DI void g_pro(const unsigned (&m)[4], iseq<Is...>) { (g_pro1<B + Is>(m), ...); }
template <bool TR, bool SCL, class HOOK> DI void g_loop(GM& g, unsigned evm, HOOK&& hook) {
  constexpr int nkt = SCL ? 7 : 16, P0 = SCL ? 0 : 1;
  asm volatile("" : "+s"(evm));
#pragma unroll 1
  for (int t = 1;; t += 2) {
    g_half<TR, P0 ^ 1, 0, 0>(g);
    if (__builtin_expect((evm >> t) & 1, 0)) { hook(t); GAP(); }
    g_half<TR, P0 ^ 1, 1, 0>(g);
    if (SCL && t + 2 >= nkt) break;
    g_half<TR, P0, 0, 0>(g);
    if constexpr (SCL) { if (__builtin_expect(t == 3, 0)) { hook(4); GAP(); } }
    else asm volatile("s_cmp_eq_u32 %0, 13\n\ts_cselect_b32 s78, 0x28000, s78" ::"s"(t) : "scc");
    g_half<TR, P0, 1, 0>(g);
    if (!SCL && t + 3 >= nkt) break;
  }
}

typedef __attribute__((ext_vector_type(4))) unsigned u32x4;
typedef __attribute__((ext_vector_type(4))) float f32x4;

constexpr int R_S = 128, R_P = 192, R_CI = 224, R_E = 96;
constexpr int R_TS = 112, R_RS = 114, R_M = 116;
constexpr int R_PD = 118, R_THR = 120, R_TMP = 121;
constexpr int r_dk(int q) { return q < 6 ? 90 + q : 120 + q; }
constexpr int S_V = 96, S_T = 97, S_K = 98, S_I = 99, S_EV = 100;
struct AC {
  u64 redo;
  unsigned rare;
};
template <int X, int F> DI void kread() {
  constexpr int r = 192 + 4 * (F & 15);
  asm volatile("ds_read_b128 a[%c0:%c1], v84 offset:%c2" ::"i"(r), "i"(r + 3), "i"(X * 32768 + F * 1024));
}
template <int X, int F> DI void vread() {
  constexpr int r = 192 + 4 * (F & 15);
  asm volatile("ds_read_b128 a[%c0:%c1], v85 offset:%c2" ::"i"(r), "i"(r + 3), "i"(X * 32768 + (F & 1) * 16384 + (F >> 1) * 1024));
}
template <int X, int... Fs> DI void kreads(iseq<Fs...>) { (kread<X, Fs>(), ...); }
template <int X, int J> DI void dma_m0() {
  if constexpr (J == 0) asm volatile("s_mov_b32 m0, s%c0" ::"i"(78 + X));
  else if constexpr (J == 4) asm volatile("s_or_b32 m0, s%c0, 0x1000" ::"i"(78 + X));
  else if constexpr (J == 8) asm volatile("s_bitset0_b32 m0, 16");
  else if constexpr (J == 12) asm volatile("s_bitset0_b32 m0, 12");
}
template <int J> DI void dma_at() {
  if constexpr (J < 8)
    asm volatile("buffer_load_dwordx4 v%c0, s[88:91], s96 offen offset:%1 sc1 lds" ::"i"(86 + (J >= 4)), "i"((J & 3) * 1024) : "memory");
  else
    asm volatile("buffer_load_dwordx4 v%c0, s[92:95], s98 offen offset:%1 sc1 lds" ::"i"(r_dk(J - 8)), "i"((J & 3) * 1024) : "memory");
}
template <int Q> DI void dma4k() {
  asm volatile("s_add_u32 m0, s79, %0\n\ts_add_u32 s48, s98, %1\n\tbuffer_load_dwordx4 v%c2, s[88:91], s48 offen sc1 lds\n\t"
               "buffer_load_dwordx4 v%c2, s[88:91], s48 offen offset:1024 sc1 lds\n\tbuffer_load_dwordx4 v%c2, s[88:91], s48 offen offset:2048 sc1 lds\n\t"
               "buffer_load_dwordx4 v%c2, s[88:91], s48 offen offset:3072 sc1 lds"
               ::"i"((Q >> 1) * 32768 + (Q & 1) * 4096), "i"((Q >> 1) * 32768), "i"(86 + (Q & 1)) : "memory", "scc");
}
template <int Q> DI void acc_zero2() {
  asm volatile("v_accvgpr_write_b32 a[%c0], 0\n\tv_accvgpr_write_b32 a[%c1], 0" ::"i"(2 * Q), "i"(2 * Q + 1));
}
struct SMS { int ex[128], op0[128], nop[128], last; };
constexpr SMS sm_sched() {
  SMS z{};
  int et[32] = {}, e = 0;
  for (int t = 0; t < 128; ++t) {
    const int g = t & 15;
    const bool x = t < 48 ? (g >= 8 && (g & 1)) : (g >= 8 && g != 10 && g != 13 && g != 15);
    z.ex[t] = x && e < 32 ? e : -1;
    if (z.ex[t] >= 0) et[e++] = t;
  }
  int o = 0;
  for (int t = 0; t < 128; ++t) {
    const int g = t & 15;
    int cap = g < 8 ? (g == 0 ? 0 : 1) : (z.ex[t] >= 0 || t < 48 ? 0 : 2);
    z.op0[t] = o; z.nop[t] = 0;
    while (cap > 0 && o < 48 && et[2 * (o / 3) + (o % 3 == 0 ? 0 : 1)] + 2 <= t) {
      const int el = 2 * (o / 3) + o % 3;
      if (!(o % 3 < 2 && (el == 0 || el == 4))) --cap;
      ++o; ++z.nop[t]; z.last = t;
    }
  }
  return z;
}
constexpr SMS SM = sm_sched();
static_assert(SM.last <= 115);
constexpr int p_reg(int Y, int k) { return R_P + 16 * Y + 4 * ((k >> 3) * 2 + ((k >> 1) & 1)) + ((k >> 2) & 1) * 2 + (k & 1); }
template <int Y, int O> DI void sm_op() {
  constexpr int k = O / 3, e = 2 * k + O % 3, ts = R_TS + ((e >> 2) & 1);
  if constexpr (O % 3 == 2) asm volatile("v_cvt_pk_bf16_f32 v%c0, v%c1, v%c2" ::"i"(p_reg(Y, k)), "i"(R_E + ((2 * k) & 15)), "i"(R_E + ((2 * k + 1) & 15)));
  else if constexpr (e == 1 || e == 5) asm volatile("v_add_f32_e32 v%c0, v%c1, v%c2" ::"i"(ts), "i"(R_E + e - 1), "i"(R_E + e));
  else if constexpr (e != 0 && e != 4) asm volatile("v_add_f32_e32 v%c0, v%c0, v%c1" ::"i"(ts), "i"(R_E + (e & 15)));
}
template <int Y, int T> DI void sm_gap(AC& c) {
  if constexpr (SM.ex[T] >= 0) asm volatile("v_exp_f32 v%c0, v%c1" ::"i"(R_E + (SM.ex[T] & 15)), "i"(R_S + 32 * Y + SM.ex[T]));
  if constexpr (SM.nop[T] > 0) sm_op<Y, SM.op0[T]>();
  if constexpr (SM.nop[T] > 1) sm_op<Y, SM.op0[T] + 1>();
  if constexpr (SM.nop[T] > 2) sm_op<Y, SM.op0[T] + 2>();
  if constexpr (T == SM.last + 1) asm volatile("v_add_f32_e32 v%c0, v%c1, v%c2" ::"i"(R_TMP), "i"(R_TS), "i"(R_TS + 1));
  if constexpr (T == SM.last + 2) asm volatile("v_cmp_nle_f32_e64 %0, v%c1, v%c2" : "=s"(c.redo) : "i"(R_TMP), "i"(R_THR));
}
template <int X, int MODE, int G> DI void gapA(AC& c) {
  constexpr bool FIRST = MODE == 1 || MODE == 2, NOQK = MODE == 3;
  constexpr int Xp = X ^ 1, kb = G >> 4, g = G & 15, qb = g >> 3, s = g & 7;
  if constexpr (NOQK && G < 48 && G % 3 == 0) {
    constexpr int f = G / 3;
    asm volatile("buffer_load_dwordx4 a[%c0:%c1], v122, s[88:91], s75 offen offset:%2 sc1" ::"i"(128 + 4 * f), "i"(131 + 4 * f), "i"((f & 3) * 1024) : "memory");
    if constexpr ((f & 3) == 3) asm volatile("s_addk_i32 s75, 4096");
  }
  if constexpr (G == 48) asm volatile("s_waitcnt vmcnt(%0) lgkmcnt(0)\n\ts_barrier" ::"i"(NOQK ? 16 : MODE == 2 ? 12 : 0) : "memory");
  else if constexpr (g == 0 && !NOQK) asm volatile("s_waitcnt lgkmcnt(0)");
  if constexpr (!NOQK) {
    constexpr int r = 192 + 4 * ((kb & 1) * 8 + s), q = 128 + 4 * g;
    constexpr int d = R_S + 32 * X + 4 * (kb * 2 + qb), ci = s == 0 ? R_CI + 4 * (kb * 2 + qb) : d;
    if constexpr (FIRST && s == 0)
      asm volatile("v_mfma_f32_16x16x32_bf16 v[%c0:%c1], a[%c2:%c3], a[%c4:%c5], 0" ::"i"(d), "i"(d + 3), "i"(r), "i"(r + 3), "i"(q), "i"(q + 3));
    else
      asm volatile("v_mfma_f32_16x16x32_bf16 v[%c0:%c1], a[%c2:%c3], a[%c4:%c5], v[%c6:%c7]" ::"i"(d), "i"(d + 3), "i"(r), "i"(r + 3), "i"(q), "i"(q + 3),
                   "i"(ci), "i"(ci + 3));
    if constexpr (g < 8 && kb < 3) kread<X, (kb + 1) * 8 + g>();
    if constexpr (G == 54) asm volatile("s_add_u32 s96, s97, 0x1000000");
    if constexpr (G == 55) dma_m0<X, 0>();
    if constexpr (G >= 56 && !(G & 1)) dma_at<(G - 56) / 2>();
    if constexpr (G == 57) asm volatile("s_mov_b32 s97, s98\n\ts_lshr_b32 s100, s100, 1\n\ts_add_u32 s98, s98, s99");
    if constexpr (G == 59 && !FIRST)
      asm volatile("s_and_b32 s101, s100, 1\n\ts_or_b32 s101, s101, %1\n\ts_or_b32 %0, s101, %2" : "=s"(c.rare) : "s"((unsigned)c.redo), "s"((unsigned)(c.redo >> 32)));
    if constexpr (G == 59 && FIRST) asm volatile("s_and_b32 %0, s100, 1" : "=s"(c.rare));
  } else if constexpr (SM.nop[64 + G] > 0) asm volatile("s_nop 0");
  if constexpr (!FIRST && g < 8 && kb == 3) vread<Xp, g>();
  if constexpr (MODE == 2 && (G & 3) == 1) d_store<16 + G / 4>();
  if constexpr (FIRST) acc_zero2<G>();
  else sm_gap<Xp, 64 + G>(c);
  GAP();
}
template <int R> DI void rescale1(float alpha) {
  float t;
  asm volatile("v_accvgpr_read_b32 %0, a[%c2]\n\tv_mul_f32 %0, %0, %1\n\tv_accvgpr_write_b32 a[%c2], %0" : "=&v"(t) : "v"(alpha), "i"(R));
}
template <int... Rs> DI void rescale_all(const float (&alpha)[2], iseq<Rs...>) { (rescale1<Rs>(alpha[(Rs >> 2) & 1]), ...); }
template <bool MAX> DI float row_reduce(float v) {
  auto p = __builtin_amdgcn_permlane32_swap(__float_as_uint(v), __float_as_uint(v), false, false);
  v = MAX ? fmaxf(__uint_as_float(p[0]), __uint_as_float(p[1])) : __uint_as_float(p[0]) + __uint_as_float(p[1]);
  p = __builtin_amdgcn_permlane16_swap(__float_as_uint(v), __float_as_uint(v), false, false);
  return MAX ? fmaxf(__uint_as_float(p[0]), __uint_as_float(p[1])) : __uint_as_float(p[0]) + __uint_as_float(p[1]);
}
template <int Y, int QB> DI float row_max() {
  constexpr int r = R_S + 32 * Y + 4 * QB;
  float mx;
  asm volatile("v_max_f32 %0, v%c1, v%c2\n\tv_max3_f32 %0, %0, v%c3, v%c4" : "=&v"(mx) : "i"(r), "i"(r + 1), "i"(r + 2), "i"(r + 3));
  static_for([&](auto K) {
    constexpr int q = r + 8 * (K + 1);
    float t = mx;
    asm volatile("v_max3_f32 %0, %0, v%c1, v%c2\n\tv_max3_f32 %0, %0, v%c3, v%c4" : "+v"(t) : "i"(q), "i"(q + 1), "i"(q + 2), "i"(q + 3));
    mx = t;
  }, make_iseq<3>{});
  return row_reduce<true>(mx);
}
template <int R> DI void v_sub(float d) { asm volatile("v_sub_f32 v%c0, v%c0, %1" ::"i"(R), "v"(d)); }
template <int B> DI void s_shift(const float (&d)[2]) { static_for([&](auto E) { v_sub<B + E>(d[(E >> 2) & 1]); }, make_iseq<32>{}); }
DI void m_shift(const float (&d)[2]) {
  s_shift<R_CI>(d);
  v_sub<R_M>(-d[0]); v_sub<R_M + 1>(-d[1]);
}
DI void set_mask0(int wid) {
  const int mr = wid * 32 + __float_as_int(vget<89>());
  static_for([&](auto I) {
    constexpr int e = I, k = (e >> 3) * 16 - ((e >> 2) & 1) * 16 + (e & 3);
    const float x = vget<R_S + e>();
    vset<R_S + e>(k > mr ? -INFINITY : x);
  }, make_iseq<32>{});
}
template <int X> DI void sm_redo() {
  constexpr int Xp = X ^ 1;
  float d[2], dt[2], alpha[2], ts[2] = {0.f, 0.f};
  static_for([&](auto Q) {
    constexpr int qb = Q;
    const float pd = vget<R_PD + qb>();
    d[qb] = fmaxf(row_max<Xp, qb>() - pd, 0.f);
    dt[qb] = d[qb] + pd;
    vset<R_PD + qb>(d[qb]);
    alpha[qb] = __builtin_amdgcn_exp2f(-d[qb]);
    vset<R_RS + qb>(vget<R_RS + qb>() * alpha[qb]);
  }, make_iseq<2>{});
  vset<R_THR>(d[0] > 0.f || d[1] > 0.f ? -1.f : 4096.f);
  m_shift(d);
  static_for([&](auto K) {
    constexpr int k = K, qb = (k >> 1) & 1;
    const float e0 = __builtin_amdgcn_exp2f(vget<R_S + 32 * Xp + 2 * k>() - dt[qb]), e1 = __builtin_amdgcn_exp2f(vget<R_S + 32 * Xp + 2 * k + 1>() - dt[qb]);
    ts[qb] += e0; ts[qb] += e1;
    vset<p_reg(Xp, k)>(__uint_as_float(pk2(e0, e1)));
  }, make_iseq<16>{});
  vset<R_TS>(ts[0]); vset<R_TS + 1>(ts[1]);
  rescale_all(alpha, make_iseq<128>{});
  asm volatile("s_nop 1" ::: "memory");
  GAP();
}
template <int X, int MODE, int... Gs> DI void phaseA_(AC& c, iseq<Gs...>) { (gapA<X, MODE, Gs>(c), ...); }
template <int X, int MODE> DI void phaseA(AC& c) { phaseA_<X, MODE>(c, make_iseq<64>{}); }

template <int X, bool SMX, int G> DI void gapB(AC& c) {
  constexpr int Xp = X ^ 1, blk = G >> 4, g = G & 15;
  if constexpr (g == 0) asm volatile("s_waitcnt lgkmcnt(0)");
  if constexpr (SMX && g == 7 && blk < 3) dma_m0<X, blk * 4 + 4>();
  constexpr int qb = (g >> 1) & 1, s = g & 1, r = 192 + 4 * ((blk & 1) * 8 + (g >> 2) * 2 + s), o = 4 * ((blk * 4 + (g >> 2)) * 2 + qb);
  constexpr int p = R_P + 16 * Xp + 4 * (s * 2 + qb);
  asm volatile("v_mfma_f32_16x16x32_bf16 a[%c0:%c1], a[%c2:%c3], v[%c4:%c5], a[%c0:%c1]" ::"i"(o), "i"(o + 3), "i"(r), "i"(r + 3), "i"(p), "i"(p + 3));
  if constexpr (SMX && (G == 1 || G == 2)) asm volatile("v_add_f32_e32 v%c0, v%c0, v%c1" ::"i"(R_RS + G - 1), "i"(R_TS + G - 1));
  if constexpr (g < 8) {
    if constexpr (blk < 3) vread<Xp, (blk + 1) * 8 + g>();
    else if constexpr (SMX) kread<Xp, g>();
  }
  if constexpr (SMX) {
    if constexpr (g >= 8 && !(g & 1) && blk < 3) dma_at<blk * 4 + 4 + (g - 8) / 2>();
    if constexpr (G == 58) asm volatile(FL_POLL ::: "memory");
    sm_gap<X, G>(c);
  }
  GAP();
}
template <int X, int... Gs> DI void phaseB_(AC& c, iseq<Gs...>) { (gapB<X, true, Gs>(c), ...); }
template <int X> DI void phaseB(AC& c) { phaseB_<X>(c, make_iseq<64>{}); }
constexpr int sm_nex() { int n = 0; for (int t = 0; t < 64; ++t) n += SM.ex[t] >= 0; return n; }
DI void phaseB0(unsigned mode, int wid) {
  dma_m0<0, 4>();
  kreads<1>(make_iseq<8>{});
  dma_at<4>();
  asm volatile("s_nop 3");
  float d[2];
  if (mode & 2) {
    d[0] = vget<122>(); d[1] = vget<123>();
    dma_at<5>();
  } else {
    if (__builtin_expect(mode & 4, 0)) set_mask0(wid);
    d[0] = row_max<0, 0>(); GAP(); dma_at<5>();
    d[1] = row_max<0, 1>();
#pragma unroll
    for (int qb = 0; qb < 2; ++qb) if (!(d[qb] > -3.0e38f)) d[qb] = 0.f;
  }
  GAP();
  vset<R_M>(d[0]); vset<R_M + 1>(d[1]);
  dma_at<6>();
  const f32x2c nd[2] = {{-d[0], -d[0]}, {-d[1], -d[1]}};
  static_for([&](auto E) {
    constexpr int e = E;
    const f32x2c v = nd[(e >> 1) & 1];
    asm volatile("v_pk_add_f32 v[%c0:%c1], v[%c0:%c1], %4\n\tv_mov_b64 v[%c2:%c3], %4" ::"i"(R_S + 2 * e), "i"(R_S + 2 * e + 1), "i"(R_CI + 2 * e), "i"(R_CI + 2 * e + 1), "v"(v));
    if constexpr (e == 3) dma_at<7>();
    if constexpr (e == 6) dma_m0<0, 8>();
    if constexpr (e == 7 || e == 11 || e == 15) dma_at<8 + (e - 7) / 4>();
  }, make_iseq<16>{});
  static_assert(sm_nex() >= 16 && SM.op0[64] >= 3 * ((sm_nex() - 15) / 2) && SM.op0[64] <= 24);
  static_for([&](auto E) {
    constexpr int e = E;
    asm volatile("v_exp_f32 v%c0, v%c1" ::"i"(R_E + e), "i"(R_S + e));
    if constexpr (e == 3) dma_at<11>();
    if constexpr (e == 6) dma_m0<0, 12>();
    if constexpr (e == 7 || e == 11 || e == 15) dma_at<12 + (e - 7) / 4>();
  }, make_iseq<16>{});
  static_for([&](auto O) { sm_op<0, O>(); }, make_iseq<SM.op0[64]>{});
  dma_at<15>();
  static_for([&](auto E) { asm volatile("v_exp_f32 v%c0, v%c1" ::"i"(R_E + (int)E), "i"(R_S + 16 + (int)E)); }, make_iseq<sm_nex() - 16>{});
  asm volatile("buffer_store_dwordx2 v[%c0:%c1], v88, s[88:91], s76 offen sc1\n\t" FL_POLL ::"i"(R_M), "i"(R_M + 1) : "memory");
  GAP();
}
constexpr int T_ZO = 8, T_WT = 9;
#define NINF2(R) "v_mov_b64 v[" #R ":" #R "+1], v[122:123]\n\t"
#define NINF4(R) NINF2(R) NINF2(R + 2)
#define TRI(R) "v_cndmask_b32_e64 v[" #R "], v[" #R "], v122, s[48:49]\n\tv_cndmask_b32_e64 v[" #R "+1], v[" #R "+1], v122, s[50:51]\n\t" \
               "v_cndmask_b32_e64 v[" #R "+2], v[" #R "+2], v122, s[52:53]\n\tv_cndmask_b32_e64 v[" #R "+3], v[" #R "+3], v122, s[54:55]\n\t"
DI void ev_code() {
  asm volatile(
    "s_getpc_b64 s[82:83]\n\ts_add_u32 s82, s82, 12\n\ts_addc_u32 s83, s83, 0\n\ts_branch 9f\n\t"
    "s_bitcmp0_b32 s58, s56\n\ts_cbranch_scc1 1f\n\t" FL_SET("s70") "\n\t" FL_SET("s71") "\n"
    "1:\n\ts_bitcmp0_b32 s57, s56\n\ts_cbranch_scc1 2f\n\t" FL_CHECK("s[62:63]") "\n\t"
    "s_add_u32 s98, s98, s68\n\ts_mov_b32 s68, s69\n\ts_mov_b32 s69, 0\n\ts_mov_b64 s[62:63], s[64:65]\n\ts_mov_b64 s[64:65], s[66:67]\n\t"
    FL_ARM("s[62:63]") "\n"
    "2:\n\ts_bitcmp0_b32 s59, s56\n\ts_cbranch_scc1 3f\n\ts_mov_b32 s98, s72\n\ts_bitcmp0_b32 s73, 0\n\ts_cbranch_scc1 3f\n\t"
    "v_lshrrev_b32 v90, 7, v86\n\tv_and_b32 v91, 6, v90\n\tv_lshlrev_b32 v91, 4, v91\n\tv_and_b32 v92, 0x70, v86\n\tv_xor_b32 v91, v91, v92\n\t"
    "s_movk_i32 s48, 896\n\tv_mad_u32_u24 v94, v90, s48, v91\n\ts_mov_b64 s[92:93], s[80:81]\n\ts_mov_b32 s94, 229376\n\ts_movk_i32 s99, 128\n\t"
    "v_add_u32 v90, 28672, v94\n\tv_add_u32 v91, 34816, v94\n\tv_add_u32 v92, 40960, v94\n\tv_add_u32 v93, 47104, v94\n\t"
    "v_add_u32 v95, 6144, v94\n\tv_add_u32 v126, 12288, v94\n\tv_add_u32 v127, 18432, v94\n"
    "3:\n\ts_bitcmp0_b32 s60, s56\n\ts_cbranch_scc1 4f\n\t"
    "v_readlane_b32 s54, v78, %0\n\tv_readlane_b32 s55, v78, %1\n\ts_add_u32 m0, s78, 0x10000\n\t"
    "s_getpc_b64 s[48:49]\n\ts_add_u32 s48, s48, tail_begin@rel32@lo+4\n\ts_addc_u32 s49, s49, tail_begin@rel32@hi+12\n\t"
    "s_mov_b32 s50, pf_code_end-tail_begin\n\ts_mov_b32 s51, 0x20000\n\tv_mul_u32_u24 v121, 224, v84\n\ts_lshr_b32 s53, s55, 6\n\t"
    "buffer_load_dword v121, s[48:51], s53 offen lds\n\t"
    "s_mov_b64 s[48:49], s[80:81]\n\ts_mov_b32 s50, 229376\n\tv_lshlrev_b32 v121, 3, v84\n\t"
    "buffer_load_dword v121, s[48:51], s55 offen lds\n\t"
    "v_lshlrev_b32 v121, 11, v84\n\tv_and_b32 v121, 0x38000, v121\n\tv_lshrrev_b32 v122, 7, v84\n\tv_lshl_add_u32 v121, v122, 12, v121\n\t"
    "buffer_load_dwordx4 v121, s[88:91], s54 offen sc1 lds\n\ts_sub_u32 s54, s54, 1008\n\t"
    "buffer_load_dwordx4 v121, s[88:91], s54 offen offset:1024 sc1 lds\n\ts_sub_u32 s54, s54, 1008\n\t"
    "buffer_load_dwordx4 v121, s[88:91], s54 offen offset:2048 sc1 lds\n\ts_sub_u32 s54, s54, 1008\n\t"
    "buffer_load_dwordx4 v121, s[88:91], s54 offen offset:3072 sc1 lds\n"
    "4:\n\ts_bitcmp0_b32 s61, s56\n\ts_cbranch_scc1 6f\n\t"
    "s_sub_u32 s74, s74, 64\n\ts_cmp_ge_i32 s74, 0\n\ts_cbranch_scc1 6f\n\t"
    "v_cmp_gt_i32 s[48:49], 0, v89\n\tv_cmp_gt_i32 s[50:51], 1, v89\n\tv_cmp_gt_i32 s[52:53], 2, v89\n\tv_cmp_gt_i32 s[54:55], 3, v89\n\t"
    "v_mov_b32 v122, 0xff800000\n\tv_mov_b32 v123, 0xff800000\n\t" NINF4(248)
    "s_cmp_eq_i32 s74, -32\n\ts_cbranch_scc0 5f\n\t" TRI(240) TRI(252) "s_setpc_b64 s[86:87]\n"
    "5:\n\t" NINF4(240) NINF4(244) NINF4(252) NINF4(232) TRI(224) TRI(236)
    "s_cmp_eq_i32 s74, -64\n\ts_cbranch_scc1 6f\n\t" NINF4(224) NINF4(228) NINF4(236)
    "6:\n\ts_setpc_b64 s[86:87]\n9:"
    ::"i"(T_ZO), "i"(T_WT) : "scc");
}
DI void ev_call(int i) { asm volatile("s_mov_b32 s56, %0\n\ts_swappc_b64 s[86:87], s[82:83]" ::"s"(i) : "vcc", "scc", "memory"); GAP(); }

#define ACLOB_ALL "a0","a1","a2","a3","a4","a5","a6","a7","a8","a9","a10","a11","a12","a13","a14","a15","a16","a17","a18","a19","a20","a21","a22","a23","a24","a25","a26","a27","a28","a29","a30","a31","a32","a33","a34","a35","a36","a37","a38","a39","a40","a41","a42","a43","a44","a45","a46","a47","a48","a49","a50","a51","a52","a53","a54","a55","a56","a57","a58","a59","a60","a61","a62","a63","a64","a65","a66","a67","a68","a69","a70","a71","a72","a73","a74","a75","a76","a77","a78","a79","a80","a81","a82","a83","a84","a85","a86","a87","a88","a89","a90","a91","a92","a93","a94","a95","a96","a97","a98","a99","a100","a101","a102","a103","a104","a105","a106","a107","a108","a109","a110","a111","a112","a113","a114","a115","a116","a117","a118","a119","a120","a121","a122","a123","a124","a125","a126","a127","a128","a129","a130","a131","a132","a133","a134","a135","a136","a137","a138","a139","a140","a141","a142","a143","a144","a145","a146","a147","a148","a149","a150","a151","a152","a153","a154","a155","a156","a157","a158","a159","a160","a161","a162","a163","a164","a165","a166","a167","a168","a169","a170","a171","a172","a173","a174","a175","a176","a177","a178","a179","a180","a181","a182","a183","a184","a185","a186","a187","a188","a189","a190","a191","a192","a193","a194","a195","a196","a197","a198","a199","a200","a201","a202","a203","a204","a205","a206","a207","a208","a209","a210","a211","a212","a213","a214","a215","a216","a217","a218","a219","a220","a221","a222","a223","a224","a225","a226","a227","a228","a229","a230","a231","a232","a233","a234","a235","a236","a237","a238","a239","a240","a241","a242","a243","a244","a245","a246","a247","a248","a249","a250","a251","a252","a253","a254","a255"

struct EP { float fa[2], fb[2]; };
template <int I, int E, int MODE> DI void epi_pair(const EP& p) {
  constexpr int qb = I & 1, r = 16 * (I >> 1) + 4 * qb + (E >> 1) * 8 + (E & 1) * 2, t = 96 + 2 * E, w = 128 + 4 * I + E;
  if constexpr (MODE == 2) asm volatile("v_accvgpr_read_b32 v106, a[%c0]" ::"i"(w));
  asm volatile("v_accvgpr_read_b32 v%c0, a[%c2]\n\tv_accvgpr_read_b32 v%c1, a[%c3]" ::"i"(t), "i"(t + 1), "i"(r), "i"(r + 1));
  if constexpr (MODE) asm volatile("v_mul_f32 v%c0, v%c0, %2\n\tv_mul_f32 v%c1, v%c1, %2" ::"i"(t), "i"(t + 1), "v"(p.fa[qb]));
  if constexpr (MODE == 2)
    asm volatile("v_lshlrev_b32 v104, 16, v106\n\tv_and_b32 v105, 0xffff0000, v106\n\tv_fmac_f32 v%c0, v104, %2\n\tv_fmac_f32 v%c1, v105, %2"
                 ::"i"(t), "i"(t + 1), "v"(p.fb[qb]));
  asm volatile("v_cvt_pk_bf16_f32 v%c0, v%c1, v%c2" ::"i"(w), "i"(t), "i"(t + 1));
}
template <int I> DI void epi_store() {
  asm volatile("buffer_store_dwordx4 v[%c0:%c1], v84, s[88:91], s101 offen offset:%2 sc1" ::"i"(128 + 4 * I), "i"(131 + 4 * I), "i"((I & 3) * 1024) : "memory");
  if constexpr ((I & 3) == 3) asm volatile("s_addk_i32 s101, 4096");
}
constexpr int R_TB = 107, R_VS = 111;
template <int HF, int R> DI void p_read() {
  constexpr int mb = R >> 4, r = 64 * HF + 2 * R;
  asm volatile("ds_read_b64_tr_b16 a[%c1:%c2], v%c0 offset:%c3" ::"i"(R_TB + 2 * HF + mb), "i"(r), "i"(r + 1), "i"((R & 15) * 2048));
}
template <int QB, int E> DI void p_cvt() {
  asm volatile("v_cvt_pk_bf16_f32 v%c0, v%c1, v%c2" ::"i"(208 + 4 * QB + E), "i"(192 + 8 * QB + 2 * E), "i"(193 + 8 * QB + 2 * E));
}
template <int QB, int PR> DI void p_store() {
  asm volatile("buffer_store_dwordx4 v[%c0:%c1], v%c2, s[88:91], s96 offen offset:%3 sc1" ::"i"(208 + 4 * QB), "i"(211 + 4 * QB), "i"(R_VS + QB),
               "i"(PR * 128) : "memory");
}
constexpr int p_dma(int HF, int G) { return G == 3 && !HF ? 0 : G == 11 ? 1 - HF : G == 15 ? 2 - HF : G == 19 ? 3 - HF : G == 27 && HF ? 3 : -1; }
template <int HF, int G> DI void p_gap() {
  constexpr int ch = G >> 3, t = G & 7, a = 64 * HF + 32 * (ch & 1) + 4 * t, b = 128 + 4 * (2 * t + (ch >> 1)), d = 192 + 4 * ch;
  if constexpr (t == 0)
    asm volatile("v_mfma_f32_16x16x32_bf16 v[%c0:%c1], a[%c2:%c3], v[%c4:%c5], 0" ::"i"(d), "i"(d + 3), "i"(a), "i"(a + 3), "i"(b), "i"(b + 3));
  else
    asm volatile("v_mfma_f32_16x16x32_bf16 v[%c0:%c1], a[%c2:%c3], v[%c4:%c5], v[%c0:%c1]" ::"i"(d), "i"(d + 3), "i"(a), "i"(a + 3), "i"(b), "i"(b + 3));
  p_read<HF ^ 1, G>();
  if constexpr (G == 0) asm volatile("v_xor_b32 v%c0, s100, v%c0" ::"i"(R_TB + 2 * HF + 1));
  if constexpr (G == 17) asm volatile("v_xor_b32 v%c0, s100, v%c0" ::"i"(R_TB + 2 * (HF ^ 1)));
  if constexpr (G >= 4 && G <= 8 && G != 7) p_cvt<1, (G < 7 ? G - 4 : 3)>();
  if constexpr (G == 24 || (G == 7 && HF == 0)) asm volatile("s_waitcnt lgkmcnt(8)");
  if constexpr (G == 7 && HF == 1) asm volatile("s_waitcnt vmcnt(13) lgkmcnt(8)\n\ts_barrier" ::: "memory");
  if constexpr (G == 10) p_store<1, HF ^ 1>();
  if constexpr (G >= 20 && G < 24) p_cvt<0, G - 20>();
  if constexpr (G == 25) p_store<0, HF>();
  if constexpr (p_dma(HF, G) >= 0) dma_at<(HF ? 8 : 12) + p_dma(HF, G)>();
  if constexpr (HF == 0 && G == 2) asm volatile("s_bitset0_b32 m0, 12");
  if constexpr (HF == 0 && G == 13) asm volatile("s_add_u32 s96, s96, 0x40000");
  if constexpr (HF == 0 && G == 26) asm volatile("s_add_u32 s98, s98, 128");
  if constexpr (HF == 0 && G == 27) asm volatile("s_cmp_lt_u32 s98, 0x380");
  if constexpr (HF == 0 && G == 28) asm volatile("s_cselect_b32 s99, s99, 0x31000");
  if constexpr (HF == 1 && G == 1) asm volatile("s_xor_b32 s100, s100, 0x10000");
  if constexpr (HF == 1 && G == 2) asm volatile("s_xor_b32 s97, s97, s100");
  if constexpr (HF == 1 && G == 9) asm volatile("s_or_b32 m0, s97, s99");
  if constexpr (HF == 1 && G == 30) asm volatile("s_cmp_lt_u32 s98, 0x500");
  GAP();
}
template <int HF, int... Gs> DI void p_half(iseq<Gs...>) { (p_gap<HF, Gs>(), ...); }
constexpr int f_dma(int G) { return G == 2 ? 0 : G == 5 ? 1 : G == 8 ? 2 : G >= 12 && G <= 36 && G % 6 == 0 ? 1 + G / 6 : G >= 50 && G <= 68 && G % 6 == 2 ? G / 6 : -1; }
constexpr int f_ndma(int G) { int n = 0; for (int g = 0; g < G; ++g) n += f_dma(g) >= 0; return n; }
static_assert(f_ndma(74) == 12);
template <int MODE, int G> DI void gapF(AC& c, const EP& p) {
  if constexpr (MODE && G == 48) asm volatile("s_waitcnt lgkmcnt(0)\n\ts_barrier" ::: "memory");
  if constexpr (G < 64) gapB<0, false, G>(c);
  if constexpr (MODE == 2 && G >= 10 && (G - 10) % 16 == 0) asm volatile("s_waitcnt vmcnt(%0)" ::"i"(12 - (G - 10) / 4 + f_ndma(G)) : "memory");
  if constexpr (G >= 10) epi_pair<(G - 10) / 4, (G - 10) & 3, MODE>(p);
  if constexpr (MODE == 3 && G < 64 && (G & 1)) {
    constexpr int db = G >> 2, qb = (G >> 1) & 1, o = 4 * (db * 2 + qb), a = 224 + 4 * (db & 1), b = 128 + 4 * ((db >> 1) * 2 + qb);
    if constexpr ((G & 7) == 1) asm volatile("s_waitcnt vmcnt(%0)" ::"i"(14 - (G >> 2) + f_ndma(G)) : "memory");
    asm volatile("v_mfma_f32_16x16x32_bf16 a[%c0:%c1], v[%c2:%c3], a[%c4:%c5], a[%c0:%c1]" ::"i"(o), "i"(o + 3), "i"(a), "i"(a + 3), "i"(b), "i"(b + 3));
  }
  if constexpr (MODE == 0) {
    if constexpr (G >= 14 && ((G - 14) & 3) == 0) epi_store<(G - 14) / 4>();
    if constexpr (G == 73) epi_store<15>();
  } else {
    if constexpr (G == 0) asm volatile("s_or_b32 m0, s78, 0x1000\n\ts_movk_i32 s98, 256");
    if constexpr (G == 15) asm volatile("s_bitset0_b32 m0, 12");
    if constexpr (G == 48) asm volatile("s_or_b32 m0, s79, 0x1000\n\ts_movk_i32 s98, 384");
    if constexpr (f_dma(G) >= 0) dma_at<8 + (f_dma(G) & 7)>();
    if constexpr (G == 73) asm volatile("s_waitcnt vmcnt(4) lgkmcnt(0)" ::: "memory");
    if constexpr (G >= 48 && G < 64) { p_read<0, 2 * (G - 48)>(); p_read<0, 2 * (G - 48) + 1>(); }
  }
  GAP();
}
template <int MODE, int... Gs> DI void phaseF(AC& c, const EP& p, iseq<Gs...>) { (gapF<MODE, Gs>(c, p), ...); }
template <int U, int K> DI void r_load(unsigned vo, unsigned so) {
  asm volatile("buffer_load_dwordx4 a[%c0:%c1], %2, s[88:91], %3 offen offset:%4 sc1" ::"i"(64 * U + 4 * K), "i"(64 * U + 4 * K + 3), "v"(vo),
               "s"(so + (K >> 2) * 4096), "i"((K & 1) * 512 + ((K >> 1) & 1) * 2048) : "memory");
}
template <int U, int K> DI void r_sum() {
  constexpr int d = 100 + 4 * (K & 1), b = 64 * U + 4 * K;
  if constexpr (K < 2) asm volatile("v_mfma_f32_16x16x32_bf16 v[%c0:%c1], v[96:99], a[%c2:%c3], 0" ::"i"(d), "i"(d + 3), "i"(b), "i"(b + 3));
  else asm volatile("v_mfma_f32_16x16x32_bf16 v[%c0:%c1], v[96:99], a[%c2:%c3], v[%c0:%c1]" ::"i"(d), "i"(d + 3), "i"(b), "i"(b + 3));
}
template <int U> DI void r_fin(unsigned vo, __amdgpu_buffer_rsrc_t srd, unsigned so) {
  asm volatile("" : "+s"(srd), "+s"(so));
  static_for([&](auto K) { r_sum<U, K>(); }, make_iseq<16>{});
  asm volatile("s_nop 7");
  static_for([&](auto E) { asm volatile("v_cvt_pk_bf16_f32 v%c0, v%c1, v%c2" ::"i"(116 + 4 * U + E), "i"(100 + 2 * E), "i"(101 + 2 * E)); }, make_iseq<4>{});
  asm volatile("buffer_store_dwordx4 v[%c3:%c4], %0, %1, %2 offen" ::"v"(vo), "s"(srd), "s"(so), "i"(116 + 4 * U), "i"(119 + 4 * U) : "memory");
}

#define CMAX 12
#define NYT 16
static_assert(NYT <= 16 && NYT >= 12 && !(NYT & 1));
enum { T_Q0, T_K0, T_OQ, T_OG, T_FPREP, T_FQ, T_FG, T_FS,   T_NPRE = 10, T_NST = 12, T_TAB = 14, T_SLAB = 16, T_PART, T_IT = 18, T_ITN = 23 };
struct TRow { unsigned v[64]; };
constexpr u64 kvmask(int m0, int m1) { u64 n = 0; for (int m = m0; m <= m1; ++m) n |= 0x101ull << m; return n; }
constexpr u64 k_order(bool isY, int rr) {
  const int cls = (rr + (rr >> 2)) & 3;
  return !isY ? (cls == 0 ? 0xfedcb9875a463210ull : cls == 1 ? 0xefadc6982b574310ull : cls == 2 ? 0xfebad7693c285410ull : 0xefcba8764d392510ull)
              : (cls == 0 ? 0xfedcb987543a6210ull : cls == 1 ? 0xefadc698254b7310ull : cls == 2 ? 0xfebad769325c8410ull : 0xefcba876432d9510ull);
}
constexpr TRow t_row(int r, int wid, int j) {
  TRow t{};
  const int rr = r & 15, hsel = rr & 1, pj = rr >> 1, kv = pj & 1, h = j + 8 * hsel, u = hsel * 8 + j;
  const bool isY = r >= 16, solo = pj == 7;
  const int mt = (rr >> 2) + (isY ? 4 : 0), g = j * 128, NY = solo ? 18 : NYT;
  const int nx0 = 32 - 2 * pj - NY, cx = nx0 < CMAX ? nx0 : CMAX, ex = nx0 - cx, qbF = isY ? 15 - pj : pj;
  const unsigned oQ = (unsigned)h << 20, oK = oQ + O_K, oP = O_P + (h * 8 + pj) * PSTR;
  if (wid == 4) {
    for (int l = 0; l < 64; ++l)
      t.v[l] = fo(l < 16 ? g + hsel * 16 + l : l < 24 ? g + 16 + hsel * 8 + l : l == 24 ? g + 48 + hsel * 8 + pj : l < 32 ? g + 64 + hsel * 8 + pj :
                  l < 48 ? 2304 + pj * 16 + l - 32 : 2048 + pj * 16 + l - 48);
    return t;
  }
  auto put64 = [&](int i, u64 x) { t.v[i] = (unsigned)x; t.v[i + 1] = (unsigned)(x >> 32); };
  t.v[T_Q0] = oQ + ((solo && !isY ? pj : 15 - pj) * 128 + wid * 32) * 512;
  t.v[T_K0] = oK + (isY ? cx : 0) * 32768;
  t.v[T_OQ] = oQ + pj * 131072 + (wid >> 1) * 65536 + (wid & 1) * 4096;
  t.v[T_OG] = oK + kv * TSB + mt * 131072 + (wid >> 1) * 65536 + (wid & 1) * (kv ? 8192 : 4096);
  t.v[T_FPREP] = fo(2048 + pj * 16 + u);
  t.v[T_FQ] = fo(g + 32 + hsel * 8 + pj);
  t.v[T_FG] = fo(g + hsel * 16 + kv * 8 + mt);
  t.v[T_FS] = fo(1024 + (qbF * 4 + wid) * 16 + j * 2 + hsel);
  t.v[T_ZO] = O_Z + pj * 64 * SEQ * 2 + (u * 4 + wid) * 64;
  t.v[T_WT] = (pj * 4 + wid) * 8192;
  const u64 own = 0x1ull << (kv * 8 + mt), needQ = isY ? 1ull << (16 + ((15 - pj) >> 1)) : (1ull << (16 + (pj >> 1))) | (1ull << (16 + ((15 - pj) >> 1)));
  put64(T_NPRE, isY ? needQ | (1ull << (cx >> 2)) | (1ull << ((cx + 1) >> 2)) : solo ? needQ : needQ | 0xffffull << 48);
  put64(T_NST, isY ? kvmask(cx >> 2, 3) | (solo ? 0 : 1ull << 25) : kvmask(0, solo ? 3 : 0));
  put64(T_TAB, k_order(isY, rr));
  t.v[T_SLAB] = O_S + qbF * 0x200000 + wid * 65536 + (j * 2 + hsel) * 1024;
  t.v[T_PART] = oP;
  for (int it = 0; it < 2; ++it) {
    unsigned* p = t.v + T_IT + it * T_ITN;
    const bool last = isY || it == 1;
    const int n = isY ? NY : it ? 2 * pj + 2 : nx0 < 2 ? 2 : nx0;
    const u64 need2 = solo ? 0 : kvmask(0, (2 * pj + 1) >> 2) | (0xffffull << 32);
    u64 q[3] = {0, 0, 0};
    unsigned jmp[2] = {0, 0};
    if (isY) {
      p[0] = (1u << (14 - cx)) | (solo ? 0 : 1u << (n - 2));
      q[0] = kvmask(4, (31 - 2 * pj) >> 2) & ~own; jmp[0] = ex * 32768; q[1] = solo ? 0 : 1ull << 24;
      p[1] = 2; p[13] = p[14] = t.v[T_FG];
    } else if (it == 0) {
      p[0] = 2u | (ex ? 1u << (cx - 2) : 0);
      q[0] = kvmask(0, cx > 0 ? (cx - 1) >> 2 : 0);
      if (ex) { q[1] = kvmask(4, 4 + ((ex - 1) >> 2)); jmp[1] = (16 - cx) * 32768; q[2] = need2; } else q[1] = need2;
      p[1] = 2; p[13] = fo(2304 + pj * 16 + u) + j * (8 * 4352 - 8); p[14] = fo(g + 64 + hsel * 8 + pj);
    } else {
      p[0] = p[1] = solo ? 0 : 1;
      q[0] = need2; p[13] = p[14] = fo(g + 48 + hsel * 8 + pj);
    }
    p[2] = 1u << (n - 2);
    p[3] = isY || !it || solo ? 0 : n > 2 ? 1u << (n - 3) : 1u;
    p[4] = !last ? 0 : n > 2 ? 3u << (n - 3) : 1u << 31;
    for (int k = 0; k < 3; ++k) { p[5 + 2 * k] = (unsigned)q[k]; p[6 + 2 * k] = (unsigned)(q[k] >> 32); }
    p[11] = jmp[0]; p[12] = jmp[1];
    p[15] = oK;
    p[16] = (last ? 1 : 0) | (isY && !solo ? 2 : 0) | (n == 2 ? 4 : 0);
    p[17] = wid * 32 - (n == 2 ? 64 : 0);
    p[18] = oQ + (pj * 128 + wid * 32) * 512;
    p[19] = 0;
    p[20] = n;
    p[21] = oK + (isY ? cx : 0) * 32768;
    p[22] = ((p[0] | p[1] | p[2] | p[3] | p[4]) & 0x7fffffffu) << 1;
  }
  return t;
}
template <int I> constexpr TRow t_base = I < 160 ? t_row(I / 5, I % 5, 0) : [] {
  TRow a = t_row(0, (I - 160) * 4, 0), b = t_row(0, (I - 160) * 4, 1);
  for (int l = 0; l < 64; ++l) a.v[l] = b.v[l] - a.v[l];
  return a;
}();
template <int I> constexpr bool t_linear = [] {
  for (int j = 1; j < 8; ++j) {
    const TRow b = t_row(I / 5, I % 5, j);
    for (int l = 0; l < 64; ++l) if (b.v[l] != t_base<I>.v[l] + j * t_base<160 + (I % 5) / 4>.v[l]) return false;
  }
  return true;
}();
struct Tab { TRow row[162]; };
template <int... I> constexpr Tab t_make(iseq<I...>) {
  static_assert((t_linear<(I < 160 ? I : 0)> && ...));
  return Tab{{t_base<I>...}};
}
__device__ const Tab g_tab = t_make(make_iseq<162>{});

__global__ __launch_bounds__(256, 1) __attribute__((amdgpu_num_vgpr(78), amdgpu_num_sgpr(56))) void fused_kernel(const bf16* __restrict__ ckv, const bf16* __restrict__ wuk,
                                                       const bf16* __restrict__ wuv, const bf16* __restrict__ x,
                                                       const bf16* __restrict__ wq, bf16* Qb,
                                                       const bf16* __restrict__ ak, const bf16* __restrict__ av,
                                                       const bf16* __restrict__ ash, const bf16* __restrict__ assmk,
                                                       const bf16* __restrict__ assmD, const bf16* __restrict__ wo,
                                                       const bf16* __restrict__ aq, bf16* __restrict__ out) {
  extern __shared__ __attribute__((aligned(16))) char lds[];
  asm volatile("at_code_begin:" ::: ACLOB_ALL, "v255", "s101");
  const int tid = threadIdx.x, lane = tid & 63;
  const int wid = __builtin_amdgcn_readfirstlane(tid >> 6);
  const int b = blockIdx.x, j = b & 7, r = b >> 3, rr = r & 15, hsel = rr & 1, pj = rr >> 1, kv = pj & 1;
  const bool isY = r >= 16, solo = pj == 7, doP = isY && !solo, doA = !isY && !solo;
  const int h = j + 8 * hsel, mt = (rr >> 2) + (isY ? 4 : 0), pu = (hsel * 8 + j) * 4 + wid;
  constexpr unsigned m0d = 131072 + 24576;
  const unsigned lA = isY ? 65536 : 0, lW = lA ^ 65536, po = isY ? 0 : 32768;
  const unsigned mp[4] = {lA + wid * 1024 + po, lW + wid * 1024 + po, lA + wid * 1024 + (po ^ 32768), lW + wid * 1024 + (po ^ 32768)};
  {
    const int lda = isY ? DA : DC;
    const unsigned ra = wid * 8 + (lane >> 3), sa = ((lane & 7) ^ (((wid & 1) * 4 + (lane >> 4)) & 7)) * 16;
    const unsigned vbA = ra * (lda * 2) + sa, vbB = (4 * wid + (lane >> 4)) * LDW * 2 + 16 * ((lane & 15) ^ (((lane >> 4) << 2) | wid));
    vsetu<92>(vbA); vsetu<93>(vbB); vsetu<90>(vbA + 128); vsetu<91>(vbB + 0x80000); vsetu<94>(ra * (DC * 2) + sa);
    static_for([&](auto J) {
      sset<64 + J>((J + 1) * 64 * lda);
      asm volatile("s_mov_b32 s%c0, %1" ::"i"(71 + J), "i"((((J + 1) >> 2) * 32 + 16 * ((J + 1) & 1)) * LDW * 2 + (((J + 1) >> 1) & 1) * 256));
    }, make_iseq<7>{});
    sset<78>(lA + wid * 1024); sset<79>(lW + wid * 1024);
    GAP();
  }
  asm volatile("" : "+s"(ckv), "+s"(wuk), "+s"(wuv), "+s"(x), "+s"(wq), "+s"(Qb), "+s"(ak), "+s"(av), "+s"(ash), "+s"(assmk),
                    "+s"(assmD), "+s"(wo), "+s"(aq), "+s"(out));
  KST(0, 0);
  bf16* xqT = (bf16*)((char*)Qb + O_X);
  const bf16* gA = ckv + (size_t)mt * 256 * DC;
  const bf16* gW = (kv ? wuv : wuk) + h * 256;
  unsigned dv = 0;
  {
    GM g;
    PR pr;
    if (doP) prep_load(pr, tid, pj, hsel * 8 + j, ak, av, ash);
    srd_set<56>(isY ? x + (size_t)pj * 256 * DA : gA, isY ? 256 * DA * 2 : 256 * DC * 2);
    srd_set<60>(isY ? wq + h * 256 : gW, isY ? (DA - 1) * LDW * 2 + 512 : (DC - 1) * LDW * 2 + 512);
    g_pro<0>(mp, make_iseq<12>{});
    srd_set<88>(Qb, WSB);
    {
      const unsigned* tp = g_tab.row[r * 5].v;
      asm volatile("buffer_load_dwordx2 v[120:121], off, s[88:91], %0 sc1\n\tglobal_load_dword v78, %1, %3\n\tglobal_load_dword v79, %2, %3 offset:1024\n\t"
                   "global_load_dword v118, %2, %4\n\tglobal_load_dword v119, %2, %4 offset:256"
                   ::"s"(fo(6144)), "v"(tid * 4), "v"(lane * 4), "s"(tp), "s"(g_tab.row[160].v) : "memory");
    }
    if (doP) {
      asm volatile("s_waitcnt vmcnt(17)" ::: "memory");
#pragma unroll
      for (int q = 0; q < 7; ++q) asm volatile("" : "+v"(pr.kk[q]));
#pragma unroll
      for (int q = 0; q < 4; ++q) asm volatile("" : "+v"(pr.vv[q]));
      static_for([&](auto Ic) __attribute__((always_inline)) {
        constexpr int I = Ic;
        g_pro<12 + 4 * I>(mp, make_iseq<4>{});
        GAP();
        prep_math<I>(pr, tid, lds + 131072);
        GAP();
      }, make_iseq<4>{});
      asm volatile("s_waitcnt lgkmcnt(0)\n\ts_barrier" ::: "memory");
      prep_store(tid, pj, hsel * 8 + j, xqT, lds + 131072);
    } else g_pro<12>(mp, make_iseq<4>{});
    GAP();
    CODE_TOUCH("at_code_begin", "pf_code_end", r * 4 + wid, 128u, 8, m0d);
    {
      const int fr = lane & 15, fq = lane >> 4, q = (lane & 15) >> 2, p = lane & 3;
#pragma unroll
      for (int k2 = 0; k2 < 2; ++k2)
        g.aA[k2] = lA + (wid >> 1) * 16384 + (fr >> 3) * 1024 + (fr & 7) * 128 + (((4 * k2 + fq) ^ (fr >> 1)) * 16);
#pragma unroll
      for (int hf = 0; hf < 2; ++hf)
#pragma unroll
        for (int nf = 0; nf < 8; ++nf)
          g.aB[hf][nf] = lW + (wid & 1) * 8192 + off_b(8 * fq + 4 * hf + q, nf * 2 + (p >> 1)) + 8 * (p & 1);
      asm volatile("" : "+v"(g.aA[0]), "+v"(g.aA[1]));
#pragma unroll
      for (int nf = 0; nf < 8; ++nf) asm volatile("" : "+v"(g.aB[0][nf]), "+v"(g.aB[1][nf]));
      vsetu<81>(tid == 0 ? 0u : 0x80000000u);
      vsetu<84>(lane * 16); vsetu<85>(lane * 16 + 65536); vsetu<86>(wid * 8192 + lane * 16); vsetu<87>(wid * 8192 + lane * 16 + 4096);
      vsetu<88>(65536 + wid * 256 + fr * 16); vsetu<89>(fr - 4 * fq);
      asm volatile("s_mov_b64 s[92:93], s[88:89]\n\ts_mov_b64 s[94:95], s[90:91]");
    }
    if (doP) asm volatile("s_waitcnt vmcnt(13)" ::: "memory");
    else {
      g_pro<16>(mp, make_iseq<12>{});
      asm volatile("s_waitcnt vmcnt(8)" ::: "memory");
    }
    asm volatile("s_barrier" ::: "memory");
    if (isY) g_read0<0>(g, make_iseq<16>{});
    else g_read0<1>(g, make_iseq<16>{});
    GAP();
    {
      u64 z = (((u64)__float_as_uint(vget<121>()) << 32) | __float_as_uint(vget<120>())) + 0x9e3779b97f4a7c15ull;
      z = (z ^ (z >> 30)) * 0xbf58476d1ce4e5b9ull; z = (z ^ (z >> 27)) * 0x94d049bb133111ebull; z ^= z >> 31;
      vsetu<82>((unsigned)z); vsetu<83>((unsigned)(z >> 32));
      asm volatile("v_mad_u32_u24 v78, v118, %0, v78\n\tv_mad_u32_u24 v79, v119, %0, v79\n\tv_readfirstlane_b32 s84, v82\n\tv_readfirstlane_b32 s85, v83\n\t"
                   "v_readlane_b32 s80, v78, %1\n\tv_readlane_b32 s81, v78, %2" ::"s"(j), "i"((int)T_TAB), "i"(T_TAB + 1));
      if (isY) asm volatile("s_mov_b64 s[80:81], 0x6543210");
      asm volatile("s_bfe_u32 s82, s80, 0x40008\n\ts_lshr_b64 s[80:81], s[80:81], 12\n\tv_lshl_add_u32 v91, s82, 19, v93" ::: "scc");
    }
    asm volatile("s_waitcnt lgkmcnt(0)\n\ts_barrier" ::: "memory");
    g_pro<28>(mp, make_iseq<4>{});
    GAP();
    if (isY) {
      g_tile<false, 0, 1>(g);
      g_loop<false, true>(g, 0x22u, [&](int t) __attribute__((always_inline)) {
        if (t == 1) fl_set<T_FPREP>();
        else if (t == 4) { tload<80, T_TAB>(); tload<81, T_TAB + 1>(); srd_set<60>(gW, (DC - 1) * LDW * 2 + 512); }
        else {
          srd_set<56>(gA, 256 * DC * 2);
          asm volatile("v_mov_b32 v92, v94");
          static_for([&](auto J) { asm volatile("s_mov_b32 s%c0, %1" ::"i"(64 + J), "i"((J + 1) * 64 * DC)); }, make_iseq<7>{});
        }
      });
      tload<101, T_OQ>();
      asm volatile("v_mov_b32 v112, v84\n\tv_add_u32 v113, 8192, v84");
      GAP();
      g_tile<false, 0, 2>(g);
      KST(0, 3);
    }
#define TT 3
    auto kvtile = [&](auto TRc) __attribute__((always_inline)) {
      constexpr bool TR = TRc;
      if (isY) g_tile<TR, 1, 4>(g);
      else g_tile<TR, 1, 1>(g);
      g_loop<TR, false>(g, 0x2000u | (isY || doA ? 2u : 1u << TT), [&](int t) __attribute__((always_inline)) {
        int l = threadIdx.x & 63; asm volatile("" : "+v"(l));
        if (t == 13) {
          asm volatile("s_mov_b32 s79, 0x28000");
          fl_poll<T_NPRE>();
          if (doA) {
            const char* tp = (const char*)assmk + (size_t)(pj * 64 + pu) * (SEQ * 2) + (l & 31) * 128;
            asm volatile("s_mov_b32 m0, %1\n\ts_nop 0\n\tglobal_load_lds_dword %0, off" ::"v"(tp), "i"(m0d) : "memory");
          }
        } else if (isY) fl_set<T_FQ>();
        else if (doA) post_dma(pj, pu, aq, 131072 + wid * 8192);
        else {
          const char* tp = (const char*)ckv + 1024 * DC * 2 + (pu * 256 + l) * 128;
          asm volatile("s_mov_b32 m0, %1\n\ts_nop 0\n\tglobal_load_lds_dword %0, off" ::"v"(tp), "i"(m0d) : "memory");
          asm volatile("global_load_lds_dword %0, off" ::"v"(tp + 8192) : "memory");
          asm volatile("global_load_lds_dword %0, off" ::"v"(tp + 16384) : "memory");
          asm volatile("global_load_lds_dword %0, off" ::"v"(tp + 24576) : "memory");
        }
      });
      tload<101, T_OG>(); tload<96, T_Q0>(); tload<98, T_K0>();
      fl_check();
      tload<50, T_NST>(); tload<51, T_NST + 1>();
      asm volatile("v_mov_b32 v112, v84\n\tv_add_u32 v113, %0, v84\n\tv_mov_b32 v90, v87\n\tv_mov_b32 v91, v87\n\tv_mov_b32 v92, v87\n\tv_mov_b32 v93, v87\n\t"
                   "v_mov_b32 v94, v86\n\tv_mov_b32 v95, v86\n\tv_mov_b32 v126, v86\n\tv_mov_b32 v127, v86\n\ts_mov_b32 s99, 0x8000\n\t" FL_ARM("s[50:51]")
                   ::"i"(TR ? 4096 : 8192));
      if (doA) {
        const int d = pj * 64 + pu;
        const bf16* dp = assmD + d;
        asm volatile("global_load_ushort %0, %1, off" : "=v"(dv) : "v"(dp) : "memory");
        srd_set<56>(assmk + (size_t)d * SEQ, SEQ * 2);
        srd_set<60>(xqT + (size_t)d * SEQ, SEQ * 2);
        sset<78>(65536 + wid * 15488);
        asm volatile("s_movk_i32 s64, 992\n\tv_subrev_u32 v122, 32, v84");
        GAP();
        g_tile<TR, 0, 3>(g);
      } else {
        sset<79>(isY ? wid * 8192 : 0x30000);
        GAP();
        g_tile<TR, 0, 5>(g);
      }
    };
    if (kv) kvtile(std::true_type{});
    else kvtile(std::false_type{});
  }
  KST(0, 4);
  asm volatile("s_waitcnt vmcnt(16)" : "+v"(dv) :: "memory");
  sset<79>(wid * 8192);
  if (doA) {
    const float Dd = bf2f(dv);
    KST(0, 5);
    conv_wave(pj * 64 + pu, Dd, xqT + (size_t)DA * SEQ, lds + 65536 + wid * 15488,
              [&](int cr) __attribute__((always_inline)) {
      if (cr == 8) {
        const char* tp = (const char*)wo + (size_t)h * (HD * DA * 2) + ((pj * 4 + wid) * 64 + lane) * 128;
        asm volatile("s_mov_b32 m0, %1\n\ts_nop 0\n\tglobal_load_lds_dword %0, off" ::"v"(tp), "i"(m0d) : "memory");
      }
      if (cr == 1) {
        asm volatile("s_waitcnt vmcnt(0)\n\ts_barrier" ::: "memory");
        fl_set<T_FG>();
      } else if (cr == 2) asm volatile(FL_POLL ::: "memory");
      else if (cr == 4) {
        asm volatile("s_waitcnt vmcnt(16)" ::: "memory");
        fl_check();
        dma4k<0>();
      }
      else if (cr == 5) dma4k<1>();
      else if (cr == 6) dma4k<2>();
      else if (cr == 7) dma4k<3>();
    });
    asm volatile("s_waitcnt vmcnt(6)" ::: "memory");
    KST(0, 6);
  } else if (!isY) {
    static_for([&](auto I) { d_store<16 + I>(); }, make_iseq<16>{});
    tload<98, T_K0>();
    asm volatile("s_waitcnt vmcnt(0)\n\ts_barrier" ::: "memory");
    fl_set<T_FG>();
    KST(0, 5);
    fl_check();
    dma4k<0>(); dma4k<1>(); dma4k<2>(); dma4k<3>();
    asm volatile("s_waitcnt vmcnt(0)" ::: "memory");
    KST(0, 6);
  } else {
    tload<76, T_PART>();
    fl_check();
    asm volatile("buffer_load_dwordx2 v[122:123], v88, s[88:91], s76 offen offset:1024 sc1" ::: "memory");
  }
  AC c;
  sset<78>(65536 + wid * 8192); sset<79>(98304 + wid * 8192);
  { const bf16* wp = wo + (size_t)h * HD * DA; asm volatile("s_mov_b64 s[80:81], %0" ::"s"(wp)); }
  ev_code();
  asm volatile("s_waitcnt lgkmcnt(0)\n\ts_barrier" ::: "memory");
#pragma unroll 1
  for (int it = doA ? 0 : 1; it < 2; ++it) {
    kreads<0>(make_iseq<8>{});
    const unsigned oP = tget<T_PART>();
    if (it == 0) {
      static_for([&](auto I) { tload<57 + I, T_IT + I>(); }, make_iseq<21>{});
      tload<S_T, T_IT + 21>(); tload<S_EV, T_IT + 22>();
      asm volatile("s_sub_u32 s70, s70, %0\n\ts_add_u32 s76, %1, 1024" ::"s"(j * (8 * 4352 - 8)), "s"(oP) : "scc");
    } else {
      static_for([&](auto I) { tload<57 + I, T_IT + T_ITN + I>(); }, make_iseq<21>{});
      tload<S_T, T_IT + T_ITN + 21>(); tload<S_EV, T_IT + T_ITN + 22>();
      asm volatile("s_mov_b32 s72, 0\n\ts_mov_b32 s75, %0\n\ts_mov_b32 s76, %1" ::"s"(oP + wid * 16384), "s"(oP + 1032));
    }
    unsigned mode, n;
    asm volatile("s_add_u32 s98, s97, 0x8000\n\ts_mov_b32 %0, s73\n\ts_mov_b32 %1, s77\n\tv_mov_b64 v[114:115], 0\n\tv_mov_b64 v[118:119], 0\n\t"
                 "v_mov_b32 v120, 0x45800000\n\t" FL_ARM("s[62:63]") : "=s"(mode), "=s"(n) :: "scc");
    KST(0, 8 + 3 * (it && !isY));
    GAP();
#define STA(k)
    STA(0);
    if (isY) phaseA<0, 2>(c);
    else phaseA<0, 1>(c);
    STA(1);
    if (c.rare) ev_call(0);
    STA(2);
    phaseB0(mode, wid);
    if (__builtin_expect(mode & 4, 0)) ev_call(31);
    STA(3);
#pragma unroll 1
    for (int i = 1;; i += 2) {
      phaseA<1, 0>(c);
      STA(9 + i);
      if (__builtin_expect(c.rare != 0, 0)) { if (c.redo) sm_redo<1>(); ev_call(i); }
      phaseB<1>(c);
      if (i + 1 >= n) break;
      phaseA<0, 0>(c);
      STA(10 + i);
      if (__builtin_expect(c.rare != 0, 0)) { if (c.redo) sm_redo<0>(); ev_call(i + 1); }
      phaseB<0>(c);
    }
    STA(4);
    asm volatile("tail_begin:");
    if ((mode & 3) == 1) asm volatile("v_bfrev_b32 v122, 1");
    else asm volatile("v_mov_b32 v122, v84\n\tbuffer_load_dwordx4 v[128:131], v88, s[88:91], %0 offen sc1" ::"s"(oP) : "memory");
    GAP();
    phaseA<0, 3>(c);
    if (__builtin_expect(c.redo != 0, 0)) sm_redo<0>();
    STA(5);
    KST(0, 9 + 3 * (it && !isY));
    const float l[2] = {row_reduce<false>(vget<R_RS>() + vget<R_TS>()), row_reduce<false>(vget<R_RS + 1>() + vget<R_TS + 1>())};
    const float cm[2] = {vget<R_M>(), vget<R_M + 1>()};
    EP p;
    if (it == 0) {
      const f32x4 ml = {cm[0], l[0], cm[1], l[1]};
      asm volatile("buffer_store_dwordx4 %0, v88, s[88:91], %1 offen sc1\n\ts_mov_b32 s101, %2" ::"v"(ml), "s"(oP), "s"(oP + wid * 16384) : "memory");
      phaseF<0>(c, p, make_iseq<74>{});
      asm volatile("s_waitcnt vmcnt(16)" ::: "memory");
    } else {
      int lz = threadIdx.x & 63; asm volatile("" : "+v"(lz));
      const int rw = lz >> 2;
      static_for([&](auto E) { vsetu<R_TB + E>(rw * 128 + 16 * ((2 * E + ((lz & 3) >> 1)) ^ (rw & 6)) + 8 * (lz & 1)); }, make_iseq<4>{});
      const unsigned vs = ((lz >> 3) & 1) * 16384 + ((lz >> 4) & 1) * 512 + (lz >> 5) * 256 + (lz & 7) * 16;
      vsetu<R_VS>(vs); vsetu<R_VS + 1>(vs + 32768);
      tload<96, T_SLAB>();
      asm volatile("s_mov_b32 s97, s79\n\ts_mov_b32 s100, 0x8000\n\ts_movk_i32 s99, 0x1000");
      if (!(mode & 2)) {
        p.fa[0] = __builtin_amdgcn_rcpf(l[0]); p.fa[1] = __builtin_amdgcn_rcpf(l[1]);
        phaseF<1>(c, p, make_iseq<74>{});
      } else {
        const f32x4 ml = {vget<128>(), vget<129>(), vget<130>(), vget<131>()};
        if (__builtin_expect(__builtin_amdgcn_ballot_w64(ml[0] != cm[0] || ml[2] != cm[1]) == 0, 1)) {
          p.fa[0] = __builtin_amdgcn_rcpf(l[0] + ml[1]); p.fa[1] = __builtin_amdgcn_rcpf(l[1] + ml[3]);
          const unsigned one = (lz >> 4) != ((lz >> 2) & 3) ? 0u : (lz & 1) ? 0x3f800000u : 0x3f80u, o0 = (lz & 2) ? 0u : one, o1 = (lz & 2) ? one : 0u;
          vsetu<224>(o0); vsetu<225>(o1); vsetu<230>(o0); vsetu<231>(o1);
          asm volatile("v_mov_b64 v[226:227], 0\n\tv_mov_b64 v[228:229], 0");
          phaseF<3>(c, p, make_iseq<74>{});
        } else {
#pragma unroll
          for (int e = 0; e < 2; ++e) {
            const float mm = fmaxf(cm[e], ml[2 * e]);
            const float wa = __builtin_amdgcn_exp2f(cm[e] - mm), wb = __builtin_amdgcn_exp2f(ml[2 * e] - mm);
            const float inv = __builtin_amdgcn_rcpf(l[e] * wa + ml[2 * e + 1] * wb);
            p.fa[e] = wa * inv; p.fb[e] = wb * inv;
          }
          phaseF<2>(c, p, make_iseq<74>{});
        }
      }
    }
    KST(0, 10 + 3 * (it && !isY));
  }
  asm volatile("v_xor_b32 v%c0, s100, v%c0\np_loop:" ::"i"(R_TB));
  GAP();
  p_half<0>(make_iseq<32>{});
  p_half<1>(make_iseq<32>{});
  asm volatile("s_cbranch_scc1 p_loop\n\ts_nop 5");
  GAP();
  p_cvt<1, 0>(); p_cvt<1, 1>(); p_cvt<1, 2>(); p_cvt<1, 3>();
  p_store<1, 1>();
  GAP();
  KST(0, 27);
  {
    constexpr unsigned rdy = 98304;
    int lz = threadIdx.x & 63; asm volatile("" : "+v"(lz));
    const int fr = lz & 15, fq = lz >> 4, qbF = isY ? 15 - pj : pj, u = hsel * 8 + j;
    asm volatile("ds_write_b32 %0, %1" ::"v"(rdy), "v"(0) : "memory");
    vsetu<81>(lz == 0 ? 0u : 0x80000000u);
    vsetu<79>(fo(1024) + (qbF * 4 + (u >> 2)) * 4352 + fr * 8);
    {
      const int jj = 4 * (fr >> 3) + (fr & 3);
      const unsigned one = ((fq ^ (fr >> 2)) & 1) ? 0u : (jj & 1) ? 0x3f800000u : 0x3f80u;
      static_for([&](auto I) { vsetu<96 + I>((jj >> 1) == I ? one : 0u); }, make_iseq<4>{});
    }
    unsigned vo = (fq >> 1) * 1024 + (fq & 1) * 256 + fr * 16, vo1 = wid < 3 ? vo + 0x100000 : 0x80000000u;
    __amdgpu_buffer_rsrc_t so_ = __builtin_amdgcn_make_buffer_rsrc((void*)out, (short)0, SEQ * OUTW * 2, 0x00020000);
    unsigned vw = ((qbF * 128 + u * 8 + (fr & 7)) * OUTW + DA + wid * 64 + (fr >> 3) * 32 + fq * 8) * 2;
    unsigned so = O_S + qbF * 0x200000 + (1 + wid) * 0x40000 + u * 16384;
    asm volatile("" : "+s"(so), "+s"(so_));
    asm volatile("s_waitcnt vmcnt(0) lgkmcnt(0)" : "+v"(vo), "+v"(vo1), "+v"(vw) :: "memory");
    fl_set<T_FS>();
    asm volatile("s_barrier" ::: "memory");
    KST(0, 30);
    if (doA) post_math(pj, pu, out, lds + 131072 + wid * 8192);
    unsigned pend = 0x5555u, cnt = 0x400000;
    KST(0, 28);
    while (true) {
      unsigned nw;
      if (wid == 3) {
        u64 m;
        const u64 ex = pend | (pend << 1);
        asm volatile(FL_ARM("%1") "\n\t" FL_POLL "\n\ts_waitcnt vmcnt(0)\n\tv_cmp_eq_u64 %0, s[84:85], v[124:125]" : "=s"(m) : "s"(ex) : "memory");
        nw = (unsigned)m & ((unsigned)m >> 1) & pend;
        if (nw) asm volatile("ds_write_b32 %0, %1" ::"v"(rdy), "v"(~pend | nw) : "memory");
      } else {
        unsigned t;
        asm volatile("ds_read_b32 %0, %1\n\ts_waitcnt lgkmcnt(0)" : "=v"(t) : "v"(rdy) : "memory");
        nw = __builtin_amdgcn_readfirstlane(t) & pend;
      }
      if (--cnt == 0) nw = pend;
      if (nw == 0) {
        if (wid == 3) asm volatile("s_sleep 4");
        else asm volatile("s_sleep 1");
        continue;
      }
      asm volatile("s_nop 4");
      static_for([&](auto S) {
        if ((nw >> (2 * S)) & 1) { r_load<0, 2 * S>(vo, so); r_load<0, 2 * S + 1>(vo, so); r_load<1, 2 * S>(vo1, so); r_load<1, 2 * S + 1>(vo1, so); }
      }, make_iseq<8>{});
      pend &= ~nw;
      if (pend == 0) break;
    }
    asm volatile("s_waitcnt vmcnt(0)" ::: "memory");
    KST(0, 16);
    r_fin<0>(vw, so_, 0);
    if (wid < 3) r_fin<1>(vw, so_, 512);
    KST(0, 29);
    if (b == 128 && wid == 0) asm volatile(FL_SET("%0") ::"s"(fo(6144)) : "memory");
  }
  asm volatile("s_waitcnt vmcnt(0)" ::: "memory");
  asm volatile("pf_code_end:");
  KST(0, 1);
}

extern "C" void kernel_launch(void* const* d_in, const int* in_sizes, int n_in, void* d_out, int out_size, void* d_ws,
                              size_t ws_size, hipStream_t stream) {
  const bf16* Aq = (const bf16*)d_in[0];
  const bf16* Ak = (const bf16*)d_in[1];
  const bf16* Av = (const bf16*)d_in[2];
  const bf16* Ash = (const bf16*)d_in[3];
  const bf16* Assmk = (const bf16*)d_in[4];
  const bf16* AssmD = (const bf16*)d_in[5];
  const bf16* Bx = (const bf16*)d_in[6];
  const bf16* Bckv = (const bf16*)d_in[7];
  const bf16* Bwq = (const bf16*)d_in[8];
  const bf16* Bwuk = (const bf16*)d_in[9];
  const bf16* Bwuv = (const bf16*)d_in[10];
  const bf16* Bwo = (const bf16*)d_in[11];
  bf16* out = (bf16*)d_out;
  hipFuncSetAttribute((const void*)fused_kernel, hipFuncAttributeMaxDynamicSharedMemorySize, 163840);
  fused_kernel<<<256, 256, 163840, stream>>>(Bckv, Bwuk, Bwuv, Bx, Bwq, (bf16*)d_ws, Ak, Av, Ash, Assmk, AssmD, Bwo, Aq, out);
}
```

```cpp
#include <hip/hip_runtime.h>
#include <hip/hip_bf16.h>
#include <stdint.h>
#include <utility>

typedef __hip_bfloat16 bf16;
typedef __attribute__((ext_vector_type(4))) short bf16x4;
typedef __attribute__((ext_vector_type(2))) unsigned u32x2;

constexpr int SEQ = 2048;
constexpr int DA = 448;
constexpr int NH = 16;
constexpr int HD = 256;
constexpr int DC = 1024;
constexpr int OUTW = 896;
constexpr unsigned TSB = NH * SEQ * HD * 2, O_K = TSB, O_S = 3 * TSB, O_P = 5 * TSB, PSTR = 67584, O_F = O_P + 128 * PSTR,
                   O_X = O_F + 400 * 4352, O_Z = O_X + DA * SEQ * 2, WSB = O_Z + DA * SEQ * 2;
constexpr unsigned fo(int w) { return O_F + (w >> 4) * 4352 + (w & 15) * 8; }

#define KST(kid, which)

__device__ __forceinline__ float bf2f(unsigned short u) { return __uint_as_float(((unsigned)u) << 16); }
__device__ __forceinline__ unsigned short f2bf(float f) { return __bfloat16_as_ushort(__float2bfloat16(f)); }
__device__ __forceinline__ float rbf(float f) { return bf2f(f2bf(f)); }
typedef __attribute__((ext_vector_type(2))) __bf16 bf16x2n;
typedef __attribute__((ext_vector_type(2))) float f32x2n;
__device__ __forceinline__ unsigned pk2(float a, float b) {
  return __builtin_bit_cast(unsigned, __builtin_convertvector((f32x2n){a, b}, bf16x2n));
}

__device__ __forceinline__ int off_b(int row, int ch) { return 256 * row + 16 * (ch ^ (((row & 3) << 2) | ((row >> 2) & 3))); }

template <int R> __device__ __forceinline__ void sset(unsigned x) { asm volatile("s_mov_b32 s%c0, %1" ::"i"(R), "s"(x)); }
template <int R> __device__ __forceinline__ void vsetu(unsigned x) { asm volatile("v_mov_b32 v%c0, %1" ::"i"(R), "v"(x)); }
template <int R> __device__ __forceinline__ void srd_set(const void* p, unsigned n) {
  asm volatile("s_mov_b64 s[%c0:%c1], %4\n\ts_mov_b32 s%c2, %5\n\ts_mov_b32 s%c3, 0x20000" ::"i"(R), "i"(R + 1), "i"(R + 2), "i"(R + 3), "s"(p), "s"(n));
}
template <int R, int L> __device__ __forceinline__ void tload() { asm volatile("v_readlane_b32 s%c0, v78, %1" ::"i"(R), "i"(L)); }
template <int L> __device__ __forceinline__ unsigned tget() { unsigned t; asm volatile("v_readlane_b32 %0, v78, %1" : "=s"(t) : "i"(L)); return t; }
#define FL_SET(S) "buffer_store_dwordx2 v[82:83], v81, s[88:91], " S " offen sc1"
#define FL_ARM(N) "v_cndmask_b32_e64 v80, -2.0, v79, " N
#define FL_POLL "buffer_load_dwordx2 v[124:125], v80, s[88:91], 0 offen sc1"
#define FL_CHECK(N) "s_mov_b32 s48, 0x100000\n7:\n\tv_cmp_eq_u64 vcc, s[84:85], v[124:125]\n\ts_and_b64 vcc, vcc, " N "\n\ts_cmp_eq_u64 vcc, " N \
                    "\n\ts_cbranch_scc1 8f\n\ts_sleep 2\n\t" FL_POLL "\n\ts_waitcnt vmcnt(0)\n\ts_sub_u32 s48, s48, 1\n\ts_cbranch_scc0 7b\n8:"
template <int L> __device__ __forceinline__ void fl_set() {
  asm volatile("v_readlane_b32 s48, v78, %0\n\ts_nop 4\n\t" FL_SET("s48") ::"i"(L) : "memory");
}
template <int L> __device__ __forceinline__ void fl_poll() {
  asm volatile("v_readlane_b32 s50, v78, %0\n\tv_readlane_b32 s51, v78, %1\n\ts_nop 1\n\t" FL_ARM("s[50:51]") "\n\t" FL_POLL ::"i"(L), "i"(L + 1) : "memory");
}
__device__ __forceinline__ void fl_check() { asm volatile(FL_CHECK("s[50:51]") ::: "vcc", "scc", "memory"); }
template <int R> __device__ __forceinline__ float vget() { float t; asm volatile("v_mov_b32 %0, v%c1" : "=v"(t) : "i"(R)); return t; }
template <int R> __device__ __forceinline__ void vset(float x) { asm volatile("v_mov_b32 v%c0, %1" ::"i"(R), "v"(x)); }
template <int R> __device__ __forceinline__ float acc_read1() { float t; asm volatile("v_accvgpr_read_b32 %0, a[%c1]" : "=v"(t) : "i"(R)); return t; }
template <class F, int... I> __device__ __forceinline__ void static_for(F&& f, std::integer_sequence<int, I...>) {
  (f(std::integral_constant<int, I>{}), ...);
}
template <int... Is> using iseq = std::integer_sequence<int, Is...>;
template <int N> using make_iseq = std::make_integer_sequence<int, N>;

typedef __attribute__((ext_vector_type(4))) unsigned u32x4c;
typedef __attribute__((ext_vector_type(4))) float f32x4c;
typedef __attribute__((ext_vector_type(2))) float f32x2c;
typedef unsigned long long u64;
struct PR { u32x4c kk[7], vv[4]; };
__device__ __forceinline__ void prep_load(PR& p, int tid, int cg, int tb, const bf16* __restrict__ k, const bf16* __restrict__ v,
                                          const bf16* __restrict__ shift_k) {
  const __amdgpu_buffer_rsrc_t sk = __builtin_amdgcn_make_buffer_rsrc((void*)k, (short)0, SEQ * DA * 2, 0x00020000);
  const __amdgpu_buffer_rsrc_t sv = __builtin_amdgcn_make_buffer_rsrc((void*)v, (short)0, SEQ * DA * 2, 0x00020000);
  const __amdgpu_buffer_rsrc_t sw = __builtin_amdgcn_make_buffer_rsrc((void*)(shift_k + cg * 256), (short)0, 512, 0x00020000);
  const int vo = ((tb * 128 + (tid >> 3) * 4) * DA + cg * 64 + (tid & 7) * 8) * 2;
#pragma unroll
  for (int q = 0; q < 7; ++q)
    asm volatile("buffer_load_dwordx4 %0, %1, %2, 0 offen" : "=v"(p.kk[q]) : "v"(vo + (q - 3) * DA * 2), "s"(sk) : "memory");
#pragma unroll
  for (int q = 0; q < 4; ++q)
    asm volatile("buffer_load_dwordx4 %0, %1, %2, 0 offen offset:%3" : "=v"(p.vv[q]) : "v"(vo), "s"(sv), "i"(q * DA * 2) : "memory");
#pragma unroll
  for (int q = 0; q < 4; ++q)
    asm volatile("buffer_load_dwordx4 a[%c0:%c1], %2, %3, 0 offen offset:%4" ::"i"(4 * q), "i"(4 * q + 3), "v"((tid & 7) * 64), "s"(sw), "i"(q * 16) : "memory");
}
__device__ __forceinline__ float bfl(unsigned u) { return __uint_as_float(u << 16); }
__device__ __forceinline__ float bfh(unsigned u) { return __uint_as_float(u & 0xffff0000u); }
__device__ __forceinline__ f32x2c rbf2(f32x2c a) { const unsigned u = pk2(a[0], a[1]); return (f32x2c){bfl(u), bfh(u)}; }
template <int I> __device__ __forceinline__ void prep_math(const PR& p, int tid, char* lx) {
  const int rp = tid >> 3, c = tid & 7;
  f32x2c wt[4], kf[7];
  const unsigned w[4] = {__float_as_uint(acc_read1<4 * I>()), __float_as_uint(acc_read1<4 * I + 1>()), __float_as_uint(acc_read1<4 * I + 2>()),
                         __float_as_uint(acc_read1<4 * I + 3>())};
#pragma unroll
  for (int t = 0; t < 4; ++t)
    wt[t] = (t & 1) ? (f32x2c){bfh(w[t >> 1]), bfh(w[2 + (t >> 1)])} : (f32x2c){bfl(w[t >> 1]), bfl(w[2 + (t >> 1)])};
#pragma unroll
  for (int q = 0; q < 7; ++q) kf[q] = (f32x2c){bfl(p.kk[q][I]), bfh(p.kk[q][I])};
  f32x2c x[4];
#pragma unroll
  for (int e = 0; e < 4; ++e) {
    f32x2c s = rbf2(kf[e] * wt[3]);
    s = rbf2(s + rbf2(kf[e + 1] * wt[2]));
    s = rbf2(s + rbf2(kf[e + 2] * wt[1]));
    s = rbf2(s + rbf2(kf[e + 3] * wt[0]));
    x[e] = s * (f32x2c){bfl(p.vv[e][I]), bfh(p.vv[e][I])};
  }
#pragma unroll
  for (int u = 0; u < 2; ++u)
    *(u32x2*)(lx + (c * 8 + 2 * I + u) * 256 + (((31 - rp) * 8) ^ (c << 4))) = (u32x2){pk2(x[3][u], x[2][u]), pk2(x[1][u], x[0][u])};
}
__device__ __forceinline__ void prep_store(int tid, int cg, int tb, bf16* __restrict__ xr, const char* lx) {
  const __amdgpu_buffer_rsrc_t sx = __builtin_amdgcn_make_buffer_rsrc((void*)(xr + (size_t)cg * 64 * SEQ), (short)0, 64 * SEQ * 2, 0x00020000);
#pragma unroll
  for (int rd = 0; rd < 4; ++rd) {
    const int idx = tid + 256 * rd, ch = idx >> 4, q = idx & 15;
    u32x4c w = *(const u32x4c*)(lx + ch * 256 + 16 * (q ^ (ch >> 3)));
    __builtin_amdgcn_raw_buffer_store_b128(w, sx, (ch * SEQ + (15 - tb) * 128 + q * 8) * 2, 0, 16);
  }
}

#define CODE_TOUCH(LB, LE, wx, NW, NL, m0v) do { unsigned blo_, bhi_, elo_; \
    asm volatile("s_getpc_b64 s[48:49]\n\ts_add_u32 s48, s48, " LB "@rel32@lo+4\n\ts_addc_u32 s49, s49, " LB "@rel32@hi+12\n\t" \
                 "s_mov_b32 %0, s48\n\ts_mov_b32 %1, s49\n\t" \
                 "s_getpc_b64 s[48:49]\n\ts_add_u32 s48, s48, " LE "@rel32@lo+4\n\ts_mov_b32 %2, s48" \
                 : "=s"(blo_), "=s"(bhi_), "=s"(elo_) :: "scc"); \
    const unsigned n_ = elo_ - (blo_ & ~127u); \
    const __amdgpu_buffer_rsrc_t srd_ = __builtin_amdgcn_make_buffer_rsrc((void*)(((unsigned long long)bhi_ << 32) | (blo_ & ~127u)), (short)0, n_ < 262144u ? n_ : 0u, 0x00020000); \
    const unsigned l_ = threadIdx.x & 63, vo_ = l_ < (NL) ? ((wx) + l_ * (NW)) * 128u : 0x80000000u; \
    asm volatile("s_mov_b32 m0, %0\n\ts_nop 4\n\tbuffer_load_dword %1, %2, 0 offen lds" ::"s"(m0v), "v"(vo_), "s"(srd_) : "memory"); } while (0)
template <int I> __device__ __forceinline__ void d_store() {
  asm volatile("buffer_store_dwordx4 v[%c0:%c1], v%c2, s[88:91], s101 offen offset:%3 sc1" ::"i"(128 + 4 * I), "i"(131 + 4 * I), "i"(112 + ((I >> 2) & 1)),
               "i"((I & 3) * 1024) : "memory");
  if constexpr ((I & 7) == 7) asm volatile("s_addk_i32 s101, 0x4000" ::: "scc");
}
struct CV { unsigned pa[4], pb; };
constexpr int cv_J(int q) { int J = 7; while (q > J) { q -= J + 1; --J; } return J; }
constexpr int cv_i(int q) { int J = 7; while (q > J) { q -= J + 1; --J; } return q; }
template <int R, int N> __device__ __forceinline__ void cv_rd(CV& c) {
  if constexpr (N < 16) {
    constexpr int i = N >> 1, o = 16 * R + 128 * (i & 1) + 2 * (N & 1), r = 32 + 32 * (R & 1) + 2 * N;
    asm volatile("ds_read2_b32 a[%c1:%c2], %0 offset0:%c3 offset1:%c4" ::"v"(c.pa[i >> 1]), "i"(r), "i"(r + 1), "i"(o), "i"(o + 1));
  } else {
    constexpr int m = N - 16, r = 192 + 32 * (R & 1) + 4 * m;
    asm volatile("ds_read_b128 a[%c1:%c2], %0 offset:%c3" ::"v"(c.pb), "i"(r), "i"(r + 3), "i"(64 * R + 512 * (7 - m)));
  }
}
template <int R, int Q, int MODE> __device__ __forceinline__ void cv_slot(CV& c) {
  constexpr int i = cv_i(Q), J = cv_J(Q), d = 4 * J, a = 32 + 32 * (R & 1) + 4 * i, b = 192 + 32 * (R & 1) + 4 * (J - i);
  if constexpr (Q == 0) asm volatile("s_waitcnt lgkmcnt(0)");
  if constexpr (MODE == 1 && R == 0 && i == 0)
    asm volatile("v_mfma_f32_16x16x32_bf16 a[%c0:%c1], a[%c2:%c3], a[%c4:%c5], 0" ::"i"(d), "i"(d + 3), "i"(a), "i"(a + 3), "i"(b), "i"(b + 3));
  else
    asm volatile("v_mfma_f32_16x16x32_bf16 a[%c0:%c1], a[%c2:%c3], a[%c4:%c5], a[%c0:%c1]" ::"i"(d), "i"(d + 3), "i"(a), "i"(a + 3), "i"(b), "i"(b + 3));
  if constexpr (Q < 24 && !(MODE == 2 && R == 1)) cv_rd<R + 1, Q>(c);
  if constexpr (MODE == 1 && Q % 7 == 3) d_store<22 + 5 * R + Q / 7>();
  if constexpr (MODE == 2 && (R == 0 ? Q >= 24 && Q % 3 == 0 : Q % 3 == 0)) {
    constexpr int S_ = R == 0 ? (Q - 24) / 3 : 4 + Q / 3;
    asm volatile("buffer_load_dwordx4 a[%c0:%c1], v84, s[88:91], s96 offen offset:%2 sc1" ::"i"(128 + 4 * S_), "i"(128 + 4 * S_ + 3),
                 "i"((S_ & 3) * 1024) : "memory");
    if constexpr ((S_ & 3) == 3) asm volatile("s_addk_i32 s96, 4096" ::: "scc");
  }
  __builtin_amdgcn_sched_barrier(0);
}
template <int R, int MODE, int... Q> __device__ __forceinline__ void cv_step(CV& c, iseq<Q...>) { (cv_slot<R, Q, MODE>(c), ...); }
template <int... N> __device__ __forceinline__ void cv_rd0(CV& c, iseq<N...>) { (cv_rd<0, N>(c), ...); }
template <class HOOK>
__device__ __forceinline__ void conv_wave(int d, float Dd, bf16* __restrict__ zT, char* lw, HOOK&& hook) {
  int lane = threadIdx.x & 63; asm volatile("" : "+v"(lane));
  const int l16 = lane & 15, fq = lane >> 4;
  u32x4c w[5]; unsigned w4[5];
#pragma unroll
  for (int r = 0; r < 5; ++r) {
    w[r] = *(const u32x4c*)(lw + r * 1024 + lane * 16);
    w4[r] = *(const unsigned*)(lw + r * 1024 + lane * 16 + 16);
  }
  d_store<16>(); d_store<17>();
#pragma unroll
  for (int r = 0; r < 5; ++r)
    *(u32x4c*)(lw + 10304 + r * 1024 + lane * 16) = (u32x4c){__builtin_amdgcn_alignbit(w[r][1], w[r][0], 16), __builtin_amdgcn_alignbit(w[r][2], w[r][1], 16),
                                                             __builtin_amdgcn_alignbit(w[r][3], w[r][2], 16), __builtin_amdgcn_alignbit(w4[r], w[r][3], 16)};
  d_store<18>(); d_store<19>();
  CV c;
#pragma unroll
  for (int i = 0; i < 4; ++i) c.pa[i] = (unsigned)(size_t)lw + ((l16 + 1) & 1) * 10304 + 4 * ((8 * fq + l16 + 1) >> 1) + 1024 * i;
  c.pb = (unsigned)(size_t)lw + 5120 + (2032 - 16 * l16 + 8 * fq) * 2 - 512 * 7;
  asm volatile("" ::: "memory");
  cv_rd0(c, make_iseq<24>{});
  d_store<20>(); d_store<21>();
  __builtin_amdgcn_sched_barrier(0);
  cv_step<0, 1>(c, make_iseq<36>{});
  cv_step<1, 1>(c, make_iseq<36>{});
#pragma unroll 1
  for (int r = 1; r < 3; ++r) {
#pragma unroll
    for (int i = 0; i < 4; ++i) c.pa[i] += 128;
    c.pb += 128;
    __builtin_amdgcn_sched_barrier(0);
    cv_step<0, 0>(c, make_iseq<36>{});
    cv_step<1, 0>(c, make_iseq<36>{});
    hook(r);
  }
#pragma unroll
  for (int i = 0; i < 4; ++i) c.pa[i] += 128;
  c.pb += 128;
  __builtin_amdgcn_sched_barrier(0);
  cv_step<0, 2>(c, make_iseq<36>{});
  cv_step<1, 2>(c, make_iseq<36>{});
  const __amdgpu_buffer_rsrc_t srdZ = __builtin_amdgcn_make_buffer_rsrc((void*)(zT + (size_t)d * SEQ), (short)0, SEQ * 2, 0x00020000);
  asm volatile("s_nop 7");
  static_for([&](auto Jc) __attribute__((always_inline)) {
    constexpr int J = Jc;
    hook(4 + J);
    __builtin_amdgcn_sched_barrier(0);
    const int t = 256 * J + 16 * l16 + 4 * fq;
    bf16x4 xv = *(const bf16x4*)(lw + 5120 + (2044 - t) * 2);
    const float y[4] = {acc_read1<4 * J>(), acc_read1<4 * J + 1>(), acc_read1<4 * J + 2>(), acc_read1<4 * J + 3>()};
    float z[4];
#pragma unroll
    for (int e = 0; e < 4; ++e) z[e] = rbf(y[e]) + rbf(bf2f(xv[3 - e]) * Dd);
    u32x2 w = {pk2(z[0], z[1]), pk2(z[2], z[3])};
    __builtin_amdgcn_raw_buffer_store_b64(w, srdZ, t * 2, 0, 16);
  }, make_iseq<8>{});
}

__device__ __forceinline__ void post_dma(int cg, int tb, const bf16* __restrict__ q, unsigned m0w) {
  int lane = threadIdx.x & 63; asm volatile("" : "+v"(lane));
  const __amdgpu_buffer_rsrc_t sq = __builtin_amdgcn_make_buffer_rsrc((void*)(q + (size_t)tb * 32 * DA + cg * 64), (short)0, 32 * DA * 2, 0x00020000);
  const unsigned vq = ((lane >> 3) * DA + (lane & 7) * 8) * 2;
  asm volatile("s_mov_b32 m0, %0\n\ts_nop 4\n\tbuffer_load_dwordx4 %1, %2, 0 offen lds\n\tbuffer_load_dwordx4 %1, %2, %3 offen offset:1024 lds\n\t"
               "buffer_load_dwordx4 %1, %2, %4 offen offset:2048 lds\n\tbuffer_load_dwordx4 %1, %2, %5 offen offset:3072 lds"
               ::"s"(m0w + 4096), "v"(vq), "s"(sq), "s"(8 * DA * 2 - 1024), "s"(16 * DA * 2 - 2048), "s"(24 * DA * 2 - 3072) : "memory");
}
__device__ __forceinline__ void post_math(int cg, int tb, bf16* __restrict__ out, const char* lw) {
  int lane = threadIdx.x & 63; asm volatile("" : "+v"(lane));
  const int tl = lane >> 3, ck = lane & 7;
  const __amdgpu_buffer_rsrc_t so = __builtin_amdgcn_make_buffer_rsrc((void*)(out + (size_t)tb * 32 * OUTW + cg * 64), (short)0, 32 * OUTW * 2 - 2 * DA, 0x00020000);
  const unsigned vo = (tl * OUTW + ck * 8) * 2;
#pragma unroll
  for (int i = 0; i < 4; ++i) {
    const u32x4c qv = *(const u32x4c*)(lw + 4096 + i * 1024 + lane * 16);
    const char* lz = lw + ck * 64 + 16 * (i ^ (ck >> 1)) + tl * 2;
    u32x4c o;
#pragma unroll
    for (int e = 0; e < 4; ++e) {
      const float z0 = bf2f(*(const unsigned short*)(lz + 2 * e * 512));
      const float z1 = bf2f(*(const unsigned short*)(lz + (2 * e + 1) * 512));
      o[e] = pk2(bfl(qv[e]) * z0, bfh(qv[e]) * z1);
    }
    __builtin_amdgcn_raw_buffer_store_b128(o, so, vo + i * 8 * OUTW * 2, 0, 0);
  }
}

#define DI __device__ __forceinline__
#define GAP() __builtin_amdgcn_sched_barrier(0)
typedef __attribute__((ext_vector_type(4))) float f32x4g;
typedef __attribute__((ext_vector_type(4))) unsigned u32x4g;
constexpr int LDW = NH * HD;
struct GM { unsigned aA[2], aB[2][8]; };
template <bool W, int J, int V> DI void g_dma() {
  if constexpr (J == 0) asm volatile("buffer_load_dwordx4 v%c0, s[%c1:%c2], 0 offen lds" ::"i"(V), "i"(W ? 60 : 56), "i"(W ? 63 : 59) : "memory");
  else asm volatile("buffer_load_dwordx4 v%c0, s[%c1:%c2], s%c3 offen lds" ::"i"(V), "i"(W ? 60 : 56), "i"(W ? 63 : 59), "i"((W ? 70 : 63) + J) : "memory");
}
template <int P, int KS, int I> DI void g_wread(GM& g) {
  constexpr int nf = I >> 1, hf = I & 1, r = 192 - 64 * P + 8 * nf + 4 * KS + 2 * hf;
  asm volatile("ds_read_b64_tr_b16 a[%c1:%c2], %0 offset:%c3" ::"v"(g.aB[hf][nf]), "i"(r), "i"(r + 1), "i"(P * 32768 + KS * 16384));
}
template <int P, int M, int KS> DI void g_aread(GM& g) {
  constexpr int r = 96 + 8 * (M & 1) + 4 * KS;
  asm volatile("ds_read_b128 v[%c1:%c2], %0 offset:%c3" ::"v"(g.aA[KS]), "i"(r), "i"(r + 3), "i"(P * 32768 + M * 2048));
}
constexpr float CS = 0.0625f * 1.4426950408889634f;
struct ES { int op0[129], nop[129]; };
constexpr ES e_sched(bool TR, bool SCL) {
  ES z{};
  const int per = SCL ? 16 : 8;
  int o = 0;
  for (int G = 0; G <= 128; ++G) {
    int cap = G == 128 ? 10000 : SCL ? 4 : 2;
    z.op0[G] = o;
    while (o < 32 * per) {
      const int i = o / per, rdy = (TR ? 32 * (i >> 3) + 16 + 2 * (i & 7) + 1 : 4 * i + 3) + 4;
      if ((G < rdy && G < 128) || cap-- <= 0) break;
      ++o; ++z.nop[G];
    }
  }
  return z;
}
template <bool TR, bool SCL> constexpr ES ESCH = e_sched(TR, SCL);
constexpr int e_vm(int G) { return (G * 3) % 8 < 3 ? G * 3 / 8 : -1; }
constexpr int e_st(int MODE, int G) {
  if (MODE == 5) return e_vm(G) >= 32 ? e_vm(G) - 32 : -1;
  const int g0 = MODE == 4 ? 5 : MODE == 2 ? 13 : 34, gs = MODE == 2 ? 7 : 6;
  return MODE >= 2 && G >= g0 && (G - g0) % gs == 0 && (G - g0) / gs < 16 ? (G - g0) / gs + (MODE == 4 ? 16 : 0) : -1;
}
constexpr int e_nst(int MODE, int G0, int G1) { int n = 0; for (int G = G0; G < G1; ++G) n += e_st(MODE, G) >= 0; return n; }
static_assert(e_nst(2, 0, 128) == 16 && e_nst(3, 0, 128) == 16 && e_nst(5, 0, 128) == 16 && e_nst(4, 0, 104) == 16);
template <bool TR, bool SCL, int O> DI void e_op() {
  constexpr int per = SCL ? 16 : 8, i = O / per, k = O % per, r = 128 + 4 * i, t = 114 + 4 * (i & 1), c = k - (per - 4);
  if constexpr (k < 4) asm volatile("v_accvgpr_read_b32 v%c0, a[%c1]" ::"i"(t + k), "i"(4 * i + k));
  else if constexpr (c < 0) asm volatile("v_mul_f32 v%c0, %1, v%c0" ::"i"(k < 8 ? r + (k ^ 6) : t + k - 8), "s"(CS));
  else {
    constexpr int d = c == 0 ? r + 3 : c == 1 ? r + 2 : r + c - 2, x = c == 0 ? r + 2 : c == 1 ? r : t + 2 * (c - 2);
    asm volatile("v_cvt_pk_bf16_f32 v%c0, v%c1, v%c2" ::"i"(d), "i"(x), "i"(x + 1));
  }
}
template <bool TR, bool SCL, int B, int... Is> DI void e_ops(iseq<Is...>) { (e_op<TR, SCL, B + Is>(), ...); }
constexpr int e_slot(int MODE, int G) { return MODE == 3 ? (G % 3 == 0 && G < 30 ? G / 3 : -1) : e_vm(G) >= 0 && e_vm(G) < 32 ? 10 + e_vm(G) : -1; }
template <int S> DI void e_m0() {
  if constexpr (S == 0 || S == 1 || S == 5 || S == 9) asm volatile("s_add_u32 m0, s78, %0" ::"i"(S * 1024));
  if constexpr (S >= 26 && (S - 26) % 4 == 0) asm volatile("s_add_u32 m0, s79, %0" ::"i"(((S - 26) >> 3) * 32768 + ((S - 26) & 4) * 1024));
}
template <int S> DI void e_load() {
  if constexpr (S == 0) asm volatile("buffer_load_dwordx4 v122, s[56:59], 0 offen lds" ::: "memory");
  else if constexpr (S < 5) asm volatile("buffer_load_dwordx4 v84, s[56:59], s64 offen offset:%0 lds" ::"i"((S - 1) * 1024) : "memory");
  else if constexpr (S < 9) asm volatile("buffer_load_dwordx4 v84, s[60:63], 0 offen offset:%0 sc1 lds" ::"i"((S - 5) * 1024) : "memory");
  else if constexpr (S == 9) asm volatile("buffer_load_dwordx4 v84, s[60:63], s62 offen sc1 lds" ::: "memory");
  else if constexpr (S < 26) {
    constexpr int f = S - 10;
    asm volatile("buffer_load_dwordx4 a[%c0:%c1], v84, s[88:91], s96 offen offset:%2 sc1" ::"i"(128 + 4 * f), "i"(131 + 4 * f), "i"((f & 3) * 1024) : "memory");
    if constexpr ((f & 3) == 3) asm volatile("s_addk_i32 s96, 4096");
  } else {
    constexpr int f = S - 26;
    asm volatile("buffer_load_dwordx4 v%c0, s[88:91], s98 offen offset:%1 sc1 lds" ::"i"(86 + ((f >> 2) & 1)), "i"((f & 3) * 1024) : "memory");
    if constexpr (f == 7) asm volatile("s_add_u32 s98, s98, 0x8000");
    if constexpr (f == 15) asm volatile(FL_POLL ::: "memory");
  }
}
template <bool TR, int P, int H, int G, int MODE> DI void g_gap(GM& g) {
  constexpr int m = 4 * H + (G >> 4), q = G & 15, n = q >> 1, ks = q & 1, GG = 64 * H + G;
  constexpr bool inV = TR ? m & 1 : n & 1, END = MODE == 3 || MODE == 5, EPI = END || MODE == 2;
  constexpr int a = 96 + 8 * (m & 1) + 4 * ks, b = 192 - 64 * P + 8 * n + 4 * ks, d = (inV ? 128 : 0) + 4 * (TR ? 8 * (m >> 1) + n : 4 * m + (n >> 1));
  constexpr int J = END ? -1 : q == 8 ? 2 * (G >> 4) : q == 12 ? 2 * (G >> 4) + 1 : -1, gj = (J & 3) ^ ((J & 3) >> 1);
  if constexpr (END) { if constexpr (q == 0 && GG > 0) asm volatile("s_waitcnt lgkmcnt(0)"); e_m0<e_slot(MODE, GG)>(); }
  else if constexpr (q == 0 && G > 0) asm volatile("s_waitcnt lgkmcnt(4)");
  if constexpr (J == 0) asm volatile("s_add_u32 m0, s78, %0" ::"i"((P ^ H ^ 1) * 32768 + (1 - H) * 8192));
  else if constexpr (J == 4) asm volatile("s_add_u32 m0, s79, %0" ::"i"((P ^ H) * 32768 + (1 - H) * 16384));
  else if constexpr (J > 0 && (J & 3) == 1) asm volatile("s_bitset1_b32 m0, 12");
  else if constexpr (J > 0 && (J & 3) == 2) asm volatile("s_bitset1_b32 m0, %0" ::"i"(J < 4 ? 14 : 13));
  else if constexpr (J > 0) asm volatile("s_bitset0_b32 m0, 12");
#define GMF(D, X, Y, C) asm volatile("v_mfma_f32_16x16x32_bf16 " D "[%c0:%c1], " X ", " Y ", " C ::"i"(d), "i"(d + 3), "i"(a), "i"(a + 3), "i"(b), "i"(b + 3))
#define GMF2(D, C) do { if constexpr (TR) GMF(D, "v[%c2:%c3]", "a[%c4:%c5]", C); else GMF(D, "a[%c4:%c5]", "v[%c2:%c3]", C); } while (0)
  if constexpr ((MODE == 1 || MODE == 4) && ks == 0) { if constexpr (inV) GMF2("v", "0"); else GMF2("a", "0"); }
  else if constexpr (inV) GMF2("v", "v[%c0:%c1]");
  else GMF2("a", "a[%c0:%c1]");
#undef GMF2
#undef GMF
  if constexpr (e_st(MODE, GG) >= 0) {
    static_assert(MODE == 4 || ESCH<TR, MODE == 2>.op0[GG] >= (e_st(MODE, GG) + 1) * (MODE == 2 ? 16 : 8));
    d_store<e_st(MODE, GG)>();
  }
  if constexpr (EPI) e_ops<TR, MODE == 2, ESCH<TR, MODE == 2>.op0[GG]>(make_iseq<ESCH<TR, MODE == 2>.nop[GG]>{});
  if constexpr (J >= 0 && J < 4) g_dma<false, (1 - H) * 2 + (gj & 1) + (gj >> 1) * 4, 90>();
  else if constexpr (J >= 4) g_dma<true, (1 - H) * 4 + gj, 91>();
  if constexpr ((q == 1 || q == 2) && !(END && m == 7)) g_aread<(m < 7 ? P : P ^ 1), (m + 1) & 7, q - 1>(g);
  if constexpr (END) {
    if constexpr (e_slot(MODE, GG) >= 0) e_load<e_slot(MODE, GG)>();
    if constexpr (!TR && GG >= 3 && GG < 7 && e_slot(MODE, GG) < 0) asm volatile("s_nop 0");
    if constexpr (TR && GG >= 19 && GG < 21 && e_slot(MODE, GG) < 0) asm volatile("s_nop 0");
  } else {
    if constexpr (q >= 3 && q < 11 && (q & 1)) g_wread<P ^ 1, H, (G >> 4) * 4 + (q - 3) / 2>(g);
    if constexpr (H == 1 && G == 0) asm volatile("v_lshl_add_u32 v90, s82, 7, v92\n\ts_and_b32 s82, s80, 15");
    if constexpr (H == 1 && G == 4) asm volatile("s_lshr_b64 s[80:81], s[80:81], 4\n\tv_lshl_add_u32 v91, s82, 19, v93");
    constexpr int nst = e_nst(MODE, H ? 29 : 0, GG + 1);
    if constexpr (G == 61) asm volatile("s_waitcnt vmcnt(%0) lgkmcnt(0)\n\ts_barrier" ::"i"(12 + nst) : "memory");
  }
  if constexpr (EPI && GG == 127) {
    asm volatile("s_nop 7");
    e_ops<TR, MODE == 2, ESCH<TR, MODE == 2>.op0[128]>(make_iseq<ESCH<TR, MODE == 2>.nop[128]>{});
  }
  GAP();
}
template <bool TR, int P, int H, int MODE, int... Gs> DI void g_half_(GM& g, iseq<Gs...>) { (g_gap<TR, P, H, Gs, MODE>(g), ...); }
template <bool TR, int P, int H, int MODE> DI void g_half(GM& g) { g_half_<TR, P, H, MODE>(g, make_iseq<64>{}); }
template <bool TR, int P, int MODE> DI void g_tile(GM& g) { g_half<TR, P, 0, MODE>(g); g_half<TR, P, 1, MODE>(g); }
template <int P, int... Is> DI void g_read0(GM& g, iseq<Is...>) {
  (g_wread<P, 0, Is>(g), ...); (g_wread<P, 1, Is>(g), ...);
  g_aread<P, 0, 0>(g); g_aread<P, 0, 1>(g);
}
template <int I> DI void g_pro1(const unsigned (&m)[4]) {
  constexpr int grp = I >> 2, i = I & 3, t = grp < 4 ? 0 : grp < 7 ? 1 : 2;
  constexpr bool isA = grp == 0 || grp == 3 || grp == 5;
  constexpr int jj = (grp == 3 ? 2 : 0) + (i & 1) + (i >> 1) * 4, ks = grp == 2 || grp == 6;
  asm volatile("s_add_u32 m0, %0, %1\n\ts_nop 0" ::"s"(m[(t & 1) * 2 + !isA]), "i"(isA ? jj * 4096 : ks * 16384 + (i >> 1) * 8192 + (i & 1) * 4096) : "scc");
  if constexpr (isA) g_dma<false, jj, t ? 90 : 92>();
  else g_dma<true, ks * 4 + i, t ? 91 : 93>();
}
template <int B, int... Is> DI void g_pro(const unsigned (&m)[4], iseq<Is...>) { (g_pro1<B + Is>(m), ...); }
template <bool TR, bool SCL, class HOOK> DI void g_loop(GM& g, unsigned evm, HOOK&& hook) {
  constexpr int nkt = SCL ? 7 : 16, P0 = SCL ? 0 : 1;
  asm volatile("" : "+s"(evm));
#pragma unroll 1
  for (int t = 1;; t += 2) {
    g_half<TR, P0 ^ 1, 0, 0>(g);
    if (__builtin_expect((evm >> t) & 1, 0)) { hook(t); GAP(); }
    g_half<TR, P0 ^ 1, 1, 0>(g);
    if (SCL && t + 2 >= nkt) break;
    g_half<TR, P0, 0, 0>(g);
    if constexpr (SCL) { if (__builtin_expect(t == 3, 0)) { hook(4); GAP(); } }
    else asm volatile("s_cmp_eq_u32 %0, 13\n\ts_cselect_b32 s78, 0x28000, s78" ::"s"(t) : "scc");
    g_half<TR, P0, 1, 0>(g);
    if (!SCL && t + 3 >= nkt) break;
  }
}

typedef __attribute__((ext_vector_type(4))) unsigned u32x4;
typedef __attribute__((ext_vector_type(4))) float f32x4;

constexpr int R_S = 128, R_P = 192, R_CI = 224, R_E = 96;
constexpr int R_TS = 112, R_RS = 114, R_M = 116;
constexpr int R_PD = 118, R_THR = 120, R_TMP = 121;
constexpr int r_dk(int q) { return q < 6 ? 90 + q : 120 + q; }
constexpr int S_V = 96, S_T = 97, S_K = 98, S_I = 99, S_EV = 100;
struct AC {
  u64 redo;
  unsigned rare;
};
template <int X, int F> DI void kread() {
  constexpr int r = 192 + 4 * (F & 15);
  asm volatile("ds_read_b128 a[%c0:%c1], v84 offset:%c2" ::"i"(r), "i"(r + 3), "i"(X * 32768 + F * 1024));
}
template <int X, int F> DI void vread() {
  constexpr int r = 192 + 4 * (F & 15);
  asm volatile("ds_read_b128 a[%c0:%c1], v85 offset:%c2" ::"i"(r), "i"(r + 3), "i"(X * 32768 + (F & 1) * 16384 + (F >> 1) * 1024));
}
template <int X, int... Fs> DI void kreads(iseq<Fs...>) { (kread<X, Fs>(), ...); }
template <int X, int J> DI void dma_m0() {
  if constexpr (J == 0) asm volatile("s_mov_b32 m0, s%c0" ::"i"(78 + X));
  else if constexpr (J == 4) asm volatile("s_or_b32 m0, s%c0, 0x1000" ::"i"(78 + X));
  else if constexpr (J == 8) asm volatile("s_bitset0_b32 m0, 16");
  else if constexpr (J == 12) asm volatile("s_bitset0_b32 m0, 12");
}
template <int J> DI void dma_at() {
  if constexpr (J < 8)
    asm volatile("buffer_load_dwordx4 v%c0, s[88:91], s96 offen offset:%1 sc1 lds" ::"i"(86 + (J >= 4)), "i"((J & 3) * 1024) : "memory");
  else
    asm volatile("buffer_load_dwordx4 v%c0, s[92:95], s98 offen offset:%1 sc1 lds" ::"i"(r_dk(J - 8)), "i"((J & 3) * 1024) : "memory");
}
template <int Q> DI void dma4k() {
  asm volatile("s_add_u32 m0, s79, %0\n\ts_add_u32 s48, s98, %1\n\tbuffer_load_dwordx4 v%c2, s[88:91], s48 offen sc1 lds\n\t"
               "buffer_load_dwordx4 v%c2, s[88:91], s48 offen offset:1024 sc1 lds\n\tbuffer_load_dwordx4 v%c2, s[88:91], s48 offen offset:2048 sc1 lds\n\t"
               "buffer_load_dwordx4 v%c2, s[88:91], s48 offen offset:3072 sc1 lds"
               ::"i"((Q >> 1) * 32768 + (Q & 1) * 4096), "i"((Q >> 1) * 32768), "i"(86 + (Q & 1)) : "memory", "scc");
}
template <int Q> DI void acc_zero2() {
  asm volatile("v_accvgpr_write_b32 a[%c0], 0\n\tv_accvgpr_write_b32 a[%c1], 0" ::"i"(2 * Q), "i"(2 * Q + 1));
}
struct SMS { int ex[128], op0[128], nop[128], last; };
constexpr SMS sm_sched() {
  SMS z{};
  int et[32] = {}, e = 0;
  for (int t = 0; t < 128; ++t) {
    const int g = t & 15;
    const bool x = t < 48 ? (g >= 8 && (g & 1)) : (g >= 8 && g != 10 && g != 13 && g != 15);
    z.ex[t] = x && e < 32 ? e : -1;
    if (z.ex[t] >= 0) et[e++] = t;
  }
  int o = 0;
  for (int t = 0; t < 128; ++t) {
    const int g = t & 15;
    int cap = g < 8 ? (g == 0 ? 0 : 1) : (z.ex[t] >= 0 || t < 48 ? 0 : 2);
    z.op0[t] = o; z.nop[t] = 0;
    while (cap > 0 && o < 48 && et[2 * (o / 3) + (o % 3 == 0 ? 0 : 1)] + 2 <= t) {
      const int el = 2 * (o / 3) + o % 3;
      if (!(o % 3 < 2 && (el == 0 || el == 4))) --cap;
      ++o; ++z.nop[t]; z.last = t;
    }
  }
  return z;
}
constexpr SMS SM = sm_sched();
static_assert(SM.last <= 115);
constexpr int p_reg(int Y, int k) { return R_P + 16 * Y + 4 * ((k >> 3) * 2 + ((k >> 1) & 1)) + ((k >> 2) & 1) * 2 + (k & 1); }
template <int Y, int O> DI void sm_op() {
  constexpr int k = O / 3, e = 2 * k + O % 3, ts = R_TS + ((e >> 2) & 1);
  if constexpr (O % 3 == 2) asm volatile("v_cvt_pk_bf16_f32 v%c0, v%c1, v%c2" ::"i"(p_reg(Y, k)), "i"(R_E + ((2 * k) & 15)), "i"(R_E + ((2 * k + 1) & 15)));
  else if constexpr (e == 1 || e == 5) asm volatile("v_add_f32_e32 v%c0, v%c1, v%c2" ::"i"(ts), "i"(R_E + e - 1), "i"(R_E + e));
  else if constexpr (e != 0 && e != 4) asm volatile("v_add_f32_e32 v%c0, v%c0, v%c1" ::"i"(ts), "i"(R_E + (e & 15)));
}
template <int Y, int T> DI void sm_gap(AC& c) {
  if constexpr (SM.ex[T] >= 0) asm volatile("v_exp_f32 v%c0, v%c1" ::"i"(R_E + (SM.ex[T] & 15)), "i"(R_S + 32 * Y + SM.ex[T]));
  if constexpr (SM.nop[T] > 0) sm_op<Y, SM.op0[T]>();
  if constexpr (SM.nop[T] > 1) sm_op<Y, SM.op0[T] + 1>();
  if constexpr (SM.nop[T] > 2) sm_op<Y, SM.op0[T] + 2>();
  if constexpr (T == SM.last + 1) asm volatile("v_add_f32_e32 v%c0, v%c1, v%c2" ::"i"(R_TMP), "i"(R_TS), "i"(R_TS + 1));
  if constexpr (T == SM.last + 2) asm volatile("v_cmp_nle_f32_e64 %0, v%c1, v%c2" : "=s"(c.redo) : "i"(R_TMP), "i"(R_THR));
}
template <int X, int MODE, int G> DI void gapA(AC& c) {
  constexpr bool FIRST = MODE == 1 || MODE == 2, NOQK = MODE == 3;
  constexpr int Xp = X ^ 1, kb = G >> 4, g = G & 15, qb = g >> 3, s = g & 7;
  if constexpr (NOQK && G < 48 && G % 3 == 0) {
    constexpr int f = G / 3;
    asm volatile("buffer_load_dwordx4 a[%c0:%c1], v122, s[88:91], s75 offen offset:%2 sc1" ::"i"(128 + 4 * f), "i"(131 + 4 * f), "i"((f & 3) * 1024) : "memory");
    if constexpr ((f & 3) == 3) asm volatile("s_addk_i32 s75, 4096");
  }
  if constexpr (G == 48) asm volatile("s_waitcnt vmcnt(%0) lgkmcnt(0)\n\ts_barrier" ::"i"(NOQK ? 16 : MODE == 2 ? 12 : 0) : "memory");
  else if constexpr (g == 0 && !NOQK) asm volatile("s_waitcnt lgkmcnt(0)");
  if constexpr (!NOQK) {
    constexpr int r = 192 + 4 * ((kb & 1) * 8 + s), q = 128 + 4 * g;
    constexpr int d = R_S + 32 * X + 4 * (kb * 2 + qb), ci = s == 0 ? R_CI + 4 * (kb * 2 + qb) : d;
    if constexpr (FIRST && s == 0)
      asm volatile("v_mfma_f32_16x16x32_bf16 v[%c0:%c1], a[%c2:%c3], a[%c4:%c5], 0" ::"i"(d), "i"(d + 3), "i"(r), "i"(r + 3), "i"(q), "i"(q + 3));
    else
      asm volatile("v_mfma_f32_16x16x32_bf16 v[%c0:%c1], a[%c2:%c3], a[%c4:%c5], v[%c6:%c7]" ::"i"(d), "i"(d + 3), "i"(r), "i"(r + 3), "i"(q), "i"(q + 3),
                   "i"(ci), "i"(ci + 3));
    if constexpr (g < 8 && kb < 3) kread<X, (kb + 1) * 8 + g>();
    if constexpr (G == 54) asm volatile("s_add_u32 s96, s97, 0x1000000");
    if constexpr (G == 55) dma_m0<X, 0>();
    if constexpr (G >= 56 && !(G & 1)) dma_at<(G - 56) / 2>();
    if constexpr (G == 57) asm volatile("s_mov_b32 s97, s98\n\ts_lshr_b32 s100, s100, 1\n\ts_add_u32 s98, s98, s99");
    if constexpr (G == 59 && !FIRST)
      asm volatile("s_and_b32 s101, s100, 1\n\ts_or_b32 s101, s101, %1\n\ts_or_b32 %0, s101, %2" : "=s"(c.rare) : "s"((unsigned)c.redo), "s"((unsigned)(c.redo >> 32)));
    if constexpr (G == 59 && FIRST) asm volatile("s_and_b32 %0, s100, 1" : "=s"(c.rare));
  } else if constexpr (SM.nop[64 + G] > 0) asm volatile("s_nop 0");
  if constexpr (!FIRST && g < 8 && kb == 3) vread<Xp, g>();
  if constexpr (MODE == 2 && (G & 3) == 1) d_store<16 + G / 4>();
  if constexpr (FIRST) acc_zero2<G>();
  else sm_gap<Xp, 64 + G>(c);
  GAP();
}
template <int R> DI void rescale1(float alpha) {
  float t;
  asm volatile("v_accvgpr_read_b32 %0, a[%c2]\n\tv_mul_f32 %0, %0, %1\n\tv_accvgpr_write_b32 a[%c2], %0" : "=&v"(t) : "v"(alpha), "i"(R));
}
template <int... Rs> DI void rescale_all(const float (&alpha)[2], iseq<Rs...>) { (rescale1<Rs>(alpha[(Rs >> 2) & 1]), ...); }
template <bool MAX> DI float row_reduce(float v) {
  auto p = __builtin_amdgcn_permlane32_swap(__float_as_uint(v), __float_as_uint(v), false, false);
  v = MAX ? fmaxf(__uint_as_float(p[0]), __uint_as_float(p[1])) : __uint_as_float(p[0]) + __uint_as_float(p[1]);
  p = __builtin_amdgcn_permlane16_swap(__float_as_uint(v), __float_as_uint(v), false, false);
  return MAX ? fmaxf(__uint_as_float(p[0]), __uint_as_float(p[1])) : __uint_as_float(p[0]) + __uint_as_float(p[1]);
}
template <int Y, int QB> DI float row_max() {
  constexpr int r = R_S + 32 * Y + 4 * QB;
  float mx;
  asm volatile("v_max_f32 %0, v%c1, v%c2\n\tv_max3_f32 %0, %0, v%c3, v%c4" : "=&v"(mx) : "i"(r), "i"(r + 1), "i"(r + 2), "i"(r + 3));
  static_for([&](auto K) {
    constexpr int q = r + 8 * (K + 1);
    float t = mx;
    asm volatile("v_max3_f32 %0, %0, v%c1, v%c2\n\tv_max3_f32 %0, %0, v%c3, v%c4" : "+v"(t) : "i"(q), "i"(q + 1), "i"(q + 2), "i"(q + 3));
    mx = t;
  }, make_iseq<3>{});
  return row_reduce<true>(mx);
}
template <int R> DI void v_sub(float d) { asm volatile("v_sub_f32 v%c0, v%c0, %1" ::"i"(R), "v"(d)); }
template <int B> DI void s_shift(const float (&d)[2]) { static_for([&](auto E) { v_sub<B + E>(d[(E >> 2) & 1]); }, make_iseq<32>{}); }
DI void m_shift(const float (&d)[2]) {
  s_shift<R_CI>(d);
  v_sub<R_M>(-d[0]); v_sub<R_M + 1>(-d[1]);
}
DI void set_mask0(int wid) {
  const int mr = wid * 32 + __float_as_int(vget<89>());
  static_for([&](auto I) {
    constexpr int e = I, k = (e >> 3) * 16 - ((e >> 2) & 1) * 16 + (e & 3);
    const float x = vget<R_S + e>();
    vset<R_S + e>(k > mr ? -INFINITY : x);
  }, make_iseq<32>{});
}
template <int X> DI void sm_redo() {
  constexpr int Xp = X ^ 1;
  float d[2], dt[2], alpha[2], ts[2] = {0.f, 0.f};
  static_for([&](auto Q) {
    constexpr int qb = Q;
    const float pd = vget<R_PD + qb>();
    d[qb] = fmaxf(row_max<Xp, qb>() - pd, 0.f);
    dt[qb] = d[qb] + pd;
    vset<R_PD + qb>(d[qb]);
    alpha[qb] = __builtin_amdgcn_exp2f(-d[qb]);
    vset<R_RS + qb>(vget<R_RS + qb>() * alpha[qb]);
  }, make_iseq<2>{});
  vset<R_THR>(d[0] > 0.f || d[1] > 0.f ? -1.f : 4096.f);
  m_shift(d);
  static_for([&](auto K) {
    constexpr int k = K, qb = (k >> 1) & 1;
    const float e0 = __builtin_amdgcn_exp2f(vget<R_S + 32 * Xp + 2 * k>() - dt[qb]), e1 = __builtin_amdgcn_exp2f(vget<R_S + 32 * Xp + 2 * k + 1>() - dt[qb]);
    ts[qb] += e0; ts[qb] += e1;
    vset<p_reg(Xp, k)>(__uint_as_float(pk2(e0, e1)));
  }, make_iseq<16>{});
  vset<R_TS>(ts[0]); vset<R_TS + 1>(ts[1]);
  rescale_all(alpha, make_iseq<128>{});
  asm volatile("s_nop 1" ::: "memory");
  GAP();
}
template <int X, int MODE, int... Gs> DI void phaseA_(AC& c, iseq<Gs...>) { (gapA<X, MODE, Gs>(c), ...); }
template <int X, int MODE> DI void phaseA(AC& c) { phaseA_<X, MODE>(c, make_iseq<64>{}); }

#define PG 55
template <int X, bool SMX, int G> DI void gapB(AC& c) {
  constexpr int Xp = X ^ 1, blk = G >> 4, g = G & 15;
  if constexpr (g == 0) asm volatile("s_waitcnt lgkmcnt(0)");
  if constexpr (SMX && g == 7 && blk < 3) dma_m0<X, blk * 4 + 4>();
  constexpr int qb = (g >> 1) & 1, s = g & 1, r = 192 + 4 * ((blk & 1) * 8 + (g >> 2) * 2 + s), o = 4 * ((blk * 4 + (g >> 2)) * 2 + qb);
  constexpr int p = R_P + 16 * Xp + 4 * (s * 2 + qb);
  asm volatile("v_mfma_f32_16x16x32_bf16 a[%c0:%c1], a[%c2:%c3], v[%c4:%c5], a[%c0:%c1]" ::"i"(o), "i"(o + 3), "i"(r), "i"(r + 3), "i"(p), "i"(p + 3));
  if constexpr (SMX && (G == 1 || G == 2)) asm volatile("v_add_f32_e32 v%c0, v%c0, v%c1" ::"i"(R_RS + G - 1), "i"(R_TS + G - 1));
  if constexpr (g < 8) {
    if constexpr (blk < 3) vread<Xp, (blk + 1) * 8 + g>();
    else if constexpr (SMX) kread<Xp, g>();
  }
  if constexpr (SMX) {
    if constexpr (g >= 8 && !(g & 1) && blk < 3) dma_at<blk * 4 + 4 + (g - 8) / 2>();
    if constexpr (G == 3) asm volatile("s_lshr_b32 s77, s77, 1");
    if constexpr (G == 4) asm volatile("s_bitcmp1_b32 s77, 0");
    if constexpr (G == 5) asm volatile("s_cselect_b64 s[52:53], s[62:63], 0");
    if constexpr (G == 48) asm volatile(FL_ARM("s[52:53]"));
    if constexpr (G == PG) asm volatile(FL_POLL ::: "memory");
    sm_gap<X, G>(c);
  }
  GAP();
}
template <int X, int... Gs> DI void phaseB_(AC& c, iseq<Gs...>) { (gapB<X, true, Gs>(c), ...); }
template <int X> DI void phaseB(AC& c) { phaseB_<X>(c, make_iseq<64>{}); }
constexpr int sm_nex() { int n = 0; for (int t = 0; t < 64; ++t) n += SM.ex[t] >= 0; return n; }
DI void phaseB0(unsigned mode, int wid) {
  dma_m0<0, 4>();
  kreads<1>(make_iseq<8>{});
  dma_at<4>();
  asm volatile("s_nop 3");
  float d[2];
  if (mode & 2) {
    d[0] = vget<122>(); d[1] = vget<123>();
    dma_at<5>();
  } else {
    if (__builtin_expect(mode & 4, 0)) set_mask0(wid);
    d[0] = row_max<0, 0>(); GAP(); dma_at<5>();
    d[1] = row_max<0, 1>();
#pragma unroll
    for (int qb = 0; qb < 2; ++qb) if (!(d[qb] > -3.0e38f)) d[qb] = 0.f;
  }
  GAP();
  vset<R_M>(d[0]); vset<R_M + 1>(d[1]);
  dma_at<6>();
  const f32x2c nd[2] = {{-d[0], -d[0]}, {-d[1], -d[1]}};
  static_for([&](auto E) {
    constexpr int e = E;
    const f32x2c v = nd[(e >> 1) & 1];
    asm volatile("v_pk_add_f32 v[%c0:%c1], v[%c0:%c1], %4\n\tv_mov_b64 v[%c2:%c3], %4" ::"i"(R_S + 2 * e), "i"(R_S + 2 * e + 1), "i"(R_CI + 2 * e), "i"(R_CI + 2 * e + 1), "v"(v));
    if constexpr (e == 3) dma_at<7>();
    if constexpr (e == 6) dma_m0<0, 8>();
    if constexpr (e == 7 || e == 11 || e == 15) dma_at<8 + (e - 7) / 4>();
  }, make_iseq<16>{});
  static_assert(sm_nex() >= 16 && SM.op0[64] >= 3 * ((sm_nex() - 15) / 2) && SM.op0[64] <= 24);
  static_for([&](auto E) {
    constexpr int e = E;
    asm volatile("v_exp_f32 v%c0, v%c1" ::"i"(R_E + e), "i"(R_S + e));
    if constexpr (e == 3) dma_at<11>();
    if constexpr (e == 6) dma_m0<0, 12>();
    if constexpr (e == 7 || e == 11 || e == 15) dma_at<12 + (e - 7) / 4>();
  }, make_iseq<16>{});
  static_for([&](auto O) { sm_op<0, O>(); }, make_iseq<SM.op0[64]>{});
  dma_at<15>();
  static_for([&](auto E) { asm volatile("v_exp_f32 v%c0, v%c1" ::"i"(R_E + (int)E), "i"(R_S + 16 + (int)E)); }, make_iseq<sm_nex() - 16>{});
  asm volatile("buffer_store_dwordx2 v[%c0:%c1], v88, s[88:91], s76 offen sc1\n\ts_bitcmp1_b32 s77, 0\n\ts_cselect_b64 s[52:53], s[62:63], 0\n\t"
               FL_ARM("s[52:53]") "\n\t" FL_POLL ::"i"(R_M), "i"(R_M + 1) : "memory", "scc");
  GAP();
}
constexpr int T_ZO = 8, T_WT = 9;
#define NINF2(R) "v_mov_b64 v[" #R ":" #R "+1], v[122:123]\n\t"
#define NINF4(R) NINF2(R) NINF2(R + 2)
#define TRI(R) "v_cndmask_b32_e64 v[" #R "], v[" #R "], v122, s[48:49]\n\tv_cndmask_b32_e64 v[" #R "+1], v[" #R "+1], v122, s[50:51]\n\t" \
               "v_cndmask_b32_e64 v[" #R "+2], v[" #R "+2], v122, s[52:53]\n\tv_cndmask_b32_e64 v[" #R "+3], v[" #R "+3], v122, s[54:55]\n\t"
DI void ev_code() {
  asm volatile(
    "s_getpc_b64 s[82:83]\n\ts_add_u32 s82, s82, 12\n\ts_addc_u32 s83, s83, 0\n\ts_branch 9f\n\t"
    "s_bitcmp0_b32 s58, s56\n\ts_cbranch_scc1 1f\n\t" FL_SET("s70") "\n\t" FL_SET("s71") "\n"
    "1:\n\ts_bitcmp0_b32 s57, s56\n\ts_cbranch_scc1 2f\n\t" FL_CHECK("s[62:63]") "\n\t"
    "s_add_u32 s98, s98, s68\n\ts_mov_b32 s68, s69\n\ts_mov_b32 s69, 0\n\ts_mov_b64 s[62:63], s[64:65]\n\ts_mov_b64 s[64:65], s[66:67]\n"
    "2:\n\ts_bitcmp0_b32 s59, s56\n\ts_cbranch_scc1 3f\n\ts_mov_b32 s98, s72\n\ts_bitcmp0_b32 s73, 0\n\ts_cbranch_scc1 3f\n\t"
    "v_lshrrev_b32 v90, 7, v86\n\tv_and_b32 v91, 6, v90\n\tv_lshlrev_b32 v91, 4, v91\n\tv_and_b32 v92, 0x70, v86\n\tv_xor_b32 v91, v91, v92\n\t"
    "s_movk_i32 s48, 896\n\tv_mad_u32_u24 v94, v90, s48, v91\n\ts_mov_b64 s[92:93], s[80:81]\n\ts_mov_b32 s94, 229376\n\ts_movk_i32 s99, 128\n\t"
    "v_add_u32 v90, 28672, v94\n\tv_add_u32 v91, 34816, v94\n\tv_add_u32 v92, 40960, v94\n\tv_add_u32 v93, 47104, v94\n\t"
    "v_add_u32 v95, 6144, v94\n\tv_add_u32 v126, 12288, v94\n\tv_add_u32 v127, 18432, v94\n"
    "3:\n\ts_bitcmp0_b32 s60, s56\n\ts_cbranch_scc1 4f\n\t"
    "v_readlane_b32 s54, v78, %0\n\tv_readlane_b32 s55, v78, %1\n\ts_add_u32 m0, s78, 0x10000\n\t"
    "s_getpc_b64 s[48:49]\n\ts_add_u32 s48, s48, tail_begin@rel32@lo+4\n\ts_addc_u32 s49, s49, tail_begin@rel32@hi+12\n\t"
    "s_mov_b32 s50, pf_code_end-tail_begin\n\ts_mov_b32 s51, 0x20000\n\tv_mul_u32_u24 v121, 224, v84\n\ts_lshr_b32 s53, s55, 6\n\t"
    "buffer_load_dword v121, s[48:51], s53 offen lds\n\t"
    "s_mov_b64 s[48:49], s[80:81]\n\ts_mov_b32 s50, 229376\n\tv_lshlrev_b32 v121, 3, v84\n\t"
    "buffer_load_dword v121, s[48:51], s55 offen lds\n\t"
    "v_lshlrev_b32 v121, 9, v84\n\tv_and_b32 v121, 0x38000, v121\n\tv_lshrrev_b32 v122, 9, v84\n\tv_lshl_add_u32 v121, v122, 12, v121\n\t"
    "v_lshrrev_b32 v122, 3, v84\n\tv_xor_b32 v122, v122, v84\n\tv_and_or_b32 v121, v122, 48, v121\n\t"
    "buffer_load_dwordx4 v121, s[88:91], s54 offen sc1 lds\n\ts_addk_i32 s54, 7168\n\t"
    "buffer_load_dwordx4 v121, s[88:91], s54 offen offset:1024 sc1 lds\n\ts_addk_i32 s54, 7168\n\t"
    "buffer_load_dwordx4 v121, s[88:91], s54 offen offset:2048 sc1 lds\n\ts_addk_i32 s54, 7168\n\t"
    "buffer_load_dwordx4 v121, s[88:91], s54 offen offset:3072 sc1 lds\n"
    "4:\n\ts_bitcmp0_b32 s61, s56\n\ts_cbranch_scc1 6f\n\t"
    "s_sub_u32 s74, s74, 64\n\ts_cmp_ge_i32 s74, 0\n\ts_cbranch_scc1 6f\n\t"
    "v_cmp_gt_i32 s[48:49], 0, v89\n\tv_cmp_gt_i32 s[50:51], 1, v89\n\tv_cmp_gt_i32 s[52:53], 2, v89\n\tv_cmp_gt_i32 s[54:55], 3, v89\n\t"
    "v_mov_b32 v122, 0xff800000\n\tv_mov_b32 v123, 0xff800000\n\t" NINF4(248)
    "s_cmp_eq_i32 s74, -32\n\ts_cbranch_scc0 5f\n\t" TRI(240) TRI(252) "s_setpc_b64 s[86:87]\n"
    "5:\n\t" NINF4(240) NINF4(244) NINF4(252) NINF4(232) TRI(224) TRI(236)
    "s_cmp_eq_i32 s74, -64\n\ts_cbranch_scc1 6f\n\t" NINF4(224) NINF4(228) NINF4(236)
    "6:\n\ts_setpc_b64 s[86:87]\n9:"
    ::"i"(T_ZO), "i"(T_WT) : "scc");
}
DI void ev_call(int i) { asm volatile("s_mov_b32 s56, %0\n\ts_swappc_b64 s[86:87], s[82:83]" ::"s"(i) : "vcc", "scc", "memory"); GAP(); }

#define ACLOB_ALL "a0","a1","a2","a3","a4","a5","a6","a7","a8","a9","a10","a11","a12","a13","a14","a15","a16","a17","a18","a19","a20","a21","a22","a23","a24","a25","a26","a27","a28","a29","a30","a31","a32","a33","a34","a35","a36","a37","a38","a39","a40","a41","a42","a43","a44","a45","a46","a47","a48","a49","a50","a51","a52","a53","a54","a55","a56","a57","a58","a59","a60","a61","a62","a63","a64","a65","a66","a67","a68","a69","a70","a71","a72","a73","a74","a75","a76","a77","a78","a79","a80","a81","a82","a83","a84","a85","a86","a87","a88","a89","a90","a91","a92","a93","a94","a95","a96","a97","a98","a99","a100","a101","a102","a103","a104","a105","a106","a107","a108","a109","a110","a111","a112","a113","a114","a115","a116","a117","a118","a119","a120","a121","a122","a123","a124","a125","a126","a127","a128","a129","a130","a131","a132","a133","a134","a135","a136","a137","a138","a139","a140","a141","a142","a143","a144","a145","a146","a147","a148","a149","a150","a151","a152","a153","a154","a155","a156","a157","a158","a159","a160","a161","a162","a163","a164","a165","a166","a167","a168","a169","a170","a171","a172","a173","a174","a175","a176","a177","a178","a179","a180","a181","a182","a183","a184","a185","a186","a187","a188","a189","a190","a191","a192","a193","a194","a195","a196","a197","a198","a199","a200","a201","a202","a203","a204","a205","a206","a207","a208","a209","a210","a211","a212","a213","a214","a215","a216","a217","a218","a219","a220","a221","a222","a223","a224","a225","a226","a227","a228","a229","a230","a231","a232","a233","a234","a235","a236","a237","a238","a239","a240","a241","a242","a243","a244","a245","a246","a247","a248","a249","a250","a251","a252","a253","a254","a255"

struct EP { float fa[2], fb[2]; };
template <int I, int E, int MODE> DI void epi_pair(const EP& p) {
  constexpr int qb = I & 1, r = 16 * (I >> 1) + 4 * qb + (E >> 1) * 8 + (E & 1) * 2, t = 96 + 2 * E, w = 128 + 4 * I + E;
  if constexpr (MODE == 2) asm volatile("v_accvgpr_read_b32 v106, a[%c0]" ::"i"(w));
  asm volatile("v_accvgpr_read_b32 v%c0, a[%c2]\n\tv_accvgpr_read_b32 v%c1, a[%c3]" ::"i"(t), "i"(t + 1), "i"(r), "i"(r + 1));
  if constexpr (MODE) asm volatile("v_mul_f32 v%c0, v%c0, %2\n\tv_mul_f32 v%c1, v%c1, %2" ::"i"(t), "i"(t + 1), "v"(p.fa[qb]));
  if constexpr (MODE == 2)
    asm volatile("v_lshlrev_b32 v104, 16, v106\n\tv_and_b32 v105, 0xffff0000, v106\n\tv_fmac_f32 v%c0, v104, %2\n\tv_fmac_f32 v%c1, v105, %2"
                 ::"i"(t), "i"(t + 1), "v"(p.fb[qb]));
  asm volatile("v_cvt_pk_bf16_f32 v%c0, v%c1, v%c2" ::"i"(w), "i"(t), "i"(t + 1));
}
template <int I> DI void epi_store() {
  asm volatile("buffer_store_dwordx4 v[%c0:%c1], v84, s[88:91], s101 offen offset:%2 sc1" ::"i"(128 + 4 * I), "i"(131 + 4 * I), "i"((I & 3) * 1024) : "memory");
  if constexpr ((I & 3) == 3) asm volatile("s_addk_i32 s101, 4096");
}
constexpr int R_TB = 107, R_VS = 111;
template <int HF, int R> DI void p_read() {
  constexpr int mb = R >> 4, r = 64 * HF + 2 * R;
  asm volatile("ds_read_b64_tr_b16 a[%c1:%c2], v%c0 offset:%c3" ::"i"(R_TB + 2 * HF + mb), "i"(r), "i"(r + 1), "i"((R & 15) * 2048));
}
template <int QB, int E> DI void p_cvt() {
  asm volatile("v_cvt_pk_bf16_f32 v%c0, v%c1, v%c2" ::"i"(208 + 4 * QB + E), "i"(192 + 8 * QB + 2 * E), "i"(193 + 8 * QB + 2 * E));
}
template <int QB, int PR> DI void p_store() {
  asm volatile("buffer_store_dwordx4 v[%c0:%c1], v%c2, s[88:91], s96 offen offset:%3 sc1" ::"i"(208 + 4 * QB), "i"(211 + 4 * QB), "i"(R_VS + QB),
               "i"(PR * 128) : "memory");
}
constexpr int p_dma(int HF, int G) { return G == 3 && !HF ? 0 : G == 11 ? 1 - HF : G == 15 ? 2 - HF : G == 19 ? 3 - HF : G == 27 && HF ? 3 : -1; }
template <int HF, int G> DI void p_gap() {
  constexpr int ch = G >> 3, t = G & 7, a = 64 * HF + 32 * (ch & 1) + 4 * t, b = 128 + 4 * (2 * t + (ch >> 1)), d = 192 + 4 * ch;
  if constexpr (t == 0)
    asm volatile("v_mfma_f32_16x16x32_bf16 v[%c0:%c1], a[%c2:%c3], v[%c4:%c5], 0" ::"i"(d), "i"(d + 3), "i"(a), "i"(a + 3), "i"(b), "i"(b + 3));
  else
    asm volatile("v_mfma_f32_16x16x32_bf16 v[%c0:%c1], a[%c2:%c3], v[%c4:%c5], v[%c0:%c1]" ::"i"(d), "i"(d + 3), "i"(a), "i"(a + 3), "i"(b), "i"(b + 3));
  p_read<HF ^ 1, G>();
  if constexpr (G == 0) asm volatile("v_xor_b32 v%c0, s100, v%c0" ::"i"(R_TB + 2 * HF + 1));
  if constexpr (G == 17) asm volatile("v_xor_b32 v%c0, s100, v%c0" ::"i"(R_TB + 2 * (HF ^ 1)));
  if constexpr (G >= 4 && G <= 8 && G != 7) p_cvt<1, (G < 7 ? G - 4 : 3)>();
  if constexpr (G == 24 || (G == 7 && HF == 0)) asm volatile("s_waitcnt lgkmcnt(8)");
  if constexpr (G == 7 && HF == 1) asm volatile("s_waitcnt vmcnt(13) lgkmcnt(8)\n\ts_barrier" ::: "memory");
  if constexpr (G == 10) p_store<1, HF ^ 1>();
  if constexpr (G >= 20 && G < 24) p_cvt<0, G - 20>();
  if constexpr (G == 25) p_store<0, HF>();
  if constexpr (p_dma(HF, G) >= 0) dma_at<(HF ? 8 : 12) + p_dma(HF, G)>();
  if constexpr (HF == 0 && G == 2) asm volatile("s_bitset0_b32 m0, 12");
  if constexpr (HF == 0 && G == 13) asm volatile("s_add_u32 s96, s96, 0x40000");
  if constexpr (HF == 0 && G == 26) asm volatile("s_add_u32 s98, s98, 128");
  if constexpr (HF == 0 && G == 27) asm volatile("s_cmp_lt_u32 s98, 0x380");
  if constexpr (HF == 0 && G == 28) asm volatile("s_cselect_b32 s99, s99, 0x31000");
  if constexpr (HF == 1 && G == 1) asm volatile("s_xor_b32 s100, s100, 0x10000");
  if constexpr (HF == 1 && G == 2) asm volatile("s_xor_b32 s97, s97, s100");
  if constexpr (HF == 1 && G == 9) asm volatile("s_or_b32 m0, s97, s99");
  if constexpr (HF == 1 && G == 30) asm volatile("s_cmp_lt_u32 s98, 0x500");
  GAP();
}
template <int HF, int... Gs> DI void p_half(iseq<Gs...>) { (p_gap<HF, Gs>(), ...); }
constexpr int f_dma(int G) { return G == 2 ? 0 : G == 5 ? 1 : G == 8 ? 2 : G >= 12 && G <= 36 && G % 6 == 0 ? 1 + G / 6 : G >= 50 && G <= 68 && G % 6 == 2 ? G / 6 : -1; }
constexpr int f_ndma(int G) { int n = 0; for (int g = 0; g < G; ++g) n += f_dma(g) >= 0; return n; }
static_assert(f_ndma(74) == 12);
template <int MODE, int G> DI void gapF(AC& c, const EP& p) {
  if constexpr (MODE && G == 48) asm volatile("s_waitcnt lgkmcnt(0)\n\ts_barrier" ::: "memory");
  if constexpr (G < 64) gapB<0, false, G>(c);
  if constexpr (MODE == 2 && G >= 10 && (G - 10) % 16 == 0) asm volatile("s_waitcnt vmcnt(%0)" ::"i"(12 - (G - 10) / 4 + f_ndma(G)) : "memory");
  if constexpr (G >= 10) epi_pair<(G - 10) / 4, (G - 10) & 3, MODE>(p);
  if constexpr (MODE == 3 && G < 64 && (G & 1)) {
    constexpr int db = G >> 2, qb = (G >> 1) & 1, o = 4 * (db * 2 + qb), a = 224 + 4 * (db & 1), b = 128 + 4 * ((db >> 1) * 2 + qb);
    if constexpr ((G & 7) == 1) asm volatile("s_waitcnt vmcnt(%0)" ::"i"(14 - (G >> 2) + f_ndma(G)) : "memory");
    asm volatile("v_mfma_f32_16x16x32_bf16 a[%c0:%c1], v[%c2:%c3], a[%c4:%c5], a[%c0:%c1]" ::"i"(o), "i"(o + 3), "i"(a), "i"(a + 3), "i"(b), "i"(b + 3));
  }
  if constexpr (MODE == 0) {
    if constexpr (G >= 14 && ((G - 14) & 3) == 0) epi_store<(G - 14) / 4>();
    if constexpr (G == 73) epi_store<15>();
  } else {
    if constexpr (G == 0) asm volatile("s_or_b32 m0, s78, 0x1000\n\ts_movk_i32 s98, 256");
    if constexpr (G == 15) asm volatile("s_bitset0_b32 m0, 12");
    if constexpr (G == 48) asm volatile("s_or_b32 m0, s79, 0x1000\n\ts_movk_i32 s98, 384");
    if constexpr (f_dma(G) >= 0) dma_at<8 + (f_dma(G) & 7)>();
    if constexpr (G == 73) asm volatile("s_waitcnt vmcnt(4) lgkmcnt(0)" ::: "memory");
    if constexpr (G >= 48 && G < 64) { p_read<0, 2 * (G - 48)>(); p_read<0, 2 * (G - 48) + 1>(); }
  }
  GAP();
}
template <int MODE, int... Gs> DI void phaseF(AC& c, const EP& p, iseq<Gs...>) { (gapF<MODE, Gs>(c, p), ...); }
template <int U, int K> DI void r_load(unsigned vo, unsigned so) {
  asm volatile("buffer_load_dwordx4 a[%c0:%c1], %2, s[88:91], %3 offen offset:%4 sc1" ::"i"(64 * U + 4 * K), "i"(64 * U + 4 * K + 3), "v"(vo),
               "s"(so + (K >> 2) * 4096), "i"((K & 1) * 512 + ((K >> 1) & 1) * 2048) : "memory");
}
template <int U, int K> DI void r_sum() {
  constexpr int d = 100 + 4 * (K & 1), b = 64 * U + 4 * K;
  if constexpr (K < 2) asm volatile("v_mfma_f32_16x16x32_bf16 v[%c0:%c1], v[96:99], a[%c2:%c3], 0" ::"i"(d), "i"(d + 3), "i"(b), "i"(b + 3));
  else asm volatile("v_mfma_f32_16x16x32_bf16 v[%c0:%c1], v[96:99], a[%c2:%c3], v[%c0:%c1]" ::"i"(d), "i"(d + 3), "i"(b), "i"(b + 3));
}
template <int U> DI void r_fin(unsigned vo, __amdgpu_buffer_rsrc_t srd, unsigned so) {
  asm volatile("" : "+s"(srd), "+s"(so));
  static_for([&](auto K) { r_sum<U, K>(); }, make_iseq<16>{});
  asm volatile("s_nop 7");
  static_for([&](auto E) { asm volatile("v_cvt_pk_bf16_f32 v%c0, v%c1, v%c2" ::"i"(116 + 4 * U + E), "i"(100 + 2 * E), "i"(101 + 2 * E)); }, make_iseq<4>{});
  asm volatile("buffer_store_dwordx4 v[%c3:%c4], %0, %1, %2 offen" ::"v"(vo), "s"(srd), "s"(so), "i"(116 + 4 * U), "i"(119 + 4 * U) : "memory");
}

#define CMAX 12
#define NYT 16
static_assert(NYT <= 16 && NYT >= 12 && !(NYT & 1));
enum { T_Q0, T_K0, T_OQ, T_OG, T_FPREP, T_FQ, T_FG, T_FS,   T_NPRE = 10, T_NST = 12, T_TAB = 14, T_SLAB = 16, T_PART, T_IT = 18, T_ITN = 23 };
struct TRow { unsigned v[64]; };
constexpr u64 kvmask(int m0, int m1) { u64 n = 0; for (int m = m0; m <= m1; ++m) n |= 0x101ull << m; return n; }
constexpr u64 k_order(bool isY, int rr) {
  const int cls = (rr + (rr >> 2)) & 3;
  return !isY ? (cls == 0 ? 0xfedcb9875a463210ull : cls == 1 ? 0xefadc6982b574310ull : cls == 2 ? 0xfebad7693c285410ull : 0xefcba8764d392510ull)
              : (cls == 0 ? 0xfedcb987543a6210ull : cls == 1 ? 0xefadc698254b7310ull : cls == 2 ? 0xfebad769325c8410ull : 0xefcba876432d9510ull);
}
constexpr TRow t_row(int r, int wid, int j) {
  TRow t{};
  const int rr = r & 15, hsel = rr & 1, pj = rr >> 1, kv = pj & 1, h = j + 8 * hsel, u = hsel * 8 + j;
  const bool isY = r >= 16, solo = pj == 7;
  const int mt = (rr >> 2) + (isY ? 4 : 0), g = j * 128, NY = solo ? 18 : NYT;
  const int nx0 = 32 - 2 * pj - NY, cx = nx0 < CMAX ? nx0 : CMAX, ex = nx0 - cx, qbF = isY ? 15 - pj : pj;
  const unsigned oQ = (unsigned)h << 20, oK = oQ + O_K, oP = O_P + (h * 8 + pj) * PSTR;
  if (wid == 4) {
    for (int l = 0; l < 64; ++l)
      t.v[l] = fo(l < 16 ? g + hsel * 16 + l : l < 24 ? g + 16 + hsel * 8 + l : l == 24 ? g + 48 + hsel * 8 + pj : l < 32 ? g + 64 + hsel * 8 + pj :
                  l < 48 ? 2304 + pj * 16 + l - 32 : 2048 + pj * 16 + l - 48);
    return t;
  }
  auto put64 = [&](int i, u64 x) { t.v[i] = (unsigned)x; t.v[i + 1] = (unsigned)(x >> 32); };
  t.v[T_Q0] = oQ + ((solo && !isY ? pj : 15 - pj) * 128 + wid * 32) * 512;
  t.v[T_K0] = oK + (isY ? cx : 0) * 32768;
  t.v[T_OQ] = oQ + pj * 131072 + (wid >> 1) * 65536 + (wid & 1) * 4096;
  t.v[T_OG] = oK + kv * TSB + mt * 131072 + (wid >> 1) * 65536 + (wid & 1) * (kv ? 8192 : 4096);
  t.v[T_FPREP] = fo(2048 + pj * 16 + u);
  t.v[T_FQ] = fo(g + 32 + hsel * 8 + pj);
  t.v[T_FG] = fo(g + hsel * 16 + kv * 8 + mt);
  t.v[T_FS] = fo(1024 + (qbF * 4 + wid) * 16 + j * 2 + hsel);
  t.v[T_ZO] = O_Z + pj * 64 * SEQ * 2 + (u * 4 + wid) * 64;
  t.v[T_WT] = (pj * 4 + wid) * 8192;
  const u64 own = 0x1ull << (kv * 8 + mt), needQ = isY ? 1ull << (16 + ((15 - pj) >> 1)) : (1ull << (16 + (pj >> 1))) | (1ull << (16 + ((15 - pj) >> 1)));
  put64(T_NPRE, isY ? needQ | (1ull << (cx >> 2)) | (1ull << ((cx + 1) >> 2)) : solo ? needQ : needQ | 0xffffull << 48);
  put64(T_NST, isY ? kvmask(cx >> 2, 3) | (solo ? 0 : 1ull << 25) : kvmask(0, solo ? 3 : 0));
  put64(T_TAB, k_order(isY, rr));
  t.v[T_SLAB] = O_S + qbF * 0x200000 + wid * 65536 + (j * 2 + hsel) * 1024;
  t.v[T_PART] = oP;
  for (int it = 0; it < 2; ++it) {
    unsigned* p = t.v + T_IT + it * T_ITN;
    const bool last = isY || it == 1;
    const int n = isY ? NY : it ? 2 * pj + 2 : nx0 < 2 ? 2 : nx0;
    const u64 need2 = solo ? 0 : kvmask(0, (2 * pj + 1) >> 2) | (0xffffull << 32);
    u64 q[3] = {0, 0, 0};
    unsigned jmp[2] = {0, 0};
    if (isY) {
      p[0] = (1u << (14 - cx)) | (solo ? 0 : 1u << (n - 2));
      q[0] = kvmask(4, (31 - 2 * pj) >> 2) & ~own; jmp[0] = ex * 32768; q[1] = solo ? 0 : 1ull << 24;
      p[1] = 2; p[13] = t.v[T_FG]; p[14] = fo(g + 96 + r);
    } else if (it == 0) {
      p[0] = 2u | (ex ? 1u << (cx - 2) : 0);
      q[0] = kvmask(0, cx > 0 ? (cx - 1) >> 2 : 0);
      if (ex) { q[1] = kvmask(4, 4 + ((ex - 1) >> 2)); jmp[1] = (16 - cx) * 32768; q[2] = need2; } else q[1] = need2;
      p[1] = 2; p[13] = fo(2304 + pj * 16 + u) + j * (8 * 4352 - 8); p[14] = fo(g + 64 + hsel * 8 + pj);
    } else {
      p[0] = p[1] = solo ? 0 : 1;
      q[0] = need2; p[13] = fo(g + 48 + hsel * 8 + pj); p[14] = fo(g + 96 + r);
    }
    p[2] = 1u << (n - 2);
    p[3] = isY || !it || solo ? 0 : n > 2 ? 1u << (n - 3) : 1u;
    p[4] = !last ? 0 : n > 2 ? 3u << (n - 3) : 1u << 31;
    for (int k = 0; k < 3; ++k) { p[5 + 2 * k] = (unsigned)q[k]; p[6 + 2 * k] = (unsigned)(q[k] >> 32); }
    p[11] = jmp[0]; p[12] = jmp[1];
    p[15] = oK;
    p[16] = (last ? 1 : 0) | (isY && !solo ? 2 : 0) | (n == 2 ? 4 : 0) | n << 8;
    p[17] = wid * 32 - (n == 2 ? 64 : 0);
    p[18] = oQ + (pj * 128 + wid * 32) * 512;
    p[19] = 0;
    p[20] = (p[0] | (last ? 0 : 1u << n)) >> 1;
    p[21] = oK + (isY ? cx : 0) * 32768;
    p[22] = ((p[0] | p[1] | p[2] | p[3] | p[4]) & 0x7fffffffu) << 1;
  }
  return t;
}
template <int I> constexpr TRow t_base = I < 160 ? t_row(I / 5, I % 5, 0) : [] {
  TRow a = t_row(0, (I - 160) * 4, 0), b = t_row(0, (I - 160) * 4, 1);
  for (int l = 0; l < 64; ++l) a.v[l] = b.v[l] - a.v[l];
  return a;
}();
template <int I> constexpr bool t_linear = [] {
  for (int j = 1; j < 8; ++j) {
    const TRow b = t_row(I / 5, I % 5, j);
    for (int l = 0; l < 64; ++l) if (b.v[l] != t_base<I>.v[l] + j * t_base<160 + (I % 5) / 4>.v[l]) return false;
  }
  return true;
}();
struct Tab { TRow row[162]; };
template <int... I> constexpr Tab t_make(iseq<I...>) {
  static_assert((t_linear<(I < 160 ? I : 0)> && ...));
  return Tab{{t_base<I>...}};
}
__device__ const Tab g_tab = t_make(make_iseq<162>{});

__global__ __launch_bounds__(256, 1) __attribute__((amdgpu_num_vgpr(78), amdgpu_num_sgpr(56))) void fused_kernel(const bf16* __restrict__ ckv, const bf16* __restrict__ wuk,
                                                       const bf16* __restrict__ wuv, const bf16* __restrict__ x,
                                                       const bf16* __restrict__ wq, bf16* Qb,
                                                       const bf16* __restrict__ ak, const bf16* __restrict__ av,
                                                       const bf16* __restrict__ ash, const bf16* __restrict__ assmk,
                                                       const bf16* __restrict__ assmD, const bf16* __restrict__ wo,
                                                       const bf16* __restrict__ aq, bf16* __restrict__ out) {
  extern __shared__ __attribute__((aligned(16))) char lds[];
  asm volatile("at_code_begin:" ::: ACLOB_ALL, "v255", "s101");
  const int tid = threadIdx.x, lane = tid & 63;
  const int wid = __builtin_amdgcn_readfirstlane(tid >> 6);
  const int b = blockIdx.x, j = b & 7, r = b >> 3, rr = r & 15, hsel = rr & 1, pj = rr >> 1, kv = pj & 1;
  const bool isY = r >= 16, solo = pj == 7, doP = isY && !solo, doA = !isY && !solo;
  const int h = j + 8 * hsel, mt = (rr >> 2) + (isY ? 4 : 0), pu = (hsel * 8 + j) * 4 + wid;
  constexpr unsigned m0d = 131072 + 24576;
  const unsigned lA = isY ? 65536 : 0, lW = lA ^ 65536, po = isY ? 0 : 32768;
  const unsigned mp[4] = {lA + wid * 1024 + po, lW + wid * 1024 + po, lA + wid * 1024 + (po ^ 32768), lW + wid * 1024 + (po ^ 32768)};
  {
    const int lda = isY ? DA : DC;
    const unsigned ra = wid * 8 + (lane >> 3), sa = ((lane & 7) ^ (((wid & 1) * 4 + (lane >> 4)) & 7)) * 16;
    const unsigned vbA = ra * (lda * 2) + sa, vbB = (4 * wid + (lane >> 4)) * LDW * 2 + 16 * ((lane & 15) ^ (((lane >> 4) << 2) | wid));
    vsetu<92>(vbA); vsetu<93>(vbB); vsetu<90>(vbA + 128); vsetu<91>(vbB + 0x80000); vsetu<94>(ra * (DC * 2) + sa);
    static_for([&](auto J) {
      sset<64 + J>((J + 1) * 64 * lda);
      asm volatile("s_mov_b32 s%c0, %1" ::"i"(71 + J), "i"((((J + 1) >> 2) * 32 + 16 * ((J + 1) & 1)) * LDW * 2 + (((J + 1) >> 1) & 1) * 256));
    }, make_iseq<7>{});
    sset<78>(lA + wid * 1024); sset<79>(lW + wid * 1024);
    GAP();
  }
  asm volatile("" : "+s"(ckv), "+s"(wuk), "+s"(wuv), "+s"(x), "+s"(wq), "+s"(Qb), "+s"(ak), "+s"(av), "+s"(ash), "+s"(assmk),
                    "+s"(assmD), "+s"(wo), "+s"(aq), "+s"(out));
  KST(0, 0);
  bf16* xqT = (bf16*)((char*)Qb + O_X);
  const bf16* gA = ckv + (size_t)mt * 256 * DC;
  const bf16* gW = (kv ? wuv : wuk) + h * 256;
  unsigned dv = 0;
  {
    GM g;
    PR pr;
    if (doP) prep_load(pr, tid, pj, hsel * 8 + j, ak, av, ash);
    srd_set<56>(isY ? x + (size_t)pj * 256 * DA : gA, isY ? 256 * DA * 2 : 256 * DC * 2);
    srd_set<60>(isY ? wq + h * 256 : gW, isY ? (DA - 1) * LDW * 2 + 512 : (DC - 1) * LDW * 2 + 512);
    g_pro<0>(mp, make_iseq<12>{});
    srd_set<88>(Qb, WSB);
    {
      const unsigned* tp = g_tab.row[r * 5].v;
      asm volatile("buffer_load_dwordx2 v[120:121], off, s[88:91], %0 sc1\n\tglobal_load_dword v78, %1, %3\n\tglobal_load_dword v79, %2, %3 offset:1024\n\t"
                   "global_load_dword v118, %2, %4\n\tglobal_load_dword v119, %2, %4 offset:256"
                   ::"s"(fo(6144)), "v"(tid * 4), "v"(lane * 4), "s"(tp), "s"(g_tab.row[160].v) : "memory");
    }
    if (doP) {
      asm volatile("s_waitcnt vmcnt(17)" ::: "memory");
#pragma unroll
      for (int q = 0; q < 7; ++q) asm volatile("" : "+v"(pr.kk[q]));
#pragma unroll
      for (int q = 0; q < 4; ++q) asm volatile("" : "+v"(pr.vv[q]));
      static_for([&](auto Ic) __attribute__((always_inline)) {
        constexpr int I = Ic;
        g_pro<12 + 4 * I>(mp, make_iseq<4>{});
        GAP();
        prep_math<I>(pr, tid, lds + 131072);
        GAP();
      }, make_iseq<4>{});
      asm volatile("s_waitcnt lgkmcnt(0)\n\ts_barrier" ::: "memory");
      prep_store(tid, pj, hsel * 8 + j, xqT, lds + 131072);
    } else g_pro<12>(mp, make_iseq<4>{});
    GAP();
    CODE_TOUCH("at_code_begin", "pf_code_end", r * 4 + wid, 128u, 8, m0d);
    {
      const int fr = lane & 15, fq = lane >> 4, q = (lane & 15) >> 2, p = lane & 3;
#pragma unroll
      for (int k2 = 0; k2 < 2; ++k2)
        g.aA[k2] = lA + (wid >> 1) * 16384 + (fr >> 3) * 1024 + (fr & 7) * 128 + (((4 * k2 + fq) ^ (fr >> 1)) * 16);
#pragma unroll
      for (int hf = 0; hf < 2; ++hf)
#pragma unroll
        for (int nf = 0; nf < 8; ++nf)
          g.aB[hf][nf] = lW + (wid & 1) * 8192 + off_b(8 * fq + 4 * hf + q, nf * 2 + (p >> 1)) + 8 * (p & 1);
      asm volatile("" : "+v"(g.aA[0]), "+v"(g.aA[1]));
#pragma unroll
      for (int nf = 0; nf < 8; ++nf) asm volatile("" : "+v"(g.aB[0][nf]), "+v"(g.aB[1][nf]));
      vsetu<81>(tid == 0 ? 0u : 0x80000000u);
      vsetu<84>(lane * 16); vsetu<85>(lane * 16 + 65536); vsetu<86>(wid * 8192 + lane * 16); vsetu<87>(wid * 8192 + lane * 16 + 4096);
      vsetu<88>(65536 + wid * 256 + fr * 16); vsetu<89>(fr - 4 * fq);
      asm volatile("s_mov_b64 s[92:93], s[88:89]\n\ts_mov_b64 s[94:95], s[90:91]");
    }
    if (doP) asm volatile("s_waitcnt vmcnt(13)" ::: "memory");
    else {
      g_pro<16>(mp, make_iseq<12>{});
      asm volatile("s_waitcnt vmcnt(8)" ::: "memory");
    }
    asm volatile("s_barrier" ::: "memory");
    if (isY) g_read0<0>(g, make_iseq<16>{});
    else g_read0<1>(g, make_iseq<16>{});
    GAP();
    {
      u64 z = (((u64)__float_as_uint(vget<121>()) << 32) | __float_as_uint(vget<120>())) + 0x9e3779b97f4a7c15ull;
      z = (z ^ (z >> 30)) * 0xbf58476d1ce4e5b9ull; z = (z ^ (z >> 27)) * 0x94d049bb133111ebull; z ^= z >> 31;
      vsetu<82>((unsigned)z); vsetu<83>((unsigned)(z >> 32));
      asm volatile("v_mad_u32_u24 v78, v118, %0, v78\n\tv_mad_u32_u24 v79, v119, %0, v79\n\tv_readfirstlane_b32 s84, v82\n\tv_readfirstlane_b32 s85, v83\n\t"
                   "v_readlane_b32 s80, v78, %1\n\tv_readlane_b32 s81, v78, %2" ::"s"(j), "i"((int)T_TAB), "i"(T_TAB + 1));
      if (isY) asm volatile("s_mov_b64 s[80:81], 0x6543210");
      asm volatile("s_bfe_u32 s82, s80, 0x40008\n\ts_lshr_b64 s[80:81], s[80:81], 12\n\tv_lshl_add_u32 v91, s82, 19, v93" ::: "scc");
    }
    asm volatile("s_waitcnt lgkmcnt(0)\n\ts_barrier" ::: "memory");
    g_pro<28>(mp, make_iseq<4>{});
    GAP();
    if (isY) {
      g_tile<false, 0, 1>(g);
      g_loop<false, true>(g, 0x22u, [&](int t) __attribute__((always_inline)) {
        if (t == 1) fl_set<T_FPREP>();
        else if (t == 4) { tload<80, T_TAB>(); tload<81, T_TAB + 1>(); srd_set<60>(gW, (DC - 1) * LDW * 2 + 512); }
        else {
          srd_set<56>(gA, 256 * DC * 2);
          asm volatile("v_mov_b32 v92, v94");
          static_for([&](auto J) { asm volatile("s_mov_b32 s%c0, %1" ::"i"(64 + J), "i"((J + 1) * 64 * DC)); }, make_iseq<7>{});
        }
      });
      tload<101, T_OQ>();
      asm volatile("v_mov_b32 v112, v84\n\tv_add_u32 v113, 8192, v84");
      GAP();
      g_tile<false, 0, 2>(g);
      KST(0, 3);
    }
#define TT 3
    auto kvtile = [&](auto TRc) __attribute__((always_inline)) {
      constexpr bool TR = TRc;
      if (isY) g_tile<TR, 1, 4>(g);
      else g_tile<TR, 1, 1>(g);
      g_loop<TR, false>(g, 0x2000u | (isY || doA ? 2u : 1u << TT), [&](int t) __attribute__((always_inline)) {
        int l = threadIdx.x & 63; asm volatile("" : "+v"(l));
        if (t == 13) {
          asm volatile("s_mov_b32 s79, 0x28000");
          fl_poll<T_NPRE>();
          if (doA) {
            const char* tp = (const char*)assmk + (size_t)(pj * 64 + pu) * (SEQ * 2) + (l & 31) * 128;
            asm volatile("s_mov_b32 m0, %1\n\ts_nop 0\n\tglobal_load_lds_dword %0, off" ::"v"(tp), "i"(m0d) : "memory");
          }
        } else if (isY) fl_set<T_FQ>();
        else if (doA) post_dma(pj, pu, aq, 131072 + wid * 8192);
        else {
          const char* tp = (const char*)ckv + 1024 * DC * 2 + (pu * 256 + l) * 128;
          asm volatile("s_mov_b32 m0, %1\n\ts_nop 0\n\tglobal_load_lds_dword %0, off" ::"v"(tp), "i"(m0d) : "memory");
          asm volatile("global_load_lds_dword %0, off" ::"v"(tp + 8192) : "memory");
          asm volatile("global_load_lds_dword %0, off" ::"v"(tp + 16384) : "memory");
          asm volatile("global_load_lds_dword %0, off" ::"v"(tp + 24576) : "memory");
        }
      });
      tload<101, T_OG>(); tload<96, T_Q0>(); tload<98, T_K0>();
      fl_check();
      tload<50, T_NST>(); tload<51, T_NST + 1>();
      asm volatile("v_mov_b32 v112, v84\n\tv_add_u32 v113, %0, v84\n\tv_mov_b32 v90, v87\n\tv_mov_b32 v91, v87\n\tv_mov_b32 v92, v87\n\tv_mov_b32 v93, v87\n\t"
                   "v_mov_b32 v94, v86\n\tv_mov_b32 v95, v86\n\tv_mov_b32 v126, v86\n\tv_mov_b32 v127, v86\n\ts_mov_b32 s99, 0x8000\n\t" FL_ARM("s[50:51]")
                   ::"i"(TR ? 4096 : 8192));
      if (doA) {
        const int d = pj * 64 + pu;
        const bf16* dp = assmD + d;
        asm volatile("global_load_ushort %0, %1, off" : "=v"(dv) : "v"(dp) : "memory");
        srd_set<56>(assmk + (size_t)d * SEQ, SEQ * 2);
        srd_set<60>(xqT + (size_t)d * SEQ, SEQ * 2);
        sset<78>(65536 + wid * 15488);
        asm volatile("s_movk_i32 s64, 992\n\tv_subrev_u32 v122, 32, v84");
        GAP();
        g_tile<TR, 0, 3>(g);
      } else {
        sset<79>(isY ? wid * 8192 : 0x30000);
        GAP();
        g_tile<TR, 0, 5>(g);
      }
    };
    if (kv) kvtile(std::true_type{});
    else kvtile(std::false_type{});
  }
  KST(0, 4);
  asm volatile("s_waitcnt vmcnt(16)" : "+v"(dv) :: "memory");
  sset<79>(wid * 8192);
  if (doA) {
    const float Dd = bf2f(dv);
    KST(0, 5);
    conv_wave(pj * 64 + pu, Dd, xqT + (size_t)DA * SEQ, lds + 65536 + wid * 15488,
              [&](int cr) __attribute__((always_inline)) {
      if (cr == 8) {
        const char* tp = (const char*)wo + (size_t)h * (HD * DA * 2) + ((pj * 4 + wid) * 64 + lane) * 128;
        asm volatile("s_mov_b32 m0, %1\n\ts_nop 0\n\tglobal_load_lds_dword %0, off" ::"v"(tp), "i"(m0d) : "memory");
      }
      if (cr == 1) {
        asm volatile("s_waitcnt vmcnt(0)\n\ts_barrier" ::: "memory");
        fl_set<T_FG>();
      } else if (cr == 2) asm volatile(FL_POLL ::: "memory");
      else if (cr == 4) {
        asm volatile("s_waitcnt vmcnt(16)" ::: "memory");
        fl_check();
        dma4k<0>();
      }
      else if (cr == 5) dma4k<1>();
      else if (cr == 6) dma4k<2>();
      else if (cr == 7) dma4k<3>();
    });
    asm volatile("s_waitcnt vmcnt(6)" ::: "memory");
    KST(0, 6);
  } else if (!isY) {
    static_for([&](auto I) { d_store<16 + I>(); }, make_iseq<16>{});
    tload<98, T_K0>();
    asm volatile("s_waitcnt vmcnt(0)\n\ts_barrier" ::: "memory");
    fl_set<T_FG>();
    KST(0, 5);
    fl_check();
    dma4k<0>(); dma4k<1>(); dma4k<2>(); dma4k<3>();
    asm volatile("s_waitcnt vmcnt(0)" ::: "memory");
    KST(0, 6);
  } else {
    tload<76, T_PART>();
    fl_check();
    asm volatile("buffer_load_dwordx2 v[122:123], v88, s[88:91], s76 offen offset:1024 sc1" ::: "memory");
  }
  AC c;
  sset<78>(65536 + wid * 8192); sset<79>(98304 + wid * 8192);
  { const bf16* wp = wo + (size_t)h * HD * DA; asm volatile("s_mov_b64 s[80:81], %0" ::"s"(wp)); }
  ev_code();
  asm volatile("s_waitcnt lgkmcnt(0)\n\ts_barrier" ::: "memory");
#pragma unroll 1
  for (int it = doA ? 0 : 1; it < 2; ++it) {
    kreads<0>(make_iseq<8>{});
    const unsigned oP = tget<T_PART>();
    if (it == 0) {
      static_for([&](auto I) { tload<57 + I, T_IT + I>(); }, make_iseq<21>{});
      tload<S_T, T_IT + 21>(); tload<S_EV, T_IT + 22>();
      asm volatile("s_sub_u32 s70, s70, %0\n\ts_add_u32 s76, %1, 1024" ::"s"(j * (8 * 4352 - 8)), "s"(oP) : "scc");
    } else {
      static_for([&](auto I) { tload<57 + I, T_IT + T_ITN + I>(); }, make_iseq<21>{});
      tload<S_T, T_IT + T_ITN + 21>(); tload<S_EV, T_IT + T_ITN + 22>();
      asm volatile("s_mov_b32 s72, 0\n\ts_mov_b32 s75, %0\n\ts_mov_b32 s76, %1" ::"s"(oP + wid * 16384), "s"(oP + 1032));
    }
    unsigned mode, n;
    asm volatile("s_add_u32 s98, s97, 0x8000\n\ts_mov_b32 %0, s73\n\ts_lshr_b32 %1, s73, 8\n\tv_mov_b64 v[114:115], 0\n\tv_mov_b64 v[118:119], 0\n\t"
                 "v_mov_b32 v120, 0x45800000" : "=s"(mode), "=s"(n) :: "scc");
    KST(0, 8 + 3 * (it && !isY));
    GAP();
#define STA(k)
    STA(0);
    if (isY) phaseA<0, 2>(c);
    else phaseA<0, 1>(c);
    STA(1);
    if (c.rare) ev_call(0);
    STA(2);
    phaseB0(mode, wid);
    if (__builtin_expect(mode & 4, 0)) ev_call(31);
    STA(3);
#pragma unroll 1
    for (int i = 1;; i += 2) {
      phaseA<1, 0>(c);
      STA(9 + i);
      if (__builtin_expect(c.rare != 0, 0)) { if (c.redo) sm_redo<1>(); ev_call(i); }
      phaseB<1>(c);
      if (i + 1 >= n) break;
      phaseA<0, 0>(c);
      STA(10 + i);
      if (__builtin_expect(c.rare != 0, 0)) { if (c.redo) sm_redo<0>(); ev_call(i + 1); }
      phaseB<0>(c);
    }
    STA(4);
    asm volatile("tail_begin:");
    if ((mode & 3) == 1) asm volatile("v_bfrev_b32 v122, 1");
    else asm volatile("v_mov_b32 v122, v84\n\tbuffer_load_dwordx4 v[128:131], v88, s[88:91], %0 offen sc1" ::"s"(oP) : "memory");
    GAP();
    phaseA<0, 3>(c);
    if (__builtin_expect(c.redo != 0, 0)) sm_redo<0>();
    STA(5);
    KST(0, 9 + 3 * (it && !isY));
    const float l[2] = {row_reduce<false>(vget<R_RS>() + vget<R_TS>()), row_reduce<false>(vget<R_RS + 1>() + vget<R_TS + 1>())};
    const float cm[2] = {vget<R_M>(), vget<R_M + 1>()};
    EP p;
    if (it == 0) {
      const f32x4 ml = {cm[0], l[0], cm[1], l[1]};
      asm volatile("buffer_store_dwordx4 %0, v88, s[88:91], %1 offen sc1\n\ts_mov_b32 s101, %2" ::"v"(ml), "s"(oP), "s"(oP + wid * 16384) : "memory");
      phaseF<0>(c, p, make_iseq<74>{});
      asm volatile("s_waitcnt vmcnt(16)" ::: "memory");
    } else {
      int lz = threadIdx.x & 63; asm volatile("" : "+v"(lz));
      const int rw = lz >> 2;
      static_for([&](auto E) { vsetu<R_TB + E>(rw * 128 + 16 * ((2 * E + ((lz & 3) >> 1)) ^ (rw & 6)) + 8 * (lz & 1)); }, make_iseq<4>{});
      const unsigned vs = ((lz >> 3) & 1) * 16384 + ((lz >> 4) & 1) * 512 + (lz >> 5) * 256 + (lz & 7) * 16;
      vsetu<R_VS>(vs); vsetu<R_VS + 1>(vs + 32768);
      tload<96, T_SLAB>();
      asm volatile("s_mov_b32 s97, s79\n\ts_mov_b32 s100, 0x8000\n\ts_movk_i32 s99, 0x1000");
      if (!(mode & 2)) {
        p.fa[0] = __builtin_amdgcn_rcpf(l[0]); p.fa[1] = __builtin_amdgcn_rcpf(l[1]);
        phaseF<1>(c, p, make_iseq<74>{});
      } else {
        const f32x4 ml = {vget<128>(), vget<129>(), vget<130>(), vget<131>()};
        if (__builtin_expect(__builtin_amdgcn_ballot_w64(ml[0] != cm[0] || ml[2] != cm[1]) == 0, 1)) {
          p.fa[0] = __builtin_amdgcn_rcpf(l[0] + ml[1]); p.fa[1] = __builtin_amdgcn_rcpf(l[1] + ml[3]);
          const unsigned one = (lz >> 4) != ((lz >> 2) & 3) ? 0u : (lz & 1) ? 0x3f800000u : 0x3f80u, o0 = (lz & 2) ? 0u : one, o1 = (lz & 2) ? one : 0u;
          vsetu<224>(o0); vsetu<225>(o1); vsetu<230>(o0); vsetu<231>(o1);
          asm volatile("v_mov_b64 v[226:227], 0\n\tv_mov_b64 v[228:229], 0");
          phaseF<3>(c, p, make_iseq<74>{});
        } else {
#pragma unroll
          for (int e = 0; e < 2; ++e) {
            const float mm = fmaxf(cm[e], ml[2 * e]);
            const float wa = __builtin_amdgcn_exp2f(cm[e] - mm), wb = __builtin_amdgcn_exp2f(ml[2 * e] - mm);
            const float inv = __builtin_amdgcn_rcpf(l[e] * wa + ml[2 * e + 1] * wb);
            p.fa[e] = wa * inv; p.fb[e] = wb * inv;
          }
          phaseF<2>(c, p, make_iseq<74>{});
        }
      }
    }
    KST(0, 10 + 3 * (it && !isY));
  }
  asm volatile("v_xor_b32 v%c0, s100, v%c0\np_loop:" ::"i"(R_TB));
  GAP();
  p_half<0>(make_iseq<32>{});
  p_half<1>(make_iseq<32>{});
  asm volatile("s_cbranch_scc1 p_loop\n\ts_nop 5");
  GAP();
  p_cvt<1, 0>(); p_cvt<1, 1>(); p_cvt<1, 2>(); p_cvt<1, 3>();
  p_store<1, 1>();
  GAP();
  KST(0, 27);
  {
    constexpr unsigned rdy = 98304;
    int lz = threadIdx.x & 63; asm volatile("" : "+v"(lz));
    const int fr = lz & 15, fq = lz >> 4, qbF = isY ? 15 - pj : pj, u = hsel * 8 + j;
    asm volatile("ds_write_b32 %0, %1" ::"v"(rdy), "v"(0) : "memory");
    vsetu<81>(lz == 0 ? 0u : 0x80000000u);
    vsetu<79>(fo(1024) + (qbF * 4 + (u >> 2)) * 4352 + fr * 8);
    {
      const int jj = 4 * (fr >> 3) + (fr & 3);
      const unsigned one = ((fq ^ (fr >> 2)) & 1) ? 0u : (jj & 1) ? 0x3f800000u : 0x3f80u;
      static_for([&](auto I) { vsetu<96 + I>((jj >> 1) == I ? one : 0u); }, make_iseq<4>{});
    }
    unsigned vo = (fq >> 1) * 1024 + (fq & 1) * 256 + fr * 16, vo1 = wid < 3 ? vo + 0x100000 : 0x80000000u;
    __amdgpu_buffer_rsrc_t so_ = __builtin_amdgcn_make_buffer_rsrc((void*)out, (short)0, SEQ * OUTW * 2, 0x00020000);
    unsigned vw = ((qbF * 128 + u * 8 + (fr & 7)) * OUTW + DA + wid * 64 + (fr >> 3) * 32 + fq * 8) * 2;
    unsigned so = O_S + qbF * 0x200000 + (1 + wid) * 0x40000 + u * 16384;
    asm volatile("" : "+s"(so), "+s"(so_));
    asm volatile("s_waitcnt vmcnt(0) lgkmcnt(0)" : "+v"(vo), "+v"(vo1), "+v"(vw) :: "memory");
    fl_set<T_FS>();
    asm volatile("s_barrier" ::: "memory");
    KST(0, 30);
    if (doA) post_math(pj, pu, out, lds + 131072 + wid * 8192);
    unsigned pend = 0x5555u, cnt = 0x400000;
    KST(0, 28);
    while (true) {
      unsigned nw;
      if (wid == 3) {
        u64 m;
        const u64 ex = pend | (pend << 1);
        asm volatile(FL_ARM("%1") "\n\t" FL_POLL "\n\ts_waitcnt vmcnt(0)\n\tv_cmp_eq_u64 %0, s[84:85], v[124:125]" : "=s"(m) : "s"(ex) : "memory");
        nw = (unsigned)m & ((unsigned)m >> 1) & pend;
        if (nw) asm volatile("ds_write_b32 %0, %1" ::"v"(rdy), "v"(~pend | nw) : "memory");
      } else {
        unsigned t;
        asm volatile("ds_read_b32 %0, %1\n\ts_waitcnt lgkmcnt(0)" : "=v"(t) : "v"(rdy) : "memory");
        nw = __builtin_amdgcn_readfirstlane(t) & pend;
      }
      if (--cnt == 0) nw = pend;
      if (nw == 0) {
        if (wid == 3) asm volatile("s_sleep 4");
        else asm volatile("s_sleep 1");
        continue;
      }
      asm volatile("s_nop 4");
      static_for([&](auto S) {
        if ((nw >> (2 * S)) & 1) { r_load<0, 2 * S>(vo, so); r_load<0, 2 * S + 1>(vo, so); r_load<1, 2 * S>(vo1, so); r_load<1, 2 * S + 1>(vo1, so); }
      }, make_iseq<8>{});
      pend &= ~nw;
      if (pend == 0) break;
    }
    asm volatile("s_waitcnt vmcnt(0)" ::: "memory");
    KST(0, 16);
    r_fin<0>(vw, so_, 0);
    if (wid < 3) r_fin<1>(vw, so_, 512);
    KST(0, 29);
    if (b == 128 && wid == 0) asm volatile(FL_SET("%0") ::"s"(fo(6144)) : "memory");
  }
  asm volatile("s_waitcnt vmcnt(0)" ::: "memory");
  asm volatile("pf_code_end:");
  KST(0, 1);
}

extern "C" void kernel_launch(void* const* d_in, const int* in_sizes, int n_in, void* d_out, int out_size, void* d_ws,
                              size_t ws_size, hipStream_t stream) {
  const bf16* Aq = (const bf16*)d_in[0];
  const bf16* Ak = (const bf16*)d_in[1];
  const bf16* Av = (const bf16*)d_in[2];
  const bf16* Ash = (const bf16*)d_in[3];
  const bf16* Assmk = (const bf16*)d_in[4];
  const bf16* AssmD = (const bf16*)d_in[5];
  const bf16* Bx = (const bf16*)d_in[6];
  const bf16* Bckv = (const bf16*)d_in[7];
  const bf16* Bwq = (const bf16*)d_in[8];
  const bf16* Bwuk = (const bf16*)d_in[9];
  const bf16* Bwuv = (const bf16*)d_in[10];
  const bf16* Bwo = (const bf16*)d_in[11];
  bf16* out = (bf16*)d_out;
  hipFuncSetAttribute((const void*)fused_kernel, hipFuncAttributeMaxDynamicSharedMemorySize, 163840);
  fused_kernel<<<256, 256, 163840, stream>>>(Bckv, Bwuk, Bwuv, Bx, Bwq, (bf16*)d_ws, Ak, Av, Ash, Assmk, AssmD, Bwo, Aq, out);
}
```

```cpp
#include <hip/hip_runtime.h>
#include <hip/hip_bf16.h>
#include <stdint.h>
#include <utility>

typedef __hip_bfloat16 bf16;
typedef __attribute__((ext_vector_type(4))) short bf16x4;
typedef __attribute__((ext_vector_type(2))) unsigned u32x2;

constexpr int SEQ = 2048;
constexpr int DA = 448;
constexpr int NH = 16;
constexpr int HD = 256;
constexpr int DC = 1024;
constexpr int OUTW = 896;
constexpr unsigned TSB = NH * SEQ * HD * 2, O_K = TSB, O_S = 3 * TSB, O_P = 5 * TSB, PSTR = 67584, O_F = O_P + 128 * PSTR,
                   O_X = O_F + 400 * 4352, O_Z = O_X + DA * SEQ * 2, WSB = O_Z + DA * SEQ * 2;
constexpr unsigned fo(int w) { return O_F + (w >> 4) * 4352 + (w & 15) * 8; }

#define KST(kid, which)

__device__ __forceinline__ float bf2f(unsigned short u) { return __uint_as_float(((unsigned)u) << 16); }
__device__ __forceinline__ unsigned short f2bf(float f) { return __bfloat16_as_ushort(__float2bfloat16(f)); }
__device__ __forceinline__ float rbf(float f) { return bf2f(f2bf(f)); }
typedef __attribute__((ext_vector_type(2))) __bf16 bf16x2n;
typedef __attribute__((ext_vector_type(2))) float f32x2n;
__device__ __forceinline__ unsigned pk2(float a, float b) {
  return __builtin_bit_cast(unsigned, __builtin_convertvector((f32x2n){a, b}, bf16x2n));
}

__device__ __forceinline__ int off_b(int row, int ch) { return 256 * row + 16 * (ch ^ (((row & 3) << 2) | ((row >> 2) & 3))); }

template <int R> __device__ __forceinline__ void sset(unsigned x) { asm volatile("s_mov_b32 s%c0, %1" ::"i"(R), "s"(x)); }
template <int R> __device__ __forceinline__ void vsetu(unsigned x) { asm volatile("v_mov_b32 v%c0, %1" ::"i"(R), "v"(x)); }
template <int R> __device__ __forceinline__ void srd_set(const void* p, unsigned n) {
  asm volatile("s_mov_b64 s[%c0:%c1], %4\n\ts_mov_b32 s%c2, %5\n\ts_mov_b32 s%c3, 0x20000" ::"i"(R), "i"(R + 1), "i"(R + 2), "i"(R + 3), "s"(p), "s"(n));
}
template <int R, int L> __device__ __forceinline__ void tload() { asm volatile("v_readlane_b32 s%c0, v78, %1" ::"i"(R), "i"(L)); }
template <int L> __device__ __forceinline__ unsigned tget() { unsigned t; asm volatile("v_readlane_b32 %0, v78, %1" : "=s"(t) : "i"(L)); return t; }
#define FL_SET(S) "buffer_store_dwordx2 v[82:83], v81, s[88:91], " S " offen sc1"
#define FL_ARM(N) "v_cndmask_b32_e64 v80, -2.0, v79, " N
#define FL_POLL "buffer_load_dwordx2 v[124:125], v80, s[88:91], 0 offen sc1"
#define FL_CHECK(N) "s_mov_b32 s48, 0x100000\n7:\n\tv_cmp_eq_u64 vcc, s[84:85], v[124:125]\n\ts_and_b64 vcc, vcc, " N "\n\ts_cmp_eq_u64 vcc, " N \
                    "\n\ts_cbranch_scc1 8f\n\ts_sleep 2\n\t" FL_POLL "\n\ts_waitcnt vmcnt(0)\n\ts_sub_u32 s48, s48, 1\n\ts_cbranch_scc0 7b\n8:"
template <int L> __device__ __forceinline__ void fl_set() {
  asm volatile("v_readlane_b32 s48, v78, %0\n\ts_nop 4\n\t" FL_SET("s48") ::"i"(L) : "memory");
}
template <int L> __device__ __forceinline__ void fl_poll() {
  asm volatile("v_readlane_b32 s50, v78, %0\n\tv_readlane_b32 s51, v78, %1\n\ts_nop 1\n\t" FL_ARM("s[50:51]") "\n\t" FL_POLL ::"i"(L), "i"(L + 1) : "memory");
}
__device__ __forceinline__ void fl_check() { asm volatile(FL_CHECK("s[50:51]") ::: "vcc", "scc", "memory"); }
template <int R> __device__ __forceinline__ float vget() { float t; asm volatile("v_mov_b32 %0, v%c1" : "=v"(t) : "i"(R)); return t; }
template <int R> __device__ __forceinline__ void vset(float x) { asm volatile("v_mov_b32 v%c0, %1" ::"i"(R), "v"(x)); }
template <int R> __device__ __forceinline__ float acc_read1() { float t; asm volatile("v_accvgpr_read_b32 %0, a[%c1]" : "=v"(t) : "i"(R)); return t; }
template <class F, int... I> __device__ __forceinline__ void static_for(F&& f, std::integer_sequence<int, I...>) {
  (f(std::integral_constant<int, I>{}), ...);
}
template <int... Is> using iseq = std::integer_sequence<int, Is...>;
template <int N> using make_iseq = std::make_integer_sequence<int, N>;

typedef __attribute__((ext_vector_type(4))) unsigned u32x4c;
typedef __attribute__((ext_vector_type(4))) float f32x4c;
typedef __attribute__((ext_vector_type(2))) float f32x2c;
typedef unsigned long long u64;
struct PR { u32x4c kk[7], vv[4]; };
__device__ __forceinline__ void prep_load(PR& p, int tid, int cg, int tb, const bf16* __restrict__ k, const bf16* __restrict__ v,
                                          const bf16* __restrict__ shift_k) {
  const __amdgpu_buffer_rsrc_t sk = __builtin_amdgcn_make_buffer_rsrc((void*)k, (short)0, SEQ * DA * 2, 0x00020000);
  const __amdgpu_buffer_rsrc_t sv = __builtin_amdgcn_make_buffer_rsrc((void*)v, (short)0, SEQ * DA * 2, 0x00020000);
  const __amdgpu_buffer_rsrc_t sw = __builtin_amdgcn_make_buffer_rsrc((void*)(shift_k + cg * 256), (short)0, 512, 0x00020000);
  const int vo = ((tb * 128 + (tid >> 3) * 4) * DA + cg * 64 + (tid & 7) * 8) * 2;
#pragma unroll
  for (int q = 0; q < 7; ++q)
    asm volatile("buffer_load_dwordx4 %0, %1, %2, 0 offen" : "=v"(p.kk[q]) : "v"(vo + (q - 3) * DA * 2), "s"(sk) : "memory");
#pragma unroll
  for (int q = 0; q < 4; ++q)
    asm volatile("buffer_load_dwordx4 %0, %1, %2, 0 offen offset:%3" : "=v"(p.vv[q]) : "v"(vo), "s"(sv), "i"(q * DA * 2) : "memory");
#pragma unroll
  for (int q = 0; q < 4; ++q)
    asm volatile("buffer_load_dwordx4 a[%c0:%c1], %2, %3, 0 offen offset:%4" ::"i"(4 * q), "i"(4 * q + 3), "v"((tid & 7) * 64), "s"(sw), "i"(q * 16) : "memory");
}
__device__ __forceinline__ float bfl(unsigned u) { return __uint_as_float(u << 16); }
__device__ __forceinline__ float bfh(unsigned u) { return __uint_as_float(u & 0xffff0000u); }
__device__ __forceinline__ f32x2c rbf2(f32x2c a) { const unsigned u = pk2(a[0], a[1]); return (f32x2c){bfl(u), bfh(u)}; }
template <int I> __device__ __forceinline__ void prep_math(const PR& p, int tid, char* lx) {
  const int rp = tid >> 3, c = tid & 7;
  f32x2c wt[4], kf[7];
  const unsigned w[4] = {__float_as_uint(acc_read1<4 * I>()), __float_as_uint(acc_read1<4 * I + 1>()), __float_as_uint(acc_read1<4 * I + 2>()),
                         __float_as_uint(acc_read1<4 * I + 3>())};
#pragma unroll
  for (int t = 0; t < 4; ++t)
    wt[t] = (t & 1) ? (f32x2c){bfh(w[t >> 1]), bfh(w[2 + (t >> 1)])} : (f32x2c){bfl(w[t >> 1]), bfl(w[2 + (t >> 1)])};
#pragma unroll
  for (int q = 0; q < 7; ++q) kf[q] = (f32x2c){bfl(p.kk[q][I]), bfh(p.kk[q][I])};
  f32x2c x[4];
#pragma unroll
  for (int e = 0; e < 4; ++e) {
    f32x2c s = rbf2(kf[e] * wt[3]);
    s = rbf2(s + rbf2(kf[e + 1] * wt[2]));
    s = rbf2(s + rbf2(kf[e + 2] * wt[1]));
    s = rbf2(s + rbf2(kf[e + 3] * wt[0]));
    x[e] = s * (f32x2c){bfl(p.vv[e][I]), bfh(p.vv[e][I])};
  }
#pragma unroll
  for (int u = 0; u < 2; ++u)
    *(u32x2*)(lx + (c * 8 + 2 * I + u) * 256 + (((31 - rp) * 8) ^ (c << 4))) = (u32x2){pk2(x[3][u], x[2][u]), pk2(x[1][u], x[0][u])};
}
__device__ __forceinline__ void prep_store(int tid, int cg, int tb, bf16* __restrict__ xr, const char* lx) {
  const __amdgpu_buffer_rsrc_t sx = __builtin_amdgcn_make_buffer_rsrc((void*)(xr + (size_t)cg * 64 * SEQ), (short)0, 64 * SEQ * 2, 0x00020000);
#pragma unroll
  for (int rd = 0; rd < 4; ++rd) {
    const int idx = tid + 256 * rd, ch = idx >> 4, q = idx & 15;
    u32x4c w = *(const u32x4c*)(lx + ch * 256 + 16 * (q ^ (ch >> 3)));
    __builtin_amdgcn_raw_buffer_store_b128(w, sx, (ch * SEQ + (15 - tb) * 128 + q * 8) * 2, 0, 16);
  }
}

#define CODE_TOUCH(LB, LE, wx, NW, NL, m0v) do { unsigned blo_, bhi_, elo_; \
    asm volatile("s_getpc_b64 s[48:49]\n\ts_add_u32 s48, s48, " LB "@rel32@lo+4\n\ts_addc_u32 s49, s49, " LB "@rel32@hi+12\n\t" \
                 "s_mov_b32 %0, s48\n\ts_mov_b32 %1, s49\n\t" \
                 "s_getpc_b64 s[48:49]\n\ts_add_u32 s48, s48, " LE "@rel32@lo+4\n\ts_mov_b32 %2, s48" \
                 : "=s"(blo_), "=s"(bhi_), "=s"(elo_) :: "scc"); \
    const unsigned n_ = elo_ - (blo_ & ~127u); \
    const __amdgpu_buffer_rsrc_t srd_ = __builtin_amdgcn_make_buffer_rsrc((void*)(((unsigned long long)bhi_ << 32) | (blo_ & ~127u)), (short)0, n_ < 262144u ? n_ : 0u, 0x00020000); \
    const unsigned l_ = threadIdx.x & 63, vo_ = l_ < (NL) ? ((wx) + l_ * (NW)) * 128u : 0x80000000u; \
    asm volatile("s_mov_b32 m0, %0\n\ts_nop 4\n\tbuffer_load_dword %1, %2, 0 offen lds" ::"s"(m0v), "v"(vo_), "s"(srd_) : "memory"); } while (0)
template <int I> __device__ __forceinline__ void d_store() {
  asm volatile("buffer_store_dwordx4 v[%c0:%c1], v%c2, s[88:91], s101 offen offset:%3 sc1" ::"i"(128 + 4 * I), "i"(131 + 4 * I), "i"(112 + ((I >> 2) & 1)),
               "i"((I & 3) * 1024) : "memory");
  if constexpr ((I & 7) == 7) asm volatile("s_addk_i32 s101, 0x4000" ::: "scc");
}
constexpr int NS_X = 19;
struct CV { unsigned pa[4], pb, px, vz, sz; float D; };
constexpr int cv_J(bool up, int q) { int J = up ? 0 : 7; while (q > J) { q -= J + 1; J += up ? 1 : -1; } return J; }
constexpr int cv_i(bool up, int q) { int J = up ? 0 : 7; while (q > J) { q -= J + 1; J += up ? 1 : -1; } return q; }
template <int R, int N> __device__ __forceinline__ void cv_rd(CV& c) {
  if constexpr (N < 16) {
    constexpr int i = N >> 1, o = 16 * R + 128 * (i & 1) + 2 * (N & 1), r = 32 + 32 * (R & 1) + 2 * N;
    asm volatile("ds_read2_b32 a[%c1:%c2], %0 offset0:%c3 offset1:%c4" ::"v"(c.pa[i >> 1]), "i"(r), "i"(r + 1), "i"(o), "i"(o + 1));
  } else {
    constexpr int m = N - 16, r = 192 + 32 * (R & 1) + 4 * m;
    asm volatile("ds_read_b128 a[%c1:%c2], %0 offset:%c3" ::"v"(c.pb), "i"(r), "i"(r + 3), "i"(64 * R + 512 * (7 - m)));
  }
}
template <int O> __device__ __forceinline__ void cv_xd(CV& c) {
  constexpr int p = O / 7, k = O % 7, t = 160 + 2 * p, x = 96 + (p ^ 1);
  if constexpr (k == 0) asm volatile("v_and_b32 v%c0, 0xffff0000, v%c1" ::"i"(t), "i"(x));
  if constexpr (k == 1) asm volatile("v_lshlrev_b32 v%c0, 16, v%c1" ::"i"(t + 1), "i"(x));
  if constexpr (k == 2 || k == 3) asm volatile("v_mul_f32 v%c0, v%c0, %1" ::"i"(t + k - 2), "v"(c.D));
  if constexpr (k == 4) asm volatile("v_cvt_pk_bf16_f32 v%c0, v%c1, v%c0" ::"i"(t + 1), "i"(t));
  if constexpr (k == 5) asm volatile("v_lshlrev_b32 v%c0, 16, v%c1" ::"i"(t), "i"(t + 1));
  if constexpr (k == 6) asm volatile("v_and_b32 v%c0, 0xffff0000, v%c0" ::"i"(t + 1));
}
template <int O> __device__ __forceinline__ void cv_z(CV& c) {
  constexpr int J = O / 13, k = O % 13, hf = k & 1, y = 128 + 4 * J + 2 * hf;
  if constexpr (k < 2) asm volatile("v_cvt_pk_bf16_f32 v%c0, v%c1, v%c0" ::"i"(y + 1), "i"(y));
  else if constexpr (k < 4) asm volatile("v_lshlrev_b32 v%c0, 16, v%c1" ::"i"(y), "i"(y + 1));
  else if constexpr (k < 6) asm volatile("v_and_b32 v%c0, 0xffff0000, v%c0" ::"i"(y + 1));
  else if constexpr (k < 10) asm volatile("v_add_f32 v%c0, v%c0, v%c1" ::"i"(y + (k >= 8)), "i"(y + 32 + (k >= 8)));
  else if constexpr (k < 12) asm volatile("v_cvt_pk_bf16_f32 v%c0, v%c1, v%c2" ::"i"(128 + 4 * J + hf), "i"(y), "i"(y + 1));
  else asm volatile("buffer_store_dwordx2 v[%c0:%c1], %2, s[88:91], %3 offen offset:%4 sc1" ::"i"(128 + 4 * J), "i"(129 + 4 * J), "v"(c.vz), "s"(c.sz),
                    "i"(512 * J) : "memory");
}
constexpr int cv_z0(int Q) {
  int o = 0;
  for (int q = 0; q < Q; ++q)
    for (int k = 0; k < 3; ++k) { const int J = o / 13; if ((J + 1) * (J + 2) / 2 - 1 + 4 <= q) ++o; }
  return o;
}
constexpr int cv_xd0(int T) { int o = 0; for (int t = 0; t < T; ++t) o += t % 36 < 24 ? 1 : 2; return o < 112 ? o : 112; }
template <int B, int... Is> __device__ __forceinline__ void cv_zs(CV& c, iseq<Is...>) { (cv_z<B + Is>(c), ...); }
template <int B, int... Is> __device__ __forceinline__ void cv_xds(CV& c, iseq<Is...>) { (cv_xd<B + Is>(c), ...); }
template <int R, int Q> __device__ __forceinline__ void cv_slot(CV& c) {
  constexpr int i = cv_i(R == 7, Q), J = cv_J(R == 7, Q), d = 128 + 4 * J, a = 32 + 32 * (R & 1) + 4 * i, b = 192 + 32 * (R & 1) + 4 * (J - i);
  constexpr int T = 36 * R + Q, vm = Q % 4 == 1 && Q < 32 ? Q / 4 : -1;
  if constexpr (Q == 0) asm volatile("s_waitcnt lgkmcnt(0)");
  if constexpr (R >= 6 && vm >= 0 && vm % 4 == 0) asm volatile("s_add_u32 m0, s79, %0" ::"i"((R - 6) * 32768 + vm * 1024));
  if constexpr (R == 0 && i == 0)
    asm volatile("v_mfma_f32_16x16x32_bf16 v[%c0:%c1], a[%c2:%c3], a[%c4:%c5], 0" ::"i"(d), "i"(d + 3), "i"(a), "i"(a + 3), "i"(b), "i"(b + 3));
  else
    asm volatile("v_mfma_f32_16x16x32_bf16 v[%c0:%c1], a[%c2:%c3], a[%c4:%c5], v[%c0:%c1]" ::"i"(d), "i"(d + 3), "i"(a), "i"(a + 3), "i"(b), "i"(b + 3));
  if constexpr (Q < 24 && R < 7) cv_rd<R + 1, Q>(c);
  if constexpr (T % 7 == 3 && NS_X + 5 + T / 7 < 32) d_store<NS_X + 5 + T / 7>();
  if constexpr (R == 2 && Q >= 24 && Q < 32)
    asm volatile("ds_read_b64 v[%c1:%c2], %0 offset:%c3" ::"v"(c.px), "i"(96 + 2 * (Q - 24)), "i"(97 + 2 * (Q - 24)), "i"(512 * (31 - Q)));
  if constexpr (R >= 3 && R < 6) cv_xds<cv_xd0(T - 108)>(c, make_iseq<cv_xd0(T - 107) - cv_xd0(T - 108)>{});
  if constexpr ((R == 3 || R == 4) && vm >= 0) {
    constexpr int f = (R - 3) * 8 + vm;
    asm volatile("buffer_load_dwordx4 a[%c0:%c1], v84, s[88:91], s96 offen offset:%2 sc1" ::"i"(128 + 4 * f), "i"(131 + 4 * f), "i"((f & 3) * 1024) : "memory");
    if constexpr ((f & 3) == 3) asm volatile("s_addk_i32 s96, 4096");
  }
  if constexpr (R == 4 && Q == 35) asm volatile(FL_POLL ::: "memory");
  if constexpr (R >= 6 && vm >= 0)
    asm volatile("buffer_load_dwordx4 v%c0, s[88:91], s98 offen offset:%1 sc1 lds" ::"i"(86 + vm / 4), "i"((vm & 3) * 1024) : "memory");
  if constexpr (R == 6 && Q == 35) asm volatile("s_add_u32 s98, s98, 0x8000");
  if constexpr (R == 7) {
    if constexpr (Q < 4) asm volatile("s_nop 0");
    cv_zs<cv_z0(Q)>(c, make_iseq<cv_z0(Q + 1) - cv_z0(Q)>{});
  }
  __builtin_amdgcn_sched_barrier(0);
}
template <int R, int... Q> __device__ __forceinline__ void cv_step(CV& c, iseq<Q...>) { (cv_slot<R, Q>(c), ...); }
template <int... N> __device__ __forceinline__ void cv_rd0(CV& c, iseq<N...>) { (cv_rd<0, N>(c), ...); }
template <int FLAG> __device__ __forceinline__ void conv_wave(unsigned sz, float Dd, char* lw) {
  int lane = threadIdx.x & 63; asm volatile("" : "+v"(lane));
  const int l16 = lane & 15, fq = lane >> 4;
  u32x4c w[5]; unsigned w4[5];
#pragma unroll
  for (int r = 0; r < 5; ++r) {
    w[r] = *(const u32x4c*)(lw + r * 1024 + lane * 16);
    w4[r] = *(const unsigned*)(lw + r * 1024 + lane * 16 + 16);
  }
  d_store<NS_X>(); d_store<NS_X + 1>();
#pragma unroll
  for (int r = 0; r < 5; ++r)
    *(u32x4c*)(lw + 10304 + r * 1024 + lane * 16) = (u32x4c){__builtin_amdgcn_alignbit(w[r][1], w[r][0], 16), __builtin_amdgcn_alignbit(w[r][2], w[r][1], 16),
                                                             __builtin_amdgcn_alignbit(w[r][3], w[r][2], 16), __builtin_amdgcn_alignbit(w4[r], w[r][3], 16)};
  d_store<NS_X + 2>();
  CV c;
#pragma unroll
  for (int i = 0; i < 4; ++i) c.pa[i] = (unsigned)(size_t)lw + ((l16 + 1) & 1) * 10304 + 4 * ((8 * fq + l16 + 1) >> 1) + 1024 * i;
  c.pb = (unsigned)(size_t)lw + 5120 + (2032 - 16 * l16 + 8 * fq) * 2 - 512 * 7;
  c.vz = (16 * l16 + 4 * fq) * 2;
  c.px = (unsigned)(size_t)lw + 5120 + 2044 * 2 - 512 * 7 - c.vz;
  c.sz = sz; c.D = Dd;
  asm volatile("" : "+v"(c.vz), "+v"(c.px), "+v"(c.D) :: "memory");
  asm volatile("" : "+s"(c.sz));
  cv_rd0(c, make_iseq<24>{});
  d_store<NS_X + 3>(); d_store<NS_X + 4>();
  __builtin_amdgcn_sched_barrier(0);
  static_for([&](auto R) __attribute__((always_inline)) {
    cv_step<R>(c, make_iseq<36>{});
    if constexpr (R == 2) {
      asm volatile("s_waitcnt vmcnt(0)\n\ts_barrier" ::: "memory");
      fl_set<FLAG>();
    }
    if constexpr (R == 5) {
      asm volatile("s_waitcnt vmcnt(0)" ::: "memory");
      fl_check();
    }
    __builtin_amdgcn_sched_barrier(0);
  }, make_iseq<8>{});
  asm volatile("s_nop 7");
  cv_zs<cv_z0(36)>(c, make_iseq<104 - cv_z0(36)>{});
}

__device__ __forceinline__ void post_dma(int cg, int tb, const bf16* __restrict__ q, unsigned m0w) {
  int lane = threadIdx.x & 63; asm volatile("" : "+v"(lane));
  const __amdgpu_buffer_rsrc_t sq = __builtin_amdgcn_make_buffer_rsrc((void*)(q + (size_t)tb * 32 * DA + cg * 64), (short)0, 32 * DA * 2, 0x00020000);
  const unsigned vq = ((lane >> 3) * DA + (lane & 7) * 8) * 2;
  asm volatile("s_mov_b32 m0, %0\n\ts_nop 4\n\tbuffer_load_dwordx4 %1, %2, 0 offen lds\n\tbuffer_load_dwordx4 %1, %2, %3 offen offset:1024 lds\n\t"
               "buffer_load_dwordx4 %1, %2, %4 offen offset:2048 lds\n\tbuffer_load_dwordx4 %1, %2, %5 offen offset:3072 lds"
               ::"s"(m0w + 4096), "v"(vq), "s"(sq), "s"(8 * DA * 2 - 1024), "s"(16 * DA * 2 - 2048), "s"(24 * DA * 2 - 3072) : "memory");
}
__device__ __forceinline__ void post_math(int cg, int tb, bf16* __restrict__ out, const char* lw) {
  int lane = threadIdx.x & 63; asm volatile("" : "+v"(lane));
  const int tl = lane >> 3, ck = lane & 7;
  const __amdgpu_buffer_rsrc_t so = __builtin_amdgcn_make_buffer_rsrc((void*)(out + (size_t)tb * 32 * OUTW + cg * 64), (short)0, 32 * OUTW * 2 - 2 * DA, 0x00020000);
  const unsigned vo = (tl * OUTW + ck * 8) * 2;
#pragma unroll
  for (int i = 0; i < 4; ++i) {
    const u32x4c qv = *(const u32x4c*)(lw + 4096 + i * 1024 + lane * 16);
    const char* lz = lw + ck * 64 + 16 * (i ^ (ck >> 1)) + tl * 2;
    u32x4c o;
#pragma unroll
    for (int e = 0; e < 4; ++e) {
      const float z0 = bf2f(*(const unsigned short*)(lz + 2 * e * 512));
      const float z1 = bf2f(*(const unsigned short*)(lz + (2 * e + 1) * 512));
      o[e] = pk2(bfl(qv[e]) * z0, bfh(qv[e]) * z1);
    }
    __builtin_amdgcn_raw_buffer_store_b128(o, so, vo + i * 8 * OUTW * 2, 0, 0);
  }
}

#define DI __device__ __forceinline__
#define GAP() __builtin_amdgcn_sched_barrier(0)
typedef __attribute__((ext_vector_type(4))) float f32x4g;
typedef __attribute__((ext_vector_type(4))) unsigned u32x4g;
constexpr int LDW = NH * HD;
struct GM { unsigned aA[2], aB[2][8]; };
template <bool W, int J, int V> DI void g_dma() {
  if constexpr (J == 0) asm volatile("buffer_load_dwordx4 v%c0, s[%c1:%c2], 0 offen lds" ::"i"(V), "i"(W ? 60 : 56), "i"(W ? 63 : 59) : "memory");
  else asm volatile("buffer_load_dwordx4 v%c0, s[%c1:%c2], s%c3 offen lds" ::"i"(V), "i"(W ? 60 : 56), "i"(W ? 63 : 59), "i"((W ? 70 : 63) + J) : "memory");
}
template <int P, int KS, int I> DI void g_wread(GM& g) {
  constexpr int nf = I >> 1, hf = I & 1, r = 192 - 64 * P + 8 * nf + 4 * KS + 2 * hf;
  asm volatile("ds_read_b64_tr_b16 a[%c1:%c2], %0 offset:%c3" ::"v"(g.aB[hf][nf]), "i"(r), "i"(r + 1), "i"(P * 32768 + KS * 16384));
}
template <int P, int M, int KS> DI void g_aread(GM& g) {
  constexpr int r = 96 + 8 * (M & 1) + 4 * KS;
  asm volatile("ds_read_b128 v[%c1:%c2], %0 offset:%c3" ::"v"(g.aA[KS]), "i"(r), "i"(r + 3), "i"(P * 32768 + M * 2048));
}
constexpr float CS = 0.0625f * 1.4426950408889634f;
struct ES { int op0[129], nop[129]; };
constexpr ES e_sched(bool TR, bool SCL) {
  ES z{};
  const int per = SCL ? 16 : 8;
  int o = 0;
  for (int G = 0; G <= 128; ++G) {
    int cap = G == 128 ? 10000 : SCL ? 4 : 2;
    z.op0[G] = o;
    while (o < 32 * per) {
      const int i = o / per, rdy = (TR ? 32 * (i >> 3) + 16 + 2 * (i & 7) + 1 : 4 * i + 3) + 4;
      if ((G < rdy && G < 128) || cap-- <= 0) break;
      ++o; ++z.nop[G];
    }
  }
  return z;
}
template <bool TR, bool SCL> constexpr ES ESCH = e_sched(TR, SCL);
constexpr int e_vm(int G) { return (G * 3) % 8 < 3 ? G * 3 / 8 : -1; }
constexpr int e_st(int MODE, int G) {
  if (MODE == 5) return e_vm(G) >= 32 ? e_vm(G) - 32 : -1;
  if (MODE == 3) return G >= 31 && (G - 31) % 5 == 0 && (G - 31) / 5 < NS_X ? (G - 31) / 5 : -1;
  const int g0 = MODE == 4 ? 5 : 13, gs = MODE == 2 ? 7 : 6;
  return MODE >= 2 && G >= g0 && (G - g0) % gs == 0 && (G - g0) / gs < 16 ? (G - g0) / gs + (MODE == 4 ? 16 : 0) : -1;
}
constexpr int e_nst(int MODE, int G0, int G1) { int n = 0; for (int G = G0; G < G1; ++G) n += e_st(MODE, G) >= 0; return n; }
static_assert(e_nst(2, 0, 128) == 16 && e_nst(3, 30, 128) == NS_X && e_nst(5, 0, 128) == 16 && e_nst(4, 0, 104) == 16);
template <bool TR, bool SCL, int O> DI void e_op() {
  constexpr int per = SCL ? 16 : 8, i = O / per, k = O % per, r = 128 + 4 * i, t = 114 + 4 * (i & 1), c = k - (per - 4);
  if constexpr (k < 4) asm volatile("v_accvgpr_read_b32 v%c0, a[%c1]" ::"i"(t + k), "i"(4 * i + k));
  else if constexpr (c < 0) asm volatile("v_mul_f32 v%c0, %1, v%c0" ::"i"(k < 8 ? r + (k ^ 6) : t + k - 8), "s"(CS));
  else {
    constexpr int d = c == 0 ? r + 3 : c == 1 ? r + 2 : r + c - 2, x = c == 0 ? r + 2 : c == 1 ? r : t + 2 * (c - 2);
    asm volatile("v_cvt_pk_bf16_f32 v%c0, v%c1, v%c2" ::"i"(d), "i"(x), "i"(x + 1));
  }
}
template <bool TR, bool SCL, int B, int... Is> DI void e_ops(iseq<Is...>) { (e_op<TR, SCL, B + Is>(), ...); }
constexpr int e_slot(int MODE, int G) { return MODE == 3 ? (G % 3 == 0 && G < 30 ? G / 3 : -1) : e_vm(G) >= 0 && e_vm(G) < 32 ? 10 + e_vm(G) : -1; }
template <int S> DI void e_m0() {
  if constexpr (S == 0 || S == 1 || S == 5 || S == 9) asm volatile("s_add_u32 m0, s78, %0" ::"i"(S * 1024));
  if constexpr (S >= 26 && (S - 26) % 4 == 0) asm volatile("s_add_u32 m0, s79, %0" ::"i"(((S - 26) >> 3) * 32768 + ((S - 26) & 4) * 1024));
}
template <int S> DI void e_load() {
  if constexpr (S == 0) asm volatile("buffer_load_dwordx4 v122, s[56:59], 0 offen lds" ::: "memory");
  else if constexpr (S < 5) asm volatile("buffer_load_dwordx4 v84, s[56:59], s64 offen offset:%0 lds" ::"i"((S - 1) * 1024) : "memory");
  else if constexpr (S < 9) asm volatile("buffer_load_dwordx4 v84, s[60:63], 0 offen offset:%0 sc1 lds" ::"i"((S - 5) * 1024) : "memory");
  else if constexpr (S == 9) asm volatile("buffer_load_dwordx4 v84, s[60:63], s62 offen sc1 lds" ::: "memory");
  else if constexpr (S < 26) {
    constexpr int f = S - 10;
    asm volatile("buffer_load_dwordx4 a[%c0:%c1], v84, s[88:91], s96 offen offset:%2 sc1" ::"i"(128 + 4 * f), "i"(131 + 4 * f), "i"((f & 3) * 1024) : "memory");
    if constexpr ((f & 3) == 3) asm volatile("s_addk_i32 s96, 4096");
  } else {
    constexpr int f = S - 26;
    asm volatile("buffer_load_dwordx4 v%c0, s[88:91], s98 offen offset:%1 sc1 lds" ::"i"(86 + ((f >> 2) & 1)), "i"((f & 3) * 1024) : "memory");
    if constexpr (f == 7) asm volatile("s_add_u32 s98, s98, 0x8000");
    if constexpr (f == 15) asm volatile(FL_POLL ::: "memory");
  }
}
template <bool TR, int P, int H, int G, int MODE> DI void g_gap(GM& g) {
  constexpr int m = 4 * H + (G >> 4), q = G & 15, n = q >> 1, ks = q & 1, GG = 64 * H + G;
  constexpr bool inV = TR ? m & 1 : n & 1, END = MODE == 3 || MODE == 5, EPI = END || MODE == 2;
  constexpr int a = 96 + 8 * (m & 1) + 4 * ks, b = 192 - 64 * P + 8 * n + 4 * ks, d = (inV ? 128 : 0) + 4 * (TR ? 8 * (m >> 1) + n : 4 * m + (n >> 1));
  constexpr int J = END ? -1 : q == 8 ? 2 * (G >> 4) : q == 12 ? 2 * (G >> 4) + 1 : -1, gj = (J & 3) ^ ((J & 3) >> 1);
  if constexpr (END) { if constexpr (q == 0 && GG > 0) asm volatile("s_waitcnt lgkmcnt(0)"); e_m0<e_slot(MODE, GG)>(); }
  else if constexpr (q == 0 && G > 0) asm volatile("s_waitcnt lgkmcnt(4)");
  if constexpr (J == 0) asm volatile("s_add_u32 m0, s78, %0" ::"i"((P ^ H ^ 1) * 32768 + (1 - H) * 8192));
  else if constexpr (J == 4) asm volatile("s_add_u32 m0, s79, %0" ::"i"((P ^ H) * 32768 + (1 - H) * 16384));
  else if constexpr (J > 0 && (J & 3) == 1) asm volatile("s_bitset1_b32 m0, 12");
  else if constexpr (J > 0 && (J & 3) == 2) asm volatile("s_bitset1_b32 m0, %0" ::"i"(J < 4 ? 14 : 13));
  else if constexpr (J > 0) asm volatile("s_bitset0_b32 m0, 12");
#define GMF(D, X, Y, C) asm volatile("v_mfma_f32_16x16x32_bf16 " D "[%c0:%c1], " X ", " Y ", " C ::"i"(d), "i"(d + 3), "i"(a), "i"(a + 3), "i"(b), "i"(b + 3))
#define GMF2(D, C) do { if constexpr (TR) GMF(D, "v[%c2:%c3]", "a[%c4:%c5]", C); else GMF(D, "a[%c4:%c5]", "v[%c2:%c3]", C); } while (0)
  if constexpr ((MODE == 1 || MODE == 4) && ks == 0) { if constexpr (inV) GMF2("v", "0"); else GMF2("a", "0"); }
  else if constexpr (inV) GMF2("v", "v[%c0:%c1]");
  else GMF2("a", "a[%c0:%c1]");
#undef GMF2
#undef GMF
  if constexpr (e_st(MODE, GG) >= 0) {
    static_assert(MODE == 4 || ESCH<TR, MODE == 2>.op0[GG] >= (e_st(MODE, GG) + 1) * (MODE == 2 ? 16 : 8));
    d_store<e_st(MODE, GG)>();
  }
  if constexpr (EPI) e_ops<TR, MODE == 2, ESCH<TR, MODE == 2>.op0[GG]>(make_iseq<ESCH<TR, MODE == 2>.nop[GG]>{});
  if constexpr (J >= 0 && J < 4) g_dma<false, (1 - H) * 2 + (gj & 1) + (gj >> 1) * 4, 90>();
  else if constexpr (J >= 4) g_dma<true, (1 - H) * 4 + gj, 91>();
  if constexpr ((q == 1 || q == 2) && !(END && m == 7)) g_aread<(m < 7 ? P : P ^ 1), (m + 1) & 7, q - 1>(g);
  if constexpr (END) {
    if constexpr (e_slot(MODE, GG) >= 0) e_load<e_slot(MODE, GG)>();
    if constexpr (!TR && GG >= 3 && GG < 7 && e_slot(MODE, GG) < 0) asm volatile("s_nop 0");
    if constexpr (TR && GG >= 19 && GG < 21 && e_slot(MODE, GG) < 0) asm volatile("s_nop 0");
  } else {
    if constexpr (q >= 3 && q < 11 && (q & 1)) g_wread<P ^ 1, H, (G >> 4) * 4 + (q - 3) / 2>(g);
    if constexpr (H == 1 && G == 0) asm volatile("v_lshl_add_u32 v90, s82, 7, v92\n\ts_and_b32 s82, s80, 15");
    if constexpr (H == 1 && G == 4) asm volatile("s_lshr_b64 s[80:81], s[80:81], 4\n\tv_lshl_add_u32 v91, s82, 19, v93");
    constexpr int nst = e_nst(MODE, H ? 29 : 0, GG + 1);
    if constexpr (G == 61) asm volatile("s_waitcnt vmcnt(%0) lgkmcnt(0)\n\ts_barrier" ::"i"(12 + nst) : "memory");
  }
  if constexpr (EPI && GG == 127) {
    asm volatile("s_nop 7");
    e_ops<TR, MODE == 2, ESCH<TR, MODE == 2>.op0[128]>(make_iseq<ESCH<TR, MODE == 2>.nop[128]>{});
  }
  GAP();
}
template <bool TR, int P, int H, int MODE, int... Gs> DI void g_half_(GM& g, iseq<Gs...>) { (g_gap<TR, P, H, Gs, MODE>(g), ...); }
template <bool TR, int P, int H, int MODE> DI void g_half(GM& g) { g_half_<TR, P, H, MODE>(g, make_iseq<64>{}); }
template <bool TR, int P, int MODE> DI void g_tile(GM& g) { g_half<TR, P, 0, MODE>(g); g_half<TR, P, 1, MODE>(g); }
template <int P, int... Is> DI void g_read0(GM& g, iseq<Is...>) {
  (g_wread<P, 0, Is>(g), ...); (g_wread<P, 1, Is>(g), ...);
  g_aread<P, 0, 0>(g); g_aread<P, 0, 1>(g);
}
template <int I> DI void g_pro1(const unsigned (&m)[4]) {
  constexpr int grp = I >> 2, i = I & 3, t = grp < 4 ? 0 : grp < 7 ? 1 : 2;
  constexpr bool isA = grp == 0 || grp == 3 || grp == 5;
  constexpr int jj = (grp == 3 ? 2 : 0) + (i & 1) + (i >> 1) * 4, ks = grp == 2 || grp == 6;
  asm volatile("s_add_u32 m0, %0, %1\n\ts_nop 0" ::"s"(m[(t & 1) * 2 + !isA]), "i"(isA ? jj * 4096 : ks * 16384 + (i >> 1) * 8192 + (i & 1) * 4096) : "scc");
  if constexpr (isA) g_dma<false, jj, t ? 90 : 92>();
  else g_dma<true, ks * 4 + i, t ? 91 : 93>();
}
template <int B, int... Is> DI void g_pro(const unsigned (&m)[4], iseq<Is...>) { (g_pro1<B + Is>(m), ...); }
template <bool TR, bool SCL, class HOOK> DI void g_loop(GM& g, unsigned evm, HOOK&& hook) {
  constexpr int nkt = SCL ? 7 : 16, P0 = SCL ? 0 : 1;
  asm volatile("" : "+s"(evm));
#pragma unroll 1
  for (int t = 1;; t += 2) {
    g_half<TR, P0 ^ 1, 0, 0>(g);
    if (__builtin_expect((evm >> t) & 1, 0)) { hook(t); GAP(); }
    g_half<TR, P0 ^ 1, 1, 0>(g);
    if (SCL && t + 2 >= nkt) break;
    g_half<TR, P0, 0, 0>(g);
    if constexpr (SCL) { if (__builtin_expect(t == 3, 0)) { hook(4); GAP(); } }
    else asm volatile("s_cmp_eq_u32 %0, 13\n\ts_cselect_b32 s78, 0x28000, s78" ::"s"(t) : "scc");
    g_half<TR, P0, 1, 0>(g);
    if (!SCL && t + 3 >= nkt) break;
  }
}

typedef __attribute__((ext_vector_type(4))) unsigned u32x4;
typedef __attribute__((ext_vector_type(4))) float f32x4;

constexpr int R_S = 128, R_P = 192, R_CI = 224, R_E = 96;
constexpr int R_TS = 112, R_RS = 114, R_M = 116;
constexpr int R_PD = 118, R_THR = 120, R_TMP = 121;
constexpr int r_dk(int q) { return q < 6 ? 90 + q : 120 + q; }
constexpr int S_V = 96, S_T = 97, S_K = 98, S_I = 99, S_EV = 100;
struct AC {
  u64 redo;
  unsigned rare;
};
template <int X, int F> DI void kread() {
  constexpr int r = 192 + 4 * (F & 15);
  asm volatile("ds_read_b128 a[%c0:%c1], v84 offset:%c2" ::"i"(r), "i"(r + 3), "i"(X * 32768 + F * 1024));
}
template <int X, int F> DI void vread() {
  constexpr int r = 192 + 4 * (F & 15);
  asm volatile("ds_read_b128 a[%c0:%c1], v85 offset:%c2" ::"i"(r), "i"(r + 3), "i"(X * 32768 + (F & 1) * 16384 + (F >> 1) * 1024));
}
template <int X, int... Fs> DI void kreads(iseq<Fs...>) { (kread<X, Fs>(), ...); }
template <int X, int J> DI void dma_m0() {
  if constexpr (J == 0) asm volatile("s_mov_b32 m0, s%c0" ::"i"(78 + X));
  else if constexpr (J == 4) asm volatile("s_or_b32 m0, s%c0, 0x1000" ::"i"(78 + X));
  else if constexpr (J == 8) asm volatile("s_bitset0_b32 m0, 16");
  else if constexpr (J == 12) asm volatile("s_bitset0_b32 m0, 12");
}
template <int J> DI void dma_at() {
  if constexpr (J < 8)
    asm volatile("buffer_load_dwordx4 v%c0, s[88:91], s96 offen offset:%1 sc1 lds" ::"i"(86 + (J >= 4)), "i"((J & 3) * 1024) : "memory");
  else
    asm volatile("buffer_load_dwordx4 v%c0, s[92:95], s98 offen offset:%1 sc1 lds" ::"i"(r_dk(J - 8)), "i"((J & 3) * 1024) : "memory");
}
template <int Q> DI void dma4k() {
  asm volatile("s_add_u32 m0, s79, %0\n\ts_add_u32 s48, s98, %1\n\tbuffer_load_dwordx4 v%c2, s[88:91], s48 offen sc1 lds\n\t"
               "buffer_load_dwordx4 v%c2, s[88:91], s48 offen offset:1024 sc1 lds\n\tbuffer_load_dwordx4 v%c2, s[88:91], s48 offen offset:2048 sc1 lds\n\t"
               "buffer_load_dwordx4 v%c2, s[88:91], s48 offen offset:3072 sc1 lds"
               ::"i"((Q >> 1) * 32768 + (Q & 1) * 4096), "i"((Q >> 1) * 32768), "i"(86 + (Q & 1)) : "memory", "scc");
}
template <int Q> DI void acc_zero2() {
  asm volatile("v_accvgpr_write_b32 a[%c0], 0\n\tv_accvgpr_write_b32 a[%c1], 0" ::"i"(2 * Q), "i"(2 * Q + 1));
}
struct SMS { int ex[128], op0[128], nop[128], last; };
constexpr SMS sm_sched() {
  SMS z{};
  int et[32] = {}, e = 0;
  for (int t = 0; t < 128; ++t) {
    const int g = t & 15;
    const bool x = t < 48 ? (g >= 8 && (g & 1)) : (g >= 8 && g != 10 && g != 13 && g != 15);
    z.ex[t] = x && e < 32 ? e : -1;
    if (z.ex[t] >= 0) et[e++] = t;
  }
  int o = 0;
  for (int t = 0; t < 128; ++t) {
    const int g = t & 15;
    int cap = g < 8 ? (g == 0 ? 0 : 1) : (z.ex[t] >= 0 || t < 48 ? 0 : 2);
    z.op0[t] = o; z.nop[t] = 0;
    while (cap > 0 && o < 48 && et[2 * (o / 3) + (o % 3 == 0 ? 0 : 1)] + 2 <= t) {
      const int el = 2 * (o / 3) + o % 3;
      if (!(o % 3 < 2 && (el == 0 || el == 4))) --cap;
      ++o; ++z.nop[t]; z.last = t;
    }
  }
  return z;
}
constexpr SMS SM = sm_sched();
static_assert(SM.last <= 115);
constexpr int p_reg(int Y, int k) { return R_P + 16 * Y + 4 * ((k >> 3) * 2 + ((k >> 1) & 1)) + ((k >> 2) & 1) * 2 + (k & 1); }
template <int Y, int O> DI void sm_op() {
  constexpr int k = O / 3, e = 2 * k + O % 3, ts = R_TS + ((e >> 2) & 1);
  if constexpr (O % 3 == 2) asm volatile("v_cvt_pk_bf16_f32 v%c0, v%c1, v%c2" ::"i"(p_reg(Y, k)), "i"(R_E + ((2 * k) & 15)), "i"(R_E + ((2 * k + 1) & 15)));
  else if constexpr (e == 1 || e == 5) asm volatile("v_add_f32_e32 v%c0, v%c1, v%c2" ::"i"(ts), "i"(R_E + e - 1), "i"(R_E + e));
  else if constexpr (e != 0 && e != 4) asm volatile("v_add_f32_e32 v%c0, v%c0, v%c1" ::"i"(ts), "i"(R_E + (e & 15)));
}
template <int Y, int T> DI void sm_gap(AC& c) {
  if constexpr (SM.ex[T] >= 0) asm volatile("v_exp_f32 v%c0, v%c1" ::"i"(R_E + (SM.ex[T] & 15)), "i"(R_S + 32 * Y + SM.ex[T]));
  if constexpr (SM.nop[T] > 0) sm_op<Y, SM.op0[T]>();
  if constexpr (SM.nop[T] > 1) sm_op<Y, SM.op0[T] + 1>();
  if constexpr (SM.nop[T] > 2) sm_op<Y, SM.op0[T] + 2>();
  if constexpr (T == SM.last + 1) asm volatile("v_add_f32_e32 v%c0, v%c1, v%c2" ::"i"(R_TMP), "i"(R_TS), "i"(R_TS + 1));
  if constexpr (T == SM.last + 2) asm volatile("v_cmp_nle_f32_e64 %0, v%c1, v%c2" : "=s"(c.redo) : "i"(R_TMP), "i"(R_THR));
}
#define W_A0 1
template <int X, int MODE, int G> DI void gapA(AC& c) {
  constexpr bool FIRST = MODE == 1 || MODE == 2, NOQK = MODE == 3;
  constexpr int Xp = X ^ 1, kb = G >> 4, g = G & 15, qb = g >> 3, s = g & 7;
  if constexpr (NOQK && G < 48 && G % 3 == 0) {
    constexpr int f = G / 3;
    asm volatile("buffer_load_dwordx4 a[%c0:%c1], v122, s[88:91], s75 offen offset:%2 sc1" ::"i"(128 + 4 * f), "i"(131 + 4 * f), "i"((f & 3) * 1024) : "memory");
    if constexpr ((f & 3) == 3) asm volatile("s_addk_i32 s75, 4096");
  }
  if constexpr (G == 48) asm volatile("s_waitcnt vmcnt(%0) lgkmcnt(0)\n\ts_barrier" ::"i"(NOQK ? 16 : MODE == 2 ? 12 : MODE == 1 ? W_A0 : 0) : "memory");
  else if constexpr (g == 0 && !NOQK) asm volatile("s_waitcnt lgkmcnt(0)");
  if constexpr (!NOQK) {
    constexpr int r = 192 + 4 * ((kb & 1) * 8 + s), q = 128 + 4 * g;
    constexpr int d = R_S + 32 * X + 4 * (kb * 2 + qb), ci = s == 0 ? R_CI + 4 * (kb * 2 + qb) : d;
    if constexpr (FIRST && s == 0)
      asm volatile("v_mfma_f32_16x16x32_bf16 v[%c0:%c1], a[%c2:%c3], a[%c4:%c5], 0" ::"i"(d), "i"(d + 3), "i"(r), "i"(r + 3), "i"(q), "i"(q + 3));
    else
      asm volatile("v_mfma_f32_16x16x32_bf16 v[%c0:%c1], a[%c2:%c3], a[%c4:%c5], v[%c6:%c7]" ::"i"(d), "i"(d + 3), "i"(r), "i"(r + 3), "i"(q), "i"(q + 3),
                   "i"(ci), "i"(ci + 3));
    if constexpr (g < 8 && kb < 3) kread<X, (kb + 1) * 8 + g>();
    if constexpr (G == 54) asm volatile("s_add_u32 s96, s97, 0x1000000");
    if constexpr (G == 55) dma_m0<X, 0>();
    if constexpr (G >= 56 && !(G & 1)) dma_at<(G - 56) / 2>();
    if constexpr (G == 57) asm volatile("s_mov_b32 s97, s98\n\ts_lshr_b32 s100, s100, 1\n\ts_add_u32 s98, s98, s99");
    if constexpr (G == 59 && !FIRST)
      asm volatile("s_and_b32 s101, s100, 1\n\ts_or_b32 s101, s101, %1\n\ts_or_b32 %0, s101, %2" : "=s"(c.rare) : "s"((unsigned)c.redo), "s"((unsigned)(c.redo >> 32)));
    if constexpr (G == 59 && FIRST) asm volatile("s_and_b32 %0, s100, 1" : "=s"(c.rare));
  } else if constexpr (SM.nop[64 + G] > 0) asm volatile("s_nop 0");
  if constexpr (!FIRST && g < 8 && kb == 3) vread<Xp, g>();
  if constexpr (MODE == 2 && (G & 3) == 1) d_store<16 + G / 4>();
  if constexpr (FIRST) acc_zero2<G>();
  else sm_gap<Xp, 64 + G>(c);
  GAP();
}
template <int R> DI void rescale1(float alpha) {
  float t;
  asm volatile("v_accvgpr_read_b32 %0, a[%c2]\n\tv_mul_f32 %0, %0, %1\n\tv_accvgpr_write_b32 a[%c2], %0" : "=&v"(t) : "v"(alpha), "i"(R));
}
template <int... Rs> DI void rescale_all(const float (&alpha)[2], iseq<Rs...>) { (rescale1<Rs>(alpha[(Rs >> 2) & 1]), ...); }
template <bool MAX> DI float row_reduce(float v) {
  auto p = __builtin_amdgcn_permlane32_swap(__float_as_uint(v), __float_as_uint(v), false, false);
  v = MAX ? fmaxf(__uint_as_float(p[0]), __uint_as_float(p[1])) : __uint_as_float(p[0]) + __uint_as_float(p[1]);
  p = __builtin_amdgcn_permlane16_swap(__float_as_uint(v), __float_as_uint(v), false, false);
  return MAX ? fmaxf(__uint_as_float(p[0]), __uint_as_float(p[1])) : __uint_as_float(p[0]) + __uint_as_float(p[1]);
}
template <int Y, int QB> DI float row_max() {
  constexpr int r = R_S + 32 * Y + 4 * QB;
  float mx;
  asm volatile("v_max_f32 %0, v%c1, v%c2\n\tv_max3_f32 %0, %0, v%c3, v%c4" : "=&v"(mx) : "i"(r), "i"(r + 1), "i"(r + 2), "i"(r + 3));
  static_for([&](auto K) {
    constexpr int q = r + 8 * (K + 1);
    float t = mx;
    asm volatile("v_max3_f32 %0, %0, v%c1, v%c2\n\tv_max3_f32 %0, %0, v%c3, v%c4" : "+v"(t) : "i"(q), "i"(q + 1), "i"(q + 2), "i"(q + 3));
    mx = t;
  }, make_iseq<3>{});
  return row_reduce<true>(mx);
}
template <int R> DI void v_sub(float d) { asm volatile("v_sub_f32 v%c0, v%c0, %1" ::"i"(R), "v"(d)); }
template <int B> DI void s_shift(const float (&d)[2]) { static_for([&](auto E) { v_sub<B + E>(d[(E >> 2) & 1]); }, make_iseq<32>{}); }
DI void m_shift(const float (&d)[2]) {
  s_shift<R_CI>(d);
  v_sub<R_M>(-d[0]); v_sub<R_M + 1>(-d[1]);
}
DI void set_mask0(int wid) {
  const int mr = wid * 32 + __float_as_int(vget<89>());
  static_for([&](auto I) {
    constexpr int e = I, k = (e >> 3) * 16 - ((e >> 2) & 1) * 16 + (e & 3);
    const float x = vget<R_S + e>();
    vset<R_S + e>(k > mr ? -INFINITY : x);
  }, make_iseq<32>{});
}
template <int X> DI void sm_redo() {
  constexpr int Xp = X ^ 1;
  float d[2], dt[2], alpha[2], ts[2] = {0.f, 0.f};
  static_for([&](auto Q) {
    constexpr int qb = Q;
    const float pd = vget<R_PD + qb>();
    d[qb] = fmaxf(row_max<Xp, qb>() - pd, 0.f);
    dt[qb] = d[qb] + pd;
    vset<R_PD + qb>(d[qb]);
    alpha[qb] = __builtin_amdgcn_exp2f(-d[qb]);
    vset<R_RS + qb>(vget<R_RS + qb>() * alpha[qb]);
  }, make_iseq<2>{});
  vset<R_THR>(d[0] > 0.f || d[1] > 0.f ? -1.f : 4096.f);
  m_shift(d);
  static_for([&](auto K) {
    constexpr int k = K, qb = (k >> 1) & 1;
    const float e0 = __builtin_amdgcn_exp2f(vget<R_S + 32 * Xp + 2 * k>() - dt[qb]), e1 = __builtin_amdgcn_exp2f(vget<R_S + 32 * Xp + 2 * k + 1>() - dt[qb]);
    ts[qb] += e0; ts[qb] += e1;
    vset<p_reg(Xp, k)>(__uint_as_float(pk2(e0, e1)));
  }, make_iseq<16>{});
  vset<R_TS>(ts[0]); vset<R_TS + 1>(ts[1]);
  rescale_all(alpha, make_iseq<128>{});
  asm volatile("s_nop 1" ::: "memory");
  GAP();
}
template <int X, int MODE, int... Gs> DI void phaseA_(AC& c, iseq<Gs...>) { (gapA<X, MODE, Gs>(c), ...); }
template <int X, int MODE> DI void phaseA(AC& c) { phaseA_<X, MODE>(c, make_iseq<64>{}); }

#define PG 50
template <int X, bool SMX, int G> DI void gapB(AC& c) {
  constexpr int Xp = X ^ 1, blk = G >> 4, g = G & 15;
  if constexpr (g == 0) asm volatile("s_waitcnt lgkmcnt(0)");
  if constexpr (SMX && g == 7 && blk < 3) dma_m0<X, blk * 4 + 4>();
  constexpr int qb = (g >> 1) & 1, s = g & 1, r = 192 + 4 * ((blk & 1) * 8 + (g >> 2) * 2 + s), o = 4 * ((blk * 4 + (g >> 2)) * 2 + qb);
  constexpr int p = R_P + 16 * Xp + 4 * (s * 2 + qb);
  asm volatile("v_mfma_f32_16x16x32_bf16 a[%c0:%c1], a[%c2:%c3], v[%c4:%c5], a[%c0:%c1]" ::"i"(o), "i"(o + 3), "i"(r), "i"(r + 3), "i"(p), "i"(p + 3));
  if constexpr (SMX && (G == 1 || G == 2)) asm volatile("v_add_f32_e32 v%c0, v%c0, v%c1" ::"i"(R_RS + G - 1), "i"(R_TS + G - 1));
  if constexpr (g < 8) {
    if constexpr (blk < 3) vread<Xp, (blk + 1) * 8 + g>();
    else if constexpr (SMX) kread<Xp, g>();
  }
  if constexpr (SMX) {
    if constexpr (g >= 8 && !(g & 1) && blk < 3) dma_at<blk * 4 + 4 + (g - 8) / 2>();
    if constexpr (G == 3) asm volatile("s_lshr_b32 s77, s77, 1");
    if constexpr (G == 4) asm volatile("s_bitcmp1_b32 s77, 0");
    if constexpr (G == 5) asm volatile("s_cselect_b64 s[52:53], s[62:63], 0");
    if constexpr (G == 48) asm volatile(FL_ARM("s[52:53]"));
    if constexpr (G == PG) asm volatile(FL_POLL ::: "memory");
    sm_gap<X, G>(c);
  }
  GAP();
}
template <int X, int... Gs> DI void phaseB_(AC& c, iseq<Gs...>) { (gapB<X, true, Gs>(c), ...); }
template <int X> DI void phaseB(AC& c) { phaseB_<X>(c, make_iseq<64>{}); }
constexpr int sm_nex() { int n = 0; for (int t = 0; t < 64; ++t) n += SM.ex[t] >= 0; return n; }
DI void phaseB0(unsigned mode, int wid) {
  dma_m0<0, 4>();
  kreads<1>(make_iseq<8>{});
  dma_at<4>();
  asm volatile("s_nop 3");
  float d[2];
  if (mode & 2) {
    d[0] = vget<122>(); d[1] = vget<123>();
    dma_at<5>();
  } else {
    if (__builtin_expect(mode & 4, 0)) set_mask0(wid);
    d[0] = row_max<0, 0>(); GAP(); dma_at<5>();
    d[1] = row_max<0, 1>();
#pragma unroll
    for (int qb = 0; qb < 2; ++qb) if (!(d[qb] > -3.0e38f)) d[qb] = 0.f;
  }
  GAP();
  vset<R_M>(d[0]); vset<R_M + 1>(d[1]);
  dma_at<6>();
  const f32x2c nd[2] = {{-d[0], -d[0]}, {-d[1], -d[1]}};
  static_for([&](auto E) {
    constexpr int e = E;
    const f32x2c v = nd[(e >> 1) & 1];
    asm volatile("v_pk_add_f32 v[%c0:%c1], v[%c0:%c1], %4\n\tv_mov_b64 v[%c2:%c3], %4" ::"i"(R_S + 2 * e), "i"(R_S + 2 * e + 1), "i"(R_CI + 2 * e), "i"(R_CI + 2 * e + 1), "v"(v));
    if constexpr (e == 3) dma_at<7>();
    if constexpr (e == 6) dma_m0<0, 8>();
    if constexpr (e == 7 || e == 11 || e == 15) dma_at<8 + (e - 7) / 4>();
  }, make_iseq<16>{});
  static_assert(sm_nex() >= 16 && SM.op0[64] >= 3 * ((sm_nex() - 15) / 2) && SM.op0[64] <= 24);
  static_for([&](auto E) {
    constexpr int e = E;
    asm volatile("v_exp_f32 v%c0, v%c1" ::"i"(R_E + e), "i"(R_S + e));
    if constexpr (e == 3) dma_at<11>();
    if constexpr (e == 6) dma_m0<0, 12>();
    if constexpr (e == 7 || e == 11 || e == 15) dma_at<12 + (e - 7) / 4>();
  }, make_iseq<16>{});
  static_for([&](auto O) { sm_op<0, O>(); }, make_iseq<SM.op0[64]>{});
  dma_at<15>();
  static_for([&](auto E) { asm volatile("v_exp_f32 v%c0, v%c1" ::"i"(R_E + (int)E), "i"(R_S + 16 + (int)E)); }, make_iseq<sm_nex() - 16>{});
  asm volatile("buffer_store_dwordx2 v[%c0:%c1], v88, s[88:91], s76 offen sc1\n\ts_bitcmp1_b32 s77, 0\n\ts_cselect_b64 s[52:53], s[62:63], 0\n\t"
               FL_ARM("s[52:53]") "\n\t" FL_POLL ::"i"(R_M), "i"(R_M + 1) : "memory", "scc");
  GAP();
}
constexpr int T_ZO = 8;
#define NINF2(R) "v_mov_b64 v[" #R ":" #R "+1], v[122:123]\n\t"
#define NINF4(R) NINF2(R) NINF2(R + 2)
#define TRI(R) "v_cndmask_b32_e64 v[" #R "], v[" #R "], v122, s[48:49]\n\tv_cndmask_b32_e64 v[" #R "+1], v[" #R "+1], v122, s[50:51]\n\t" \
               "v_cndmask_b32_e64 v[" #R "+2], v[" #R "+2], v122, s[52:53]\n\tv_cndmask_b32_e64 v[" #R "+3], v[" #R "+3], v122, s[54:55]\n\t"
DI void ev_code() {
  asm volatile(
    "s_getpc_b64 s[82:83]\n\ts_add_u32 s82, s82, 12\n\ts_addc_u32 s83, s83, 0\n\ts_branch 9f\n\t"
    "s_bitcmp0_b32 s58, s56\n\ts_cbranch_scc1 1f\n\t" FL_SET("s70") "\n\t" FL_SET("s71") "\n"
    "1:\n\ts_bitcmp0_b32 s57, s56\n\ts_cbranch_scc1 2f\n\t" FL_CHECK("s[62:63]") "\n\t"
    "s_add_u32 s98, s98, s68\n\ts_mov_b32 s68, s69\n\ts_mov_b32 s69, 0\n\ts_mov_b64 s[62:63], s[64:65]\n\ts_mov_b64 s[64:65], s[66:67]\n"
    "2:\n\ts_bitcmp0_b32 s59, s56\n\ts_cbranch_scc1 3f\n\ts_mov_b32 s98, s72\n\ts_bitcmp0_b32 s73, 0\n\ts_cbranch_scc1 3f\n\t"
    "v_lshrrev_b32 v90, 7, v86\n\tv_and_b32 v91, 6, v90\n\tv_lshlrev_b32 v91, 4, v91\n\tv_and_b32 v92, 0x70, v86\n\tv_xor_b32 v91, v91, v92\n\t"
    "s_movk_i32 s48, 896\n\tv_mad_u32_u24 v94, v90, s48, v91\n\ts_mov_b64 s[92:93], s[80:81]\n\ts_mov_b32 s94, 229376\n\ts_movk_i32 s99, 128\n\t"
    "v_add_u32 v90, 28672, v94\n\tv_add_u32 v91, 34816, v94\n\tv_add_u32 v92, 40960, v94\n\tv_add_u32 v93, 47104, v94\n\t"
    "v_add_u32 v95, 6144, v94\n\tv_add_u32 v126, 12288, v94\n\tv_add_u32 v127, 18432, v94\n"
    "3:\n\ts_bitcmp0_b32 s60, s56\n\ts_cbranch_scc1 4f\n\t"
    "v_readlane_b32 s54, v78, %0\n\ts_add_u32 m0, s78, 0x10000\n\t"
    "v_lshlrev_b32 v121, 9, v84\n\tv_and_b32 v121, 0x38000, v121\n\tv_lshrrev_b32 v122, 9, v84\n\tv_lshl_add_u32 v121, v122, 12, v121\n\t"
    "v_lshrrev_b32 v122, 3, v84\n\tv_xor_b32 v122, v122, v84\n\tv_and_or_b32 v121, v122, 48, v121\n\t"
    "buffer_load_dwordx4 v121, s[88:91], s54 offen sc1 lds\n\ts_addk_i32 s54, 7168\n\t"
    "buffer_load_dwordx4 v121, s[88:91], s54 offen offset:1024 sc1 lds\n\ts_addk_i32 s54, 7168\n\t"
    "buffer_load_dwordx4 v121, s[88:91], s54 offen offset:2048 sc1 lds\n\ts_addk_i32 s54, 7168\n\t"
    "buffer_load_dwordx4 v121, s[88:91], s54 offen offset:3072 sc1 lds\n"
    "4:\n\ts_bitcmp0_b32 s61, s56\n\ts_cbranch_scc1 6f\n\t"
    "s_sub_u32 s74, s74, 64\n\ts_cmp_ge_i32 s74, 0\n\ts_cbranch_scc1 6f\n\t"
    "v_cmp_gt_i32 s[48:49], 0, v89\n\tv_cmp_gt_i32 s[50:51], 1, v89\n\tv_cmp_gt_i32 s[52:53], 2, v89\n\tv_cmp_gt_i32 s[54:55], 3, v89\n\t"
    "v_mov_b32 v122, 0xff800000\n\tv_mov_b32 v123, 0xff800000\n\t" NINF4(248)
    "s_cmp_eq_i32 s74, -32\n\ts_cbranch_scc0 5f\n\t" TRI(240) TRI(252) "s_setpc_b64 s[86:87]\n"
    "5:\n\t" NINF4(240) NINF4(244) NINF4(252) NINF4(232) TRI(224) TRI(236)
    "s_cmp_eq_i32 s74, -64\n\ts_cbranch_scc1 6f\n\t" NINF4(224) NINF4(228) NINF4(236)
    "6:\n\ts_setpc_b64 s[86:87]\n9:"
    ::"i"(T_ZO) : "scc");
}
DI void ev_call(int i) { asm volatile("s_mov_b32 s56, %0\n\ts_swappc_b64 s[86:87], s[82:83]" ::"s"(i) : "vcc", "scc", "memory"); GAP(); }

#define ACLOB_ALL "a0","a1","a2","a3","a4","a5","a6","a7","a8","a9","a10","a11","a12","a13","a14","a15","a16","a17","a18","a19","a20","a21","a22","a23","a24","a25","a26","a27","a28","a29","a30","a31","a32","a33","a34","a35","a36","a37","a38","a39","a40","a41","a42","a43","a44","a45","a46","a47","a48","a49","a50","a51","a52","a53","a54","a55","a56","a57","a58","a59","a60","a61","a62","a63","a64","a65","a66","a67","a68","a69","a70","a71","a72","a73","a74","a75","a76","a77","a78","a79","a80","a81","a82","a83","a84","a85","a86","a87","a88","a89","a90","a91","a92","a93","a94","a95","a96","a97","a98","a99","a100","a101","a102","a103","a104","a105","a106","a107","a108","a109","a110","a111","a112","a113","a114","a115","a116","a117","a118","a119","a120","a121","a122","a123","a124","a125","a126","a127","a128","a129","a130","a131","a132","a133","a134","a135","a136","a137","a138","a139","a140","a141","a142","a143","a144","a145","a146","a147","a148","a149","a150","a151","a152","a153","a154","a155","a156","a157","a158","a159","a160","a161","a162","a163","a164","a165","a166","a167","a168","a169","a170","a171","a172","a173","a174","a175","a176","a177","a178","a179","a180","a181","a182","a183","a184","a185","a186","a187","a188","a189","a190","a191","a192","a193","a194","a195","a196","a197","a198","a199","a200","a201","a202","a203","a204","a205","a206","a207","a208","a209","a210","a211","a212","a213","a214","a215","a216","a217","a218","a219","a220","a221","a222","a223","a224","a225","a226","a227","a228","a229","a230","a231","a232","a233","a234","a235","a236","a237","a238","a239","a240","a241","a242","a243","a244","a245","a246","a247","a248","a249","a250","a251","a252","a253","a254","a255"

struct EP { float fa[2], fb[2]; };
template <int I, int E, int MODE> DI void epi_pair(const EP& p) {
  constexpr int qb = I & 1, r = 16 * (I >> 1) + 4 * qb + (E >> 1) * 8 + (E & 1) * 2, t = 96 + 2 * E, w = 128 + 4 * I + E;
  if constexpr (MODE == 2) asm volatile("v_accvgpr_read_b32 v106, a[%c0]" ::"i"(w));
  asm volatile("v_accvgpr_read_b32 v%c0, a[%c2]\n\tv_accvgpr_read_b32 v%c1, a[%c3]" ::"i"(t), "i"(t + 1), "i"(r), "i"(r + 1));
  if constexpr (MODE) asm volatile("v_mul_f32 v%c0, v%c0, %2\n\tv_mul_f32 v%c1, v%c1, %2" ::"i"(t), "i"(t + 1), "v"(p.fa[qb]));
  if constexpr (MODE == 2)
    asm volatile("v_lshlrev_b32 v104, 16, v106\n\tv_and_b32 v105, 0xffff0000, v106\n\tv_fmac_f32 v%c0, v104, %2\n\tv_fmac_f32 v%c1, v105, %2"
                 ::"i"(t), "i"(t + 1), "v"(p.fb[qb]));
  asm volatile("v_cvt_pk_bf16_f32 v%c0, v%c1, v%c2" ::"i"(w), "i"(t), "i"(t + 1));
}
template <int I> DI void epi_store() {
  asm volatile("buffer_store_dwordx4 v[%c0:%c1], v84, s[88:91], s101 offen offset:%2 sc1" ::"i"(128 + 4 * I), "i"(131 + 4 * I), "i"((I & 3) * 1024) : "memory");
  if constexpr ((I & 3) == 3) asm volatile("s_addk_i32 s101, 4096");
}
constexpr int R_TB = 107, R_VS = 111;
template <int HF, int R> DI void p_read() {
  constexpr int mb = R >> 4, r = 64 * HF + 2 * R;
  asm volatile("ds_read_b64_tr_b16 a[%c1:%c2], v%c0 offset:%c3" ::"i"(R_TB + 2 * HF + mb), "i"(r), "i"(r + 1), "i"((R & 15) * 2048));
}
template <int QB, int E> DI void p_cvt() {
  asm volatile("v_cvt_pk_bf16_f32 v%c0, v%c1, v%c2" ::"i"(208 + 4 * QB + E), "i"(192 + 8 * QB + 2 * E), "i"(193 + 8 * QB + 2 * E));
}
template <int QB, int PR> DI void p_store() {
  asm volatile("buffer_store_dwordx4 v[%c0:%c1], v%c2, s[88:91], s96 offen offset:%3 sc1" ::"i"(208 + 4 * QB), "i"(211 + 4 * QB), "i"(R_VS + QB),
               "i"(PR * 128) : "memory");
}
constexpr int p_dma(int HF, int G) { return G == 3 && !HF ? 0 : G == 11 ? 1 - HF : G == 15 ? 2 - HF : G == 19 ? 3 - HF : G == 27 && HF ? 3 : -1; }
template <int HF, int G> DI void p_gap() {
  constexpr int ch = G >> 3, t = G & 7, a = 64 * HF + 32 * (ch & 1) + 4 * t, b = 128 + 4 * (2 * t + (ch >> 1)), d = 192 + 4 * ch;
  if constexpr (t == 0)
    asm volatile("v_mfma_f32_16x16x32_bf16 v[%c0:%c1], a[%c2:%c3], v[%c4:%c5], 0" ::"i"(d), "i"(d + 3), "i"(a), "i"(a + 3), "i"(b), "i"(b + 3));
  else
    asm volatile("v_mfma_f32_16x16x32_bf16 v[%c0:%c1], a[%c2:%c3], v[%c4:%c5], v[%c0:%c1]" ::"i"(d), "i"(d + 3), "i"(a), "i"(a + 3), "i"(b), "i"(b + 3));
  p_read<HF ^ 1, G>();
  if constexpr (G == 0) asm volatile("v_xor_b32 v%c0, s100, v%c0" ::"i"(R_TB + 2 * HF + 1));
  if constexpr (G == 17) asm volatile("v_xor_b32 v%c0, s100, v%c0" ::"i"(R_TB + 2 * (HF ^ 1)));
  if constexpr (G >= 4 && G <= 8 && G != 7) p_cvt<1, (G < 7 ? G - 4 : 3)>();
  if constexpr (G == 24 || (G == 7 && HF == 0)) asm volatile("s_waitcnt lgkmcnt(8)");
  if constexpr (G == 7 && HF == 1) asm volatile("s_waitcnt vmcnt(13) lgkmcnt(8)\n\ts_barrier" ::: "memory");
  if constexpr (G == 10) p_store<1, HF ^ 1>();
  if constexpr (G >= 20 && G < 24) p_cvt<0, G - 20>();
  if constexpr (G == 25) p_store<0, HF>();
  if constexpr (p_dma(HF, G) >= 0) dma_at<(HF ? 8 : 12) + p_dma(HF, G)>();
  if constexpr (HF == 0 && G == 2) asm volatile("s_bitset0_b32 m0, 12");
  if constexpr (HF == 0 && G == 13) asm volatile("s_add_u32 s96, s96, 0x40000");
  if constexpr (HF == 0 && G == 26) asm volatile("s_add_u32 s98, s98, 128");
  if constexpr (HF == 0 && G == 27) asm volatile("s_cmp_lt_u32 s98, 0x380");
  if constexpr (HF == 0 && G == 28) asm volatile("s_cselect_b32 s99, s99, 0x31000");
  if constexpr (HF == 1 && G == 1) asm volatile("s_xor_b32 s100, s100, 0x10000");
  if constexpr (HF == 1 && G == 2) asm volatile("s_xor_b32 s97, s97, s100");
  if constexpr (HF == 1 && G == 9) asm volatile("s_or_b32 m0, s97, s99");
  if constexpr (HF == 1 && G == 30) asm volatile("s_cmp_lt_u32 s98, 0x500");
  GAP();
}
template <int HF, int... Gs> DI void p_half(iseq<Gs...>) { (p_gap<HF, Gs>(), ...); }
constexpr int f_dma(int G) { return G == 2 ? 0 : G == 5 ? 1 : G == 8 ? 2 : G >= 12 && G <= 36 && G % 6 == 0 ? 1 + G / 6 : G >= 50 && G <= 68 && G % 6 == 2 ? G / 6 : -1; }
constexpr int f_ndma(int G) { int n = 0; for (int g = 0; g < G; ++g) n += f_dma(g) >= 0; return n; }
static_assert(f_ndma(74) == 12);
template <int MODE, int G> DI void gapF(AC& c, const EP& p) {
  if constexpr (MODE && G == 48) asm volatile("s_waitcnt lgkmcnt(0)\n\ts_barrier" ::: "memory");
  if constexpr (G < 64) gapB<0, false, G>(c);
  if constexpr (MODE == 2 && G >= 10 && (G - 10) % 16 == 0) asm volatile("s_waitcnt vmcnt(%0)" ::"i"(12 - (G - 10) / 4 + f_ndma(G)) : "memory");
  if constexpr (G >= 10) epi_pair<(G - 10) / 4, (G - 10) & 3, MODE>(p);
  if constexpr (MODE == 3 && G < 64 && (G & 1)) {
    constexpr int db = G >> 2, qb = (G >> 1) & 1, o = 4 * (db * 2 + qb), a = 224 + 4 * (db & 1), b = 128 + 4 * ((db >> 1) * 2 + qb);
    if constexpr ((G & 7) == 1) asm volatile("s_waitcnt vmcnt(%0)" ::"i"(14 - (G >> 2) + f_ndma(G)) : "memory");
    asm volatile("v_mfma_f32_16x16x32_bf16 a[%c0:%c1], v[%c2:%c3], a[%c4:%c5], a[%c0:%c1]" ::"i"(o), "i"(o + 3), "i"(a), "i"(a + 3), "i"(b), "i"(b + 3));
  }
  if constexpr (MODE == 0) {
    if constexpr (G >= 14 && ((G - 14) & 3) == 0) epi_store<(G - 14) / 4>();
    if constexpr (G == 73) epi_store<15>();
  } else {
    if constexpr (G == 0) asm volatile("s_or_b32 m0, s78, 0x1000\n\ts_movk_i32 s98, 256");
    if constexpr (G == 15) asm volatile("s_bitset0_b32 m0, 12");
    if constexpr (G == 48) asm volatile("s_or_b32 m0, s79, 0x1000\n\ts_movk_i32 s98, 384");
    if constexpr (f_dma(G) >= 0) dma_at<8 + (f_dma(G) & 7)>();
    if constexpr (G == 73) asm volatile("s_waitcnt vmcnt(4) lgkmcnt(0)" ::: "memory");
    if constexpr (G >= 48 && G < 64) { p_read<0, 2 * (G - 48)>(); p_read<0, 2 * (G - 48) + 1>(); }
  }
  GAP();
}
template <int MODE, int... Gs> DI void phaseF(AC& c, const EP& p, iseq<Gs...>) { (gapF<MODE, Gs>(c, p), ...); }
template <int U, int K> DI void r_load(unsigned vo, unsigned so) {
  asm volatile("buffer_load_dwordx4 a[%c0:%c1], %2, s[88:91], %3 offen offset:%4 sc1" ::"i"(64 * U + 4 * K), "i"(64 * U + 4 * K + 3), "v"(vo),
               "s"(so + (K >> 2) * 4096), "i"((K & 1) * 512 + ((K >> 1) & 1) * 2048) : "memory");
}
template <int U, int K> DI void r_sum() {
  constexpr int d = 100 + 4 * (K & 1), b = 64 * U + 4 * K;
  if constexpr (K < 2) asm volatile("v_mfma_f32_16x16x32_bf16 v[%c0:%c1], v[96:99], a[%c2:%c3], 0" ::"i"(d), "i"(d + 3), "i"(b), "i"(b + 3));
  else asm volatile("v_mfma_f32_16x16x32_bf16 v[%c0:%c1], v[96:99], a[%c2:%c3], v[%c0:%c1]" ::"i"(d), "i"(d + 3), "i"(b), "i"(b + 3));
}
template <int U> DI void r_fin(unsigned vo, __amdgpu_buffer_rsrc_t srd, unsigned so) {
  asm volatile("" : "+s"(srd), "+s"(so));
  static_for([&](auto K) { r_sum<U, K>(); }, make_iseq<16>{});
  asm volatile("s_nop 7");
  static_for([&](auto E) { asm volatile("v_cvt_pk_bf16_f32 v%c0, v%c1, v%c2" ::"i"(116 + 4 * U + E), "i"(100 + 2 * E), "i"(101 + 2 * E)); }, make_iseq<4>{});
  asm volatile("buffer_store_dwordx4 v[%c3:%c4], %0, %1, %2 offen" ::"v"(vo), "s"(srd), "s"(so), "i"(116 + 4 * U), "i"(119 + 4 * U) : "memory");
}

#define CMAX 12
#define NYT 16
static_assert(NYT <= 16 && NYT >= 12 && !(NYT & 1));
enum { T_Q0, T_K0, T_OQ, T_OG, T_FPREP, T_FQ, T_FG, T_FS,   T_NPRE = 10, T_NST = 12, T_TAB = 14, T_SLAB = 16, T_PART, T_IT = 18, T_ITN = 23 };
struct TRow { unsigned v[64]; };
constexpr u64 kvmask(int m0, int m1) { u64 n = 0; for (int m = m0; m <= m1; ++m) n |= 0x101ull << m; return n; }
constexpr u64 k_order(bool isY, int rr) {
  const int cls = (rr + (rr >> 2)) & 3;
  return !isY ? (cls == 0 ? 0xfedcb9875a463210ull : cls == 1 ? 0xefadc6982b574310ull : cls == 2 ? 0xfebad7693c285410ull : 0xefcba8764d392510ull)
              : (cls == 0 ? 0xfedcb987543a6210ull : cls == 1 ? 0xefadc698254b7310ull : cls == 2 ? 0xfebad769325c8410ull : 0xefcba876432d9510ull);
}
constexpr TRow t_row(int r, int wid, int j) {
  TRow t{};
  const int rr = r & 15, hsel = rr & 1, pj = rr >> 1, kv = pj & 1, h = j + 8 * hsel, u = hsel * 8 + j;
  const bool isY = r >= 16, solo = pj == 7;
  const int mt = (rr >> 2) + (isY ? 4 : 0), g = j * 128, NY = solo ? 18 : NYT;
  const int nx0 = 32 - 2 * pj - NY, cx = nx0 < CMAX ? nx0 : CMAX, ex = nx0 - cx, qbF = isY ? 15 - pj : pj;
  const unsigned oQ = (unsigned)h << 20, oK = oQ + O_K, oP = O_P + (h * 8 + pj) * PSTR;
  if (wid == 4) {
    for (int l = 0; l < 64; ++l)
      t.v[l] = fo(l < 16 ? g + hsel * 16 + l : l < 24 ? g + 16 + hsel * 8 + l : l == 24 ? g + 48 + hsel * 8 + pj : l < 32 ? g + 64 + hsel * 8 + pj :
                  l < 48 ? 2304 + pj * 16 + l - 32 : 2048 + pj * 16 + l - 48);
    return t;
  }
  auto put64 = [&](int i, u64 x) { t.v[i] = (unsigned)x; t.v[i + 1] = (unsigned)(x >> 32); };
  t.v[T_Q0] = oQ + ((solo && !isY ? pj : 15 - pj) * 128 + wid * 32) * 512;
  t.v[T_K0] = oK + (isY ? cx : 0) * 32768;
  t.v[T_OQ] = oQ + pj * 131072 + (wid >> 1) * 65536 + (wid & 1) * 4096;
  t.v[T_OG] = oK + kv * TSB + mt * 131072 + (wid >> 1) * 65536 + (wid & 1) * (kv ? 8192 : 4096);
  t.v[T_FPREP] = fo(2048 + pj * 16 + u);
  t.v[T_FQ] = fo(g + 32 + hsel * 8 + pj);
  t.v[T_FG] = fo(g + hsel * 16 + kv * 8 + mt);
  t.v[T_FS] = fo(1024 + (qbF * 4 + wid) * 16 + j * 2 + hsel);
  t.v[T_ZO] = O_Z + pj * 64 * SEQ * 2 + (u * 4 + wid) * 64;
  const u64 own = 0x1ull << (kv * 8 + mt), needQ = isY ? 1ull << (16 + ((15 - pj) >> 1)) : (1ull << (16 + (pj >> 1))) | (1ull << (16 + ((15 - pj) >> 1)));
  put64(T_NPRE, isY ? needQ | (1ull << (cx >> 2)) | (1ull << ((cx + 1) >> 2)) : solo ? needQ : needQ | 0xffffull << 48);
  put64(T_NST, isY ? kvmask(cx >> 2, 3) | (solo ? 0 : 1ull << 25) : kvmask(0, solo ? 3 : 0));
  put64(T_TAB, k_order(isY, rr));
  t.v[T_SLAB] = O_S + qbF * 0x200000 + wid * 65536 + (j * 2 + hsel) * 1024;
  t.v[T_PART] = oP;
  for (int it = 0; it < 2; ++it) {
    unsigned* p = t.v + T_IT + it * T_ITN;
    const bool last = isY || it == 1;
    const int n = isY ? NY : it ? 2 * pj + 2 : nx0 < 2 ? 2 : nx0;
    const u64 need2 = solo ? 0 : kvmask(0, (2 * pj + 1) >> 2) | (0xffffull << 32);
    u64 q[3] = {0, 0, 0};
    unsigned jmp[2] = {0, 0};
    if (isY) {
      p[0] = (1u << (14 - cx)) | (solo ? 0 : 1u << (n - 2));
      q[0] = kvmask(4, (31 - 2 * pj) >> 2) & ~own; jmp[0] = ex * 32768; q[1] = solo ? 0 : 1ull << 24;
      p[1] = 2; p[13] = t.v[T_FG]; p[14] = fo(g + 96 + r);
    } else if (it == 0) {
      p[0] = 2u | (ex ? 1u << (cx - 2) : 0);
      q[0] = kvmask(0, cx > 0 ? (cx - 1) >> 2 : 0);
      if (ex) { q[1] = kvmask(4, 4 + ((ex - 1) >> 2)); jmp[1] = (16 - cx) * 32768; q[2] = need2; } else q[1] = need2;
      p[1] = 2; p[13] = fo(2304 + pj * 16 + u) + j * (8 * 4352 - 8); p[14] = fo(g + 64 + hsel * 8 + pj);
    } else {
      p[0] = p[1] = solo ? 0 : 1;
      q[0] = need2; p[13] = fo(g + 48 + hsel * 8 + pj); p[14] = fo(g + 96 + r);
    }
    p[2] = 1u << (n - 2);
    p[3] = isY || !it || solo ? 0 : n > 2 ? 1u << (n - 3) : 1u;
    p[4] = !last ? 0 : n > 2 ? 3u << (n - 3) : 1u << 31;
    for (int k = 0; k < 3; ++k) { p[5 + 2 * k] = (unsigned)q[k]; p[6 + 2 * k] = (unsigned)(q[k] >> 32); }
    p[11] = jmp[0]; p[12] = jmp[1];
    p[15] = oK;
    p[16] = (last ? 1 : 0) | (isY && !solo ? 2 : 0) | (n == 2 ? 4 : 0) | n << 8;
    p[17] = wid * 32 - (n == 2 ? 64 : 0);
    p[18] = oQ + (pj * 128 + wid * 32) * 512;
    p[19] = 0;
    p[20] = (p[0] | (last ? 0 : 1u << n)) >> 1;
    p[21] = oK + (isY ? cx : 0) * 32768;
    p[22] = ((p[0] | p[1] | p[2] | p[3] | p[4]) & 0x7fffffffu) << 1;
  }
  return t;
}
template <int I> constexpr TRow t_base = I < 160 ? t_row(I / 5, I % 5, 0) : [] {
  TRow a = t_row(0, (I - 160) * 4, 0), b = t_row(0, (I - 160) * 4, 1);
  for (int l = 0; l < 64; ++l) a.v[l] = b.v[l] - a.v[l];
  return a;
}();
template <int I> constexpr bool t_linear = [] {
  for (int j = 1; j < 8; ++j) {
    const TRow b = t_row(I / 5, I % 5, j);
    for (int l = 0; l < 64; ++l) if (b.v[l] != t_base<I>.v[l] + j * t_base<160 + (I % 5) / 4>.v[l]) return false;
  }
  return true;
}();
struct Tab { TRow row[162]; };
template <int... I> constexpr Tab t_make(iseq<I...>) {
  static_assert((t_linear<(I < 160 ? I : 0)> && ...));
  return Tab{{t_base<I>...}};
}
__device__ const Tab g_tab = t_make(make_iseq<162>{});

__global__ __launch_bounds__(256, 1) __attribute__((amdgpu_num_vgpr(78), amdgpu_num_sgpr(56))) void fused_kernel(const bf16* __restrict__ ckv, const bf16* __restrict__ wuk,
                                                       const bf16* __restrict__ wuv, const bf16* __restrict__ x,
                                                       const bf16* __restrict__ wq, bf16* Qb,
                                                       const bf16* __restrict__ ak, const bf16* __restrict__ av,
                                                       const bf16* __restrict__ ash, const bf16* __restrict__ assmk,
                                                       const bf16* __restrict__ assmD, const bf16* __restrict__ wo,
                                                       const bf16* __restrict__ aq, bf16* __restrict__ out) {
  extern __shared__ __attribute__((aligned(16))) char lds[];
  asm volatile("at_code_begin:" ::: ACLOB_ALL, "v255", "s101");
  const int tid = threadIdx.x, lane = tid & 63;
  const int wid = __builtin_amdgcn_readfirstlane(tid >> 6);
  const int b = blockIdx.x, j = b & 7, r = b >> 3, rr = r & 15, hsel = rr & 1, pj = rr >> 1, kv = pj & 1;
  const bool isY = r >= 16, solo = pj == 7, doP = isY && !solo, doA = !isY && !solo;
  const int h = j + 8 * hsel, mt = (rr >> 2) + (isY ? 4 : 0), pu = (hsel * 8 + j) * 4 + wid;
  constexpr unsigned m0d = 131072 + 24576;
  const unsigned lA = isY ? 65536 : 0, lW = lA ^ 65536, po = isY ? 0 : 32768;
  const unsigned mp[4] = {lA + wid * 1024 + po, lW + wid * 1024 + po, lA + wid * 1024 + (po ^ 32768), lW + wid * 1024 + (po ^ 32768)};
  {
    const int lda = isY ? DA : DC;
    const unsigned ra = wid * 8 + (lane >> 3), sa = ((lane & 7) ^ (((wid & 1) * 4 + (lane >> 4)) & 7)) * 16;
    const unsigned vbA = ra * (lda * 2) + sa, vbB = (4 * wid + (lane >> 4)) * LDW * 2 + 16 * ((lane & 15) ^ (((lane >> 4) << 2) | wid));
    vsetu<92>(vbA); vsetu<93>(vbB); vsetu<90>(vbA + 128); vsetu<91>(vbB + 0x80000); vsetu<94>(ra * (DC * 2) + sa);
    static_for([&](auto J) {
      sset<64 + J>((J + 1) * 64 * lda);
      asm volatile("s_mov_b32 s%c0, %1" ::"i"(71 + J), "i"((((J + 1) >> 2) * 32 + 16 * ((J + 1) & 1)) * LDW * 2 + (((J + 1) >> 1) & 1) * 256));
    }, make_iseq<7>{});
    sset<78>(lA + wid * 1024); sset<79>(lW + wid * 1024);
    GAP();
  }
  asm volatile("" : "+s"(ckv), "+s"(wuk), "+s"(wuv), "+s"(x), "+s"(wq), "+s"(Qb), "+s"(ak), "+s"(av), "+s"(ash), "+s"(assmk),
                    "+s"(assmD), "+s"(wo), "+s"(aq), "+s"(out));
  KST(0, 0);
  bf16* xqT = (bf16*)((char*)Qb + O_X);
  const bf16* gA = ckv + (size_t)mt * 256 * DC;
  const bf16* gW = (kv ? wuv : wuk) + h * 256;
  unsigned dv = 0;
  {
    GM g;
    PR pr;
    if (doP) prep_load(pr, tid, pj, hsel * 8 + j, ak, av, ash);
    srd_set<56>(isY ? x + (size_t)pj * 256 * DA : gA, isY ? 256 * DA * 2 : 256 * DC * 2);
    srd_set<60>(isY ? wq + h * 256 : gW, isY ? (DA - 1) * LDW * 2 + 512 : (DC - 1) * LDW * 2 + 512);
    g_pro<0>(mp, make_iseq<12>{});
    srd_set<88>(Qb, WSB);
    {
      const unsigned* tp = g_tab.row[r * 5].v;
      asm volatile("buffer_load_dwordx2 v[120:121], off, s[88:91], %0 sc1\n\tglobal_load_dword v78, %1, %3\n\tglobal_load_dword v79, %2, %3 offset:1024\n\t"
                   "global_load_dword v118, %2, %4\n\tglobal_load_dword v119, %2, %4 offset:256"
                   ::"s"(fo(6144)), "v"(tid * 4), "v"(lane * 4), "s"(tp), "s"(g_tab.row[160].v) : "memory");
    }
    if (doP) {
      asm volatile("s_waitcnt vmcnt(17)" ::: "memory");
#pragma unroll
      for (int q = 0; q < 7; ++q) asm volatile("" : "+v"(pr.kk[q]));
#pragma unroll
      for (int q = 0; q < 4; ++q) asm volatile("" : "+v"(pr.vv[q]));
      static_for([&](auto Ic) __attribute__((always_inline)) {
        constexpr int I = Ic;
        g_pro<12 + 4 * I>(mp, make_iseq<4>{});
        GAP();
        prep_math<I>(pr, tid, lds + 131072);
        GAP();
      }, make_iseq<4>{});
      asm volatile("s_waitcnt lgkmcnt(0)\n\ts_barrier" ::: "memory");
      prep_store(tid, pj, hsel * 8 + j, xqT, lds + 131072);
    } else g_pro<12>(mp, make_iseq<4>{});
    GAP();
    CODE_TOUCH("at_code_begin", "pf_code_end", r * 4 + wid, 128u, 8, m0d);
    {
      const int fr = lane & 15, fq = lane >> 4, q = (lane & 15) >> 2, p = lane & 3;
#pragma unroll
      for (int k2 = 0; k2 < 2; ++k2)
        g.aA[k2] = lA + (wid >> 1) * 16384 + (fr >> 3) * 1024 + (fr & 7) * 128 + (((4 * k2 + fq) ^ (fr >> 1)) * 16);
#pragma unroll
      for (int hf = 0; hf < 2; ++hf)
#pragma unroll
        for (int nf = 0; nf < 8; ++nf)
          g.aB[hf][nf] = lW + (wid & 1) * 8192 + off_b(8 * fq + 4 * hf + q, nf * 2 + (p >> 1)) + 8 * (p & 1);
      asm volatile("" : "+v"(g.aA[0]), "+v"(g.aA[1]));
#pragma unroll
      for (int nf = 0; nf < 8; ++nf) asm volatile("" : "+v"(g.aB[0][nf]), "+v"(g.aB[1][nf]));
      vsetu<81>(tid == 0 ? 0u : 0x80000000u);
      vsetu<84>(lane * 16); vsetu<85>(lane * 16 + 65536); vsetu<86>(wid * 8192 + lane * 16); vsetu<87>(wid * 8192 + lane * 16 + 4096);
      vsetu<88>(65536 + wid * 256 + fr * 16); vsetu<89>(fr - 4 * fq);
      asm volatile("s_mov_b64 s[92:93], s[88:89]\n\ts_mov_b64 s[94:95], s[90:91]");
    }
    if (doP) asm volatile("s_waitcnt vmcnt(13)" ::: "memory");
    else {
      g_pro<16>(mp, make_iseq<12>{});
      asm volatile("s_waitcnt vmcnt(8)" ::: "memory");
    }
    asm volatile("s_barrier" ::: "memory");
    if (isY) g_read0<0>(g, make_iseq<16>{});
    else g_read0<1>(g, make_iseq<16>{});
    GAP();
    {
      u64 z = (((u64)__float_as_uint(vget<121>()) << 32) | __float_as_uint(vget<120>())) + 0x9e3779b97f4a7c15ull;
      z = (z ^ (z >> 30)) * 0xbf58476d1ce4e5b9ull; z = (z ^ (z >> 27)) * 0x94d049bb133111ebull; z ^= z >> 31;
      vsetu<82>((unsigned)z); vsetu<83>((unsigned)(z >> 32));
      asm volatile("v_mad_u32_u24 v78, v118, %0, v78\n\tv_mad_u32_u24 v79, v119, %0, v79\n\tv_readfirstlane_b32 s84, v82\n\tv_readfirstlane_b32 s85, v83\n\t"
                   "v_readlane_b32 s80, v78, %1\n\tv_readlane_b32 s81, v78, %2" ::"s"(j), "i"((int)T_TAB), "i"(T_TAB + 1));
      if (isY) asm volatile("s_mov_b64 s[80:81], 0x6543210");
      asm volatile("s_bfe_u32 s82, s80, 0x40008\n\ts_lshr_b64 s[80:81], s[80:81], 12\n\tv_lshl_add_u32 v91, s82, 19, v93" ::: "scc");
    }
    asm volatile("s_waitcnt lgkmcnt(0)\n\ts_barrier" ::: "memory");
    g_pro<28>(mp, make_iseq<4>{});
    GAP();
    if (isY) {
      g_tile<false, 0, 1>(g);
      g_loop<false, true>(g, 0x22u, [&](int t) __attribute__((always_inline)) {
        if (t == 1) fl_set<T_FPREP>();
        else if (t == 4) { tload<80, T_TAB>(); tload<81, T_TAB + 1>(); srd_set<60>(gW, (DC - 1) * LDW * 2 + 512); }
        else {
          srd_set<56>(gA, 256 * DC * 2);
          asm volatile("v_mov_b32 v92, v94");
          static_for([&](auto J) { asm volatile("s_mov_b32 s%c0, %1" ::"i"(64 + J), "i"((J + 1) * 64 * DC)); }, make_iseq<7>{});
        }
      });
      tload<101, T_OQ>();
      asm volatile("v_mov_b32 v112, v84\n\tv_add_u32 v113, 8192, v84");
      GAP();
      g_tile<false, 0, 2>(g);
      KST(0, 3);
    }
#define TT 3
    auto kvtile = [&](auto TRc) __attribute__((always_inline)) {
      constexpr bool TR = TRc;
      if (isY) g_tile<TR, 1, 4>(g);
      else g_tile<TR, 1, 1>(g);
      g_loop<TR, false>(g, 0x2000u | (isY || doA ? 2u : 1u << TT), [&](int t) __attribute__((always_inline)) {
        int l = threadIdx.x & 63; asm volatile("" : "+v"(l));
        if (t == 13) {
          asm volatile("s_mov_b32 s79, 0x28000");
          fl_poll<T_NPRE>();
          if (!doA && !isY) {
            CODE_TOUCH("at_code_begin", "pf_code_end", hsel * 8 + wid * 2, 16u, 64, m0d);
            CODE_TOUCH("at_code_begin", "pf_code_end", hsel * 8 + wid * 2 + 1, 16u, 64, m0d);
          }
          if (doA) {
            const char* tp = (const char*)assmk + (size_t)(pj * 64 + pu) * (SEQ * 2) + (l & 31) * 128;
            asm volatile("s_mov_b32 m0, %1\n\ts_nop 0\n\tglobal_load_lds_dword %0, off" ::"v"(tp), "i"(m0d) : "memory");
          }
        } else if (isY) fl_set<T_FQ>();
        else if (doA) post_dma(pj, pu, aq, 131072 + wid * 8192);
        else {
          const char* tp = (const char*)ckv + 1024 * DC * 2 + (pu * 256 + l) * 128;
          asm volatile("s_mov_b32 m0, %1\n\ts_nop 0\n\tglobal_load_lds_dword %0, off" ::"v"(tp), "i"(m0d) : "memory");
          asm volatile("global_load_lds_dword %0, off" ::"v"(tp + 8192) : "memory");
          asm volatile("global_load_lds_dword %0, off" ::"v"(tp + 16384) : "memory");
          asm volatile("global_load_lds_dword %0, off" ::"v"(tp + 24576) : "memory");
        }
      });
      tload<101, T_OG>(); tload<96, T_Q0>(); tload<98, T_K0>();
      fl_check();
      tload<50, T_NST>(); tload<51, T_NST + 1>();
      asm volatile("v_mov_b32 v112, v84\n\tv_add_u32 v113, %0, v84\n\tv_mov_b32 v90, v87\n\tv_mov_b32 v91, v87\n\tv_mov_b32 v92, v87\n\tv_mov_b32 v93, v87\n\t"
                   "v_mov_b32 v94, v86\n\tv_mov_b32 v95, v86\n\tv_mov_b32 v126, v86\n\tv_mov_b32 v127, v86\n\ts_mov_b32 s99, 0x8000\n\t" FL_ARM("s[50:51]")
                   ::"i"(TR ? 4096 : 8192));
      if (doA) {
        const int d = pj * 64 + pu;
        const bf16* dp = assmD + d;
        asm volatile("global_load_ushort %0, %1, off" : "=v"(dv) : "v"(dp) : "memory");
        srd_set<56>(assmk + (size_t)d * SEQ, SEQ * 2);
        srd_set<60>(xqT + (size_t)d * SEQ, SEQ * 2);
        sset<78>(65536 + wid * 15488);
        asm volatile("s_movk_i32 s64, 992\n\tv_subrev_u32 v122, 32, v84");
        GAP();
        g_tile<TR, 0, 3>(g);
      } else {
        sset<79>(isY ? wid * 8192 : 0x30000);
        GAP();
        g_tile<TR, 0, 5>(g);
      }
    };
    if (kv) kvtile(std::true_type{});
    else kvtile(std::false_type{});
  }
  KST(0, 4);
  asm volatile("s_waitcnt vmcnt(16)" : "+v"(dv) :: "memory");
  sset<79>(wid * 8192);
  if (doA) {
    const float Dd = bf2f(dv);
    KST(0, 5);
    conv_wave<T_FG>(O_Z + (pj * 64 + pu) * SEQ * 2, Dd, lds + 65536 + wid * 15488);
    const char* tp = (const char*)wo + (size_t)h * (HD * DA * 2) + ((pj * 4 + wid) * 64 + lane) * 128;
    asm volatile("s_mov_b32 m0, %1\n\ts_nop 0\n\tglobal_load_lds_dword %0, off\n\ts_waitcnt vmcnt(17)" ::"v"(tp), "i"(m0d) : "memory");
    KST(0, 6);
  } else if (!isY) {
    static_for([&](auto I) { d_store<16 + I>(); }, make_iseq<16>{});
    tload<98, T_K0>();
    asm volatile("s_waitcnt vmcnt(0)\n\ts_barrier" ::: "memory");
    fl_set<T_FG>();
    KST(0, 5);
    fl_check();
    dma4k<0>(); dma4k<1>(); dma4k<2>(); dma4k<3>();
    asm volatile("s_waitcnt vmcnt(0)" ::: "memory");
    KST(0, 6);
  } else {
    tload<76, T_PART>();
    fl_check();
    asm volatile("buffer_load_dwordx2 v[122:123], v88, s[88:91], s76 offen offset:1024 sc1" ::: "memory");
  }
  AC c;
  sset<78>(65536 + wid * 8192); sset<79>(98304 + wid * 8192);
  { const bf16* wp = wo + (size_t)h * HD * DA; asm volatile("s_mov_b64 s[80:81], %0" ::"s"(wp)); }
  ev_code();
  asm volatile("s_waitcnt lgkmcnt(0)\n\ts_barrier" ::: "memory");
#pragma unroll 1
  for (int it = doA ? 0 : 1; it < 2; ++it) {
    kreads<0>(make_iseq<8>{});
    const unsigned oP = tget<T_PART>();
    if (it == 0) {
      static_for([&](auto I) { tload<57 + I, T_IT + I>(); }, make_iseq<21>{});
      tload<S_T, T_IT + 21>(); tload<S_EV, T_IT + 22>();
      asm volatile("s_sub_u32 s70, s70, %0\n\ts_add_u32 s76, %1, 1024" ::"s"(j * (8 * 4352 - 8)), "s"(oP) : "scc");
    } else {
      static_for([&](auto I) { tload<57 + I, T_IT + T_ITN + I>(); }, make_iseq<21>{});
      tload<S_T, T_IT + T_ITN + 21>(); tload<S_EV, T_IT + T_ITN + 22>();
      asm volatile("s_mov_b32 s72, 0\n\ts_mov_b32 s75, %0\n\ts_mov_b32 s76, %1" ::"s"(oP + wid * 16384), "s"(oP + 1032));
    }
    unsigned mode, n;
    asm volatile("s_add_u32 s98, s97, 0x8000\n\ts_mov_b32 %0, s73\n\ts_lshr_b32 %1, s73, 8\n\tv_mov_b64 v[114:115], 0\n\tv_mov_b64 v[118:119], 0\n\t"
                 "v_mov_b32 v120, 0x45800000" : "=s"(mode), "=s"(n) :: "scc");
    KST(0, 8 + 3 * (it && !isY));
    GAP();
#define STA(k)
    STA(0);
    if (isY) phaseA<0, 2>(c);
    else phaseA<0, 1>(c);
    STA(1);
    if (c.rare) ev_call(0);
    STA(2);
    phaseB0(mode, wid);
    if (__builtin_expect(mode & 4, 0)) ev_call(31);
    STA(3);
#pragma unroll 1
    for (int i = 1;; i += 2) {
      phaseA<1, 0>(c);
      STA(9 + i);
      if (__builtin_expect(c.rare != 0, 0)) { if (c.redo) sm_redo<1>(); ev_call(i); }
      phaseB<1>(c);
      if (i + 1 >= n) break;
      phaseA<0, 0>(c);
      STA(10 + i);
      if (__builtin_expect(c.rare != 0, 0)) { if (c.redo) sm_redo<0>(); ev_call(i + 1); }
      phaseB<0>(c);
    }
    STA(4);
    if ((mode & 3) == 1) asm volatile("v_bfrev_b32 v122, 1");
    else asm volatile("v_mov_b32 v122, v84\n\tbuffer_load_dwordx4 v[128:131], v88, s[88:91], %0 offen sc1" ::"s"(oP) : "memory");
    GAP();
    phaseA<0, 3>(c);
    if (__builtin_expect(c.redo != 0, 0)) sm_redo<0>();
    STA(5);
    KST(0, 9 + 3 * (it && !isY));
    const float l[2] = {row_reduce<false>(vget<R_RS>() + vget<R_TS>()), row_reduce<false>(vget<R_RS + 1>() + vget<R_TS + 1>())};
    const float cm[2] = {vget<R_M>(), vget<R_M + 1>()};
    EP p;
    if (it == 0) {
      const f32x4 ml = {cm[0], l[0], cm[1], l[1]};
      asm volatile("buffer_store_dwordx4 %0, v88, s[88:91], %1 offen sc1\n\ts_mov_b32 s101, %2" ::"v"(ml), "s"(oP), "s"(oP + wid * 16384) : "memory");
      phaseF<0>(c, p, make_iseq<74>{});
      asm volatile(FL_POLL "\n\ts_waitcnt vmcnt(17)" ::: "memory");
    } else {
      int lz = threadIdx.x & 63; asm volatile("" : "+v"(lz));
      const int rw = lz >> 2;
      static_for([&](auto E) { vsetu<R_TB + E>(rw * 128 + 16 * ((2 * E + ((lz & 3) >> 1)) ^ (rw & 6)) + 8 * (lz & 1)); }, make_iseq<4>{});
      const unsigned vs = ((lz >> 3) & 1) * 16384 + ((lz >> 4) & 1) * 512 + (lz >> 5) * 256 + (lz & 7) * 16;
      vsetu<R_VS>(vs); vsetu<R_VS + 1>(vs + 32768);
      tload<96, T_SLAB>();
      asm volatile("s_mov_b32 s97, s79\n\ts_mov_b32 s100, 0x8000\n\ts_movk_i32 s99, 0x1000");
      if (!(mode & 2)) {
        p.fa[0] = __builtin_amdgcn_rcpf(l[0]); p.fa[1] = __builtin_amdgcn_rcpf(l[1]);
        phaseF<1>(c, p, make_iseq<74>{});
      } else {
        const f32x4 ml = {vget<128>(), vget<129>(), vget<130>(), vget<131>()};
        if (__builtin_expect(__builtin_amdgcn_ballot_w64(ml[0] != cm[0] || ml[2] != cm[1]) == 0, 1)) {
          p.fa[0] = __builtin_amdgcn_rcpf(l[0] + ml[1]); p.fa[1] = __builtin_amdgcn_rcpf(l[1] + ml[3]);
          const unsigned one = (lz >> 4) != ((lz >> 2) & 3) ? 0u : (lz & 1) ? 0x3f800000u : 0x3f80u, o0 = (lz & 2) ? 0u : one, o1 = (lz & 2) ? one : 0u;
          vsetu<224>(o0); vsetu<225>(o1); vsetu<230>(o0); vsetu<231>(o1);
          asm volatile("v_mov_b64 v[226:227], 0\n\tv_mov_b64 v[228:229], 0");
          phaseF<3>(c, p, make_iseq<74>{});
        } else {
#pragma unroll
          for (int e = 0; e < 2; ++e) {
            const float mm = fmaxf(cm[e], ml[2 * e]);
            const float wa = __builtin_amdgcn_exp2f(cm[e] - mm), wb = __builtin_amdgcn_exp2f(ml[2 * e] - mm);
            const float inv = __builtin_amdgcn_rcpf(l[e] * wa + ml[2 * e + 1] * wb);
            p.fa[e] = wa * inv; p.fb[e] = wb * inv;
          }
          phaseF<2>(c, p, make_iseq<74>{});
        }
      }
    }
    KST(0, 10 + 3 * (it && !isY));
  }
  asm volatile("v_xor_b32 v%c0, s100, v%c0\np_loop:" ::"i"(R_TB));
  GAP();
  p_half<0>(make_iseq<32>{});
  p_half<1>(make_iseq<32>{});
  asm volatile("s_cbranch_scc1 p_loop\n\ts_nop 5");
  GAP();
  p_cvt<1, 0>(); p_cvt<1, 1>(); p_cvt<1, 2>(); p_cvt<1, 3>();
  p_store<1, 1>();
  GAP();
  KST(0, 27);
  {
    constexpr unsigned rdy = 98304;
    int lz = threadIdx.x & 63; asm volatile("" : "+v"(lz));
    const int fr = lz & 15, fq = lz >> 4, qbF = isY ? 15 - pj : pj, u = hsel * 8 + j;
    asm volatile("ds_write_b32 %0, %1" ::"v"(rdy), "v"(0) : "memory");
    vsetu<81>(lz == 0 ? 0u : 0x80000000u);
    vsetu<79>(fo(1024) + (qbF * 4 + (u >> 2)) * 4352 + fr * 8);
    {
      const int jj = 4 * (fr >> 3) + (fr & 3);
      const unsigned one = ((fq ^ (fr >> 2)) & 1) ? 0u : (jj & 1) ? 0x3f800000u : 0x3f80u;
      static_for([&](auto I) { vsetu<96 + I>((jj >> 1) == I ? one : 0u); }, make_iseq<4>{});
    }
    unsigned vo = (fq >> 1) * 1024 + (fq & 1) * 256 + fr * 16, vo1 = wid < 3 ? vo + 0x100000 : 0x80000000u;
    __amdgpu_buffer_rsrc_t so_ = __builtin_amdgcn_make_buffer_rsrc((void*)out, (short)0, SEQ * OUTW * 2, 0x00020000);
    unsigned vw = ((qbF * 128 + u * 8 + (fr & 7)) * OUTW + DA + wid * 64 + (fr >> 3) * 32 + fq * 8) * 2;
    unsigned so = O_S + qbF * 0x200000 + (1 + wid) * 0x40000 + u * 16384;
    asm volatile("" : "+s"(so), "+s"(so_));
    asm volatile("s_waitcnt vmcnt(0) lgkmcnt(0)" : "+v"(vo), "+v"(vo1), "+v"(vw) :: "memory");
    fl_set<T_FS>();
    asm volatile("s_barrier" ::: "memory");
    KST(0, 30);
    if (doA) post_math(pj, pu, out, lds + 131072 + wid * 8192);
    unsigned pend = 0x5555u, cnt = 0x400000;
    KST(0, 28);
    while (true) {
      unsigned nw;
      if (wid == 3) {
        u64 m;
        const u64 ex = pend | (pend << 1);
        asm volatile(FL_ARM("%1") "\n\t" FL_POLL "\n\ts_waitcnt vmcnt(0)\n\tv_cmp_eq_u64 %0, s[84:85], v[124:125]" : "=s"(m) : "s"(ex) : "memory");
        nw = (unsigned)m & ((unsigned)m >> 1) & pend;
        if (nw) asm volatile("ds_write_b32 %0, %1" ::"v"(rdy), "v"(~pend | nw) : "memory");
      } else {
        unsigned t;
        asm volatile("ds_read_b32 %0, %1\n\ts_waitcnt lgkmcnt(0)" : "=v"(t) : "v"(rdy) : "memory");
        nw = __builtin_amdgcn_readfirstlane(t) & pend;
      }
      if (--cnt == 0) nw = pend;
      if (nw == 0) {
        if (wid == 3) asm volatile("s_sleep 4");
        else asm volatile("s_sleep 1");
        continue;
      }
      asm volatile("s_nop 4");
      static_for([&](auto S) {
        if ((nw >> (2 * S)) & 1) { r_load<0, 2 * S>(vo, so); r_load<0, 2 * S + 1>(vo, so); r_load<1, 2 * S>(vo1, so); r_load<1, 2 * S + 1>(vo1, so); }
      }, make_iseq<8>{});
      pend &= ~nw;
      if (pend == 0) break;
    }
    asm volatile("s_waitcnt vmcnt(0)" ::: "memory");
    KST(0, 16);
    r_fin<0>(vw, so_, 0);
    if (wid < 3) r_fin<1>(vw, so_, 512);
    KST(0, 29);
    if (b == 128 && wid == 0) asm volatile(FL_SET("%0") ::"s"(fo(6144)) : "memory");
  }
  asm volatile("s_waitcnt vmcnt(0)" ::: "memory");
  asm volatile("pf_code_end:");
  KST(0, 1);
}

extern "C" void kernel_launch(void* const* d_in, const int* in_sizes, int n_in, void* d_out, int out_size, void* d_ws,
                              size_t ws_size, hipStream_t stream) {
  const bf16* Aq = (const bf16*)d_in[0];
  const bf16* Ak = (const bf16*)d_in[1];
  const bf16* Av = (const bf16*)d_in[2];
  const bf16* Ash = (const bf16*)d_in[3];
  const bf16* Assmk = (const bf16*)d_in[4];
  const bf16* AssmD = (const bf16*)d_in[5];
  const bf16* Bx = (const bf16*)d_in[6];
  const bf16* Bckv = (const bf16*)d_in[7];
  const bf16* Bwq = (const bf16*)d_in[8];
  const bf16* Bwuk = (const bf16*)d_in[9];
  const bf16* Bwuv = (const bf16*)d_in[10];
  const bf16* Bwo = (const bf16*)d_in[11];
  bf16* out = (bf16*)d_out;
  hipFuncSetAttribute((const void*)fused_kernel, hipFuncAttributeMaxDynamicSharedMemorySize, 163840);
  fused_kernel<<<256, 256, 163840, stream>>>(Bckv, Bwuk, Bwuv, Bx, Bwq, (bf16*)d_ws, Ak, Av, Ash, Assmk, AssmD, Bwo, Aq, out);
}
```

```cpp
#include <hip/hip_runtime.h>
#include <hip/hip_bf16.h>
#include <stdint.h>
#include <utility>

typedef __hip_bfloat16 bf16;
typedef __attribute__((ext_vector_type(4))) short bf16x4;
typedef __attribute__((ext_vector_type(2))) unsigned u32x2;

constexpr int SEQ = 2048;
constexpr int DA = 448;
constexpr int NH = 16;
constexpr int HD = 256;
constexpr int DC = 1024;
constexpr int OUTW = 896;
constexpr unsigned TSB = NH * SEQ * HD * 2, O_K = TSB, O_S = 3 * TSB, O_P = 5 * TSB, PSTR = 67584, O_F = O_P + 128 * PSTR,
                   O_X = O_F + 400 * 4352, O_Z = O_X + DA * SEQ * 2, WSB = O_Z + DA * SEQ * 2;
constexpr unsigned fo(int w) { return O_F + (w >> 4) * 4352 + (w & 15) * 8; }

#define KST(kid, which)

__device__ __forceinline__ float bf2f(unsigned short u) { return __uint_as_float(((unsigned)u) << 16); }
__device__ __forceinline__ unsigned short f2bf(float f) { return __bfloat16_as_ushort(__float2bfloat16(f)); }
__device__ __forceinline__ float rbf(float f) { return bf2f(f2bf(f)); }
typedef __attribute__((ext_vector_type(2))) __bf16 bf16x2n;
typedef __attribute__((ext_vector_type(2))) float f32x2n;
__device__ __forceinline__ unsigned pk2(float a, float b) {
  return __builtin_bit_cast(unsigned, __builtin_convertvector((f32x2n){a, b}, bf16x2n));
}

__device__ __forceinline__ int off_b(int row, int ch) { return 256 * row + 16 * (ch ^ (((row & 3) << 2) | ((row >> 2) & 3))); }

template <int R> __device__ __forceinline__ void sset(unsigned x) { asm volatile("s_mov_b32 s%c0, %1" ::"i"(R), "s"(x)); }
template <int R> __device__ __forceinline__ void vsetu(unsigned x) { asm volatile("v_mov_b32 v%c0, %1" ::"i"(R), "v"(x)); }
template <int R> __device__ __forceinline__ void srd_set(const void* p, unsigned n) {
  asm volatile("s_mov_b64 s[%c0:%c1], %4\n\ts_mov_b32 s%c2, %5\n\ts_mov_b32 s%c3, 0x20000" ::"i"(R), "i"(R + 1), "i"(R + 2), "i"(R + 3), "s"(p), "s"(n));
}
template <int R, int L> __device__ __forceinline__ void tload() { asm volatile("v_readlane_b32 s%c0, v78, %1" ::"i"(R), "i"(L)); }
template <int L> __device__ __forceinline__ unsigned tget() { unsigned t; asm volatile("v_readlane_b32 %0, v78, %1" : "=s"(t) : "i"(L)); return t; }
#define FL_SET(S) "buffer_store_dwordx2 v[82:83], v81, s[88:91], " S " offen sc1"
#define FL_ARM(N) "v_cndmask_b32_e64 v80, -2.0, v79, " N
#define FL_POLL "buffer_load_dwordx2 v[124:125], v80, s[88:91], 0 offen sc1"
#define FL_CHECK(N) "s_mov_b32 s48, 0x100000\n7:\n\tv_cmp_eq_u64 vcc, s[84:85], v[124:125]\n\ts_and_b64 vcc, vcc, " N "\n\ts_cmp_eq_u64 vcc, " N \
                    "\n\ts_cbranch_scc1 8f\n\ts_sleep 2\n\t" FL_POLL "\n\ts_waitcnt vmcnt(0)\n\ts_sub_u32 s48, s48, 1\n\ts_cbranch_scc0 7b\n8:"
template <int L> __device__ __forceinline__ void fl_set() {
  asm volatile("v_readlane_b32 s48, v78, %0\n\ts_nop 4\n\t" FL_SET("s48") ::"i"(L) : "memory");
}
template <int L> __device__ __forceinline__ void fl_poll() {
  asm volatile("v_readlane_b32 s50, v78, %0\n\tv_readlane_b32 s51, v78, %1\n\ts_nop 1\n\t" FL_ARM("s[50:51]") "\n\t" FL_POLL ::"i"(L), "i"(L + 1) : "memory");
}
__device__ __forceinline__ void fl_check() { asm volatile(FL_CHECK("s[50:51]") ::: "vcc", "scc", "memory"); }
template <int R> __device__ __forceinline__ float vget() { float t; asm volatile("v_mov_b32 %0, v%c1" : "=v"(t) : "i"(R)); return t; }
template <int R> __device__ __forceinline__ void vset(float x) { asm volatile("v_mov_b32 v%c0, %1" ::"i"(R), "v"(x)); }
template <int R> __device__ __forceinline__ float acc_read1() { float t; asm volatile("v_accvgpr_read_b32 %0, a[%c1]" : "=v"(t) : "i"(R)); return t; }
template <class F, int... I> __device__ __forceinline__ void static_for(F&& f, std::integer_sequence<int, I...>) {
  (f(std::integral_constant<int, I>{}), ...);
}
template <int... Is> using iseq = std::integer_sequence<int, Is...>;
template <int N> using make_iseq = std::make_integer_sequence<int, N>;

typedef __attribute__((ext_vector_type(4))) unsigned u32x4c;
typedef __attribute__((ext_vector_type(4))) float f32x4c;
typedef __attribute__((ext_vector_type(2))) float f32x2c;
typedef unsigned long long u64;
struct PR { u32x4c kk[7], vv[4]; };
__device__ __forceinline__ void prep_load(PR& p, int tid, int cg, int tb, const bf16* __restrict__ k, const bf16* __restrict__ v,
                                          const bf16* __restrict__ shift_k) {
  const __amdgpu_buffer_rsrc_t sk = __builtin_amdgcn_make_buffer_rsrc((void*)k, (short)0, SEQ * DA * 2, 0x00020000);
  const __amdgpu_buffer_rsrc_t sv = __builtin_amdgcn_make_buffer_rsrc((void*)v, (short)0, SEQ * DA * 2, 0x00020000);
  const __amdgpu_buffer_rsrc_t sw = __builtin_amdgcn_make_buffer_rsrc((void*)(shift_k + cg * 256), (short)0, 512, 0x00020000);
  const int vo = ((tb * 128 + (tid >> 3) * 4) * DA + cg * 64 + (tid & 7) * 8) * 2;
#pragma unroll
  for (int q = 0; q < 7; ++q)
    asm volatile("buffer_load_dwordx4 %0, %1, %2, 0 offen" : "=v"(p.kk[q]) : "v"(vo + (q - 3) * DA * 2), "s"(sk) : "memory");
#pragma unroll
  for (int q = 0; q < 4; ++q)
    asm volatile("buffer_load_dwordx4 %0, %1, %2, 0 offen offset:%3" : "=v"(p.vv[q]) : "v"(vo), "s"(sv), "i"(q * DA * 2) : "memory");
#pragma unroll
  for (int q = 0; q < 4; ++q)
    asm volatile("buffer_load_dwordx4 a[%c0:%c1], %2, %3, 0 offen offset:%4" ::"i"(4 * q), "i"(4 * q + 3), "v"((tid & 7) * 64), "s"(sw), "i"(q * 16) : "memory");
}
__device__ __forceinline__ float bfl(unsigned u) { return __uint_as_float(u << 16); }
__device__ __forceinline__ float bfh(unsigned u) { return __uint_as_float(u & 0xffff0000u); }
__device__ __forceinline__ f32x2c rbf2(f32x2c a) { const unsigned u = pk2(a[0], a[1]); return (f32x2c){bfl(u), bfh(u)}; }
template <int I> __device__ __forceinline__ void prep_math(const PR& p, int tid, char* lx) {
  const int rp = tid >> 3, c = tid & 7;
  f32x2c wt[4], kf[7];
  const unsigned w[4] = {__float_as_uint(acc_read1<4 * I>()), __float_as_uint(acc_read1<4 * I + 1>()), __float_as_uint(acc_read1<4 * I + 2>()),
                         __float_as_uint(acc_read1<4 * I + 3>())};
#pragma unroll
  for (int t = 0; t < 4; ++t)
    wt[t] = (t & 1) ? (f32x2c){bfh(w[t >> 1]), bfh(w[2 + (t >> 1)])} : (f32x2c){bfl(w[t >> 1]), bfl(w[2 + (t >> 1)])};
#pragma unroll
  for (int q = 0; q < 7; ++q) kf[q] = (f32x2c){bfl(p.kk[q][I]), bfh(p.kk[q][I])};
  f32x2c x[4];
#pragma unroll
  for (int e = 0; e < 4; ++e) {
    f32x2c s = rbf2(kf[e] * wt[3]);
    s = rbf2(s + rbf2(kf[e + 1] * wt[2]));
    s = rbf2(s + rbf2(kf[e + 2] * wt[1]));
    s = rbf2(s + rbf2(kf[e + 3] * wt[0]));
    x[e] = s * (f32x2c){bfl(p.vv[e][I]), bfh(p.vv[e][I])};
  }
#pragma unroll
  for (int u = 0; u < 2; ++u)
    *(u32x2*)(lx + (c * 8 + 2 * I + u) * 256 + (((31 - rp) * 8) ^ (c << 4))) = (u32x2){pk2(x[3][u], x[2][u]), pk2(x[1][u], x[0][u])};
}
__device__ __forceinline__ void prep_store(int tid, int cg, int tb, bf16* __restrict__ xr, const char* lx) {
  const __amdgpu_buffer_rsrc_t sx = __builtin_amdgcn_make_buffer_rsrc((void*)(xr + (size_t)cg * 64 * SEQ), (short)0, 64 * SEQ * 2, 0x00020000);
#pragma unroll
  for (int rd = 0; rd < 4; ++rd) {
    const int idx = tid + 256 * rd, ch = idx >> 4, q = idx & 15;
    u32x4c w = *(const u32x4c*)(lx + ch * 256 + 16 * (q ^ (ch >> 3)));
    __builtin_amdgcn_raw_buffer_store_b128(w, sx, (ch * SEQ + (15 - tb) * 128 + q * 8) * 2, 0, 16);
  }
}

#define CODE_TOUCH(LB, LE, wx, NW, NL, m0v) do { unsigned blo_, bhi_, elo_; \
    asm volatile("s_getpc_b64 s[48:49]\n\ts_add_u32 s48, s48, " LB "@rel32@lo+4\n\ts_addc_u32 s49, s49, " LB "@rel32@hi+12\n\t" \
                 "s_mov_b32 %0, s48\n\ts_mov_b32 %1, s49\n\t" \
                 "s_getpc_b64 s[48:49]\n\ts_add_u32 s48, s48, " LE "@rel32@lo+4\n\ts_mov_b32 %2, s48" \
                 : "=s"(blo_), "=s"(bhi_), "=s"(elo_) :: "scc"); \
    const unsigned n_ = elo_ - (blo_ & ~127u); \
    const __amdgpu_buffer_rsrc_t srd_ = __builtin_amdgcn_make_buffer_rsrc((void*)(((unsigned long long)bhi_ << 32) | (blo_ & ~127u)), (short)0, n_ < 262144u ? n_ : 0u, 0x00020000); \
    const unsigned l_ = threadIdx.x & 63, vo_ = l_ < (NL) ? ((wx) + l_ * (NW)) * 128u : 0x80000000u; \
    asm volatile("s_mov_b32 m0, %0\n\ts_nop 4\n\tbuffer_load_dword %1, %2, 0 offen lds" ::"s"(m0v), "v"(vo_), "s"(srd_) : "memory"); } while (0)
template <int I> __device__ __forceinline__ void d_store() {
  asm volatile("buffer_store_dwordx4 v[%c0:%c1], v%c2, s[88:91], s101 offen offset:%3 sc1" ::"i"(128 + 4 * I), "i"(131 + 4 * I), "i"(112 + ((I >> 2) & 1)),
               "i"((I & 3) * 1024) : "memory");
  if constexpr ((I & 7) == 7) asm volatile("s_addk_i32 s101, 0x4000" ::: "scc");
}
constexpr int NS_X = 19;
struct CV { unsigned pa[4], pb, px, vz, sz; float D; };
constexpr int cv_J(bool up, int q) { int J = up ? 0 : 7; while (q > J) { q -= J + 1; J += up ? 1 : -1; } return J; }
constexpr int cv_i(bool up, int q) { int J = up ? 0 : 7; while (q > J) { q -= J + 1; J += up ? 1 : -1; } return q; }
template <int R, int N> __device__ __forceinline__ void cv_rd(CV& c) {
  if constexpr (N < 16) {
    constexpr int i = N >> 1, o = 16 * R + 128 * (i & 1) + 2 * (N & 1), r = 32 + 32 * (R & 1) + 2 * N;
    asm volatile("ds_read2_b32 a[%c1:%c2], %0 offset0:%c3 offset1:%c4" ::"v"(c.pa[i >> 1]), "i"(r), "i"(r + 1), "i"(o), "i"(o + 1));
  } else {
    constexpr int m = N - 16, r = 192 + 32 * (R & 1) + 4 * m;
    asm volatile("ds_read_b128 a[%c1:%c2], %0 offset:%c3" ::"v"(c.pb), "i"(r), "i"(r + 3), "i"(64 * R + 512 * (7 - m)));
  }
}
template <int O> __device__ __forceinline__ void cv_xd(CV& c) {
  constexpr int p = O / 7, k = O % 7, t = 160 + 2 * p, x = 96 + (p ^ 1);
  if constexpr (k == 0) asm volatile("v_and_b32 v%c0, 0xffff0000, v%c1" ::"i"(t), "i"(x));
  if constexpr (k == 1) asm volatile("v_lshlrev_b32 v%c0, 16, v%c1" ::"i"(t + 1), "i"(x));
  if constexpr (k == 2 || k == 3) asm volatile("v_mul_f32 v%c0, v%c0, %1" ::"i"(t + k - 2), "v"(c.D));
  if constexpr (k == 4) asm volatile("v_cvt_pk_bf16_f32 v%c0, v%c1, v%c0" ::"i"(t + 1), "i"(t));
  if constexpr (k == 5) asm volatile("v_lshlrev_b32 v%c0, 16, v%c1" ::"i"(t), "i"(t + 1));
  if constexpr (k == 6) asm volatile("v_and_b32 v%c0, 0xffff0000, v%c0" ::"i"(t + 1));
}
template <int O> __device__ __forceinline__ void cv_z(CV& c) {
  constexpr int J = O / 13, k = O % 13, hf = k & 1, y = 128 + 4 * J + 2 * hf;
  if constexpr (k < 2) asm volatile("v_cvt_pk_bf16_f32 v%c0, v%c1, v%c0" ::"i"(y + 1), "i"(y));
  else if constexpr (k < 4) asm volatile("v_lshlrev_b32 v%c0, 16, v%c1" ::"i"(y), "i"(y + 1));
  else if constexpr (k < 6) asm volatile("v_and_b32 v%c0, 0xffff0000, v%c0" ::"i"(y + 1));
  else if constexpr (k < 10) asm volatile("v_add_f32 v%c0, v%c0, v%c1" ::"i"(y + (k >= 8)), "i"(y + 32 + (k >= 8)));
  else if constexpr (k < 12) asm volatile("v_cvt_pk_bf16_f32 v%c0, v%c1, v%c2" ::"i"(128 + 4 * J + hf), "i"(y), "i"(y + 1));
  else asm volatile("buffer_store_dwordx2 v[%c0:%c1], %2, s[88:91], %3 offen offset:%4 sc1" ::"i"(128 + 4 * J), "i"(129 + 4 * J), "v"(c.vz), "s"(c.sz),
                    "i"(512 * J) : "memory");
}
constexpr int cv_z0(int Q) {
  int o = 0;
  for (int q = 0; q < Q; ++q)
    for (int k = 0; k < 3; ++k) { const int J = o / 13; if ((J + 1) * (J + 2) / 2 - 1 + 4 <= q) ++o; }
  return o;
}
constexpr int cv_xd0(int T) { int o = 0; for (int t = 0; t < T; ++t) o += t % 36 < 24 ? 1 : 2; return o < 112 ? o : 112; }
template <int B, int... Is> __device__ __forceinline__ void cv_zs(CV& c, iseq<Is...>) { (cv_z<B + Is>(c), ...); }
template <int B, int... Is> __device__ __forceinline__ void cv_xds(CV& c, iseq<Is...>) { (cv_xd<B + Is>(c), ...); }
template <int R, int Q> __device__ __forceinline__ void cv_slot(CV& c) {
  constexpr int i = cv_i(R == 7, Q), J = cv_J(R == 7, Q), d = 128 + 4 * J, a = 32 + 32 * (R & 1) + 4 * i, b = 192 + 32 * (R & 1) + 4 * (J - i);
  constexpr int T = 36 * R + Q, vm = Q % 4 == 1 && Q < 32 ? Q / 4 : -1;
  if constexpr (Q == 0) asm volatile("s_waitcnt lgkmcnt(0)");
  if constexpr (R >= 6 && vm >= 0 && vm % 4 == 0) asm volatile("s_add_u32 m0, s79, %0" ::"i"((R - 6) * 32768 + vm * 1024));
  if constexpr (R == 0 && i == 0)
    asm volatile("v_mfma_f32_16x16x32_bf16 v[%c0:%c1], a[%c2:%c3], a[%c4:%c5], 0" ::"i"(d), "i"(d + 3), "i"(a), "i"(a + 3), "i"(b), "i"(b + 3));
  else
    asm volatile("v_mfma_f32_16x16x32_bf16 v[%c0:%c1], a[%c2:%c3], a[%c4:%c5], v[%c0:%c1]" ::"i"(d), "i"(d + 3), "i"(a), "i"(a + 3), "i"(b), "i"(b + 3));
  if constexpr (Q < 24 && R < 7) cv_rd<R + 1, Q>(c);
  if constexpr (T % 7 == 3 && NS_X + 5 + T / 7 < 32) d_store<NS_X + 5 + T / 7>();
  if constexpr (R == 2 && Q >= 24 && Q < 32)
    asm volatile("ds_read_b64 v[%c1:%c2], %0 offset:%c3" ::"v"(c.px), "i"(96 + 2 * (Q - 24)), "i"(97 + 2 * (Q - 24)), "i"(512 * (31 - Q)));
  if constexpr (R >= 3 && R < 6) cv_xds<cv_xd0(T - 108)>(c, make_iseq<cv_xd0(T - 107) - cv_xd0(T - 108)>{});
  if constexpr ((R == 3 || R == 4) && vm >= 0) {
    constexpr int f = (R - 3) * 8 + vm;
    asm volatile("buffer_load_dwordx4 a[%c0:%c1], v84, s[88:91], s96 offen offset:%2 sc1" ::"i"(128 + 4 * f), "i"(131 + 4 * f), "i"((f & 3) * 1024) : "memory");
    if constexpr ((f & 3) == 3) asm volatile("s_addk_i32 s96, 4096");
  }
  if constexpr (R == 4 && Q == 35) asm volatile(FL_POLL ::: "memory");
  if constexpr (R >= 6 && vm >= 0)
    asm volatile("buffer_load_dwordx4 v%c0, s[88:91], s98 offen offset:%1 sc1 lds" ::"i"(86 + vm / 4), "i"((vm & 3) * 1024) : "memory");
  if constexpr (R == 6 && Q == 35) asm volatile("s_add_u32 s98, s98, 0x8000");
  if constexpr (R == 7) {
    if constexpr (Q < 4) asm volatile("s_nop 0");
    cv_zs<cv_z0(Q)>(c, make_iseq<cv_z0(Q + 1) - cv_z0(Q)>{});
  }
  __builtin_amdgcn_sched_barrier(0);
}
template <int R, int... Q> __device__ __forceinline__ void cv_step(CV& c, iseq<Q...>) { (cv_slot<R, Q>(c), ...); }
template <int... N> __device__ __forceinline__ void cv_rd0(CV& c, iseq<N...>) { (cv_rd<0, N>(c), ...); }
template <int FLAG> __device__ __forceinline__ void conv_wave(unsigned sz, float Dd, char* lw) {
  int lane = threadIdx.x & 63; asm volatile("" : "+v"(lane));
  const int l16 = lane & 15, fq = lane >> 4;
  u32x4c w[5]; unsigned w4[5];
#pragma unroll
  for (int r = 0; r < 5; ++r) {
    w[r] = *(const u32x4c*)(lw + r * 1024 + lane * 16);
    w4[r] = *(const unsigned*)(lw + r * 1024 + lane * 16 + 16);
  }
  d_store<NS_X>(); d_store<NS_X + 1>();
#pragma unroll
  for (int r = 0; r < 5; ++r)
    *(u32x4c*)(lw + 10304 + r * 1024 + lane * 16) = (u32x4c){__builtin_amdgcn_alignbit(w[r][1], w[r][0], 16), __builtin_amdgcn_alignbit(w[r][2], w[r][1], 16),
                                                             __builtin_amdgcn_alignbit(w[r][3], w[r][2], 16), __builtin_amdgcn_alignbit(w4[r], w[r][3], 16)};
  d_store<NS_X + 2>();
  CV c;
#pragma unroll
  for (int i = 0; i < 4; ++i) c.pa[i] = (unsigned)(size_t)lw + ((l16 + 1) & 1) * 10304 + 4 * ((8 * fq + l16 + 1) >> 1) + 1024 * i;
  c.pb = (unsigned)(size_t)lw + 5120 + (2032 - 16 * l16 + 8 * fq) * 2 - 512 * 7;
  c.vz = (16 * l16 + 4 * fq) * 2;
  c.px = (unsigned)(size_t)lw + 5120 + 2044 * 2 - 512 * 7 - c.vz;
  c.sz = sz; c.D = Dd;
  asm volatile("" : "+v"(c.vz), "+v"(c.px), "+v"(c.D) :: "memory");
  asm volatile("" : "+s"(c.sz));
  cv_rd0(c, make_iseq<24>{});
  d_store<NS_X + 3>(); d_store<NS_X + 4>();
  __builtin_amdgcn_sched_barrier(0);
  static_for([&](auto R) __attribute__((always_inline)) {
    cv_step<R>(c, make_iseq<36>{});
    if constexpr (R == 2) {
      asm volatile("s_waitcnt vmcnt(0)\n\ts_barrier" ::: "memory");
      fl_set<FLAG>();
    }
    if constexpr (R == 5) {
      asm volatile("s_waitcnt vmcnt(0)" ::: "memory");
      fl_check();
    }
    __builtin_amdgcn_sched_barrier(0);
  }, make_iseq<8>{});
  asm volatile("s_nop 7");
  cv_zs<cv_z0(36)>(c, make_iseq<104 - cv_z0(36)>{});
}

__device__ __forceinline__ void post_dma(int cg, int tb, const bf16* __restrict__ q, unsigned m0w) {
  int lane = threadIdx.x & 63; asm volatile("" : "+v"(lane));
  const __amdgpu_buffer_rsrc_t sq = __builtin_amdgcn_make_buffer_rsrc((void*)(q + (size_t)tb * 32 * DA + cg * 64), (short)0, 32 * DA * 2, 0x00020000);
  const unsigned vq = ((lane >> 3) * DA + (lane & 7) * 8) * 2;
  asm volatile("s_mov_b32 m0, %0\n\ts_nop 4\n\tbuffer_load_dwordx4 %1, %2, 0 offen lds\n\tbuffer_load_dwordx4 %1, %2, %3 offen offset:1024 lds\n\t"
               "buffer_load_dwordx4 %1, %2, %4 offen offset:2048 lds\n\tbuffer_load_dwordx4 %1, %2, %5 offen offset:3072 lds"
               ::"s"(m0w + 4096), "v"(vq), "s"(sq), "s"(8 * DA * 2 - 1024), "s"(16 * DA * 2 - 2048), "s"(24 * DA * 2 - 3072) : "memory");
}
__device__ __forceinline__ void post_math(int cg, int tb, bf16* __restrict__ out, const char* lw) {
  int lane = threadIdx.x & 63; asm volatile("" : "+v"(lane));
  const int tl = lane >> 3, ck = lane & 7;
  const __amdgpu_buffer_rsrc_t so = __builtin_amdgcn_make_buffer_rsrc((void*)(out + (size_t)tb * 32 * OUTW + cg * 64), (short)0, 32 * OUTW * 2 - 2 * DA, 0x00020000);
  const unsigned vo = (tl * OUTW + ck * 8) * 2;
#pragma unroll
  for (int i = 0; i < 4; ++i) {
    const u32x4c qv = *(const u32x4c*)(lw + 4096 + i * 1024 + lane * 16);
    const char* lz = lw + ck * 64 + 16 * (i ^ (ck >> 1)) + tl * 2;
    u32x4c o;
#pragma unroll
    for (int e = 0; e < 4; ++e) {
      const float z0 = bf2f(*(const unsigned short*)(lz + 2 * e * 512));
      const float z1 = bf2f(*(const unsigned short*)(lz + (2 * e + 1) * 512));
      o[e] = pk2(bfl(qv[e]) * z0, bfh(qv[e]) * z1);
    }
    __builtin_amdgcn_raw_buffer_store_b128(o, so, vo + i * 8 * OUTW * 2, 0, 0);
  }
}

#define DI __device__ __forceinline__
#define GAP() __builtin_amdgcn_sched_barrier(0)
typedef __attribute__((ext_vector_type(4))) float f32x4g;
typedef __attribute__((ext_vector_type(4))) unsigned u32x4g;
constexpr int LDW = NH * HD;
struct GM { unsigned aA[2], aB[2][8]; };
template <bool W, int J, int V> DI void g_dma() {
  if constexpr (J == 0) asm volatile("buffer_load_dwordx4 v%c0, s[%c1:%c2], 0 offen lds" ::"i"(V), "i"(W ? 60 : 56), "i"(W ? 63 : 59) : "memory");
  else asm volatile("buffer_load_dwordx4 v%c0, s[%c1:%c2], s%c3 offen lds" ::"i"(V), "i"(W ? 60 : 56), "i"(W ? 63 : 59), "i"((W ? 70 : 63) + J) : "memory");
}
template <int P, int KS, int I> DI void g_wread(GM& g) {
  constexpr int nf = I >> 1, hf = I & 1, r = 192 - 64 * P + 8 * nf + 4 * KS + 2 * hf;
  asm volatile("ds_read_b64_tr_b16 a[%c1:%c2], %0 offset:%c3" ::"v"(g.aB[hf][nf]), "i"(r), "i"(r + 1), "i"(P * 32768 + KS * 16384));
}
template <int P, int M, int KS> DI void g_aread(GM& g) {
  constexpr int r = 96 + 8 * (M & 1) + 4 * KS;
  asm volatile("ds_read_b128 v[%c1:%c2], %0 offset:%c3" ::"v"(g.aA[KS]), "i"(r), "i"(r + 3), "i"(P * 32768 + M * 2048));
}
constexpr float CS = 0.0625f * 1.4426950408889634f;
struct ES { int op0[129], nop[129]; };
constexpr ES e_sched(bool TR, bool SCL) {
  ES z{};
  const int per = SCL ? 16 : 8;
  int o = 0;
  for (int G = 0; G <= 128; ++G) {
    int cap = G == 128 ? 10000 : SCL ? 4 : 2;
    z.op0[G] = o;
    while (o < 32 * per) {
      const int i = o / per, rdy = (TR ? 32 * (i >> 3) + 16 + 2 * (i & 7) + 1 : 4 * i + 3) + 4;
      if ((G < rdy && G < 128) || cap-- <= 0) break;
      ++o; ++z.nop[G];
    }
  }
  return z;
}
template <bool TR, bool SCL> constexpr ES ESCH = e_sched(TR, SCL);
constexpr int e_vm(int G) { return (G * 3) % 8 < 3 ? G * 3 / 8 : -1; }
constexpr int e_st(int MODE, int G) {
  if (MODE == 5) return e_vm(G) >= 32 ? e_vm(G) - 32 : -1;
  if (MODE == 3) return G >= 31 && (G - 31) % 5 == 0 && (G - 31) / 5 < NS_X ? (G - 31) / 5 : -1;
  const int g0 = MODE == 4 ? 5 : 13, gs = MODE == 2 ? 7 : 6;
  return MODE >= 2 && G >= g0 && (G - g0) % gs == 0 && (G - g0) / gs < 16 ? (G - g0) / gs + (MODE == 4 ? 16 : 0) : -1;
}
constexpr int e_nst(int MODE, int G0, int G1) { int n = 0; for (int G = G0; G < G1; ++G) n += e_st(MODE, G) >= 0; return n; }
static_assert(e_nst(2, 0, 128) == 16 && e_nst(3, 30, 128) == NS_X && e_nst(5, 0, 128) == 16 && e_nst(4, 0, 104) == 16);
template <bool TR, bool SCL, int O> DI void e_op() {
  constexpr int per = SCL ? 16 : 8, i = O / per, k = O % per, r = 128 + 4 * i, t = 114 + 4 * (i & 1), c = k - (per - 4);
  if constexpr (k < 4) asm volatile("v_accvgpr_read_b32 v%c0, a[%c1]" ::"i"(t + k), "i"(4 * i + k));
  else if constexpr (c < 0) asm volatile("v_mul_f32 v%c0, %1, v%c0" ::"i"(k < 8 ? r + (k ^ 6) : t + k - 8), "s"(CS));
  else {
    constexpr int d = c == 0 ? r + 3 : c == 1 ? r + 2 : r + c - 2, x = c == 0 ? r + 2 : c == 1 ? r : t + 2 * (c - 2);
    asm volatile("v_cvt_pk_bf16_f32 v%c0, v%c1, v%c2" ::"i"(d), "i"(x), "i"(x + 1));
  }
}
template <bool TR, bool SCL, int B, int... Is> DI void e_ops(iseq<Is...>) { (e_op<TR, SCL, B + Is>(), ...); }
constexpr int e_slot(int MODE, int G) { return MODE == 3 ? (G % 3 == 0 && G < 30 ? G / 3 : -1) : e_vm(G) >= 0 && e_vm(G) < 32 ? 10 + e_vm(G) : -1; }
template <int S> DI void e_m0() {
  if constexpr (S == 0 || S == 1 || S == 5 || S == 9) asm volatile("s_add_u32 m0, s78, %0" ::"i"(S * 1024));
  if constexpr (S >= 26 && (S - 26) % 4 == 0) asm volatile("s_add_u32 m0, s79, %0" ::"i"(((S - 26) >> 3) * 32768 + ((S - 26) & 4) * 1024));
}
template <int S> DI void e_load() {
  if constexpr (S == 0) asm volatile("buffer_load_dwordx4 v122, s[56:59], 0 offen lds" ::: "memory");
  else if constexpr (S < 5) asm volatile("buffer_load_dwordx4 v84, s[56:59], s64 offen offset:%0 lds" ::"i"((S - 1) * 1024) : "memory");
  else if constexpr (S < 9) asm volatile("buffer_load_dwordx4 v84, s[60:63], 0 offen offset:%0 sc1 lds" ::"i"((S - 5) * 1024) : "memory");
  else if constexpr (S == 9) asm volatile("buffer_load_dwordx4 v84, s[60:63], s62 offen sc1 lds" ::: "memory");
  else if constexpr (S < 26) {
    constexpr int f = S - 10;
    asm volatile("buffer_load_dwordx4 a[%c0:%c1], v84, s[88:91], s96 offen offset:%2 sc1" ::"i"(128 + 4 * f), "i"(131 + 4 * f), "i"((f & 3) * 1024) : "memory");
    if constexpr ((f & 3) == 3) asm volatile("s_addk_i32 s96, 4096");
  } else {
    constexpr int f = S - 26;
    asm volatile("buffer_load_dwordx4 v%c0, s[88:91], s98 offen offset:%1 sc1 lds" ::"i"(86 + ((f >> 2) & 1)), "i"((f & 3) * 1024) : "memory");
    if constexpr (f == 7) asm volatile("s_add_u32 s98, s98, 0x8000");
    if constexpr (f == 15) asm volatile(FL_POLL ::: "memory");
  }
}
template <bool TR, int P, int H, int G, int MODE> DI void g_gap(GM& g) {
  constexpr int m = 4 * H + (G >> 4), q = G & 15, n = q >> 1, ks = q & 1, GG = 64 * H + G;
  constexpr bool inV = TR ? m & 1 : n & 1, END = MODE == 3 || MODE == 5, EPI = END || MODE == 2;
  constexpr int a = 96 + 8 * (m & 1) + 4 * ks, b = 192 - 64 * P + 8 * n + 4 * ks, d = (inV ? 128 : 0) + 4 * (TR ? 8 * (m >> 1) + n : 4 * m + (n >> 1));
  constexpr int J = END ? -1 : q == 8 ? 2 * (G >> 4) : q == 12 ? 2 * (G >> 4) + 1 : -1, gj = (J & 3) ^ ((J & 3) >> 1);
  if constexpr (END) { if constexpr (q == 0 && GG > 0) asm volatile("s_waitcnt lgkmcnt(0)"); e_m0<e_slot(MODE, GG)>(); }
  else if constexpr (q == 0 && G > 0) asm volatile("s_waitcnt lgkmcnt(4)");
  if constexpr (J == 0) asm volatile("s_add_u32 m0, s78, %0" ::"i"((P ^ H ^ 1) * 32768 + (1 - H) * 8192));
  else if constexpr (J == 4) asm volatile("s_add_u32 m0, s79, %0" ::"i"((P ^ H) * 32768 + (1 - H) * 16384));
  else if constexpr (J > 0 && (J & 3) == 1) asm volatile("s_bitset1_b32 m0, 12");
  else if constexpr (J > 0 && (J & 3) == 2) asm volatile("s_bitset1_b32 m0, %0" ::"i"(J < 4 ? 14 : 13));
  else if constexpr (J > 0) asm volatile("s_bitset0_b32 m0, 12");
#define GMF(D, X, Y, C) asm volatile("v_mfma_f32_16x16x32_bf16 " D "[%c0:%c1], " X ", " Y ", " C ::"i"(d), "i"(d + 3), "i"(a), "i"(a + 3), "i"(b), "i"(b + 3))
#define GMF2(D, C) do { if constexpr (TR) GMF(D, "v[%c2:%c3]", "a[%c4:%c5]", C); else GMF(D, "a[%c4:%c5]", "v[%c2:%c3]", C); } while (0)
  if constexpr ((MODE == 1 || MODE == 4) && ks == 0) { if constexpr (inV) GMF2("v", "0"); else GMF2("a", "0"); }
  else if constexpr (inV) GMF2("v", "v[%c0:%c1]");
  else GMF2("a", "a[%c0:%c1]");
#undef GMF2
#undef GMF
  if constexpr (e_st(MODE, GG) >= 0) {
    static_assert(MODE == 4 || ESCH<TR, MODE == 2>.op0[GG] >= (e_st(MODE, GG) + 1) * (MODE == 2 ? 16 : 8));
    d_store<e_st(MODE, GG)>();
  }
  if constexpr (EPI) e_ops<TR, MODE == 2, ESCH<TR, MODE == 2>.op0[GG]>(make_iseq<ESCH<TR, MODE == 2>.nop[GG]>{});
  if constexpr (J >= 0 && J < 4) g_dma<false, (1 - H) * 2 + (gj & 1) + (gj >> 1) * 4, 90>();
  else if constexpr (J >= 4) g_dma<true, (1 - H) * 4 + gj, 91>();
  if constexpr ((q == 1 || q == 2) && !(END && m == 7)) g_aread<(m < 7 ? P : P ^ 1), (m + 1) & 7, q - 1>(g);
  if constexpr (END) {
    if constexpr (e_slot(MODE, GG) >= 0) e_load<e_slot(MODE, GG)>();
    if constexpr (!TR && GG >= 3 && GG < 7 && e_slot(MODE, GG) < 0) asm volatile("s_nop 0");
    if constexpr (TR && GG >= 19 && GG < 21 && e_slot(MODE, GG) < 0) asm volatile("s_nop 0");
  } else {
    if constexpr (q >= 3 && q < 11 && (q & 1)) g_wread<P ^ 1, H, (G >> 4) * 4 + (q - 3) / 2>(g);
    if constexpr (H == 1 && G == 0) asm volatile("v_lshl_add_u32 v90, s82, 7, v92\n\ts_and_b32 s82, s80, 15");
    if constexpr (H == 1 && G == 4) asm volatile("s_lshr_b64 s[80:81], s[80:81], 4\n\tv_lshl_add_u32 v91, s82, 19, v93");
    constexpr int nst = e_nst(MODE, H ? 29 : 0, GG + 1);
    if constexpr (G == 61) asm volatile("s_waitcnt vmcnt(%0) lgkmcnt(0)\n\ts_barrier" ::"i"(12 + nst) : "memory");
  }
  if constexpr (EPI && GG == 127) {
    asm volatile("s_nop 7");
    e_ops<TR, MODE == 2, ESCH<TR, MODE == 2>.op0[128]>(make_iseq<ESCH<TR, MODE == 2>.nop[128]>{});
  }
  GAP();
}
template <bool TR, int P, int H, int MODE, int... Gs> DI void g_half_(GM& g, iseq<Gs...>) { (g_gap<TR, P, H, Gs, MODE>(g), ...); }
template <bool TR, int P, int H, int MODE> DI void g_half(GM& g) { g_half_<TR, P, H, MODE>(g, make_iseq<64>{}); }
template <bool TR, int P, int MODE> DI void g_tile(GM& g) { g_half<TR, P, 0, MODE>(g); g_half<TR, P, 1, MODE>(g); }
template <int P, int... Is> DI void g_read0(GM& g, iseq<Is...>) {
  (g_wread<P, 0, Is>(g), ...); (g_wread<P, 1, Is>(g), ...);
  g_aread<P, 0, 0>(g); g_aread<P, 0, 1>(g);
}
template <int I> DI void g_pro1(const unsigned (&m)[4]) {
  constexpr int grp = I >> 2, i = I & 3, t = grp < 4 ? 0 : grp < 7 ? 1 : 2;
  constexpr bool isA = grp == 0 || grp == 3 || grp == 5;
  constexpr int jj = (grp == 3 ? 2 : 0) + (i & 1) + (i >> 1) * 4, ks = grp == 2 || grp == 6;
  asm volatile("s_add_u32 m0, %0, %1\n\ts_nop 0" ::"s"(m[(t & 1) * 2 + !isA]), "i"(isA ? jj * 4096 : ks * 16384 + (i >> 1) * 8192 + (i & 1) * 4096) : "scc");
  if constexpr (isA) g_dma<false, jj, t ? 90 : 92>();
  else g_dma<true, ks * 4 + i, t ? 91 : 93>();
}
template <int B, int... Is> DI void g_pro(const unsigned (&m)[4], iseq<Is...>) { (g_pro1<B + Is>(m), ...); }
template <bool TR, bool SCL, class HOOK> DI void g_loop(GM& g, unsigned evm, HOOK&& hook) {
  constexpr int nkt = SCL ? 7 : 16, P0 = SCL ? 0 : 1;
  asm volatile("" : "+s"(evm));
#pragma unroll 1
  for (int t = 1;; t += 2) {
    g_half<TR, P0 ^ 1, 0, 0>(g);
    if (__builtin_expect((evm >> t) & 1, 0)) { hook(t); GAP(); }
    g_half<TR, P0 ^ 1, 1, 0>(g);
    if (SCL && t + 2 >= nkt) break;
    g_half<TR, P0, 0, 0>(g);
    if constexpr (SCL) { if (__builtin_expect(t == 3, 0)) { hook(4); GAP(); } }
    else asm volatile("s_cmp_eq_u32 %0, 13\n\ts_cselect_b32 s78, 0x28000, s78" ::"s"(t) : "scc");
    g_half<TR, P0, 1, 0>(g);
    if (!SCL && t + 3 >= nkt) break;
  }
}

typedef __attribute__((ext_vector_type(4))) unsigned u32x4;
typedef __attribute__((ext_vector_type(4))) float f32x4;

constexpr int R_S = 128, R_P = 192, R_CI = 224, R_E = 96;
constexpr int R_TS = 112, R_RS = 114, R_M = 116;
constexpr int R_PD = 118, R_THR = 120, R_TMP = 121;
constexpr int r_dk(int q) { return q < 6 ? 90 + q : 120 + q; }
constexpr int S_V = 96, S_T = 97, S_K = 98, S_I = 99, S_EV = 100;
struct AC {
  u64 redo;
  unsigned rare;
};
template <int X, int F> DI void kread() {
  constexpr int r = 192 + 4 * (F & 15);
  asm volatile("ds_read_b128 a[%c0:%c1], v84 offset:%c2" ::"i"(r), "i"(r + 3), "i"(X * 32768 + F * 1024));
}
template <int X, int F> DI void vread() {
  constexpr int r = 192 + 4 * (F & 15);
  asm volatile("ds_read_b128 a[%c0:%c1], v85 offset:%c2" ::"i"(r), "i"(r + 3), "i"(X * 32768 + (F & 1) * 16384 + (F >> 1) * 1024));
}
template <int X, int... Fs> DI void kreads(iseq<Fs...>) { (kread<X, Fs>(), ...); }
template <int X, int J> DI void dma_m0() {
  if constexpr (J == 0) asm volatile("s_mov_b32 m0, s%c0" ::"i"(78 + X));
  else if constexpr (J == 4) asm volatile("s_or_b32 m0, s%c0, 0x1000" ::"i"(78 + X));
  else if constexpr (J == 8) asm volatile("s_bitset0_b32 m0, 16");
  else if constexpr (J == 12) asm volatile("s_bitset0_b32 m0, 12");
}
template <int J> DI void dma_at() {
  if constexpr (J < 8)
    asm volatile("buffer_load_dwordx4 v%c0, s[88:91], s96 offen offset:%1 sc1 lds" ::"i"(86 + (J >= 4)), "i"((J & 3) * 1024) : "memory");
  else
    asm volatile("buffer_load_dwordx4 v%c0, s[92:95], s98 offen offset:%1 sc1 lds" ::"i"(r_dk(J - 8)), "i"((J & 3) * 1024) : "memory");
}
template <int Q> DI void dma4k() {
  asm volatile("s_add_u32 m0, s79, %0\n\ts_add_u32 s48, s98, %1\n\tbuffer_load_dwordx4 v%c2, s[88:91], s48 offen sc1 lds\n\t"
               "buffer_load_dwordx4 v%c2, s[88:91], s48 offen offset:1024 sc1 lds\n\tbuffer_load_dwordx4 v%c2, s[88:91], s48 offen offset:2048 sc1 lds\n\t"
               "buffer_load_dwordx4 v%c2, s[88:91], s48 offen offset:3072 sc1 lds"
               ::"i"((Q >> 1) * 32768 + (Q & 1) * 4096), "i"((Q >> 1) * 32768), "i"(86 + (Q & 1)) : "memory", "scc");
}
template <int Q> DI void acc_zero2() {
  asm volatile("v_accvgpr_write_b32 a[%c0], 0\n\tv_accvgpr_write_b32 a[%c1], 0" ::"i"(2 * Q), "i"(2 * Q + 1));
}
struct SMS { int ex[128], op0[128], nop[128], last; };
constexpr SMS sm_sched() {
  SMS z{};
  int et[32] = {}, e = 0;
  for (int t = 0; t < 128; ++t) {
    const int g = t & 15;
    const bool x = t < 48 ? (g >= 8 && (g & 1)) : (g >= 8 && g != 10 && g != 13 && g != 15);
    z.ex[t] = x && e < 32 ? e : -1;
    if (z.ex[t] >= 0) et[e++] = t;
  }
  int o = 0;
  for (int t = 0; t < 128; ++t) {
    const int g = t & 15;
    int cap = g < 8 ? (g == 0 ? 0 : 1) : (z.ex[t] >= 0 || t < 48 ? 0 : 2);
    z.op0[t] = o; z.nop[t] = 0;
    while (cap > 0 && o < 48 && et[2 * (o / 3) + (o % 3 == 0 ? 0 : 1)] + 2 <= t) {
      const int el = 2 * (o / 3) + o % 3;
      if (!(o % 3 < 2 && (el == 0 || el == 4))) --cap;
      ++o; ++z.nop[t]; z.last = t;
    }
  }
  return z;
}
constexpr SMS SM = sm_sched();
static_assert(SM.last <= 115);
constexpr int p_reg(int Y, int k) { return R_P + 16 * Y + 4 * ((k >> 3) * 2 + ((k >> 1) & 1)) + ((k >> 2) & 1) * 2 + (k & 1); }
template <int Y, int O> DI void sm_op() {
  constexpr int k = O / 3, e = 2 * k + O % 3, ts = R_TS + ((e >> 2) & 1);
  if constexpr (O % 3 == 2) asm volatile("v_cvt_pk_bf16_f32 v%c0, v%c1, v%c2" ::"i"(p_reg(Y, k)), "i"(R_E + ((2 * k) & 15)), "i"(R_E + ((2 * k + 1) & 15)));
  else if constexpr (e == 1 || e == 5) asm volatile("v_add_f32_e32 v%c0, v%c1, v%c2" ::"i"(ts), "i"(R_E + e - 1), "i"(R_E + e));
  else if constexpr (e != 0 && e != 4) asm volatile("v_add_f32_e32 v%c0, v%c0, v%c1" ::"i"(ts), "i"(R_E + (e & 15)));
}
template <int Y, int T> DI void sm_gap(AC& c) {
  if constexpr (SM.ex[T] >= 0) asm volatile("v_exp_f32 v%c0, v%c1" ::"i"(R_E + (SM.ex[T] & 15)), "i"(R_S + 32 * Y + SM.ex[T]));
  if constexpr (SM.nop[T] > 0) sm_op<Y, SM.op0[T]>();
  if constexpr (SM.nop[T] > 1) sm_op<Y, SM.op0[T] + 1>();
  if constexpr (SM.nop[T] > 2) sm_op<Y, SM.op0[T] + 2>();
  if constexpr (T == SM.last + 1) asm volatile("v_add_f32_e32 v%c0, v%c1, v%c2" ::"i"(R_TMP), "i"(R_TS), "i"(R_TS + 1));
  if constexpr (T == SM.last + 2) asm volatile("v_cmp_nle_f32_e64 %0, v%c1, v%c2" : "=s"(c.redo) : "i"(R_TMP), "i"(R_THR));
}
#define W_A0 1
template <int X, int MODE, int G> DI void gapA(AC& c) {
  constexpr bool FIRST = MODE == 1 || MODE == 2, NOQK = MODE == 3;
  constexpr int Xp = X ^ 1, kb = G >> 4, g = G & 15, qb = g >> 3, s = g & 7;
  if constexpr (NOQK && G < 48 && G % 3 == 0) {
    constexpr int f = G / 3;
    asm volatile("buffer_load_dwordx4 a[%c0:%c1], v122, s[88:91], s75 offen offset:%2 sc1" ::"i"(128 + 4 * f), "i"(131 + 4 * f), "i"((f & 3) * 1024) : "memory");
    if constexpr ((f & 3) == 3) asm volatile("s_addk_i32 s75, 4096");
  }
  if constexpr (G == 48) asm volatile("s_waitcnt vmcnt(%0) lgkmcnt(0)\n\ts_barrier" ::"i"(NOQK ? 16 : MODE == 2 ? 12 : MODE == 1 ? W_A0 : 0) : "memory");
  else if constexpr (g == 0 && !NOQK) asm volatile("s_waitcnt lgkmcnt(0)");
  if constexpr (!NOQK) {
    constexpr int r = 192 + 4 * ((kb & 1) * 8 + s), q = 128 + 4 * g;
    constexpr int d = R_S + 32 * X + 4 * (kb * 2 + qb), ci = s == 0 ? R_CI + 4 * (kb * 2 + qb) : d;
    if constexpr (FIRST && s == 0)
      asm volatile("v_mfma_f32_16x16x32_bf16 v[%c0:%c1], a[%c2:%c3], a[%c4:%c5], 0" ::"i"(d), "i"(d + 3), "i"(r), "i"(r + 3), "i"(q), "i"(q + 3));
    else
      asm volatile("v_mfma_f32_16x16x32_bf16 v[%c0:%c1], a[%c2:%c3], a[%c4:%c5], v[%c6:%c7]" ::"i"(d), "i"(d + 3), "i"(r), "i"(r + 3), "i"(q), "i"(q + 3),
                   "i"(ci), "i"(ci + 3));
    if constexpr (g < 8 && kb < 3) kread<X, (kb + 1) * 8 + g>();
    if constexpr (G == 54) asm volatile("s_add_u32 s96, s97, 0x1000000");
    if constexpr (G == 55) dma_m0<X, 0>();
    if constexpr (G >= 56 && !(G & 1)) dma_at<(G - 56) / 2>();
    if constexpr (G == 57) asm volatile("s_mov_b32 s97, s98\n\ts_lshr_b32 s100, s100, 1\n\ts_add_u32 s98, s98, s99");
    if constexpr (G == 59 && !FIRST)
      asm volatile("s_and_b32 s101, s100, 1\n\ts_or_b32 s101, s101, %1\n\ts_or_b32 %0, s101, %2" : "=s"(c.rare) : "s"((unsigned)c.redo), "s"((unsigned)(c.redo >> 32)));
    if constexpr (G == 59 && FIRST) asm volatile("s_and_b32 %0, s100, 1" : "=s"(c.rare));
  } else if constexpr (SM.nop[64 + G] > 0) asm volatile("s_nop 0");
  if constexpr (!FIRST && g < 8 && kb == 3) vread<Xp, g>();
  if constexpr (MODE == 2 && (G & 3) == 1) d_store<16 + G / 4>();
  if constexpr (FIRST) acc_zero2<G>();
  else sm_gap<Xp, 64 + G>(c);
  GAP();
}
template <int R> DI void rescale1(float alpha) {
  float t;
  asm volatile("v_accvgpr_read_b32 %0, a[%c2]\n\tv_mul_f32 %0, %0, %1\n\tv_accvgpr_write_b32 a[%c2], %0" : "=&v"(t) : "v"(alpha), "i"(R));
}
template <int... Rs> DI void rescale_all(const float (&alpha)[2], iseq<Rs...>) { (rescale1<Rs>(alpha[(Rs >> 2) & 1]), ...); }
template <bool MAX> DI float row_reduce(float v) {
  auto p = __builtin_amdgcn_permlane32_swap(__float_as_uint(v), __float_as_uint(v), false, false);
  v = MAX ? fmaxf(__uint_as_float(p[0]), __uint_as_float(p[1])) : __uint_as_float(p[0]) + __uint_as_float(p[1]);
  p = __builtin_amdgcn_permlane16_swap(__float_as_uint(v), __float_as_uint(v), false, false);
  return MAX ? fmaxf(__uint_as_float(p[0]), __uint_as_float(p[1])) : __uint_as_float(p[0]) + __uint_as_float(p[1]);
}
template <int Y, int QB> DI float row_max() {
  constexpr int r = R_S + 32 * Y + 4 * QB;
  float mx;
  asm volatile("v_max_f32 %0, v%c1, v%c2\n\tv_max3_f32 %0, %0, v%c3, v%c4" : "=&v"(mx) : "i"(r), "i"(r + 1), "i"(r + 2), "i"(r + 3));
  static_for([&](auto K) {
    constexpr int q = r + 8 * (K + 1);
    float t = mx;
    asm volatile("v_max3_f32 %0, %0, v%c1, v%c2\n\tv_max3_f32 %0, %0, v%c3, v%c4" : "+v"(t) : "i"(q), "i"(q + 1), "i"(q + 2), "i"(q + 3));
    mx = t;
  }, make_iseq<3>{});
  return row_reduce<true>(mx);
}
template <int R> DI void v_sub(float d) { asm volatile("v_sub_f32 v%c0, v%c0, %1" ::"i"(R), "v"(d)); }
template <int B> DI void s_shift(const float (&d)[2]) { static_for([&](auto E) { v_sub<B + E>(d[(E >> 2) & 1]); }, make_iseq<32>{}); }
DI void m_shift(const float (&d)[2]) {
  s_shift<R_CI>(d);
  v_sub<R_M>(-d[0]); v_sub<R_M + 1>(-d[1]);
}
DI void set_mask0(int wid) {
  const int mr = wid * 32 + __float_as_int(vget<89>());
  static_for([&](auto I) {
    constexpr int e = I, k = (e >> 3) * 16 - ((e >> 2) & 1) * 16 + (e & 3);
    const float x = vget<R_S + e>();
    vset<R_S + e>(k > mr ? -INFINITY : x);
  }, make_iseq<32>{});
}
template <int X> DI void sm_redo() {
  constexpr int Xp = X ^ 1;
  float d[2], dt[2], alpha[2], ts[2] = {0.f, 0.f};
  static_for([&](auto Q) {
    constexpr int qb = Q;
    const float pd = vget<R_PD + qb>();
    d[qb] = fmaxf(row_max<Xp, qb>() - pd, 0.f);
    dt[qb] = d[qb] + pd;
    vset<R_PD + qb>(d[qb]);
    alpha[qb] = __builtin_amdgcn_exp2f(-d[qb]);
    vset<R_RS + qb>(vget<R_RS + qb>() * alpha[qb]);
  }, make_iseq<2>{});
  vset<R_THR>(d[0] > 0.f || d[1] > 0.f ? -1.f : 4096.f);
  m_shift(d);
  static_for([&](auto K) {
    constexpr int k = K, qb = (k >> 1) & 1;
    const float e0 = __builtin_amdgcn_exp2f(vget<R_S + 32 * Xp + 2 * k>() - dt[qb]), e1 = __builtin_amdgcn_exp2f(vget<R_S + 32 * Xp + 2 * k + 1>() - dt[qb]);
    ts[qb] += e0; ts[qb] += e1;
    vset<p_reg(Xp, k)>(__uint_as_float(pk2(e0, e1)));
  }, make_iseq<16>{});
  vset<R_TS>(ts[0]); vset<R_TS + 1>(ts[1]);
  rescale_all(alpha, make_iseq<128>{});
  asm volatile("s_nop 1" ::: "memory");
  GAP();
}
template <int X, int MODE, int... Gs> DI void phaseA_(AC& c, iseq<Gs...>) { (gapA<X, MODE, Gs>(c), ...); }
template <int X, int MODE> DI void phaseA(AC& c) { phaseA_<X, MODE>(c, make_iseq<64>{}); }

#define PG 50
template <int X, bool SMX, int G> DI void gapB(AC& c) {
  constexpr int Xp = X ^ 1, blk = G >> 4, g = G & 15;
  if constexpr (g == 0) asm volatile("s_waitcnt lgkmcnt(0)");
  if constexpr (SMX && g == 7 && blk < 3) dma_m0<X, blk * 4 + 4>();
  constexpr int qb = (g >> 1) & 1, s = g & 1, r = 192 + 4 * ((blk & 1) * 8 + (g >> 2) * 2 + s), o = 4 * ((blk * 4 + (g >> 2)) * 2 + qb);
  constexpr int p = R_P + 16 * Xp + 4 * (s * 2 + qb);
  asm volatile("v_mfma_f32_16x16x32_bf16 a[%c0:%c1], a[%c2:%c3], v[%c4:%c5], a[%c0:%c1]" ::"i"(o), "i"(o + 3), "i"(r), "i"(r + 3), "i"(p), "i"(p + 3));
  if constexpr (SMX && (G == 1 || G == 2)) asm volatile("v_add_f32_e32 v%c0, v%c0, v%c1" ::"i"(R_RS + G - 1), "i"(R_TS + G - 1));
  if constexpr (g < 8) {
    if constexpr (blk < 3) vread<Xp, (blk + 1) * 8 + g>();
    else if constexpr (SMX) kread<Xp, g>();
  }
  if constexpr (SMX) {
    if constexpr (g >= 8 && !(g & 1) && blk < 3) dma_at<blk * 4 + 4 + (g - 8) / 2>();
    if constexpr (G == 3) asm volatile("s_lshr_b32 s77, s77, 1");
    if constexpr (G == 4) asm volatile("s_bitcmp1_b32 s77, 0");
    if constexpr (G == 5) asm volatile("s_cselect_b64 s[52:53], s[62:63], 0");
    if constexpr (G == 48) asm volatile(FL_ARM("s[52:53]"));
    if constexpr (G == PG) asm volatile(FL_POLL ::: "memory");
    sm_gap<X, G>(c);
  }
  GAP();
}
template <int X, int... Gs> DI void phaseB_(AC& c, iseq<Gs...>) { (gapB<X, true, Gs>(c), ...); }
template <int X> DI void phaseB(AC& c) { phaseB_<X>(c, make_iseq<64>{}); }
constexpr int sm_nex() { int n = 0; for (int t = 0; t < 64; ++t) n += SM.ex[t] >= 0; return n; }
DI void phaseB0(unsigned mode, int wid) {
  dma_m0<0, 4>();
  kreads<1>(make_iseq<8>{});
  dma_at<4>();
  asm volatile("s_nop 3");
  float d[2];
  if (mode & 2) {
    d[0] = vget<122>(); d[1] = vget<123>();
    dma_at<5>();
  } else {
    if (__builtin_expect(mode & 4, 0)) set_mask0(wid);
    d[0] = row_max<0, 0>(); GAP(); dma_at<5>();
    d[1] = row_max<0, 1>();
#pragma unroll
    for (int qb = 0; qb < 2; ++qb) if (!(d[qb] > -3.0e38f)) d[qb] = 0.f;
  }
  GAP();
  vset<R_M>(d[0]); vset<R_M + 1>(d[1]);
  dma_at<6>();
  const f32x2c nd[2] = {{-d[0], -d[0]}, {-d[1], -d[1]}};
  static_for([&](auto E) {
    constexpr int e = E;
    const f32x2c v = nd[(e >> 1) & 1];
    asm volatile("v_pk_add_f32 v[%c0:%c1], v[%c0:%c1], %4\n\tv_mov_b64 v[%c2:%c3], %4" ::"i"(R_S + 2 * e), "i"(R_S + 2 * e + 1), "i"(R_CI + 2 * e), "i"(R_CI + 2 * e + 1), "v"(v));
    if constexpr (e == 3) dma_at<7>();
    if constexpr (e == 6) dma_m0<0, 8>();
    if constexpr (e == 7 || e == 11 || e == 15) dma_at<8 + (e - 7) / 4>();
  }, make_iseq<16>{});
  static_assert(sm_nex() >= 16 && SM.op0[64] >= 3 * ((sm_nex() - 15) / 2) && SM.op0[64] <= 24);
  static_for([&](auto E) {
    constexpr int e = E;
    asm volatile("v_exp_f32 v%c0, v%c1" ::"i"(R_E + e), "i"(R_S + e));
    if constexpr (e == 3) dma_at<11>();
    if constexpr (e == 6) dma_m0<0, 12>();
    if constexpr (e == 7 || e == 11 || e == 15) dma_at<12 + (e - 7) / 4>();
  }, make_iseq<16>{});
  static_for([&](auto O) { sm_op<0, O>(); }, make_iseq<SM.op0[64]>{});
  dma_at<15>();
  static_for([&](auto E) { asm volatile("v_exp_f32 v%c0, v%c1" ::"i"(R_E + (int)E), "i"(R_S + 16 + (int)E)); }, make_iseq<sm_nex() - 16>{});
  asm volatile("buffer_store_dwordx2 v[%c0:%c1], v88, s[88:91], s76 offen sc1\n\ts_bitcmp1_b32 s77, 0\n\ts_cselect_b64 s[52:53], s[62:63], 0\n\t"
               FL_ARM("s[52:53]") "\n\t" FL_POLL ::"i"(R_M), "i"(R_M + 1) : "memory", "scc");
  GAP();
}
constexpr int T_ZO = 8;
#define NINF2(R) "v_mov_b64 v[" #R ":" #R "+1], v[122:123]\n\t"
#define NINF4(R) NINF2(R) NINF2(R + 2)
#define TRI(R) "v_cndmask_b32_e64 v[" #R "], v[" #R "], v122, s[48:49]\n\tv_cndmask_b32_e64 v[" #R "+1], v[" #R "+1], v122, s[50:51]\n\t" \
               "v_cndmask_b32_e64 v[" #R "+2], v[" #R "+2], v122, s[52:53]\n\tv_cndmask_b32_e64 v[" #R "+3], v[" #R "+3], v122, s[54:55]\n\t"
DI void ev_code() {
  asm volatile(
    "s_getpc_b64 s[82:83]\n\ts_add_u32 s82, s82, 12\n\ts_addc_u32 s83, s83, 0\n\ts_branch 9f\n\t"
    "s_bitcmp0_b32 s58, s56\n\ts_cbranch_scc1 1f\n\t" FL_SET("s70") "\n\t" FL_SET("s71") "\n"
    "1:\n\ts_bitcmp0_b32 s57, s56\n\ts_cbranch_scc1 2f\n\t" FL_CHECK("s[62:63]") "\n\t"
    "s_add_u32 s98, s98, s68\n\ts_mov_b32 s68, s69\n\ts_mov_b32 s69, 0\n\ts_mov_b64 s[62:63], s[64:65]\n\ts_mov_b64 s[64:65], s[66:67]\n"
    "2:\n\ts_bitcmp0_b32 s59, s56\n\ts_cbranch_scc1 3f\n\ts_mov_b32 s98, s72\n\ts_bitcmp0_b32 s73, 0\n\ts_cbranch_scc1 3f\n\t"
    "v_lshrrev_b32 v90, 7, v86\n\tv_and_b32 v91, 6, v90\n\tv_lshlrev_b32 v91, 4, v91\n\tv_and_b32 v92, 0x70, v86\n\tv_xor_b32 v91, v91, v92\n\t"
    "s_movk_i32 s48, 896\n\tv_mad_u32_u24 v94, v90, s48, v91\n\ts_mov_b64 s[92:93], s[80:81]\n\ts_mov_b32 s94, 229376\n\ts_movk_i32 s99, 128\n\t"
    "v_add_u32 v90, 28672, v94\n\tv_add_u32 v91, 34816, v94\n\tv_add_u32 v92, 40960, v94\n\tv_add_u32 v93, 47104, v94\n\t"
    "v_add_u32 v95, 6144, v94\n\tv_add_u32 v126, 12288, v94\n\tv_add_u32 v127, 18432, v94\n"
    "3:\n\ts_bitcmp0_b32 s60, s56\n\ts_cbranch_scc1 4f\n\t"
    "v_readlane_b32 s54, v78, %0\n\ts_add_u32 m0, s78, 0x10000\n\t"
    "v_lshlrev_b32 v121, 9, v84\n\tv_and_b32 v121, 0x38000, v121\n\tv_lshrrev_b32 v122, 9, v84\n\tv_lshl_add_u32 v121, v122, 12, v121\n\t"
    "v_lshrrev_b32 v122, 3, v84\n\tv_xor_b32 v122, v122, v84\n\tv_and_or_b32 v121, v122, 48, v121\n\t"
    "buffer_load_dwordx4 v121, s[88:91], s54 offen sc1 lds\n\ts_addk_i32 s54, 7168\n\t"
    "buffer_load_dwordx4 v121, s[88:91], s54 offen offset:1024 sc1 lds\n\ts_addk_i32 s54, 7168\n\t"
    "buffer_load_dwordx4 v121, s[88:91], s54 offen offset:2048 sc1 lds\n\ts_addk_i32 s54, 7168\n\t"
    "buffer_load_dwordx4 v121, s[88:91], s54 offen offset:3072 sc1 lds\n"
    "4:\n\ts_bitcmp0_b32 s61, s56\n\ts_cbranch_scc1 6f\n\t"
    "s_sub_u32 s74, s74, 64\n\ts_cmp_ge_i32 s74, 0\n\ts_cbranch_scc1 6f\n\t"
    "v_cmp_gt_i32 s[48:49], 0, v89\n\tv_cmp_gt_i32 s[50:51], 1, v89\n\tv_cmp_gt_i32 s[52:53], 2, v89\n\tv_cmp_gt_i32 s[54:55], 3, v89\n\t"
    "v_mov_b32 v122, 0xff800000\n\tv_mov_b32 v123, 0xff800000\n\t" NINF4(248)
    "s_cmp_eq_i32 s74, -32\n\ts_cbranch_scc0 5f\n\t" TRI(240) TRI(252) "s_setpc_b64 s[86:87]\n"
    "5:\n\t" NINF4(240) NINF4(244) NINF4(252) NINF4(232) TRI(224) TRI(236)
    "s_cmp_eq_i32 s74, -64\n\ts_cbranch_scc1 6f\n\t" NINF4(224) NINF4(228) NINF4(236)
    "6:\n\ts_setpc_b64 s[86:87]\n9:"
    ::"i"(T_ZO) : "scc");
}
DI void ev_call(int i) { asm volatile("s_mov_b32 s56, %0\n\ts_swappc_b64 s[86:87], s[82:83]" ::"s"(i) : "vcc", "scc", "memory"); GAP(); }

#define ACLOB_ALL "a0","a1","a2","a3","a4","a5","a6","a7","a8","a9","a10","a11","a12","a13","a14","a15","a16","a17","a18","a19","a20","a21","a22","a23","a24","a25","a26","a27","a28","a29","a30","a31","a32","a33","a34","a35","a36","a37","a38","a39","a40","a41","a42","a43","a44","a45","a46","a47","a48","a49","a50","a51","a52","a53","a54","a55","a56","a57","a58","a59","a60","a61","a62","a63","a64","a65","a66","a67","a68","a69","a70","a71","a72","a73","a74","a75","a76","a77","a78","a79","a80","a81","a82","a83","a84","a85","a86","a87","a88","a89","a90","a91","a92","a93","a94","a95","a96","a97","a98","a99","a100","a101","a102","a103","a104","a105","a106","a107","a108","a109","a110","a111","a112","a113","a114","a115","a116","a117","a118","a119","a120","a121","a122","a123","a124","a125","a126","a127","a128","a129","a130","a131","a132","a133","a134","a135","a136","a137","a138","a139","a140","a141","a142","a143","a144","a145","a146","a147","a148","a149","a150","a151","a152","a153","a154","a155","a156","a157","a158","a159","a160","a161","a162","a163","a164","a165","a166","a167","a168","a169","a170","a171","a172","a173","a174","a175","a176","a177","a178","a179","a180","a181","a182","a183","a184","a185","a186","a187","a188","a189","a190","a191","a192","a193","a194","a195","a196","a197","a198","a199","a200","a201","a202","a203","a204","a205","a206","a207","a208","a209","a210","a211","a212","a213","a214","a215","a216","a217","a218","a219","a220","a221","a222","a223","a224","a225","a226","a227","a228","a229","a230","a231","a232","a233","a234","a235","a236","a237","a238","a239","a240","a241","a242","a243","a244","a245","a246","a247","a248","a249","a250","a251","a252","a253","a254","a255"

struct EP { float fa[2], fb[2]; };
template <int I, int E, int MODE> DI void epi_pair(const EP& p) {
  constexpr int qb = I & 1, r = 16 * (I >> 1) + 4 * qb + (E >> 1) * 8 + (E & 1) * 2, t = 96 + 2 * E, w = 128 + 4 * I + E;
  if constexpr (MODE == 2) asm volatile("v_accvgpr_read_b32 v106, a[%c0]" ::"i"(w));
  asm volatile("v_accvgpr_read_b32 v%c0, a[%c2]\n\tv_accvgpr_read_b32 v%c1, a[%c3]" ::"i"(t), "i"(t + 1), "i"(r), "i"(r + 1));
  if constexpr (MODE) asm volatile("v_mul_f32 v%c0, v%c0, %2\n\tv_mul_f32 v%c1, v%c1, %2" ::"i"(t), "i"(t + 1), "v"(p.fa[qb]));
  if constexpr (MODE == 2)
    asm volatile("v_lshlrev_b32 v104, 16, v106\n\tv_and_b32 v105, 0xffff0000, v106\n\tv_fmac_f32 v%c0, v104, %2\n\tv_fmac_f32 v%c1, v105, %2"
                 ::"i"(t), "i"(t + 1), "v"(p.fb[qb]));
  asm volatile("v_cvt_pk_bf16_f32 v%c0, v%c1, v%c2" ::"i"(w), "i"(t), "i"(t + 1));
}
template <int I> DI void epi_store() {
  asm volatile("buffer_store_dwordx4 v[%c0:%c1], v84, s[88:91], s101 offen offset:%2 sc1" ::"i"(128 + 4 * I), "i"(131 + 4 * I), "i"((I & 3) * 1024) : "memory");
  if constexpr ((I & 3) == 3) asm volatile("s_addk_i32 s101, 4096");
}
constexpr int R_TB = 107, R_VS = 111;
template <int HF, int R> DI void p_read() {
  constexpr int mb = R >> 4, r = 64 * HF + 2 * R;
  asm volatile("ds_read_b64_tr_b16 a[%c1:%c2], v%c0 offset:%c3" ::"i"(R_TB + 2 * HF + mb), "i"(r), "i"(r + 1), "i"((R & 15) * 2048));
}
template <int QB, int E> DI void p_cvt() {
  asm volatile("v_cvt_pk_bf16_f32 v%c0, v%c1, v%c2" ::"i"(208 + 4 * QB + E), "i"(192 + 8 * QB + 2 * E), "i"(193 + 8 * QB + 2 * E));
}
template <int QB, int PR> DI void p_store() {
  asm volatile("buffer_store_dwordx4 v[%c0:%c1], v%c2, s[88:91], s96 offen offset:%3 sc1" ::"i"(208 + 4 * QB), "i"(211 + 4 * QB), "i"(R_VS + QB),
               "i"(PR * 128) : "memory");
}
constexpr int p_dma(int HF, int G) { return G == 3 && !HF ? 0 : G == 11 ? 1 - HF : G == 15 ? 2 - HF : G == 19 ? 3 - HF : G == 27 && HF ? 3 : -1; }
template <int HF, int G> DI void p_gap() {
  constexpr int ch = G >> 3, t = G & 7, a = 64 * HF + 32 * (ch & 1) + 4 * t, b = 128 + 4 * (2 * t + (ch >> 1)), d = 192 + 4 * ch;
  if constexpr (t == 0)
    asm volatile("v_mfma_f32_16x16x32_bf16 v[%c0:%c1], a[%c2:%c3], v[%c4:%c5], 0" ::"i"(d), "i"(d + 3), "i"(a), "i"(a + 3), "i"(b), "i"(b + 3));
  else
    asm volatile("v_mfma_f32_16x16x32_bf16 v[%c0:%c1], a[%c2:%c3], v[%c4:%c5], v[%c0:%c1]" ::"i"(d), "i"(d + 3), "i"(a), "i"(a + 3), "i"(b), "i"(b + 3));
  p_read<HF ^ 1, G>();
  if constexpr (G == 0) asm volatile("v_xor_b32 v%c0, s100, v%c0" ::"i"(R_TB + 2 * HF + 1));
  if constexpr (G == 17) asm volatile("v_xor_b32 v%c0, s100, v%c0" ::"i"(R_TB + 2 * (HF ^ 1)));
  if constexpr (G >= 4 && G <= 8 && G != 7) p_cvt<1, (G < 7 ? G - 4 : 3)>();
  if constexpr (G == 24 || (G == 7 && HF == 0)) asm volatile("s_waitcnt lgkmcnt(8)");
  if constexpr (G == 7 && HF == 1) asm volatile("s_waitcnt vmcnt(13) lgkmcnt(8)\n\ts_barrier" ::: "memory");
  if constexpr (G == 10) p_store<1, HF ^ 1>();
  if constexpr (G >= 20 && G < 24) p_cvt<0, G - 20>();
  if constexpr (G == 25) p_store<0, HF>();
  if constexpr (p_dma(HF, G) >= 0) dma_at<(HF ? 8 : 12) + p_dma(HF, G)>();
  if constexpr (HF == 0 && G == 2) asm volatile("s_bitset0_b32 m0, 12");
  if constexpr (HF == 0 && G == 13) asm volatile("s_add_u32 s96, s96, 0x40000");
  if constexpr (HF == 0 && G == 26) asm volatile("s_add_u32 s98, s98, 128");
  if constexpr (HF == 0 && G == 27) asm volatile("s_cmp_lt_u32 s98, 0x380");
  if constexpr (HF == 0 && G == 28) asm volatile("s_cselect_b32 s99, s99, 0x31000");
  if constexpr (HF == 1 && G == 1) asm volatile("s_xor_b32 s100, s100, 0x10000");
  if constexpr (HF == 1 && G == 2) asm volatile("s_xor_b32 s97, s97, s100");
  if constexpr (HF == 1 && G == 9) asm volatile("s_or_b32 m0, s97, s99");
  if constexpr (HF == 1 && G == 30) asm volatile("s_cmp_lt_u32 s98, 0x500");
  GAP();
}
template <int HF, int... Gs> DI void p_half(iseq<Gs...>) { (p_gap<HF, Gs>(), ...); }
constexpr int f_dma(int G) { return G == 2 ? 0 : G == 5 ? 1 : G == 8 ? 2 : G >= 12 && G <= 36 && G % 6 == 0 ? 1 + G / 6 : G >= 50 && G <= 68 && G % 6 == 2 ? G / 6 : -1; }
constexpr int f_ndma(int G) { int n = 0; for (int g = 0; g < G; ++g) n += f_dma(g) >= 0; return n; }
static_assert(f_ndma(74) == 12);
template <int MODE, int G> DI void gapF(AC& c, const EP& p) {
  if constexpr (MODE && G == 48) asm volatile("s_waitcnt lgkmcnt(0)\n\ts_barrier" ::: "memory");
  if constexpr (G < 64) gapB<0, false, G>(c);
  if constexpr (MODE == 2 && G >= 10 && (G - 10) % 16 == 0) asm volatile("s_waitcnt vmcnt(%0)" ::"i"(12 - (G - 10) / 4 + f_ndma(G)) : "memory");
  if constexpr (G >= 10) epi_pair<(G - 10) / 4, (G - 10) & 3, MODE>(p);
  if constexpr (MODE == 3 && G < 64 && (G & 1)) {
    constexpr int db = G >> 2, qb = (G >> 1) & 1, o = 4 * (db * 2 + qb), a = 224 + 4 * (db & 1), b = 128 + 4 * ((db >> 1) * 2 + qb);
    if constexpr ((G & 7) == 1) asm volatile("s_waitcnt vmcnt(%0)" ::"i"(14 - (G >> 2) + f_ndma(G)) : "memory");
    asm volatile("v_mfma_f32_16x16x32_bf16 a[%c0:%c1], v[%c2:%c3], a[%c4:%c5], a[%c0:%c1]" ::"i"(o), "i"(o + 3), "i"(a), "i"(a + 3), "i"(b), "i"(b + 3));
  }
  if constexpr (MODE == 0) {
    if constexpr (G >= 14 && ((G - 14) & 3) == 0) epi_store<(G - 14) / 4>();
    if constexpr (G == 73) epi_store<15>();
  } else {
    if constexpr (G == 0) asm volatile("s_or_b32 m0, s78, 0x1000\n\ts_movk_i32 s98, 256");
    if constexpr (G == 15) asm volatile("s_bitset0_b32 m0, 12");
    if constexpr (G == 48) asm volatile("s_or_b32 m0, s79, 0x1000\n\ts_movk_i32 s98, 384");
    if constexpr (f_dma(G) >= 0) dma_at<8 + (f_dma(G) & 7)>();
    if constexpr (G == 73) asm volatile("s_waitcnt vmcnt(4) lgkmcnt(0)" ::: "memory");
    if constexpr (G >= 48 && G < 64) { p_read<0, 2 * (G - 48)>(); p_read<0, 2 * (G - 48) + 1>(); }
  }
  GAP();
}
template <int MODE, int... Gs> DI void phaseF(AC& c, const EP& p, iseq<Gs...>) { (gapF<MODE, Gs>(c, p), ...); }
template <int U, int K> DI void r_load(unsigned vo, unsigned so) {
  asm volatile("buffer_load_dwordx4 a[%c0:%c1], %2, s[88:91], %3 offen offset:%4 sc1" ::"i"(64 * U + 4 * K), "i"(64 * U + 4 * K + 3), "v"(vo),
               "s"(so + (K >> 2) * 4096), "i"((K & 1) * 512 + ((K >> 1) & 1) * 2048) : "memory");
}
template <int U, int K> DI void r_sum() {
  constexpr int d = 100 + 4 * (K & 1), b = 64 * U + 4 * K;
  if constexpr (K < 2) asm volatile("v_mfma_f32_16x16x32_bf16 v[%c0:%c1], v[96:99], a[%c2:%c3], 0" ::"i"(d), "i"(d + 3), "i"(b), "i"(b + 3));
  else asm volatile("v_mfma_f32_16x16x32_bf16 v[%c0:%c1], v[96:99], a[%c2:%c3], v[%c0:%c1]" ::"i"(d), "i"(d + 3), "i"(b), "i"(b + 3));
}
template <int U> DI void r_fin(unsigned vo, __amdgpu_buffer_rsrc_t srd, unsigned so) {
  asm volatile("" : "+s"(srd), "+s"(so));
  static_for([&](auto K) { r_sum<U, K>(); }, make_iseq<16>{});
  asm volatile("s_nop 7");
  static_for([&](auto E) { asm volatile("v_cvt_pk_bf16_f32 v%c0, v%c1, v%c2" ::"i"(116 + 4 * U + E), "i"(100 + 2 * E), "i"(101 + 2 * E)); }, make_iseq<4>{});
  asm volatile("buffer_store_dwordx4 v[%c3:%c4], %0, %1, %2 offen" ::"v"(vo), "s"(srd), "s"(so), "i"(116 + 4 * U), "i"(119 + 4 * U) : "memory");
}

#define CMAX 12
#define PREPX false
#define NYT 16
static_assert(NYT <= 16 && NYT >= 12 && !(NYT & 1));
enum { T_Q0, T_K0, T_OQ, T_OG, T_FPREP, T_FQ, T_FG, T_FS,   T_NPRE = 10, T_NST = 12, T_TAB = 14, T_SLAB = 16, T_PART, T_IT = 18, T_ITN = 23 };
struct TRow { unsigned v[64]; };
constexpr u64 kvmask(int m0, int m1) { u64 n = 0; for (int m = m0; m <= m1; ++m) n |= 0x101ull << m; return n; }
constexpr u64 k_order(bool isY, int rr) {
  const int cls = (rr + (rr >> 2)) & 3;
  return !isY ? (cls == 0 ? 0xfedcb9875a463210ull : cls == 1 ? 0xefadc6982b574310ull : cls == 2 ? 0xfebad7693c285410ull : 0xefcba8764d392510ull)
              : (cls == 0 ? 0xfedcb987543a6210ull : cls == 1 ? 0xefadc698254b7310ull : cls == 2 ? 0xfebad769325c8410ull : 0xefcba876432d9510ull);
}
constexpr TRow t_row(int r, int wid, int j) {
  TRow t{};
  const int rr = r & 15, hsel = rr & 1, pj = rr >> 1, kv = pj & 1, h = j + 8 * hsel, u = hsel * 8 + j;
  const bool isY = r >= 16, solo = pj == 7;
  const int mt = (rr >> 2) + (isY ? 4 : 0), g = j * 128, NY = solo ? 18 : NYT;
  const int nx0 = 32 - 2 * pj - NY, cx = nx0 < CMAX ? nx0 : CMAX, ex = nx0 - cx, qbF = isY ? 15 - pj : pj;
  const unsigned oQ = (unsigned)h << 20, oK = oQ + O_K, oP = O_P + (h * 8 + pj) * PSTR;
  if (wid == 4) {
    for (int l = 0; l < 64; ++l)
      t.v[l] = fo(l < 16 ? g + hsel * 16 + l : l < 24 ? g + 16 + hsel * 8 + l : l == 24 ? g + 48 + hsel * 8 + pj : l < 32 ? g + 64 + hsel * 8 + pj :
                  l < 48 ? 2304 + pj * 16 + l - 32 : 2048 + pj * 16 + l - 48);
    return t;
  }
  auto put64 = [&](int i, u64 x) { t.v[i] = (unsigned)x; t.v[i + 1] = (unsigned)(x >> 32); };
  t.v[T_Q0] = oQ + ((solo && !isY ? pj : 15 - pj) * 128 + wid * 32) * 512;
  t.v[T_K0] = oK + (isY ? cx : 0) * 32768;
  t.v[T_OQ] = oQ + pj * 131072 + (wid >> 1) * 65536 + (wid & 1) * 4096;
  t.v[T_OG] = oK + kv * TSB + mt * 131072 + (wid >> 1) * 65536 + (wid & 1) * (kv ? 8192 : 4096);
  t.v[T_FPREP] = fo(2048 + pj * 16 + u);
  t.v[T_FQ] = fo(g + 32 + hsel * 8 + pj);
  t.v[T_FG] = fo(g + hsel * 16 + kv * 8 + mt);
  t.v[T_FS] = fo(1024 + (qbF * 4 + wid) * 16 + j * 2 + hsel);
  t.v[T_ZO] = O_Z + pj * 64 * SEQ * 2 + (u * 4 + wid) * 64;
  const u64 own = 0x1ull << (kv * 8 + mt), needQ = isY ? 1ull << (16 + ((15 - pj) >> 1)) : (1ull << (16 + (pj >> 1))) | (1ull << (16 + ((15 - pj) >> 1)));
  put64(T_NPRE, isY ? needQ | (1ull << (cx >> 2)) | (1ull << ((cx + 1) >> 2)) : solo ? needQ : needQ | 0xffffull << 48);
  put64(T_NST, isY ? kvmask(cx >> 2, 3) | (solo ? 0 : 1ull << 25) : kvmask(0, solo ? 3 : 0));
  put64(T_TAB, k_order(isY, rr));
  t.v[T_SLAB] = O_S + qbF * 0x200000 + wid * 65536 + (j * 2 + hsel) * 1024;
  t.v[T_PART] = oP;
  for (int it = 0; it < 2; ++it) {
    unsigned* p = t.v + T_IT + it * T_ITN;
    const bool last = isY || it == 1;
    const int n = isY ? NY : it ? 2 * pj + 2 : nx0 < 2 ? 2 : nx0;
    const u64 need2 = solo ? 0 : kvmask(0, (2 * pj + 1) >> 2) | (0xffffull << 32);
    u64 q[3] = {0, 0, 0};
    unsigned jmp[2] = {0, 0};
    if (isY) {
      p[0] = (1u << (14 - cx)) | (solo ? 0 : 1u << (n - 2));
      q[0] = kvmask(4, (31 - 2 * pj) >> 2) & ~own; jmp[0] = ex * 32768; q[1] = solo ? 0 : 1ull << 24;
      p[1] = 2; p[13] = t.v[T_FG]; p[14] = fo(g + 96 + r);
    } else if (it == 0) {
      p[0] = 2u | (ex ? 1u << (cx - 2) : 0);
      q[0] = kvmask(0, cx > 0 ? (cx - 1) >> 2 : 0);
      if (ex) { q[1] = kvmask(4, 4 + ((ex - 1) >> 2)); jmp[1] = (16 - cx) * 32768; q[2] = need2; } else q[1] = need2;
      p[1] = 2; p[13] = fo(2304 + pj * 16 + u) + j * (8 * 4352 - 8); p[14] = fo(g + 64 + hsel * 8 + pj);
    } else {
      p[0] = p[1] = solo ? 0 : 1;
      q[0] = need2; p[13] = fo(g + 48 + hsel * 8 + pj); p[14] = fo(g + 96 + r);
    }
    p[2] = 1u << (n - 2);
    p[3] = isY || !it || solo ? 0 : n > 2 ? 1u << (n - 3) : 1u;
    p[4] = !last ? 0 : n > 2 ? 3u << (n - 3) : 1u << 31;
    for (int k = 0; k < 3; ++k) { p[5 + 2 * k] = (unsigned)q[k]; p[6 + 2 * k] = (unsigned)(q[k] >> 32); }
    p[11] = jmp[0]; p[12] = jmp[1];
    p[15] = oK;
    p[16] = (last ? 1 : 0) | (isY && !solo ? 2 : 0) | (n == 2 ? 4 : 0) | n << 8;
    p[17] = wid * 32 - (n == 2 ? 64 : 0);
    p[18] = oQ + (pj * 128 + wid * 32) * 512;
    p[19] = 0;
    p[20] = (p[0] | (last ? 0 : 1u << n)) >> 1;
    p[21] = oK + (isY ? cx : 0) * 32768;
    p[22] = ((p[0] | p[1] | p[2] | p[3] | p[4]) & 0x7fffffffu) << 1;
  }
  return t;
}
template <int I> constexpr TRow t_base = I < 160 ? t_row(I / 5, I % 5, 0) : [] {
  TRow a = t_row(0, (I - 160) * 4, 0), b = t_row(0, (I - 160) * 4, 1);
  for (int l = 0; l < 64; ++l) a.v[l] = b.v[l] - a.v[l];
  return a;
}();
template <int I> constexpr bool t_linear = [] {
  for (int j = 1; j < 8; ++j) {
    const TRow b = t_row(I / 5, I % 5, j);
    for (int l = 0; l < 64; ++l) if (b.v[l] != t_base<I>.v[l] + j * t_base<160 + (I % 5) / 4>.v[l]) return false;
  }
  return true;
}();
struct Tab { TRow row[162]; };
template <int... I> constexpr Tab t_make(iseq<I...>) {
  static_assert((t_linear<(I < 160 ? I : 0)> && ...));
  return Tab{{t_base<I>...}};
}
__device__ const Tab g_tab = t_make(make_iseq<162>{});

__global__ __launch_bounds__(256, 1) __attribute__((amdgpu_num_vgpr(78), amdgpu_num_sgpr(56))) void fused_kernel(const bf16* __restrict__ ckv, const bf16* __restrict__ wuk,
                                                       const bf16* __restrict__ wuv, const bf16* __restrict__ x,
                                                       const bf16* __restrict__ wq, bf16* Qb,
                                                       const bf16* __restrict__ ak, const bf16* __restrict__ av,
                                                       const bf16* __restrict__ ash, const bf16* __restrict__ assmk,
                                                       const bf16* __restrict__ assmD, const bf16* __restrict__ wo,
                                                       const bf16* __restrict__ aq, bf16* __restrict__ out) {
  extern __shared__ __attribute__((aligned(16))) char lds[];
  asm volatile("at_code_begin:" ::: ACLOB_ALL, "v255", "s101");
  const int tid = threadIdx.x, lane = tid & 63;
  const int wid = __builtin_amdgcn_readfirstlane(tid >> 6);
  const int b = blockIdx.x, j = b & 7, r = b >> 3, rr = r & 15, hsel = rr & 1, pj = rr >> 1, kv = pj & 1;
  const bool isY = r >= 16, solo = pj == 7, doP = isY != PREPX && !solo, doA = !isY && !solo;
  const int h = j + 8 * hsel, mt = (rr >> 2) + (isY ? 4 : 0), pu = (hsel * 8 + j) * 4 + wid;
  constexpr unsigned m0d = 131072 + 24576;
  const unsigned lA = isY ? 65536 : 0, lW = lA ^ 65536, po = isY ? 0 : 32768;
  const unsigned mp[4] = {lA + wid * 1024 + po, lW + wid * 1024 + po, lA + wid * 1024 + (po ^ 32768), lW + wid * 1024 + (po ^ 32768)};
  {
    const int lda = isY ? DA : DC;
    const unsigned ra = wid * 8 + (lane >> 3), sa = ((lane & 7) ^ (((wid & 1) * 4 + (lane >> 4)) & 7)) * 16;
    const unsigned vbA = ra * (lda * 2) + sa, vbB = (4 * wid + (lane >> 4)) * LDW * 2 + 16 * ((lane & 15) ^ (((lane >> 4) << 2) | wid));
    vsetu<92>(vbA); vsetu<93>(vbB); vsetu<90>(vbA + 128); vsetu<91>(vbB + 0x80000); vsetu<94>(ra * (DC * 2) + sa);
    static_for([&](auto J) {
      sset<64 + J>((J + 1) * 64 * lda);
      asm volatile("s_mov_b32 s%c0, %1" ::"i"(71 + J), "i"((((J + 1) >> 2) * 32 + 16 * ((J + 1) & 1)) * LDW * 2 + (((J + 1) >> 1) & 1) * 256));
    }, make_iseq<7>{});
    sset<78>(lA + wid * 1024); sset<79>(lW + wid * 1024);
    GAP();
  }
  asm volatile("" : "+s"(ckv), "+s"(wuk), "+s"(wuv), "+s"(x), "+s"(wq), "+s"(Qb), "+s"(ak), "+s"(av), "+s"(ash), "+s"(assmk),
                    "+s"(assmD), "+s"(wo), "+s"(aq), "+s"(out));
  KST(0, 0);
  bf16* xqT = (bf16*)((char*)Qb + O_X);
  const bf16* gA = ckv + (size_t)mt * 256 * DC;
  const bf16* gW = (kv ? wuv : wuk) + h * 256;
  unsigned dv = 0;
  {
    GM g;
    PR pr;
    if (doP) prep_load(pr, tid, pj, hsel * 8 + j, ak, av, ash);
    srd_set<56>(isY ? x + (size_t)pj * 256 * DA : gA, isY ? 256 * DA * 2 : 256 * DC * 2);
    srd_set<60>(isY ? wq + h * 256 : gW, isY ? (DA - 1) * LDW * 2 + 512 : (DC - 1) * LDW * 2 + 512);
    g_pro<0>(mp, make_iseq<12>{});
    srd_set<88>(Qb, WSB);
    {
      const unsigned* tp = g_tab.row[r * 5].v;
      asm volatile("buffer_load_dwordx2 v[120:121], off, s[88:91], %0 sc1\n\tglobal_load_dword v78, %1, %3\n\tglobal_load_dword v79, %2, %3 offset:1024\n\t"
                   "global_load_dword v118, %2, %4\n\tglobal_load_dword v119, %2, %4 offset:256"
                   ::"s"(fo(6144)), "v"(tid * 4), "v"(lane * 4), "s"(tp), "s"(g_tab.row[160].v) : "memory");
    }
    if (doP) {
      asm volatile("s_waitcnt vmcnt(17)" ::: "memory");
#pragma unroll
      for (int q = 0; q < 7; ++q) asm volatile("" : "+v"(pr.kk[q]));
#pragma unroll
      for (int q = 0; q < 4; ++q) asm volatile("" : "+v"(pr.vv[q]));
      static_for([&](auto Ic) __attribute__((always_inline)) {
        constexpr int I = Ic;
        g_pro<12 + 4 * I>(mp, make_iseq<4>{});
        GAP();
        prep_math<I>(pr, tid, lds + 131072);
        GAP();
      }, make_iseq<4>{});
      asm volatile("s_waitcnt lgkmcnt(0)\n\ts_barrier" ::: "memory");
      prep_store(tid, pj, hsel * 8 + j, xqT, lds + 131072);
    } else g_pro<12>(mp, make_iseq<4>{});
    GAP();
    CODE_TOUCH("at_code_begin", "pf_code_end", r * 4 + wid, 128u, 8, m0d);
    {
      const int fr = lane & 15, fq = lane >> 4, q = (lane & 15) >> 2, p = lane & 3;
#pragma unroll
      for (int k2 = 0; k2 < 2; ++k2)
        g.aA[k2] = lA + (wid >> 1) * 16384 + (fr >> 3) * 1024 + (fr & 7) * 128 + (((4 * k2 + fq) ^ (fr >> 1)) * 16);
#pragma unroll
      for (int hf = 0; hf < 2; ++hf)
#pragma unroll
        for (int nf = 0; nf < 8; ++nf)
          g.aB[hf][nf] = lW + (wid & 1) * 8192 + off_b(8 * fq + 4 * hf + q, nf * 2 + (p >> 1)) + 8 * (p & 1);
      asm volatile("" : "+v"(g.aA[0]), "+v"(g.aA[1]));
#pragma unroll
      for (int nf = 0; nf < 8; ++nf) asm volatile("" : "+v"(g.aB[0][nf]), "+v"(g.aB[1][nf]));
      vsetu<81>(tid == 0 ? 0u : 0x80000000u);
      vsetu<84>(lane * 16); vsetu<85>(lane * 16 + 65536); vsetu<86>(wid * 8192 + lane * 16); vsetu<87>(wid * 8192 + lane * 16 + 4096);
      vsetu<88>(65536 + wid * 256 + fr * 16); vsetu<89>(fr - 4 * fq);
      asm volatile("s_mov_b64 s[92:93], s[88:89]\n\ts_mov_b64 s[94:95], s[90:91]");
    }
    if (doP) asm volatile("s_waitcnt vmcnt(13)" ::: "memory");
    else {
      g_pro<16>(mp, make_iseq<12>{});
      asm volatile("s_waitcnt vmcnt(8)" ::: "memory");
    }
    asm volatile("s_barrier" ::: "memory");
    if (isY) g_read0<0>(g, make_iseq<16>{});
    else g_read0<1>(g, make_iseq<16>{});
    GAP();
    {
      u64 z = (((u64)__float_as_uint(vget<121>()) << 32) | __float_as_uint(vget<120>())) + 0x9e3779b97f4a7c15ull;
      z = (z ^ (z >> 30)) * 0xbf58476d1ce4e5b9ull; z = (z ^ (z >> 27)) * 0x94d049bb133111ebull; z ^= z >> 31;
      vsetu<82>((unsigned)z); vsetu<83>((unsigned)(z >> 32));
      asm volatile("v_mad_u32_u24 v78, v118, %0, v78\n\tv_mad_u32_u24 v79, v119, %0, v79\n\tv_readfirstlane_b32 s84, v82\n\tv_readfirstlane_b32 s85, v83\n\t"
                   "v_readlane_b32 s80, v78, %1\n\tv_readlane_b32 s81, v78, %2" ::"s"(j), "i"((int)T_TAB), "i"(T_TAB + 1));
      if (isY) asm volatile("s_mov_b64 s[80:81], 0x6543210");
      asm volatile("s_bfe_u32 s82, s80, 0x40008\n\ts_lshr_b64 s[80:81], s[80:81], 12\n\tv_lshl_add_u32 v91, s82, 19, v93" ::: "scc");
    }
    asm volatile("s_waitcnt lgkmcnt(0)\n\ts_barrier" ::: "memory");
    g_pro<28>(mp, make_iseq<4>{});
    GAP();
    if (isY) {
      g_tile<false, 0, 1>(g);
      g_loop<false, true>(g, PREPX ? 0x20u : 0x22u, [&](int t) __attribute__((always_inline)) {
        if (t == 1) fl_set<T_FPREP>();
        else if (t == 4) { tload<80, T_TAB>(); tload<81, T_TAB + 1>(); srd_set<60>(gW, (DC - 1) * LDW * 2 + 512); }
        else {
          srd_set<56>(gA, 256 * DC * 2);
          asm volatile("v_mov_b32 v92, v94");
          static_for([&](auto J) { asm volatile("s_mov_b32 s%c0, %1" ::"i"(64 + J), "i"((J + 1) * 64 * DC)); }, make_iseq<7>{});
        }
      });
      tload<101, T_OQ>();
      asm volatile("v_mov_b32 v112, v84\n\tv_add_u32 v113, 8192, v84");
      GAP();
      g_tile<false, 0, 2>(g);
      KST(0, 3);
    }
#define TT 3
    auto kvtile = [&](auto TRc) __attribute__((always_inline)) {
      constexpr bool TR = TRc;
      if (isY) g_tile<TR, 1, 4>(g);
      else g_tile<TR, 1, 1>(g);
      g_loop<TR, false>(g, 0x2000u | (isY || doA ? 2u : 1u << TT), [&](int t) __attribute__((always_inline)) {
        int l = threadIdx.x & 63; asm volatile("" : "+v"(l));
        if (t == 13) {
          asm volatile("s_mov_b32 s79, 0x28000");
          fl_poll<T_NPRE>();
          if (!doA && !isY) {
            CODE_TOUCH("at_code_begin", "pf_code_end", hsel * 8 + wid * 2, 16u, 64, m0d);
            CODE_TOUCH("at_code_begin", "pf_code_end", hsel * 8 + wid * 2 + 1, 16u, 64, m0d);
          }
          if (doA) {
            const char* tp = (const char*)assmk + (size_t)(pj * 64 + pu) * (SEQ * 2) + (l & 31) * 128;
            asm volatile("s_mov_b32 m0, %1\n\ts_nop 0\n\tglobal_load_lds_dword %0, off" ::"v"(tp), "i"(m0d) : "memory");
          }
        } else if (isY) fl_set<T_FQ>();
        else if (doA) { if (PREPX) fl_set<T_FPREP>(); post_dma(pj, pu, aq, 131072 + wid * 8192); }
        else {
          const char* tp = (const char*)ckv + 1024 * DC * 2 + (pu * 256 + l) * 128;
          asm volatile("s_mov_b32 m0, %1\n\ts_nop 0\n\tglobal_load_lds_dword %0, off" ::"v"(tp), "i"(m0d) : "memory");
          asm volatile("global_load_lds_dword %0, off" ::"v"(tp + 8192) : "memory");
          asm volatile("global_load_lds_dword %0, off" ::"v"(tp + 16384) : "memory");
          asm volatile("global_load_lds_dword %0, off" ::"v"(tp + 24576) : "memory");
        }
      });
      tload<101, T_OG>(); tload<96, T_Q0>(); tload<98, T_K0>();
      fl_check();
      tload<50, T_NST>(); tload<51, T_NST + 1>();
      asm volatile("v_mov_b32 v112, v84\n\tv_add_u32 v113, %0, v84\n\tv_mov_b32 v90, v87\n\tv_mov_b32 v91, v87\n\tv_mov_b32 v92, v87\n\tv_mov_b32 v93, v87\n\t"
                   "v_mov_b32 v94, v86\n\tv_mov_b32 v95, v86\n\tv_mov_b32 v126, v86\n\tv_mov_b32 v127, v86\n\ts_mov_b32 s99, 0x8000\n\t" FL_ARM("s[50:51]")
                   ::"i"(TR ? 4096 : 8192));
      if (doA) {
        const int d = pj * 64 + pu;
        const bf16* dp = assmD + d;
        asm volatile("global_load_ushort %0, %1, off" : "=v"(dv) : "v"(dp) : "memory");
        srd_set<56>(assmk + (size_t)d * SEQ, SEQ * 2);
        srd_set<60>(xqT + (size_t)d * SEQ, SEQ * 2);
        sset<78>(65536 + wid * 15488);
        asm volatile("s_movk_i32 s64, 992\n\tv_subrev_u32 v122, 32, v84");
        GAP();
        g_tile<TR, 0, 3>(g);
      } else {
        sset<79>(isY ? wid * 8192 : 0x30000);
        GAP();
        g_tile<TR, 0, 5>(g);
      }
    };
    if (kv) kvtile(std::true_type{});
    else kvtile(std::false_type{});
  }
  KST(0, 4);
  asm volatile("s_waitcnt vmcnt(16)" : "+v"(dv) :: "memory");
  sset<79>(wid * 8192);
  if (doA) {
    const float Dd = bf2f(dv);
    KST(0, 5);
    conv_wave<T_FG>(O_Z + (pj * 64 + pu) * SEQ * 2, Dd, lds + 65536 + wid * 15488);
    const char* tp = (const char*)wo + (size_t)h * (HD * DA * 2) + ((pj * 4 + wid) * 64 + lane) * 128;
    asm volatile("s_mov_b32 m0, %1\n\ts_nop 0\n\tglobal_load_lds_dword %0, off\n\ts_waitcnt vmcnt(17)" ::"v"(tp), "i"(m0d) : "memory");
    KST(0, 6);
  } else if (!isY) {
    static_for([&](auto I) { d_store<16 + I>(); }, make_iseq<16>{});
    tload<98, T_K0>();
    asm volatile("s_waitcnt vmcnt(0)\n\ts_barrier" ::: "memory");
    fl_set<T_FG>();
    KST(0, 5);
    fl_check();
    CODE_TOUCH("att_begin", "pf_code_end", hsel * 4 + wid, 8u, 64, m0d);
    dma4k<0>(); dma4k<1>(); dma4k<2>(); dma4k<3>();
    asm volatile("s_waitcnt vmcnt(0)" ::: "memory");
    KST(0, 6);
  } else {
    tload<76, T_PART>();
    fl_check();
    asm volatile("buffer_load_dwordx2 v[122:123], v88, s[88:91], s76 offen offset:1024 sc1" ::: "memory");
  }
  AC c;
  sset<78>(65536 + wid * 8192); sset<79>(98304 + wid * 8192);
  { const bf16* wp = wo + (size_t)h * HD * DA; asm volatile("s_mov_b64 s[80:81], %0" ::"s"(wp)); }
  asm volatile("att_begin:");
  ev_code();
  asm volatile("s_waitcnt lgkmcnt(0)\n\ts_barrier" ::: "memory");
#pragma unroll 1
  for (int it = doA ? 0 : 1; it < 2; ++it) {
    kreads<0>(make_iseq<8>{});
    const unsigned oP = tget<T_PART>();
    if (it == 0) {
      static_for([&](auto I) { tload<57 + I, T_IT + I>(); }, make_iseq<21>{});
      tload<S_T, T_IT + 21>(); tload<S_EV, T_IT + 22>();
      asm volatile("s_sub_u32 s70, s70, %0\n\ts_add_u32 s76, %1, 1024" ::"s"(j * (8 * 4352 - 8)), "s"(oP) : "scc");
    } else {
      static_for([&](auto I) { tload<57 + I, T_IT + T_ITN + I>(); }, make_iseq<21>{});
      tload<S_T, T_IT + T_ITN + 21>(); tload<S_EV, T_IT + T_ITN + 22>();
      asm volatile("s_mov_b32 s72, 0\n\ts_mov_b32 s75, %0\n\ts_mov_b32 s76, %1" ::"s"(oP + wid * 16384), "s"(oP + 1032));
    }
    unsigned mode, n;
    asm volatile("s_add_u32 s98, s97, 0x8000\n\ts_mov_b32 %0, s73\n\ts_lshr_b32 %1, s73, 8\n\tv_mov_b64 v[114:115], 0\n\tv_mov_b64 v[118:119], 0\n\t"
                 "v_mov_b32 v120, 0x45800000" : "=s"(mode), "=s"(n) :: "scc");
    KST(0, 8 + 3 * (it && !isY));
    GAP();
#define STA(k)
    STA(0);
    if (isY) phaseA<0, 2>(c);
    else phaseA<0, 1>(c);
    STA(1);
    if (c.rare) ev_call(0);
    STA(2);
    phaseB0(mode, wid);
    if (__builtin_expect(mode & 4, 0)) ev_call(31);
    STA(3);
#pragma unroll 1
    for (int i = 1;; i += 2) {
      phaseA<1, 0>(c);
      STA(9 + i);
      if (__builtin_expect(c.rare != 0, 0)) { if (c.redo) sm_redo<1>(); ev_call(i); }
      phaseB<1>(c);
      if (i + 1 >= n) break;
      phaseA<0, 0>(c);
      STA(10 + i);
      if (__builtin_expect(c.rare != 0, 0)) { if (c.redo) sm_redo<0>(); ev_call(i + 1); }
      phaseB<0>(c);
    }
    STA(4);
    if ((mode & 3) == 1) asm volatile("v_bfrev_b32 v122, 1");
    else asm volatile("v_mov_b32 v122, v84\n\tbuffer_load_dwordx4 v[128:131], v88, s[88:91], %0 offen sc1" ::"s"(oP) : "memory");
    GAP();
    phaseA<0, 3>(c);
    if (__builtin_expect(c.redo != 0, 0)) sm_redo<0>();
    STA(5);
    KST(0, 9 + 3 * (it && !isY));
    const float l[2] = {row_reduce<false>(vget<R_RS>() + vget<R_TS>()), row_reduce<false>(vget<R_RS + 1>() + vget<R_TS + 1>())};
    const float cm[2] = {vget<R_M>(), vget<R_M + 1>()};
    EP p;
    if (it == 0) {
      const f32x4 ml = {cm[0], l[0], cm[1], l[1]};
      asm volatile("buffer_store_dwordx4 %0, v88, s[88:91], %1 offen sc1\n\ts_mov_b32 s101, %2" ::"v"(ml), "s"(oP), "s"(oP + wid * 16384) : "memory");
      phaseF<0>(c, p, make_iseq<74>{});
      asm volatile(FL_POLL "\n\ts_waitcnt vmcnt(17)" ::: "memory");
    } else {
      int lz = threadIdx.x & 63; asm volatile("" : "+v"(lz));
      const int rw = lz >> 2;
      static_for([&](auto E) { vsetu<R_TB + E>(rw * 128 + 16 * ((2 * E + ((lz & 3) >> 1)) ^ (rw & 6)) + 8 * (lz & 1)); }, make_iseq<4>{});
      const unsigned vs = ((lz >> 3) & 1) * 16384 + ((lz >> 4) & 1) * 512 + (lz >> 5) * 256 + (lz & 7) * 16;
      vsetu<R_VS>(vs); vsetu<R_VS + 1>(vs + 32768);
      tload<96, T_SLAB>();
      asm volatile("s_mov_b32 s97, s79\n\ts_mov_b32 s100, 0x8000\n\ts_movk_i32 s99, 0x1000");
      if (!(mode & 2)) {
        p.fa[0] = __builtin_amdgcn_rcpf(l[0]); p.fa[1] = __builtin_amdgcn_rcpf(l[1]);
        phaseF<1>(c, p, make_iseq<74>{});
      } else {
        const f32x4 ml = {vget<128>(), vget<129>(), vget<130>(), vget<131>()};
        if (__builtin_expect(__builtin_amdgcn_ballot_w64(ml[0] != cm[0] || ml[2] != cm[1]) == 0, 1)) {
          p.fa[0] = __builtin_amdgcn_rcpf(l[0] + ml[1]); p.fa[1] = __builtin_amdgcn_rcpf(l[1] + ml[3]);
          const unsigned one = (lz >> 4) != ((lz >> 2) & 3) ? 0u : (lz & 1) ? 0x3f800000u : 0x3f80u, o0 = (lz & 2) ? 0u : one, o1 = (lz & 2) ? one : 0u;
          vsetu<224>(o0); vsetu<225>(o1); vsetu<230>(o0); vsetu<231>(o1);
          asm volatile("v_mov_b64 v[226:227], 0\n\tv_mov_b64 v[228:229], 0");
          phaseF<3>(c, p, make_iseq<74>{});
        } else {
#pragma unroll
          for (int e = 0; e < 2; ++e) {
            const float mm = fmaxf(cm[e], ml[2 * e]);
            const float wa = __builtin_amdgcn_exp2f(cm[e] - mm), wb = __builtin_amdgcn_exp2f(ml[2 * e] - mm);
            const float inv = __builtin_amdgcn_rcpf(l[e] * wa + ml[2 * e + 1] * wb);
            p.fa[e] = wa * inv; p.fb[e] = wb * inv;
          }
          phaseF<2>(c, p, make_iseq<74>{});
        }
      }
    }
    KST(0, 10 + 3 * (it && !isY));
  }
  asm volatile("v_xor_b32 v%c0, s100, v%c0\np_loop:" ::"i"(R_TB));
  GAP();
  p_half<0>(make_iseq<32>{});
  p_half<1>(make_iseq<32>{});
  asm volatile("s_cbranch_scc1 p_loop\n\ts_nop 5");
  GAP();
  p_cvt<1, 0>(); p_cvt<1, 1>(); p_cvt<1, 2>(); p_cvt<1, 3>();
  p_store<1, 1>();
  GAP();
  KST(0, 27);
  {
    constexpr unsigned rdy = 98304;
    int lz = threadIdx.x & 63; asm volatile("" : "+v"(lz));
    const int fr = lz & 15, fq = lz >> 4, qbF = isY ? 15 - pj : pj, u = hsel * 8 + j;
    asm volatile("ds_write_b32 %0, %1" ::"v"(rdy), "v"(0) : "memory");
    vsetu<81>(lz == 0 ? 0u : 0x80000000u);
    vsetu<79>(fo(1024) + (qbF * 4 + (u >> 2)) * 4352 + fr * 8);
    {
      const int jj = 4 * (fr >> 3) + (fr & 3);
      const unsigned one = ((fq ^ (fr >> 2)) & 1) ? 0u : (jj & 1) ? 0x3f800000u : 0x3f80u;
      static_for([&](auto I) { vsetu<96 + I>((jj >> 1) == I ? one : 0u); }, make_iseq<4>{});
    }
    unsigned vo = (fq >> 1) * 1024 + (fq & 1) * 256 + fr * 16, vo1 = wid < 3 ? vo + 0x100000 : 0x80000000u;
    __amdgpu_buffer_rsrc_t so_ = __builtin_amdgcn_make_buffer_rsrc((void*)out, (short)0, SEQ * OUTW * 2, 0x00020000);
    unsigned vw = ((qbF * 128 + u * 8 + (fr & 7)) * OUTW + DA + wid * 64 + (fr >> 3) * 32 + fq * 8) * 2;
    unsigned so = O_S + qbF * 0x200000 + (1 + wid) * 0x40000 + u * 16384;
    asm volatile("" : "+s"(so), "+s"(so_));
    asm volatile("s_waitcnt vmcnt(0) lgkmcnt(0)" : "+v"(vo), "+v"(vo1), "+v"(vw) :: "memory");
    fl_set<T_FS>();
    asm volatile("s_barrier" ::: "memory");
    KST(0, 30);
    if (doA) post_math(pj, pu, out, lds + 131072 + wid * 8192);
    unsigned pend = 0x5555u, cnt = 0x400000;
    KST(0, 28);
    while (true) {
      unsigned nw;
      if (wid == 3) {
        u64 m;
        const u64 ex = pend | (pend << 1);
        asm volatile(FL_ARM("%1") "\n\t" FL_POLL "\n\ts_waitcnt vmcnt(0)\n\tv_cmp_eq_u64 %0, s[84:85], v[124:125]" : "=s"(m) : "s"(ex) : "memory");
        nw = (unsigned)m & ((unsigned)m >> 1) & pend;
        if (nw) asm volatile("ds_write_b32 %0, %1" ::"v"(rdy), "v"(~pend | nw) : "memory");
      } else {
        unsigned t;
        asm volatile("ds_read_b32 %0, %1\n\ts_waitcnt lgkmcnt(0)" : "=v"(t) : "v"(rdy) : "memory");
        nw = __builtin_amdgcn_readfirstlane(t) & pend;
      }
      if (--cnt == 0) nw = pend;
      if (nw == 0) {
        if (wid == 3) asm volatile("s_sleep 4");
        else asm volatile("s_sleep 1");
        continue;
      }
      asm volatile("s_nop 4");
      static_for([&](auto S) {
        if ((nw >> (2 * S)) & 1) { r_load<0, 2 * S>(vo, so); r_load<0, 2 * S + 1>(vo, so); r_load<1, 2 * S>(vo1, so); r_load<1, 2 * S + 1>(vo1, so); }
      }, make_iseq<8>{});
      pend &= ~nw;
      if (pend == 0) break;
    }
    asm volatile("s_waitcnt vmcnt(0)" ::: "memory");
    KST(0, 16);
    r_fin<0>(vw, so_, 0);
    if (wid < 3) r_fin<1>(vw, so_, 512);
    KST(0, 29);
    if (b == 128 && wid == 0) asm volatile(FL_SET("%0") ::"s"(fo(6144)) : "memory");
  }
  asm volatile("s_waitcnt vmcnt(0)" ::: "memory");
  asm volatile("pf_code_end:");
  KST(0, 1);
}

extern "C" void kernel_launch(void* const* d_in, const int* in_sizes, int n_in, void* d_out, int out_size, void* d_ws,
                              size_t ws_size, hipStream_t stream) {
  const bf16* Aq = (const bf16*)d_in[0];
  const bf16* Ak = (const bf16*)d_in[1];
  const bf16* Av = (const bf16*)d_in[2];
  const bf16* Ash = (const bf16*)d_in[3];
  const bf16* Assmk = (const bf16*)d_in[4];
  const bf16* AssmD = (const bf16*)d_in[5];
  const bf16* Bx = (const bf16*)d_in[6];
  const bf16* Bckv = (const bf16*)d_in[7];
  const bf16* Bwq = (const bf16*)d_in[8];
  const bf16* Bwuk = (const bf16*)d_in[9];
  const bf16* Bwuv = (const bf16*)d_in[10];
  const bf16* Bwo = (const bf16*)d_in[11];
  bf16* out = (bf16*)d_out;
  hipFuncSetAttribute((const void*)fused_kernel, hipFuncAttributeMaxDynamicSharedMemorySize, 163840);
  fused_kernel<<<256, 256, 163840, stream>>>(Bckv, Bwuk, Bwuv, Bx, Bwq, (bf16*)d_ws, Ak, Av, Ash, Assmk, AssmD, Bwo, Aq, out);
}
```

```cpp
#include <hip/hip_runtime.h>
#include <hip/hip_bf16.h>
#include <stdint.h>
#include <utility>

typedef __hip_bfloat16 bf16;
typedef __attribute__((ext_vector_type(4))) short bf16x4;
typedef __attribute__((ext_vector_type(2))) unsigned u32x2;

constexpr int SEQ = 2048;
constexpr int DA = 448;
constexpr int NH = 16;
constexpr int HD = 256;
constexpr int DC = 1024;
constexpr int OUTW = 896;
#define OUTSC 0
#define OUTSCS ""
constexpr unsigned TSB = NH * SEQ * HD * 2, O_K = TSB, O_S = 3 * TSB, O_P = 5 * TSB, PSTR = 67584, O_F = O_P + 128 * PSTR,
                   O_X = O_F + 400 * 4352, O_Z = O_X + DA * SEQ * 2, WSB = O_Z + DA * SEQ * 2;
constexpr unsigned fo(int w) { return O_F + (w >> 4) * 4352 + (w & 15) * 8; }

#define KST(kid, which)

__device__ __forceinline__ float bf2f(unsigned short u) { return __uint_as_float(((unsigned)u) << 16); }
__device__ __forceinline__ unsigned short f2bf(float f) { return __bfloat16_as_ushort(__float2bfloat16(f)); }
__device__ __forceinline__ float rbf(float f) { return bf2f(f2bf(f)); }
typedef __attribute__((ext_vector_type(2))) __bf16 bf16x2n;
typedef __attribute__((ext_vector_type(2))) float f32x2n;
__device__ __forceinline__ unsigned pk2(float a, float b) {
  return __builtin_bit_cast(unsigned, __builtin_convertvector((f32x2n){a, b}, bf16x2n));
}

__device__ __forceinline__ int off_b(int row, int ch) { return 256 * row + 16 * (ch ^ (((row & 3) << 2) | ((row >> 2) & 3))); }

template <int R> __device__ __forceinline__ void sset(unsigned x) { asm volatile("s_mov_b32 s%c0, %1" ::"i"(R), "s"(x)); }
template <int R> __device__ __forceinline__ void vsetu(unsigned x) { asm volatile("v_mov_b32 v%c0, %1" ::"i"(R), "v"(x)); }
template <int R> __device__ __forceinline__ void srd_set(const void* p, unsigned n) {
  asm volatile("s_mov_b64 s[%c0:%c1], %4\n\ts_mov_b32 s%c2, %5\n\ts_mov_b32 s%c3, 0x20000" ::"i"(R), "i"(R + 1), "i"(R + 2), "i"(R + 3), "s"(p), "s"(n));
}
template <int R, int L> __device__ __forceinline__ void tload() { asm volatile("v_readlane_b32 s%c0, v78, %1" ::"i"(R), "i"(L)); }
template <int L> __device__ __forceinline__ unsigned tget() { unsigned t; asm volatile("v_readlane_b32 %0, v78, %1" : "=s"(t) : "i"(L)); return t; }
#define FL_SET(S) "buffer_store_dwordx2 v[82:83], v81, s[88:91], " S " offen sc1"
#define FL_ARM(N) "v_cndmask_b32_e64 v80, -2.0, v79, " N
#define FL_POLL "buffer_load_dwordx2 v[124:125], v80, s[88:91], 0 offen sc1"
#define FL_CHECK(N) "s_mov_b32 s48, 0x100000\n7:\n\tv_cmp_eq_u64 vcc, s[84:85], v[124:125]\n\ts_and_b64 vcc, vcc, " N "\n\ts_cmp_eq_u64 vcc, " N \
                    "\n\ts_cbranch_scc1 8f\n\ts_sleep 2\n\t" FL_POLL "\n\ts_waitcnt vmcnt(0)\n\ts_sub_u32 s48, s48, 1\n\ts_cbranch_scc0 7b\n8:"
template <int L> __device__ __forceinline__ void fl_set() {
  asm volatile("v_readlane_b32 s48, v78, %0\n\ts_nop 4\n\t" FL_SET("s48") ::"i"(L) : "memory");
}
template <int L> __device__ __forceinline__ void fl_poll() {
  asm volatile("v_readlane_b32 s50, v78, %0\n\tv_readlane_b32 s51, v78, %1\n\ts_nop 1\n\t" FL_ARM("s[50:51]") "\n\t" FL_POLL ::"i"(L), "i"(L + 1) : "memory");
}
__device__ __forceinline__ void fl_check() { asm volatile(FL_CHECK("s[50:51]") ::: "vcc", "scc", "memory"); }
template <int R> __device__ __forceinline__ float vget() { float t; asm volatile("v_mov_b32 %0, v%c1" : "=v"(t) : "i"(R)); return t; }
template <int R> __device__ __forceinline__ void vset(float x) { asm volatile("v_mov_b32 v%c0, %1" ::"i"(R), "v"(x)); }
template <int R> __device__ __forceinline__ float acc_read1() { float t; asm volatile("v_accvgpr_read_b32 %0, a[%c1]" : "=v"(t) : "i"(R)); return t; }
template <class F, int... I> __device__ __forceinline__ void static_for(F&& f, std::integer_sequence<int, I...>) {
  (f(std::integral_constant<int, I>{}), ...);
}
template <int... Is> using iseq = std::integer_sequence<int, Is...>;
template <int N> using make_iseq = std::make_integer_sequence<int, N>;

typedef __attribute__((ext_vector_type(4))) unsigned u32x4c;
typedef __attribute__((ext_vector_type(4))) float f32x4c;
typedef __attribute__((ext_vector_type(2))) float f32x2c;
typedef unsigned long long u64;
struct PR { u32x4c kk[7], vv[4]; };
__device__ __forceinline__ void prep_load(PR& p, int tid, int cg, int tb, const bf16* __restrict__ k, const bf16* __restrict__ v,
                                          const bf16* __restrict__ shift_k) {
  const __amdgpu_buffer_rsrc_t sk = __builtin_amdgcn_make_buffer_rsrc((void*)k, (short)0, SEQ * DA * 2, 0x00020000);
  const __amdgpu_buffer_rsrc_t sv = __builtin_amdgcn_make_buffer_rsrc((void*)v, (short)0, SEQ * DA * 2, 0x00020000);
  const __amdgpu_buffer_rsrc_t sw = __builtin_amdgcn_make_buffer_rsrc((void*)(shift_k + cg * 256), (short)0, 512, 0x00020000);
  const int vo = ((tb * 128 + (tid >> 3) * 4) * DA + cg * 64 + (tid & 7) * 8) * 2;
#pragma unroll
  for (int q = 0; q < 7; ++q)
    asm volatile("buffer_load_dwordx4 %0, %1, %2, 0 offen" : "=v"(p.kk[q]) : "v"(vo + (q - 3) * DA * 2), "s"(sk) : "memory");
#pragma unroll
  for (int q = 0; q < 4; ++q)
    asm volatile("buffer_load_dwordx4 %0, %1, %2, 0 offen offset:%3" : "=v"(p.vv[q]) : "v"(vo), "s"(sv), "i"(q * DA * 2) : "memory");
#pragma unroll
  for (int q = 0; q < 4; ++q)
    asm volatile("buffer_load_dwordx4 a[%c0:%c1], %2, %3, 0 offen offset:%4" ::"i"(4 * q), "i"(4 * q + 3), "v"((tid & 7) * 64), "s"(sw), "i"(q * 16) : "memory");
}
__device__ __forceinline__ float bfl(unsigned u) { return __uint_as_float(u << 16); }
__device__ __forceinline__ float bfh(unsigned u) { return __uint_as_float(u & 0xffff0000u); }
__device__ __forceinline__ f32x2c rbf2(f32x2c a) { const unsigned u = pk2(a[0], a[1]); return (f32x2c){bfl(u), bfh(u)}; }
template <int I> __device__ __forceinline__ void prep_math(const PR& p, int tid, char* lx) {
  const int rp = tid >> 3, c = tid & 7;
  f32x2c wt[4], kf[7];
  const unsigned w[4] = {__float_as_uint(acc_read1<4 * I>()), __float_as_uint(acc_read1<4 * I + 1>()), __float_as_uint(acc_read1<4 * I + 2>()),
                         __float_as_uint(acc_read1<4 * I + 3>())};
#pragma unroll
  for (int t = 0; t < 4; ++t)
    wt[t] = (t & 1) ? (f32x2c){bfh(w[t >> 1]), bfh(w[2 + (t >> 1)])} : (f32x2c){bfl(w[t >> 1]), bfl(w[2 + (t >> 1)])};
#pragma unroll
  for (int q = 0; q < 7; ++q) kf[q] = (f32x2c){bfl(p.kk[q][I]), bfh(p.kk[q][I])};
  f32x2c x[4];
#pragma unroll
  for (int e = 0; e < 4; ++e) {
    f32x2c s = rbf2(kf[e] * wt[3]);
    s = rbf2(s + rbf2(kf[e + 1] * wt[2]));
    s = rbf2(s + rbf2(kf[e + 2] * wt[1]));
    s = rbf2(s + rbf2(kf[e + 3] * wt[0]));
    x[e] = s * (f32x2c){bfl(p.vv[e][I]), bfh(p.vv[e][I])};
  }
#pragma unroll
  for (int u = 0; u < 2; ++u)
    *(u32x2*)(lx + (c * 8 + 2 * I + u) * 256 + (((31 - rp) * 8) ^ (c << 4))) = (u32x2){pk2(x[3][u], x[2][u]), pk2(x[1][u], x[0][u])};
}
__device__ __forceinline__ void prep_store(int tid, int cg, int tb, bf16* __restrict__ xr, const char* lx) {
  const __amdgpu_buffer_rsrc_t sx = __builtin_amdgcn_make_buffer_rsrc((void*)(xr + (size_t)cg * 64 * SEQ), (short)0, 64 * SEQ * 2, 0x00020000);
#pragma unroll
  for (int rd = 0; rd < 4; ++rd) {
    const int idx = tid + 256 * rd, ch = idx >> 4, q = idx & 15;
    u32x4c w = *(const u32x4c*)(lx + ch * 256 + 16 * (q ^ (ch >> 3)));
    __builtin_amdgcn_raw_buffer_store_b128(w, sx, (ch * SEQ + (15 - tb) * 128 + q * 8) * 2, 0, 16);
  }
}

#define CODE_TOUCH(LB, LE, wx, NW, NL, m0v) do { unsigned blo_, bhi_, elo_; \
    asm volatile("s_getpc_b64 s[48:49]\n\ts_add_u32 s48, s48, " LB "@rel32@lo+4\n\ts_addc_u32 s49, s49, " LB "@rel32@hi+12\n\t" \
                 "s_mov_b32 %0, s48\n\ts_mov_b32 %1, s49\n\t" \
                 "s_getpc_b64 s[48:49]\n\ts_add_u32 s48, s48, " LE "@rel32@lo+4\n\ts_mov_b32 %2, s48" \
                 : "=s"(blo_), "=s"(bhi_), "=s"(elo_) :: "scc"); \
    const unsigned n_ = elo_ - (blo_ & ~127u); \
    const __amdgpu_buffer_rsrc_t srd_ = __builtin_amdgcn_make_buffer_rsrc((void*)(((unsigned long long)bhi_ << 32) | (blo_ & ~127u)), (short)0, n_ < 262144u ? n_ : 0u, 0x00020000); \
    const unsigned l_ = threadIdx.x & 63, vo_ = l_ < (NL) ? ((wx) + l_ * (NW)) * 128u : 0x80000000u; \
    asm volatile("s_mov_b32 m0, %0\n\ts_nop 4\n\tbuffer_load_dword %1, %2, 0 offen lds" ::"s"(m0v), "v"(vo_), "s"(srd_) : "memory"); } while (0)
template <int I> __device__ __forceinline__ void d_store() {
  asm volatile("buffer_store_dwordx4 v[%c0:%c1], v%c2, s[88:91], s101 offen offset:%3 sc1" ::"i"(128 + 4 * I), "i"(131 + 4 * I), "i"(112 + ((I >> 2) & 1)),
               "i"((I & 3) * 1024) : "memory");
  if constexpr ((I & 7) == 7) asm volatile("s_addk_i32 s101, 0x4000" ::: "scc");
}
constexpr int NS_X = 19;
struct CV { unsigned pa[4], pb, px, vz, sz; float D; };
constexpr int cv_J(bool up, int q) { int J = up ? 0 : 7; while (q > J) { q -= J + 1; J += up ? 1 : -1; } return J; }
constexpr int cv_i(bool up, int q) { int J = up ? 0 : 7; while (q > J) { q -= J + 1; J += up ? 1 : -1; } return q; }
template <int R, int N> __device__ __forceinline__ void cv_rd(CV& c) {
  if constexpr (N < 16) {
    constexpr int i = N >> 1, o = 16 * R + 128 * (i & 1) + 2 * (N & 1), r = 32 + 32 * (R & 1) + 2 * N;
    asm volatile("ds_read2_b32 a[%c1:%c2], %0 offset0:%c3 offset1:%c4" ::"v"(c.pa[i >> 1]), "i"(r), "i"(r + 1), "i"(o), "i"(o + 1));
  } else {
    constexpr int m = N - 16, r = 192 + 32 * (R & 1) + 4 * m;
    asm volatile("ds_read_b128 a[%c1:%c2], %0 offset:%c3" ::"v"(c.pb), "i"(r), "i"(r + 3), "i"(64 * R + 512 * (7 - m)));
  }
}
template <int O> __device__ __forceinline__ void cv_xd(CV& c) {
  constexpr int p = O / 7, k = O % 7, t = 160 + 2 * p, x = 96 + (p ^ 1);
  if constexpr (k == 0) asm volatile("v_and_b32 v%c0, 0xffff0000, v%c1" ::"i"(t), "i"(x));
  if constexpr (k == 1) asm volatile("v_lshlrev_b32 v%c0, 16, v%c1" ::"i"(t + 1), "i"(x));
  if constexpr (k == 2 || k == 3) asm volatile("v_mul_f32 v%c0, v%c0, %1" ::"i"(t + k - 2), "v"(c.D));
  if constexpr (k == 4) asm volatile("v_cvt_pk_bf16_f32 v%c0, v%c1, v%c0" ::"i"(t + 1), "i"(t));
  if constexpr (k == 5) asm volatile("v_lshlrev_b32 v%c0, 16, v%c1" ::"i"(t), "i"(t + 1));
  if constexpr (k == 6) asm volatile("v_and_b32 v%c0, 0xffff0000, v%c0" ::"i"(t + 1));
}
template <int O> __device__ __forceinline__ void cv_z(CV& c) {
  constexpr int J = O / 13, k = O % 13, hf = k & 1, y = 128 + 4 * J + 2 * hf;
  if constexpr (k < 2) asm volatile("v_cvt_pk_bf16_f32 v%c0, v%c1, v%c0" ::"i"(y + 1), "i"(y));
  else if constexpr (k < 4) asm volatile("v_lshlrev_b32 v%c0, 16, v%c1" ::"i"(y), "i"(y + 1));
  else if constexpr (k < 6) asm volatile("v_and_b32 v%c0, 0xffff0000, v%c0" ::"i"(y + 1));
  else if constexpr (k < 10) asm volatile("v_add_f32 v%c0, v%c0, v%c1" ::"i"(y + (k >= 8)), "i"(y + 32 + (k >= 8)));
  else if constexpr (k < 12) asm volatile("v_cvt_pk_bf16_f32 v%c0, v%c1, v%c2" ::"i"(128 + 4 * J + hf), "i"(y), "i"(y + 1));
  else asm volatile("buffer_store_dwordx2 v[%c0:%c1], %2, s[88:91], %3 offen offset:%4 sc1" ::"i"(128 + 4 * J), "i"(129 + 4 * J), "v"(c.vz), "s"(c.sz),
                    "i"(512 * J) : "memory");
}
constexpr int cv_z0(int Q) {
  int o = 0;
  for (int q = 0; q < Q; ++q)
    for (int k = 0; k < 3; ++k) { const int J = o / 13; if ((J + 1) * (J + 2) / 2 - 1 + 4 <= q) ++o; }
  return o;
}
constexpr int cv_xd0(int T) { int o = 0; for (int t = 0; t < T; ++t) o += t % 36 < 24 ? 1 : 2; return o < 112 ? o : 112; }
template <int B, int... Is> __device__ __forceinline__ void cv_zs(CV& c, iseq<Is...>) { (cv_z<B + Is>(c), ...); }
template <int B, int... Is> __device__ __forceinline__ void cv_xds(CV& c, iseq<Is...>) { (cv_xd<B + Is>(c), ...); }
template <int R, int Q> __device__ __forceinline__ void cv_slot(CV& c) {
  constexpr int i = cv_i(R == 7, Q), J = cv_J(R == 7, Q), d = 128 + 4 * J, a = 32 + 32 * (R & 1) + 4 * i, b = 192 + 32 * (R & 1) + 4 * (J - i);
  constexpr int T = 36 * R + Q, vm = Q % 4 == 1 && Q < 32 ? Q / 4 : -1;
  if constexpr (Q == 0) asm volatile("s_waitcnt lgkmcnt(0)");
  if constexpr (R >= 6 && vm >= 0 && vm % 4 == 0) asm volatile("s_add_u32 m0, s79, %0" ::"i"((R - 6) * 32768 + vm * 1024));
  if constexpr (R == 0 && i == 0)
    asm volatile("v_mfma_f32_16x16x32_bf16 v[%c0:%c1], a[%c2:%c3], a[%c4:%c5], 0" ::"i"(d), "i"(d + 3), "i"(a), "i"(a + 3), "i"(b), "i"(b + 3));
  else
    asm volatile("v_mfma_f32_16x16x32_bf16 v[%c0:%c1], a[%c2:%c3], a[%c4:%c5], v[%c0:%c1]" ::"i"(d), "i"(d + 3), "i"(a), "i"(a + 3), "i"(b), "i"(b + 3));
  if constexpr (Q < 24 && R < 7) cv_rd<R + 1, Q>(c);
  if constexpr (T % 7 == 3 && NS_X + 5 + T / 7 < 32) d_store<NS_X + 5 + T / 7>();
  if constexpr (R == 2 && Q >= 24 && Q < 32)
    asm volatile("ds_read_b64 v[%c1:%c2], %0 offset:%c3" ::"v"(c.px), "i"(96 + 2 * (Q - 24)), "i"(97 + 2 * (Q - 24)), "i"(512 * (31 - Q)));
  if constexpr (R >= 3 && R < 6) cv_xds<cv_xd0(T - 108)>(c, make_iseq<cv_xd0(T - 107) - cv_xd0(T - 108)>{});
  if constexpr ((R == 3 || R == 4) && vm >= 0) {
    constexpr int f = (R - 3) * 8 + vm;
    asm volatile("buffer_load_dwordx4 a[%c0:%c1], v84, s[88:91], s96 offen offset:%2 sc1" ::"i"(128 + 4 * f), "i"(131 + 4 * f), "i"((f & 3) * 1024) : "memory");
    if constexpr ((f & 3) == 3) asm volatile("s_addk_i32 s96, 4096");
  }
  if constexpr (R == 4 && Q == 35) asm volatile(FL_POLL ::: "memory");
  if constexpr (R >= 6 && vm >= 0)
    asm volatile("buffer_load_dwordx4 v%c0, s[88:91], s98 offen offset:%1 sc1 lds" ::"i"(86 + vm / 4), "i"((vm & 3) * 1024) : "memory");
  if constexpr (R == 6 && Q == 35) asm volatile("s_add_u32 s98, s98, 0x8000");
  if constexpr (R == 7) {
    if constexpr (Q < 4) asm volatile("s_nop 0");
    cv_zs<cv_z0(Q)>(c, make_iseq<cv_z0(Q + 1) - cv_z0(Q)>{});
  }
  __builtin_amdgcn_sched_barrier(0);
}
template <int R, int... Q> __device__ __forceinline__ void cv_step(CV& c, iseq<Q...>) { (cv_slot<R, Q>(c), ...); }
template <int... N> __device__ __forceinline__ void cv_rd0(CV& c, iseq<N...>) { (cv_rd<0, N>(c), ...); }
template <int FLAG> __device__ __forceinline__ void conv_wave(unsigned sz, float Dd, char* lw) {
  int lane = threadIdx.x & 63; asm volatile("" : "+v"(lane));
  const int l16 = lane & 15, fq = lane >> 4;
  u32x4c w[5]; unsigned w4[5];
#pragma unroll
  for (int r = 0; r < 5; ++r) {
    w[r] = *(const u32x4c*)(lw + r * 1024 + lane * 16);
    w4[r] = *(const unsigned*)(lw + r * 1024 + lane * 16 + 16);
  }
  d_store<NS_X>(); d_store<NS_X + 1>();
#pragma unroll
  for (int r = 0; r < 5; ++r)
    *(u32x4c*)(lw + 10304 + r * 1024 + lane * 16) = (u32x4c){__builtin_amdgcn_alignbit(w[r][1], w[r][0], 16), __builtin_amdgcn_alignbit(w[r][2], w[r][1], 16),
                                                             __builtin_amdgcn_alignbit(w[r][3], w[r][2], 16), __builtin_amdgcn_alignbit(w4[r], w[r][3], 16)};
  d_store<NS_X + 2>();
  CV c;
#pragma unroll
  for (int i = 0; i < 4; ++i) c.pa[i] = (unsigned)(size_t)lw + ((l16 + 1) & 1) * 10304 + 4 * ((8 * fq + l16 + 1) >> 1) + 1024 * i;
  c.pb = (unsigned)(size_t)lw + 5120 + (2032 - 16 * l16 + 8 * fq) * 2 - 512 * 7;
  c.vz = (16 * l16 + 4 * fq) * 2;
  c.px = (unsigned)(size_t)lw + 5120 + 2044 * 2 - 512 * 7 - c.vz;
  c.sz = sz; c.D = Dd;
  asm volatile("" : "+v"(c.vz), "+v"(c.px), "+v"(c.D) :: "memory");
  asm volatile("" : "+s"(c.sz));
  cv_rd0(c, make_iseq<24>{});
  d_store<NS_X + 3>(); d_store<NS_X + 4>();
  __builtin_amdgcn_sched_barrier(0);
  static_for([&](auto R) __attribute__((always_inline)) {
    cv_step<R>(c, make_iseq<36>{});
    if constexpr (R == 2) {
      asm volatile("s_waitcnt vmcnt(0)\n\ts_barrier" ::: "memory");
      fl_set<FLAG>();
    }
    if constexpr (R == 5) {
      asm volatile("s_waitcnt vmcnt(0)" ::: "memory");
      fl_check();
    }
    __builtin_amdgcn_sched_barrier(0);
  }, make_iseq<8>{});
  asm volatile("s_nop 7");
  cv_zs<cv_z0(36)>(c, make_iseq<104 - cv_z0(36)>{});
}

__device__ __forceinline__ void post_dma(int cg, int tb, const bf16* __restrict__ q, unsigned m0w) {
  int lane = threadIdx.x & 63; asm volatile("" : "+v"(lane));
  const __amdgpu_buffer_rsrc_t sq = __builtin_amdgcn_make_buffer_rsrc((void*)(q + (size_t)tb * 32 * DA + cg * 64), (short)0, 32 * DA * 2, 0x00020000);
  const unsigned vq = ((lane >> 3) * DA + (lane & 7) * 8) * 2;
  asm volatile("s_mov_b32 m0, %0\n\ts_nop 4\n\tbuffer_load_dwordx4 %1, %2, 0 offen lds\n\tbuffer_load_dwordx4 %1, %2, %3 offen offset:1024 lds\n\t"
               "buffer_load_dwordx4 %1, %2, %4 offen offset:2048 lds\n\tbuffer_load_dwordx4 %1, %2, %5 offen offset:3072 lds"
               ::"s"(m0w + 4096), "v"(vq), "s"(sq), "s"(8 * DA * 2 - 1024), "s"(16 * DA * 2 - 2048), "s"(24 * DA * 2 - 3072) : "memory");
}
__device__ __forceinline__ void post_math(int cg, int tb, bf16* __restrict__ out, const char* lw) {
  int lane = threadIdx.x & 63; asm volatile("" : "+v"(lane));
  const int tl = lane >> 3, ck = lane & 7;
  const __amdgpu_buffer_rsrc_t so = __builtin_amdgcn_make_buffer_rsrc((void*)(out + (size_t)tb * 32 * OUTW + cg * 64), (short)0, 32 * OUTW * 2 - 2 * DA, 0x00020000);
  const unsigned vo = (tl * OUTW + ck * 8) * 2;
#pragma unroll
  for (int i = 0; i < 4; ++i) {
    const u32x4c qv = *(const u32x4c*)(lw + 4096 + i * 1024 + lane * 16);
    const char* lz = lw + ck * 64 + 16 * (i ^ (ck >> 1)) + tl * 2;
    u32x4c o;
#pragma unroll
    for (int e = 0; e < 4; ++e) {
      const float z0 = bf2f(*(const unsigned short*)(lz + 2 * e * 512));
      const float z1 = bf2f(*(const unsigned short*)(lz + (2 * e + 1) * 512));
      o[e] = pk2(bfl(qv[e]) * z0, bfh(qv[e]) * z1);
    }
    __builtin_amdgcn_raw_buffer_store_b128(o, so, vo + i * 8 * OUTW * 2, 0, OUTSC);
  }
}

#define DI __device__ __forceinline__
#define GAP() __builtin_amdgcn_sched_barrier(0)
typedef __attribute__((ext_vector_type(4))) float f32x4g;
typedef __attribute__((ext_vector_type(4))) unsigned u32x4g;
constexpr int LDW = NH * HD;
struct GM { unsigned aA[2], aB[2][8]; };
template <bool W, int J, int V> DI void g_dma() {
  if constexpr (J == 0) asm volatile("buffer_load_dwordx4 v%c0, s[%c1:%c2], 0 offen lds" ::"i"(V), "i"(W ? 60 : 56), "i"(W ? 63 : 59) : "memory");
  else asm volatile("buffer_load_dwordx4 v%c0, s[%c1:%c2], s%c3 offen lds" ::"i"(V), "i"(W ? 60 : 56), "i"(W ? 63 : 59), "i"((W ? 70 : 63) + J) : "memory");
}
template <int P, int KS, int I> DI void g_wread(GM& g) {
  constexpr int nf = I >> 1, hf = I & 1, r = 192 - 64 * P + 8 * nf + 4 * KS + 2 * hf;
  asm volatile("ds_read_b64_tr_b16 a[%c1:%c2], %0 offset:%c3" ::"v"(g.aB[hf][nf]), "i"(r), "i"(r + 1), "i"(P * 32768 + KS * 16384));
}
template <int P, int M, int KS> DI void g_aread(GM& g) {
  constexpr int r = 96 + 8 * (M & 1) + 4 * KS;
  asm volatile("ds_read_b128 v[%c1:%c2], %0 offset:%c3" ::"v"(g.aA[KS]), "i"(r), "i"(r + 3), "i"(P * 32768 + M * 2048));
}
constexpr float CS = 0.0625f * 1.4426950408889634f;
struct ES { int op0[129], nop[129]; };
constexpr ES e_sched(bool TR, bool SCL) {
  ES z{};
  const int per = SCL ? 16 : 8;
  int o = 0;
  for (int G = 0; G <= 128; ++G) {
    int cap = G == 128 ? 10000 : SCL ? 4 : 2;
    z.op0[G] = o;
    while (o < 32 * per) {
      const int i = o / per, rdy = (TR ? 32 * (i >> 3) + 16 + 2 * (i & 7) + 1 : 4 * i + 3) + 4;
      if ((G < rdy && G < 128) || cap-- <= 0) break;
      ++o; ++z.nop[G];
    }
  }
  return z;
}
template <bool TR, bool SCL> constexpr ES ESCH = e_sched(TR, SCL);
constexpr int e_vm(int G) { return (G * 3) % 8 < 3 ? G * 3 / 8 : -1; }
constexpr int e_st(int MODE, int G) {
  if (MODE == 5) return e_vm(G) >= 32 ? e_vm(G) - 32 : -1;
  if (MODE == 3) return G >= 31 && (G - 31) % 5 == 0 && (G - 31) / 5 < NS_X ? (G - 31) / 5 : -1;
  const int g0 = MODE == 4 ? 5 : 13, gs = MODE == 2 ? 7 : 6;
  return MODE >= 2 && G >= g0 && (G - g0) % gs == 0 && (G - g0) / gs < 16 ? (G - g0) / gs + (MODE == 4 ? 16 : 0) : -1;
}
constexpr int e_nst(int MODE, int G0, int G1) { int n = 0; for (int G = G0; G < G1; ++G) n += e_st(MODE, G) >= 0; return n; }
static_assert(e_nst(2, 0, 128) == 16 && e_nst(3, 30, 128) == NS_X && e_nst(5, 0, 128) == 16 && e_nst(4, 0, 104) == 16);
template <bool TR, bool SCL, int O> DI void e_op() {
  constexpr int per = SCL ? 16 : 8, i = O / per, k = O % per, r = 128 + 4 * i, t = 114 + 4 * (i & 1), c = k - (per - 4);
  if constexpr (k < 4) asm volatile("v_accvgpr_read_b32 v%c0, a[%c1]" ::"i"(t + k), "i"(4 * i + k));
  else if constexpr (c < 0) asm volatile("v_mul_f32 v%c0, %1, v%c0" ::"i"(k < 8 ? r + (k ^ 6) : t + k - 8), "s"(CS));
  else {
    constexpr int d = c == 0 ? r + 3 : c == 1 ? r + 2 : r + c - 2, x = c == 0 ? r + 2 : c == 1 ? r : t + 2 * (c - 2);
    asm volatile("v_cvt_pk_bf16_f32 v%c0, v%c1, v%c2" ::"i"(d), "i"(x), "i"(x + 1));
  }
}
template <bool TR, bool SCL, int B, int... Is> DI void e_ops(iseq<Is...>) { (e_op<TR, SCL, B + Is>(), ...); }
constexpr int e_slot(int MODE, int G) { return MODE == 3 ? (G % 3 == 0 && G < 30 ? G / 3 : -1) : e_vm(G) >= 0 && e_vm(G) < 32 ? 10 + e_vm(G) : -1; }
template <int S> DI void e_m0() {
  if constexpr (S == 0 || S == 1 || S == 5 || S == 9) asm volatile("s_add_u32 m0, s78, %0" ::"i"(S * 1024));
  if constexpr (S >= 26 && (S - 26) % 4 == 0) asm volatile("s_add_u32 m0, s79, %0" ::"i"(((S - 26) >> 3) * 32768 + ((S - 26) & 4) * 1024));
}
template <int S> DI void e_load() {
  if constexpr (S == 0) asm volatile("buffer_load_dwordx4 v122, s[56:59], 0 offen lds" ::: "memory");
  else if constexpr (S < 5) asm volatile("buffer_load_dwordx4 v84, s[56:59], s64 offen offset:%0 lds" ::"i"((S - 1) * 1024) : "memory");
  else if constexpr (S < 9) asm volatile("buffer_load_dwordx4 v84, s[60:63], 0 offen offset:%0 sc1 lds" ::"i"((S - 5) * 1024) : "memory");
  else if constexpr (S == 9) asm volatile("buffer_load_dwordx4 v84, s[60:63], s62 offen sc1 lds" ::: "memory");
  else if constexpr (S < 26) {
    constexpr int f = S - 10;
    asm volatile("buffer_load_dwordx4 a[%c0:%c1], v84, s[88:91], s96 offen offset:%2 sc1" ::"i"(128 + 4 * f), "i"(131 + 4 * f), "i"((f & 3) * 1024) : "memory");
    if constexpr ((f & 3) == 3) asm volatile("s_addk_i32 s96, 4096");
  } else {
    constexpr int f = S - 26;
    asm volatile("buffer_load_dwordx4 v%c0, s[88:91], s98 offen offset:%1 sc1 lds" ::"i"(86 + ((f >> 2) & 1)), "i"((f & 3) * 1024) : "memory");
    if constexpr (f == 7) asm volatile("s_add_u32 s98, s98, 0x8000");
    if constexpr (f == 15) asm volatile(FL_POLL ::: "memory");
  }
}
template <bool TR, int P, int H, int G, int MODE> DI void g_gap(GM& g) {
  constexpr int m = 4 * H + (G >> 4), q = G & 15, n = q >> 1, ks = q & 1, GG = 64 * H + G;
  constexpr bool inV = TR ? m & 1 : n & 1, END = MODE == 3 || MODE == 5, EPI = END || MODE == 2;
  constexpr int a = 96 + 8 * (m & 1) + 4 * ks, b = 192 - 64 * P + 8 * n + 4 * ks, d = (inV ? 128 : 0) + 4 * (TR ? 8 * (m >> 1) + n : 4 * m + (n >> 1));
  constexpr int J = END ? -1 : q == 8 ? 2 * (G >> 4) : q == 12 ? 2 * (G >> 4) + 1 : -1, gj = (J & 3) ^ ((J & 3) >> 1);
  if constexpr (END) { if constexpr (q == 0 && GG > 0) asm volatile("s_waitcnt lgkmcnt(0)"); e_m0<e_slot(MODE, GG)>(); }
  else if constexpr (q == 0 && G > 0) asm volatile("s_waitcnt lgkmcnt(4)");
  if constexpr (J == 0) asm volatile("s_add_u32 m0, s78, %0" ::"i"((P ^ H ^ 1) * 32768 + (1 - H) * 8192));
  else if constexpr (J == 4) asm volatile("s_add_u32 m0, s79, %0" ::"i"((P ^ H) * 32768 + (1 - H) * 16384));
  else if constexpr (J > 0 && (J & 3) == 1) asm volatile("s_bitset1_b32 m0, 12");
  else if constexpr (J > 0 && (J & 3) == 2) asm volatile("s_bitset1_b32 m0, %0" ::"i"(J < 4 ? 14 : 13));
  else if constexpr (J > 0) asm volatile("s_bitset0_b32 m0, 12");
#define GMF(D, X, Y, C) asm volatile("v_mfma_f32_16x16x32_bf16 " D "[%c0:%c1], " X ", " Y ", " C ::"i"(d), "i"(d + 3), "i"(a), "i"(a + 3), "i"(b), "i"(b + 3))
#define GMF2(D, C) do { if constexpr (TR) GMF(D, "v[%c2:%c3]", "a[%c4:%c5]", C); else GMF(D, "a[%c4:%c5]", "v[%c2:%c3]", C); } while (0)
  if constexpr ((MODE == 1 || MODE == 4) && ks == 0) { if constexpr (inV) GMF2("v", "0"); else GMF2("a", "0"); }
  else if constexpr (inV) GMF2("v", "v[%c0:%c1]");
  else GMF2("a", "a[%c0:%c1]");
#undef GMF2
#undef GMF
  if constexpr (e_st(MODE, GG) >= 0) {
    static_assert(MODE == 4 || ESCH<TR, MODE == 2>.op0[GG] >= (e_st(MODE, GG) + 1) * (MODE == 2 ? 16 : 8));
    d_store<e_st(MODE, GG)>();
  }
  if constexpr (EPI) e_ops<TR, MODE == 2, ESCH<TR, MODE == 2>.op0[GG]>(make_iseq<ESCH<TR, MODE == 2>.nop[GG]>{});
  if constexpr (J >= 0 && J < 4) g_dma<false, (1 - H) * 2 + (gj & 1) + (gj >> 1) * 4, 90>();
  else if constexpr (J >= 4) g_dma<true, (1 - H) * 4 + gj, 91>();
  if constexpr ((q == 1 || q == 2) && !(END && m == 7)) g_aread<(m < 7 ? P : P ^ 1), (m + 1) & 7, q - 1>(g);
  if constexpr (END) {
    if constexpr (e_slot(MODE, GG) >= 0) e_load<e_slot(MODE, GG)>();
    if constexpr (!TR && GG >= 3 && GG < 7 && e_slot(MODE, GG) < 0) asm volatile("s_nop 0");
    if constexpr (TR && GG >= 19 && GG < 21 && e_slot(MODE, GG) < 0) asm volatile("s_nop 0");
  } else {
    if constexpr (q >= 3 && q < 11 && (q & 1)) g_wread<P ^ 1, H, (G >> 4) * 4 + (q - 3) / 2>(g);
    if constexpr (H == 1 && G == 0) asm volatile("v_lshl_add_u32 v90, s82, 7, v92\n\ts_and_b32 s82, s80, 15");
    if constexpr (H == 1 && G == 4) asm volatile("s_lshr_b64 s[80:81], s[80:81], 4\n\tv_lshl_add_u32 v91, s82, 19, v93");
    constexpr int nst = e_nst(MODE, H ? 29 : 0, GG + 1);
    if constexpr (G == 61) asm volatile("s_waitcnt vmcnt(%0) lgkmcnt(0)\n\ts_barrier" ::"i"(12 + nst) : "memory");
  }
  if constexpr (EPI && GG == 127) {
    asm volatile("s_nop 7");
    e_ops<TR, MODE == 2, ESCH<TR, MODE == 2>.op0[128]>(make_iseq<ESCH<TR, MODE == 2>.nop[128]>{});
  }
  GAP();
}
template <bool TR, int P, int H, int MODE, int... Gs> DI void g_half_(GM& g, iseq<Gs...>) { (g_gap<TR, P, H, Gs, MODE>(g), ...); }
template <bool TR, int P, int H, int MODE> DI void g_half(GM& g) { g_half_<TR, P, H, MODE>(g, make_iseq<64>{}); }
template <bool TR, int P, int MODE> DI void g_tile(GM& g) { g_half<TR, P, 0, MODE>(g); g_half<TR, P, 1, MODE>(g); }
template <int P, int... Is> DI void g_read0(GM& g, iseq<Is...>) {
  (g_wread<P, 0, Is>(g), ...); (g_wread<P, 1, Is>(g), ...);
  g_aread<P, 0, 0>(g); g_aread<P, 0, 1>(g);
}
template <int I> DI void g_pro1(const unsigned (&m)[4]) {
  constexpr int grp = I >> 2, i = I & 3, t = grp < 4 ? 0 : grp < 7 ? 1 : 2;
  constexpr bool isA = grp == 0 || grp == 3 || grp == 5;
  constexpr int jj = (grp == 3 ? 2 : 0) + (i & 1) + (i >> 1) * 4, ks = grp == 2 || grp == 6;
  asm volatile("s_add_u32 m0, %0, %1\n\ts_nop 0" ::"s"(m[(t & 1) * 2 + !isA]), "i"(isA ? jj * 4096 : ks * 16384 + (i >> 1) * 8192 + (i & 1) * 4096) : "scc");
  if constexpr (isA) g_dma<false, jj, t ? 90 : 92>();
  else g_dma<true, ks * 4 + i, t ? 91 : 93>();
}
template <int B, int... Is> DI void g_pro(const unsigned (&m)[4], iseq<Is...>) { (g_pro1<B + Is>(m), ...); }
template <bool TR, bool SCL, class HOOK> DI void g_loop(GM& g, unsigned evm, HOOK&& hook) {
  constexpr int nkt = SCL ? 7 : 16, P0 = SCL ? 0 : 1;
  asm volatile("" : "+s"(evm));
#pragma unroll 1
  for (int t = 1;; t += 2) {
    g_half<TR, P0 ^ 1, 0, 0>(g);
    if (__builtin_expect((evm >> t) & 1, 0)) { hook(t); GAP(); }
    g_half<TR, P0 ^ 1, 1, 0>(g);
    if (SCL && t + 2 >= nkt) break;
    g_half<TR, P0, 0, 0>(g);
    if constexpr (SCL) { if (__builtin_expect(t == 3, 0)) { hook(4); GAP(); } }
    else asm volatile("s_cmp_eq_u32 %0, 13\n\ts_cselect_b32 s78, 0x28000, s78" ::"s"(t) : "scc");
    g_half<TR, P0, 1, 0>(g);
    if (!SCL && t + 3 >= nkt) break;
  }
}

typedef __attribute__((ext_vector_type(4))) unsigned u32x4;
typedef __attribute__((ext_vector_type(4))) float f32x4;

constexpr int R_S = 128, R_P = 192, R_CI = 224, R_E = 96;
constexpr int R_TS = 112, R_RS = 114, R_M = 116;
constexpr int R_PD = 118, R_THR = 120, R_TMP = 121;
constexpr int r_dk(int q) { return q < 6 ? 90 + q : 120 + q; }
constexpr int S_V = 96, S_T = 97, S_K = 98, S_I = 99, S_EV = 100;
struct AC {
  u64 redo;
  unsigned rare;
};
template <int X, int F> DI void kread() {
  constexpr int r = 192 + 4 * (F & 15);
  asm volatile("ds_read_b128 a[%c0:%c1], v84 offset:%c2" ::"i"(r), "i"(r + 3), "i"(X * 32768 + F * 1024));
}
template <int X, int F> DI void vread() {
  constexpr int r = 192 + 4 * (F & 15);
  asm volatile("ds_read_b128 a[%c0:%c1], v85 offset:%c2" ::"i"(r), "i"(r + 3), "i"(X * 32768 + (F & 1) * 16384 + (F >> 1) * 1024));
}
template <int X, int... Fs> DI void kreads(iseq<Fs...>) { (kread<X, Fs>(), ...); }
template <int X, int J> DI void dma_m0() {
  if constexpr (J == 0) asm volatile("s_mov_b32 m0, s%c0" ::"i"(78 + X));
  else if constexpr (J == 4) asm volatile("s_or_b32 m0, s%c0, 0x1000" ::"i"(78 + X));
  else if constexpr (J == 8) asm volatile("s_bitset0_b32 m0, 16");
  else if constexpr (J == 12) asm volatile("s_bitset0_b32 m0, 12");
}
template <int J> DI void dma_at() {
  if constexpr (J < 8)
    asm volatile("buffer_load_dwordx4 v%c0, s[88:91], s96 offen offset:%1 sc1 lds" ::"i"(86 + (J >= 4)), "i"((J & 3) * 1024) : "memory");
  else
    asm volatile("buffer_load_dwordx4 v%c0, s[92:95], s98 offen offset:%1 sc1 lds" ::"i"(r_dk(J - 8)), "i"((J & 3) * 1024) : "memory");
}
template <int Q> DI void dma4k() {
  asm volatile("s_add_u32 m0, s79, %0\n\ts_add_u32 s48, s98, %1\n\tbuffer_load_dwordx4 v%c2, s[88:91], s48 offen sc1 lds\n\t"
               "buffer_load_dwordx4 v%c2, s[88:91], s48 offen offset:1024 sc1 lds\n\tbuffer_load_dwordx4 v%c2, s[88:91], s48 offen offset:2048 sc1 lds\n\t"
               "buffer_load_dwordx4 v%c2, s[88:91], s48 offen offset:3072 sc1 lds"
               ::"i"((Q >> 1) * 32768 + (Q & 1) * 4096), "i"((Q >> 1) * 32768), "i"(86 + (Q & 1)) : "memory", "scc");
}
template <int Q> DI void acc_zero2() {
  asm volatile("v_accvgpr_write_b32 a[%c0], 0\n\tv_accvgpr_write_b32 a[%c1], 0" ::"i"(2 * Q), "i"(2 * Q + 1));
}
struct SMS { int ex[128], op0[128], nop[128], last; };
constexpr SMS sm_sched() {
  SMS z{};
  int et[32] = {}, e = 0;
  for (int t = 0; t < 128; ++t) {
    const int g = t & 15;
    const bool x = t < 48 ? (g >= 8 && (g & 1)) : (g >= 8 && g != 10 && g != 13 && g != 15);
    z.ex[t] = x && e < 32 ? e : -1;
    if (z.ex[t] >= 0) et[e++] = t;
  }
  int o = 0;
  for (int t = 0; t < 128; ++t) {
    const int g = t & 15;
    int cap = g < 8 ? (g == 0 ? 0 : 1) : (z.ex[t] >= 0 || t < 48 ? 0 : 2);
    z.op0[t] = o; z.nop[t] = 0;
    while (cap > 0 && o < 48 && et[2 * (o / 3) + (o % 3 == 0 ? 0 : 1)] + 2 <= t) {
      const int el = 2 * (o / 3) + o % 3;
      if (!(o % 3 < 2 && (el == 0 || el == 4))) --cap;
      ++o; ++z.nop[t]; z.last = t;
    }
  }
  return z;
}
constexpr SMS SM = sm_sched();
static_assert(SM.last <= 115);
constexpr int p_reg(int Y, int k) { return R_P + 16 * Y + 4 * ((k >> 3) * 2 + ((k >> 1) & 1)) + ((k >> 2) & 1) * 2 + (k & 1); }
template <int Y, int O> DI void sm_op() {
  constexpr int k = O / 3, e = 2 * k + O % 3, ts = R_TS + ((e >> 2) & 1);
  if constexpr (O % 3 == 2) asm volatile("v_cvt_pk_bf16_f32 v%c0, v%c1, v%c2" ::"i"(p_reg(Y, k)), "i"(R_E + ((2 * k) & 15)), "i"(R_E + ((2 * k + 1) & 15)));
  else if constexpr (e == 1 || e == 5) asm volatile("v_add_f32_e32 v%c0, v%c1, v%c2" ::"i"(ts), "i"(R_E + e - 1), "i"(R_E + e));
  else if constexpr (e != 0 && e != 4) asm volatile("v_add_f32_e32 v%c0, v%c0, v%c1" ::"i"(ts), "i"(R_E + (e & 15)));
}
template <int Y, int T> DI void sm_gap(AC& c) {
  if constexpr (SM.ex[T] >= 0) asm volatile("v_exp_f32 v%c0, v%c1" ::"i"(R_E + (SM.ex[T] & 15)), "i"(R_S + 32 * Y + SM.ex[T]));
  if constexpr (SM.nop[T] > 0) sm_op<Y, SM.op0[T]>();
  if constexpr (SM.nop[T] > 1) sm_op<Y, SM.op0[T] + 1>();
  if constexpr (SM.nop[T] > 2) sm_op<Y, SM.op0[T] + 2>();
  if constexpr (T == SM.last + 1) asm volatile("v_add_f32_e32 v%c0, v%c1, v%c2" ::"i"(R_TMP), "i"(R_TS), "i"(R_TS + 1));
  if constexpr (T == SM.last + 2) asm volatile("v_cmp_nle_f32_e64 %0, v%c1, v%c2" : "=s"(c.redo) : "i"(R_TMP), "i"(R_THR));
}
#define W_A0 1
template <int X, int MODE, int G> DI void gapA(AC& c) {
  constexpr bool FIRST = MODE == 1 || MODE == 2, NOQK = MODE == 3;
  constexpr int Xp = X ^ 1, kb = G >> 4, g = G & 15, qb = g >> 3, s = g & 7;
  if constexpr (NOQK && G < 48 && G % 3 == 0) {
    constexpr int f = G / 3;
    asm volatile("buffer_load_dwordx4 a[%c0:%c1], v122, s[88:91], s75 offen offset:%2 sc1" ::"i"(128 + 4 * f), "i"(131 + 4 * f), "i"((f & 3) * 1024) : "memory");
    if constexpr ((f & 3) == 3) asm volatile("s_addk_i32 s75, 4096");
  }
  if constexpr (G == 48) asm volatile("s_waitcnt vmcnt(%0) lgkmcnt(0)\n\ts_barrier" ::"i"(NOQK ? 16 : MODE == 2 ? 12 : MODE == 1 ? W_A0 : 0) : "memory");
  else if constexpr (g == 0 && !NOQK) asm volatile("s_waitcnt lgkmcnt(0)");
  if constexpr (!NOQK) {
    constexpr int r = 192 + 4 * ((kb & 1) * 8 + s), q = 128 + 4 * g;
    constexpr int d = R_S + 32 * X + 4 * (kb * 2 + qb), ci = s == 0 ? R_CI + 4 * (kb * 2 + qb) : d;
    if constexpr (FIRST && s == 0)
      asm volatile("v_mfma_f32_16x16x32_bf16 v[%c0:%c1], a[%c2:%c3], a[%c4:%c5], 0" ::"i"(d), "i"(d + 3), "i"(r), "i"(r + 3), "i"(q), "i"(q + 3));
    else
      asm volatile("v_mfma_f32_16x16x32_bf16 v[%c0:%c1], a[%c2:%c3], a[%c4:%c5], v[%c6:%c7]" ::"i"(d), "i"(d + 3), "i"(r), "i"(r + 3), "i"(q), "i"(q + 3),
                   "i"(ci), "i"(ci + 3));
    if constexpr (g < 8 && kb < 3) kread<X, (kb + 1) * 8 + g>();
    if constexpr (G == 54) asm volatile("s_add_u32 s96, s97, 0x1000000");
    if constexpr (G == 55) dma_m0<X, 0>();
    if constexpr (G >= 56 && !(G & 1)) dma_at<(G - 56) / 2>();
    if constexpr (G == 57) asm volatile("s_mov_b32 s97, s98\n\ts_lshr_b32 s100, s100, 1\n\ts_add_u32 s98, s98, s99");
    if constexpr (G == 59 && !FIRST)
      asm volatile("s_and_b32 s101, s100, 1\n\ts_or_b32 s101, s101, %1\n\ts_or_b32 %0, s101, %2" : "=s"(c.rare) : "s"((unsigned)c.redo), "s"((unsigned)(c.redo >> 32)));
    if constexpr (G == 59 && FIRST) asm volatile("s_and_b32 %0, s100, 1" : "=s"(c.rare));
  } else if constexpr (SM.nop[64 + G] > 0) asm volatile("s_nop 0");
  if constexpr (!FIRST && g < 8 && kb == 3) vread<Xp, g>();
  if constexpr (MODE == 2 && (G & 3) == 1) d_store<16 + G / 4>();
  if constexpr (FIRST) acc_zero2<G>();
  else sm_gap<Xp, 64 + G>(c);
  GAP();
}
template <int R> DI void rescale1(float alpha) {
  float t;
  asm volatile("v_accvgpr_read_b32 %0, a[%c2]\n\tv_mul_f32 %0, %0, %1\n\tv_accvgpr_write_b32 a[%c2], %0" : "=&v"(t) : "v"(alpha), "i"(R));
}
template <int... Rs> DI void rescale_all(const float (&alpha)[2], iseq<Rs...>) { (rescale1<Rs>(alpha[(Rs >> 2) & 1]), ...); }
template <bool MAX> DI float row_reduce(float v) {
  auto p = __builtin_amdgcn_permlane32_swap(__float_as_uint(v), __float_as_uint(v), false, false);
  v = MAX ? fmaxf(__uint_as_float(p[0]), __uint_as_float(p[1])) : __uint_as_float(p[0]) + __uint_as_float(p[1]);
  p = __builtin_amdgcn_permlane16_swap(__float_as_uint(v), __float_as_uint(v), false, false);
  return MAX ? fmaxf(__uint_as_float(p[0]), __uint_as_float(p[1])) : __uint_as_float(p[0]) + __uint_as_float(p[1]);
}
template <int Y, int QB> DI float row_max() {
  constexpr int r = R_S + 32 * Y + 4 * QB;
  float mx;
  asm volatile("v_max_f32 %0, v%c1, v%c2\n\tv_max3_f32 %0, %0, v%c3, v%c4" : "=&v"(mx) : "i"(r), "i"(r + 1), "i"(r + 2), "i"(r + 3));
  static_for([&](auto K) {
    constexpr int q = r + 8 * (K + 1);
    float t = mx;
    asm volatile("v_max3_f32 %0, %0, v%c1, v%c2\n\tv_max3_f32 %0, %0, v%c3, v%c4" : "+v"(t) : "i"(q), "i"(q + 1), "i"(q + 2), "i"(q + 3));
    mx = t;
  }, make_iseq<3>{});
  return row_reduce<true>(mx);
}
template <int R> DI void v_sub(float d) { asm volatile("v_sub_f32 v%c0, v%c0, %1" ::"i"(R), "v"(d)); }
template <int B> DI void s_shift(const float (&d)[2]) { static_for([&](auto E) { v_sub<B + E>(d[(E >> 2) & 1]); }, make_iseq<32>{}); }
DI void m_shift(const float (&d)[2]) {
  s_shift<R_CI>(d);
  v_sub<R_M>(-d[0]); v_sub<R_M + 1>(-d[1]);
}
DI void set_mask0(int wid) {
  const int mr = wid * 32 + __float_as_int(vget<89>());
  static_for([&](auto I) {
    constexpr int e = I, k = (e >> 3) * 16 - ((e >> 2) & 1) * 16 + (e & 3);
    const float x = vget<R_S + e>();
    vset<R_S + e>(k > mr ? -INFINITY : x);
  }, make_iseq<32>{});
}
template <int X> DI void sm_redo() {
  constexpr int Xp = X ^ 1;
  float d[2], dt[2], alpha[2], ts[2] = {0.f, 0.f};
  static_for([&](auto Q) {
    constexpr int qb = Q;
    const float pd = vget<R_PD + qb>();
    d[qb] = fmaxf(row_max<Xp, qb>() - pd, 0.f);
    dt[qb] = d[qb] + pd;
    vset<R_PD + qb>(d[qb]);
    alpha[qb] = __builtin_amdgcn_exp2f(-d[qb]);
    vset<R_RS + qb>(vget<R_RS + qb>() * alpha[qb]);
  }, make_iseq<2>{});
  vset<R_THR>(d[0] > 0.f || d[1] > 0.f ? -1.f : 4096.f);
  m_shift(d);
  static_for([&](auto K) {
    constexpr int k = K, qb = (k >> 1) & 1;
    const float e0 = __builtin_amdgcn_exp2f(vget<R_S + 32 * Xp + 2 * k>() - dt[qb]), e1 = __builtin_amdgcn_exp2f(vget<R_S + 32 * Xp + 2 * k + 1>() - dt[qb]);
    ts[qb] += e0; ts[qb] += e1;
    vset<p_reg(Xp, k)>(__uint_as_float(pk2(e0, e1)));
  }, make_iseq<16>{});
  vset<R_TS>(ts[0]); vset<R_TS + 1>(ts[1]);
  rescale_all(alpha, make_iseq<128>{});
  asm volatile("s_nop 1" ::: "memory");
  GAP();
}
template <int X, int MODE, int... Gs> DI void phaseA_(AC& c, iseq<Gs...>) { (gapA<X, MODE, Gs>(c), ...); }
template <int X, int MODE> DI void phaseA(AC& c) { phaseA_<X, MODE>(c, make_iseq<64>{}); }

#define PG 50
template <int X, bool SMX, int G> DI void gapB(AC& c) {
  constexpr int Xp = X ^ 1, blk = G >> 4, g = G & 15;
  if constexpr (g == 0) asm volatile("s_waitcnt lgkmcnt(0)");
  if constexpr (SMX && g == 7 && blk < 3) dma_m0<X, blk * 4 + 4>();
  constexpr int qb = (g >> 1) & 1, s = g & 1, r = 192 + 4 * ((blk & 1) * 8 + (g >> 2) * 2 + s), o = 4 * ((blk * 4 + (g >> 2)) * 2 + qb);
  constexpr int p = R_P + 16 * Xp + 4 * (s * 2 + qb);
  asm volatile("v_mfma_f32_16x16x32_bf16 a[%c0:%c1], a[%c2:%c3], v[%c4:%c5], a[%c0:%c1]" ::"i"(o), "i"(o + 3), "i"(r), "i"(r + 3), "i"(p), "i"(p + 3));
  if constexpr (SMX && (G == 1 || G == 2)) asm volatile("v_add_f32_e32 v%c0, v%c0, v%c1" ::"i"(R_RS + G - 1), "i"(R_TS + G - 1));
  if constexpr (g < 8) {
    if constexpr (blk < 3) vread<Xp, (blk + 1) * 8 + g>();
    else if constexpr (SMX) kread<Xp, g>();
  }
  if constexpr (SMX) {
    if constexpr (g >= 8 && !(g & 1) && blk < 3) dma_at<blk * 4 + 4 + (g - 8) / 2>();
    if constexpr (G == 3) asm volatile("s_lshr_b32 s77, s77, 1");
    if constexpr (G == 4) asm volatile("s_bitcmp1_b32 s77, 0");
    if constexpr (G == 5) asm volatile("s_cselect_b64 s[52:53], s[62:63], 0");
    if constexpr (G == 48) asm volatile(FL_ARM("s[52:53]"));
    if constexpr (G == PG) asm volatile(FL_POLL ::: "memory");
    sm_gap<X, G>(c);
  }
  GAP();
}
template <int X, int... Gs> DI void phaseB_(AC& c, iseq<Gs...>) { (gapB<X, true, Gs>(c), ...); }
template <int X> DI void phaseB(AC& c) { phaseB_<X>(c, make_iseq<64>{}); }
constexpr int sm_nex() { int n = 0; for (int t = 0; t < 64; ++t) n += SM.ex[t] >= 0; return n; }
DI void phaseB0(unsigned mode, int wid) {
  dma_m0<0, 4>();
  kreads<1>(make_iseq<8>{});
  dma_at<4>();
  asm volatile("s_nop 3");
  float d[2];
  if (mode & 2) {
    d[0] = vget<122>(); d[1] = vget<123>();
    dma_at<5>();
  } else {
    if (__builtin_expect(mode & 4, 0)) set_mask0(wid);
    d[0] = row_max<0, 0>(); GAP(); dma_at<5>();
    d[1] = row_max<0, 1>();
#pragma unroll
    for (int qb = 0; qb < 2; ++qb) if (!(d[qb] > -3.0e38f)) d[qb] = 0.f;
  }
  GAP();
  vset<R_M>(d[0]); vset<R_M + 1>(d[1]);
  dma_at<6>();
  const f32x2c nd[2] = {{-d[0], -d[0]}, {-d[1], -d[1]}};
  static_for([&](auto E) {
    constexpr int e = E;
    const f32x2c v = nd[(e >> 1) & 1];
    asm volatile("v_pk_add_f32 v[%c0:%c1], v[%c0:%c1], %4\n\tv_mov_b64 v[%c2:%c3], %4" ::"i"(R_S + 2 * e), "i"(R_S + 2 * e + 1), "i"(R_CI + 2 * e), "i"(R_CI + 2 * e + 1), "v"(v));
    if constexpr (e == 3) dma_at<7>();
    if constexpr (e == 6) dma_m0<0, 8>();
    if constexpr (e == 7 || e == 11 || e == 15) dma_at<8 + (e - 7) / 4>();
  }, make_iseq<16>{});
  static_assert(sm_nex() >= 16 && SM.op0[64] >= 3 * ((sm_nex() - 15) / 2) && SM.op0[64] <= 24);
  static_for([&](auto E) {
    constexpr int e = E;
    asm volatile("v_exp_f32 v%c0, v%c1" ::"i"(R_E + e), "i"(R_S + e));
    if constexpr (e == 3) dma_at<11>();
    if constexpr (e == 6) dma_m0<0, 12>();
    if constexpr (e == 7 || e == 11 || e == 15) dma_at<12 + (e - 7) / 4>();
  }, make_iseq<16>{});
  static_for([&](auto O) { sm_op<0, O>(); }, make_iseq<SM.op0[64]>{});
  dma_at<15>();
  static_for([&](auto E) { asm volatile("v_exp_f32 v%c0, v%c1" ::"i"(R_E + (int)E), "i"(R_S + 16 + (int)E)); }, make_iseq<sm_nex() - 16>{});
  asm volatile("buffer_store_dwordx2 v[%c0:%c1], v88, s[88:91], s76 offen sc1\n\ts_bitcmp1_b32 s77, 0\n\ts_cselect_b64 s[52:53], s[62:63], 0\n\t"
               FL_ARM("s[52:53]") "\n\t" FL_POLL ::"i"(R_M), "i"(R_M + 1) : "memory", "scc");
  GAP();
}
constexpr int T_ZO = 8;
#define NINF2(R) "v_mov_b64 v[" #R ":" #R "+1], v[122:123]\n\t"
#define NINF4(R) NINF2(R) NINF2(R + 2)
#define TRI(R) "v_cndmask_b32_e64 v[" #R "], v[" #R "], v122, s[48:49]\n\tv_cndmask_b32_e64 v[" #R "+1], v[" #R "+1], v122, s[50:51]\n\t" \
               "v_cndmask_b32_e64 v[" #R "+2], v[" #R "+2], v122, s[52:53]\n\tv_cndmask_b32_e64 v[" #R "+3], v[" #R "+3], v122, s[54:55]\n\t"
DI void ev_code() {
  asm volatile(
    "s_getpc_b64 s[82:83]\n\ts_add_u32 s82, s82, 12\n\ts_addc_u32 s83, s83, 0\n\ts_branch 9f\n\t"
    "s_bitcmp0_b32 s58, s56\n\ts_cbranch_scc1 1f\n\t" FL_SET("s70") "\n\t" FL_SET("s71") "\n"
    "1:\n\ts_bitcmp0_b32 s57, s56\n\ts_cbranch_scc1 2f\n\t" FL_CHECK("s[62:63]") "\n\t"
    "s_add_u32 s98, s98, s68\n\ts_mov_b32 s68, s69\n\ts_mov_b32 s69, 0\n\ts_mov_b64 s[62:63], s[64:65]\n\ts_mov_b64 s[64:65], s[66:67]\n"
    "2:\n\ts_bitcmp0_b32 s59, s56\n\ts_cbranch_scc1 3f\n\ts_mov_b32 s98, s72\n\ts_bitcmp0_b32 s73, 0\n\ts_cbranch_scc1 3f\n\t"
    "v_lshrrev_b32 v90, 7, v86\n\tv_and_b32 v91, 6, v90\n\tv_lshlrev_b32 v91, 4, v91\n\tv_and_b32 v92, 0x70, v86\n\tv_xor_b32 v91, v91, v92\n\t"
    "s_movk_i32 s48, 896\n\tv_mad_u32_u24 v94, v90, s48, v91\n\ts_mov_b64 s[92:93], s[80:81]\n\ts_mov_b32 s94, 229376\n\ts_movk_i32 s99, 128\n\t"
    "v_add_u32 v90, 28672, v94\n\tv_add_u32 v91, 34816, v94\n\tv_add_u32 v92, 40960, v94\n\tv_add_u32 v93, 47104, v94\n\t"
    "v_add_u32 v95, 6144, v94\n\tv_add_u32 v126, 12288, v94\n\tv_add_u32 v127, 18432, v94\n"
    "3:\n\ts_bitcmp0_b32 s60, s56\n\ts_cbranch_scc1 4f\n\t"
    "v_readlane_b32 s54, v78, %0\n\ts_add_u32 m0, s78, 0x10000\n\t"
    "v_lshlrev_b32 v121, 9, v84\n\tv_and_b32 v121, 0x38000, v121\n\tv_lshrrev_b32 v122, 9, v84\n\tv_lshl_add_u32 v121, v122, 12, v121\n\t"
    "v_lshrrev_b32 v122, 3, v84\n\tv_xor_b32 v122, v122, v84\n\tv_and_or_b32 v121, v122, 48, v121\n\t"
    "buffer_load_dwordx4 v121, s[88:91], s54 offen sc1 lds\n\ts_addk_i32 s54, 7168\n\t"
    "buffer_load_dwordx4 v121, s[88:91], s54 offen offset:1024 sc1 lds\n\ts_addk_i32 s54, 7168\n\t"
    "buffer_load_dwordx4 v121, s[88:91], s54 offen offset:2048 sc1 lds\n\ts_addk_i32 s54, 7168\n\t"
    "buffer_load_dwordx4 v121, s[88:91], s54 offen offset:3072 sc1 lds\n"
    "4:\n\ts_bitcmp0_b32 s61, s56\n\ts_cbranch_scc1 6f\n\t"
    "s_sub_u32 s74, s74, 64\n\ts_cmp_ge_i32 s74, 0\n\ts_cbranch_scc1 6f\n\t"
    "v_cmp_gt_i32 s[48:49], 0, v89\n\tv_cmp_gt_i32 s[50:51], 1, v89\n\tv_cmp_gt_i32 s[52:53], 2, v89\n\tv_cmp_gt_i32 s[54:55], 3, v89\n\t"
    "v_mov_b32 v122, 0xff800000\n\tv_mov_b32 v123, 0xff800000\n\t" NINF4(248)
    "s_cmp_eq_i32 s74, -32\n\ts_cbranch_scc0 5f\n\t" TRI(240) TRI(252) "s_setpc_b64 s[86:87]\n"
    "5:\n\t" NINF4(240) NINF4(244) NINF4(252) NINF4(232) TRI(224) TRI(236)
    "s_cmp_eq_i32 s74, -64\n\ts_cbranch_scc1 6f\n\t" NINF4(224) NINF4(228) NINF4(236)
    "6:\n\ts_setpc_b64 s[86:87]\n9:"
    ::"i"(T_ZO) : "scc");
}
DI void ev_call(int i) { asm volatile("s_mov_b32 s56, %0\n\ts_swappc_b64 s[86:87], s[82:83]" ::"s"(i) : "vcc", "scc", "memory"); GAP(); }

#define ACLOB_ALL "a0","a1","a2","a3","a4","a5","a6","a7","a8","a9","a10","a11","a12","a13","a14","a15","a16","a17","a18","a19","a20","a21","a22","a23","a24","a25","a26","a27","a28","a29","a30","a31","a32","a33","a34","a35","a36","a37","a38","a39","a40","a41","a42","a43","a44","a45","a46","a47","a48","a49","a50","a51","a52","a53","a54","a55","a56","a57","a58","a59","a60","a61","a62","a63","a64","a65","a66","a67","a68","a69","a70","a71","a72","a73","a74","a75","a76","a77","a78","a79","a80","a81","a82","a83","a84","a85","a86","a87","a88","a89","a90","a91","a92","a93","a94","a95","a96","a97","a98","a99","a100","a101","a102","a103","a104","a105","a106","a107","a108","a109","a110","a111","a112","a113","a114","a115","a116","a117","a118","a119","a120","a121","a122","a123","a124","a125","a126","a127","a128","a129","a130","a131","a132","a133","a134","a135","a136","a137","a138","a139","a140","a141","a142","a143","a144","a145","a146","a147","a148","a149","a150","a151","a152","a153","a154","a155","a156","a157","a158","a159","a160","a161","a162","a163","a164","a165","a166","a167","a168","a169","a170","a171","a172","a173","a174","a175","a176","a177","a178","a179","a180","a181","a182","a183","a184","a185","a186","a187","a188","a189","a190","a191","a192","a193","a194","a195","a196","a197","a198","a199","a200","a201","a202","a203","a204","a205","a206","a207","a208","a209","a210","a211","a212","a213","a214","a215","a216","a217","a218","a219","a220","a221","a222","a223","a224","a225","a226","a227","a228","a229","a230","a231","a232","a233","a234","a235","a236","a237","a238","a239","a240","a241","a242","a243","a244","a245","a246","a247","a248","a249","a250","a251","a252","a253","a254","a255"

struct EP { float fa[2], fb[2]; };
template <int I, int E, int MODE> DI void epi_pair(const EP& p) {
  constexpr int qb = I & 1, r = 16 * (I >> 1) + 4 * qb + (E >> 1) * 8 + (E & 1) * 2, t = 96 + 2 * E, w = 128 + 4 * I + E;
  if constexpr (MODE == 2) asm volatile("v_accvgpr_read_b32 v106, a[%c0]" ::"i"(w));
  asm volatile("v_accvgpr_read_b32 v%c0, a[%c2]\n\tv_accvgpr_read_b32 v%c1, a[%c3]" ::"i"(t), "i"(t + 1), "i"(r), "i"(r + 1));
  if constexpr (MODE) asm volatile("v_mul_f32 v%c0, v%c0, %2\n\tv_mul_f32 v%c1, v%c1, %2" ::"i"(t), "i"(t + 1), "v"(p.fa[qb]));
  if constexpr (MODE == 2)
    asm volatile("v_lshlrev_b32 v104, 16, v106\n\tv_and_b32 v105, 0xffff0000, v106\n\tv_fmac_f32 v%c0, v104, %2\n\tv_fmac_f32 v%c1, v105, %2"
                 ::"i"(t), "i"(t + 1), "v"(p.fb[qb]));
  asm volatile("v_cvt_pk_bf16_f32 v%c0, v%c1, v%c2" ::"i"(w), "i"(t), "i"(t + 1));
}
template <int I> DI void epi_store() {
  asm volatile("buffer_store_dwordx4 v[%c0:%c1], v84, s[88:91], s101 offen offset:%2 sc1" ::"i"(128 + 4 * I), "i"(131 + 4 * I), "i"((I & 3) * 1024) : "memory");
  if constexpr ((I & 3) == 3) asm volatile("s_addk_i32 s101, 4096");
}
constexpr int R_TB = 107, R_VS = 111;
template <int HF, int R> DI void p_read() {
  constexpr int mb = R >> 4, r = 64 * HF + 2 * R;
  asm volatile("ds_read_b64_tr_b16 a[%c1:%c2], v%c0 offset:%c3" ::"i"(R_TB + 2 * HF + mb), "i"(r), "i"(r + 1), "i"((R & 15) * 2048));
}
template <int QB, int E> DI void p_cvt() {
  asm volatile("v_cvt_pk_bf16_f32 v%c0, v%c1, v%c2" ::"i"(208 + 4 * QB + E), "i"(192 + 8 * QB + 2 * E), "i"(193 + 8 * QB + 2 * E));
}
template <int QB, int PR> DI void p_store() {
  asm volatile("buffer_store_dwordx4 v[%c0:%c1], v%c2, s[88:91], s96 offen offset:%3 sc1" ::"i"(208 + 4 * QB), "i"(211 + 4 * QB), "i"(R_VS + QB),
               "i"(PR * 128) : "memory");
}
constexpr int p_dma(int HF, int G) { return G == 3 && !HF ? 0 : G == 11 ? 1 - HF : G == 15 ? 2 - HF : G == 19 ? 3 - HF : G == 27 && HF ? 3 : -1; }
template <int HF, int G> DI void p_gap() {
  constexpr int ch = G >> 3, t = G & 7, a = 64 * HF + 32 * (ch & 1) + 4 * t, b = 128 + 4 * (2 * t + (ch >> 1)), d = 192 + 4 * ch;
  if constexpr (t == 0)
    asm volatile("v_mfma_f32_16x16x32_bf16 v[%c0:%c1], a[%c2:%c3], v[%c4:%c5], 0" ::"i"(d), "i"(d + 3), "i"(a), "i"(a + 3), "i"(b), "i"(b + 3));
  else
    asm volatile("v_mfma_f32_16x16x32_bf16 v[%c0:%c1], a[%c2:%c3], v[%c4:%c5], v[%c0:%c1]" ::"i"(d), "i"(d + 3), "i"(a), "i"(a + 3), "i"(b), "i"(b + 3));
  p_read<HF ^ 1, G>();
  if constexpr (G == 0) asm volatile("v_xor_b32 v%c0, s100, v%c0" ::"i"(R_TB + 2 * HF + 1));
  if constexpr (G == 17) asm volatile("v_xor_b32 v%c0, s100, v%c0" ::"i"(R_TB + 2 * (HF ^ 1)));
  if constexpr (G >= 4 && G <= 8 && G != 7) p_cvt<1, (G < 7 ? G - 4 : 3)>();
  if constexpr (G == 24 || (G == 7 && HF == 0)) asm volatile("s_waitcnt lgkmcnt(8)");
  if constexpr (G == 7 && HF == 1) asm volatile("s_waitcnt vmcnt(13) lgkmcnt(8)\n\ts_barrier" ::: "memory");
  if constexpr (G == 10) p_store<1, HF ^ 1>();
  if constexpr (G >= 20 && G < 24) p_cvt<0, G - 20>();
  if constexpr (G == 25) p_store<0, HF>();
  if constexpr (p_dma(HF, G) >= 0) dma_at<(HF ? 8 : 12) + p_dma(HF, G)>();
  if constexpr (HF == 0 && G == 2) asm volatile("s_bitset0_b32 m0, 12");
  if constexpr (HF == 0 && G == 13) asm volatile("s_add_u32 s96, s96, 0x40000");
  if constexpr (HF == 0 && G == 26) asm volatile("s_add_u32 s98, s98, 128");
  if constexpr (HF == 0 && G == 27) asm volatile("s_cmp_lt_u32 s98, 0x380");
  if constexpr (HF == 0 && G == 28) asm volatile("s_cselect_b32 s99, s99, 0x31000");
  if constexpr (HF == 1 && G == 1) asm volatile("s_xor_b32 s100, s100, 0x10000");
  if constexpr (HF == 1 && G == 2) asm volatile("s_xor_b32 s97, s97, s100");
  if constexpr (HF == 1 && G == 9) asm volatile("s_or_b32 m0, s97, s99");
  if constexpr (HF == 1 && G == 30) asm volatile("s_cmp_lt_u32 s98, 0x500");
  GAP();
}
template <int HF, int... Gs> DI void p_half(iseq<Gs...>) { (p_gap<HF, Gs>(), ...); }
constexpr int f_dma(int G) { return G == 2 ? 0 : G == 5 ? 1 : G == 8 ? 2 : G >= 12 && G <= 36 && G % 6 == 0 ? 1 + G / 6 : G >= 50 && G <= 68 && G % 6 == 2 ? G / 6 : -1; }
constexpr int f_ndma(int G) { int n = 0; for (int g = 0; g < G; ++g) n += f_dma(g) >= 0; return n; }
static_assert(f_ndma(74) == 12);
template <int MODE, int G> DI void gapF(AC& c, const EP& p) {
  if constexpr (MODE && G == 48) asm volatile("s_waitcnt lgkmcnt(0)\n\ts_barrier" ::: "memory");
  if constexpr (G < 64) gapB<0, false, G>(c);
  if constexpr (MODE == 2 && G >= 10 && (G - 10) % 16 == 0) asm volatile("s_waitcnt vmcnt(%0)" ::"i"(12 - (G - 10) / 4 + f_ndma(G)) : "memory");
  if constexpr (G >= 10) epi_pair<(G - 10) / 4, (G - 10) & 3, MODE>(p);
  if constexpr (MODE == 3 && G < 64 && (G & 1)) {
    constexpr int db = G >> 2, qb = (G >> 1) & 1, o = 4 * (db * 2 + qb), a = 224 + 4 * (db & 1), b = 128 + 4 * ((db >> 1) * 2 + qb);
    if constexpr ((G & 7) == 1) asm volatile("s_waitcnt vmcnt(%0)" ::"i"(14 - (G >> 2) + f_ndma(G)) : "memory");
    asm volatile("v_mfma_f32_16x16x32_bf16 a[%c0:%c1], v[%c2:%c3], a[%c4:%c5], a[%c0:%c1]" ::"i"(o), "i"(o + 3), "i"(a), "i"(a + 3), "i"(b), "i"(b + 3));
  }
  if constexpr (MODE == 0) {
    if constexpr (G >= 14 && ((G - 14) & 3) == 0) epi_store<(G - 14) / 4>();
    if constexpr (G == 73) epi_store<15>();
  } else {
    if constexpr (G == 0) asm volatile("s_or_b32 m0, s78, 0x1000\n\ts_movk_i32 s98, 256");
    if constexpr (G == 15) asm volatile("s_bitset0_b32 m0, 12");
    if constexpr (G == 48) asm volatile("s_or_b32 m0, s79, 0x1000\n\ts_movk_i32 s98, 384");
    if constexpr (f_dma(G) >= 0) dma_at<8 + (f_dma(G) & 7)>();
    if constexpr (G == 73) asm volatile("s_waitcnt vmcnt(4) lgkmcnt(0)" ::: "memory");
    if constexpr (G >= 48 && G < 64) { p_read<0, 2 * (G - 48)>(); p_read<0, 2 * (G - 48) + 1>(); }
  }
  GAP();
}
template <int MODE, int... Gs> DI void phaseF(AC& c, const EP& p, iseq<Gs...>) { (gapF<MODE, Gs>(c, p), ...); }
template <int U, int K> DI void r_load(unsigned vo, unsigned so) {
  asm volatile("buffer_load_dwordx4 a[%c0:%c1], %2, s[88:91], %3 offen offset:%4 sc1" ::"i"(64 * U + 4 * K), "i"(64 * U + 4 * K + 3), "v"(vo),
               "s"(so + (K >> 2) * 4096), "i"((K & 1) * 512 + ((K >> 1) & 1) * 2048) : "memory");
}
template <int U, int K> DI void r_sum() {
  constexpr int d = 100 + 4 * (K & 1), b = 64 * U + 4 * K;
  if constexpr (K < 2) asm volatile("v_mfma_f32_16x16x32_bf16 v[%c0:%c1], v[96:99], a[%c2:%c3], 0" ::"i"(d), "i"(d + 3), "i"(b), "i"(b + 3));
  else asm volatile("v_mfma_f32_16x16x32_bf16 v[%c0:%c1], v[96:99], a[%c2:%c3], v[%c0:%c1]" ::"i"(d), "i"(d + 3), "i"(b), "i"(b + 3));
}
template <int U> DI void r_fin(unsigned vo, __amdgpu_buffer_rsrc_t srd, unsigned so) {
  asm volatile("" : "+s"(srd), "+s"(so));
  static_for([&](auto K) { r_sum<U, K>(); }, make_iseq<16>{});
  asm volatile("s_nop 7");
  static_for([&](auto E) { asm volatile("v_cvt_pk_bf16_f32 v%c0, v%c1, v%c2" ::"i"(116 + 4 * U + E), "i"(100 + 2 * E), "i"(101 + 2 * E)); }, make_iseq<4>{});
  asm volatile("buffer_store_dwordx4 v[%c3:%c4], %0, %1, %2 offen" OUTSCS ::"v"(vo), "s"(srd), "s"(so), "i"(116 + 4 * U), "i"(119 + 4 * U) : "memory");
}

#define CMAX 12
#define PREPX false
#define NYT 16
static_assert(NYT <= 16 && NYT >= 12 && !(NYT & 1));
enum { T_Q0, T_K0, T_OQ, T_OG, T_FPREP, T_FQ, T_FG, T_FS,   T_NPRE = 10, T_NST = 12, T_TAB = 14, T_SLAB = 16, T_PART, T_IT = 18, T_ITN = 23 };
struct TRow { unsigned v[64]; };
constexpr u64 kvmask(int m0, int m1) { u64 n = 0; for (int m = m0; m <= m1; ++m) n |= 0x101ull << m; return n; }
constexpr u64 k_order(bool isY, int rr) {
  const int cls = (rr + (rr >> 2)) & 3;
  return !isY ? (cls == 0 ? 0xfedcb9875a463210ull : cls == 1 ? 0xefadc6982b574310ull : cls == 2 ? 0xfebad7693c285410ull : 0xefcba8764d392510ull)
              : (cls == 0 ? 0xfedcb987543a6210ull : cls == 1 ? 0xefadc698254b7310ull : cls == 2 ? 0xfebad769325c8410ull : 0xefcba876432d9510ull);
}
constexpr TRow t_row(int r, int wid, int j) {
  TRow t{};
  const int rr = r & 15, hsel = rr & 1, pj = rr >> 1, kv = pj & 1, h = j + 8 * hsel, u = hsel * 8 + j;
  const bool isY = r >= 16, solo = pj == 7;
  const int mt = (rr >> 2) + (isY ? 4 : 0), g = j * 128, NY = solo ? 18 : NYT;
  const int nx0 = 32 - 2 * pj - NY, cx = nx0 < CMAX ? nx0 : CMAX, ex = nx0 - cx, qbF = isY ? 15 - pj : pj;
  const unsigned oQ = (unsigned)h << 20, oK = oQ + O_K, oP = O_P + (h * 8 + pj) * PSTR;
  if (wid == 4) {
    for (int l = 0; l < 64; ++l)
      t.v[l] = fo(l < 16 ? g + hsel * 16 + l : l < 24 ? g + 16 + hsel * 8 + l : l == 24 ? g + 48 + hsel * 8 + pj : l < 32 ? g + 64 + hsel * 8 + pj :
                  l < 48 ? 2304 + pj * 16 + l - 32 : 2048 + pj * 16 + l - 48);
    return t;
  }
  auto put64 = [&](int i, u64 x) { t.v[i] = (unsigned)x; t.v[i + 1] = (unsigned)(x >> 32); };
  t.v[T_Q0] = oQ + ((solo && !isY ? pj : 15 - pj) * 128 + wid * 32) * 512;
  t.v[T_K0] = oK + (isY ? cx : 0) * 32768;
  t.v[T_OQ] = oQ + pj * 131072 + (wid >> 1) * 65536 + (wid & 1) * 4096;
  t.v[T_OG] = oK + kv * TSB + mt * 131072 + (wid >> 1) * 65536 + (wid & 1) * (kv ? 8192 : 4096);
  t.v[T_FPREP] = fo(2048 + pj * 16 + u);
  t.v[T_FQ] = fo(g + 32 + hsel * 8 + pj);
  t.v[T_FG] = fo(g + hsel * 16 + kv * 8 + mt);
  t.v[T_FS] = fo(1024 + (qbF * 4 + wid) * 16 + j * 2 + hsel);
  t.v[T_ZO] = O_Z + pj * 64 * SEQ * 2 + (u * 4 + wid) * 64;
  const u64 own = 0x1ull << (kv * 8 + mt), needQ = isY ? 1ull << (16 + ((15 - pj) >> 1)) : (1ull << (16 + (pj >> 1))) | (1ull << (16 + ((15 - pj) >> 1)));
  put64(T_NPRE, isY ? needQ | (1ull << (cx >> 2)) | (1ull << ((cx + 1) >> 2)) : solo ? needQ : needQ | 0xffffull << 48);
  put64(T_NST, isY ? kvmask(cx >> 2, 3) | (solo ? 0 : 1ull << 25) : kvmask(0, solo ? 3 : 0));
  put64(T_TAB, k_order(isY, rr));
  t.v[T_SLAB] = O_S + qbF * 0x200000 + wid * 65536 + (j * 2 + hsel) * 1024;
  t.v[T_PART] = oP;
  for (int it = 0; it < 2; ++it) {
    unsigned* p = t.v + T_IT + it * T_ITN;
    const bool last = isY || it == 1;
    const int n = isY ? NY : it ? 2 * pj + 2 : nx0 < 2 ? 2 : nx0;
    const u64 need2 = solo ? 0 : kvmask(0, (2 * pj + 1) >> 2) | (0xffffull << 32);
    u64 q[3] = {0, 0, 0};
    unsigned jmp[2] = {0, 0};
    if (isY) {
      p[0] = (1u << (14 - cx)) | (solo ? 0 : 1u << (n - 2));
      q[0] = kvmask(4, (31 - 2 * pj) >> 2) & ~own; jmp[0] = ex * 32768; q[1] = solo ? 0 : 1ull << 24;
      p[1] = 2; p[13] = t.v[T_FG]; p[14] = fo(g + 96 + r);
    } else if (it == 0) {
      p[0] = 2u | (ex ? 1u << (cx - 2) : 0);
      q[0] = kvmask(0, cx > 0 ? (cx - 1) >> 2 : 0);
      if (ex) { q[1] = kvmask(4, 4 + ((ex - 1) >> 2)); jmp[1] = (16 - cx) * 32768; q[2] = need2; } else q[1] = need2;
      p[1] = 2; p[13] = fo(2304 + pj * 16 + u) + j * (8 * 4352 - 8); p[14] = fo(g + 64 + hsel * 8 + pj);
    } else {
      p[0] = p[1] = solo ? 0 : 1;
      q[0] = need2; p[13] = fo(g + 48 + hsel * 8 + pj); p[14] = fo(g + 96 + r);
    }
    p[2] = 1u << (n - 2);
    p[3] = isY || !it || solo ? 0 : n > 2 ? 1u << (n - 3) : 1u;
    p[4] = !last ? 0 : n > 2 ? 3u << (n - 3) : 1u << 31;
    for (int k = 0; k < 3; ++k) { p[5 + 2 * k] = (unsigned)q[k]; p[6 + 2 * k] = (unsigned)(q[k] >> 32); }
    p[11] = jmp[0]; p[12] = jmp[1];
    p[15] = oK;
    p[16] = (last ? 1 : 0) | (isY && !solo ? 2 : 0) | (n == 2 ? 4 : 0) | n << 8;
    p[17] = wid * 32 - (n == 2 ? 64 : 0);
    p[18] = oQ + (pj * 128 + wid * 32) * 512;
    p[19] = 0;
    p[20] = (p[0] | (last ? 0 : 1u << n)) >> 1;
    p[21] = oK + (isY ? cx : 0) * 32768;
    p[22] = ((p[0] | p[1] | p[2] | p[3] | p[4]) & 0x7fffffffu) << 1;
  }
  return t;
}
template <int I> constexpr TRow t_base = I < 160 ? t_row(I / 5, I % 5, 0) : [] {
  TRow a = t_row(0, (I - 160) * 4, 0), b = t_row(0, (I - 160) * 4, 1);
  for (int l = 0; l < 64; ++l) a.v[l] = b.v[l] - a.v[l];
  return a;
}();
template <int I> constexpr bool t_linear = [] {
  for (int j = 1; j < 8; ++j) {
    const TRow b = t_row(I / 5, I % 5, j);
    for (int l = 0; l < 64; ++l) if (b.v[l] != t_base<I>.v[l] + j * t_base<160 + (I % 5) / 4>.v[l]) return false;
  }
  return true;
}();
struct Tab { TRow row[162]; };
template <int... I> constexpr Tab t_make(iseq<I...>) {
  static_assert((t_linear<(I < 160 ? I : 0)> && ...));
  return Tab{{t_base<I>...}};
}
__device__ const Tab g_tab = t_make(make_iseq<162>{});

typedef __attribute__((ext_vector_type(16))) unsigned u32x16;
template <class T, int I> DI T* arg_late() {
  unsigned lo, hi;
  asm volatile("v_readlane_b32 %0, v77, %2\n\tv_readlane_b32 %1, v77, %3\n\ts_nop 4" : "=s"(lo), "=s"(hi) : "i"(2 * I), "i"(2 * I + 1));
  return (T*)(((u64)hi << 32) | lo);
}
__global__ __launch_bounds__(256, 1) __attribute__((amdgpu_num_vgpr(77), amdgpu_num_sgpr(56))) void fused_kernel(
    const bf16* ckv_, const bf16* wuk_, const bf16* wuv_, const bf16* x_, const bf16* wq_, bf16* ws_, const bf16* ak_, const bf16* av_,
    const bf16* ash_, const bf16* assmk_, const bf16* assmD_, const bf16* wo_, const bf16* aq_, bf16* out_) {
  extern __shared__ __attribute__((aligned(16))) char lds[];
  const void* kap = (const void*)__builtin_amdgcn_kernarg_segment_ptr();
  u32x16 ka; u64 ka8;
  asm volatile("at_code_begin:\n\ts_load_dwordx16 %0, %2, 0\n\ts_load_dwordx2 %1, %2, 0x40" : "=&s"(ka), "=&s"(ka8) : "s"(kap) : ACLOB_ALL, "v255", "s101");
  GAP();
  const int tid = threadIdx.x, lane = tid & 63;
  const int wid = __builtin_amdgcn_readfirstlane(tid >> 6);
  const int b = blockIdx.x, j = b & 7, r = b >> 3, rr = r & 15, hsel = rr & 1, pj = rr >> 1, kv = pj & 1;
  const bool isY = r >= 16, solo = pj == 7, doP = isY != PREPX && !solo, doA = !isY && !solo;
  const int h = j + 8 * hsel, mt = (rr >> 2) + (isY ? 4 : 0), pu = (hsel * 8 + j) * 4 + wid;
  constexpr unsigned m0d = 131072 + 24576;
  const unsigned lA = isY ? 65536 : 0, lW = lA ^ 65536, po = isY ? 0 : 32768;
  const unsigned mp[4] = {lA + wid * 1024 + po, lW + wid * 1024 + po, lA + wid * 1024 + (po ^ 32768), lW + wid * 1024 + (po ^ 32768)};
  {
    const int lda = isY ? DA : DC;
    const unsigned ra = wid * 8 + (lane >> 3), sa = ((lane & 7) ^ (((wid & 1) * 4 + (lane >> 4)) & 7)) * 16;
    const unsigned vbA = ra * (lda * 2) + sa, vbB = (4 * wid + (lane >> 4)) * LDW * 2 + 16 * ((lane & 15) ^ (((lane >> 4) << 2) | wid));
    vsetu<92>(vbA); vsetu<93>(vbB); vsetu<90>(vbA + 128); vsetu<91>(vbB + 0x80000); vsetu<94>(ra * (DC * 2) + sa);
    static_for([&](auto J) {
      sset<64 + J>((J + 1) * 64 * lda);
      asm volatile("s_mov_b32 s%c0, %1" ::"i"(71 + J), "i"((((J + 1) >> 2) * 32 + 16 * ((J + 1) & 1)) * LDW * 2 + (((J + 1) >> 1) & 1) * 256));
    }, make_iseq<7>{});
    sset<78>(lA + wid * 1024); sset<79>(lW + wid * 1024);
    GAP();
  }
  asm volatile("s_waitcnt lgkmcnt(0)" : "+s"(ka), "+s"(ka8));
  auto arg = [&](int i) { return (const bf16*)(((u64)ka[2 * i + 1] << 32) | ka[2 * i]); };
  const bf16 *ckv = arg(0), *wuk = arg(1), *wuv = arg(2), *x = arg(3), *wq = arg(4), *ak = arg(6), *av = arg(7), *ash = (const bf16*)ka8;
  bf16* Qb = (bf16*)arg(5);
  KST(0, 0);
  bf16* xqT = (bf16*)((char*)Qb + O_X);
  const bf16* gA = ckv + (size_t)mt * 256 * DC;
  const bf16* gW = (kv ? wuv : wuk) + h * 256;
  unsigned dv = 0;
  {
    GM g;
    PR pr;
    if (doP) prep_load(pr, tid, pj, hsel * 8 + j, ak, av, ash);
    srd_set<56>(isY ? x + (size_t)pj * 256 * DA : gA, isY ? 256 * DA * 2 : 256 * DC * 2);
    srd_set<60>(isY ? wq + h * 256 : gW, isY ? (DA - 1) * LDW * 2 + 512 : (DC - 1) * LDW * 2 + 512);
    g_pro<0>(mp, make_iseq<12>{});
    srd_set<88>(Qb, WSB);
    {
      const unsigned* tp = g_tab.row[r * 5].v;
      asm volatile("buffer_load_dwordx2 v[120:121], off, s[88:91], %0 sc1\n\tglobal_load_dword v78, %1, %3\n\tglobal_load_dword v79, %2, %3 offset:1024\n\t"
                   "global_load_dword v118, %2, %4\n\tglobal_load_dword v119, %2, %4 offset:256\n\tglobal_load_dword v77, %5, %6"
                   ::"s"(fo(6144)), "v"(tid * 4), "v"(lane * 4), "s"(tp), "s"(g_tab.row[160].v), "v"((lane < 27 ? lane : 27) * 4), "s"(kap) : "memory");
    }
    if (doP) {
      asm volatile("s_waitcnt vmcnt(18)" ::: "memory");
#pragma unroll
      for (int q = 0; q < 7; ++q) asm volatile("" : "+v"(pr.kk[q]));
#pragma unroll
      for (int q = 0; q < 4; ++q) asm volatile("" : "+v"(pr.vv[q]));
      static_for([&](auto Ic) __attribute__((always_inline)) {
        constexpr int I = Ic;
        g_pro<12 + 4 * I>(mp, make_iseq<4>{});
        GAP();
        prep_math<I>(pr, tid, lds + 131072);
        GAP();
      }, make_iseq<4>{});
      asm volatile("s_waitcnt lgkmcnt(0)\n\ts_barrier" ::: "memory");
      prep_store(tid, pj, hsel * 8 + j, xqT, lds + 131072);
    } else g_pro<12>(mp, make_iseq<4>{});
    GAP();
    CODE_TOUCH("at_code_begin", "pf_code_end", r * 4 + wid, 128u, 8, m0d);
    {
      const int fr = lane & 15, fq = lane >> 4, q = (lane & 15) >> 2, p = lane & 3;
#pragma unroll
      for (int k2 = 0; k2 < 2; ++k2)
        g.aA[k2] = lA + (wid >> 1) * 16384 + (fr >> 3) * 1024 + (fr & 7) * 128 + (((4 * k2 + fq) ^ (fr >> 1)) * 16);
#pragma unroll
      for (int hf = 0; hf < 2; ++hf)
#pragma unroll
        for (int nf = 0; nf < 8; ++nf)
          g.aB[hf][nf] = lW + (wid & 1) * 8192 + off_b(8 * fq + 4 * hf + q, nf * 2 + (p >> 1)) + 8 * (p & 1);
      asm volatile("" : "+v"(g.aA[0]), "+v"(g.aA[1]));
#pragma unroll
      for (int nf = 0; nf < 8; ++nf) asm volatile("" : "+v"(g.aB[0][nf]), "+v"(g.aB[1][nf]));
      vsetu<81>(tid == 0 ? 0u : 0x80000000u);
      vsetu<84>(lane * 16); vsetu<85>(lane * 16 + 65536); vsetu<86>(wid * 8192 + lane * 16); vsetu<87>(wid * 8192 + lane * 16 + 4096);
      vsetu<88>(65536 + wid * 256 + fr * 16); vsetu<89>(fr - 4 * fq);
      asm volatile("s_mov_b64 s[92:93], s[88:89]\n\ts_mov_b64 s[94:95], s[90:91]");
    }
    if (doP) asm volatile("s_waitcnt vmcnt(13)" ::: "memory");
    else {
      g_pro<16>(mp, make_iseq<12>{});
      asm volatile("s_waitcnt vmcnt(8)" ::: "memory");
    }
    asm volatile("s_barrier" ::: "memory");
    if (isY) g_read0<0>(g, make_iseq<16>{});
    else g_read0<1>(g, make_iseq<16>{});
    GAP();
    {
      u64 z = (((u64)__float_as_uint(vget<121>()) << 32) | __float_as_uint(vget<120>())) + 0x9e3779b97f4a7c15ull;
      z = (z ^ (z >> 30)) * 0xbf58476d1ce4e5b9ull; z = (z ^ (z >> 27)) * 0x94d049bb133111ebull; z ^= z >> 31;
      vsetu<82>((unsigned)z); vsetu<83>((unsigned)(z >> 32));
      asm volatile("v_mad_u32_u24 v78, v118, %0, v78\n\tv_mad_u32_u24 v79, v119, %0, v79\n\tv_readfirstlane_b32 s84, v82\n\tv_readfirstlane_b32 s85, v83\n\t"
                   "v_readlane_b32 s80, v78, %1\n\tv_readlane_b32 s81, v78, %2" ::"s"(j), "i"((int)T_TAB), "i"(T_TAB + 1));
      if (isY) asm volatile("s_mov_b64 s[80:81], 0x6543210");
      asm volatile("s_bfe_u32 s82, s80, 0x40008\n\ts_lshr_b64 s[80:81], s[80:81], 12\n\tv_lshl_add_u32 v91, s82, 19, v93" ::: "scc");
    }
    asm volatile("s_waitcnt lgkmcnt(0)\n\ts_barrier" ::: "memory");
    g_pro<28>(mp, make_iseq<4>{});
    GAP();
    if (isY) {
      g_tile<false, 0, 1>(g);
      g_loop<false, true>(g, PREPX ? 0x20u : 0x22u, [&](int t) __attribute__((always_inline)) {
        if (t == 1) fl_set<T_FPREP>();
        else if (t == 4) { tload<80, T_TAB>(); tload<81, T_TAB + 1>(); srd_set<60>(gW, (DC - 1) * LDW * 2 + 512); }
        else {
          srd_set<56>(gA, 256 * DC * 2);
          asm volatile("v_mov_b32 v92, v94");
          static_for([&](auto J) { asm volatile("s_mov_b32 s%c0, %1" ::"i"(64 + J), "i"((J + 1) * 64 * DC)); }, make_iseq<7>{});
        }
      });
      tload<101, T_OQ>();
      asm volatile("v_mov_b32 v112, v84\n\tv_add_u32 v113, 8192, v84");
      GAP();
      g_tile<false, 0, 2>(g);
      KST(0, 3);
    }
#define TT 3
    auto kvtile = [&](auto TRc) __attribute__((always_inline)) {
      constexpr bool TR = TRc;
      if (isY) g_tile<TR, 1, 4>(g);
      else g_tile<TR, 1, 1>(g);
      g_loop<TR, false>(g, 0x2000u | (isY || doA ? 2u : 1u << TT), [&](int t) __attribute__((always_inline)) {
        int l = threadIdx.x & 63; asm volatile("" : "+v"(l));
        if (t == 13) {
          asm volatile("s_mov_b32 s79, 0x28000");
          fl_poll<T_NPRE>();
          if (!doA && !isY) {
            CODE_TOUCH("at_code_begin", "pf_code_end", hsel * 8 + wid * 2, 16u, 64, m0d);
            CODE_TOUCH("at_code_begin", "pf_code_end", hsel * 8 + wid * 2 + 1, 16u, 64, m0d);
          }
          if (doA) {
            const char* tp = arg_late<const char, 9>() + (size_t)(pj * 64 + pu) * (SEQ * 2) + (l & 31) * 128;
            asm volatile("s_mov_b32 m0, %1\n\ts_nop 0\n\tglobal_load_lds_dword %0, off" ::"v"(tp), "i"(m0d) : "memory");
          }
        } else if (isY) fl_set<T_FQ>();
        else if (doA) { if (PREPX) fl_set<T_FPREP>(); post_dma(pj, pu, arg_late<const bf16, 12>(), 131072 + wid * 8192); }
        else {
          const char* tp = arg_late<const char, 0>() + 1024 * DC * 2 + (pu * 256 + l) * 128;
          asm volatile("s_mov_b32 m0, %1\n\ts_nop 0\n\tglobal_load_lds_dword %0, off" ::"v"(tp), "i"(m0d) : "memory");
          asm volatile("global_load_lds_dword %0, off" ::"v"(tp + 8192) : "memory");
          asm volatile("global_load_lds_dword %0, off" ::"v"(tp + 16384) : "memory");
          asm volatile("global_load_lds_dword %0, off" ::"v"(tp + 24576) : "memory");
        }
      });
      tload<101, T_OG>(); tload<96, T_Q0>(); tload<98, T_K0>();
      fl_check();
      tload<50, T_NST>(); tload<51, T_NST + 1>();
      asm volatile("v_mov_b32 v112, v84\n\tv_add_u32 v113, %0, v84\n\tv_mov_b32 v90, v87\n\tv_mov_b32 v91, v87\n\tv_mov_b32 v92, v87\n\tv_mov_b32 v93, v87\n\t"
                   "v_mov_b32 v94, v86\n\tv_mov_b32 v95, v86\n\tv_mov_b32 v126, v86\n\tv_mov_b32 v127, v86\n\ts_mov_b32 s99, 0x8000\n\t" FL_ARM("s[50:51]")
                   ::"i"(TR ? 4096 : 8192));
      if (doA) {
        const int d = pj * 64 + pu;
        const bf16* dp = arg_late<const bf16, 10>() + d;
        asm volatile("global_load_ushort %0, %1, off" : "=v"(dv) : "v"(dp) : "memory");
        srd_set<56>(arg_late<const bf16, 9>() + (size_t)d * SEQ, SEQ * 2);
        srd_set<60>(arg_late<const char, 5>() + O_X + d * SEQ * 2, SEQ * 2);
        sset<78>(65536 + wid * 15488);
        asm volatile("s_movk_i32 s64, 992\n\tv_subrev_u32 v122, 32, v84");
        GAP();
        g_tile<TR, 0, 3>(g);
      } else {
        sset<79>(isY ? wid * 8192 : 0x30000);
        GAP();
        g_tile<TR, 0, 5>(g);
      }
    };
    if (kv) kvtile(std::true_type{});
    else kvtile(std::false_type{});
  }
  KST(0, 4);
  asm volatile("s_waitcnt vmcnt(16)" : "+v"(dv) :: "memory");
  sset<79>(wid * 8192);
  if (doA) {
    const float Dd = bf2f(dv);
    KST(0, 5);
    conv_wave<T_FG>(O_Z + (pj * 64 + pu) * SEQ * 2, Dd, lds + 65536 + wid * 15488);
    const char* tp = arg_late<const char, 11>() + (size_t)h * (HD * DA * 2) + ((pj * 4 + wid) * 64 + lane) * 128;
    asm volatile("s_mov_b32 m0, %1\n\ts_nop 0\n\tglobal_load_lds_dword %0, off\n\ts_waitcnt vmcnt(17)" ::"v"(tp), "i"(m0d) : "memory");
    KST(0, 6);
  } else if (!isY) {
    static_for([&](auto I) { d_store<16 + I>(); }, make_iseq<16>{});
    tload<98, T_K0>();
    asm volatile("s_waitcnt vmcnt(0)\n\ts_barrier" ::: "memory");
    fl_set<T_FG>();
    KST(0, 5);
    fl_check();
    CODE_TOUCH("att_begin", "pf_code_end", hsel * 4 + wid, 8u, 64, m0d);
    dma4k<0>(); dma4k<1>(); dma4k<2>(); dma4k<3>();
    asm volatile("s_waitcnt vmcnt(0)" ::: "memory");
    KST(0, 6);
  } else {
    tload<76, T_PART>();
    fl_check();
    asm volatile("buffer_load_dwordx2 v[122:123], v88, s[88:91], s76 offen offset:1024 sc1" ::: "memory");
  }
  AC c;
  sset<78>(65536 + wid * 8192); sset<79>(98304 + wid * 8192);
  { const bf16* wp = arg_late<const bf16, 11>() + (size_t)h * HD * DA; asm volatile("s_mov_b64 s[80:81], %0" ::"s"(wp)); }
  asm volatile("att_begin:");
  ev_code();
  asm volatile("s_waitcnt lgkmcnt(0)\n\ts_barrier" ::: "memory");
#pragma unroll 1
  for (int it = doA ? 0 : 1; it < 2; ++it) {
    kreads<0>(make_iseq<8>{});
    const unsigned oP = tget<T_PART>();
    if (it == 0) {
      static_for([&](auto I) { tload<57 + I, T_IT + I>(); }, make_iseq<21>{});
      tload<S_T, T_IT + 21>(); tload<S_EV, T_IT + 22>();
      asm volatile("s_sub_u32 s70, s70, %0\n\ts_add_u32 s76, %1, 1024" ::"s"(j * (8 * 4352 - 8)), "s"(oP) : "scc");
    } else {
      static_for([&](auto I) { tload<57 + I, T_IT + T_ITN + I>(); }, make_iseq<21>{});
      tload<S_T, T_IT + T_ITN + 21>(); tload<S_EV, T_IT + T_ITN + 22>();
      asm volatile("s_mov_b32 s72, 0\n\ts_mov_b32 s75, %0\n\ts_mov_b32 s76, %1" ::"s"(oP + wid * 16384), "s"(oP + 1032));
    }
    unsigned mode, n;
    asm volatile("s_add_u32 s98, s97, 0x8000\n\ts_mov_b32 %0, s73\n\ts_lshr_b32 %1, s73, 8\n\tv_mov_b64 v[114:115], 0\n\tv_mov_b64 v[118:119], 0\n\t"
                 "v_mov_b32 v120, 0x45800000" : "=s"(mode), "=s"(n) :: "scc");
    KST(0, 8 + 3 * (it && !isY));
    GAP();
#define STA(k)
    STA(0);
    if (isY) phaseA<0, 2>(c);
    else phaseA<0, 1>(c);
    STA(1);
    if (c.rare) ev_call(0);
    STA(2);
    phaseB0(mode, wid);
    if (__builtin_expect(mode & 4, 0)) ev_call(31);
    STA(3);
#pragma unroll 1
    for (int i = 1;; i += 2) {
      phaseA<1, 0>(c);
      STA(9 + i);
      if (__builtin_expect(c.rare != 0, 0)) { if (c.redo) sm_redo<1>(); ev_call(i); }
      phaseB<1>(c);
      if (i + 1 >= n) break;
      phaseA<0, 0>(c);
      STA(10 + i);
      if (__builtin_expect(c.rare != 0, 0)) { if (c.redo) sm_redo<0>(); ev_call(i + 1); }
      phaseB<0>(c);
    }
    STA(4);
    if ((mode & 3) == 1) asm volatile("v_bfrev_b32 v122, 1");
    else asm volatile("v_mov_b32 v122, v84\n\tbuffer_load_dwordx4 v[128:131], v88, s[88:91], %0 offen sc1" ::"s"(oP) : "memory");
    GAP();
    phaseA<0, 3>(c);
    if (__builtin_expect(c.redo != 0, 0)) sm_redo<0>();
    STA(5);
    KST(0, 9 + 3 * (it && !isY));
    const float l[2] = {row_reduce<false>(vget<R_RS>() + vget<R_TS>()), row_reduce<false>(vget<R_RS + 1>() + vget<R_TS + 1>())};
    const float cm[2] = {vget<R_M>(), vget<R_M + 1>()};
    EP p;
    if (it == 0) {
      const f32x4 ml = {cm[0], l[0], cm[1], l[1]};
      asm volatile("buffer_store_dwordx4 %0, v88, s[88:91], %1 offen sc1\n\ts_mov_b32 s101, %2" ::"v"(ml), "s"(oP), "s"(oP + wid * 16384) : "memory");
      phaseF<0>(c, p, make_iseq<74>{});
      asm volatile(FL_POLL "\n\ts_waitcnt vmcnt(17)" ::: "memory");
    } else {
      int lz = threadIdx.x & 63; asm volatile("" : "+v"(lz));
      const int rw = lz >> 2;
      static_for([&](auto E) { vsetu<R_TB + E>(rw * 128 + 16 * ((2 * E + ((lz & 3) >> 1)) ^ (rw & 6)) + 8 * (lz & 1)); }, make_iseq<4>{});
      const unsigned vs = ((lz >> 3) & 1) * 16384 + ((lz >> 4) & 1) * 512 + (lz >> 5) * 256 + (lz & 7) * 16;
      vsetu<R_VS>(vs); vsetu<R_VS + 1>(vs + 32768);
      tload<96, T_SLAB>();
      asm volatile("s_mov_b32 s97, s79\n\ts_mov_b32 s100, 0x8000\n\ts_movk_i32 s99, 0x1000");
      if (!(mode & 2)) {
        p.fa[0] = __builtin_amdgcn_rcpf(l[0]); p.fa[1] = __builtin_amdgcn_rcpf(l[1]);
        phaseF<1>(c, p, make_iseq<74>{});
      } else {
        const f32x4 ml = {vget<128>(), vget<129>(), vget<130>(), vget<131>()};
        if (__builtin_expect(__builtin_amdgcn_ballot_w64(ml[0] != cm[0] || ml[2] != cm[1]) == 0, 1)) {
          p.fa[0] = __builtin_amdgcn_rcpf(l[0] + ml[1]); p.fa[1] = __builtin_amdgcn_rcpf(l[1] + ml[3]);
          const unsigned one = (lz >> 4) != ((lz >> 2) & 3) ? 0u : (lz & 1) ? 0x3f800000u : 0x3f80u, o0 = (lz & 2) ? 0u : one, o1 = (lz & 2) ? one : 0u;
          vsetu<224>(o0); vsetu<225>(o1); vsetu<230>(o0); vsetu<231>(o1);
          asm volatile("v_mov_b64 v[226:227], 0\n\tv_mov_b64 v[228:229], 0");
          phaseF<3>(c, p, make_iseq<74>{});
        } else {
#pragma unroll
          for (int e = 0; e < 2; ++e) {
            const float mm = fmaxf(cm[e], ml[2 * e]);
            const float wa = __builtin_amdgcn_exp2f(cm[e] - mm), wb = __builtin_amdgcn_exp2f(ml[2 * e] - mm);
            const float inv = __builtin_amdgcn_rcpf(l[e] * wa + ml[2 * e + 1] * wb);
            p.fa[e] = wa * inv; p.fb[e] = wb * inv;
          }
          phaseF<2>(c, p, make_iseq<74>{});
        }
      }
    }
    KST(0, 10 + 3 * (it && !isY));
  }
  asm volatile("v_xor_b32 v%c0, s100, v%c0\np_loop:" ::"i"(R_TB));
  GAP();
  p_half<0>(make_iseq<32>{});
  p_half<1>(make_iseq<32>{});
  asm volatile("s_cbranch_scc1 p_loop\n\ts_nop 5");
  GAP();
  p_cvt<1, 0>(); p_cvt<1, 1>(); p_cvt<1, 2>(); p_cvt<1, 3>();
  p_store<1, 1>();
  GAP();
  KST(0, 27);
  {
    constexpr unsigned rdy = 98304;
    int lz = threadIdx.x & 63; asm volatile("" : "+v"(lz));
    const int fr = lz & 15, fq = lz >> 4, qbF = isY ? 15 - pj : pj, u = hsel * 8 + j;
    asm volatile("ds_write_b32 %0, %1" ::"v"(rdy), "v"(0) : "memory");
    vsetu<81>(lz == 0 ? 0u : 0x80000000u);
    vsetu<79>(fo(1024) + (qbF * 4 + (u >> 2)) * 4352 + fr * 8);
    {
      const int jj = 4 * (fr >> 3) + (fr & 3);
      const unsigned one = ((fq ^ (fr >> 2)) & 1) ? 0u : (jj & 1) ? 0x3f800000u : 0x3f80u;
      static_for([&](auto I) { vsetu<96 + I>((jj >> 1) == I ? one : 0u); }, make_iseq<4>{});
    }
    unsigned vo = (fq >> 1) * 1024 + (fq & 1) * 256 + fr * 16, vo1 = wid < 3 ? vo + 0x100000 : 0x80000000u;
    bf16* out = arg_late<bf16, 13>();
    __amdgpu_buffer_rsrc_t so_ = __builtin_amdgcn_make_buffer_rsrc((void*)out, (short)0, SEQ * OUTW * 2, 0x00020000);
    unsigned vw = ((qbF * 128 + u * 8 + (fr & 7)) * OUTW + DA + wid * 64 + (fr >> 3) * 32 + fq * 8) * 2;
    unsigned so = O_S + qbF * 0x200000 + (1 + wid) * 0x40000 + u * 16384;
    asm volatile("" : "+s"(so), "+s"(so_));
    asm volatile("s_waitcnt vmcnt(0) lgkmcnt(0)" : "+v"(vo), "+v"(vo1), "+v"(vw) :: "memory");
    fl_set<T_FS>();
    asm volatile("s_barrier" ::: "memory");
    KST(0, 30);
    if (doA) post_math(pj, pu, out, lds + 131072 + wid * 8192);
    unsigned pend = 0x5555u, cnt = 0x400000;
    KST(0, 28);
    while (true) {
      unsigned nw;
      if (wid == 3) {
        u64 m;
        const u64 ex = pend | (pend << 1);
        asm volatile(FL_ARM("%1") "\n\t" FL_POLL "\n\ts_waitcnt vmcnt(0)\n\tv_cmp_eq_u64 %0, s[84:85], v[124:125]" : "=s"(m) : "s"(ex) : "memory");
        nw = (unsigned)m & ((unsigned)m >> 1) & pend;
        if (nw) asm volatile("ds_write_b32 %0, %1" ::"v"(rdy), "v"(~pend | nw) : "memory");
      } else {
        unsigned t;
        asm volatile("ds_read_b32 %0, %1\n\ts_waitcnt lgkmcnt(0)" : "=v"(t) : "v"(rdy) : "memory");
        nw = __builtin_amdgcn_readfirstlane(t) & pend;
      }
      if (--cnt == 0) nw = pend;
      if (nw == 0) {
        if (wid == 3) asm volatile("s_sleep 4");
        else asm volatile("s_sleep 1");
        continue;
      }
      asm volatile("s_nop 4");
      static_for([&](auto S) {
        if ((nw >> (2 * S)) & 1) { r_load<0, 2 * S>(vo, so); r_load<0, 2 * S + 1>(vo, so); r_load<1, 2 * S>(vo1, so); r_load<1, 2 * S + 1>(vo1, so); }
      }, make_iseq<8>{});
      pend &= ~nw;
      if (pend == 0) break;
    }
    asm volatile("s_waitcnt vmcnt(0)" ::: "memory");
    KST(0, 16);
    r_fin<0>(vw, so_, 0);
    if (wid < 3) r_fin<1>(vw, so_, 512);
    KST(0, 29);
    if (b == 128 && wid == 0) asm volatile(FL_SET("%0") ::"s"(fo(6144)) : "memory");
  }
  asm volatile("s_waitcnt vmcnt(0)" ::: "memory");
  asm volatile("pf_code_end:");
  KST(0, 1);
}

extern "C" void kernel_launch(void* const* d_in, const int* in_sizes, int n_in, void* d_out, int out_size, void* d_ws,
                              size_t ws_size, hipStream_t stream) {
  const bf16* Aq = (const bf16*)d_in[0];
  const bf16* Ak = (const bf16*)d_in[1];
  const bf16* Av = (const bf16*)d_in[2];
  const bf16* Ash = (const bf16*)d_in[3];
  const bf16* Assmk = (const bf16*)d_in[4];
  const bf16* AssmD = (const bf16*)d_in[5];
  const bf16* Bx = (const bf16*)d_in[6];
  const bf16* Bckv = (const bf16*)d_in[7];
  const bf16* Bwq = (const bf16*)d_in[8];
  const bf16* Bwuk = (const bf16*)d_in[9];
  const bf16* Bwuv = (const bf16*)d_in[10];
  const bf16* Bwo = (const bf16*)d_in[11];
  bf16* out = (bf16*)d_out;
  hipFuncSetAttribute((const void*)fused_kernel, hipFuncAttributeMaxDynamicSharedMemorySize, 163840);
  fused_kernel<<<256, 256, 163840, stream>>>(Bckv, Bwuk, Bwuv, Bx, Bwq, (bf16*)d_ws, Ak, Av, Ash, Assmk, AssmD, Bwo, Aq, out);
}
```

```cpp
#include <hip/hip_runtime.h>
#include <hip/hip_bf16.h>
#include <stdint.h>
#include <utility>

typedef __hip_bfloat16 bf16;
typedef __attribute__((ext_vector_type(4))) short bf16x4;
typedef __attribute__((ext_vector_type(2))) unsigned u32x2;

constexpr int SEQ = 2048;
constexpr int DA = 448;
constexpr int NH = 16;
constexpr int HD = 256;
constexpr int DC = 1024;
constexpr int OUTW = 896;
#define OUTSC 17
#define OUTSCS " sc0 sc1"
#define NTPS " nt"
#define NTVS " nt"
#define NTQS ""
#define NTW 1
constexpr unsigned TSB = NH * SEQ * HD * 2, O_K = TSB, O_S = 3 * TSB, O_P = 5 * TSB, PSTR = 67584, O_F = O_P + 128 * PSTR,
                   O_X = O_F + 400 * 4352, O_Z = O_X + DA * SEQ * 2, WSB = O_Z + DA * SEQ * 2;
constexpr unsigned fo(int w) { return O_F + (w >> 4) * 4352 + (w & 15) * 8; }

#define KST(kid, which)

__device__ __forceinline__ float bf2f(unsigned short u) { return __uint_as_float(((unsigned)u) << 16); }
__device__ __forceinline__ unsigned short f2bf(float f) { return __bfloat16_as_ushort(__float2bfloat16(f)); }
__device__ __forceinline__ float rbf(float f) { return bf2f(f2bf(f)); }
typedef __attribute__((ext_vector_type(2))) __bf16 bf16x2n;
typedef __attribute__((ext_vector_type(2))) float f32x2n;
__device__ __forceinline__ unsigned pk2(float a, float b) {
  return __builtin_bit_cast(unsigned, __builtin_convertvector((f32x2n){a, b}, bf16x2n));
}

__device__ __forceinline__ int off_b(int row, int ch) { return 256 * row + 16 * (ch ^ (((row & 3) << 2) | ((row >> 2) & 3))); }

template <int R> __device__ __forceinline__ void sset(unsigned x) { asm volatile("s_mov_b32 s%c0, %1" ::"i"(R), "s"(x)); }
template <int R> __device__ __forceinline__ void vsetu(unsigned x) { asm volatile("v_mov_b32 v%c0, %1" ::"i"(R), "v"(x)); }
template <int R> __device__ __forceinline__ void srd_set(const void* p, unsigned n) {
  asm volatile("s_mov_b64 s[%c0:%c1], %4\n\ts_mov_b32 s%c2, %5\n\ts_mov_b32 s%c3, 0x20000" ::"i"(R), "i"(R + 1), "i"(R + 2), "i"(R + 3), "s"(p), "s"(n));
}
template <int R, int L> __device__ __forceinline__ void tload() { asm volatile("v_readlane_b32 s%c0, v78, %1" ::"i"(R), "i"(L)); }
template <int L> __device__ __forceinline__ unsigned tget() { unsigned t; asm volatile("v_readlane_b32 %0, v78, %1" : "=s"(t) : "i"(L)); return t; }
#define FL_SET(S) "buffer_store_dwordx2 v[82:83], v81, s[88:91], " S " offen sc1"
#define FL_ARM(N) "v_cndmask_b32_e64 v80, -2.0, v79, " N
#define FL_POLL "buffer_load_dwordx2 v[124:125], v80, s[88:91], 0 offen sc1"
#define FL_CHECK(N) "s_mov_b32 s48, 0x100000\n7:\n\tv_cmp_eq_u64 vcc, s[84:85], v[124:125]\n\ts_and_b64 vcc, vcc, " N "\n\ts_cmp_eq_u64 vcc, " N \
                    "\n\ts_cbranch_scc1 8f\n\ts_sleep 2\n\t" FL_POLL "\n\ts_waitcnt vmcnt(0)\n\ts_sub_u32 s48, s48, 1\n\ts_cbranch_scc0 7b\n8:"
template <int L> __device__ __forceinline__ void fl_set() {
  asm volatile("v_readlane_b32 s48, v78, %0\n\ts_nop 4\n\t" FL_SET("s48") ::"i"(L) : "memory");
}
template <int L> __device__ __forceinline__ void fl_poll() {
  asm volatile("v_readlane_b32 s50, v78, %0\n\tv_readlane_b32 s51, v78, %1\n\ts_nop 1\n\t" FL_ARM("s[50:51]") "\n\t" FL_POLL ::"i"(L), "i"(L + 1) : "memory");
}
__device__ __forceinline__ void fl_check() { asm volatile(FL_CHECK("s[50:51]") ::: "vcc", "scc", "memory"); }
template <int R> __device__ __forceinline__ float vget() { float t; asm volatile("v_mov_b32 %0, v%c1" : "=v"(t) : "i"(R)); return t; }
template <int R> __device__ __forceinline__ void vset(float x) { asm volatile("v_mov_b32 v%c0, %1" ::"i"(R), "v"(x)); }
template <int R> __device__ __forceinline__ float acc_read1() { float t; asm volatile("v_accvgpr_read_b32 %0, a[%c1]" : "=v"(t) : "i"(R)); return t; }
template <class F, int... I> __device__ __forceinline__ void static_for(F&& f, std::integer_sequence<int, I...>) {
  (f(std::integral_constant<int, I>{}), ...);
}
template <int... Is> using iseq = std::integer_sequence<int, Is...>;
template <int N> using make_iseq = std::make_integer_sequence<int, N>;

typedef __attribute__((ext_vector_type(4))) unsigned u32x4c;
typedef __attribute__((ext_vector_type(4))) float f32x4c;
typedef __attribute__((ext_vector_type(2))) float f32x2c;
typedef unsigned long long u64;
struct PR { u32x4c kk[7], vv[4]; };
__device__ __forceinline__ void prep_load(PR& p, int tid, int cg, int tb, const bf16* __restrict__ k, const bf16* __restrict__ v,
                                          const bf16* __restrict__ shift_k) {
  const __amdgpu_buffer_rsrc_t sk = __builtin_amdgcn_make_buffer_rsrc((void*)k, (short)0, SEQ * DA * 2, 0x00020000);
  const __amdgpu_buffer_rsrc_t sv = __builtin_amdgcn_make_buffer_rsrc((void*)v, (short)0, SEQ * DA * 2, 0x00020000);
  const __amdgpu_buffer_rsrc_t sw = __builtin_amdgcn_make_buffer_rsrc((void*)(shift_k + cg * 256), (short)0, 512, 0x00020000);
  const int vo = ((tb * 128 + (tid >> 3) * 4) * DA + cg * 64 + (tid & 7) * 8) * 2;
#pragma unroll
  for (int q = 0; q < 7; ++q)
    asm volatile("buffer_load_dwordx4 %0, %1, %2, 0 offen" NTPS : "=v"(p.kk[q]) : "v"(vo + (q - 3) * DA * 2), "s"(sk) : "memory");
#pragma unroll
  for (int q = 0; q < 4; ++q)
    asm volatile("buffer_load_dwordx4 %0, %1, %2, 0 offen offset:%3" NTVS : "=v"(p.vv[q]) : "v"(vo), "s"(sv), "i"(q * DA * 2) : "memory");
#pragma unroll
  for (int q = 0; q < 4; ++q)
    asm volatile("buffer_load_dwordx4 a[%c0:%c1], %2, %3, 0 offen offset:%4" ::"i"(4 * q), "i"(4 * q + 3), "v"((tid & 7) * 64), "s"(sw), "i"(q * 16) : "memory");
}
__device__ __forceinline__ float bfl(unsigned u) { return __uint_as_float(u << 16); }
__device__ __forceinline__ float bfh(unsigned u) { return __uint_as_float(u & 0xffff0000u); }
__device__ __forceinline__ f32x2c rbf2(f32x2c a) { const unsigned u = pk2(a[0], a[1]); return (f32x2c){bfl(u), bfh(u)}; }
template <int I> __device__ __forceinline__ void prep_math(const PR& p, int tid, char* lx) {
  const int rp = tid >> 3, c = tid & 7;
  f32x2c wt[4], kf[7];
  const unsigned w[4] = {__float_as_uint(acc_read1<4 * I>()), __float_as_uint(acc_read1<4 * I + 1>()), __float_as_uint(acc_read1<4 * I + 2>()),
                         __float_as_uint(acc_read1<4 * I + 3>())};
#pragma unroll
  for (int t = 0; t < 4; ++t)
    wt[t] = (t & 1) ? (f32x2c){bfh(w[t >> 1]), bfh(w[2 + (t >> 1)])} : (f32x2c){bfl(w[t >> 1]), bfl(w[2 + (t >> 1)])};
#pragma unroll
  for (int q = 0; q < 7; ++q) kf[q] = (f32x2c){bfl(p.kk[q][I]), bfh(p.kk[q][I])};
  f32x2c x[4];
#pragma unroll
  for (int e = 0; e < 4; ++e) {
    f32x2c s = rbf2(kf[e] * wt[3]);
    s = rbf2(s + rbf2(kf[e + 1] * wt[2]));
    s = rbf2(s + rbf2(kf[e + 2] * wt[1]));
    s = rbf2(s + rbf2(kf[e + 3] * wt[0]));
    x[e] = s * (f32x2c){bfl(p.vv[e][I]), bfh(p.vv[e][I])};
  }
#pragma unroll
  for (int u = 0; u < 2; ++u)
    *(u32x2*)(lx + (c * 8 + 2 * I + u) * 256 + (((31 - rp) * 8) ^ (c << 4))) = (u32x2){pk2(x[3][u], x[2][u]), pk2(x[1][u], x[0][u])};
}
__device__ __forceinline__ void prep_store(int tid, int cg, int tb, bf16* __restrict__ xr, const char* lx) {
  const __amdgpu_buffer_rsrc_t sx = __builtin_amdgcn_make_buffer_rsrc((void*)(xr + (size_t)cg * 64 * SEQ), (short)0, 64 * SEQ * 2, 0x00020000);
#pragma unroll
  for (int rd = 0; rd < 4; ++rd) {
    const int idx = tid + 256 * rd, ch = idx >> 4, q = idx & 15;
    u32x4c w = *(const u32x4c*)(lx + ch * 256 + 16 * (q ^ (ch >> 3)));
    __builtin_amdgcn_raw_buffer_store_b128(w, sx, (ch * SEQ + (15 - tb) * 128 + q * 8) * 2, 0, 16);
  }
}

#define CODE_TOUCH(LB, LE, wx, NW, NL, m0v) do { unsigned blo_, bhi_, elo_; \
    asm volatile("s_getpc_b64 s[48:49]\n\ts_add_u32 s48, s48, " LB "@rel32@lo+4\n\ts_addc_u32 s49, s49, " LB "@rel32@hi+12\n\t" \
                 "s_mov_b32 %0, s48\n\ts_mov_b32 %1, s49\n\t" \
                 "s_getpc_b64 s[48:49]\n\ts_add_u32 s48, s48, " LE "@rel32@lo+4\n\ts_mov_b32 %2, s48" \
                 : "=s"(blo_), "=s"(bhi_), "=s"(elo_) :: "scc"); \
    const unsigned n_ = elo_ - (blo_ & ~127u); \
    const __amdgpu_buffer_rsrc_t srd_ = __builtin_amdgcn_make_buffer_rsrc((void*)(((unsigned long long)bhi_ << 32) | (blo_ & ~127u)), (short)0, n_ < 262144u ? n_ : 0u, 0x00020000); \
    const unsigned l_ = threadIdx.x & 63, vo_ = l_ < (NL) ? ((wx) + l_ * (NW)) * 128u : 0x80000000u; \
    asm volatile("s_mov_b32 m0, %0\n\ts_nop 4\n\tbuffer_load_dword %1, %2, 0 offen lds" ::"s"(m0v), "v"(vo_), "s"(srd_) : "memory"); } while (0)
template <int I> __device__ __forceinline__ void d_store() {
  asm volatile("buffer_store_dwordx4 v[%c0:%c1], v%c2, s[88:91], s101 offen offset:%3 sc1" ::"i"(128 + 4 * I), "i"(131 + 4 * I), "i"(112 + ((I >> 2) & 1)),
               "i"((I & 3) * 1024) : "memory");
  if constexpr ((I & 7) == 7) asm volatile("s_addk_i32 s101, 0x4000" ::: "scc");
}
constexpr int NS_X = 19;
struct CV { unsigned pa[4], pb, px, vz, sz; float D; };
constexpr int cv_J(bool up, int q) { int J = up ? 0 : 7; while (q > J) { q -= J + 1; J += up ? 1 : -1; } return J; }
constexpr int cv_i(bool up, int q) { int J = up ? 0 : 7; while (q > J) { q -= J + 1; J += up ? 1 : -1; } return q; }
template <int R, int N> __device__ __forceinline__ void cv_rd(CV& c) {
  if constexpr (N < 16) {
    constexpr int i = N >> 1, o = 16 * R + 128 * (i & 1) + 2 * (N & 1), r = 32 + 32 * (R & 1) + 2 * N;
    asm volatile("ds_read2_b32 a[%c1:%c2], %0 offset0:%c3 offset1:%c4" ::"v"(c.pa[i >> 1]), "i"(r), "i"(r + 1), "i"(o), "i"(o + 1));
  } else {
    constexpr int m = N - 16, r = 192 + 32 * (R & 1) + 4 * m;
    asm volatile("ds_read_b128 a[%c1:%c2], %0 offset:%c3" ::"v"(c.pb), "i"(r), "i"(r + 3), "i"(64 * R + 512 * (7 - m)));
  }
}
template <int O> __device__ __forceinline__ void cv_xd(CV& c) {
  constexpr int p = O / 7, k = O % 7, t = 160 + 2 * p, x = 96 + (p ^ 1);
  if constexpr (k == 0) asm volatile("v_and_b32 v%c0, 0xffff0000, v%c1" ::"i"(t), "i"(x));
  if constexpr (k == 1) asm volatile("v_lshlrev_b32 v%c0, 16, v%c1" ::"i"(t + 1), "i"(x));
  if constexpr (k == 2 || k == 3) asm volatile("v_mul_f32 v%c0, v%c0, %1" ::"i"(t + k - 2), "v"(c.D));
  if constexpr (k == 4) asm volatile("v_cvt_pk_bf16_f32 v%c0, v%c1, v%c0" ::"i"(t + 1), "i"(t));
  if constexpr (k == 5) asm volatile("v_lshlrev_b32 v%c0, 16, v%c1" ::"i"(t), "i"(t + 1));
  if constexpr (k == 6) asm volatile("v_and_b32 v%c0, 0xffff0000, v%c0" ::"i"(t + 1));
}
template <int O> __device__ __forceinline__ void cv_z(CV& c) {
  constexpr int J = O / 13, k = O % 13, hf = k & 1, y = 128 + 4 * J + 2 * hf;
  if constexpr (k < 2) asm volatile("v_cvt_pk_bf16_f32 v%c0, v%c1, v%c0" ::"i"(y + 1), "i"(y));
  else if constexpr (k < 4) asm volatile("v_lshlrev_b32 v%c0, 16, v%c1" ::"i"(y), "i"(y + 1));
  else if constexpr (k < 6) asm volatile("v_and_b32 v%c0, 0xffff0000, v%c0" ::"i"(y + 1));
  else if constexpr (k < 10) asm volatile("v_add_f32 v%c0, v%c0, v%c1" ::"i"(y + (k >= 8)), "i"(y + 32 + (k >= 8)));
  else if constexpr (k < 12) asm volatile("v_cvt_pk_bf16_f32 v%c0, v%c1, v%c2" ::"i"(128 + 4 * J + hf), "i"(y), "i"(y + 1));
  else asm volatile("buffer_store_dwordx2 v[%c0:%c1], %2, s[88:91], %3 offen offset:%4 sc1" ::"i"(128 + 4 * J), "i"(129 + 4 * J), "v"(c.vz), "s"(c.sz),
                    "i"(512 * J) : "memory");
}
constexpr int cv_z0(int Q) {
  int o = 0;
  for (int q = 0; q < Q; ++q)
    for (int k = 0; k < 3; ++k) { const int J = o / 13; if ((J + 1) * (J + 2) / 2 - 1 + 4 <= q) ++o; }
  return o;
}
constexpr int cv_xd0(int T) { int o = 0; for (int t = 0; t < T; ++t) o += t % 36 < 24 ? 1 : 2; return o < 112 ? o : 112; }
template <int B, int... Is> __device__ __forceinline__ void cv_zs(CV& c, iseq<Is...>) { (cv_z<B + Is>(c), ...); }
template <int B, int... Is> __device__ __forceinline__ void cv_xds(CV& c, iseq<Is...>) { (cv_xd<B + Is>(c), ...); }
template <int R, int Q> __device__ __forceinline__ void cv_slot(CV& c) {
  constexpr int i = cv_i(R == 7, Q), J = cv_J(R == 7, Q), d = 128 + 4 * J, a = 32 + 32 * (R & 1) + 4 * i, b = 192 + 32 * (R & 1) + 4 * (J - i);
  constexpr int T = 36 * R + Q, vm = Q % 4 == 1 && Q < 32 ? Q / 4 : -1;
  if constexpr (Q == 0) asm volatile("s_waitcnt lgkmcnt(0)");
  if constexpr (R >= 6 && vm >= 0 && vm % 4 == 0) asm volatile("s_add_u32 m0, s79, %0" ::"i"((R - 6) * 32768 + vm * 1024));
  if constexpr (R == 0 && i == 0)
    asm volatile("v_mfma_f32_16x16x32_bf16 v[%c0:%c1], a[%c2:%c3], a[%c4:%c5], 0" ::"i"(d), "i"(d + 3), "i"(a), "i"(a + 3), "i"(b), "i"(b + 3));
  else
    asm volatile("v_mfma_f32_16x16x32_bf16 v[%c0:%c1], a[%c2:%c3], a[%c4:%c5], v[%c0:%c1]" ::"i"(d), "i"(d + 3), "i"(a), "i"(a + 3), "i"(b), "i"(b + 3));
  if constexpr (Q < 24 && R < 7) cv_rd<R + 1, Q>(c);
  if constexpr (T % 7 == 3 && NS_X + 5 + T / 7 < 32) d_store<NS_X + 5 + T / 7>();
  if constexpr (R == 2 && Q >= 24 && Q < 32)
    asm volatile("ds_read_b64 v[%c1:%c2], %0 offset:%c3" ::"v"(c.px), "i"(96 + 2 * (Q - 24)), "i"(97 + 2 * (Q - 24)), "i"(512 * (31 - Q)));
  if constexpr (R >= 3 && R < 6) cv_xds<cv_xd0(T - 108)>(c, make_iseq<cv_xd0(T - 107) - cv_xd0(T - 108)>{});
  if constexpr ((R == 3 || R == 4) && vm >= 0) {
    constexpr int f = (R - 3) * 8 + vm;
    asm volatile("buffer_load_dwordx4 a[%c0:%c1], v84, s[88:91], s96 offen offset:%2 sc1" ::"i"(128 + 4 * f), "i"(131 + 4 * f), "i"((f & 3) * 1024) : "memory");
    if constexpr ((f & 3) == 3) asm volatile("s_addk_i32 s96, 4096");
  }
  if constexpr (R == 4 && Q == 35) asm volatile(FL_POLL ::: "memory");
  if constexpr (R >= 6 && vm >= 0)
    asm volatile("buffer_load_dwordx4 v%c0, s[88:91], s98 offen offset:%1 sc1 lds" ::"i"(86 + vm / 4), "i"((vm & 3) * 1024) : "memory");
  if constexpr (R == 6 && Q == 35) asm volatile("s_add_u32 s98, s98, 0x8000");
  if constexpr (R == 7) {
    if constexpr (Q < 4) asm volatile("s_nop 0");
    cv_zs<cv_z0(Q)>(c, make_iseq<cv_z0(Q + 1) - cv_z0(Q)>{});
  }
  __builtin_amdgcn_sched_barrier(0);
}
template <int R, int... Q> __device__ __forceinline__ void cv_step(CV& c, iseq<Q...>) { (cv_slot<R, Q>(c), ...); }
template <int... N> __device__ __forceinline__ void cv_rd0(CV& c, iseq<N...>) { (cv_rd<0, N>(c), ...); }
template <int FLAG> __device__ __forceinline__ void conv_wave(unsigned sz, float Dd, char* lw) {
  int lane = threadIdx.x & 63; asm volatile("" : "+v"(lane));
  const int l16 = lane & 15, fq = lane >> 4;
  u32x4c w[5]; unsigned w4[5];
#pragma unroll
  for (int r = 0; r < 5; ++r) {
    w[r] = *(const u32x4c*)(lw + r * 1024 + lane * 16);
    w4[r] = *(const unsigned*)(lw + r * 1024 + lane * 16 + 16);
  }
  d_store<NS_X>(); d_store<NS_X + 1>();
#pragma unroll
  for (int r = 0; r < 5; ++r)
    *(u32x4c*)(lw + 10304 + r * 1024 + lane * 16) = (u32x4c){__builtin_amdgcn_alignbit(w[r][1], w[r][0], 16), __builtin_amdgcn_alignbit(w[r][2], w[r][1], 16),
                                                             __builtin_amdgcn_alignbit(w[r][3], w[r][2], 16), __builtin_amdgcn_alignbit(w4[r], w[r][3], 16)};
  d_store<NS_X + 2>();
  CV c;
#pragma unroll
  for (int i = 0; i < 4; ++i) c.pa[i] = (unsigned)(size_t)lw + ((l16 + 1) & 1) * 10304 + 4 * ((8 * fq + l16 + 1) >> 1) + 1024 * i;
  c.pb = (unsigned)(size_t)lw + 5120 + (2032 - 16 * l16 + 8 * fq) * 2 - 512 * 7;
  c.vz = (16 * l16 + 4 * fq) * 2;
  c.px = (unsigned)(size_t)lw + 5120 + 2044 * 2 - 512 * 7 - c.vz;
  c.sz = sz; c.D = Dd;
  asm volatile("" : "+v"(c.vz), "+v"(c.px), "+v"(c.D) :: "memory");
  asm volatile("" : "+s"(c.sz));
  cv_rd0(c, make_iseq<24>{});
  d_store<NS_X + 3>(); d_store<NS_X + 4>();
  __builtin_amdgcn_sched_barrier(0);
  static_for([&](auto R) __attribute__((always_inline)) {
    cv_step<R>(c, make_iseq<36>{});
    if constexpr (R == 2) {
      asm volatile("s_waitcnt vmcnt(0)\n\ts_barrier" ::: "memory");
      fl_set<FLAG>();
    }
    if constexpr (R == 5) {
      asm volatile("s_waitcnt vmcnt(0)" ::: "memory");
      fl_check();
    }
    __builtin_amdgcn_sched_barrier(0);
  }, make_iseq<8>{});
  asm volatile("s_nop 7");
  cv_zs<cv_z0(36)>(c, make_iseq<104 - cv_z0(36)>{});
}

__device__ __forceinline__ void post_dma(int cg, int tb, const bf16* __restrict__ q, unsigned m0w) {
  int lane = threadIdx.x & 63; asm volatile("" : "+v"(lane));
  const __amdgpu_buffer_rsrc_t sq = __builtin_amdgcn_make_buffer_rsrc((void*)(q + (size_t)tb * 32 * DA + cg * 64), (short)0, 32 * DA * 2, 0x00020000);
  const unsigned vq = ((lane >> 3) * DA + (lane & 7) * 8) * 2;
  asm volatile("s_mov_b32 m0, %0\n\ts_nop 4\n\tbuffer_load_dwordx4 %1, %2, 0 offen" NTQS " lds\n\tbuffer_load_dwordx4 %1, %2, %3 offen offset:1024" NTQS " lds\n\t"
               "buffer_load_dwordx4 %1, %2, %4 offen offset:2048" NTQS " lds\n\tbuffer_load_dwordx4 %1, %2, %5 offen offset:3072" NTQS " lds"
               ::"s"(m0w + 4096), "v"(vq), "s"(sq), "s"(8 * DA * 2 - 1024), "s"(16 * DA * 2 - 2048), "s"(24 * DA * 2 - 3072) : "memory");
}
__device__ __forceinline__ void post_math(int cg, int tb, bf16* __restrict__ out, const char* lw) {
  int lane = threadIdx.x & 63; asm volatile("" : "+v"(lane));
  const int tl = lane >> 3, ck = lane & 7;
  const __amdgpu_buffer_rsrc_t so = __builtin_amdgcn_make_buffer_rsrc((void*)(out + (size_t)tb * 32 * OUTW + cg * 64), (short)0, 32 * OUTW * 2 - 2 * DA, 0x00020000);
  const unsigned vo = (tl * OUTW + ck * 8) * 2;
#pragma unroll
  for (int i = 0; i < 4; ++i) {
    const u32x4c qv = *(const u32x4c*)(lw + 4096 + i * 1024 + lane * 16);
    const char* lz = lw + ck * 64 + 16 * (i ^ (ck >> 1)) + tl * 2;
    u32x4c o;
#pragma unroll
    for (int e = 0; e < 4; ++e) {
      const float z0 = bf2f(*(const unsigned short*)(lz + 2 * e * 512));
      const float z1 = bf2f(*(const unsigned short*)(lz + (2 * e + 1) * 512));
      o[e] = pk2(bfl(qv[e]) * z0, bfh(qv[e]) * z1);
    }
    __builtin_amdgcn_raw_buffer_store_b128(o, so, vo + i * 8 * OUTW * 2, 0, OUTSC);
  }
}

#define DI __device__ __forceinline__
#define GAP() __builtin_amdgcn_sched_barrier(0)
typedef __attribute__((ext_vector_type(4))) float f32x4g;
typedef __attribute__((ext_vector_type(4))) unsigned u32x4g;
constexpr int LDW = NH * HD;
struct GM { unsigned aA[2], aB[2][8]; };
template <bool W, int J, int V, bool NT = false> DI void g_dma() {
  if constexpr (NT && J == 0) asm volatile("buffer_load_dwordx4 v%c0, s[60:63], 0 offen nt lds" ::"i"(V) : "memory");
  else if constexpr (NT) asm volatile("buffer_load_dwordx4 v%c0, s[60:63], s%c1 offen nt lds" ::"i"(V), "i"(70 + J) : "memory");
  else
  if constexpr (J == 0) asm volatile("buffer_load_dwordx4 v%c0, s[%c1:%c2], 0 offen lds" ::"i"(V), "i"(W ? 60 : 56), "i"(W ? 63 : 59) : "memory");
  else asm volatile("buffer_load_dwordx4 v%c0, s[%c1:%c2], s%c3 offen lds" ::"i"(V), "i"(W ? 60 : 56), "i"(W ? 63 : 59), "i"((W ? 70 : 63) + J) : "memory");
}
template <int P, int KS, int I> DI void g_wread(GM& g) {
  constexpr int nf = I >> 1, hf = I & 1, r = 192 - 64 * P + 8 * nf + 4 * KS + 2 * hf;
  asm volatile("ds_read_b64_tr_b16 a[%c1:%c2], %0 offset:%c3" ::"v"(g.aB[hf][nf]), "i"(r), "i"(r + 1), "i"(P * 32768 + KS * 16384));
}
template <int P, int M, int KS> DI void g_aread(GM& g) {
  constexpr int r = 96 + 8 * (M & 1) + 4 * KS;
  asm volatile("ds_read_b128 v[%c1:%c2], %0 offset:%c3" ::"v"(g.aA[KS]), "i"(r), "i"(r + 3), "i"(P * 32768 + M * 2048));
}
constexpr float CS = 0.0625f * 1.4426950408889634f;
struct ES { int op0[129], nop[129]; };
constexpr ES e_sched(bool TR, bool SCL) {
  ES z{};
  const int per = SCL ? 16 : 8;
  int o = 0;
  for (int G = 0; G <= 128; ++G) {
    int cap = G == 128 ? 10000 : SCL ? 4 : 2;
    z.op0[G] = o;
    while (o < 32 * per) {
      const int i = o / per, rdy = (TR ? 32 * (i >> 3) + 16 + 2 * (i & 7) + 1 : 4 * i + 3) + 4;
      if ((G < rdy && G < 128) || cap-- <= 0) break;
      ++o; ++z.nop[G];
    }
  }
  return z;
}
template <bool TR, bool SCL> constexpr ES ESCH = e_sched(TR, SCL);
constexpr int e_vm(int G) { return (G * 3) % 8 < 3 ? G * 3 / 8 : -1; }
constexpr int e_st(int MODE, int G) {
  if (MODE == 5) return e_vm(G) >= 32 ? e_vm(G) - 32 : -1;
  if (MODE == 3) return G >= 31 && (G - 31) % 5 == 0 && (G - 31) / 5 < NS_X ? (G - 31) / 5 : -1;
  const int g0 = MODE == 4 ? 5 : 13, gs = MODE == 2 ? 7 : 6;
  return MODE >= 2 && G >= g0 && (G - g0) % gs == 0 && (G - g0) / gs < 16 ? (G - g0) / gs + (MODE == 4 ? 16 : 0) : -1;
}
constexpr int e_nst(int MODE, int G0, int G1) { int n = 0; for (int G = G0; G < G1; ++G) n += e_st(MODE, G) >= 0; return n; }
static_assert(e_nst(2, 0, 128) == 16 && e_nst(3, 30, 128) == NS_X && e_nst(5, 0, 128) == 16 && e_nst(4, 0, 104) == 16);
template <bool TR, bool SCL, int O> DI void e_op() {
  constexpr int per = SCL ? 16 : 8, i = O / per, k = O % per, r = 128 + 4 * i, t = 114 + 4 * (i & 1), c = k - (per - 4);
  if constexpr (k < 4) asm volatile("v_accvgpr_read_b32 v%c0, a[%c1]" ::"i"(t + k), "i"(4 * i + k));
  else if constexpr (c < 0) asm volatile("v_mul_f32 v%c0, %1, v%c0" ::"i"(k < 8 ? r + (k ^ 6) : t + k - 8), "s"(CS));
  else {
    constexpr int d = c == 0 ? r + 3 : c == 1 ? r + 2 : r + c - 2, x = c == 0 ? r + 2 : c == 1 ? r : t + 2 * (c - 2);
    asm volatile("v_cvt_pk_bf16_f32 v%c0, v%c1, v%c2" ::"i"(d), "i"(x), "i"(x + 1));
  }
}
template <bool TR, bool SCL, int B, int... Is> DI void e_ops(iseq<Is...>) { (e_op<TR, SCL, B + Is>(), ...); }
constexpr int e_slot(int MODE, int G) { return MODE == 3 ? (G % 3 == 0 && G < 30 ? G / 3 : -1) : e_vm(G) >= 0 && e_vm(G) < 32 ? 10 + e_vm(G) : -1; }
template <int S> DI void e_m0() {
  if constexpr (S == 0 || S == 1 || S == 5 || S == 9) asm volatile("s_add_u32 m0, s78, %0" ::"i"(S * 1024));
  if constexpr (S >= 26 && (S - 26) % 4 == 0) asm volatile("s_add_u32 m0, s79, %0" ::"i"(((S - 26) >> 3) * 32768 + ((S - 26) & 4) * 1024));
}
template <int S> DI void e_load() {
  if constexpr (S == 0) asm volatile("buffer_load_dwordx4 v122, s[56:59], 0 offen lds" ::: "memory");
  else if constexpr (S < 5) asm volatile("buffer_load_dwordx4 v84, s[56:59], s64 offen offset:%0 lds" ::"i"((S - 1) * 1024) : "memory");
  else if constexpr (S < 9) asm volatile("buffer_load_dwordx4 v84, s[60:63], 0 offen offset:%0 sc1 lds" ::"i"((S - 5) * 1024) : "memory");
  else if constexpr (S == 9) asm volatile("buffer_load_dwordx4 v84, s[60:63], s62 offen sc1 lds" ::: "memory");
  else if constexpr (S < 26) {
    constexpr int f = S - 10;
    asm volatile("buffer_load_dwordx4 a[%c0:%c1], v84, s[88:91], s96 offen offset:%2 sc1" ::"i"(128 + 4 * f), "i"(131 + 4 * f), "i"((f & 3) * 1024) : "memory");
    if constexpr ((f & 3) == 3) asm volatile("s_addk_i32 s96, 4096");
  } else {
    constexpr int f = S - 26;
    asm volatile("buffer_load_dwordx4 v%c0, s[88:91], s98 offen offset:%1 sc1 lds" ::"i"(86 + ((f >> 2) & 1)), "i"((f & 3) * 1024) : "memory");
    if constexpr (f == 7) asm volatile("s_add_u32 s98, s98, 0x8000");
    if constexpr (f == 15) asm volatile(FL_POLL ::: "memory");
  }
}
template <bool TR, int P, int H, int G, int MODE, bool NT> DI void g_gap(GM& g) {
  constexpr int m = 4 * H + (G >> 4), q = G & 15, n = q >> 1, ks = q & 1, GG = 64 * H + G;
  constexpr bool inV = TR ? m & 1 : n & 1, END = MODE == 3 || MODE == 5, EPI = END || MODE == 2;
  constexpr int a = 96 + 8 * (m & 1) + 4 * ks, b = 192 - 64 * P + 8 * n + 4 * ks, d = (inV ? 128 : 0) + 4 * (TR ? 8 * (m >> 1) + n : 4 * m + (n >> 1));
  constexpr int J = END ? -1 : q == 8 ? 2 * (G >> 4) : q == 12 ? 2 * (G >> 4) + 1 : -1, gj = (J & 3) ^ ((J & 3) >> 1);
  if constexpr (END) { if constexpr (q == 0 && GG > 0) asm volatile("s_waitcnt lgkmcnt(0)"); e_m0<e_slot(MODE, GG)>(); }
  else if constexpr (q == 0 && G > 0) asm volatile("s_waitcnt lgkmcnt(4)");
  if constexpr (J == 0) asm volatile("s_add_u32 m0, s78, %0" ::"i"((P ^ H ^ 1) * 32768 + (1 - H) * 8192));
  else if constexpr (J == 4) asm volatile("s_add_u32 m0, s79, %0" ::"i"((P ^ H) * 32768 + (1 - H) * 16384));
  else if constexpr (J > 0 && (J & 3) == 1) asm volatile("s_bitset1_b32 m0, 12");
  else if constexpr (J > 0 && (J & 3) == 2) asm volatile("s_bitset1_b32 m0, %0" ::"i"(J < 4 ? 14 : 13));
  else if constexpr (J > 0) asm volatile("s_bitset0_b32 m0, 12");
#define GMF(D, X, Y, C) asm volatile("v_mfma_f32_16x16x32_bf16 " D "[%c0:%c1], " X ", " Y ", " C ::"i"(d), "i"(d + 3), "i"(a), "i"(a + 3), "i"(b), "i"(b + 3))
#define GMF2(D, C) do { if constexpr (TR) GMF(D, "v[%c2:%c3]", "a[%c4:%c5]", C); else GMF(D, "a[%c4:%c5]", "v[%c2:%c3]", C); } while (0)
  if constexpr ((MODE == 1 || MODE == 4) && ks == 0) { if constexpr (inV) GMF2("v", "0"); else GMF2("a", "0"); }
  else if constexpr (inV) GMF2("v", "v[%c0:%c1]");
  else GMF2("a", "a[%c0:%c1]");
#undef GMF2
#undef GMF
  if constexpr (e_st(MODE, GG) >= 0) {
    static_assert(MODE == 4 || ESCH<TR, MODE == 2>.op0[GG] >= (e_st(MODE, GG) + 1) * (MODE == 2 ? 16 : 8));
    d_store<e_st(MODE, GG)>();
  }
  if constexpr (EPI) e_ops<TR, MODE == 2, ESCH<TR, MODE == 2>.op0[GG]>(make_iseq<ESCH<TR, MODE == 2>.nop[GG]>{});
  if constexpr (J >= 0 && J < 4) g_dma<false, (1 - H) * 2 + (gj & 1) + (gj >> 1) * 4, 90>();
  else if constexpr (J >= 4) g_dma<true, (1 - H) * 4 + gj, 91, NT>();
  if constexpr ((q == 1 || q == 2) && !(END && m == 7)) g_aread<(m < 7 ? P : P ^ 1), (m + 1) & 7, q - 1>(g);
  if constexpr (END) {
    if constexpr (e_slot(MODE, GG) >= 0) e_load<e_slot(MODE, GG)>();
    if constexpr (!TR && GG >= 3 && GG < 7 && e_slot(MODE, GG) < 0) asm volatile("s_nop 0");
    if constexpr (TR && GG >= 19 && GG < 21 && e_slot(MODE, GG) < 0) asm volatile("s_nop 0");
  } else {
    if constexpr (q >= 3 && q < 11 && (q & 1)) g_wread<P ^ 1, H, (G >> 4) * 4 + (q - 3) / 2>(g);
    if constexpr (H == 1 && G == 0) asm volatile("v_lshl_add_u32 v90, s82, 7, v92\n\ts_and_b32 s82, s80, 15");
    if constexpr (H == 1 && G == 4) asm volatile("s_lshr_b64 s[80:81], s[80:81], 4\n\tv_lshl_add_u32 v91, s82, 19, v93");
    constexpr int nst = e_nst(MODE, H ? 29 : 0, GG + 1);
    if constexpr (G == 61) asm volatile("s_waitcnt vmcnt(%0) lgkmcnt(0)\n\ts_barrier" ::"i"(12 + nst) : "memory");
  }
  if constexpr (EPI && GG == 127) {
    asm volatile("s_nop 7");
    e_ops<TR, MODE == 2, ESCH<TR, MODE == 2>.op0[128]>(make_iseq<ESCH<TR, MODE == 2>.nop[128]>{});
  }
  GAP();
}
template <bool TR, int P, int H, int MODE, bool NT, int... Gs> DI void g_half_(GM& g, iseq<Gs...>) { (g_gap<TR, P, H, Gs, MODE, NT>(g), ...); }
template <bool TR, int P, int H, int MODE, bool NT = false> DI void g_half(GM& g) { g_half_<TR, P, H, MODE, NT>(g, make_iseq<64>{}); }
template <bool TR, int P, int MODE, bool NT = false> DI void g_tile(GM& g) { g_half<TR, P, 0, MODE, NT>(g); g_half<TR, P, 1, MODE, NT>(g); }
template <int P, int... Is> DI void g_read0(GM& g, iseq<Is...>) {
  (g_wread<P, 0, Is>(g), ...); (g_wread<P, 1, Is>(g), ...);
  g_aread<P, 0, 0>(g); g_aread<P, 0, 1>(g);
}
template <int I> DI void g_pro1(const unsigned (&m)[4]) {
  constexpr int grp = I >> 2, i = I & 3, t = grp < 4 ? 0 : grp < 7 ? 1 : 2;
  constexpr bool isA = grp == 0 || grp == 3 || grp == 5;
  constexpr int jj = (grp == 3 ? 2 : 0) + (i & 1) + (i >> 1) * 4, ks = grp == 2 || grp == 6;
  asm volatile("s_add_u32 m0, %0, %1\n\ts_nop 0" ::"s"(m[(t & 1) * 2 + !isA]), "i"(isA ? jj * 4096 : ks * 16384 + (i >> 1) * 8192 + (i & 1) * 4096) : "scc");
  if constexpr (isA) g_dma<false, jj, t ? 90 : 92>();
  else g_dma<true, ks * 4 + i, t ? 91 : 93, NTW >= 2>();
}
template <int B, int... Is> DI void g_pro(const unsigned (&m)[4], iseq<Is...>) { (g_pro1<B + Is>(m), ...); }
template <bool TR, bool SCL, class HOOK> DI void g_loop(GM& g, unsigned evm, HOOK&& hook) {
  constexpr int nkt = SCL ? 7 : 16, P0 = SCL ? 0 : 1;
  asm volatile("" : "+s"(evm));
#pragma unroll 1
  for (int t = 1;; t += 2) {
    g_half<TR, P0 ^ 1, 0, 0, (SCL ? NTW >= 1 : NTW >= 2)>(g);
    if (__builtin_expect((evm >> t) & 1, 0)) { hook(t); GAP(); }
    g_half<TR, P0 ^ 1, 1, 0, (SCL ? NTW >= 1 : NTW >= 2)>(g);
    if (SCL && t + 2 >= nkt) break;
    g_half<TR, P0, 0, 0, (SCL ? NTW >= 1 : NTW >= 2)>(g);
    if constexpr (SCL) { if (__builtin_expect(t == 3, 0)) { hook(4); GAP(); } }
    else asm volatile("s_cmp_eq_u32 %0, 13\n\ts_cselect_b32 s78, 0x28000, s78" ::"s"(t) : "scc");
    g_half<TR, P0, 1, 0, (SCL ? NTW >= 1 : NTW >= 2)>(g);
    if (!SCL && t + 3 >= nkt) break;
  }
}

typedef __attribute__((ext_vector_type(4))) unsigned u32x4;
typedef __attribute__((ext_vector_type(4))) float f32x4;

constexpr int R_S = 128, R_P = 192, R_CI = 224, R_E = 96;
constexpr int R_TS = 112, R_RS = 114, R_M = 116;
constexpr int R_PD = 118, R_THR = 120, R_TMP = 121;
constexpr int r_dk(int q) { return q < 6 ? 90 + q : 120 + q; }
constexpr int S_V = 96, S_T = 97, S_K = 98, S_I = 99, S_EV = 100;
struct AC {
  unsigned rare;
};
DI bool redo() { u64 t; asm volatile("s_mov_b64 %0, s[54:55]" : "=s"(t)); return t != 0; }
template <int X, int F> DI void kread() {
  constexpr int r = 192 + 4 * (F & 15);
  asm volatile("ds_read_b128 a[%c0:%c1], v84 offset:%c2" ::"i"(r), "i"(r + 3), "i"(X * 32768 + F * 1024));
}
template <int X, int F> DI void vread() {
  constexpr int r = 192 + 4 * (F & 15);
  asm volatile("ds_read_b128 a[%c0:%c1], v85 offset:%c2" ::"i"(r), "i"(r + 3), "i"(X * 32768 + (F & 1) * 16384 + (F >> 1) * 1024));
}
template <int X, int... Fs> DI void kreads(iseq<Fs...>) { (kread<X, Fs>(), ...); }
template <int X, int J> DI void dma_m0() {
  if constexpr (J == 0) asm volatile("s_mov_b32 m0, s%c0" ::"i"(78 + X));
  else if constexpr (J == 4) asm volatile("s_or_b32 m0, s%c0, 0x1000" ::"i"(78 + X));
  else if constexpr (J == 8) asm volatile("s_bitset0_b32 m0, 16");
  else if constexpr (J == 12) asm volatile("s_bitset0_b32 m0, 12");
}
template <int J> DI void dma_at() {
  if constexpr (J < 8)
    asm volatile("buffer_load_dwordx4 v%c0, s[88:91], s96 offen offset:%1 sc1 lds" ::"i"(86 + (J >= 4)), "i"((J & 3) * 1024) : "memory");
  else
    asm volatile("buffer_load_dwordx4 v%c0, s[92:95], s98 offen offset:%1 sc1 lds" ::"i"(r_dk(J - 8)), "i"((J & 3) * 1024) : "memory");
}
template <int Q> DI void dma4k() {
  asm volatile("s_add_u32 m0, s79, %0\n\ts_add_u32 s48, s98, %1\n\tbuffer_load_dwordx4 v%c2, s[88:91], s48 offen sc1 lds\n\t"
               "buffer_load_dwordx4 v%c2, s[88:91], s48 offen offset:1024 sc1 lds\n\tbuffer_load_dwordx4 v%c2, s[88:91], s48 offen offset:2048 sc1 lds\n\t"
               "buffer_load_dwordx4 v%c2, s[88:91], s48 offen offset:3072 sc1 lds"
               ::"i"((Q >> 1) * 32768 + (Q & 1) * 4096), "i"((Q >> 1) * 32768), "i"(86 + (Q & 1)) : "memory", "scc");
}
template <int Q> DI void acc_zero2() {
  asm volatile("v_accvgpr_write_b32 a[%c0], 0\n\tv_accvgpr_write_b32 a[%c1], 0" ::"i"(2 * Q), "i"(2 * Q + 1));
}
#define SMV 0
struct SMS { int ex[128], op0[128], nop[128], last; };
constexpr SMS sm_sched() {
  SMS z{};
  int et[32] = {}, e = 0;
  for (int t = 0; t < 128; ++t) {
    const int g = t & 15;
    const bool x = t < 48 ? (g >= 8 && (g & 1) && g < 15) : t < 64 ? (g >= 9 && g < 15) : (g >= 8 && g < 14);
    z.ex[t] = x && e < 32 ? e : -1;
    if (z.ex[t] >= 0) et[e++] = t;
  }
  int o = 0;
  for (int t = 0; t < 128; ++t) {
    const int g = t & 15;
    int cap = z.ex[t] >= 0 || t < 8 || (t < 48 && (g == 7 || (g >= 8 && !(g & 1)))) || t == 56 || t == 63 || t == 111 ? 0 : 1;
    z.op0[t] = o; z.nop[t] = 0;
    while (cap > 0 && o < 48 && et[2 * (o / 3) + (o % 3 == 0 ? 0 : 1)] + 2 <= t) {
      const int el = 2 * (o / 3) + o % 3;
      if (!(o % 3 < 2 && (el == 0 || el == 4))) --cap;
      ++o; ++z.nop[t]; z.last = t;
    }
  }
  for (int k = 16; k < 32; ++k) if (z.op0[et[k]] < 3 * ((k - 16) / 2 + 1)) z.last = 1000;
  return z;
}
constexpr SMS SM = sm_sched();
static_assert(SM.last <= 117);
constexpr int p_reg(int Y, int k) { return R_P + 16 * Y + 4 * ((k >> 3) * 2 + ((k >> 1) & 1)) + ((k >> 2) & 1) * 2 + (k & 1); }
template <int Y, int O> DI void sm_op() {
  constexpr int k = O / 3, e = 2 * k + O % 3, ts = R_TS + ((e >> 2) & 1);
  if constexpr (O % 3 == 2) asm volatile("v_cvt_pk_bf16_f32 v%c0, v%c1, v%c2" ::"i"(p_reg(Y, k)), "i"(R_E + ((2 * k) & 15)), "i"(R_E + ((2 * k + 1) & 15)));
  else if constexpr (e == 1 || e == 5) asm volatile("v_add_f32_e32 v%c0, v%c1, v%c2" ::"i"(ts), "i"(R_E + e - 1), "i"(R_E + e));
  else if constexpr (e != 0 && e != 4) asm volatile("v_add_f32_e32 v%c0, v%c0, v%c1" ::"i"(ts), "i"(R_E + (e & 15)));
}
template <int Y, int T> DI void sm_gap(AC& c) {
  if constexpr (SM.ex[T] >= 0) asm volatile("v_exp_f32 v%c0, v%c1" ::"i"(R_E + (SM.ex[T] & 15)), "i"(R_S + 32 * Y + SM.ex[T]));
  if constexpr (SM.nop[T] > 0) sm_op<Y, SM.op0[T]>();
  if constexpr (SM.nop[T] > 1) sm_op<Y, SM.op0[T] + 1>();
  if constexpr (SM.nop[T] > 2) sm_op<Y, SM.op0[T] + 2>();
  if constexpr (T == SM.last + 1) asm volatile("v_add_f32_e32 v%c0, v%c1, v%c2" ::"i"(R_TMP), "i"(R_TS), "i"(R_TS + 1));
  if constexpr (T == SM.last + 2) asm volatile("v_cmp_nle_f32_e64 s[54:55], v%c0, v%c1" ::"i"(R_TMP), "i"(R_THR));
}
#define W_A0 1
template <int X, int MODE, int G> DI void gapA(AC& c) {
  constexpr bool FIRST = MODE == 1 || MODE == 2, NOQK = MODE >= 3;
  constexpr int Xp = X ^ 1, kb = G >> 4, g = G & 15, qb = g >> 3, s = g & 7;
  if constexpr (MODE == 3 && G < 48 && G % 3 == 0) {
    constexpr int f = G / 3;
    asm volatile("buffer_load_dwordx4 a[%c0:%c1], v84, s[88:91], s75 offen offset:%2 sc1" ::"i"(128 + 4 * f), "i"(131 + 4 * f), "i"((f & 3) * 1024) : "memory");
    if constexpr ((f & 3) == 3) asm volatile("s_addk_i32 s75, 4096");
  }
  if constexpr (G == 48) asm volatile("s_waitcnt vmcnt(%0) lgkmcnt(0)\n\ts_barrier" ::"i"(MODE == 3 ? 16 : MODE == 2 ? 12 : MODE == 1 ? W_A0 : 0) : "memory");
  else if constexpr (g == 0 && !NOQK) asm volatile("s_waitcnt lgkmcnt(0)");
  if constexpr (!NOQK) {
    constexpr int r = 192 + 4 * ((kb & 1) * 8 + s), q = 128 + 4 * g;
    constexpr int d = R_S + 32 * X + 4 * (kb * 2 + qb), ci = s == 0 ? R_CI + 4 * (kb * 2 + qb) : d;
    if constexpr (FIRST && s == 0)
      asm volatile("v_mfma_f32_16x16x32_bf16 v[%c0:%c1], a[%c2:%c3], a[%c4:%c5], 0" ::"i"(d), "i"(d + 3), "i"(r), "i"(r + 3), "i"(q), "i"(q + 3));
    else
      asm volatile("v_mfma_f32_16x16x32_bf16 v[%c0:%c1], a[%c2:%c3], a[%c4:%c5], v[%c6:%c7]" ::"i"(d), "i"(d + 3), "i"(r), "i"(r + 3), "i"(q), "i"(q + 3),
                   "i"(ci), "i"(ci + 3));
    if constexpr (g < 8 && kb < 3) kread<X, (kb + 1) * 8 + g>();
    constexpr int S0 = SMV ? 40 : 14, S1 = SMV ? 41 : 30, S2 = SMV ? 42 : 45, S3 = SMV ? 43 : 46;
    if constexpr (G == S0) asm volatile("s_add_u32 s96, s97, 0x1000000");
    if constexpr (G == S1) dma_m0<X, 0>();
    if constexpr (G == S2) asm volatile("s_mov_b32 s97, s98");
    if constexpr (G == S3) asm volatile("s_add_u32 s98, s98, s99");
    if constexpr (G >= 56 && !(G & 1)) dma_at<(G - 56) / 2>();
    if constexpr (G == 57) asm volatile("s_lshr_b32 s100, s100, 1\n\ts_add_u32 s56, s56, 1");
    if constexpr (G == 59 && !FIRST) asm volatile("s_and_b32 s49, s100, 1\n\ts_or_b32 s49, s49, s54");
    if constexpr (G == 61 && !FIRST) asm volatile("s_or_b32 %0, s49, s55" : "=s"(c.rare));
    if constexpr (G == 61 && FIRST) asm volatile("s_and_b32 %0, s100, 1" : "=s"(c.rare));
  } else if constexpr (SM.nop[64 + G] > 0) asm volatile("s_nop 0");
  if constexpr (!FIRST && g < 8 && kb == 3) vread<Xp, g>();
  if constexpr (MODE == 2 && (G & 3) == 1) d_store<16 + G / 4>();
  if constexpr (FIRST) acc_zero2<G>();
  else sm_gap<Xp, 64 + G>(c);
  GAP();
}
template <int R> DI void rescale1(float alpha) {
  float t;
  asm volatile("v_accvgpr_read_b32 %0, a[%c2]\n\tv_mul_f32 %0, %0, %1\n\tv_accvgpr_write_b32 a[%c2], %0" : "=&v"(t) : "v"(alpha), "i"(R));
}
template <int... Rs> DI void rescale_all(const float (&alpha)[2], iseq<Rs...>) { (rescale1<Rs>(alpha[(Rs >> 2) & 1]), ...); }
template <bool MAX> DI float row_reduce(float v) {
  auto p = __builtin_amdgcn_permlane32_swap(__float_as_uint(v), __float_as_uint(v), false, false);
  v = MAX ? fmaxf(__uint_as_float(p[0]), __uint_as_float(p[1])) : __uint_as_float(p[0]) + __uint_as_float(p[1]);
  p = __builtin_amdgcn_permlane16_swap(__float_as_uint(v), __float_as_uint(v), false, false);
  return MAX ? fmaxf(__uint_as_float(p[0]), __uint_as_float(p[1])) : __uint_as_float(p[0]) + __uint_as_float(p[1]);
}
template <int Y, int QB> DI float row_max() {
  constexpr int r = R_S + 32 * Y + 4 * QB;
  float mx;
  asm volatile("v_max_f32 %0, v%c1, v%c2\n\tv_max3_f32 %0, %0, v%c3, v%c4" : "=&v"(mx) : "i"(r), "i"(r + 1), "i"(r + 2), "i"(r + 3));
  static_for([&](auto K) {
    constexpr int q = r + 8 * (K + 1);
    float t = mx;
    asm volatile("v_max3_f32 %0, %0, v%c1, v%c2\n\tv_max3_f32 %0, %0, v%c3, v%c4" : "+v"(t) : "i"(q), "i"(q + 1), "i"(q + 2), "i"(q + 3));
    mx = t;
  }, make_iseq<3>{});
  return row_reduce<true>(mx);
}
template <int R> DI void v_sub(float d) { asm volatile("v_sub_f32 v%c0, v%c0, %1" ::"i"(R), "v"(d)); }
template <int B> DI void s_shift(const float (&d)[2]) { static_for([&](auto E) { v_sub<B + E>(d[(E >> 2) & 1]); }, make_iseq<32>{}); }
DI void m_shift(const float (&d)[2]) {
  s_shift<R_CI>(d);
  v_sub<R_M>(-d[0]); v_sub<R_M + 1>(-d[1]);
}
DI void set_mask0(int wid) {
  const int mr = wid * 32 + __float_as_int(vget<89>());
  static_for([&](auto I) {
    constexpr int e = I, k = (e >> 3) * 16 - ((e >> 2) & 1) * 16 + (e & 3);
    const float x = vget<R_S + e>();
    vset<R_S + e>(k > mr ? -INFINITY : x);
  }, make_iseq<32>{});
}
template <int X> DI void sm_redo() {
  constexpr int Xp = X ^ 1;
  float d[2], dt[2], alpha[2], ts[2] = {0.f, 0.f};
  static_for([&](auto Q) {
    constexpr int qb = Q;
    const float pd = vget<R_PD + qb>();
    d[qb] = fmaxf(row_max<Xp, qb>() - pd, 0.f);
    dt[qb] = d[qb] + pd;
    vset<R_PD + qb>(d[qb]);
    alpha[qb] = __builtin_amdgcn_exp2f(-d[qb]);
    vset<R_RS + qb>(vget<R_RS + qb>() * alpha[qb]);
  }, make_iseq<2>{});
  vset<R_THR>(d[0] > 0.f || d[1] > 0.f ? -1.f : 4096.f);
  m_shift(d);
  static_for([&](auto K) {
    constexpr int k = K, qb = (k >> 1) & 1;
    const float e0 = __builtin_amdgcn_exp2f(vget<R_S + 32 * Xp + 2 * k>() - dt[qb]), e1 = __builtin_amdgcn_exp2f(vget<R_S + 32 * Xp + 2 * k + 1>() - dt[qb]);
    ts[qb] += e0; ts[qb] += e1;
    vset<p_reg(Xp, k)>(__uint_as_float(pk2(e0, e1)));
  }, make_iseq<16>{});
  vset<R_TS>(ts[0]); vset<R_TS + 1>(ts[1]);
  rescale_all(alpha, make_iseq<128>{});
  asm volatile("s_nop 1" ::: "memory");
  GAP();
}
template <int X, int MODE, int... Gs> DI void phaseA_(AC& c, iseq<Gs...>) { (gapA<X, MODE, Gs>(c), ...); }
template <int X, int MODE> DI void phaseA(AC& c) { phaseA_<X, MODE>(c, make_iseq<64>{}); }

template <int X, bool SMX, int G> DI void gapB(AC& c) {
  constexpr int Xp = X ^ 1, blk = G >> 4, g = G & 15;
  if constexpr (g == 0) asm volatile("s_waitcnt lgkmcnt(0)");
  constexpr int qb = (g >> 1) & 1, s = g & 1, r = 192 + 4 * ((blk & 1) * 8 + (g >> 2) * 2 + s), o = 4 * ((blk * 4 + (g >> 2)) * 2 + qb);
  constexpr int p = R_P + 16 * Xp + 4 * (s * 2 + qb);
  asm volatile("v_mfma_f32_16x16x32_bf16 a[%c0:%c1], a[%c2:%c3], v[%c4:%c5], a[%c0:%c1]" ::"i"(o), "i"(o + 3), "i"(r), "i"(r + 3), "i"(p), "i"(p + 3));
  if constexpr (g < 8) {
    if constexpr (blk < 3) vread<Xp, (blk + 1) * 8 + g>();
    else if constexpr (SMX) kread<Xp, g>();
  }
  if constexpr (SMX) {
    if constexpr (G == 1 || G == 2) asm volatile("v_add_f32_e32 v%c0, v%c0, v%c1" ::"i"(R_RS + G - 1), "i"(R_TS + G - 1));
    if constexpr (g == 7 && blk < 3) dma_m0<X, blk * 4 + 4>();
    if constexpr (g >= 8 && !(g & 1) && blk < 3) dma_at<blk * 4 + 4 + (g - 8) / 2>();
    if constexpr (G == 55 && X == 1) asm volatile("s_cmp_eq_u32 s56, s76");
    if constexpr (G == 3) asm volatile("s_lshr_b32 s77, s77, 1");
    if constexpr (G == 4) asm volatile("s_bitcmp1_b32 s77, 0");
    if constexpr (G == 5) asm volatile("s_cselect_b64 s[52:53], s[62:63], 0");
    if constexpr (G == 6) asm volatile(FL_ARM("s[52:53]"));
    if constexpr (G == 56) asm volatile(FL_POLL ::: "memory");
    sm_gap<X, G>(c);
  }
  GAP();
}
template <int X, int... Gs> DI void phaseB_(AC& c, iseq<Gs...>) { (gapB<X, true, Gs>(c), ...); }
template <int X> DI void phaseB(AC& c) { phaseB_<X>(c, make_iseq<64>{}); }
constexpr int sm_nex() { int n = 0; for (int t = 0; t < 64; ++t) n += SM.ex[t] >= 0; return n; }
DI void phaseB0(unsigned mode, int wid) {
  dma_m0<0, 4>();
  kreads<1>(make_iseq<8>{});
  dma_at<4>();
  asm volatile("s_nop 3");
  float d[2];
  if (mode & 2) {
    d[0] = vget<122>(); d[1] = vget<123>();
    dma_at<5>();
  } else {
    if (__builtin_expect(mode & 4, 0)) set_mask0(wid);
    d[0] = row_max<0, 0>(); GAP(); dma_at<5>();
    d[1] = row_max<0, 1>();
#pragma unroll
    for (int qb = 0; qb < 2; ++qb) if (!(d[qb] > -3.0e38f)) d[qb] = 0.f;
  }
  GAP();
  vset<R_M>(d[0]); vset<R_M + 1>(d[1]);
  dma_at<6>();
  const f32x2c nd[2] = {{-d[0], -d[0]}, {-d[1], -d[1]}};
  static_for([&](auto E) {
    constexpr int e = E;
    const f32x2c v = nd[(e >> 1) & 1];
    asm volatile("v_pk_add_f32 v[%c0:%c1], v[%c0:%c1], %4\n\tv_mov_b64 v[%c2:%c3], %4" ::"i"(R_S + 2 * e), "i"(R_S + 2 * e + 1), "i"(R_CI + 2 * e), "i"(R_CI + 2 * e + 1), "v"(v));
    if constexpr (e == 3) dma_at<7>();
    if constexpr (e == 6) dma_m0<0, 8>();
    if constexpr (e == 7 || e == 11 || e == 15) dma_at<8 + (e - 7) / 4>();
  }, make_iseq<16>{});
  static_assert(SM.op0[64] >= 3 * ((sm_nex() - 15) / 2) && SM.op0[64] <= 24);
  static_for([&](auto E) {
    constexpr int e = E;
    if constexpr (e < sm_nex()) asm volatile("v_exp_f32 v%c0, v%c1" ::"i"(R_E + e), "i"(R_S + e));
    if constexpr (e == 3) dma_at<11>();
    if constexpr (e == 6) dma_m0<0, 12>();
    if constexpr (e == 7 || e == 11 || e == 15) dma_at<12 + (e - 7) / 4>();
  }, make_iseq<16>{});
  static_for([&](auto O) { sm_op<0, O>(); }, make_iseq<SM.op0[64]>{});
  dma_at<15>();
  static_for([&](auto E) { asm volatile("v_exp_f32 v%c0, v%c1" ::"i"(R_E + (int)E), "i"(R_S + 16 + (int)E)); }, make_iseq<(sm_nex() > 16 ? sm_nex() - 16 : 0)>{});
  asm volatile("buffer_store_dwordx2 v[%c0:%c1], v88, s[88:91], s76 offen sc1\n\ts_bitcmp1_b32 s77, 0\n\ts_cselect_b64 s[52:53], s[62:63], 0\n\t"
               FL_ARM("s[52:53]") "\n\t" FL_POLL ::"i"(R_M), "i"(R_M + 1) : "memory", "scc");
  GAP();
}
constexpr int T_ZO = 8;
#define NINF2(R) "v_mov_b64 v[" #R ":" #R "+1], v[122:123]\n\t"
#define NINF4(R) NINF2(R) NINF2(R + 2)
#define TRI(R) "v_cndmask_b32_e64 v[" #R "], v[" #R "], v122, s[48:49]\n\tv_cndmask_b32_e64 v[" #R "+1], v[" #R "+1], v122, s[50:51]\n\t" \
               "v_cndmask_b32_e64 v[" #R "+2], v[" #R "+2], v122, s[52:53]\n\tv_cndmask_b32_e64 v[" #R "+3], v[" #R "+3], v122, s[54:55]\n\t"
DI void ev_code() {
  asm volatile(
    "s_getpc_b64 s[82:83]\n\ts_add_u32 s82, s82, 12\n\ts_addc_u32 s83, s83, 0\n\ts_branch 9f\n\t"
    "s_bitcmp0_b32 s58, s56\n\ts_cbranch_scc1 1f\n\t" FL_SET("s70") "\n\t" FL_SET("s71") "\n"
    "1:\n\ts_bitcmp0_b32 s57, s56\n\ts_cbranch_scc1 2f\n\t" FL_CHECK("s[62:63]") "\n\t"
    "s_add_u32 s98, s98, s68\n\ts_mov_b32 s68, s69\n\ts_mov_b32 s69, 0\n\ts_mov_b64 s[62:63], s[64:65]\n\ts_mov_b64 s[64:65], s[66:67]\n"
    "2:\n\ts_bitcmp0_b32 s59, s56\n\ts_cbranch_scc1 3f\n\ts_mov_b32 s98, s72\n\ts_bitcmp0_b32 s73, 0\n\ts_cbranch_scc1 3f\n\t"
    "v_lshrrev_b32 v90, 7, v86\n\tv_and_b32 v91, 6, v90\n\tv_lshlrev_b32 v91, 4, v91\n\tv_and_b32 v92, 0x70, v86\n\tv_xor_b32 v91, v91, v92\n\t"
    "s_movk_i32 s48, 896\n\tv_mad_u32_u24 v94, v90, s48, v91\n\ts_mov_b64 s[92:93], s[80:81]\n\ts_mov_b32 s94, 229376\n\ts_movk_i32 s99, 128\n\t"
    "v_add_u32 v90, 28672, v94\n\tv_add_u32 v91, 34816, v94\n\tv_add_u32 v92, 40960, v94\n\tv_add_u32 v93, 47104, v94\n\t"
    "v_add_u32 v95, 6144, v94\n\tv_add_u32 v126, 12288, v94\n\tv_add_u32 v127, 18432, v94\n"
    "3:\n\ts_bitcmp0_b32 s60, s56\n\ts_cbranch_scc1 4f\n\t"
    "v_readlane_b32 s54, v78, %0\n\ts_add_u32 m0, s78, 0x10000\n\t"
    "v_lshlrev_b32 v121, 9, v84\n\tv_and_b32 v121, 0x38000, v121\n\tv_lshrrev_b32 v122, 9, v84\n\tv_lshl_add_u32 v121, v122, 12, v121\n\t"
    "v_lshrrev_b32 v122, 3, v84\n\tv_xor_b32 v122, v122, v84\n\tv_and_or_b32 v121, v122, 48, v121\n\t"
    "buffer_load_dwordx4 v121, s[88:91], s54 offen sc1 lds\n\ts_addk_i32 s54, 7168\n\t"
    "buffer_load_dwordx4 v121, s[88:91], s54 offen offset:1024 sc1 lds\n\ts_addk_i32 s54, 7168\n\t"
    "buffer_load_dwordx4 v121, s[88:91], s54 offen offset:2048 sc1 lds\n\ts_addk_i32 s54, 7168\n\t"
    "buffer_load_dwordx4 v121, s[88:91], s54 offen offset:3072 sc1 lds\n"
    "4:\n\ts_bitcmp0_b32 s61, s56\n\ts_cbranch_scc1 6f\n\t"
    "s_sub_u32 s74, s74, 64\n\ts_cmp_ge_i32 s74, 0\n\ts_cbranch_scc1 6f\n\t"
    "v_cmp_gt_i32 s[48:49], 0, v89\n\tv_cmp_gt_i32 s[50:51], 1, v89\n\tv_cmp_gt_i32 s[52:53], 2, v89\n\tv_cmp_gt_i32 s[54:55], 3, v89\n\t"
    "v_mov_b32 v122, 0xff800000\n\tv_mov_b32 v123, 0xff800000\n\t" NINF4(248)
    "s_cmp_eq_i32 s74, -32\n\ts_cbranch_scc0 5f\n\t" TRI(240) TRI(252) "s_setpc_b64 s[86:87]\n"
    "5:\n\t" NINF4(240) NINF4(244) NINF4(252) NINF4(232) TRI(224) TRI(236)
    "s_cmp_eq_i32 s74, -64\n\ts_cbranch_scc1 6f\n\t" NINF4(224) NINF4(228) NINF4(236)
    "6:\n\ts_setpc_b64 s[86:87]\n9:"
    ::"i"(T_ZO) : "scc");
}
DI void ev_call() { asm volatile("s_swappc_b64 s[86:87], s[82:83]" ::: "vcc", "scc", "memory"); GAP(); }

#define ACLOB_ALL "a0","a1","a2","a3","a4","a5","a6","a7","a8","a9","a10","a11","a12","a13","a14","a15","a16","a17","a18","a19","a20","a21","a22","a23","a24","a25","a26","a27","a28","a29","a30","a31","a32","a33","a34","a35","a36","a37","a38","a39","a40","a41","a42","a43","a44","a45","a46","a47","a48","a49","a50","a51","a52","a53","a54","a55","a56","a57","a58","a59","a60","a61","a62","a63","a64","a65","a66","a67","a68","a69","a70","a71","a72","a73","a74","a75","a76","a77","a78","a79","a80","a81","a82","a83","a84","a85","a86","a87","a88","a89","a90","a91","a92","a93","a94","a95","a96","a97","a98","a99","a100","a101","a102","a103","a104","a105","a106","a107","a108","a109","a110","a111","a112","a113","a114","a115","a116","a117","a118","a119","a120","a121","a122","a123","a124","a125","a126","a127","a128","a129","a130","a131","a132","a133","a134","a135","a136","a137","a138","a139","a140","a141","a142","a143","a144","a145","a146","a147","a148","a149","a150","a151","a152","a153","a154","a155","a156","a157","a158","a159","a160","a161","a162","a163","a164","a165","a166","a167","a168","a169","a170","a171","a172","a173","a174","a175","a176","a177","a178","a179","a180","a181","a182","a183","a184","a185","a186","a187","a188","a189","a190","a191","a192","a193","a194","a195","a196","a197","a198","a199","a200","a201","a202","a203","a204","a205","a206","a207","a208","a209","a210","a211","a212","a213","a214","a215","a216","a217","a218","a219","a220","a221","a222","a223","a224","a225","a226","a227","a228","a229","a230","a231","a232","a233","a234","a235","a236","a237","a238","a239","a240","a241","a242","a243","a244","a245","a246","a247","a248","a249","a250","a251","a252","a253","a254","a255"

struct EP { float fa[2], fb[2]; };
template <int I, int E, int MODE> DI void epi_pair(const EP& p) {
  constexpr int qb = I & 1, r = 16 * (I >> 1) + 4 * qb + (E >> 1) * 8 + (E & 1) * 2, t = 96 + 2 * E, w = 128 + 4 * I + E;
  if constexpr (MODE == 2) asm volatile("v_accvgpr_read_b32 v106, a[%c0]" ::"i"(w));
  asm volatile("v_accvgpr_read_b32 v%c0, a[%c2]\n\tv_accvgpr_read_b32 v%c1, a[%c3]" ::"i"(t), "i"(t + 1), "i"(r), "i"(r + 1));
  if constexpr (MODE) asm volatile("v_mul_f32 v%c0, v%c0, %2\n\tv_mul_f32 v%c1, v%c1, %2" ::"i"(t), "i"(t + 1), "v"(p.fa[qb]));
  if constexpr (MODE == 2)
    asm volatile("v_lshlrev_b32 v104, 16, v106\n\tv_and_b32 v105, 0xffff0000, v106\n\tv_fmac_f32 v%c0, v104, %2\n\tv_fmac_f32 v%c1, v105, %2"
                 ::"i"(t), "i"(t + 1), "v"(p.fb[qb]));
  asm volatile("v_cvt_pk_bf16_f32 v%c0, v%c1, v%c2" ::"i"(w), "i"(t), "i"(t + 1));
}
template <int I> DI void epi_store() {
  asm volatile("buffer_store_dwordx4 v[%c0:%c1], v84, s[88:91], s101 offen offset:%2 sc1" ::"i"(128 + 4 * I), "i"(131 + 4 * I), "i"((I & 3) * 1024) : "memory");
  if constexpr ((I & 3) == 3) asm volatile("s_addk_i32 s101, 4096");
}
constexpr int R_TB = 107, R_VS = 111;
template <int HF, int R> DI void p_read() {
  constexpr int mb = R >> 4, r = 64 * HF + 2 * R;
  asm volatile("ds_read_b64_tr_b16 a[%c1:%c2], v%c0 offset:%c3" ::"i"(R_TB + 2 * HF + mb), "i"(r), "i"(r + 1), "i"((R & 15) * 2048));
}
template <int QB, int E> DI void p_cvt() {
  asm volatile("v_cvt_pk_bf16_f32 v%c0, v%c1, v%c2" ::"i"(208 + 4 * QB + E), "i"(192 + 8 * QB + 2 * E), "i"(193 + 8 * QB + 2 * E));
}
template <int QB, int PR> DI void p_store() {
  asm volatile("buffer_store_dwordx4 v[%c0:%c1], v%c2, s[88:91], s96 offen offset:%3 sc1" ::"i"(208 + 4 * QB), "i"(211 + 4 * QB), "i"(R_VS + QB),
               "i"(PR * 128) : "memory");
}
constexpr int p_dma(int HF, int G) { return G == 3 && !HF ? 0 : G == 11 ? 1 - HF : G == 15 ? 2 - HF : G == 19 ? 3 - HF : G == 27 && HF ? 3 : -1; }
template <int HF, int G> DI void p_gap() {
  constexpr int ch = G >> 3, t = G & 7, a = 64 * HF + 32 * (ch & 1) + 4 * t, b = 128 + 4 * (2 * t + (ch >> 1)), d = 192 + 4 * ch;
  if constexpr (t == 0)
    asm volatile("v_mfma_f32_16x16x32_bf16 v[%c0:%c1], a[%c2:%c3], v[%c4:%c5], 0" ::"i"(d), "i"(d + 3), "i"(a), "i"(a + 3), "i"(b), "i"(b + 3));
  else
    asm volatile("v_mfma_f32_16x16x32_bf16 v[%c0:%c1], a[%c2:%c3], v[%c4:%c5], v[%c0:%c1]" ::"i"(d), "i"(d + 3), "i"(a), "i"(a + 3), "i"(b), "i"(b + 3));
  p_read<HF ^ 1, G>();
  if constexpr (G == 0) asm volatile("v_xor_b32 v%c0, s100, v%c0" ::"i"(R_TB + 2 * HF + 1));
  if constexpr (G == 17) asm volatile("v_xor_b32 v%c0, s100, v%c0" ::"i"(R_TB + 2 * (HF ^ 1)));
  if constexpr (G >= 4 && G <= 8 && G != 7) p_cvt<1, (G < 7 ? G - 4 : 3)>();
  if constexpr (G == 24 || (G == 7 && HF == 0)) asm volatile("s_waitcnt lgkmcnt(8)");
  if constexpr (G == 7 && HF == 1) asm volatile("s_waitcnt vmcnt(13) lgkmcnt(8)\n\ts_barrier" ::: "memory");
  if constexpr (G == 10) p_store<1, HF ^ 1>();
  if constexpr (G >= 20 && G < 24) p_cvt<0, G - 20>();
  if constexpr (G == 25) p_store<0, HF>();
  if constexpr (p_dma(HF, G) >= 0) dma_at<(HF ? 8 : 12) + p_dma(HF, G)>();
  if constexpr (HF == 0 && G == 2) asm volatile("s_bitset0_b32 m0, 12");
  if constexpr (HF == 0 && G == 13) asm volatile("s_add_u32 s96, s96, 0x40000");
  if constexpr (HF == 0 && G == 26) asm volatile("s_add_u32 s98, s98, 128");
  if constexpr (HF == 0 && G == 27) asm volatile("s_cmp_lt_u32 s98, 0x380");
  if constexpr (HF == 0 && G == 28) asm volatile("s_cselect_b32 s99, s99, 0x31000");
  if constexpr (HF == 1 && G == 1) asm volatile("s_xor_b32 s100, s100, 0x10000");
  if constexpr (HF == 1 && G == 2) asm volatile("s_xor_b32 s97, s97, s100");
  if constexpr (HF == 1 && G == 9) asm volatile("s_or_b32 m0, s97, s99");
  if constexpr (HF == 1 && G == 30) asm volatile("s_cmp_lt_u32 s98, 0x500");
  GAP();
}
template <int HF, int... Gs> DI void p_half(iseq<Gs...>) { (p_gap<HF, Gs>(), ...); }
constexpr int f_dma(int G) { return G == 2 ? 0 : G == 5 ? 1 : G == 8 ? 2 : G >= 12 && G <= 36 && G % 6 == 0 ? 1 + G / 6 : G >= 50 && G <= 68 && G % 6 == 2 ? G / 6 : -1; }
constexpr int f_ndma(int G) { int n = 0; for (int g = 0; g < G; ++g) n += f_dma(g) >= 0; return n; }
static_assert(f_ndma(74) == 12);
template <int MODE, int G> DI void gapF(AC& c, const EP& p) {
  if constexpr (MODE && G == 48) asm volatile("s_waitcnt lgkmcnt(0)\n\ts_barrier" ::: "memory");
  if constexpr (G < 64) gapB<0, false, G>(c);
  if constexpr (MODE == 2 && G >= 10 && (G - 10) % 16 == 0) asm volatile("s_waitcnt vmcnt(%0)" ::"i"(12 - (G - 10) / 4 + f_ndma(G)) : "memory");
  if constexpr (G >= 10) epi_pair<(G - 10) / 4, (G - 10) & 3, MODE>(p);
  if constexpr (MODE == 3 && G < 64 && (G & 1)) {
    constexpr int db = G >> 2, qb = (G >> 1) & 1, o = 4 * (db * 2 + qb), a = 224 + 4 * (db & 1), b = 128 + 4 * ((db >> 1) * 2 + qb);
    if constexpr ((G & 7) == 1) asm volatile("s_waitcnt vmcnt(%0)" ::"i"(14 - (G >> 2) + f_ndma(G)) : "memory");
    asm volatile("v_mfma_f32_16x16x32_bf16 a[%c0:%c1], v[%c2:%c3], a[%c4:%c5], a[%c0:%c1]" ::"i"(o), "i"(o + 3), "i"(a), "i"(a + 3), "i"(b), "i"(b + 3));
  }
  if constexpr (MODE == 0) {
    if constexpr (G >= 14 && ((G - 14) & 3) == 0) epi_store<(G - 14) / 4>();
    if constexpr (G == 73) epi_store<15>();
  } else {
    if constexpr (G == 0) asm volatile("s_or_b32 m0, s78, 0x1000\n\ts_movk_i32 s98, 256");
    if constexpr (G == 15) asm volatile("s_bitset0_b32 m0, 12");
    if constexpr (G == 48) asm volatile("s_or_b32 m0, s79, 0x1000\n\ts_movk_i32 s98, 384");
    if constexpr (f_dma(G) >= 0) dma_at<8 + (f_dma(G) & 7)>();
    if constexpr (G == 73) asm volatile("s_waitcnt vmcnt(4) lgkmcnt(0)" ::: "memory");
    if constexpr (G >= 48 && G < 64) { p_read<0, 2 * (G - 48)>(); p_read<0, 2 * (G - 48) + 1>(); }
  }
  GAP();
}
template <int MODE, int... Gs> DI void phaseF(AC& c, const EP& p, iseq<Gs...>) { (gapF<MODE, Gs>(c, p), ...); }
template <int U, int K> DI void r_load(unsigned vo, unsigned so) {
  asm volatile("buffer_load_dwordx4 a[%c0:%c1], %2, s[88:91], %3 offen offset:%4 sc1" ::"i"(64 * U + 4 * K), "i"(64 * U + 4 * K + 3), "v"(vo),
               "s"(so + (K >> 2) * 4096), "i"((K & 1) * 512 + ((K >> 1) & 1) * 2048) : "memory");
}
template <int U, int K> DI void r_sum() {
  constexpr int d = 100 + 4 * (K & 1), b = 64 * U + 4 * K;
  if constexpr (K < 2) asm volatile("v_mfma_f32_16x16x32_bf16 v[%c0:%c1], v[96:99], a[%c2:%c3], 0" ::"i"(d), "i"(d + 3), "i"(b), "i"(b + 3));
  else asm volatile("v_mfma_f32_16x16x32_bf16 v[%c0:%c1], v[96:99], a[%c2:%c3], v[%c0:%c1]" ::"i"(d), "i"(d + 3), "i"(b), "i"(b + 3));
}
template <int U> DI void r_fin(unsigned vo, __amdgpu_buffer_rsrc_t srd, unsigned so) {
  asm volatile("" : "+s"(srd), "+s"(so));
  static_for([&](auto K) { r_sum<U, K>(); }, make_iseq<16>{});
  asm volatile("s_nop 7");
  static_for([&](auto E) { asm volatile("v_cvt_pk_bf16_f32 v%c0, v%c1, v%c2" ::"i"(116 + 4 * U + E), "i"(100 + 2 * E), "i"(101 + 2 * E)); }, make_iseq<4>{});
  asm volatile("buffer_store_dwordx4 v[%c3:%c4], %0, %1, %2 offen" OUTSCS ::"v"(vo), "s"(srd), "s"(so), "i"(116 + 4 * U), "i"(119 + 4 * U) : "memory");
}

#define CMAX 12
#define PREPX false
#define NYM 0
#define NYT 16
static_assert(NYT <= 16 && NYT >= 12 && !(NYT & 1));
enum { T_Q0, T_K0, T_OQ, T_OG, T_FPREP, T_FQ, T_FG, T_FS,   T_NPRE = 10, T_NST = 12, T_TAB = 14, T_SLAB = 16, T_PART, T_IT = 18, T_ITN = 23 };
struct TRow { unsigned v[64]; };
constexpr u64 kvmask(int m0, int m1) { u64 n = 0; for (int m = m0; m <= m1; ++m) n |= 0x101ull << m; return n; }
constexpr u64 k_order(bool isY, int rr) {
  const int cls = (rr + (rr >> 2)) & 3;
  return !isY ? (cls == 0 ? 0xfedcb9875a463210ull : cls == 1 ? 0xefadc6982b574310ull : cls == 2 ? 0xfebad7693c285410ull : 0xefcba8764d392510ull)
              : (cls == 0 ? 0xfedcb987543a6210ull : cls == 1 ? 0xefadc698254b7310ull : cls == 2 ? 0xfebad769325c8410ull : 0xefcba876432d9510ull);
}
constexpr TRow t_row(int r, int wid, int j) {
  TRow t{};
  const int rr = r & 15, hsel = rr & 1, pj = rr >> 1, kv = pj & 1, h = j + 8 * hsel, u = hsel * 8 + j;
  const bool isY = r >= 16, solo = pj == 7;
  const int mt = (rr >> 2) + (isY ? 4 : 0), g = j * 128, NY = solo ? 18 : NYT - 2 * ((NYM >> pj) & 1);
  const int nx0 = 32 - 2 * pj - NY, cx = nx0 < CMAX ? nx0 : CMAX, ex = nx0 - cx, qbF = isY ? 15 - pj : pj;
  const unsigned oQ = (unsigned)h << 20, oK = oQ + O_K, oP = O_P + (h * 8 + pj) * PSTR;
  if (wid == 4) {
    for (int l = 0; l < 64; ++l)
      t.v[l] = fo(l < 16 ? g + hsel * 16 + l : l < 24 ? g + 16 + hsel * 8 + l : l == 24 ? g + 48 + hsel * 8 + pj : l < 32 ? g + 64 + hsel * 8 + pj :
                  l < 48 ? 2304 + pj * 16 + l - 32 : 2048 + pj * 16 + l - 48);
    return t;
  }
  auto put64 = [&](int i, u64 x) { t.v[i] = (unsigned)x; t.v[i + 1] = (unsigned)(x >> 32); };
  t.v[T_Q0] = oQ + ((solo && !isY ? pj : 15 - pj) * 128 + wid * 32) * 512;
  t.v[T_K0] = oK + (isY ? cx : 0) * 32768;
  t.v[T_OQ] = oQ + pj * 131072 + (wid >> 1) * 65536 + (wid & 1) * 4096;
  t.v[T_OG] = oK + kv * TSB + mt * 131072 + (wid >> 1) * 65536 + (wid & 1) * (kv ? 8192 : 4096);
  t.v[T_FPREP] = fo(2048 + pj * 16 + u);
  t.v[T_FQ] = fo(g + 32 + hsel * 8 + pj);
  t.v[T_FG] = fo(g + hsel * 16 + kv * 8 + mt);
  t.v[T_FS] = fo(1024 + (qbF * 4 + wid) * 16 + j * 2 + hsel);
  t.v[T_ZO] = O_Z + pj * 64 * SEQ * 2 + (u * 4 + wid) * 64;
  const u64 own = 0x1ull << (kv * 8 + mt), needQ = isY ? 1ull << (16 + ((15 - pj) >> 1)) : (1ull << (16 + (pj >> 1))) | (1ull << (16 + ((15 - pj) >> 1)));
  put64(T_NPRE, isY ? needQ | (1ull << (cx >> 2)) | (1ull << ((cx + 1) >> 2)) : solo ? needQ : needQ | 0xffffull << 48);
  put64(T_NST, isY ? kvmask(cx >> 2, 3) | (solo ? 0 : 1ull << 25) : kvmask(0, solo ? 3 : 0));
  put64(T_TAB, k_order(isY, rr));
  t.v[T_SLAB] = O_S + qbF * 0x200000 + wid * 65536 + (j * 2 + hsel) * 1024;
  t.v[T_PART] = oP;
  for (int it = 0; it < 2; ++it) {
    unsigned* p = t.v + T_IT + it * T_ITN;
    const bool last = isY || it == 1;
    const int n = isY ? NY : it ? 2 * pj + 2 : nx0 < 2 ? 2 : nx0;
    const u64 need2 = solo ? 0 : kvmask(0, (2 * pj + 1) >> 2) | (0xffffull << 32);
    u64 q[3] = {0, 0, 0};
    unsigned jmp[2] = {0, 0};
    if (isY) {
      p[0] = (1u << (14 - cx)) | (solo ? 0 : 1u << (n - 2));
      q[0] = kvmask(4, (31 - 2 * pj) >> 2) & ~own; jmp[0] = ex * 32768; q[1] = solo ? 0 : 1ull << 24;
      p[1] = 2; p[13] = t.v[T_FG]; p[14] = fo(g + 96 + r);
    } else if (it == 0) {
      p[0] = 2u | (ex ? 1u << (cx - 2) : 0);
      q[0] = kvmask(0, cx > 0 ? (cx - 1) >> 2 : 0);
      if (ex) { q[1] = kvmask(4, 4 + ((ex - 1) >> 2)); jmp[1] = (16 - cx) * 32768; q[2] = need2; } else q[1] = need2;
      p[1] = 2; p[13] = fo(2304 + pj * 16 + u) + j * (8 * 4352 - 8); p[14] = fo(g + 64 + hsel * 8 + pj);
    } else {
      p[0] = p[1] = solo ? 0 : 1;
      q[0] = need2; p[13] = fo(g + 48 + hsel * 8 + pj); p[14] = fo(g + 96 + r);
    }
    p[2] = 1u << (n - 2);
    p[3] = isY || !it || solo ? 0 : n > 2 ? 1u << (n - 3) : 1u;
    p[4] = !last ? 0 : n > 2 ? 3u << (n - 3) : 1u << 31;
    for (int k = 0; k < 3; ++k) { p[5 + 2 * k] = (unsigned)q[k]; p[6 + 2 * k] = (unsigned)(q[k] >> 32); }
    p[11] = jmp[0]; p[12] = jmp[1];
    p[15] = oK;
    p[16] = (last ? 1 : 0) | (isY && !solo ? 2 : 0) | (n == 2 ? 4 : 0) | (n - 1) << 8;
    p[17] = wid * 32 - (n == 2 ? 64 : 0);
    p[18] = oQ + (pj * 128 + wid * 32) * 512;
    p[19] = 0;
    p[20] = (p[0] | (last ? 0 : 1u << n)) >> 1;
    p[21] = oK + (isY ? cx : 0) * 32768;
    p[22] = ((p[0] | p[1] | p[2] | p[3] | p[4]) & 0x7fffffffu) << 1;
  }
  return t;
}
template <int I> constexpr TRow t_base = I < 160 ? t_row(I / 5, I % 5, 0) : [] {
  TRow a = t_row(0, (I - 160) * 4, 0), b = t_row(0, (I - 160) * 4, 1);
  for (int l = 0; l < 64; ++l) a.v[l] = b.v[l] - a.v[l];
  return a;
}();
template <int I> constexpr bool t_linear = [] {
  for (int j = 1; j < 8; ++j) {
    const TRow b = t_row(I / 5, I % 5, j);
    for (int l = 0; l < 64; ++l) if (b.v[l] != t_base<I>.v[l] + j * t_base<160 + (I % 5) / 4>.v[l]) return false;
  }
  return true;
}();
struct Tab { TRow row[162]; };
template <int... I> constexpr Tab t_make(iseq<I...>) {
  static_assert((t_linear<(I < 160 ? I : 0)> && ...));
  return Tab{{t_base<I>...}};
}
__device__ const Tab g_tab = t_make(make_iseq<162>{});

typedef __attribute__((ext_vector_type(16))) unsigned u32x16;
template <class T, int I> DI T* arg_late() {
  unsigned lo, hi;
  asm volatile("v_readlane_b32 %0, v77, %2\n\tv_readlane_b32 %1, v77, %3\n\ts_nop 4" : "=s"(lo), "=s"(hi) : "i"(2 * I), "i"(2 * I + 1));
  return (T*)(((u64)hi << 32) | lo);
}
__global__ __launch_bounds__(256, 1) __attribute__((amdgpu_num_vgpr(77), amdgpu_num_sgpr(56))) void fused_kernel(
    const bf16* ckv_, const bf16* wuk_, const bf16* wuv_, const bf16* x_, const bf16* wq_, bf16* ws_, const bf16* ak_, const bf16* av_,
    const bf16* ash_, const bf16* assmk_, const bf16* assmD_, const bf16* wo_, const bf16* aq_, bf16* out_) {
  extern __shared__ __attribute__((aligned(16))) char lds[];
  const void* kap = (const void*)__builtin_amdgcn_kernarg_segment_ptr();
  u32x16 ka; u64 ka8;
  asm volatile("at_code_begin:\n\ts_load_dwordx16 %0, %2, 0\n\ts_load_dwordx2 %1, %2, 0x40" : "=&s"(ka), "=&s"(ka8) : "s"(kap) : ACLOB_ALL, "v255", "s101");
  GAP();
  const int tid = threadIdx.x, lane = tid & 63;
  const int wid = __builtin_amdgcn_readfirstlane(tid >> 6);
  const int b = blockIdx.x, j = b & 7, r = b >> 3, rr = r & 15, hsel = rr & 1, pj = rr >> 1, kv = pj & 1;
  const bool isY = r >= 16, solo = pj == 7, doP = isY != PREPX && !solo, doA = !isY && !solo;
  const int h = j + 8 * hsel, mt = (rr >> 2) + (isY ? 4 : 0), pu = (hsel * 8 + j) * 4 + wid;
  constexpr unsigned m0d = 131072 + 24576;
  const unsigned lA = isY ? 65536 : 0, lW = lA ^ 65536, po = isY ? 0 : 32768;
  const unsigned mp[4] = {lA + wid * 1024 + po, lW + wid * 1024 + po, lA + wid * 1024 + (po ^ 32768), lW + wid * 1024 + (po ^ 32768)};
  {
    const int lda = isY ? DA : DC;
    const unsigned ra = wid * 8 + (lane >> 3), sa = ((lane & 7) ^ (((wid & 1) * 4 + (lane >> 4)) & 7)) * 16;
    const unsigned vbA = ra * (lda * 2) + sa, vbB = (4 * wid + (lane >> 4)) * LDW * 2 + 16 * ((lane & 15) ^ (((lane >> 4) << 2) | wid));
    vsetu<92>(vbA); vsetu<93>(vbB); vsetu<90>(vbA + 128); vsetu<91>(vbB + 0x80000); vsetu<94>(ra * (DC * 2) + sa);
    static_for([&](auto J) {
      sset<64 + J>((J + 1) * 64 * lda);
      asm volatile("s_mov_b32 s%c0, %1" ::"i"(71 + J), "i"((((J + 1) >> 2) * 32 + 16 * ((J + 1) & 1)) * LDW * 2 + (((J + 1) >> 1) & 1) * 256));
    }, make_iseq<7>{});
    sset<78>(lA + wid * 1024); sset<79>(lW + wid * 1024);
    GAP();
  }
  asm volatile("s_waitcnt lgkmcnt(0)" : "+s"(ka), "+s"(ka8));
  auto arg = [&](int i) { return (const bf16*)(((u64)ka[2 * i + 1] << 32) | ka[2 * i]); };
  const bf16 *ckv = arg(0), *wuk = arg(1), *wuv = arg(2), *x = arg(3), *wq = arg(4), *ak = arg(6), *av = arg(7), *ash = (const bf16*)ka8;
  bf16* Qb = (bf16*)arg(5);
  KST(0, 0);
  bf16* xqT = (bf16*)((char*)Qb + O_X);
  const bf16* gA = ckv + (size_t)mt * 256 * DC;
  const bf16* gW = (kv ? wuv : wuk) + h * 256;
  unsigned dv = 0;
  {
    GM g;
    PR pr;
    if (doP) prep_load(pr, tid, pj, hsel * 8 + j, ak, av, ash);
    srd_set<56>(isY ? x + (size_t)pj * 256 * DA : gA, isY ? 256 * DA * 2 : 256 * DC * 2);
    srd_set<60>(isY ? wq + h * 256 : gW, isY ? (DA - 1) * LDW * 2 + 512 : (DC - 1) * LDW * 2 + 512);
    g_pro<0>(mp, make_iseq<12>{});
    srd_set<88>(Qb, WSB);
    {
      const unsigned* tp = g_tab.row[r * 5].v;
      asm volatile("buffer_load_dwordx2 v[120:121], off, s[88:91], %0 sc1\n\tglobal_load_dword v78, %1, %3\n\tglobal_load_dword v79, %2, %3 offset:1024\n\t"
                   "global_load_dword v118, %2, %4\n\tglobal_load_dword v119, %2, %4 offset:256\n\tglobal_load_dword v77, %5, %6"
                   ::"s"(fo(6144)), "v"(tid * 4), "v"(lane * 4), "s"(tp), "s"(g_tab.row[160].v), "v"((lane < 27 ? lane : 27) * 4), "s"(kap) : "memory");
    }
    if (doP) {
      asm volatile("s_waitcnt vmcnt(18)" ::: "memory");
#pragma unroll
      for (int q = 0; q < 7; ++q) asm volatile("" : "+v"(pr.kk[q]));
#pragma unroll
      for (int q = 0; q < 4; ++q) asm volatile("" : "+v"(pr.vv[q]));
      static_for([&](auto Ic) __attribute__((always_inline)) {
        constexpr int I = Ic;
        g_pro<12 + 4 * I>(mp, make_iseq<4>{});
        GAP();
        prep_math<I>(pr, tid, lds + 131072);
        GAP();
      }, make_iseq<4>{});
      asm volatile("s_waitcnt lgkmcnt(0)\n\ts_barrier" ::: "memory");
      prep_store(tid, pj, hsel * 8 + j, xqT, lds + 131072);
    } else g_pro<12>(mp, make_iseq<4>{});
    GAP();
    CODE_TOUCH("at_code_begin", "pf_code_end", r * 4 + wid, 128u, 8, m0d);
    {
      const int fr = lane & 15, fq = lane >> 4, q = (lane & 15) >> 2, p = lane & 3;
#pragma unroll
      for (int k2 = 0; k2 < 2; ++k2)
        g.aA[k2] = lA + (wid >> 1) * 16384 + (fr >> 3) * 1024 + (fr & 7) * 128 + (((4 * k2 + fq) ^ (fr >> 1)) * 16);
#pragma unroll
      for (int hf = 0; hf < 2; ++hf)
#pragma unroll
        for (int nf = 0; nf < 8; ++nf)
          g.aB[hf][nf] = lW + (wid & 1) * 8192 + off_b(8 * fq + 4 * hf + q, nf * 2 + (p >> 1)) + 8 * (p & 1);
      asm volatile("" : "+v"(g.aA[0]), "+v"(g.aA[1]));
#pragma unroll
      for (int nf = 0; nf < 8; ++nf) asm volatile("" : "+v"(g.aB[0][nf]), "+v"(g.aB[1][nf]));
      vsetu<81>(tid == 0 ? 0u : 0x80000000u);
      vsetu<84>(lane * 16); vsetu<85>(lane * 16 + 65536); vsetu<86>(wid * 8192 + lane * 16); vsetu<87>(wid * 8192 + lane * 16 + 4096);
      vsetu<88>(65536 + wid * 256 + fr * 16); vsetu<89>(fr - 4 * fq);
      asm volatile("s_mov_b64 s[92:93], s[88:89]\n\ts_mov_b64 s[94:95], s[90:91]");
    }
    if (doP) asm volatile("s_waitcnt vmcnt(13)" ::: "memory");
    else {
      g_pro<16>(mp, make_iseq<12>{});
      asm volatile("s_waitcnt vmcnt(8)" ::: "memory");
    }
    asm volatile("s_barrier" ::: "memory");
    if (isY) g_read0<0>(g, make_iseq<16>{});
    else g_read0<1>(g, make_iseq<16>{});
    GAP();
    {
      u64 z = (((u64)__float_as_uint(vget<121>()) << 32) | __float_as_uint(vget<120>())) + 0x9e3779b97f4a7c15ull;
      z = (z ^ (z >> 30)) * 0xbf58476d1ce4e5b9ull; z = (z ^ (z >> 27)) * 0x94d049bb133111ebull; z ^= z >> 31;
      vsetu<82>((unsigned)z); vsetu<83>((unsigned)(z >> 32));
      asm volatile("v_mad_u32_u24 v78, v118, %0, v78\n\tv_mad_u32_u24 v79, v119, %0, v79\n\tv_readfirstlane_b32 s84, v82\n\tv_readfirstlane_b32 s85, v83\n\t"
                   "v_readlane_b32 s80, v78, %1\n\tv_readlane_b32 s81, v78, %2" ::"s"(j), "i"((int)T_TAB), "i"(T_TAB + 1));
      if (isY) asm volatile("s_mov_b64 s[80:81], 0x6543210");
      asm volatile("s_bfe_u32 s82, s80, 0x40008\n\ts_lshr_b64 s[80:81], s[80:81], 12\n\tv_lshl_add_u32 v91, s82, 19, v93" ::: "scc");
    }
    asm volatile("s_waitcnt lgkmcnt(0)\n\ts_barrier" ::: "memory");
    g_pro<28>(mp, make_iseq<4>{});
    GAP();
    if (isY) {
      g_tile<false, 0, 1, NTW >= 1>(g);
      g_loop<false, true>(g, PREPX ? 0x20u : 0x22u, [&](int t) __attribute__((always_inline)) {
        if (t == 1) fl_set<T_FPREP>();
        else if (t == 4) { tload<80, T_TAB>(); tload<81, T_TAB + 1>(); srd_set<60>(gW, (DC - 1) * LDW * 2 + 512); }
        else {
          srd_set<56>(gA, 256 * DC * 2);
          asm volatile("v_mov_b32 v92, v94");
          static_for([&](auto J) { asm volatile("s_mov_b32 s%c0, %1" ::"i"(64 + J), "i"((J + 1) * 64 * DC)); }, make_iseq<7>{});
        }
      });
      tload<101, T_OQ>();
      asm volatile("v_mov_b32 v112, v84\n\tv_add_u32 v113, 8192, v84");
      GAP();
      g_tile<false, 0, 2, NTW >= 2>(g);
      KST(0, 3);
    }
#define TT 3
    auto kvtile = [&](auto TRc) __attribute__((always_inline)) {
      constexpr bool TR = TRc;
      if (isY) g_tile<TR, 1, 4, NTW >= 2>(g);
      else g_tile<TR, 1, 1, NTW >= 2>(g);
      g_loop<TR, false>(g, 0x2000u | (isY || doA ? 2u : 1u << TT), [&](int t) __attribute__((always_inline)) {
        int l = threadIdx.x & 63; asm volatile("" : "+v"(l));
        if (t == 13) {
          asm volatile("s_mov_b32 s79, 0x28000");
          fl_poll<T_NPRE>();
          if (!doA && !isY) {
            CODE_TOUCH("at_code_begin", "pf_code_end", hsel * 8 + wid * 2, 16u, 64, m0d);
            CODE_TOUCH("at_code_begin", "pf_code_end", hsel * 8 + wid * 2 + 1, 16u, 64, m0d);
          }
          if (doA) {
            const char* tp = arg_late<const char, 9>() + (size_t)(pj * 64 + pu) * (SEQ * 2) + (l & 31) * 128;
            asm volatile("s_mov_b32 m0, %1\n\ts_nop 0\n\tglobal_load_lds_dword %0, off" ::"v"(tp), "i"(m0d) : "memory");
          }
        } else if (isY) fl_set<T_FQ>();
        else if (doA) { if (PREPX) fl_set<T_FPREP>(); post_dma(pj, pu, arg_late<const bf16, 12>(), 131072 + wid * 8192); }
        else {
          const char* tp = arg_late<const char, 0>() + 1024 * DC * 2 + (pu * 256 + l) * 128;
          asm volatile("s_mov_b32 m0, %1\n\ts_nop 0\n\tglobal_load_lds_dword %0, off" ::"v"(tp), "i"(m0d) : "memory");
          asm volatile("global_load_lds_dword %0, off" ::"v"(tp + 8192) : "memory");
          asm volatile("global_load_lds_dword %0, off" ::"v"(tp + 16384) : "memory");
          asm volatile("global_load_lds_dword %0, off" ::"v"(tp + 24576) : "memory");
        }
      });
      tload<101, T_OG>(); tload<96, T_Q0>(); tload<98, T_K0>();
      fl_check();
      tload<50, T_NST>(); tload<51, T_NST + 1>();
      asm volatile("v_mov_b32 v112, v84\n\tv_add_u32 v113, %0, v84\n\tv_mov_b32 v90, v87\n\tv_mov_b32 v91, v87\n\tv_mov_b32 v92, v87\n\tv_mov_b32 v93, v87\n\t"
                   "v_mov_b32 v94, v86\n\tv_mov_b32 v95, v86\n\tv_mov_b32 v126, v86\n\tv_mov_b32 v127, v86\n\ts_mov_b32 s99, 0x8000\n\t" FL_ARM("s[50:51]")
                   ::"i"(TR ? 4096 : 8192));
      if (doA) {
        const int d = pj * 64 + pu;
        const bf16* dp = arg_late<const bf16, 10>() + d;
        asm volatile("global_load_ushort %0, %1, off" : "=v"(dv) : "v"(dp) : "memory");
        srd_set<56>(arg_late<const bf16, 9>() + (size_t)d * SEQ, SEQ * 2);
        srd_set<60>(arg_late<const char, 5>() + O_X + d * SEQ * 2, SEQ * 2);
        sset<78>(65536 + wid * 15488);
        asm volatile("s_movk_i32 s64, 992\n\tv_subrev_u32 v122, 32, v84");
        GAP();
        g_tile<TR, 0, 3>(g);
      } else {
        sset<79>(isY ? wid * 8192 : 0x30000);
        GAP();
        g_tile<TR, 0, 5>(g);
      }
    };
    if (kv) kvtile(std::true_type{});
    else kvtile(std::false_type{});
  }
  KST(0, 4);
  asm volatile("s_waitcnt vmcnt(16)" : "+v"(dv) :: "memory");
  sset<79>(wid * 8192);
  if (doA) {
    const float Dd = bf2f(dv);
    KST(0, 5);
    conv_wave<T_FG>(O_Z + (pj * 64 + pu) * SEQ * 2, Dd, lds + 65536 + wid * 15488);
    const char* tp = arg_late<const char, 11>() + (size_t)h * (HD * DA * 2) + ((pj * 4 + wid) * 64 + lane) * 128;
    asm volatile("s_mov_b32 m0, %1\n\ts_nop 0\n\tglobal_load_lds_dword %0, off\n\ts_waitcnt vmcnt(17)" ::"v"(tp), "i"(m0d) : "memory");
    KST(0, 6);
  } else if (!isY) {
    static_for([&](auto I) { d_store<16 + I>(); }, make_iseq<16>{});
    tload<98, T_K0>();
    asm volatile("s_waitcnt vmcnt(0)\n\ts_barrier" ::: "memory");
    fl_set<T_FG>();
    KST(0, 5);
    fl_check();
    CODE_TOUCH("att_begin", "pf_code_end", hsel * 4 + wid, 8u, 64, m0d);
    dma4k<0>(); dma4k<1>(); dma4k<2>(); dma4k<3>();
    asm volatile("s_waitcnt vmcnt(0)" ::: "memory");
    KST(0, 6);
  } else {
    tload<76, T_PART>();
    fl_check();
    asm volatile("buffer_load_dwordx2 v[122:123], v88, s[88:91], s76 offen offset:1024 sc1" ::: "memory");
  }
  AC c;
  sset<78>(65536 + wid * 8192); sset<79>(98304 + wid * 8192);
  { const bf16* wp = arg_late<const bf16, 11>() + (size_t)h * HD * DA; asm volatile("s_mov_b64 s[80:81], %0" ::"s"(wp)); }
  asm volatile("att_begin:");
  ev_code();
  asm volatile("s_waitcnt lgkmcnt(0)\n\ts_barrier" ::: "memory");
#pragma unroll 1
  for (int it = doA ? 0 : 1; it < 2; ++it) {
    kreads<0>(make_iseq<8>{});
    const unsigned oP = tget<T_PART>();
    if (it == 0) {
      static_for([&](auto I) { tload<57 + I, T_IT + I>(); }, make_iseq<21>{});
      tload<S_T, T_IT + 21>(); tload<S_EV, T_IT + 22>();
      asm volatile("s_sub_u32 s70, s70, %0\n\ts_add_u32 s76, %1, 1024" ::"s"(j * (8 * 4352 - 8)), "s"(oP) : "scc");
    } else {
      static_for([&](auto I) { tload<57 + I, T_IT + T_ITN + I>(); }, make_iseq<21>{});
      tload<S_T, T_IT + T_ITN + 21>(); tload<S_EV, T_IT + T_ITN + 22>();
      asm volatile("s_mov_b32 s72, 0\n\ts_mov_b32 s75, %0\n\ts_mov_b32 s76, %1" ::"s"(oP + wid * 16384), "s"(oP + 1032));
    }
    unsigned mode;
    asm volatile("s_add_u32 s98, s97, 0x8000\n\ts_mov_b32 %0, s73\n\ts_mov_b32 s56, -1\n\tv_mov_b64 v[114:115], 0\n\tv_mov_b64 v[118:119], 0\n\t"
                 "v_mov_b32 v120, 0x45800000" : "=s"(mode) :: "scc");
    KST(0, 8 + 3 * (it && !isY));
    GAP();
#define STA(k)
#define STI()
    STA(0);
    if (isY) phaseA<0, 2>(c);
    else phaseA<0, 1>(c);
    STA(1);
    if (c.rare) ev_call();
    STA(2);
    phaseB0(mode, wid);
    if (__builtin_expect(mode & 4, 0)) { asm volatile("s_mov_b32 s56, 31"); ev_call(); asm volatile("s_mov_b32 s56, 0"); }
    STA(3);
    asm volatile("s_lshr_b32 s76, s73, 8\natt_loop:" ::: "scc");
    GAP();
    phaseA<1, 0>(c);
    STI();
    if (__builtin_expect(c.rare != 0, 0)) { if (redo()) sm_redo<1>(); ev_call(); }
    phaseB<1>(c);
    asm volatile("s_cbranch_scc1 att_done");
    phaseA<0, 0>(c);
    STI();
    if (__builtin_expect(c.rare != 0, 0)) { if (redo()) sm_redo<0>(); ev_call(); }
    phaseB<0>(c);
    asm volatile("s_branch att_loop\natt_done:");
    GAP();
    STA(4);
    if ((mode & 3) == 1) phaseA<0, 4>(c);
    else {
      asm volatile("buffer_load_dwordx4 v[128:131], v88, s[88:91], %0 offen sc1" ::"s"(oP) : "memory");
      GAP();
      phaseA<0, 3>(c);
    }
    if (__builtin_expect(redo(), 0)) sm_redo<0>();
    STA(5);
    KST(0, 9 + 3 * (it && !isY));
    const float l[2] = {row_reduce<false>(vget<R_RS>() + vget<R_TS>()), row_reduce<false>(vget<R_RS + 1>() + vget<R_TS + 1>())};
    const float cm[2] = {vget<R_M>(), vget<R_M + 1>()};
    EP p;
    if (it == 0) {
      const f32x4 ml = {cm[0], l[0], cm[1], l[1]};
      asm volatile("buffer_store_dwordx4 %0, v88, s[88:91], %1 offen sc1\n\ts_mov_b32 s101, %2" ::"v"(ml), "s"(oP), "s"(oP + wid * 16384) : "memory");
      phaseF<0>(c, p, make_iseq<74>{});
      asm volatile(FL_POLL "\n\ts_waitcnt vmcnt(17)" ::: "memory");
    } else {
      int lz = threadIdx.x & 63; asm volatile("" : "+v"(lz));
      const int rw = lz >> 2;
      static_for([&](auto E) { vsetu<R_TB + E>(rw * 128 + 16 * ((2 * E + ((lz & 3) >> 1)) ^ (rw & 6)) + 8 * (lz & 1)); }, make_iseq<4>{});
      const unsigned vs = ((lz >> 3) & 1) * 16384 + ((lz >> 4) & 1) * 512 + (lz >> 5) * 256 + (lz & 7) * 16;
      vsetu<R_VS>(vs); vsetu<R_VS + 1>(vs + 32768);
      tload<96, T_SLAB>();
      asm volatile("s_mov_b32 s97, s79\n\ts_mov_b32 s100, 0x8000\n\ts_movk_i32 s99, 0x1000");
      if (!(mode & 2)) {
        p.fa[0] = __builtin_amdgcn_rcpf(l[0]); p.fa[1] = __builtin_amdgcn_rcpf(l[1]);
        phaseF<1>(c, p, make_iseq<74>{});
      } else {
        const f32x4 ml = {vget<128>(), vget<129>(), vget<130>(), vget<131>()};
        if (__builtin_expect(__builtin_amdgcn_ballot_w64(ml[0] != cm[0] || ml[2] != cm[1]) == 0, 1)) {
          p.fa[0] = __builtin_amdgcn_rcpf(l[0] + ml[1]); p.fa[1] = __builtin_amdgcn_rcpf(l[1] + ml[3]);
          const unsigned one = (lz >> 4) != ((lz >> 2) & 3) ? 0u : (lz & 1) ? 0x3f800000u : 0x3f80u, o0 = (lz & 2) ? 0u : one, o1 = (lz & 2) ? one : 0u;
          vsetu<224>(o0); vsetu<225>(o1); vsetu<230>(o0); vsetu<231>(o1);
          asm volatile("v_mov_b64 v[226:227], 0\n\tv_mov_b64 v[228:229], 0");
          phaseF<3>(c, p, make_iseq<74>{});
        } else {
#pragma unroll
          for (int e = 0; e < 2; ++e) {
            const float mm = fmaxf(cm[e], ml[2 * e]);
            const float wa = __builtin_amdgcn_exp2f(cm[e] - mm), wb = __builtin_amdgcn_exp2f(ml[2 * e] - mm);
            const float inv = __builtin_amdgcn_rcpf(l[e] * wa + ml[2 * e + 1] * wb);
            p.fa[e] = wa * inv; p.fb[e] = wb * inv;
          }
          phaseF<2>(c, p, make_iseq<74>{});
        }
      }
    }
    KST(0, 10 + 3 * (it && !isY));
  }
  asm volatile("v_xor_b32 v%c0, s100, v%c0\np_loop:" ::"i"(R_TB));
  GAP();
  p_half<0>(make_iseq<32>{});
  p_half<1>(make_iseq<32>{});
  asm volatile("s_cbranch_scc1 p_loop\n\ts_nop 5");
  GAP();
  p_cvt<1, 0>(); p_cvt<1, 1>(); p_cvt<1, 2>(); p_cvt<1, 3>();
  p_store<1, 1>();
  GAP();
  KST(0, 27);
  {
    constexpr unsigned rdy = 98304;
    int lz = threadIdx.x & 63; asm volatile("" : "+v"(lz));
    const int fr = lz & 15, fq = lz >> 4, qbF = isY ? 15 - pj : pj, u = hsel * 8 + j;
    asm volatile("ds_write_b32 %0, %1" ::"v"(rdy), "v"(0) : "memory");
    vsetu<81>(lz == 0 ? 0u : 0x80000000u);
    vsetu<79>(fo(1024) + (qbF * 4 + (u >> 2)) * 4352 + fr * 8);
    {
      const int jj = 4 * (fr >> 3) + (fr & 3);
      const unsigned one = ((fq ^ (fr >> 2)) & 1) ? 0u : (jj & 1) ? 0x3f800000u : 0x3f80u;
      static_for([&](auto I) { vsetu<96 + I>((jj >> 1) == I ? one : 0u); }, make_iseq<4>{});
    }
    unsigned vo = (fq >> 1) * 1024 + (fq & 1) * 256 + fr * 16, vo1 = wid < 3 ? vo + 0x100000 : 0x80000000u;
    bf16* out = arg_late<bf16, 13>();
    __amdgpu_buffer_rsrc_t so_ = __builtin_amdgcn_make_buffer_rsrc((void*)out, (short)0, SEQ * OUTW * 2, 0x00020000);
    unsigned vw = ((qbF * 128 + u * 8 + (fr & 7)) * OUTW + DA + wid * 64 + (fr >> 3) * 32 + fq * 8) * 2;
    unsigned so = O_S + qbF * 0x200000 + (1 + wid) * 0x40000 + u * 16384;
    asm volatile("" : "+s"(so), "+s"(so_));
    asm volatile("s_waitcnt vmcnt(0) lgkmcnt(0)" : "+v"(vo), "+v"(vo1), "+v"(vw) :: "memory");
    fl_set<T_FS>();
    asm volatile("s_barrier" ::: "memory");
    KST(0, 30);
    if (doA) post_math(pj, pu, out, lds + 131072 + wid * 8192);
    unsigned pend = 0x5555u, cnt = 0x400000;
    KST(0, 28);
    while (true) {
      unsigned nw;
      if (wid == 3) {
        u64 m;
        const u64 ex = pend | (pend << 1);
        asm volatile(FL_ARM("%1") "\n\t" FL_POLL "\n\ts_waitcnt vmcnt(0)\n\tv_cmp_eq_u64 %0, s[84:85], v[124:125]" : "=s"(m) : "s"(ex) : "memory");
        nw = (unsigned)m & ((unsigned)m >> 1) & pend;
        if (nw) asm volatile("ds_write_b32 %0, %1" ::"v"(rdy), "v"(~pend | nw) : "memory");
      } else {
        unsigned t;
        asm volatile("ds_read_b32 %0, %1\n\ts_waitcnt lgkmcnt(0)" : "=v"(t) : "v"(rdy) : "memory");
        nw = __builtin_amdgcn_readfirstlane(t) & pend;
      }
      if (--cnt == 0) nw = pend;
      if (nw == 0) {
        if (wid == 3) asm volatile("s_sleep 4");
        else asm volatile("s_sleep 1");
        continue;
      }
      asm volatile("s_nop 4");
      static_for([&](auto S) {
        if ((nw >> (2 * S)) & 1) { r_load<0, 2 * S>(vo, so); r_load<0, 2 * S + 1>(vo, so); r_load<1, 2 * S>(vo1, so); r_load<1, 2 * S + 1>(vo1, so); }
      }, make_iseq<8>{});
      pend &= ~nw;
      if (pend == 0) break;
    }
    asm volatile("s_waitcnt vmcnt(0)" ::: "memory");
    KST(0, 16);
    r_fin<0>(vw, so_, 0);
    if (wid < 3) r_fin<1>(vw, so_, 512);
    KST(0, 29);
    if (b == 128 && wid == 0) asm volatile(FL_SET("%0") ::"s"(fo(6144)) : "memory");
  }
  asm volatile("s_waitcnt vmcnt(0)" ::: "memory");
  asm volatile("pf_code_end:");
  KST(0, 1);
}

extern "C" void kernel_launch(void* const* d_in, const int* in_sizes, int n_in, void* d_out, int out_size, void* d_ws,
                              size_t ws_size, hipStream_t stream) {
  const bf16* Aq = (const bf16*)d_in[0];
  const bf16* Ak = (const bf16*)d_in[1];
  const bf16* Av = (const bf16*)d_in[2];
  const bf16* Ash = (const bf16*)d_in[3];
  const bf16* Assmk = (const bf16*)d_in[4];
  const bf16* AssmD = (const bf16*)d_in[5];
  const bf16* Bx = (const bf16*)d_in[6];
  const bf16* Bckv = (const bf16*)d_in[7];
  const bf16* Bwq = (const bf16*)d_in[8];
  const bf16* Bwuk = (const bf16*)d_in[9];
  const bf16* Bwuv = (const bf16*)d_in[10];
  const bf16* Bwo = (const bf16*)d_in[11];
  bf16* out = (bf16*)d_out;
  hipFuncSetAttribute((const void*)fused_kernel, hipFuncAttributeMaxDynamicSharedMemorySize, 163840);
  fused_kernel<<<256, 256, 163840, stream>>>(Bckv, Bwuk, Bwuv, Bx, Bwq, (bf16*)d_ws, Ak, Av, Ash, Assmk, AssmD, Bwo, Aq, out);
}
```

```cpp
#include <hip/hip_runtime.h>
#include <hip/hip_bf16.h>
#include <stdint.h>
#include <utility>

typedef __hip_bfloat16 bf16;
typedef __attribute__((ext_vector_type(4))) short bf16x4;
typedef __attribute__((ext_vector_type(2))) unsigned u32x2;

constexpr int SEQ = 2048;
constexpr int DA = 448;
constexpr int NH = 16;
constexpr int HD = 256;
constexpr int DC = 1024;
constexpr int OUTW = 896;
#define OUTSC 17
#define OUTSCS " sc0 sc1"
#define NTPS " nt"
#define NTVS " nt"
#define NTQS ""
#define NTW 1
#define NYT 15
#define SWAP7 1
constexpr unsigned TSB = NH * SEQ * HD * 2, O_K = TSB, O_S = 3 * TSB, O_P = 5 * TSB, PSTR = 67584, O_F = O_P + 128 * PSTR,
                   O_X = O_F + 400 * 4352, O_Z = O_X + DA * SEQ * 2, WSB = O_Z + DA * SEQ * 2;
constexpr unsigned fo(int w) { return O_F + (w >> 4) * 4352 + (w & 15) * 8; }

#define KST(kid, which)

__device__ __forceinline__ float bf2f(unsigned short u) { return __uint_as_float(((unsigned)u) << 16); }
__device__ __forceinline__ unsigned short f2bf(float f) { return __bfloat16_as_ushort(__float2bfloat16(f)); }
__device__ __forceinline__ float rbf(float f) { return bf2f(f2bf(f)); }
typedef __attribute__((ext_vector_type(2))) __bf16 bf16x2n;
typedef __attribute__((ext_vector_type(2))) float f32x2n;
__device__ __forceinline__ unsigned pk2(float a, float b) {
  return __builtin_bit_cast(unsigned, __builtin_convertvector((f32x2n){a, b}, bf16x2n));
}

__device__ __forceinline__ int off_b(int row, int ch) { return 256 * row + 16 * (ch ^ (((row & 3) << 2) | ((row >> 2) & 3))); }

template <int R> __device__ __forceinline__ void sset(unsigned x) { asm volatile("s_mov_b32 s%c0, %1" ::"i"(R), "s"(x)); }
template <int R> __device__ __forceinline__ void vsetu(unsigned x) { asm volatile("v_mov_b32 v%c0, %1" ::"i"(R), "v"(x)); }
template <int R> __device__ __forceinline__ void srd_set(const void* p, unsigned n) {
  asm volatile("s_mov_b64 s[%c0:%c1], %4\n\ts_mov_b32 s%c2, %5\n\ts_mov_b32 s%c3, 0x20000" ::"i"(R), "i"(R + 1), "i"(R + 2), "i"(R + 3), "s"(p), "s"(n));
}
template <int R, int L> __device__ __forceinline__ void tload() { asm volatile("v_readlane_b32 s%c0, v78, %1" ::"i"(R), "i"(L)); }
template <int L> __device__ __forceinline__ unsigned tget() { unsigned t; asm volatile("v_readlane_b32 %0, v78, %1" : "=s"(t) : "i"(L)); return t; }
#define FL_SET(S) "buffer_store_dwordx2 v[82:83], v81, s[88:91], " S " offen sc1"
#define FL_ARM(N) "v_cndmask_b32_e64 v80, -2.0, v79, " N
#define FL_POLL "buffer_load_dwordx2 v[124:125], v80, s[88:91], 0 offen sc1"
#define FL_CHECK(N) "s_mov_b32 s48, 0x100000\n7:\n\tv_cmp_eq_u64 vcc, s[84:85], v[124:125]\n\ts_and_b64 vcc, vcc, " N "\n\ts_cmp_eq_u64 vcc, " N \
                    "\n\ts_cbranch_scc1 8f\n\ts_sleep 2\n\t" FL_POLL "\n\ts_waitcnt vmcnt(0)\n\ts_sub_u32 s48, s48, 1\n\ts_cbranch_scc0 7b\n8:"
template <int L> __device__ __forceinline__ void fl_set() {
  asm volatile("v_readlane_b32 s48, v78, %0\n\ts_nop 4\n\t" FL_SET("s48") ::"i"(L) : "memory");
}
template <int L> __device__ __forceinline__ void fl_poll() {
  asm volatile("v_readlane_b32 s50, v78, %0\n\tv_readlane_b32 s51, v78, %1\n\ts_nop 1\n\t" FL_ARM("s[50:51]") "\n\t" FL_POLL ::"i"(L), "i"(L + 1) : "memory");
}
#define TICKET "v_readlane_b32 s48, v78, %0\n\tv_mov_b32 v122, 1\n\tv_mov_b32 v123, 0\n\ts_nop 2\n\tbuffer_atomic_add_x2 v[122:123], v81, s[88:91], s48 offen sc0"
__device__ __forceinline__ void fl_check() { asm volatile(FL_CHECK("s[50:51]") ::: "vcc", "scc", "memory"); }
template <int R> __device__ __forceinline__ float vget() { float t; asm volatile("v_mov_b32 %0, v%c1" : "=v"(t) : "i"(R)); return t; }
template <int R> __device__ __forceinline__ void vset(float x) { asm volatile("v_mov_b32 v%c0, %1" ::"i"(R), "v"(x)); }
template <int R> __device__ __forceinline__ float acc_read1() { float t; asm volatile("v_accvgpr_read_b32 %0, a[%c1]" : "=v"(t) : "i"(R)); return t; }
template <class F, int... I> __device__ __forceinline__ void static_for(F&& f, std::integer_sequence<int, I...>) {
  (f(std::integral_constant<int, I>{}), ...);
}
template <int... Is> using iseq = std::integer_sequence<int, Is...>;
template <int N> using make_iseq = std::make_integer_sequence<int, N>;

typedef __attribute__((ext_vector_type(4))) unsigned u32x4c;
typedef __attribute__((ext_vector_type(4))) float f32x4c;
typedef __attribute__((ext_vector_type(2))) float f32x2c;
typedef unsigned long long u64;
struct PR { u32x4c kk[7], vv[4]; };
__device__ __forceinline__ void prep_load(PR& p, int tid, int cg, int tb, const bf16* __restrict__ k, const bf16* __restrict__ v,
                                          const bf16* __restrict__ shift_k) {
  const __amdgpu_buffer_rsrc_t sk = __builtin_amdgcn_make_buffer_rsrc((void*)k, (short)0, SEQ * DA * 2, 0x00020000);
  const __amdgpu_buffer_rsrc_t sv = __builtin_amdgcn_make_buffer_rsrc((void*)v, (short)0, SEQ * DA * 2, 0x00020000);
  const __amdgpu_buffer_rsrc_t sw = __builtin_amdgcn_make_buffer_rsrc((void*)(shift_k + cg * 256), (short)0, 512, 0x00020000);
  const int vo = ((tb * 128 + (tid >> 3) * 4) * DA + cg * 64 + (tid & 7) * 8) * 2;
#pragma unroll
  for (int q = 0; q < 7; ++q)
    asm volatile("buffer_load_dwordx4 %0, %1, %2, 0 offen" NTPS : "=v"(p.kk[q]) : "v"(vo + (q - 3) * DA * 2), "s"(sk) : "memory");
#pragma unroll
  for (int q = 0; q < 4; ++q)
    asm volatile("buffer_load_dwordx4 %0, %1, %2, 0 offen offset:%3" NTVS : "=v"(p.vv[q]) : "v"(vo), "s"(sv), "i"(q * DA * 2) : "memory");
#pragma unroll
  for (int q = 0; q < 4; ++q)
    asm volatile("buffer_load_dwordx4 a[%c0:%c1], %2, %3, 0 offen offset:%4" ::"i"(4 * q), "i"(4 * q + 3), "v"((tid & 7) * 64), "s"(sw), "i"(q * 16) : "memory");
}
__device__ __forceinline__ float bfl(unsigned u) { return __uint_as_float(u << 16); }
__device__ __forceinline__ float bfh(unsigned u) { return __uint_as_float(u & 0xffff0000u); }
__device__ __forceinline__ f32x2c rbf2(f32x2c a) { const unsigned u = pk2(a[0], a[1]); return (f32x2c){bfl(u), bfh(u)}; }
template <int I> __device__ __forceinline__ void prep_math(const PR& p, int tid, char* lx) {
  const int rp = tid >> 3, c = tid & 7;
  f32x2c wt[4], kf[7];
  const unsigned w[4] = {__float_as_uint(acc_read1<4 * I>()), __float_as_uint(acc_read1<4 * I + 1>()), __float_as_uint(acc_read1<4 * I + 2>()),
                         __float_as_uint(acc_read1<4 * I + 3>())};
#pragma unroll
  for (int t = 0; t < 4; ++t)
    wt[t] = (t & 1) ? (f32x2c){bfh(w[t >> 1]), bfh(w[2 + (t >> 1)])} : (f32x2c){bfl(w[t >> 1]), bfl(w[2 + (t >> 1)])};
#pragma unroll
  for (int q = 0; q < 7; ++q) kf[q] = (f32x2c){bfl(p.kk[q][I]), bfh(p.kk[q][I])};
  f32x2c x[4];
#pragma unroll
  for (int e = 0; e < 4; ++e) {
    f32x2c s = rbf2(kf[e] * wt[3]);
    s = rbf2(s + rbf2(kf[e + 1] * wt[2]));
    s = rbf2(s + rbf2(kf[e + 2] * wt[1]));
    s = rbf2(s + rbf2(kf[e + 3] * wt[0]));
    x[e] = s * (f32x2c){bfl(p.vv[e][I]), bfh(p.vv[e][I])};
  }
#pragma unroll
  for (int u = 0; u < 2; ++u)
    *(u32x2*)(lx + (c * 8 + 2 * I + u) * 256 + (((31 - rp) * 8) ^ (c << 4))) = (u32x2){pk2(x[3][u], x[2][u]), pk2(x[1][u], x[0][u])};
}
__device__ __forceinline__ void prep_store(int tid, int cg, int tb, bf16* __restrict__ xr, const char* lx) {
  const __amdgpu_buffer_rsrc_t sx = __builtin_amdgcn_make_buffer_rsrc((void*)(xr + (size_t)cg * 64 * SEQ), (short)0, 64 * SEQ * 2, 0x00020000);
#pragma unroll
  for (int rd = 0; rd < 4; ++rd) {
    const int idx = tid + 256 * rd, ch = idx >> 4, q = idx & 15;
    u32x4c w = *(const u32x4c*)(lx + ch * 256 + 16 * (q ^ (ch >> 3)));
    __builtin_amdgcn_raw_buffer_store_b128(w, sx, (ch * SEQ + (15 - tb) * 128 + q * 8) * 2, 0, 16);
  }
}

#define CODE_TOUCH(LB, LE, wx, NW, NL, m0v) do { unsigned blo_, bhi_, elo_; \
    asm volatile("s_getpc_b64 s[48:49]\n\ts_add_u32 s48, s48, " LB "@rel32@lo+4\n\ts_addc_u32 s49, s49, " LB "@rel32@hi+12\n\t" \
                 "s_mov_b32 %0, s48\n\ts_mov_b32 %1, s49\n\t" \
                 "s_getpc_b64 s[48:49]\n\ts_add_u32 s48, s48, " LE "@rel32@lo+4\n\ts_mov_b32 %2, s48" \
                 : "=s"(blo_), "=s"(bhi_), "=s"(elo_) :: "scc"); \
    const unsigned n_ = elo_ - (blo_ & ~127u); \
    const __amdgpu_buffer_rsrc_t srd_ = __builtin_amdgcn_make_buffer_rsrc((void*)(((unsigned long long)bhi_ << 32) | (blo_ & ~127u)), (short)0, n_ < 262144u ? n_ : 0u, 0x00020000); \
    const unsigned l_ = threadIdx.x & 63, vo_ = l_ < (NL) ? ((wx) + l_ * (NW)) * 128u : 0x80000000u; \
    asm volatile("s_mov_b32 m0, %0\n\ts_nop 4\n\tbuffer_load_dword %1, %2, 0 offen lds" ::"s"(m0v), "v"(vo_), "s"(srd_) : "memory"); } while (0)
template <int I> __device__ __forceinline__ void d_store() {
  asm volatile("buffer_store_dwordx4 v[%c0:%c1], v%c2, s[88:91], s101 offen offset:%3 sc1" ::"i"(128 + 4 * I), "i"(131 + 4 * I), "i"(112 + ((I >> 2) & 1)),
               "i"((I & 3) * 1024) : "memory");
  if constexpr ((I & 7) == 7) asm volatile("s_addk_i32 s101, 0x4000" ::: "scc");
}
constexpr int NS_X = 19;
struct CV { unsigned pa[4], pb, px, vz, sz; float D; };
constexpr int cv_J(bool up, int q) { int J = up ? 0 : 7; while (q > J) { q -= J + 1; J += up ? 1 : -1; } return J; }
constexpr int cv_i(bool up, int q) { int J = up ? 0 : 7; while (q > J) { q -= J + 1; J += up ? 1 : -1; } return q; }
template <int R, int N> __device__ __forceinline__ void cv_rd(CV& c) {
  if constexpr (N < 16) {
    constexpr int i = N >> 1, o = 16 * R + 128 * (i & 1) + 2 * (N & 1), r = 32 + 32 * (R & 1) + 2 * N;
    asm volatile("ds_read2_b32 a[%c1:%c2], %0 offset0:%c3 offset1:%c4" ::"v"(c.pa[i >> 1]), "i"(r), "i"(r + 1), "i"(o), "i"(o + 1));
  } else {
    constexpr int m = N - 16, r = 192 + 32 * (R & 1) + 4 * m;
    asm volatile("ds_read_b128 a[%c1:%c2], %0 offset:%c3" ::"v"(c.pb), "i"(r), "i"(r + 3), "i"(64 * R + 512 * (7 - m)));
  }
}
template <int O> __device__ __forceinline__ void cv_xd(CV& c) {
  constexpr int p = O / 7, k = O % 7, t = 160 + 2 * p, x = 96 + (p ^ 1);
  if constexpr (k == 0) asm volatile("v_and_b32 v%c0, 0xffff0000, v%c1" ::"i"(t), "i"(x));
  if constexpr (k == 1) asm volatile("v_lshlrev_b32 v%c0, 16, v%c1" ::"i"(t + 1), "i"(x));
  if constexpr (k == 2 || k == 3) asm volatile("v_mul_f32 v%c0, v%c0, %1" ::"i"(t + k - 2), "v"(c.D));
  if constexpr (k == 4) asm volatile("v_cvt_pk_bf16_f32 v%c0, v%c1, v%c0" ::"i"(t + 1), "i"(t));
  if constexpr (k == 5) asm volatile("v_lshlrev_b32 v%c0, 16, v%c1" ::"i"(t), "i"(t + 1));
  if constexpr (k == 6) asm volatile("v_and_b32 v%c0, 0xffff0000, v%c0" ::"i"(t + 1));
}
template <int O> __device__ __forceinline__ void cv_z(CV& c) {
  constexpr int J = O / 13, k = O % 13, hf = k & 1, y = 128 + 4 * J + 2 * hf;
  if constexpr (k < 2) asm volatile("v_cvt_pk_bf16_f32 v%c0, v%c1, v%c0" ::"i"(y + 1), "i"(y));
  else if constexpr (k < 4) asm volatile("v_lshlrev_b32 v%c0, 16, v%c1" ::"i"(y), "i"(y + 1));
  else if constexpr (k < 6) asm volatile("v_and_b32 v%c0, 0xffff0000, v%c0" ::"i"(y + 1));
  else if constexpr (k < 10) asm volatile("v_add_f32 v%c0, v%c0, v%c1" ::"i"(y + (k >= 8)), "i"(y + 32 + (k >= 8)));
  else if constexpr (k < 12) asm volatile("v_cvt_pk_bf16_f32 v%c0, v%c1, v%c2" ::"i"(128 + 4 * J + hf), "i"(y), "i"(y + 1));
  else asm volatile("buffer_store_dwordx2 v[%c0:%c1], %2, s[88:91], %3 offen offset:%4 sc1" ::"i"(128 + 4 * J), "i"(129 + 4 * J), "v"(c.vz), "s"(c.sz),
                    "i"(512 * J) : "memory");
}
constexpr int cv_z0(int Q) {
  int o = 0;
  for (int q = 0; q < Q; ++q)
    for (int k = 0; k < 3; ++k) { const int J = o / 13; if ((J + 1) * (J + 2) / 2 - 1 + 4 <= q) ++o; }
  return o;
}
constexpr int cv_xd0(int T) { int o = 0; for (int t = 0; t < T; ++t) o += t % 36 < 24 ? 1 : 2; return o < 112 ? o : 112; }
template <int B, int... Is> __device__ __forceinline__ void cv_zs(CV& c, iseq<Is...>) { (cv_z<B + Is>(c), ...); }
template <int B, int... Is> __device__ __forceinline__ void cv_xds(CV& c, iseq<Is...>) { (cv_xd<B + Is>(c), ...); }
template <int R, int Q> __device__ __forceinline__ void cv_slot(CV& c) {
  constexpr int i = cv_i(R == 7, Q), J = cv_J(R == 7, Q), d = 128 + 4 * J, a = 32 + 32 * (R & 1) + 4 * i, b = 192 + 32 * (R & 1) + 4 * (J - i);
  constexpr int T = 36 * R + Q, vm = Q % 4 == 1 && Q < 32 ? Q / 4 : -1;
  if constexpr (Q == 0) asm volatile("s_waitcnt lgkmcnt(0)");
  if constexpr (R >= 6 && vm >= 0 && vm % 4 == 0) asm volatile("s_add_u32 m0, s79, %0" ::"i"(((R - 6) ^ (NYT & 1)) * 32768 + vm * 1024));
  if constexpr (R == 0 && i == 0)
    asm volatile("v_mfma_f32_16x16x32_bf16 v[%c0:%c1], a[%c2:%c3], a[%c4:%c5], 0" ::"i"(d), "i"(d + 3), "i"(a), "i"(a + 3), "i"(b), "i"(b + 3));
  else
    asm volatile("v_mfma_f32_16x16x32_bf16 v[%c0:%c1], a[%c2:%c3], a[%c4:%c5], v[%c0:%c1]" ::"i"(d), "i"(d + 3), "i"(a), "i"(a + 3), "i"(b), "i"(b + 3));
  if constexpr (Q < 24 && R < 7) cv_rd<R + 1, Q>(c);
  if constexpr (T % 7 == 3 && NS_X + 5 + T / 7 < 32) d_store<NS_X + 5 + T / 7>();
  if constexpr (R == 2 && Q >= 24 && Q < 32)
    asm volatile("ds_read_b64 v[%c1:%c2], %0 offset:%c3" ::"v"(c.px), "i"(96 + 2 * (Q - 24)), "i"(97 + 2 * (Q - 24)), "i"(512 * (31 - Q)));
  if constexpr (R >= 3 && R < 6) cv_xds<cv_xd0(T - 108)>(c, make_iseq<cv_xd0(T - 107) - cv_xd0(T - 108)>{});
  if constexpr ((R == 3 || R == 4) && vm >= 0) {
    constexpr int f = (R - 3) * 8 + vm;
    asm volatile("buffer_load_dwordx4 a[%c0:%c1], v84, s[88:91], s96 offen offset:%2 sc1" ::"i"(128 + 4 * f), "i"(131 + 4 * f), "i"((f & 3) * 1024) : "memory");
    if constexpr ((f & 3) == 3) asm volatile("s_addk_i32 s96, 4096");
  }
  if constexpr (R == 4 && Q == 35) asm volatile(FL_POLL ::: "memory");
  if constexpr (R >= 6 && vm >= 0)
    asm volatile("buffer_load_dwordx4 v%c0, s[88:91], s98 offen offset:%1 sc1 lds" ::"i"(86 + vm / 4), "i"((vm & 3) * 1024) : "memory");
  if constexpr (R == 6 && Q == 35) asm volatile("s_add_u32 s98, s98, 0x8000");
  if constexpr (R == 7) {
    if constexpr (Q < 4) asm volatile("s_nop 0");
    cv_zs<cv_z0(Q)>(c, make_iseq<cv_z0(Q + 1) - cv_z0(Q)>{});
  }
  __builtin_amdgcn_sched_barrier(0);
}
template <int R, int... Q> __device__ __forceinline__ void cv_step(CV& c, iseq<Q...>) { (cv_slot<R, Q>(c), ...); }
template <int... N> __device__ __forceinline__ void cv_rd0(CV& c, iseq<N...>) { (cv_rd<0, N>(c), ...); }
template <int FLAG> __device__ __forceinline__ void conv_wave(unsigned sz, float Dd, char* lw) {
  int lane = threadIdx.x & 63; asm volatile("" : "+v"(lane));
  const int l16 = lane & 15, fq = lane >> 4;
  u32x4c w[5]; unsigned w4[5];
#pragma unroll
  for (int r = 0; r < 5; ++r) {
    w[r] = *(const u32x4c*)(lw + r * 1024 + lane * 16);
    w4[r] = *(const unsigned*)(lw + r * 1024 + lane * 16 + 16);
  }
  d_store<NS_X>(); d_store<NS_X + 1>();
#pragma unroll
  for (int r = 0; r < 5; ++r)
    *(u32x4c*)(lw + 10304 + r * 1024 + lane * 16) = (u32x4c){__builtin_amdgcn_alignbit(w[r][1], w[r][0], 16), __builtin_amdgcn_alignbit(w[r][2], w[r][1], 16),
                                                             __builtin_amdgcn_alignbit(w[r][3], w[r][2], 16), __builtin_amdgcn_alignbit(w4[r], w[r][3], 16)};
  d_store<NS_X + 2>();
  CV c;
#pragma unroll
  for (int i = 0; i < 4; ++i) c.pa[i] = (unsigned)(size_t)lw + ((l16 + 1) & 1) * 10304 + 4 * ((8 * fq + l16 + 1) >> 1) + 1024 * i;
  c.pb = (unsigned)(size_t)lw + 5120 + (2032 - 16 * l16 + 8 * fq) * 2 - 512 * 7;
  c.vz = (16 * l16 + 4 * fq) * 2;
  c.px = (unsigned)(size_t)lw + 5120 + 2044 * 2 - 512 * 7 - c.vz;
  c.sz = sz; c.D = Dd;
  asm volatile("" : "+v"(c.vz), "+v"(c.px), "+v"(c.D) :: "memory");
  asm volatile("" : "+s"(c.sz));
  cv_rd0(c, make_iseq<24>{});
  d_store<NS_X + 3>(); d_store<NS_X + 4>();
  __builtin_amdgcn_sched_barrier(0);
  static_for([&](auto R) __attribute__((always_inline)) {
    cv_step<R>(c, make_iseq<36>{});
    if constexpr (R == 2) {
      asm volatile("s_waitcnt vmcnt(0)\n\ts_barrier" ::: "memory");
      fl_set<FLAG>();
    }
    if constexpr (R == 5) {
      asm volatile("s_waitcnt vmcnt(0)" ::: "memory");
      fl_check();
    }
    __builtin_amdgcn_sched_barrier(0);
  }, make_iseq<8>{});
  asm volatile("s_nop 7");
  cv_zs<cv_z0(36)>(c, make_iseq<104 - cv_z0(36)>{});
}

__device__ __forceinline__ void post_dma(int cg, int tb, const bf16* __restrict__ q, unsigned m0w) {
  int lane = threadIdx.x & 63; asm volatile("" : "+v"(lane));
  const __amdgpu_buffer_rsrc_t sq = __builtin_amdgcn_make_buffer_rsrc((void*)(q + (size_t)tb * 32 * DA + cg * 64), (short)0, 32 * DA * 2, 0x00020000);
  const unsigned vq = ((lane >> 3) * DA + (lane & 7) * 8) * 2;
  asm volatile("s_mov_b32 m0, %0\n\ts_nop 4\n\tbuffer_load_dwordx4 %1, %2, 0 offen" NTQS " lds\n\tbuffer_load_dwordx4 %1, %2, %3 offen offset:1024" NTQS " lds\n\t"
               "buffer_load_dwordx4 %1, %2, %4 offen offset:2048" NTQS " lds\n\tbuffer_load_dwordx4 %1, %2, %5 offen offset:3072" NTQS " lds"
               ::"s"(m0w + 4096), "v"(vq), "s"(sq), "s"(8 * DA * 2 - 1024), "s"(16 * DA * 2 - 2048), "s"(24 * DA * 2 - 3072) : "memory");
}
__device__ __forceinline__ void post_math(int cg, int tb, bf16* __restrict__ out, const char* lw) {
  int lane = threadIdx.x & 63; asm volatile("" : "+v"(lane));
  const int tl = lane >> 3, ck = lane & 7;
  const __amdgpu_buffer_rsrc_t so = __builtin_amdgcn_make_buffer_rsrc((void*)(out + (size_t)tb * 32 * OUTW + cg * 64), (short)0, 32 * OUTW * 2 - 2 * DA, 0x00020000);
  const unsigned vo = (tl * OUTW + ck * 8) * 2;
#pragma unroll
  for (int i = 0; i < 4; ++i) {
    const u32x4c qv = *(const u32x4c*)(lw + 4096 + i * 1024 + lane * 16);
    const char* lz = lw + ck * 64 + 16 * (i ^ (ck >> 1)) + tl * 2;
    u32x4c o;
#pragma unroll
    for (int e = 0; e < 4; ++e) {
      const float z0 = bf2f(*(const unsigned short*)(lz + 2 * e * 512));
      const float z1 = bf2f(*(const unsigned short*)(lz + (2 * e + 1) * 512));
      o[e] = pk2(bfl(qv[e]) * z0, bfh(qv[e]) * z1);
    }
    __builtin_amdgcn_raw_buffer_store_b128(o, so, vo + i * 8 * OUTW * 2, 0, OUTSC);
  }
}

#define DI __device__ __forceinline__
#define GAP() __builtin_amdgcn_sched_barrier(0)
typedef __attribute__((ext_vector_type(4))) float f32x4g;
typedef __attribute__((ext_vector_type(4))) unsigned u32x4g;
constexpr int LDW = NH * HD;
struct GM { unsigned aA[2], aB[2][8]; };
template <bool W, int J, int V, bool NT = false> DI void g_dma() {
  if constexpr (NT && J == 0) asm volatile("buffer_load_dwordx4 v%c0, s[60:63], 0 offen nt lds" ::"i"(V) : "memory");
  else if constexpr (NT) asm volatile("buffer_load_dwordx4 v%c0, s[60:63], s%c1 offen nt lds" ::"i"(V), "i"(70 + J) : "memory");
  else
  if constexpr (J == 0) asm volatile("buffer_load_dwordx4 v%c0, s[%c1:%c2], 0 offen lds" ::"i"(V), "i"(W ? 60 : 56), "i"(W ? 63 : 59) : "memory");
  else asm volatile("buffer_load_dwordx4 v%c0, s[%c1:%c2], s%c3 offen lds" ::"i"(V), "i"(W ? 60 : 56), "i"(W ? 63 : 59), "i"((W ? 70 : 63) + J) : "memory");
}
template <int P, int KS, int I> DI void g_wread(GM& g) {
  constexpr int nf = I >> 1, hf = I & 1, r = 192 - 64 * P + 8 * nf + 4 * KS + 2 * hf;
  asm volatile("ds_read_b64_tr_b16 a[%c1:%c2], %0 offset:%c3" ::"v"(g.aB[hf][nf]), "i"(r), "i"(r + 1), "i"(P * 32768 + KS * 16384));
}
template <int P, int M, int KS> DI void g_aread(GM& g) {
  constexpr int r = 96 + 8 * (M & 1) + 4 * KS;
  asm volatile("ds_read_b128 v[%c1:%c2], %0 offset:%c3" ::"v"(g.aA[KS]), "i"(r), "i"(r + 3), "i"(P * 32768 + M * 2048));
}
constexpr float CS = 0.0625f * 1.4426950408889634f;
struct ES { int op0[129], nop[129]; };
constexpr ES e_sched(bool TR, bool SCL) {
  ES z{};
  const int per = SCL ? 16 : 8;
  int o = 0;
  for (int G = 0; G <= 128; ++G) {
    int cap = G == 128 ? 10000 : SCL ? 4 : 2;
    z.op0[G] = o;
    while (o < 32 * per) {
      const int i = o / per, rdy = (TR ? 32 * (i >> 3) + 16 + 2 * (i & 7) + 1 : 4 * i + 3) + 4;
      if ((G < rdy && G < 128) || cap-- <= 0) break;
      ++o; ++z.nop[G];
    }
  }
  return z;
}
template <bool TR, bool SCL> constexpr ES ESCH = e_sched(TR, SCL);
constexpr int e_vm(int G) { return (G * 3) % 8 < 3 ? G * 3 / 8 : -1; }
constexpr int e_st(int MODE, int G) {
  if (MODE == 5) return e_vm(G) >= 32 ? e_vm(G) - 32 : -1;
  if (MODE == 3) return G >= 31 && (G - 31) % 5 == 0 && (G - 31) / 5 < NS_X ? (G - 31) / 5 : -1;
  const int g0 = MODE == 4 ? 5 : 13, gs = MODE == 2 ? 7 : 6;
  return MODE >= 2 && G >= g0 && (G - g0) % gs == 0 && (G - g0) / gs < 16 ? (G - g0) / gs + (MODE == 4 ? 16 : 0) : -1;
}
constexpr int e_nst(int MODE, int G0, int G1) { int n = 0; for (int G = G0; G < G1; ++G) n += e_st(MODE, G) >= 0; return n; }
static_assert(e_nst(2, 0, 128) == 16 && e_nst(3, 30, 128) == NS_X && e_nst(5, 0, 128) == 16 && e_nst(4, 0, 104) == 16);
template <bool TR, bool SCL, int O> DI void e_op() {
  constexpr int per = SCL ? 16 : 8, i = O / per, k = O % per, r = 128 + 4 * i, t = 114 + 4 * (i & 1), c = k - (per - 4);
  if constexpr (k < 4) asm volatile("v_accvgpr_read_b32 v%c0, a[%c1]" ::"i"(t + k), "i"(4 * i + k));
  else if constexpr (c < 0) asm volatile("v_mul_f32 v%c0, %1, v%c0" ::"i"(k < 8 ? r + (k ^ 6) : t + k - 8), "s"(CS));
  else {
    constexpr int d = c == 0 ? r + 3 : c == 1 ? r + 2 : r + c - 2, x = c == 0 ? r + 2 : c == 1 ? r : t + 2 * (c - 2);
    asm volatile("v_cvt_pk_bf16_f32 v%c0, v%c1, v%c2" ::"i"(d), "i"(x), "i"(x + 1));
  }
}
template <bool TR, bool SCL, int B, int... Is> DI void e_ops(iseq<Is...>) { (e_op<TR, SCL, B + Is>(), ...); }
constexpr int e_slot(int MODE, int G) { return MODE == 3 ? (G % 3 == 0 && G < 30 ? G / 3 : -1) : e_vm(G) >= 0 && e_vm(G) < 32 ? 10 + e_vm(G) : -1; }
template <int S> DI void e_m0() {
  if constexpr (S == 0 || S == 1 || S == 5 || S == 9) asm volatile("s_add_u32 m0, s78, %0" ::"i"(S * 1024));
  if constexpr (S >= 26 && (S - 26) % 4 == 0) asm volatile("s_add_u32 m0, s79, %0" ::"i"(((S - 26) >> 3) * 32768 + ((S - 26) & 4) * 1024));
}
template <int S> DI void e_load() {
  if constexpr (S == 0) asm volatile("buffer_load_dwordx4 v122, s[56:59], 0 offen lds" ::: "memory");
  else if constexpr (S < 5) asm volatile("buffer_load_dwordx4 v84, s[56:59], s64 offen offset:%0 lds" ::"i"((S - 1) * 1024) : "memory");
  else if constexpr (S < 9) asm volatile("buffer_load_dwordx4 v84, s[60:63], 0 offen offset:%0 sc1 lds" ::"i"((S - 5) * 1024) : "memory");
  else if constexpr (S == 9) asm volatile("buffer_load_dwordx4 v84, s[60:63], s62 offen sc1 lds" ::: "memory");
  else if constexpr (S < 26) {
    constexpr int f = S - 10;
    asm volatile("buffer_load_dwordx4 a[%c0:%c1], v84, s[88:91], s96 offen offset:%2 sc1" ::"i"(128 + 4 * f), "i"(131 + 4 * f), "i"((f & 3) * 1024) : "memory");
    if constexpr ((f & 3) == 3) asm volatile("s_addk_i32 s96, 4096");
  } else {
    constexpr int f = S - 26;
    asm volatile("buffer_load_dwordx4 v%c0, s[88:91], s98 offen offset:%1 sc1 lds" ::"i"(86 + ((f >> 2) & 1)), "i"((f & 3) * 1024) : "memory");
    if constexpr (f == 7) asm volatile("s_add_u32 s98, s98, s97");
    if constexpr (f == 15) asm volatile(FL_POLL ::: "memory");
  }
}
template <bool TR, int P, int H, int G, int MODE, bool NT> DI void g_gap(GM& g) {
  constexpr int m = 4 * H + (G >> 4), q = G & 15, n = q >> 1, ks = q & 1, GG = 64 * H + G;
  constexpr bool inV = TR ? m & 1 : n & 1, END = MODE == 3 || MODE == 5, EPI = END || MODE == 2;
  constexpr int a = 96 + 8 * (m & 1) + 4 * ks, b = 192 - 64 * P + 8 * n + 4 * ks, d = (inV ? 128 : 0) + 4 * (TR ? 8 * (m >> 1) + n : 4 * m + (n >> 1));
  constexpr int J = END ? -1 : q == 8 ? 2 * (G >> 4) : q == 12 ? 2 * (G >> 4) + 1 : -1, gj = (J & 3) ^ ((J & 3) >> 1);
  if constexpr (END) { if constexpr (q == 0 && GG > 0) asm volatile("s_waitcnt lgkmcnt(0)"); e_m0<e_slot(MODE, GG)>(); }
  else if constexpr (q == 0 && G > 0) asm volatile("s_waitcnt lgkmcnt(4)");
  if constexpr (J == 0) asm volatile("s_add_u32 m0, s78, %0" ::"i"((P ^ H ^ 1) * 32768 + (1 - H) * 8192));
  else if constexpr (J == 4) asm volatile("s_add_u32 m0, s79, %0" ::"i"((P ^ H) * 32768 + (1 - H) * 16384));
  else if constexpr (J > 0 && (J & 3) == 1) asm volatile("s_bitset1_b32 m0, 12");
  else if constexpr (J > 0 && (J & 3) == 2) asm volatile("s_bitset1_b32 m0, %0" ::"i"(J < 4 ? 14 : 13));
  else if constexpr (J > 0) asm volatile("s_bitset0_b32 m0, 12");
#define GMF(D, X, Y, C) asm volatile("v_mfma_f32_16x16x32_bf16 " D "[%c0:%c1], " X ", " Y ", " C ::"i"(d), "i"(d + 3), "i"(a), "i"(a + 3), "i"(b), "i"(b + 3))
#define GMF2(D, C) do { if constexpr (TR) GMF(D, "v[%c2:%c3]", "a[%c4:%c5]", C); else GMF(D, "a[%c4:%c5]", "v[%c2:%c3]", C); } while (0)
  if constexpr ((MODE == 1 || MODE == 4) && ks == 0) { if constexpr (inV) GMF2("v", "0"); else GMF2("a", "0"); }
  else if constexpr (inV) GMF2("v", "v[%c0:%c1]");
  else GMF2("a", "a[%c0:%c1]");
#undef GMF2
#undef GMF
  if constexpr (e_st(MODE, GG) >= 0) {
    static_assert(MODE == 4 || ESCH<TR, MODE == 2>.op0[GG] >= (e_st(MODE, GG) + 1) * (MODE == 2 ? 16 : 8));
    d_store<e_st(MODE, GG)>();
  }
  if constexpr (EPI) e_ops<TR, MODE == 2, ESCH<TR, MODE == 2>.op0[GG]>(make_iseq<ESCH<TR, MODE == 2>.nop[GG]>{});
  if constexpr (J >= 0 && J < 4) g_dma<false, (1 - H) * 2 + (gj & 1) + (gj >> 1) * 4, 90>();
  else if constexpr (J >= 4) g_dma<true, (1 - H) * 4 + gj, 91, NT>();
  if constexpr ((q == 1 || q == 2) && !(END && m == 7)) g_aread<(m < 7 ? P : P ^ 1), (m + 1) & 7, q - 1>(g);
  if constexpr (END) {
    if constexpr (e_slot(MODE, GG) >= 0) e_load<e_slot(MODE, GG)>();
    if constexpr (!TR && GG >= 3 && GG < 7 && e_slot(MODE, GG) < 0) asm volatile("s_nop 0");
    if constexpr (TR && GG >= 19 && GG < 21 && e_slot(MODE, GG) < 0) asm volatile("s_nop 0");
  } else {
    if constexpr (q >= 3 && q < 11 && (q & 1)) g_wread<P ^ 1, H, (G >> 4) * 4 + (q - 3) / 2>(g);
    if constexpr (H == 1 && G == 0) asm volatile("v_lshl_add_u32 v90, s82, 7, v92\n\ts_and_b32 s82, s80, 15");
    if constexpr (H == 1 && G == 4) asm volatile("s_lshr_b64 s[80:81], s[80:81], 4\n\tv_lshl_add_u32 v91, s82, 19, v93");
    constexpr int nst = e_nst(MODE, H ? 29 : 0, GG + 1);
    if constexpr (G == 61) asm volatile("s_waitcnt vmcnt(%0) lgkmcnt(0)\n\ts_barrier" ::"i"(12 + nst) : "memory");
  }
  if constexpr (EPI && GG == 127) {
    asm volatile("s_nop 7");
    e_ops<TR, MODE == 2, ESCH<TR, MODE == 2>.op0[128]>(make_iseq<ESCH<TR, MODE == 2>.nop[128]>{});
  }
  GAP();
}
template <bool TR, int P, int H, int MODE, bool NT, int... Gs> DI void g_half_(GM& g, iseq<Gs...>) { (g_gap<TR, P, H, Gs, MODE, NT>(g), ...); }
template <bool TR, int P, int H, int MODE, bool NT = false> DI void g_half(GM& g) { g_half_<TR, P, H, MODE, NT>(g, make_iseq<64>{}); }
template <bool TR, int P, int MODE, bool NT = false> DI void g_tile(GM& g) { g_half<TR, P, 0, MODE, NT>(g); g_half<TR, P, 1, MODE, NT>(g); }
template <int P, int... Is> DI void g_read0(GM& g, iseq<Is...>) {
  (g_wread<P, 0, Is>(g), ...); (g_wread<P, 1, Is>(g), ...);
  g_aread<P, 0, 0>(g); g_aread<P, 0, 1>(g);
}
template <int I> DI void g_pro1(const unsigned (&m)[4]) {
  constexpr int grp = I >> 2, i = I & 3, t = grp < 4 ? 0 : grp < 7 ? 1 : 2;
  constexpr bool isA = grp == 0 || grp == 3 || grp == 5;
  constexpr int jj = (grp == 3 ? 2 : 0) + (i & 1) + (i >> 1) * 4, ks = grp == 2 || grp == 6;
  asm volatile("s_add_u32 m0, %0, %1\n\ts_nop 0" ::"s"(m[(t & 1) * 2 + !isA]), "i"(isA ? jj * 4096 : ks * 16384 + (i >> 1) * 8192 + (i & 1) * 4096) : "scc");
  if constexpr (isA) g_dma<false, jj, t ? 90 : 92>();
  else g_dma<true, ks * 4 + i, t ? 91 : 93, NTW >= 2>();
}
template <int B, int... Is> DI void g_pro(const unsigned (&m)[4], iseq<Is...>) { (g_pro1<B + Is>(m), ...); }
template <bool TR, bool SCL, class HOOK> DI void g_loop(GM& g, unsigned evm, HOOK&& hook) {
  constexpr int nkt = SCL ? 7 : 16, P0 = SCL ? 0 : 1;
  asm volatile("" : "+s"(evm));
#pragma unroll 1
  for (int t = 1;; t += 2) {
    g_half<TR, P0 ^ 1, 0, 0, (SCL ? NTW >= 1 : NTW >= 2)>(g);
    if (__builtin_expect((evm >> t) & 1, 0)) { hook(t); GAP(); }
    g_half<TR, P0 ^ 1, 1, 0, (SCL ? NTW >= 1 : NTW >= 2)>(g);
    if (SCL && t + 2 >= nkt) break;
    g_half<TR, P0, 0, 0, (SCL ? NTW >= 1 : NTW >= 2)>(g);
    if constexpr (SCL) { if (__builtin_expect(t == 3, 0)) { hook(4); GAP(); } }
    else asm volatile("s_cmp_eq_u32 %0, 13\n\ts_cselect_b32 s78, 0x28000, s78" ::"s"(t) : "scc");
    g_half<TR, P0, 1, 0, (SCL ? NTW >= 1 : NTW >= 2)>(g);
    if (!SCL && t + 3 >= nkt) break;
  }
}

typedef __attribute__((ext_vector_type(4))) unsigned u32x4;
typedef __attribute__((ext_vector_type(4))) float f32x4;

constexpr int R_S = 128, R_P = 192, R_CI = 224, R_E = 96;
constexpr int R_TS = 112, R_RS = 114, R_M = 116;
constexpr int R_PD = 118, R_THR = 120, R_TMP = 121;
constexpr int r_dk(int q) { return q < 6 ? 90 + q : 120 + q; }
constexpr int S_V = 96, S_T = 97, S_K = 98, S_I = 99, S_EV = 100;
struct AC {
  unsigned rare;
};
DI bool redo() { u64 t; asm volatile("s_mov_b64 %0, s[54:55]" : "=s"(t)); return t != 0; }
template <int X, int F> DI void kread() {
  constexpr int r = 192 + 4 * (F & 15);
  asm volatile("ds_read_b128 a[%c0:%c1], v84 offset:%c2" ::"i"(r), "i"(r + 3), "i"(X * 32768 + F * 1024));
}
template <int X, int F> DI void vread() {
  constexpr int r = 192 + 4 * (F & 15);
  asm volatile("ds_read_b128 a[%c0:%c1], v85 offset:%c2" ::"i"(r), "i"(r + 3), "i"(X * 32768 + (F & 1) * 16384 + (F >> 1) * 1024));
}
template <int X, int... Fs> DI void kreads(iseq<Fs...>) { (kread<X, Fs>(), ...); }
template <int X, int J> DI void dma_m0() {
  if constexpr (J == 0) asm volatile("s_mov_b32 m0, s%c0" ::"i"(78 + X));
  else if constexpr (J == 4) asm volatile("s_or_b32 m0, s%c0, 0x1000" ::"i"(78 + X));
  else if constexpr (J == 8) asm volatile("s_bitset0_b32 m0, 16");
  else if constexpr (J == 12) asm volatile("s_bitset0_b32 m0, 12");
}
template <int J> DI void dma_at() {
  if constexpr (J < 8)
    asm volatile("buffer_load_dwordx4 v%c0, s[88:91], s96 offen offset:%1 sc1 lds" ::"i"(86 + (J >= 4)), "i"((J & 3) * 1024) : "memory");
  else
    asm volatile("buffer_load_dwordx4 v%c0, s[92:95], s98 offen offset:%1 sc1 lds" ::"i"(r_dk(J - 8)), "i"((J & 3) * 1024) : "memory");
}
template <int Q> DI void dma4k() {
  asm volatile("s_add_u32 m0, s79, %0\n\ts_add_u32 s48, s98, %1\n\tbuffer_load_dwordx4 v%c2, s[88:91], s48 offen sc1 lds\n\t"
               "buffer_load_dwordx4 v%c2, s[88:91], s48 offen offset:1024 sc1 lds\n\tbuffer_load_dwordx4 v%c2, s[88:91], s48 offen offset:2048 sc1 lds\n\t"
               "buffer_load_dwordx4 v%c2, s[88:91], s48 offen offset:3072 sc1 lds"
               ::"i"((Q >> 1) * 32768 + (Q & 1) * 4096), "i"((Q >> 1) * 32768), "i"(86 + (Q & 1)) : "memory", "scc");
}
template <int Q> DI void acc_zero2() {
  asm volatile("v_accvgpr_write_b32 a[%c0], 0\n\tv_accvgpr_write_b32 a[%c1], 0" ::"i"(2 * Q), "i"(2 * Q + 1));
}
#define SMV 0
struct SMS { int ex[128], op0[128], nop[128], last; };
constexpr SMS sm_sched() {
  SMS z{};
  int et[32] = {}, e = 0;
  for (int t = 0; t < 128; ++t) {
    const int g = t & 15;
    const bool x = t < 48 ? (g >= 8 && (g & 1) && g < 15) : t < 64 ? (g >= 9 && g < 15) : (g >= 8 && g < 14);
    z.ex[t] = x && e < 32 ? e : -1;
    if (z.ex[t] >= 0) et[e++] = t;
  }
  int o = 0;
  for (int t = 0; t < 128; ++t) {
    const int g = t & 15;
    int cap = z.ex[t] >= 0 || t < 8 || (t < 48 && (g == 7 || (g >= 8 && !(g & 1)))) || t == 56 || t == 63 || t == 111 ? 0 : 1;
    z.op0[t] = o; z.nop[t] = 0;
    while (cap > 0 && o < 48 && et[2 * (o / 3) + (o % 3 == 0 ? 0 : 1)] + 2 <= t) {
      const int el = 2 * (o / 3) + o % 3;
      if (!(o % 3 < 2 && (el == 0 || el == 4))) --cap;
      ++o; ++z.nop[t]; z.last = t;
    }
  }
  for (int k = 16; k < 32; ++k) if (z.op0[et[k]] < 3 * ((k - 16) / 2 + 1)) z.last = 1000;
  return z;
}
constexpr SMS SM = sm_sched();
static_assert(SM.last <= 117);
constexpr int p_reg(int Y, int k) { return R_P + 16 * Y + 4 * ((k >> 3) * 2 + ((k >> 1) & 1)) + ((k >> 2) & 1) * 2 + (k & 1); }
template <int Y, int O> DI void sm_op() {
  constexpr int k = O / 3, e = 2 * k + O % 3, ts = R_TS + ((e >> 2) & 1);
  if constexpr (O % 3 == 2) asm volatile("v_cvt_pk_bf16_f32 v%c0, v%c1, v%c2" ::"i"(p_reg(Y, k)), "i"(R_E + ((2 * k) & 15)), "i"(R_E + ((2 * k + 1) & 15)));
  else if constexpr (e == 1 || e == 5) asm volatile("v_add_f32_e32 v%c0, v%c1, v%c2" ::"i"(ts), "i"(R_E + e - 1), "i"(R_E + e));
  else if constexpr (e != 0 && e != 4) asm volatile("v_add_f32_e32 v%c0, v%c0, v%c1" ::"i"(ts), "i"(R_E + (e & 15)));
}
template <int Y, int T> DI void sm_gap(AC& c) {
  if constexpr (SM.ex[T] >= 0) asm volatile("v_exp_f32 v%c0, v%c1" ::"i"(R_E + (SM.ex[T] & 15)), "i"(R_S + 32 * Y + SM.ex[T]));
  if constexpr (SM.nop[T] > 0) sm_op<Y, SM.op0[T]>();
  if constexpr (SM.nop[T] > 1) sm_op<Y, SM.op0[T] + 1>();
  if constexpr (SM.nop[T] > 2) sm_op<Y, SM.op0[T] + 2>();
  if constexpr (T == SM.last + 1) asm volatile("v_add_f32_e32 v%c0, v%c1, v%c2" ::"i"(R_TMP), "i"(R_TS), "i"(R_TS + 1));
  if constexpr (T == SM.last + 2) asm volatile("v_cmp_nle_f32_e64 s[54:55], v%c0, v%c1" ::"i"(R_TMP), "i"(R_THR));
}
#define W_A0 1
template <int X, int MODE, int G> DI void gapA(AC& c) {
  constexpr bool FIRST = MODE == 1 || MODE == 2, NOQK = MODE >= 3;
  constexpr int Xp = X ^ 1, kb = G >> 4, g = G & 15, qb = g >> 3, s = g & 7;
  if constexpr (MODE == 3 && G < 48 && G % 3 == 0) {
    constexpr int f = G / 3;
    asm volatile("buffer_load_dwordx4 a[%c0:%c1], v84, s[88:91], s75 offen offset:%2 sc1" ::"i"(128 + 4 * f), "i"(131 + 4 * f), "i"((f & 3) * 1024) : "memory");
    if constexpr ((f & 3) == 3) asm volatile("s_addk_i32 s75, 4096");
  }
  if constexpr (G == 48) asm volatile("s_waitcnt vmcnt(%0) lgkmcnt(0)\n\ts_barrier" ::"i"(MODE == 3 ? 16 : MODE == 2 ? 12 : MODE == 1 ? W_A0 : 0) : "memory");
  else if constexpr (g == 0 && !NOQK) asm volatile("s_waitcnt lgkmcnt(0)");
  if constexpr (!NOQK) {
    constexpr int r = 192 + 4 * ((kb & 1) * 8 + s), q = 128 + 4 * g;
    constexpr int d = R_S + 32 * X + 4 * (kb * 2 + qb), ci = s == 0 ? R_CI + 4 * (kb * 2 + qb) : d;
    if constexpr (FIRST && s == 0)
      asm volatile("v_mfma_f32_16x16x32_bf16 v[%c0:%c1], a[%c2:%c3], a[%c4:%c5], 0" ::"i"(d), "i"(d + 3), "i"(r), "i"(r + 3), "i"(q), "i"(q + 3));
    else
      asm volatile("v_mfma_f32_16x16x32_bf16 v[%c0:%c1], a[%c2:%c3], a[%c4:%c5], v[%c6:%c7]" ::"i"(d), "i"(d + 3), "i"(r), "i"(r + 3), "i"(q), "i"(q + 3),
                   "i"(ci), "i"(ci + 3));
    if constexpr (g < 8 && kb < 3) kread<X, (kb + 1) * 8 + g>();
    constexpr int S0 = SMV ? 40 : 14, S1 = SMV ? 41 : 30, S2 = SMV ? 42 : 45, S3 = SMV ? 43 : 46;
    if constexpr (G == S0) asm volatile("s_add_u32 s96, s97, 0x1000000");
    if constexpr (G == S1) dma_m0<X, 0>();
    if constexpr (G == S2) asm volatile("s_mov_b32 s97, s98");
    if constexpr (G == S3) asm volatile("s_add_u32 s98, s98, s99");
    if constexpr (G >= 56 && !(G & 1)) dma_at<(G - 56) / 2>();
    if constexpr (G == 57) asm volatile("s_lshr_b32 s100, s100, 1\n\ts_add_u32 s56, s56, 1");
    if constexpr (G == 59 && !FIRST) asm volatile("s_and_b32 s49, s100, 1\n\ts_or_b32 s49, s49, s54");
    if constexpr (G == 61 && !FIRST) asm volatile("s_or_b32 %0, s49, s55" : "=s"(c.rare));
    if constexpr (G == 61 && FIRST) asm volatile("s_and_b32 %0, s100, 1" : "=s"(c.rare));
  } else if constexpr (SM.nop[64 + G] > 0) asm volatile("s_nop 0");
  if constexpr (!FIRST && g < 8 && kb == 3) vread<Xp, g>();
  if constexpr (MODE == 2 && (G & 3) == 1) d_store<16 + G / 4>();
  if constexpr (FIRST) acc_zero2<G>();
  else sm_gap<Xp, 64 + G>(c);
  GAP();
}
template <int R> DI void rescale1(float alpha) {
  float t;
  asm volatile("v_accvgpr_read_b32 %0, a[%c2]\n\tv_mul_f32 %0, %0, %1\n\tv_accvgpr_write_b32 a[%c2], %0" : "=&v"(t) : "v"(alpha), "i"(R));
}
template <int... Rs> DI void rescale_all(const float (&alpha)[2], iseq<Rs...>) { (rescale1<Rs>(alpha[(Rs >> 2) & 1]), ...); }
template <bool MAX> DI float row_reduce(float v) {
  auto p = __builtin_amdgcn_permlane32_swap(__float_as_uint(v), __float_as_uint(v), false, false);
  v = MAX ? fmaxf(__uint_as_float(p[0]), __uint_as_float(p[1])) : __uint_as_float(p[0]) + __uint_as_float(p[1]);
  p = __builtin_amdgcn_permlane16_swap(__float_as_uint(v), __float_as_uint(v), false, false);
  return MAX ? fmaxf(__uint_as_float(p[0]), __uint_as_float(p[1])) : __uint_as_float(p[0]) + __uint_as_float(p[1]);
}
template <int Y, int QB> DI float row_max() {
  constexpr int r = R_S + 32 * Y + 4 * QB;
  float mx;
  asm volatile("v_max_f32 %0, v%c1, v%c2\n\tv_max3_f32 %0, %0, v%c3, v%c4" : "=&v"(mx) : "i"(r), "i"(r + 1), "i"(r + 2), "i"(r + 3));
  static_for([&](auto K) {
    constexpr int q = r + 8 * (K + 1);
    float t = mx;
    asm volatile("v_max3_f32 %0, %0, v%c1, v%c2\n\tv_max3_f32 %0, %0, v%c3, v%c4" : "+v"(t) : "i"(q), "i"(q + 1), "i"(q + 2), "i"(q + 3));
    mx = t;
  }, make_iseq<3>{});
  return row_reduce<true>(mx);
}
template <int R> DI void v_sub(float d) { asm volatile("v_sub_f32 v%c0, v%c0, %1" ::"i"(R), "v"(d)); }
template <int B> DI void s_shift(const float (&d)[2]) { static_for([&](auto E) { v_sub<B + E>(d[(E >> 2) & 1]); }, make_iseq<32>{}); }
DI void m_shift(const float (&d)[2]) {
  s_shift<R_CI>(d);
  v_sub<R_M>(-d[0]); v_sub<R_M + 1>(-d[1]);
}
DI void set_mask0(int wid) {
  const int mr = wid * 32 + __float_as_int(vget<89>());
  static_for([&](auto I) {
    constexpr int e = I, k = (e >> 3) * 16 - ((e >> 2) & 1) * 16 + (e & 3);
    const float x = vget<R_S + e>();
    vset<R_S + e>(k > mr ? -INFINITY : x);
  }, make_iseq<32>{});
}
template <int X> DI void sm_redo() {
  constexpr int Xp = X ^ 1;
  float d[2], dt[2], alpha[2], ts[2] = {0.f, 0.f};
  static_for([&](auto Q) {
    constexpr int qb = Q;
    const float pd = vget<R_PD + qb>();
    d[qb] = fmaxf(row_max<Xp, qb>() - pd, 0.f);
    dt[qb] = d[qb] + pd;
    vset<R_PD + qb>(d[qb]);
    alpha[qb] = __builtin_amdgcn_exp2f(-d[qb]);
    vset<R_RS + qb>(vget<R_RS + qb>() * alpha[qb]);
  }, make_iseq<2>{});
  vset<R_THR>(d[0] > 0.f || d[1] > 0.f ? -1.f : 4096.f);
  m_shift(d);
  static_for([&](auto K) {
    constexpr int k = K, qb = (k >> 1) & 1;
    const float e0 = __builtin_amdgcn_exp2f(vget<R_S + 32 * Xp + 2 * k>() - dt[qb]), e1 = __builtin_amdgcn_exp2f(vget<R_S + 32 * Xp + 2 * k + 1>() - dt[qb]);
    ts[qb] += e0; ts[qb] += e1;
    vset<p_reg(Xp, k)>(__uint_as_float(pk2(e0, e1)));
  }, make_iseq<16>{});
  vset<R_TS>(ts[0]); vset<R_TS + 1>(ts[1]);
  rescale_all(alpha, make_iseq<128>{});
  asm volatile("s_nop 1" ::: "memory");
  GAP();
}
template <int X, int MODE, int... Gs> DI void phaseA_(AC& c, iseq<Gs...>) { (gapA<X, MODE, Gs>(c), ...); }
template <int X, int MODE> DI void phaseA(AC& c) { phaseA_<X, MODE>(c, make_iseq<64>{}); }

template <int X, bool SMX, int G> DI void gapB(AC& c) {
  constexpr int Xp = X ^ 1, blk = G >> 4, g = G & 15;
  if constexpr (g == 0) asm volatile("s_waitcnt lgkmcnt(0)");
  constexpr int qb = (g >> 1) & 1, s = g & 1, r = 192 + 4 * ((blk & 1) * 8 + (g >> 2) * 2 + s), o = 4 * ((blk * 4 + (g >> 2)) * 2 + qb);
  constexpr int p = R_P + 16 * Xp + 4 * (s * 2 + qb);
  asm volatile("v_mfma_f32_16x16x32_bf16 a[%c0:%c1], a[%c2:%c3], v[%c4:%c5], a[%c0:%c1]" ::"i"(o), "i"(o + 3), "i"(r), "i"(r + 3), "i"(p), "i"(p + 3));
  if constexpr (g < 8) {
    if constexpr (blk < 3) vread<Xp, (blk + 1) * 8 + g>();
    else if constexpr (SMX) kread<Xp, g>();
  }
  if constexpr (SMX) {
    if constexpr (G == 1 || G == 2) asm volatile("v_add_f32_e32 v%c0, v%c0, v%c1" ::"i"(R_RS + G - 1), "i"(R_TS + G - 1));
    if constexpr (g == 7 && blk < 3) dma_m0<X, blk * 4 + 4>();
    if constexpr (g >= 8 && !(g & 1) && blk < 3) dma_at<blk * 4 + 4 + (g - 8) / 2>();
    if constexpr (G == 55 && X == 1) asm volatile("s_cmp_eq_u32 s56, s76");
    if constexpr (G == 3) asm volatile("s_lshr_b32 s77, s77, 1");
    if constexpr (G == 4) asm volatile("s_bitcmp1_b32 s77, 0");
    if constexpr (G == 5) asm volatile("s_cselect_b64 s[52:53], s[62:63], 0");
    if constexpr (G == 6) asm volatile(FL_ARM("s[52:53]"));
    if constexpr (G == 56) asm volatile(FL_POLL ::: "memory");
    sm_gap<X, G>(c);
  }
  GAP();
}
template <int X, int... Gs> DI void phaseB_(AC& c, iseq<Gs...>) { (gapB<X, true, Gs>(c), ...); }
template <int X> DI void phaseB(AC& c) { phaseB_<X>(c, make_iseq<64>{}); }
constexpr int sm_nex() { int n = 0; for (int t = 0; t < 64; ++t) n += SM.ex[t] >= 0; return n; }
template <int X> DI void phaseB0(unsigned mode, int wid) {
  dma_m0<X, 4>();
  kreads<X ^ 1>(make_iseq<8>{});
  dma_at<4>();
  asm volatile("s_nop 3");
  float d[2];
  if (mode & 2) {
    d[0] = vget<122>(); d[1] = vget<123>();
    dma_at<5>();
  } else {
    if constexpr (X == 0) if (__builtin_expect(mode & 4, 0)) set_mask0(wid);
    d[0] = row_max<X, 0>(); GAP(); dma_at<5>();
    d[1] = row_max<X, 1>();
#pragma unroll
    for (int qb = 0; qb < 2; ++qb) if (!(d[qb] > -3.0e38f)) d[qb] = 0.f;
  }
  GAP();
  vset<R_M>(d[0]); vset<R_M + 1>(d[1]);
  dma_at<6>();
  const f32x2c nd[2] = {{-d[0], -d[0]}, {-d[1], -d[1]}};
  static_for([&](auto E) {
    constexpr int e = E;
    const f32x2c v = nd[(e >> 1) & 1];
    asm volatile("v_pk_add_f32 v[%c0:%c1], v[%c0:%c1], %4\n\tv_mov_b64 v[%c2:%c3], %4" ::"i"(R_S + 32 * X + 2 * e), "i"(R_S + 32 * X + 2 * e + 1), "i"(R_CI + 2 * e), "i"(R_CI + 2 * e + 1), "v"(v));
    if constexpr (e == 3) dma_at<7>();
    if constexpr (e == 6) dma_m0<X, 8>();
    if constexpr (e == 7 || e == 11 || e == 15) dma_at<8 + (e - 7) / 4>();
  }, make_iseq<16>{});
  static_assert(SM.op0[64] >= 3 * ((sm_nex() - 15) / 2) && SM.op0[64] <= 24);
  static_for([&](auto E) {
    constexpr int e = E;
    if constexpr (e < sm_nex()) asm volatile("v_exp_f32 v%c0, v%c1" ::"i"(R_E + e), "i"(R_S + 32 * X + e));
    if constexpr (e == 3) dma_at<11>();
    if constexpr (e == 6) dma_m0<X, 12>();
    if constexpr (e == 7 || e == 11 || e == 15) dma_at<12 + (e - 7) / 4>();
  }, make_iseq<16>{});
  static_for([&](auto O) { sm_op<X, O>(); }, make_iseq<SM.op0[64]>{});
  dma_at<15>();
  static_for([&](auto E) { asm volatile("v_exp_f32 v%c0, v%c1" ::"i"(R_E + (int)E), "i"(R_S + 32 * X + 16 + (int)E)); }, make_iseq<(sm_nex() > 16 ? sm_nex() - 16 : 0)>{});
  asm volatile("buffer_store_dwordx2 v[%c0:%c1], v88, s[88:91], s76 offen sc1\n\ts_bitcmp1_b32 s77, 0\n\ts_cselect_b64 s[52:53], s[62:63], 0\n\t"
               FL_ARM("s[52:53]") "\n\t" FL_POLL ::"i"(R_M), "i"(R_M + 1) : "memory", "scc");
  GAP();
}
constexpr int T_ZO = 8;
#define NINF2(R) "v_mov_b64 v[" #R ":" #R "+1], v[122:123]\n\t"
#define NINF4(R) NINF2(R) NINF2(R + 2)
#define TRI(R) "v_cndmask_b32_e64 v[" #R "], v[" #R "], v122, s[48:49]\n\tv_cndmask_b32_e64 v[" #R "+1], v[" #R "+1], v122, s[50:51]\n\t" \
               "v_cndmask_b32_e64 v[" #R "+2], v[" #R "+2], v122, s[52:53]\n\tv_cndmask_b32_e64 v[" #R "+3], v[" #R "+3], v122, s[54:55]\n\t"
DI void ev_code() {
  asm volatile(
    "s_getpc_b64 s[82:83]\n\ts_add_u32 s82, s82, 12\n\ts_addc_u32 s83, s83, 0\n\ts_branch 9f\n\t"
    "s_bitcmp0_b32 s58, s56\n\ts_cbranch_scc1 1f\n\t" FL_SET("s70") "\n\t" FL_SET("s71") "\n"
    "1:\n\ts_bitcmp0_b32 s57, s56\n\ts_cbranch_scc1 2f\n\t" FL_CHECK("s[62:63]") "\n\t"
    "s_add_u32 s98, s98, s68\n\ts_mov_b32 s68, s69\n\ts_mov_b32 s69, 0\n\ts_mov_b64 s[62:63], s[64:65]\n\ts_mov_b64 s[64:65], s[66:67]\n"
    "2:\n\ts_bitcmp0_b32 s59, s56\n\ts_cbranch_scc1 3f\n\ts_mov_b32 s98, s72\n\ts_bitcmp0_b32 s73, 0\n\ts_cbranch_scc1 3f\n\t"
    "v_lshrrev_b32 v90, 7, v86\n\tv_and_b32 v91, 6, v90\n\tv_lshlrev_b32 v91, 4, v91\n\tv_and_b32 v92, 0x70, v86\n\tv_xor_b32 v91, v91, v92\n\t"
    "s_movk_i32 s48, 896\n\tv_mad_u32_u24 v94, v90, s48, v91\n\ts_mov_b64 s[92:93], s[80:81]\n\ts_mov_b32 s94, 229376\n\ts_movk_i32 s99, 128\n\t"
    "v_add_u32 v90, 28672, v94\n\tv_add_u32 v91, 34816, v94\n\tv_add_u32 v92, 40960, v94\n\tv_add_u32 v93, 47104, v94\n\t"
    "v_add_u32 v95, 6144, v94\n\tv_add_u32 v126, 12288, v94\n\tv_add_u32 v127, 18432, v94\n"
    "3:\n\ts_bitcmp0_b32 s60, s56\n\ts_cbranch_scc1 4f\n\t"
    "v_readlane_b32 s54, v78, %0\n\ts_add_u32 m0, s78, 0x10000\n\t"
    "s_lshr_b32 s53, s73, 16\n\ts_mulk_i32 s53, 3584\n\ts_mov_b64 s[48:49], s[80:81]\n\ts_add_u32 s50, s53, 3584\n\ts_mov_b32 s51, 0x20000\n\t"
    "v_lshlrev_b32 v121, 3, v84\n\tbuffer_load_dword v121, s[48:51], s53 offen lds\n\t"
    "s_bitcmp0_b32 s73, 3\n\ts_cbranch_scc1 4f\n\t"
    "v_lshlrev_b32 v121, 9, v84\n\tv_and_b32 v121, 0x38000, v121\n\tv_lshrrev_b32 v122, 9, v84\n\tv_lshl_add_u32 v121, v122, 12, v121\n\t"
    "v_lshrrev_b32 v122, 3, v84\n\tv_xor_b32 v122, v122, v84\n\tv_and_or_b32 v121, v122, 48, v121\n\t"
    "buffer_load_dwordx4 v121, s[88:91], s54 offen sc1 lds\n\ts_addk_i32 s54, 7168\n\t"
    "buffer_load_dwordx4 v121, s[88:91], s54 offen offset:1024 sc1 lds\n\ts_addk_i32 s54, 7168\n\t"
    "buffer_load_dwordx4 v121, s[88:91], s54 offen offset:2048 sc1 lds\n\ts_addk_i32 s54, 7168\n\t"
    "buffer_load_dwordx4 v121, s[88:91], s54 offen offset:3072 sc1 lds\n"
    "4:\n\ts_bitcmp0_b32 s61, s56\n\ts_cbranch_scc1 6f\n\t"
    "s_sub_u32 s74, s74, 64\n\ts_cmp_ge_i32 s74, 0\n\ts_cbranch_scc1 6f\n\t"
    "v_cmp_gt_i32 s[48:49], 0, v89\n\tv_cmp_gt_i32 s[50:51], 1, v89\n\tv_cmp_gt_i32 s[52:53], 2, v89\n\tv_cmp_gt_i32 s[54:55], 3, v89\n\t"
    "v_mov_b32 v122, 0xff800000\n\tv_mov_b32 v123, 0xff800000\n\t" NINF4(248)
    "s_cmp_eq_i32 s74, -32\n\ts_cbranch_scc0 5f\n\t" TRI(240) TRI(252) "s_setpc_b64 s[86:87]\n"
    "5:\n\t" NINF4(240) NINF4(244) NINF4(252) NINF4(232) TRI(224) TRI(236)
    "s_cmp_eq_i32 s74, -64\n\ts_cbranch_scc1 6f\n\t" NINF4(224) NINF4(228) NINF4(236)
    "6:\n\ts_setpc_b64 s[86:87]\n9:"
    ::"i"(T_ZO) : "scc");
}
DI void ev_call() { asm volatile("s_swappc_b64 s[86:87], s[82:83]" ::: "vcc", "scc", "memory"); GAP(); }

#define ACLOB_ALL "a0","a1","a2","a3","a4","a5","a6","a7","a8","a9","a10","a11","a12","a13","a14","a15","a16","a17","a18","a19","a20","a21","a22","a23","a24","a25","a26","a27","a28","a29","a30","a31","a32","a33","a34","a35","a36","a37","a38","a39","a40","a41","a42","a43","a44","a45","a46","a47","a48","a49","a50","a51","a52","a53","a54","a55","a56","a57","a58","a59","a60","a61","a62","a63","a64","a65","a66","a67","a68","a69","a70","a71","a72","a73","a74","a75","a76","a77","a78","a79","a80","a81","a82","a83","a84","a85","a86","a87","a88","a89","a90","a91","a92","a93","a94","a95","a96","a97","a98","a99","a100","a101","a102","a103","a104","a105","a106","a107","a108","a109","a110","a111","a112","a113","a114","a115","a116","a117","a118","a119","a120","a121","a122","a123","a124","a125","a126","a127","a128","a129","a130","a131","a132","a133","a134","a135","a136","a137","a138","a139","a140","a141","a142","a143","a144","a145","a146","a147","a148","a149","a150","a151","a152","a153","a154","a155","a156","a157","a158","a159","a160","a161","a162","a163","a164","a165","a166","a167","a168","a169","a170","a171","a172","a173","a174","a175","a176","a177","a178","a179","a180","a181","a182","a183","a184","a185","a186","a187","a188","a189","a190","a191","a192","a193","a194","a195","a196","a197","a198","a199","a200","a201","a202","a203","a204","a205","a206","a207","a208","a209","a210","a211","a212","a213","a214","a215","a216","a217","a218","a219","a220","a221","a222","a223","a224","a225","a226","a227","a228","a229","a230","a231","a232","a233","a234","a235","a236","a237","a238","a239","a240","a241","a242","a243","a244","a245","a246","a247","a248","a249","a250","a251","a252","a253","a254","a255"

struct EP { float fa[2], fb[2]; };
template <int I, int E, int MODE> DI void epi_pair(const EP& p) {
  constexpr int qb = I & 1, r = 16 * (I >> 1) + 4 * qb + (E >> 1) * 8 + (E & 1) * 2, t = 96 + 2 * E, w = 128 + 4 * I + E;
  if constexpr (MODE == 2) asm volatile("v_accvgpr_read_b32 v106, a[%c0]" ::"i"(w));
  asm volatile("v_accvgpr_read_b32 v%c0, a[%c2]\n\tv_accvgpr_read_b32 v%c1, a[%c3]" ::"i"(t), "i"(t + 1), "i"(r), "i"(r + 1));
  if constexpr (MODE) asm volatile("v_mul_f32 v%c0, v%c0, %2\n\tv_mul_f32 v%c1, v%c1, %2" ::"i"(t), "i"(t + 1), "v"(p.fa[qb]));
  if constexpr (MODE == 2)
    asm volatile("v_lshlrev_b32 v104, 16, v106\n\tv_and_b32 v105, 0xffff0000, v106\n\tv_fmac_f32 v%c0, v104, %2\n\tv_fmac_f32 v%c1, v105, %2"
                 ::"i"(t), "i"(t + 1), "v"(p.fb[qb]));
  asm volatile("v_cvt_pk_bf16_f32 v%c0, v%c1, v%c2" ::"i"(w), "i"(t), "i"(t + 1));
}
template <int I> DI void epi_store() {
  asm volatile("buffer_store_dwordx4 v[%c0:%c1], v84, s[88:91], s101 offen offset:%2 sc1" ::"i"(128 + 4 * I), "i"(131 + 4 * I), "i"((I & 3) * 1024) : "memory");
  if constexpr ((I & 3) == 3) asm volatile("s_addk_i32 s101, 4096");
}
constexpr int R_TB = 107, R_VS = 111;
template <int HF, int R> DI void p_read() {
  constexpr int mb = R >> 4, r = 64 * HF + 2 * R;
  asm volatile("ds_read_b64_tr_b16 a[%c1:%c2], v%c0 offset:%c3" ::"i"(R_TB + 2 * HF + mb), "i"(r), "i"(r + 1), "i"((R & 15) * 2048));
}
template <int QB, int E> DI void p_cvt() {
  asm volatile("v_cvt_pk_bf16_f32 v%c0, v%c1, v%c2" ::"i"(208 + 4 * QB + E), "i"(192 + 8 * QB + 2 * E), "i"(193 + 8 * QB + 2 * E));
}
template <int QB, int PR> DI void p_store() {
  asm volatile("buffer_store_dwordx4 v[%c0:%c1], v%c2, s[88:91], s96 offen offset:%3 sc1" ::"i"(208 + 4 * QB), "i"(211 + 4 * QB), "i"(R_VS + QB),
               "i"(PR * 128) : "memory");
}
constexpr int p_dma(int HF, int G) { return G == 3 && !HF ? 0 : G == 11 ? 1 - HF : G == 15 ? 2 - HF : G == 19 ? 3 - HF : G == 27 && HF ? 3 : -1; }
template <int HF, int G> DI void p_gap() {
  constexpr int ch = G >> 3, t = G & 7, a = 64 * HF + 32 * (ch & 1) + 4 * t, b = 128 + 4 * (2 * t + (ch >> 1)), d = 192 + 4 * ch;
  if constexpr (t == 0)
    asm volatile("v_mfma_f32_16x16x32_bf16 v[%c0:%c1], a[%c2:%c3], v[%c4:%c5], 0" ::"i"(d), "i"(d + 3), "i"(a), "i"(a + 3), "i"(b), "i"(b + 3));
  else
    asm volatile("v_mfma_f32_16x16x32_bf16 v[%c0:%c1], a[%c2:%c3], v[%c4:%c5], v[%c0:%c1]" ::"i"(d), "i"(d + 3), "i"(a), "i"(a + 3), "i"(b), "i"(b + 3));
  p_read<HF ^ 1, G>();
  if constexpr (G == 0) asm volatile("v_xor_b32 v%c0, s100, v%c0" ::"i"(R_TB + 2 * HF + 1));
  if constexpr (G == 17) asm volatile("v_xor_b32 v%c0, s100, v%c0" ::"i"(R_TB + 2 * (HF ^ 1)));
  if constexpr (G >= 4 && G <= 8 && G != 7) p_cvt<1, (G < 7 ? G - 4 : 3)>();
  if constexpr (G == 24 || (G == 7 && HF == 0)) asm volatile("s_waitcnt lgkmcnt(8)");
  if constexpr (G == 7 && HF == 1) asm volatile("s_waitcnt vmcnt(13) lgkmcnt(8)\n\ts_barrier" ::: "memory");
  if constexpr (G == 10) p_store<1, HF ^ 1>();
  if constexpr (G >= 20 && G < 24) p_cvt<0, G - 20>();
  if constexpr (G == 25) p_store<0, HF>();
  if constexpr (p_dma(HF, G) >= 0) dma_at<(HF ? 8 : 12) + p_dma(HF, G)>();
  if constexpr (HF == 0 && G == 2) asm volatile("s_bitset0_b32 m0, 12");
  if constexpr (HF == 0 && G == 13) asm volatile("s_add_u32 s96, s96, 0x40000");
  if constexpr (HF == 0 && G == 26) asm volatile("s_add_u32 s98, s98, 128");
  if constexpr (HF == 0 && G == 27) asm volatile("s_cmp_lt_u32 s98, 0x380");
  if constexpr (HF == 0 && G == 28) asm volatile("s_cselect_b32 s99, s99, 0x31000");
  if constexpr (HF == 1 && G == 1) asm volatile("s_xor_b32 s100, s100, 0x10000");
  if constexpr (HF == 1 && G == 2) asm volatile("s_xor_b32 s97, s97, s100");
  if constexpr (HF == 1 && G == 9) asm volatile("s_or_b32 m0, s97, s99");
  if constexpr (HF == 1 && G == 30) asm volatile("s_cmp_lt_u32 s98, 0x500");
  GAP();
}
template <int HF, int... Gs> DI void p_half(iseq<Gs...>) { (p_gap<HF, Gs>(), ...); }
constexpr int f_dma(int G) { return G == 2 ? 0 : G == 5 ? 1 : G == 8 ? 2 : G >= 12 && G <= 36 && G % 6 == 0 ? 1 + G / 6 : G >= 50 && G <= 68 && G % 6 == 2 ? G / 6 : -1; }
constexpr int f_ndma(int G) { int n = 0; for (int g = 0; g < G; ++g) n += f_dma(g) >= 0; return n; }
static_assert(f_ndma(74) == 12);
template <int MODE, int G> DI void gapF(AC& c, const EP& p) {
  if constexpr (MODE && G == 48) asm volatile("s_waitcnt lgkmcnt(0)\n\ts_barrier" ::: "memory");
  if constexpr (G < 64) gapB<0, false, G>(c);
  if constexpr (MODE == 2 && G >= 10 && (G - 10) % 16 == 0) asm volatile("s_waitcnt vmcnt(%0)" ::"i"(13 - (G - 10) / 4 + f_ndma(G)) : "memory");
  if constexpr (G >= 10) epi_pair<(G - 10) / 4, (G - 10) & 3, MODE>(p);
  if constexpr (MODE == 3 && G < 64 && (G & 1)) {
    constexpr int db = G >> 2, qb = (G >> 1) & 1, o = 4 * (db * 2 + qb), a = 224 + 4 * (db & 1), b = 128 + 4 * ((db >> 1) * 2 + qb);
    if constexpr ((G & 7) == 1) asm volatile("s_waitcnt vmcnt(%0)" ::"i"(15 - (G >> 2) + f_ndma(G)) : "memory");
    asm volatile("v_mfma_f32_16x16x32_bf16 a[%c0:%c1], v[%c2:%c3], a[%c4:%c5], a[%c0:%c1]" ::"i"(o), "i"(o + 3), "i"(a), "i"(a + 3), "i"(b), "i"(b + 3));
  }
  if constexpr (MODE == 0) {
    if constexpr (G >= 14 && ((G - 14) & 3) == 0) epi_store<(G - 14) / 4>();
    if constexpr (G == 73) epi_store<15>();
  } else {
    if constexpr (G == 0) asm volatile("s_or_b32 m0, s78, 0x1000\n\ts_movk_i32 s98, 256");
    if constexpr (G == 15) asm volatile("s_bitset0_b32 m0, 12");
    if constexpr (G == 48) asm volatile("s_or_b32 m0, s79, 0x1000\n\ts_movk_i32 s98, 384");
    if constexpr (f_dma(G) >= 0) dma_at<8 + (f_dma(G) & 7)>();
    if constexpr (G == 73) asm volatile("s_waitcnt vmcnt(4) lgkmcnt(0)" ::: "memory");
    if constexpr (G >= 48 && G < 64) { p_read<0, 2 * (G - 48)>(); p_read<0, 2 * (G - 48) + 1>(); }
  }
  GAP();
}
template <int MODE, int... Gs> DI void phaseF(AC& c, const EP& p, iseq<Gs...>) { (gapF<MODE, Gs>(c, p), ...); }
template <int U, int K> DI void r_load(unsigned vo, unsigned so) {
  asm volatile("buffer_load_dwordx4 a[%c0:%c1], %2, s[88:91], %3 offen offset:%4 sc1" ::"i"(64 * U + 4 * K), "i"(64 * U + 4 * K + 3), "v"(vo),
               "s"(so + (K >> 2) * 4096), "i"((K & 1) * 512 + ((K >> 1) & 1) * 2048) : "memory");
}
template <int U, int K> DI void r_sum() {
  constexpr int d = 100 + 4 * (K & 1), b = 64 * U + 4 * K;
  if constexpr (K < 2) asm volatile("v_mfma_f32_16x16x32_bf16 v[%c0:%c1], v[96:99], a[%c2:%c3], 0" ::"i"(d), "i"(d + 3), "i"(b), "i"(b + 3));
  else asm volatile("v_mfma_f32_16x16x32_bf16 v[%c0:%c1], v[96:99], a[%c2:%c3], v[%c0:%c1]" ::"i"(d), "i"(d + 3), "i"(b), "i"(b + 3));
}
template <int U> DI void r_fin(unsigned vo, __amdgpu_buffer_rsrc_t srd, unsigned so) {
  asm volatile("" : "+s"(srd), "+s"(so));
  static_for([&](auto K) { r_sum<U, K>(); }, make_iseq<16>{});
  asm volatile("s_nop 7");
  static_for([&](auto E) { asm volatile("v_cvt_pk_bf16_f32 v%c0, v%c1, v%c2" ::"i"(116 + 4 * U + E), "i"(100 + 2 * E), "i"(101 + 2 * E)); }, make_iseq<4>{});
  asm volatile("buffer_store_dwordx4 v[%c3:%c4], %0, %1, %2 offen" OUTSCS ::"v"(vo), "s"(srd), "s"(so), "i"(116 + 4 * U), "i"(119 + 4 * U) : "memory");
}

#define CMAX 12
#define PREPX false
static_assert(NYT <= 16 && NYT >= 12);
enum { T_Q0, T_K0, T_OQ, T_OG, T_FPREP, T_FQ, T_FG, T_FS,   T_CNT = 9, T_NPRE = 10, T_NST = 12, T_TAB = 14, T_SLAB = 16, T_PART, T_IT = 18, T_ITN = 23 };
struct TRow { unsigned v[64]; };
constexpr u64 kvmask(int m0, int m1) { u64 n = 0; for (int m = m0; m <= m1; ++m) n |= 0x101ull << m; return n; }
constexpr u64 k_order(bool isY, int rr) {
  const int cls = (rr + (rr >> 2)) & 3;
  return !isY ? (cls == 0 ? 0xfedcb9875a463210ull : cls == 1 ? 0xefadc6982b574310ull : cls == 2 ? 0xfebad7693c285410ull : 0xefcba8764d392510ull)
              : (cls == 0 ? 0xfedcb987543a6210ull : cls == 1 ? 0xefadc698254b7310ull : cls == 2 ? 0xfebad769325c8410ull : 0xefcba876432d9510ull);
}
constexpr TRow t_row(int r, int wid, int j) {
  TRow t{};
  const int rr = r & 15, hsel = rr & 1, pj = rr >> 1, kv = pj & 1, h = j + 8 * hsel, u = hsel * 8 + j;
  const bool isY = r >= 16, solo = pj == 7;
  const int mt = (rr >> 2) + (isY ? 4 : 0), g = j * 128, NY = solo ? 18 : NYT;
  const int nx0 = 32 - 2 * pj - NY, cx = nx0 < CMAX ? nx0 : CMAX, ex = nx0 - cx, qbF = isY != (solo && SWAP7) ? 15 - pj : pj;
  const unsigned oQ = (unsigned)h << 20, oK = oQ + O_K, oP = O_P + (h * 8 + pj) * PSTR;
  if (wid == 4) {
    for (int l = 0; l < 64; ++l)
      t.v[l] = fo(l < 16 ? g + hsel * 16 + l : l < 24 ? g + 16 + hsel * 8 + l : l == 24 ? g + 48 + hsel * 8 + pj : l < 32 ? g + 64 + hsel * 8 + pj :
                  l < 48 ? 2304 + pj * 16 + l - 32 : 2048 + pj * 16 + l - 48);
    return t;
  }
  auto put64 = [&](int i, u64 x) { t.v[i] = (unsigned)x; t.v[i + 1] = (unsigned)(x >> 32); };
  t.v[T_Q0] = oQ + ((solo ? qbF : 15 - pj) * 128 + wid * 32) * 512;
  t.v[T_K0] = oK + (isY ? cx : 0) * 32768;
  t.v[T_OQ] = oQ + pj * 131072 + (wid >> 1) * 65536 + (wid & 1) * 4096;
  t.v[T_OG] = oK + kv * TSB + mt * 131072 + (wid >> 1) * 65536 + (wid & 1) * (kv ? 8192 : 4096);
  t.v[T_FPREP] = fo(2048 + pj * 16 + u);
  t.v[T_FQ] = fo(g + 32 + hsel * 8 + pj);
  t.v[T_FG] = fo(g + hsel * 16 + kv * 8 + mt);
  t.v[T_FS] = fo(1024 + qbF * 16 + j * 2 + hsel);
  t.v[T_CNT] = fo(2560 + qbF * 16);
  t.v[T_ZO] = O_Z + pj * 64 * SEQ * 2 + (u * 4 + wid) * 64;
  const u64 own = 0x1ull << (kv * 8 + mt), needQ = isY ? 1ull << (16 + (qbF >> 1)) : (1ull << (16 + (pj >> 1))) | (1ull << (16 + ((15 - pj) >> 1)));
  put64(T_NPRE, isY ? needQ | (1ull << (cx >> 2)) | (1ull << ((cx + 1) >> 2)) : solo ? needQ : needQ | 0xffffull << 48);
  put64(T_NST, isY ? kvmask(cx >> 2, 3) | (solo ? 0 : 1ull << 25) : kvmask(0, solo ? 3 : 0));
  put64(T_TAB, k_order(isY, rr));
  t.v[T_SLAB] = O_S + qbF * 0x200000 + wid * 65536 + (j * 2 + hsel) * 1024;
  t.v[T_PART] = oP;
  for (int it = 0; it < 2; ++it) {
    unsigned* p = t.v + T_IT + it * T_ITN;
    const bool last = isY || it == 1;
    const int n = solo ? 2 * qbF + 2 : isY ? NY : it ? 2 * pj + 2 : nx0;
    const u64 need2 = solo ? 0 : kvmask(0, (2 * pj + 1) >> 2) | (0xffffull << 32);
    u64 q[3] = {0, 0, 0};
    unsigned jmp[2] = {0, 0};
    if (isY) {
      p[0] = (solo && SWAP7 ? 0 : 1u << (14 - cx)) | (solo ? 0 : 1u << (n - 2));
      q[0] = kvmask(4, (31 - 2 * pj) >> 2) & ~own; jmp[0] = ex * 32768; q[1] = solo ? 0 : 1ull << 24;
      p[1] = 2; p[13] = t.v[T_FG]; p[14] = fo(g + 96 + r);
    } else if (it == 0) {
      p[0] = 2u | (ex ? 1u << (cx - 2) : 0);
      q[0] = kvmask(0, cx > 0 ? (cx - 1) >> 2 : 0);
      if (ex) { q[1] = kvmask(4, 4 + ((ex - 1) >> 2)); jmp[1] = (16 - cx) * 32768; q[2] = need2; } else q[1] = need2;
      p[1] = 2; p[13] = fo(2304 + pj * 16 + u) + j * (8 * 4352 - 8); p[14] = fo(g + 64 + hsel * 8 + pj);
    } else {
      p[0] = p[1] = solo ? 0 : 1;
      q[0] = need2;
      if (solo && SWAP7) { p[0] = 1u << 14; q[0] = kvmask(4, 4); } p[13] = fo(g + 48 + hsel * 8 + pj); p[14] = fo(g + 96 + r);
    }
    p[2] = 1u << (n - 2);
    p[3] = !last ? 0 : n > 2 ? 1u << (n - 3) : 1u;
    p[4] = !last ? 0 : n > 2 ? 3u << (n - 3) : 1u << 31;
    for (int k = 0; k < 3; ++k) { p[5 + 2 * k] = (unsigned)q[k]; p[6 + 2 * k] = (unsigned)(q[k] >> 32); }
    p[11] = jmp[0]; p[12] = jmp[1];
    p[15] = oK;
    p[16] = (last ? 1 : 0) | (isY && !solo ? 2 : 0) | (n == 2 ? 4 : 0) | (isY || solo ? 0 : 8) | (n - 1) << 8 | ((r >> 1) * 4 + wid) << 16;
    p[17] = wid * 32 - (n == 2 ? 64 : 0);
    p[18] = oQ + (pj * 128 + wid * 32) * 512;
    p[19] = 0;
    p[20] = (p[0] | (last ? 0 : 1u << n)) >> 1;
    p[21] = oK + (isY ? cx : 0) * 32768;
    p[22] = ((p[0] | p[1] | p[2] | p[3] | p[4]) & 0x7fffffffu) << 1;
  }
  return t;
}
template <int I> constexpr TRow t_base = I < 160 ? t_row(I / 5, I % 5, 0) : [] {
  TRow a = t_row(0, (I - 160) * 4, 0), b = t_row(0, (I - 160) * 4, 1);
  for (int l = 0; l < 64; ++l) a.v[l] = b.v[l] - a.v[l];
  return a;
}();
template <int I> constexpr bool t_linear = [] {
  for (int j = 1; j < 8; ++j) {
    const TRow b = t_row(I / 5, I % 5, j);
    for (int l = 0; l < 64; ++l) if (b.v[l] != t_base<I>.v[l] + j * t_base<160 + (I % 5) / 4>.v[l]) return false;
  }
  return true;
}();
struct Tab { TRow row[162]; };
template <int... I> constexpr Tab t_make(iseq<I...>) {
  static_assert((t_linear<(I < 160 ? I : 0)> && ...));
  return Tab{{t_base<I>...}};
}
__device__ const Tab g_tab = t_make(make_iseq<162>{});

typedef __attribute__((ext_vector_type(16))) unsigned u32x16;
template <class T, int I> DI T* arg_late() {
  unsigned lo, hi;
  asm volatile("v_readlane_b32 %0, v77, %2\n\tv_readlane_b32 %1, v77, %3\n\ts_nop 4" : "=s"(lo), "=s"(hi) : "i"(2 * I), "i"(2 * I + 1));
  return (T*)(((u64)hi << 32) | lo);
}
__global__ __launch_bounds__(256, 1) __attribute__((amdgpu_num_vgpr(77), amdgpu_num_sgpr(56))) void fused_kernel(
    const bf16* ckv_, const bf16* wuk_, const bf16* wuv_, const bf16* x_, const bf16* wq_, bf16* ws_, const bf16* ak_, const bf16* av_,
    const bf16* ash_, const bf16* assmk_, const bf16* assmD_, const bf16* wo_, const bf16* aq_, bf16* out_) {
  extern __shared__ __attribute__((aligned(16))) char lds[];
  const void* kap = (const void*)__builtin_amdgcn_kernarg_segment_ptr();
  u32x16 ka; u64 ka8;
  asm volatile("at_code_begin:\n\ts_load_dwordx16 %0, %2, 0\n\ts_load_dwordx2 %1, %2, 0x40" : "=&s"(ka), "=&s"(ka8) : "s"(kap) : ACLOB_ALL, "v255", "s101");
  GAP();
  const int tid = threadIdx.x, lane = tid & 63;
  const int wid = __builtin_amdgcn_readfirstlane(tid >> 6);
  const int b = blockIdx.x, j = b & 7, r = b >> 3, rr = r & 15, hsel = rr & 1, pj = rr >> 1, kv = pj & 1;
  const bool isY = r >= 16, solo = pj == 7, doP = isY != PREPX && !solo, doA = !isY && !solo;
  const int h = j + 8 * hsel, mt = (rr >> 2) + (isY ? 4 : 0), pu = (hsel * 8 + j) * 4 + wid;
  constexpr unsigned m0d = 131072 + 24576;
  const unsigned lA = isY ? 65536 : 0, lW = lA ^ 65536, po = isY ? 0 : 32768;
  const unsigned mp[4] = {lA + wid * 1024 + po, lW + wid * 1024 + po, lA + wid * 1024 + (po ^ 32768), lW + wid * 1024 + (po ^ 32768)};
  {
    const int lda = isY ? DA : DC;
    const unsigned ra = wid * 8 + (lane >> 3), sa = ((lane & 7) ^ (((wid & 1) * 4 + (lane >> 4)) & 7)) * 16;
    const unsigned vbA = ra * (lda * 2) + sa, vbB = (4 * wid + (lane >> 4)) * LDW * 2 + 16 * ((lane & 15) ^ (((lane >> 4) << 2) | wid));
    vsetu<92>(vbA); vsetu<93>(vbB); vsetu<90>(vbA + 128); vsetu<91>(vbB + 0x80000); vsetu<94>(ra * (DC * 2) + sa);
    static_for([&](auto J) {
      sset<64 + J>((J + 1) * 64 * lda);
      asm volatile("s_mov_b32 s%c0, %1" ::"i"(71 + J), "i"((((J + 1) >> 2) * 32 + 16 * ((J + 1) & 1)) * LDW * 2 + (((J + 1) >> 1) & 1) * 256));
    }, make_iseq<7>{});
    sset<78>(lA + wid * 1024); sset<79>(lW + wid * 1024);
    GAP();
  }
  asm volatile("s_waitcnt lgkmcnt(0)" : "+s"(ka), "+s"(ka8));
  auto arg = [&](int i) { return (const bf16*)(((u64)ka[2 * i + 1] << 32) | ka[2 * i]); };
  const bf16 *ckv = arg(0), *wuk = arg(1), *wuv = arg(2), *x = arg(3), *wq = arg(4), *ak = arg(6), *av = arg(7), *ash = (const bf16*)ka8;
  bf16* Qb = (bf16*)arg(5);
  KST(0, 0);
  bf16* xqT = (bf16*)((char*)Qb + O_X);
  const bf16* gA = ckv + (size_t)mt * 256 * DC;
  const bf16* gW = (kv ? wuv : wuk) + h * 256;
  unsigned dv = 0;
  {
    GM g;
    PR pr;
    if (doP) prep_load(pr, tid, pj, hsel * 8 + j, ak, av, ash);
    srd_set<56>(isY ? x + (size_t)pj * 256 * DA : gA, isY ? 256 * DA * 2 : 256 * DC * 2);
    srd_set<60>(isY ? wq + h * 256 : gW, isY ? (DA - 1) * LDW * 2 + 512 : (DC - 1) * LDW * 2 + 512);
    g_pro<0>(mp, make_iseq<12>{});
    srd_set<88>(Qb, WSB);
    {
      const unsigned* tp = g_tab.row[r * 5].v;
      asm volatile("buffer_load_dwordx2 v[120:121], off, s[88:91], %0 sc1\n\tglobal_load_dword v78, %1, %3\n\tglobal_load_dword v79, %2, %3 offset:1024\n\t"
                   "global_load_dword v118, %2, %4\n\tglobal_load_dword v119, %2, %4 offset:256\n\tglobal_load_dword v77, %5, %6"
                   ::"s"(fo(6144)), "v"(tid * 4), "v"(lane * 4), "s"(tp), "s"(g_tab.row[160].v), "v"((lane < 27 ? lane : 27) * 4), "s"(kap) : "memory");
    }
    if (doP) {
      asm volatile("s_waitcnt vmcnt(18)" ::: "memory");
#pragma unroll
      for (int q = 0; q < 7; ++q) asm volatile("" : "+v"(pr.kk[q]));
#pragma unroll
      for (int q = 0; q < 4; ++q) asm volatile("" : "+v"(pr.vv[q]));
      static_for([&](auto Ic) __attribute__((always_inline)) {
        constexpr int I = Ic;
        g_pro<12 + 4 * I>(mp, make_iseq<4>{});
        GAP();
        prep_math<I>(pr, tid, lds + 131072);
        GAP();
      }, make_iseq<4>{});
      asm volatile("s_waitcnt lgkmcnt(0)\n\ts_barrier" ::: "memory");
      prep_store(tid, pj, hsel * 8 + j, xqT, lds + 131072);
    } else g_pro<12>(mp, make_iseq<4>{});
    GAP();
    CODE_TOUCH("at_code_begin", "pf_code_end", r * 4 + wid, 128u, 8, m0d);
    {
      const int fr = lane & 15, fq = lane >> 4, q = (lane & 15) >> 2, p = lane & 3;
#pragma unroll
      for (int k2 = 0; k2 < 2; ++k2)
        g.aA[k2] = lA + (wid >> 1) * 16384 + (fr >> 3) * 1024 + (fr & 7) * 128 + (((4 * k2 + fq) ^ (fr >> 1)) * 16);
#pragma unroll
      for (int hf = 0; hf < 2; ++hf)
#pragma unroll
        for (int nf = 0; nf < 8; ++nf)
          g.aB[hf][nf] = lW + (wid & 1) * 8192 + off_b(8 * fq + 4 * hf + q, nf * 2 + (p >> 1)) + 8 * (p & 1);
      asm volatile("" : "+v"(g.aA[0]), "+v"(g.aA[1]));
#pragma unroll
      for (int nf = 0; nf < 8; ++nf) asm volatile("" : "+v"(g.aB[0][nf]), "+v"(g.aB[1][nf]));
      vsetu<81>(tid == 0 ? 0u : 0x80000000u);
      vsetu<84>(lane * 16); vsetu<85>(lane * 16 + 65536); vsetu<86>(wid * 8192 + lane * 16); vsetu<87>(wid * 8192 + lane * 16 + 4096);
      vsetu<88>(65536 + wid * 256 + fr * 16); vsetu<89>(fr - 4 * fq);
      asm volatile("s_mov_b64 s[92:93], s[88:89]\n\ts_mov_b64 s[94:95], s[90:91]");
    }
    if (doP) asm volatile("s_waitcnt vmcnt(13)" ::: "memory");
    else {
      g_pro<16>(mp, make_iseq<12>{});
      asm volatile("s_waitcnt vmcnt(8)" ::: "memory");
    }
    asm volatile("s_barrier" ::: "memory");
    if (isY) g_read0<0>(g, make_iseq<16>{});
    else g_read0<1>(g, make_iseq<16>{});
    GAP();
    {
      u64 z = (((u64)__float_as_uint(vget<121>()) << 32) | __float_as_uint(vget<120>())) + 0x9e3779b97f4a7c15ull;
      z = (z ^ (z >> 30)) * 0xbf58476d1ce4e5b9ull; z = (z ^ (z >> 27)) * 0x94d049bb133111ebull; z ^= z >> 31;
      vsetu<82>((unsigned)z); vsetu<83>((unsigned)(z >> 32));
      asm volatile("v_mad_u32_u24 v78, v118, %0, v78\n\tv_mad_u32_u24 v79, v119, %0, v79\n\tv_readfirstlane_b32 s84, v82\n\tv_readfirstlane_b32 s85, v83\n\t"
                   "v_readlane_b32 s80, v78, %1\n\tv_readlane_b32 s81, v78, %2" ::"s"(j), "i"((int)T_TAB), "i"(T_TAB + 1));
      if (isY) asm volatile("s_mov_b64 s[80:81], 0x6543210");
      asm volatile("s_bfe_u32 s82, s80, 0x40008\n\ts_lshr_b64 s[80:81], s[80:81], 12\n\tv_lshl_add_u32 v91, s82, 19, v93" ::: "scc");
    }
    asm volatile("s_waitcnt lgkmcnt(0)\n\ts_barrier" ::: "memory");
    g_pro<28>(mp, make_iseq<4>{});
    GAP();
    if (isY) {
      g_tile<false, 0, 1, NTW >= 1>(g);
      g_loop<false, true>(g, PREPX ? 0x20u : 0x22u, [&](int t) __attribute__((always_inline)) {
        if (t == 1) fl_set<T_FPREP>();
        else if (t == 4) { tload<80, T_TAB>(); tload<81, T_TAB + 1>(); srd_set<60>(gW, (DC - 1) * LDW * 2 + 512); }
        else {
          srd_set<56>(gA, 256 * DC * 2);
          asm volatile("v_mov_b32 v92, v94");
          static_for([&](auto J) { asm volatile("s_mov_b32 s%c0, %1" ::"i"(64 + J), "i"((J + 1) * 64 * DC)); }, make_iseq<7>{});
        }
      });
      tload<101, T_OQ>();
      asm volatile("v_mov_b32 v112, v84\n\tv_add_u32 v113, 8192, v84");
      GAP();
      g_tile<false, 0, 2, NTW >= 2>(g);
      KST(0, 3);
    }
#define TT 3
    auto kvtile = [&](auto TRc) __attribute__((always_inline)) {
      constexpr bool TR = TRc;
      if (isY) g_tile<TR, 1, 4, NTW >= 2>(g);
      else g_tile<TR, 1, 1, NTW >= 2>(g);
      g_loop<TR, false>(g, 0x2000u | (isY || doA ? 2u : 1u << TT), [&](int t) __attribute__((always_inline)) {
        int l = threadIdx.x & 63; asm volatile("" : "+v"(l));
        if (t == 13) {
          asm volatile("s_mov_b32 s79, 0x28000");
          fl_poll<T_NPRE>();
          if ((b & 15) == 0)
            asm volatile("v_readlane_b32 s48, v78, %0\n\tv_and_b32 v122, 0xffffff00, v82\n\tv_mov_b32 v123, v83\n\ts_nop 2\n\t"
                         "buffer_store_dwordx2 v[122:123], v81, s[88:91], s48 offen sc1" ::"i"((int)T_CNT) : "memory");
          if (!doA && !isY) {
            CODE_TOUCH("at_code_begin", "pf_code_end", hsel * 8 + wid * 2, 16u, 64, m0d);
            CODE_TOUCH("at_code_begin", "pf_code_end", hsel * 8 + wid * 2 + 1, 16u, 64, m0d);
          }
          if (doA) {
            const char* tp = arg_late<const char, 9>() + (size_t)(pj * 64 + pu) * (SEQ * 2) + (l & 31) * 128;
            asm volatile("s_mov_b32 m0, %1\n\ts_nop 0\n\tglobal_load_lds_dword %0, off" ::"v"(tp), "i"(m0d) : "memory");
          }
        } else if (isY) fl_set<T_FQ>();
        else if (doA) { if (PREPX) fl_set<T_FPREP>(); post_dma(pj, pu, arg_late<const bf16, 12>(), 131072 + wid * 8192); }
        else {
          const char* tp = arg_late<const char, 0>() + 1024 * DC * 2 + (pu * 256 + l) * 128;
          asm volatile("s_mov_b32 m0, %1\n\ts_nop 0\n\tglobal_load_lds_dword %0, off" ::"v"(tp), "i"(m0d) : "memory");
          asm volatile("global_load_lds_dword %0, off" ::"v"(tp + 8192) : "memory");
          asm volatile("global_load_lds_dword %0, off" ::"v"(tp + 16384) : "memory");
          asm volatile("global_load_lds_dword %0, off" ::"v"(tp + 24576) : "memory");
        }
      });
      tload<101, T_OG>(); tload<96, T_Q0>(); tload<98, T_K0>();
      fl_check();
      tload<50, T_NST>(); tload<51, T_NST + 1>();
      asm volatile("v_mov_b32 v112, v84\n\tv_add_u32 v113, %0, v84\n\tv_mov_b32 v90, v87\n\tv_mov_b32 v91, v87\n\tv_mov_b32 v92, v87\n\tv_mov_b32 v93, v87\n\t"
                   "v_mov_b32 v94, v86\n\tv_mov_b32 v95, v86\n\tv_mov_b32 v126, v86\n\tv_mov_b32 v127, v86\n\ts_mov_b32 s99, 0x8000\n\t" FL_ARM("s[50:51]")
                   ::"i"(TR ? 4096 : 8192));
      if (doA) {
        const int d = pj * 64 + pu;
        const bf16* dp = arg_late<const bf16, 10>() + d;
        asm volatile("global_load_ushort %0, %1, off" : "=v"(dv) : "v"(dp) : "memory");
        srd_set<56>(arg_late<const bf16, 9>() + (size_t)d * SEQ, SEQ * 2);
        srd_set<60>(arg_late<const char, 5>() + O_X + d * SEQ * 2, SEQ * 2);
        sset<78>(65536 + wid * 15488);
        asm volatile("s_movk_i32 s64, 992\n\tv_subrev_u32 v122, 32, v84");
        GAP();
        g_tile<TR, 0, 3>(g);
      } else {
        sset<79>(isY ? wid * 8192 : 0x30000);
        if ((NYT & 1) && !solo) asm volatile("s_add_u32 s98, s98, 0x8000\n\ts_mov_b32 s97, 0xffff8000" ::: "scc");
        else asm volatile("s_mov_b32 s97, 0x8000");
        GAP();
        g_tile<TR, 0, 5>(g);
      }
    };
    if (kv) kvtile(std::true_type{});
    else kvtile(std::false_type{});
  }
  KST(0, 4);
  asm volatile("s_waitcnt vmcnt(16)" : "+v"(dv) :: "memory");
  sset<79>(wid * 8192);
  if (doA) {
    const float Dd = bf2f(dv);
    KST(0, 5);
    conv_wave<T_FG>(O_Z + (pj * 64 + pu) * SEQ * 2, Dd, lds + 65536 + wid * 15488);
    const char* tp = arg_late<const char, 11>() + (size_t)h * (HD * DA * 2) + ((pj * 4 + wid) * 64 + lane) * 128;
    asm volatile("s_mov_b32 m0, %1\n\ts_nop 0\n\tglobal_load_lds_dword %0, off\n\ts_waitcnt vmcnt(17)" ::"v"(tp), "i"(m0d) : "memory");
    KST(0, 6);
  } else if (!isY) {
    static_for([&](auto I) { d_store<16 + I>(); }, make_iseq<16>{});
    tload<98, T_K0>();
    asm volatile("s_waitcnt vmcnt(0)\n\ts_barrier" ::: "memory");
    fl_set<T_FG>();
    KST(0, 5);
    fl_check();
    CODE_TOUCH("att_begin", "pf_code_end", hsel * 4 + wid, 8u, 64, m0d);
    dma4k<0>(); dma4k<1>(); dma4k<2>(); dma4k<3>();
    asm volatile("s_waitcnt vmcnt(0)" ::: "memory");
    KST(0, 6);
  } else {
    tload<76, T_PART>();
    fl_check();
    asm volatile("buffer_load_dwordx2 v[122:123], v88, s[88:91], s76 offen offset:1024 sc1" ::: "memory");
  }
  AC c;
  sset<78>(65536 + wid * 8192); sset<79>(98304 + wid * 8192);
  { const bf16* wp = arg_late<const bf16, 11>() + (size_t)h * HD * DA; asm volatile("s_mov_b64 s[80:81], %0" ::"s"(wp)); }
  asm volatile("att_begin:");
  ev_code();
  asm volatile("s_waitcnt lgkmcnt(0)\n\ts_barrier" ::: "memory");
#pragma unroll 1
  for (int it = doA ? 0 : 1; it < 2; ++it) {
    const bool odd = (NYT & 1) && !solo && (isY || it == 0);
    if (odd) kreads<1>(make_iseq<8>{});
    else kreads<0>(make_iseq<8>{});
    const unsigned oP = tget<T_PART>();
    if (it == 0) {
      static_for([&](auto I) { tload<57 + I, T_IT + I>(); }, make_iseq<21>{});
      tload<S_T, T_IT + 21>(); tload<S_EV, T_IT + 22>();
      asm volatile("s_sub_u32 s70, s70, %0\n\ts_add_u32 s76, %1, 1024" ::"s"(j * (8 * 4352 - 8)), "s"(oP) : "scc");
    } else {
      static_for([&](auto I) { tload<57 + I, T_IT + T_ITN + I>(); }, make_iseq<21>{});
      tload<S_T, T_IT + T_ITN + 21>(); tload<S_EV, T_IT + T_ITN + 22>();
      asm volatile("s_mov_b32 s72, 0\n\ts_mov_b32 s75, %0\n\ts_mov_b32 s76, %1" ::"s"(oP + wid * 16384), "s"(oP + 1032));
    }
    unsigned mode;
    asm volatile("s_add_u32 s98, s97, 0x8000\n\ts_mov_b32 %0, s73\n\ts_mov_b32 s56, -1\n\tv_mov_b64 v[114:115], 0\n\tv_mov_b64 v[118:119], 0\n\t"
                 "v_mov_b32 v120, 0x45800000" : "=s"(mode) :: "scc");
    KST(0, 8 + 3 * (it && !isY));
    GAP();
#define STA(k)
#define STI()
    STA(0);
    if (odd) {
      if (isY) phaseA<1, 2>(c);
      else phaseA<1, 1>(c);
      STA(1);
      STA(2);
      phaseB0<1>(mode, wid);
      STA(3);
      asm volatile("s_bfe_u32 s76, s73, 0x80008\n\ts_branch att_mid" ::: "scc");
    } else {
      if (isY) phaseA<0, 2>(c);
      else phaseA<0, 1>(c);
      STA(1);
      if (c.rare) ev_call();
      STA(2);
      phaseB0<0>(mode, wid);
      if (__builtin_expect(mode & 4, 0)) { asm volatile("s_mov_b32 s56, 31"); ev_call(); asm volatile("s_mov_b32 s56, 0"); }
      STA(3);
    }
    GAP();
    asm volatile("s_bfe_u32 s76, s73, 0x80008\natt_loop:" ::: "scc");
    GAP();
    phaseA<1, 0>(c);
    STI();
    if (__builtin_expect(c.rare != 0, 0)) { if (redo()) sm_redo<1>(); ev_call(); }
    phaseB<1>(c);
    asm volatile("s_cbranch_scc1 att_done\natt_mid:");
    phaseA<0, 0>(c);
    STI();
    if (__builtin_expect(c.rare != 0, 0)) { if (redo()) sm_redo<0>(); ev_call(); }
    phaseB<0>(c);
    asm volatile("s_branch att_loop\natt_done:");
    GAP();
    STA(4);
    if ((mode & 3) == 1) phaseA<0, 4>(c);
    else {
      asm volatile("buffer_load_dwordx4 v[128:131], v88, s[88:91], %0 offen sc1" ::"s"(oP) : "memory");
      GAP();
      phaseA<0, 3>(c);
    }
    if (__builtin_expect(redo(), 0)) sm_redo<0>();
    STA(5);
    KST(0, 9 + 3 * (it && !isY));
    const float l[2] = {row_reduce<false>(vget<R_RS>() + vget<R_TS>()), row_reduce<false>(vget<R_RS + 1>() + vget<R_TS + 1>())};
    const float cm[2] = {vget<R_M>(), vget<R_M + 1>()};
    EP p;
    if (it == 0) {
      const f32x4 ml = {cm[0], l[0], cm[1], l[1]};
      asm volatile("buffer_store_dwordx4 %0, v88, s[88:91], %1 offen sc1\n\ts_mov_b32 s101, %2" ::"v"(ml), "s"(oP), "s"(oP + wid * 16384) : "memory");
      phaseF<0>(c, p, make_iseq<74>{});
      asm volatile(FL_POLL "\n\ts_waitcnt vmcnt(17)" ::: "memory");
    } else {
      int lz = threadIdx.x & 63; asm volatile("" : "+v"(lz));
      const int rw = lz >> 2;
      static_for([&](auto E) { vsetu<R_TB + E>(rw * 128 + 16 * ((2 * E + ((lz & 3) >> 1)) ^ (rw & 6)) + 8 * (lz & 1)); }, make_iseq<4>{});
      const unsigned vs = ((lz >> 3) & 1) * 16384 + ((lz >> 4) & 1) * 512 + (lz >> 5) * 256 + (lz & 7) * 16;
      vsetu<R_VS>(vs); vsetu<R_VS + 1>(vs + 32768);
      tload<96, T_SLAB>();
      asm volatile(TICKET ::"i"((int)T_CNT) : "memory");
      asm volatile("s_mov_b32 s97, s79\n\ts_mov_b32 s100, 0x8000\n\ts_movk_i32 s99, 0x1000");
      if (!(mode & 2)) {
        p.fa[0] = __builtin_amdgcn_rcpf(l[0]); p.fa[1] = __builtin_amdgcn_rcpf(l[1]);
        phaseF<1>(c, p, make_iseq<74>{});
      } else {
        const f32x4 ml = {vget<128>(), vget<129>(), vget<130>(), vget<131>()};
        if (__builtin_expect(__builtin_amdgcn_ballot_w64(ml[0] != cm[0] || ml[2] != cm[1]) == 0, 1)) {
          p.fa[0] = __builtin_amdgcn_rcpf(l[0] + ml[1]); p.fa[1] = __builtin_amdgcn_rcpf(l[1] + ml[3]);
          const unsigned one = (lz >> 4) != ((lz >> 2) & 3) ? 0u : (lz & 1) ? 0x3f800000u : 0x3f80u, o0 = (lz & 2) ? 0u : one, o1 = (lz & 2) ? one : 0u;
          vsetu<224>(o0); vsetu<225>(o1); vsetu<230>(o0); vsetu<231>(o1);
          asm volatile("v_mov_b64 v[226:227], 0\n\tv_mov_b64 v[228:229], 0");
          phaseF<3>(c, p, make_iseq<74>{});
        } else {
#pragma unroll
          for (int e = 0; e < 2; ++e) {
            const float mm = fmaxf(cm[e], ml[2 * e]);
            const float wa = __builtin_amdgcn_exp2f(cm[e] - mm), wb = __builtin_amdgcn_exp2f(ml[2 * e] - mm);
            const float inv = __builtin_amdgcn_rcpf(l[e] * wa + ml[2 * e + 1] * wb);
            p.fa[e] = wa * inv; p.fb[e] = wb * inv;
          }
          phaseF<2>(c, p, make_iseq<74>{});
        }
      }
    }
    KST(0, 10 + 3 * (it && !isY));
  }
  asm volatile("v_xor_b32 v%c0, s100, v%c0\np_loop:" ::"i"(R_TB));
  GAP();
  p_half<0>(make_iseq<32>{});
  p_half<1>(make_iseq<32>{});
  asm volatile("s_cbranch_scc1 p_loop\n\ts_nop 5");
  GAP();
  p_cvt<1, 0>(); p_cvt<1, 1>(); p_cvt<1, 2>(); p_cvt<1, 3>();
  p_store<1, 1>();
  GAP();
  KST(0, 27);
  {
    constexpr unsigned rdy = 98304;
    int lz = threadIdx.x & 63; asm volatile("" : "+v"(lz));
    const int fr = lz & 15, fq = lz >> 4, qbF = isY != (solo && SWAP7) ? 15 - pj : pj;
    if (wid == 0) {
#pragma unroll 1
      for (int q = 0; q < 0x100000; ++q) {
        unsigned lo, hi, tl, th;
        asm volatile("v_readfirstlane_b32 %0, v122\n\tv_readfirstlane_b32 %1, v123\n\ts_mov_b32 %2, s84\n\ts_mov_b32 %3, s85" : "=s"(lo), "=s"(hi), "=s"(tl), "=s"(th));
        if (__builtin_expect((((lo ^ tl) >> 8) | (hi ^ th)) == 0, 1)) break;
        asm volatile("s_sleep 4\n\t" TICKET "\n\ts_waitcnt vmcnt(0)" ::"i"((int)T_CNT) : "memory");
      }
      asm volatile("v_mov_b32 v123, 0\n\ts_mov_b64 exec, 1\n\tds_write_b64 %0, v[122:123]\n\ts_mov_b64 exec, -1" ::"v"(rdy) : "memory");
    }
    vsetu<79>(fo(1024) + qbF * 4352 + fr * 8);
    {
      const int jj = 4 * (fr >> 3) + (fr & 3);
      const unsigned one = ((fq ^ (fr >> 2)) & 1) ? 0u : (jj & 1) ? 0x3f800000u : 0x3f80u;
      static_for([&](auto I) { vsetu<96 + I>((jj >> 1) == I ? one : 0u); }, make_iseq<4>{});
    }
    unsigned vo = (fq >> 1) * 1024 + (fq & 1) * 256 + fr * 16;
    bf16* out = arg_late<bf16, 13>();
    __amdgpu_buffer_rsrc_t so_ = __builtin_amdgcn_make_buffer_rsrc((void*)out, (short)0, SEQ * OUTW * 2, 0x00020000);
    unsigned vw = ((qbF * 128 + (fr & 7)) * OUTW + DA + (fr >> 3) * 32 + fq * 8) * 2;
    asm volatile("" : "+s"(so_));
    asm volatile("s_waitcnt vmcnt(0) lgkmcnt(0)\n\ts_barrier" : "+v"(vo), "+v"(vw) :: "memory");
    fl_set<T_FS>();
    unsigned tk;
    asm volatile("ds_read_b32 %0, %1\n\ts_waitcnt lgkmcnt(0)" : "=v"(tk) : "v"(rdy) : "memory");
    tk = __builtin_amdgcn_readfirstlane(tk) & 255;
    KST(0, 30);
    if (doA) post_math(pj, pu, out, lds + 131072 + wid * 8192);
#define N3 0
#define N2 12
    constexpr int N1 = 28 - 3 * N3 - 2 * N2;
    static_assert(N1 >= 0 && N3 + N2 + N1 <= 16);
    const int nu = tk < N3 ? 3 : tk < N3 + N2 ? 2 : tk < N3 + N2 + N1 ? 1 : 0;
    const int u0 = 4 * (tk < N3 ? 3 * tk : tk < N3 + N2 ? N3 + 2 * tk : 2 * N3 + N2 + tk) + wid * nu;
    KST(0, 28);
    if (nu) {
      unsigned so[3];
#pragma unroll
      for (int k = 0; k < 3; ++k) so[k] = O_S + qbF * 0x200000 + 0x40000 + (u0 + k) * 16384;
      unsigned pend = 0x5555u, cnt = 0x400000;
      while (true) {
        unsigned nw;
        if (wid == 3) {
          u64 m;
          const u64 ex = pend | (pend << 1);
          asm volatile(FL_ARM("%1") "\n\t" FL_POLL "\n\ts_waitcnt vmcnt(0)\n\tv_cmp_eq_u64 %0, s[84:85], v[124:125]" : "=s"(m) : "s"(ex) : "memory");
          nw = (unsigned)m & ((unsigned)m >> 1) & pend;
          if (nw) asm volatile("ds_write_b32 %0, %1 offset:4" ::"v"(rdy), "v"(~pend | nw) : "memory");
        } else {
          unsigned t;
          asm volatile("ds_read_b32 %0, %1 offset:4\n\ts_waitcnt lgkmcnt(0)" : "=v"(t) : "v"(rdy) : "memory");
          nw = __builtin_amdgcn_readfirstlane(t) & pend;
        }
        if (--cnt == 0) nw = pend;
        if (nw == 0) {
          if (wid == 3) asm volatile("s_sleep 4");
          else asm volatile("s_sleep 1");
          continue;
        }
        asm volatile("s_nop 4");
        static_for([&](auto S) {
          if ((nw >> (2 * S)) & 1) {
            r_load<0, 2 * S>(vo, so[0]); r_load<0, 2 * S + 1>(vo, so[0]);
            if (nu > 1) { r_load<1, 2 * S>(vo, so[1]); r_load<1, 2 * S + 1>(vo, so[1]); }
            if (nu > 2) { r_load<2, 2 * S>(vo, so[2]); r_load<2, 2 * S + 1>(vo, so[2]); }
          }
        }, make_iseq<8>{});
        pend &= ~nw;
        if (pend == 0) break;
      }
      asm volatile("s_waitcnt vmcnt(0)" ::: "memory");
      KST(0, 16);
      auto oo = [&](int U) { return ((U & 15) * 8 * OUTW + (U >> 4) * 64) * 2; };
      r_fin<0>(vw, so_, oo(u0));
      if (nu > 1) r_fin<1>(vw, so_, oo(u0 + 1));
      if (nu > 2) r_fin<2>(vw, so_, oo(u0 + 2));
      if (qbF == 15 && tk == 0) asm volatile(FL_SET("%0") ::"s"(fo(6144)) : "memory");
    }
    KST(0, 29);
  }
  asm volatile("s_waitcnt vmcnt(0)" ::: "memory");
  asm volatile("pf_code_end:");
  KST(0, 1);
}

extern "C" void kernel_launch(void* const* d_in, const int* in_sizes, int n_in, void* d_out, int out_size, void* d_ws,
                              size_t ws_size, hipStream_t stream) {
  const bf16* Aq = (const bf16*)d_in[0];
  const bf16* Ak = (const bf16*)d_in[1];
  const bf16* Av = (const bf16*)d_in[2];
  const bf16* Ash = (const bf16*)d_in[3];
  const bf16* Assmk = (const bf16*)d_in[4];
  const bf16* AssmD = (const bf16*)d_in[5];
  const bf16* Bx = (const bf16*)d_in[6];
  const bf16* Bckv = (const bf16*)d_in[7];
  const bf16* Bwq = (const bf16*)d_in[8];
  const bf16* Bwuk = (const bf16*)d_in[9];
  const bf16* Bwuv = (const bf16*)d_in[10];
  const bf16* Bwo = (const bf16*)d_in[11];
  bf16* out = (bf16*)d_out;
  hipFuncSetAttribute((const void*)fused_kernel, hipFuncAttributeMaxDynamicSharedMemorySize, 163840);
  fused_kernel<<<256, 256, 163840, stream>>>(Bckv, Bwuk, Bwuv, Bx, Bwq, (bf16*)d_ws, Ak, Av, Ash, Assmk, AssmD, Bwo, Aq, out);
}
```

```cpp
#include <hip/hip_runtime.h>
#include <hip/hip_bf16.h>
#include <stdint.h>
#include <utility>

typedef __hip_bfloat16 bf16;
typedef __attribute__((ext_vector_type(4))) short bf16x4;
typedef __attribute__((ext_vector_type(2))) unsigned u32x2;

constexpr int SEQ = 2048;
constexpr int DA = 448;
constexpr int NH = 16;
constexpr int HD = 256;
constexpr int DC = 1024;
constexpr int OUTW = 896;
#define OUTSC 17
#define OUTSCS " sc0 sc1"
#define NTPS " nt"
#define NTVS " nt"
#define NTQS ""
#define NTW 1
#define NYT 15
#define SWAP7 1
constexpr unsigned TSB = NH * SEQ * HD * 2, O_K = TSB, O_S = 3 * TSB, O_P = 5 * TSB, PSTR = 67584, O_F = O_P + 128 * PSTR,
                   O_X = O_F + 400 * 4352, O_Z = O_X + DA * SEQ * 2, WSB = O_Z + DA * SEQ * 2;
constexpr unsigned fo(int w) { return O_F + (w >> 4) * 4352 + (w & 15) * 8; }

#define KST(kid, which)

__device__ __forceinline__ float bf2f(unsigned short u) { return __uint_as_float(((unsigned)u) << 16); }
__device__ __forceinline__ unsigned short f2bf(float f) { return __bfloat16_as_ushort(__float2bfloat16(f)); }
__device__ __forceinline__ float rbf(float f) { return bf2f(f2bf(f)); }
typedef __attribute__((ext_vector_type(2))) __bf16 bf16x2n;
typedef __attribute__((ext_vector_type(2))) float f32x2n;
__device__ __forceinline__ unsigned pk2(float a, float b) {
  return __builtin_bit_cast(unsigned, __builtin_convertvector((f32x2n){a, b}, bf16x2n));
}

__device__ __forceinline__ int off_b(int row, int ch) { return 256 * row + 16 * (ch ^ (((row & 3) << 2) | ((row >> 2) & 3))); }

template <int R> __device__ __forceinline__ void sset(unsigned x) { asm volatile("s_mov_b32 s%c0, %1" ::"i"(R), "s"(x)); }
template <int R> __device__ __forceinline__ void vsetu(unsigned x) { asm volatile("v_mov_b32 v%c0, %1" ::"i"(R), "v"(x)); }
template <int R> __device__ __forceinline__ void srd_set(const void* p, unsigned n) {
  asm volatile("s_mov_b64 s[%c0:%c1], %4\n\ts_mov_b32 s%c2, %5\n\ts_mov_b32 s%c3, 0x20000" ::"i"(R), "i"(R + 1), "i"(R + 2), "i"(R + 3), "s"(p), "s"(n));
}
template <int R, int L> __device__ __forceinline__ void tload() { asm volatile("v_readlane_b32 s%c0, v78, %1" ::"i"(R), "i"(L)); }
template <int L> __device__ __forceinline__ unsigned tget() { unsigned t; asm volatile("v_readlane_b32 %0, v78, %1" : "=s"(t) : "i"(L)); return t; }
#define FL_SET(S) "buffer_store_dwordx2 v[82:83], v81, s[88:91], " S " offen sc1"
#define FL_ARM(N) "v_cndmask_b32_e64 v80, -2.0, v79, " N
#define FL_POLL "buffer_load_dwordx2 v[124:125], v80, s[88:91], 0 offen sc1"
#define FL_CHECK(N) "s_mov_b32 s48, 0x100000\n7:\n\tv_cmp_eq_u64 vcc, s[84:85], v[124:125]\n\ts_and_b64 vcc, vcc, " N "\n\ts_cmp_eq_u64 vcc, " N \
                    "\n\ts_cbranch_scc1 8f\n\ts_sleep 2\n\t" FL_POLL "\n\ts_waitcnt vmcnt(0)\n\ts_sub_u32 s48, s48, 1\n\ts_cbranch_scc0 7b\n8:"
template <int L> __device__ __forceinline__ void fl_set() {
  asm volatile("v_readlane_b32 s48, v78, %0\n\ts_nop 4\n\t" FL_SET("s48") ::"i"(L) : "memory");
}
template <int L> __device__ __forceinline__ void fl_poll() {
  asm volatile("v_readlane_b32 s50, v78, %0\n\tv_readlane_b32 s51, v78, %1\n\ts_nop 1\n\t" FL_ARM("s[50:51]") "\n\t" FL_POLL ::"i"(L), "i"(L + 1) : "memory");
}
#define TICKET "v_readlane_b32 s48, v78, %0\n\tv_mov_b32 v122, 1\n\tv_mov_b32 v123, 0\n\ts_nop 2\n\tbuffer_atomic_add_x2 v[122:123], v81, s[88:91], s48 offen sc0"
__device__ __forceinline__ void fl_check() { asm volatile(FL_CHECK("s[50:51]") ::: "vcc", "scc", "memory"); }
template <int R> __device__ __forceinline__ float vget() { float t; asm volatile("v_mov_b32 %0, v%c1" : "=v"(t) : "i"(R)); return t; }
template <int R> __device__ __forceinline__ void vset(float x) { asm volatile("v_mov_b32 v%c0, %1" ::"i"(R), "v"(x)); }
template <int R> __device__ __forceinline__ float acc_read1() { float t; asm volatile("v_accvgpr_read_b32 %0, a[%c1]" : "=v"(t) : "i"(R)); return t; }
template <class F, int... I> __device__ __forceinline__ void static_for(F&& f, std::integer_sequence<int, I...>) {
  (f(std::integral_constant<int, I>{}), ...);
}
template <int... Is> using iseq = std::integer_sequence<int, Is...>;
template <int N> using make_iseq = std::make_integer_sequence<int, N>;

typedef __attribute__((ext_vector_type(4))) unsigned u32x4c;
typedef __attribute__((ext_vector_type(4))) float f32x4c;
typedef __attribute__((ext_vector_type(2))) float f32x2c;
typedef unsigned long long u64;
struct PR { u32x4c kk[7], vv[4]; };
__device__ __forceinline__ void prep_load(PR& p, int tid, int cg, int tb, const bf16* __restrict__ k, const bf16* __restrict__ v,
                                          const bf16* __restrict__ shift_k) {
  const __amdgpu_buffer_rsrc_t sk = __builtin_amdgcn_make_buffer_rsrc((void*)k, (short)0, SEQ * DA * 2, 0x00020000);
  const __amdgpu_buffer_rsrc_t sv = __builtin_amdgcn_make_buffer_rsrc((void*)v, (short)0, SEQ * DA * 2, 0x00020000);
  const __amdgpu_buffer_rsrc_t sw = __builtin_amdgcn_make_buffer_rsrc((void*)(shift_k + cg * 256), (short)0, 512, 0x00020000);
  const int vo = ((tb * 128 + (tid >> 3) * 4) * DA + cg * 64 + (tid & 7) * 8) * 2;
#pragma unroll
  for (int q = 0; q < 7; ++q)
    asm volatile("buffer_load_dwordx4 %0, %1, %2, 0 offen" NTPS : "=v"(p.kk[q]) : "v"(vo + (q - 3) * DA * 2), "s"(sk) : "memory");
#pragma unroll
  for (int q = 0; q < 4; ++q)
    asm volatile("buffer_load_dwordx4 %0, %1, %2, 0 offen offset:%3" NTVS : "=v"(p.vv[q]) : "v"(vo), "s"(sv), "i"(q * DA * 2) : "memory");
#pragma unroll
  for (int q = 0; q < 4; ++q)
    asm volatile("buffer_load_dwordx4 a[%c0:%c1], %2, %3, 0 offen offset:%4" ::"i"(4 * q), "i"(4 * q + 3), "v"((tid & 7) * 64), "s"(sw), "i"(q * 16) : "memory");
}
__device__ __forceinline__ float bfl(unsigned u) { return __uint_as_float(u << 16); }
__device__ __forceinline__ float bfh(unsigned u) { return __uint_as_float(u & 0xffff0000u); }
__device__ __forceinline__ f32x2c rbf2(f32x2c a) {
  return (f32x2c){__uint_as_float(pk2(0.f, a[0])), __uint_as_float(pk2(0.f, a[1]))};
}
template <int I> __device__ __forceinline__ void prep_math(const PR& p, int tid, char* lx) {
  const int rp = tid >> 3, c = tid & 7;
  f32x2c wt[4], kf[7];
  const unsigned w[4] = {__float_as_uint(acc_read1<4 * I>()), __float_as_uint(acc_read1<4 * I + 1>()), __float_as_uint(acc_read1<4 * I + 2>()),
                         __float_as_uint(acc_read1<4 * I + 3>())};
#pragma unroll
  for (int t = 0; t < 4; ++t)
    wt[t] = (t & 1) ? (f32x2c){bfh(w[t >> 1]), bfh(w[2 + (t >> 1)])} : (f32x2c){bfl(w[t >> 1]), bfl(w[2 + (t >> 1)])};
#pragma unroll
  for (int q = 0; q < 7; ++q) kf[q] = (f32x2c){bfl(p.kk[q][I]), bfh(p.kk[q][I])};
  f32x2c x[4];
#pragma unroll
  for (int e = 0; e < 4; ++e) {
    f32x2c s = rbf2(kf[e] * wt[3]);
    s = rbf2(s + rbf2(kf[e + 1] * wt[2]));
    s = rbf2(s + rbf2(kf[e + 2] * wt[1]));
    s = rbf2(s + rbf2(kf[e + 3] * wt[0]));
    x[e] = s * (f32x2c){bfl(p.vv[e][I]), bfh(p.vv[e][I])};
  }
#pragma unroll
  for (int u = 0; u < 2; ++u)
    *(u32x2*)(lx + (c * 8 + 2 * I + u) * 256 + (((31 - rp) * 8) ^ (c << 4))) = (u32x2){pk2(x[3][u], x[2][u]), pk2(x[1][u], x[0][u])};
}
__device__ __forceinline__ void prep_store(int tid, int cg, int tb, bf16* __restrict__ xr, const char* lx) {
  const __amdgpu_buffer_rsrc_t sx = __builtin_amdgcn_make_buffer_rsrc((void*)(xr + (size_t)cg * 64 * SEQ), (short)0, 64 * SEQ * 2, 0x00020000);
#pragma unroll
  for (int rd = 0; rd < 4; ++rd) {
    const int idx = tid + 256 * rd, ch = idx >> 4, q = idx & 15;
    u32x4c w = *(const u32x4c*)(lx + ch * 256 + 16 * (q ^ (ch >> 3)));
    __builtin_amdgcn_raw_buffer_store_b128(w, sx, (ch * SEQ + (15 - tb) * 128 + q * 8) * 2, 0, 16);
  }
}

#define CODE_TOUCH(LB, LE, wx, NW, NL, m0v) do { unsigned blo_, bhi_, elo_; \
    asm volatile("s_getpc_b64 s[48:49]\n\ts_add_u32 s48, s48, " LB "@rel32@lo+4\n\ts_addc_u32 s49, s49, " LB "@rel32@hi+12\n\t" \
                 "s_mov_b32 %0, s48\n\ts_mov_b32 %1, s49\n\t" \
                 "s_getpc_b64 s[48:49]\n\ts_add_u32 s48, s48, " LE "@rel32@lo+4\n\ts_mov_b32 %2, s48" \
                 : "=s"(blo_), "=s"(bhi_), "=s"(elo_) :: "scc"); \
    const unsigned n_ = elo_ - (blo_ & ~127u); \
    const __amdgpu_buffer_rsrc_t srd_ = __builtin_amdgcn_make_buffer_rsrc((void*)(((unsigned long long)bhi_ << 32) | (blo_ & ~127u)), (short)0, n_ < 262144u ? n_ : 0u, 0x00020000); \
    const unsigned l_ = threadIdx.x & 63, vo_ = l_ < (NL) ? ((wx) + l_ * (NW)) * 128u : 0x80000000u; \
    asm volatile("s_mov_b32 m0, %0\n\ts_nop 4\n\tbuffer_load_dword %1, %2, 0 offen lds" ::"s"(m0v), "v"(vo_), "s"(srd_) : "memory"); } while (0)
template <int I> __device__ __forceinline__ void d_store() {
  asm volatile("buffer_store_dwordx4 v[%c0:%c1], v%c2, s[88:91], s101 offen offset:%3 sc1" ::"i"(128 + 4 * I), "i"(131 + 4 * I), "i"(112 + ((I >> 2) & 1)),
               "i"((I & 3) * 1024) : "memory");
  if constexpr ((I & 7) == 7) asm volatile("s_addk_i32 s101, 0x4000" ::: "scc");
}
constexpr int NS_X = 19;
struct CV { unsigned pa[4], pb, px, vz, sz; float D; };
constexpr int cv_J(bool up, int q) { int J = up ? 0 : 7; while (q > J) { q -= J + 1; J += up ? 1 : -1; } return J; }
constexpr int cv_i(bool up, int q) { int J = up ? 0 : 7; while (q > J) { q -= J + 1; J += up ? 1 : -1; } return q; }
template <int R, int N> __device__ __forceinline__ void cv_rd(CV& c) {
  if constexpr (N < 16) {
    constexpr int i = N >> 1, o = 16 * R + 128 * (i & 1) + 2 * (N & 1), r = 32 + 32 * (R & 1) + 2 * N;
    asm volatile("ds_read2_b32 a[%c1:%c2], %0 offset0:%c3 offset1:%c4" ::"v"(c.pa[i >> 1]), "i"(r), "i"(r + 1), "i"(o), "i"(o + 1));
  } else {
    constexpr int m = N - 16, r = 192 + 32 * (R & 1) + 4 * m;
    asm volatile("ds_read_b128 a[%c1:%c2], %0 offset:%c3" ::"v"(c.pb), "i"(r), "i"(r + 3), "i"(64 * R + 512 * (7 - m)));
  }
}
template <int O> __device__ __forceinline__ void cv_xd(CV& c) {
  constexpr int e = O & 31, x = 96 + ((e >> 1) ^ 1);
  if constexpr (O >= 64) asm volatile("v_cvt_pk_bf16_f32 v%c0, 0, v%c0" ::"i"(160 + e));
  else if constexpr (O >= 32) asm volatile("v_mul_f32 v%c0, v%c0, %1" ::"i"(160 + e), "v"(c.D));
  else if constexpr (O & 1) asm volatile("v_lshlrev_b32 v%c0, 16, v%c1" ::"i"(160 + e), "i"(x));
  else asm volatile("v_and_b32 v%c0, 0xffff0000, v%c1" ::"i"(160 + e), "i"(x));
}
template <int O> __device__ __forceinline__ void cv_z(CV& c) {
  constexpr int J = O / 11, k = O % 11, y = 128 + 4 * J;
  if constexpr (k < 4) asm volatile("v_cvt_pk_bf16_f32 v%c0, 0, v%c0" ::"i"(y + k));
  else if constexpr (k < 8) asm volatile("v_add_f32 v%c0, v%c0, v%c1" ::"i"(y + k - 4), "i"(y + 28 + k));
  else if constexpr (k < 10) asm volatile("v_cvt_pk_bf16_f32 v%c0, v%c1, v%c2" ::"i"(y + k - 8), "i"(y + 2 * k - 16), "i"(y + 2 * k - 15));
  else asm volatile("buffer_store_dwordx2 v[%c0:%c1], %2, s[88:91], %3 offen offset:%4 sc1" ::"i"(y), "i"(y + 1), "v"(c.vz), "s"(c.sz),
                    "i"(512 * J) : "memory");
}
constexpr int cv_z0(int Q) {
  int o = 0;
  for (int q = 0; q < Q; ++q)
    for (int k = 0; k < (q % 4 == 1 ? 0 : 3); ++k) { const int J = o / 11; if ((J + 1) * (J + 2) / 2 - 1 + (J ? 4 : 7) <= q) ++o; }
  return o;
}
constexpr int cv_xd0(int T) { int o = 0; for (int t = 0; t < T; ++t) o += t % 4 != 1 || (t >= 72 && t < 108); return o < 96 ? o : 96; }
static_assert(cv_xd0(144) == 96);
template <int B, int... Is> __device__ __forceinline__ void cv_zs(CV& c, iseq<Is...>) { (cv_z<B + Is>(c), ...); }
template <int R, int Q> __device__ __forceinline__ void cv_slot(CV& c) {
  constexpr int i = cv_i(R == 7, Q), J = cv_J(R == 7, Q), d = 128 + 4 * J, a = 32 + 32 * (R & 1) + 4 * i, b = 192 + 32 * (R & 1) + 4 * (J - i);
  constexpr int T = 36 * R + Q, vm = Q % 4 == 1 && Q < 32 ? Q / 4 : -1;
  if constexpr (Q == 0) asm volatile("s_waitcnt lgkmcnt(0)");
  if constexpr (R >= 6 && vm >= 0 && vm % 4 == 0) asm volatile("s_add_u32 m0, s79, %0" ::"i"(((R - 6) ^ (NYT & 1)) * 32768 + vm * 1024));
  if constexpr (R == 0 && i == 0)
    asm volatile("v_mfma_f32_16x16x32_bf16 v[%c0:%c1], a[%c2:%c3], a[%c4:%c5], 0" ::"i"(d), "i"(d + 3), "i"(a), "i"(a + 3), "i"(b), "i"(b + 3));
  else
    asm volatile("v_mfma_f32_16x16x32_bf16 v[%c0:%c1], a[%c2:%c3], a[%c4:%c5], v[%c0:%c1]" ::"i"(d), "i"(d + 3), "i"(a), "i"(a + 3), "i"(b), "i"(b + 3));
  if constexpr (Q < 24 && R < 7) cv_rd<R + 1, Q>(c);
  if constexpr (T % 7 == 3 && NS_X + 5 + T / 7 < 32) d_store<NS_X + 5 + T / 7>();
  if constexpr (R == 2 && Q >= 24 && Q < 32)
    asm volatile("ds_read_b64 v[%c1:%c2], %0 offset:%c3" ::"v"(c.px), "i"(96 + 2 * (Q - 24)), "i"(97 + 2 * (Q - 24)), "i"(512 * (31 - Q)));
  if constexpr (R >= 3 && R < 7) if constexpr (cv_xd0(T - 107) > cv_xd0(T - 108)) cv_xd<cv_xd0(T - 108)>(c);
  if constexpr ((R == 3 || R == 4) && vm >= 0) {
    constexpr int f = (R - 3) * 8 + vm;
    asm volatile("buffer_load_dwordx4 a[%c0:%c1], v84, s[88:91], s96 offen offset:%2 sc1" ::"i"(128 + 4 * f), "i"(131 + 4 * f), "i"((f & 3) * 1024) : "memory");
    if constexpr ((f & 3) == 3) asm volatile("s_addk_i32 s96, 4096");
  }
  if constexpr (R == 4 && Q == 35) asm volatile(FL_POLL ::: "memory");
  if constexpr (R >= 6 && vm >= 0)
    asm volatile("buffer_load_dwordx4 v%c0, s[88:91], s98 offen offset:%1 sc1 lds" ::"i"(86 + vm / 4), "i"((vm & 3) * 1024) : "memory");
  if constexpr (R == 6 && Q == 35) asm volatile("s_add_u32 s98, s98, 0x8000");
  if constexpr (R == 7) cv_zs<cv_z0(Q)>(c, make_iseq<cv_z0(Q + 1) - cv_z0(Q)>{});
  __builtin_amdgcn_sched_barrier(0);
}
template <int R, int... Q> __device__ __forceinline__ void cv_step(CV& c, iseq<Q...>) { (cv_slot<R, Q>(c), ...); }
template <int... N> __device__ __forceinline__ void cv_rd0(CV& c, iseq<N...>) { (cv_rd<0, N>(c), ...); }
template <int FLAG> __device__ __forceinline__ void conv_wave(unsigned sz, float Dd, char* lw) {
  int lane = threadIdx.x & 63; asm volatile("" : "+v"(lane));
  const int l16 = lane & 15, fq = lane >> 4;
  u32x4c w[5]; unsigned w4[5];
#pragma unroll
  for (int r = 0; r < 5; ++r) {
    w[r] = *(const u32x4c*)(lw + r * 1024 + lane * 16);
    w4[r] = *(const unsigned*)(lw + r * 1024 + lane * 16 + 16);
  }
  d_store<NS_X>(); d_store<NS_X + 1>();
#pragma unroll
  for (int r = 0; r < 5; ++r)
    *(u32x4c*)(lw + 10304 + r * 1024 + lane * 16) = (u32x4c){__builtin_amdgcn_alignbit(w[r][1], w[r][0], 16), __builtin_amdgcn_alignbit(w[r][2], w[r][1], 16),
                                                             __builtin_amdgcn_alignbit(w[r][3], w[r][2], 16), __builtin_amdgcn_alignbit(w4[r], w[r][3], 16)};
  d_store<NS_X + 2>();
  CV c;
#pragma unroll
  for (int i = 0; i < 4; ++i) c.pa[i] = (unsigned)(size_t)lw + ((l16 + 1) & 1) * 10304 + 4 * ((8 * fq + l16 + 1) >> 1) + 1024 * i;
  c.pb = (unsigned)(size_t)lw + 5120 + (2032 - 16 * l16 + 8 * fq) * 2 - 512 * 7;
  c.vz = (16 * l16 + 4 * fq) * 2;
  c.px = (unsigned)(size_t)lw + 5120 + 2044 * 2 - 512 * 7 - c.vz;
  c.sz = sz; c.D = Dd;
  asm volatile("" : "+v"(c.vz), "+v"(c.px), "+v"(c.D) :: "memory");
  asm volatile("" : "+s"(c.sz));
  cv_rd0(c, make_iseq<24>{});
  d_store<NS_X + 3>(); d_store<NS_X + 4>();
  __builtin_amdgcn_sched_barrier(0);
  static_for([&](auto R) __attribute__((always_inline)) {
    cv_step<R>(c, make_iseq<36>{});
    if constexpr (R == 2) {
      asm volatile("s_waitcnt vmcnt(0)\n\ts_barrier" ::: "memory");
      fl_set<FLAG>();
    }
    if constexpr (R == 5) {
      asm volatile("s_waitcnt vmcnt(0)" ::: "memory");
      fl_check();
    }
    __builtin_amdgcn_sched_barrier(0);
  }, make_iseq<8>{});
  asm volatile("s_nop 7");
  cv_zs<cv_z0(36)>(c, make_iseq<88 - cv_z0(36)>{});
}

__device__ __forceinline__ void post_dma(int cg, int tb, const bf16* __restrict__ q, unsigned m0w) {
  int lane = threadIdx.x & 63; asm volatile("" : "+v"(lane));
  const __amdgpu_buffer_rsrc_t sq = __builtin_amdgcn_make_buffer_rsrc((void*)(q + (size_t)tb * 32 * DA + cg * 64), (short)0, 32 * DA * 2, 0x00020000);
  const unsigned vq = ((lane >> 3) * DA + (lane & 7) * 8) * 2;
  asm volatile("s_mov_b32 m0, %0\n\ts_nop 4\n\tbuffer_load_dwordx4 %1, %2, 0 offen" NTQS " lds\n\tbuffer_load_dwordx4 %1, %2, %3 offen offset:1024" NTQS " lds\n\t"
               "buffer_load_dwordx4 %1, %2, %4 offen offset:2048" NTQS " lds\n\tbuffer_load_dwordx4 %1, %2, %5 offen offset:3072" NTQS " lds"
               ::"s"(m0w + 4096), "v"(vq), "s"(sq), "s"(8 * DA * 2 - 1024), "s"(16 * DA * 2 - 2048), "s"(24 * DA * 2 - 3072) : "memory");
}
__device__ __forceinline__ void post_math(int cg, int tb, bf16* __restrict__ out, const char* lw) {
  int lane = threadIdx.x & 63; asm volatile("" : "+v"(lane));
  const int tl = lane >> 3, ck = lane & 7;
  const __amdgpu_buffer_rsrc_t so = __builtin_amdgcn_make_buffer_rsrc((void*)(out + (size_t)tb * 32 * OUTW + cg * 64), (short)0, 32 * OUTW * 2 - 2 * DA, 0x00020000);
  const unsigned vo = (tl * OUTW + ck * 8) * 2;
#pragma unroll
  for (int i = 0; i < 4; ++i) {
    const u32x4c qv = *(const u32x4c*)(lw + 4096 + i * 1024 + lane * 16);
    const char* lz = lw + ck * 64 + 16 * (i ^ (ck >> 1)) + tl * 2;
    u32x4c o;
#pragma unroll
    for (int e = 0; e < 4; ++e) {
      const float z0 = bf2f(*(const unsigned short*)(lz + 2 * e * 512));
      const float z1 = bf2f(*(const unsigned short*)(lz + (2 * e + 1) * 512));
      o[e] = pk2(bfl(qv[e]) * z0, bfh(qv[e]) * z1);
    }
    __builtin_amdgcn_raw_buffer_store_b128(o, so, vo + i * 8 * OUTW * 2, 0, OUTSC);
  }
}

#define DI __device__ __forceinline__
#define GAP() __builtin_amdgcn_sched_barrier(0)
typedef __attribute__((ext_vector_type(4))) float f32x4g;
typedef __attribute__((ext_vector_type(4))) unsigned u32x4g;
constexpr int LDW = NH * HD;
struct GM { unsigned aA[2], aB[2][8]; };
template <bool W, int J, int V, bool NT = false> DI void g_dma() {
  if constexpr (NT && J == 0) asm volatile("buffer_load_dwordx4 v%c0, s[60:63], 0 offen nt lds" ::"i"(V) : "memory");
  else if constexpr (NT) asm volatile("buffer_load_dwordx4 v%c0, s[60:63], s%c1 offen nt lds" ::"i"(V), "i"(70 + J) : "memory");
  else
  if constexpr (J == 0) asm volatile("buffer_load_dwordx4 v%c0, s[%c1:%c2], 0 offen lds" ::"i"(V), "i"(W ? 60 : 56), "i"(W ? 63 : 59) : "memory");
  else asm volatile("buffer_load_dwordx4 v%c0, s[%c1:%c2], s%c3 offen lds" ::"i"(V), "i"(W ? 60 : 56), "i"(W ? 63 : 59), "i"((W ? 70 : 63) + J) : "memory");
}
template <int P, int KS, int I> DI void g_wread(GM& g) {
  constexpr int nf = I >> 1, hf = I & 1, r = 192 - 64 * P + 8 * nf + 4 * KS + 2 * hf;
  asm volatile("ds_read_b64_tr_b16 a[%c1:%c2], %0 offset:%c3" ::"v"(g.aB[hf][nf]), "i"(r), "i"(r + 1), "i"(P * 32768 + KS * 16384));
}
template <int P, int M, int KS> DI void g_aread(GM& g) {
  constexpr int r = 96 + 8 * (M & 1) + 4 * KS;
  asm volatile("ds_read_b128 v[%c1:%c2], %0 offset:%c3" ::"v"(g.aA[KS]), "i"(r), "i"(r + 3), "i"(P * 32768 + M * 2048));
}
constexpr float CS = 0.0625f * 1.4426950408889634f;
constexpr int NS_Q = 19;
constexpr int e_vm(int G) { return (G * 3) % 8 < 3 ? G * 3 / 8 : -1; }
constexpr int e_st(int MODE, int G) {
  const int q = G & 15, k3 = ((G >> 4) - 1) * 3 + (q == 0 ? 0 : q == 4 ? 1 : 2), k2 = NS_Q + (G >> 4) * 2 + (q == 4);
  if (MODE == 5) return e_vm(G) >= 32 ? e_vm(G) - 32 : -1;
  if (MODE == 3) return G >= 33 && (G - 33) % 5 == 0 && (G - 33) / 5 < NS_X ? (G - 33) / 5 : -1;
  if (MODE == 2) return G >= 30 && (q == 0 || q == 4 || q == 14) && k3 - 2 < NS_Q ? k3 - 2 : -1;
  if (MODE == 4) return (q == 0 || q == 4) && k2 < 32 ? k2 : -1;
  return -1;
}
constexpr int e_nst(int MODE, int G0, int G1) { int n = 0; for (int G = G0; G < G1; ++G) n += e_st(MODE, G) >= 0; return n; }
static_assert(e_nst(2, 0, 128) == NS_Q && e_nst(3, 30, 128) == NS_X && e_nst(5, 0, 128) == 16 && e_nst(4, 0, 104) == 32 - NS_Q);
constexpr int e_slot(int MODE, int G) { return MODE == 3 ? (G % 3 == 0 && G < 30 ? G / 3 : -1) : e_vm(G) >= 0 && e_vm(G) < 32 ? 10 + e_vm(G) : -1; }
struct ES { int op0[129], nop[129]; };
constexpr ES e_sched(bool TR, int MODE) {
  ES z{};
  const bool SCL = MODE == 2 || MODE == 4;
  const int per = SCL ? 16 : 8, o1 = 32 * per;
  int o = MODE == 4 ? e_sched(false, 2).op0[128] : 0;
  for (int G = 0; G <= 128; ++G) {
    const int q = G & 15;
    int cap = 2 - (q == 1 || q == 2 || q == 15);
    if (SCL) cap -= (q >= 3 && q < 11 && (q & 1)) + (q == 7 || q == 11) + 2 * (G == 70 || G == 74 || (G & 63) == 61);
    cap += MODE == 2 ? 4 + (q == 10 || q == 13) : MODE == 3 ? 1 : MODE == 5 ? 2 : 0;
    if (e_st(MODE, G) >= 0 || (SCL ? q == 8 || q == 12 : e_slot(MODE, G) >= 0) || (MODE == 4 && G < 4)) cap = 0;
    if (G == 128) cap = 10000;
    z.op0[G] = o;
    while (o < o1) {
      const int i = o / per, rdy = MODE == 4 ? 0 : (TR ? 32 * (i >> 3) + 16 + 2 * (i & 7) + 1 : 4 * i + 3) + 6;
      if ((G < rdy && G < 128) || cap-- <= 0) break;
      ++o; ++z.nop[G];
    }
  }
  return z;
}
template <bool TR, int MODE> constexpr ES ESCH = e_sched(TR, MODE);
static_assert(ESCH<false, 4>.op0[96] == 512 && ESCH<false, 4>.op0[0] >= 24 * 16);
template <bool TR, bool SCL, int O> DI void e_op() {
  constexpr int per = SCL ? 16 : 8, i = O / per, k = O % per, r = 128 + 4 * i, t = 114 + 4 * (i & 1), c = k - (per - 4);
  if constexpr (k < 4) asm volatile("v_accvgpr_read_b32 v%c0, a[%c1]" ::"i"(t + k), "i"(4 * i + k));
  else if constexpr (c < 0) asm volatile("v_mul_f32 v%c0, s83, v%c0" ::"i"(k < 8 ? r + (k ^ 6) : t + k - 8));
  else {
    constexpr int d = c == 0 ? r + 3 : c == 1 ? r + 2 : r + c - 2, x = c == 0 ? r + 2 : c == 1 ? r : t + 2 * (c - 2);
    asm volatile("v_cvt_pk_bf16_f32 v%c0, v%c1, v%c2" ::"i"(d), "i"(x), "i"(x + 1));
  }
}
template <bool TR, bool SCL, int B, int... Is> DI void e_ops(iseq<Is...>) { (e_op<TR, SCL, B + Is>(), ...); }
template <int S> DI void e_m0() {
  if constexpr (S == 0 || S == 1 || S == 5 || S == 9) asm volatile("s_add_u32 m0, s78, %0" ::"i"(S * 1024));
  if constexpr (S >= 26 && (S - 26) % 4 == 0) asm volatile("s_add_u32 m0, s79, %0" ::"i"(((S - 26) >> 3) * 32768 + ((S - 26) & 4) * 1024));
}
template <int S> DI void e_load() {
  if constexpr (S == 0) asm volatile("buffer_load_dwordx4 v122, s[56:59], 0 offen lds" ::: "memory");
  else if constexpr (S < 5) asm volatile("buffer_load_dwordx4 v84, s[56:59], s64 offen offset:%0 lds" ::"i"((S - 1) * 1024) : "memory");
  else if constexpr (S < 9) asm volatile("buffer_load_dwordx4 v84, s[60:63], 0 offen offset:%0 sc1 lds" ::"i"((S - 5) * 1024) : "memory");
  else if constexpr (S == 9) asm volatile("buffer_load_dwordx4 v84, s[60:63], s62 offen sc1 lds" ::: "memory");
  else if constexpr (S < 26) {
    constexpr int f = S - 10;
    asm volatile("buffer_load_dwordx4 a[%c0:%c1], v84, s[88:91], s96 offen offset:%2 sc1" ::"i"(128 + 4 * f), "i"(131 + 4 * f), "i"((f & 3) * 1024) : "memory");
    if constexpr ((f & 3) == 3) asm volatile("s_addk_i32 s96, 4096");
  } else {
    constexpr int f = S - 26;
    asm volatile("buffer_load_dwordx4 v%c0, s[88:91], s98 offen offset:%1 sc1 lds" ::"i"(86 + ((f >> 2) & 1)), "i"((f & 3) * 1024) : "memory");
    if constexpr (f == 7) asm volatile("s_add_u32 s98, s98, s97");
    if constexpr (f == 15) asm volatile(FL_POLL ::: "memory");
  }
}
template <bool TR, int P, int H, int G, int MODE, bool NT> DI void g_gap(GM& g) {
  constexpr int m = 4 * H + (G >> 4), q = G & 15, n = q >> 1, ks = q & 1, GG = 64 * H + G;
  constexpr bool inV = TR ? m & 1 : n & 1, END = MODE == 3 || MODE == 5;
  constexpr int a = 96 + 8 * (m & 1) + 4 * ks, b = 192 - 64 * P + 8 * n + 4 * ks, d = (inV ? 128 : 0) + 4 * (TR ? 8 * (m >> 1) + n : 4 * m + (n >> 1));
  constexpr int J = END ? -1 : q == 8 ? 2 * (G >> 4) : q == 12 ? 2 * (G >> 4) + 1 : -1, gj = (J & 3) ^ ((J & 3) >> 1);
  if constexpr (END) { if constexpr (q == 0 && GG > 0) asm volatile("s_waitcnt lgkmcnt(0)"); e_m0<e_slot(MODE, GG)>(); }
  else if constexpr (q == 0 && G > 0) asm volatile("s_waitcnt lgkmcnt(4)");
  if constexpr (J == 0) asm volatile("s_add_u32 m0, s78, %0" ::"i"((P ^ H ^ 1) * 32768 + (1 - H) * 8192));
  else if constexpr (J == 4) asm volatile("s_add_u32 m0, s79, %0" ::"i"((P ^ H) * 32768 + (1 - H) * 16384));
  else if constexpr (J > 0 && (J & 3) == 1) asm volatile("s_bitset1_b32 m0, 12");
  else if constexpr (J > 0 && (J & 3) == 2) asm volatile("s_bitset1_b32 m0, %0" ::"i"(J < 4 ? 14 : 13));
  else if constexpr (J > 0) asm volatile("s_bitset0_b32 m0, 12");
#define GMF(D, X, Y, C) asm volatile("v_mfma_f32_16x16x32_bf16 " D "[%c0:%c1], " X ", " Y ", " C ::"i"(d), "i"(d + 3), "i"(a), "i"(a + 3), "i"(b), "i"(b + 3))
#define GMF2(D, C) do { if constexpr (TR) GMF(D, "v[%c2:%c3]", "a[%c4:%c5]", C); else GMF(D, "a[%c4:%c5]", "v[%c2:%c3]", C); } while (0)
  if constexpr ((MODE == 1 || MODE == 4) && ks == 0) { if constexpr (inV) GMF2("v", "0"); else GMF2("a", "0"); }
  else if constexpr (inV) GMF2("v", "v[%c0:%c1]");
  else GMF2("a", "a[%c0:%c1]");
#undef GMF2
#undef GMF
  if constexpr ((q == 1 || q == 2) && !(END && m == 7)) g_aread<(m < 7 ? P : P ^ 1), (m + 1) & 7, q - 1>(g);
  if constexpr (!END && q >= 3 && q < 11 && (q & 1)) g_wread<P ^ 1, H, (G >> 4) * 4 + (q - 3) / 2>(g);
  if constexpr (e_st(MODE, GG) >= 0) {
    static_assert(ESCH<TR, MODE>.op0[GG] >= (e_st(MODE, GG) + 1) * (END ? 8 : 16));
    d_store<e_st(MODE, GG)>();
  }
  if constexpr (J >= 0 && J < 4) g_dma<false, (1 - H) * 2 + (gj & 1) + (gj >> 1) * 4, 90>();
  else if constexpr (J >= 4) g_dma<true, (1 - H) * 4 + gj, 91, NT>();
  if constexpr (MODE >= 2) e_ops<TR, !END, ESCH<TR, MODE>.op0[GG]>(make_iseq<ESCH<TR, MODE>.nop[GG]>{});
  if constexpr (END) {
    if constexpr (e_slot(MODE, GG) >= 0) e_load<e_slot(MODE, GG)>();
  } else {
    if constexpr (H == 1 && G == 6) asm volatile("v_lshl_add_u32 v90, s82, 7, v92\n\ts_and_b32 s82, s80, 15");
    if constexpr (H == 1 && G == 10) asm volatile("s_lshr_b64 s[80:81], s[80:81], 4\n\tv_lshl_add_u32 v91, s82, 19, v93");
    constexpr int nst = e_nst(MODE, H ? 29 : 0, GG + 1) + (MODE == 4 && !H ? e_nst(2, 93, 128) : 0);
    if constexpr (G == 61) asm volatile("s_waitcnt vmcnt(%0) lgkmcnt(0)\n\ts_barrier" ::"i"(12 + nst) : "memory");
  }
  if constexpr (END && GG == 127) {
    asm volatile("s_nop 7");
    e_ops<TR, false, ESCH<TR, MODE>.op0[128]>(make_iseq<ESCH<TR, MODE>.nop[128]>{});
  }
  GAP();
}
template <bool TR, int P, int H, int MODE, bool NT, int... Gs> DI void g_half_(GM& g, iseq<Gs...>) { (g_gap<TR, P, H, Gs, MODE, NT>(g), ...); }
template <bool TR, int P, int H, int MODE, bool NT = false> DI void g_half(GM& g) { g_half_<TR, P, H, MODE, NT>(g, make_iseq<64>{}); }
template <bool TR, int P, int MODE, bool NT = false> DI void g_tile(GM& g) { g_half<TR, P, 0, MODE, NT>(g); g_half<TR, P, 1, MODE, NT>(g); }
template <int P, int... Is> DI void g_read0(GM& g, iseq<Is...>) {
  (g_wread<P, 0, Is>(g), ...); (g_wread<P, 1, Is>(g), ...);
  g_aread<P, 0, 0>(g); g_aread<P, 0, 1>(g);
}
template <int I> DI void g_pro1(const unsigned (&m)[4]) {
  constexpr int grp = I >> 2, i = I & 3, t = grp < 4 ? 0 : grp < 7 ? 1 : 2;
  constexpr bool isA = grp == 0 || grp == 3 || grp == 5;
  constexpr int jj = (grp == 3 ? 2 : 0) + (i & 1) + (i >> 1) * 4, ks = grp == 2 || grp == 6;
  asm volatile("s_add_u32 m0, %0, %1\n\ts_nop 0" ::"s"(m[(t & 1) * 2 + !isA]), "i"(isA ? jj * 4096 : ks * 16384 + (i >> 1) * 8192 + (i & 1) * 4096) : "scc");
  if constexpr (isA) g_dma<false, jj, t ? 90 : 92>();
  else g_dma<true, ks * 4 + i, t ? 91 : 93, NTW >= 2>();
}
template <int B, int... Is> DI void g_pro(const unsigned (&m)[4], iseq<Is...>) { (g_pro1<B + Is>(m), ...); }
template <bool TR, bool SCL, class HOOK> DI void g_loop(GM& g, unsigned evm, HOOK&& hook) {
  constexpr int nkt = SCL ? 7 : 16, P0 = SCL ? 0 : 1;
  asm volatile("" : "+s"(evm));
#pragma unroll 1
  for (int t = 1;; t += 2) {
    g_half<TR, P0 ^ 1, 0, 0, (SCL ? NTW >= 1 : NTW >= 2)>(g);
    if (__builtin_expect((evm >> t) & 1, 0)) { hook(t); GAP(); }
    g_half<TR, P0 ^ 1, 1, 0, (SCL ? NTW >= 1 : NTW >= 2)>(g);
    if (SCL && t + 2 >= nkt) break;
    g_half<TR, P0, 0, 0, (SCL ? NTW >= 1 : NTW >= 2)>(g);
    if constexpr (SCL) { if (__builtin_expect(t == 3, 0)) { hook(4); GAP(); } }
    else asm volatile("s_cmp_eq_u32 %0, 13\n\ts_cselect_b32 s78, 0x28000, s78" ::"s"(t) : "scc");
    g_half<TR, P0, 1, 0, (SCL ? NTW >= 1 : NTW >= 2)>(g);
    if (!SCL && t + 3 >= nkt) break;
  }
}

typedef __attribute__((ext_vector_type(4))) unsigned u32x4;
typedef __attribute__((ext_vector_type(4))) float f32x4;

constexpr int R_S = 128, R_P = 192, R_CI = 224, R_E = 96;
constexpr int R_TS = 112, R_RS = 114, R_M = 116;
constexpr int R_PD = 118, R_THR = 120, R_TMP = 121;
constexpr int r_dk(int q) { return q < 6 ? 90 + q : 120 + q; }
constexpr int S_V = 96, S_T = 97, S_K = 98, S_I = 99, S_EV = 100;
struct AC {
  unsigned rare;
};
DI bool redo() { u64 t; asm volatile("s_mov_b64 %0, s[54:55]" : "=s"(t)); return t != 0; }
template <int X, int F> DI void kread() {
  constexpr int r = 192 + 4 * (F & 15);
  asm volatile("ds_read_b128 a[%c0:%c1], v84 offset:%c2" ::"i"(r), "i"(r + 3), "i"(X * 32768 + F * 1024));
}
template <int X, int F> DI void vread() {
  constexpr int r = 192 + 4 * (F & 15);
  asm volatile("ds_read_b128 a[%c0:%c1], v85 offset:%c2" ::"i"(r), "i"(r + 3), "i"(X * 32768 + (F & 1) * 16384 + (F >> 1) * 1024));
}
template <int X, int... Fs> DI void kreads(iseq<Fs...>) { (kread<X, Fs>(), ...); }
template <int X, int J> DI void dma_m0() {
  if constexpr (J == 0) asm volatile("s_mov_b32 m0, s%c0" ::"i"(78 + X));
  else if constexpr (J == 4) asm volatile("s_or_b32 m0, s%c0, 0x1000" ::"i"(78 + X));
  else if constexpr (J == 8) asm volatile("s_bitset0_b32 m0, 16");
  else if constexpr (J == 12) asm volatile("s_bitset0_b32 m0, 12");
}
template <int J> DI void dma_at() {
  if constexpr (J < 8)
    asm volatile("buffer_load_dwordx4 v%c0, s[88:91], s96 offen offset:%1 sc1 lds" ::"i"(86 + (J >= 4)), "i"((J & 3) * 1024) : "memory");
  else
    asm volatile("buffer_load_dwordx4 v%c0, s[92:95], s98 offen offset:%1 sc1 lds" ::"i"(r_dk(J - 8)), "i"((J & 3) * 1024) : "memory");
}
template <int Q> DI void dma4k() {
  asm volatile("s_add_u32 m0, s79, %0\n\ts_add_u32 s48, s98, %1\n\tbuffer_load_dwordx4 v%c2, s[88:91], s48 offen sc1 lds\n\t"
               "buffer_load_dwordx4 v%c2, s[88:91], s48 offen offset:1024 sc1 lds\n\tbuffer_load_dwordx4 v%c2, s[88:91], s48 offen offset:2048 sc1 lds\n\t"
               "buffer_load_dwordx4 v%c2, s[88:91], s48 offen offset:3072 sc1 lds"
               ::"i"((Q >> 1) * 32768 + (Q & 1) * 4096), "i"((Q >> 1) * 32768), "i"(86 + (Q & 1)) : "memory", "scc");
}
template <int Q> DI void acc_zero2() {
  asm volatile("v_accvgpr_write_b32 a[%c0], 0\n\tv_accvgpr_write_b32 a[%c1], 0" ::"i"(2 * Q), "i"(2 * Q + 1));
}
#define SMV 0
struct SMS { int ex[128], op0[128], nop[128], last; };
constexpr SMS sm_sched() {
  SMS z{};
  int et[32] = {}, e = 0;
  for (int t = 0; t < 128; ++t) {
    const int g = t & 15;
    const bool x = t < 48 ? (g >= 8 && (g & 1) && g < 15) : t < 64 ? (g >= 9 && g < 15) : (g >= 8 && g < 14);
    z.ex[t] = x && e < 32 ? e : -1;
    if (z.ex[t] >= 0) et[e++] = t;
  }
  int o = 0;
  for (int t = 0; t < 128; ++t) {
    const int g = t & 15;
    int cap = z.ex[t] >= 0 || t < 8 || (t < 48 && (g == 7 || (g >= 8 && !(g & 1)))) || t == 56 || t == 63 || t == 111 ? 0 : 1;
    z.op0[t] = o; z.nop[t] = 0;
    while (cap > 0 && o < 48 && et[2 * (o / 3) + (o % 3 == 0 ? 0 : 1)] + 2 <= t) {
      const int el = 2 * (o / 3) + o % 3;
      if (!(o % 3 < 2 && (el == 0 || el == 4))) --cap;
      ++o; ++z.nop[t]; z.last = t;
    }
  }
  for (int k = 16; k < 32; ++k) if (z.op0[et[k]] < 3 * ((k - 16) / 2 + 1)) z.last = 1000;
  return z;
}
constexpr SMS SM = sm_sched();
static_assert(SM.last <= 117);
constexpr int p_reg(int Y, int k) { return R_P + 16 * Y + 4 * ((k >> 3) * 2 + ((k >> 1) & 1)) + ((k >> 2) & 1) * 2 + (k & 1); }
template <int Y, int O> DI void sm_op() {
  constexpr int k = O / 3, e = 2 * k + O % 3, ts = R_TS + ((e >> 2) & 1);
  if constexpr (O % 3 == 2) asm volatile("v_cvt_pk_bf16_f32 v%c0, v%c1, v%c2" ::"i"(p_reg(Y, k)), "i"(R_E + ((2 * k) & 15)), "i"(R_E + ((2 * k + 1) & 15)));
  else if constexpr (e == 1 || e == 5) asm volatile("v_add_f32_e32 v%c0, v%c1, v%c2" ::"i"(ts), "i"(R_E + e - 1), "i"(R_E + e));
  else if constexpr (e != 0 && e != 4) asm volatile("v_add_f32_e32 v%c0, v%c0, v%c1" ::"i"(ts), "i"(R_E + (e & 15)));
}
template <int Y, int T> DI void sm_gap(AC& c) {
  if constexpr (SM.ex[T] >= 0) asm volatile("v_exp_f32 v%c0, v%c1" ::"i"(R_E + (SM.ex[T] & 15)), "i"(R_S + 32 * Y + SM.ex[T]));
  if constexpr (SM.nop[T] > 0) sm_op<Y, SM.op0[T]>();
  if constexpr (SM.nop[T] > 1) sm_op<Y, SM.op0[T] + 1>();
  if constexpr (SM.nop[T] > 2) sm_op<Y, SM.op0[T] + 2>();
  if constexpr (T == SM.last + 1) asm volatile("v_add_f32_e32 v%c0, v%c1, v%c2" ::"i"(R_TMP), "i"(R_TS), "i"(R_TS + 1));
  if constexpr (T == SM.last + 2) asm volatile("v_cmp_nle_f32_e64 s[54:55], v%c0, v%c1" ::"i"(R_TMP), "i"(R_THR));
}
#define W_A0 1
template <int X, int MODE, int G> DI void gapA(AC& c) {
  constexpr bool FIRST = MODE == 1 || MODE == 2, NOQK = MODE >= 3;
  constexpr int Xp = X ^ 1, kb = G >> 4, g = G & 15, qb = g >> 3, s = g & 7;
  if constexpr (MODE == 3 && G < 48 && G % 3 == 0) {
    constexpr int f = G / 3;
    asm volatile("buffer_load_dwordx4 a[%c0:%c1], v84, s[88:91], s75 offen offset:%2 sc1" ::"i"(128 + 4 * f), "i"(131 + 4 * f), "i"((f & 3) * 1024) : "memory");
    if constexpr ((f & 3) == 3) asm volatile("s_addk_i32 s75, 4096");
  }
  if constexpr (G == 48) asm volatile("s_waitcnt vmcnt(%0) lgkmcnt(0)\n\ts_barrier" ::"i"(MODE == 3 ? 16 : MODE == 2 ? 12 : MODE == 1 ? W_A0 : 0) : "memory");
  else if constexpr (g == 0 && !NOQK) asm volatile("s_waitcnt lgkmcnt(0)");
  if constexpr (!NOQK) {
    constexpr int r = 192 + 4 * ((kb & 1) * 8 + s), q = 128 + 4 * g;
    constexpr int d = R_S + 32 * X + 4 * (kb * 2 + qb), ci = s == 0 ? R_CI + 4 * (kb * 2 + qb) : d;
    if constexpr (FIRST && s == 0)
      asm volatile("v_mfma_f32_16x16x32_bf16 v[%c0:%c1], a[%c2:%c3], a[%c4:%c5], 0" ::"i"(d), "i"(d + 3), "i"(r), "i"(r + 3), "i"(q), "i"(q + 3));
    else
      asm volatile("v_mfma_f32_16x16x32_bf16 v[%c0:%c1], a[%c2:%c3], a[%c4:%c5], v[%c6:%c7]" ::"i"(d), "i"(d + 3), "i"(r), "i"(r + 3), "i"(q), "i"(q + 3),
                   "i"(ci), "i"(ci + 3));
    if constexpr (g < 8 && kb < 3) kread<X, (kb + 1) * 8 + g>();
    constexpr int S0 = SMV ? 40 : 14, S1 = SMV ? 41 : 30, S2 = SMV ? 42 : 45, S3 = SMV ? 43 : 46;
    if constexpr (G == S0) asm volatile("s_add_u32 s96, s97, 0x1000000");
    if constexpr (G == S1) dma_m0<X, 0>();
    if constexpr (G == S2) asm volatile("s_mov_b32 s97, s98");
    if constexpr (G == S3) asm volatile("s_add_u32 s98, s98, s99");
    if constexpr (G >= 56 && !(G & 1)) dma_at<(G - 56) / 2>();
    if constexpr (G == 57) asm volatile("s_lshr_b32 s100, s100, 1\n\ts_add_u32 s56, s56, 1");
    if constexpr (G == 59 && !FIRST) asm volatile("s_and_b32 s49, s100, 1\n\ts_or_b32 s49, s49, s54");
    if constexpr (G == 61 && !FIRST) asm volatile("s_or_b32 %0, s49, s55" : "=s"(c.rare));
    if constexpr (G == 61 && FIRST) asm volatile("s_and_b32 %0, s100, 1" : "=s"(c.rare));
  } else if constexpr (SM.nop[64 + G] > 0) asm volatile("s_nop 0");
  if constexpr (!FIRST && g < 8 && kb == 3) vread<Xp, g>();
  if constexpr (MODE == 2 && (G & 3) == 1) d_store<16 + G / 4>();
  if constexpr (FIRST) acc_zero2<G>();
  else sm_gap<Xp, 64 + G>(c);
  GAP();
}
template <int R> DI void rescale1(float alpha) {
  float t;
  asm volatile("v_accvgpr_read_b32 %0, a[%c2]\n\tv_mul_f32 %0, %0, %1\n\tv_accvgpr_write_b32 a[%c2], %0" : "=&v"(t) : "v"(alpha), "i"(R));
}
template <int... Rs> DI void rescale_all(const float (&alpha)[2], iseq<Rs...>) { (rescale1<Rs>(alpha[(Rs >> 2) & 1]), ...); }
template <bool MAX> DI float row_reduce(float v) {
  auto p = __builtin_amdgcn_permlane32_swap(__float_as_uint(v), __float_as_uint(v), false, false);
  v = MAX ? fmaxf(__uint_as_float(p[0]), __uint_as_float(p[1])) : __uint_as_float(p[0]) + __uint_as_float(p[1]);
  p = __builtin_amdgcn_permlane16_swap(__float_as_uint(v), __float_as_uint(v), false, false);
  return MAX ? fmaxf(__uint_as_float(p[0]), __uint_as_float(p[1])) : __uint_as_float(p[0]) + __uint_as_float(p[1]);
}
template <int Y, int QB> DI float row_max() {
  constexpr int r = R_S + 32 * Y + 4 * QB;
  float mx;
  asm volatile("v_max_f32 %0, v%c1, v%c2\n\tv_max3_f32 %0, %0, v%c3, v%c4" : "=&v"(mx) : "i"(r), "i"(r + 1), "i"(r + 2), "i"(r + 3));
  static_for([&](auto K) {
    constexpr int q = r + 8 * (K + 1);
    float t = mx;
    asm volatile("v_max3_f32 %0, %0, v%c1, v%c2\n\tv_max3_f32 %0, %0, v%c3, v%c4" : "+v"(t) : "i"(q), "i"(q + 1), "i"(q + 2), "i"(q + 3));
    mx = t;
  }, make_iseq<3>{});
  return row_reduce<true>(mx);
}
template <int R> DI void v_sub(float d) { asm volatile("v_sub_f32 v%c0, v%c0, %1" ::"i"(R), "v"(d)); }
template <int B> DI void s_shift(const float (&d)[2]) { static_for([&](auto E) { v_sub<B + E>(d[(E >> 2) & 1]); }, make_iseq<32>{}); }
DI void m_shift(const float (&d)[2]) {
  s_shift<R_CI>(d);
  v_sub<R_M>(-d[0]); v_sub<R_M + 1>(-d[1]);
}
DI void set_mask0(int wid) {
  const int mr = wid * 32 + __float_as_int(vget<89>());
  static_for([&](auto I) {
    constexpr int e = I, k = (e >> 3) * 16 - ((e >> 2) & 1) * 16 + (e & 3);
    const float x = vget<R_S + e>();
    vset<R_S + e>(k > mr ? -INFINITY : x);
  }, make_iseq<32>{});
}
template <int X> DI void sm_redo() {
  constexpr int Xp = X ^ 1;
  float d[2], dt[2], alpha[2], ts[2] = {0.f, 0.f};
  static_for([&](auto Q) {
    constexpr int qb = Q;
    const float pd = vget<R_PD + qb>();
    d[qb] = fmaxf(row_max<Xp, qb>() - pd, 0.f);
    dt[qb] = d[qb] + pd;
    vset<R_PD + qb>(d[qb]);
    alpha[qb] = __builtin_amdgcn_exp2f(-d[qb]);
    vset<R_RS + qb>(vget<R_RS + qb>() * alpha[qb]);
  }, make_iseq<2>{});
  vset<R_THR>(d[0] > 0.f || d[1] > 0.f ? -1.f : 4096.f);
  m_shift(d);
  static_for([&](auto K) {
    constexpr int k = K, qb = (k >> 1) & 1;
    const float e0 = __builtin_amdgcn_exp2f(vget<R_S + 32 * Xp + 2 * k>() - dt[qb]), e1 = __builtin_amdgcn_exp2f(vget<R_S + 32 * Xp + 2 * k + 1>() - dt[qb]);
    ts[qb] += e0; ts[qb] += e1;
    vset<p_reg(Xp, k)>(__uint_as_float(pk2(e0, e1)));
  }, make_iseq<16>{});
  vset<R_TS>(ts[0]); vset<R_TS + 1>(ts[1]);
  rescale_all(alpha, make_iseq<128>{});
  asm volatile("s_nop 1" ::: "memory");
  GAP();
}
template <int X, int MODE, int... Gs> DI void phaseA_(AC& c, iseq<Gs...>) { (gapA<X, MODE, Gs>(c), ...); }
template <int X, int MODE> DI void phaseA(AC& c) { phaseA_<X, MODE>(c, make_iseq<64>{}); }

template <int X, bool SMX, int G, bool WAIT = true> DI void gapB(AC& c) {
  constexpr int Xp = X ^ 1, blk = G >> 4, g = G & 15;
  if constexpr (g == 0 && WAIT) asm volatile("s_waitcnt lgkmcnt(0)");
  constexpr int qb = (g >> 1) & 1, s = g & 1, r = 192 + 4 * ((blk & 1) * 8 + (g >> 2) * 2 + s), o = 4 * ((blk * 4 + (g >> 2)) * 2 + qb);
  constexpr int p = R_P + 16 * Xp + 4 * (s * 2 + qb);
  asm volatile("v_mfma_f32_16x16x32_bf16 a[%c0:%c1], a[%c2:%c3], v[%c4:%c5], a[%c0:%c1]" ::"i"(o), "i"(o + 3), "i"(r), "i"(r + 3), "i"(p), "i"(p + 3));
  if constexpr (g < 8) {
    if constexpr (blk < 3) vread<Xp, (blk + 1) * 8 + g>();
    else if constexpr (SMX) kread<Xp, g>();
  }
  if constexpr (SMX) {
    if constexpr (G == 1 || G == 2) asm volatile("v_add_f32_e32 v%c0, v%c0, v%c1" ::"i"(R_RS + G - 1), "i"(R_TS + G - 1));
    if constexpr (g == 7 && blk < 3) dma_m0<X, blk * 4 + 4>();
    if constexpr (g >= 8 && !(g & 1) && blk < 3) dma_at<blk * 4 + 4 + (g - 8) / 2>();
    if constexpr (G == 55 && X == 1) asm volatile("s_cmp_eq_u32 s56, s76");
    if constexpr (G == 3) asm volatile("s_lshr_b32 s77, s77, 1");
    if constexpr (G == 4) asm volatile("s_bitcmp1_b32 s77, 0");
    if constexpr (G == 5) asm volatile("s_cselect_b64 s[52:53], s[62:63], 0");
    if constexpr (G == 6) asm volatile(FL_ARM("s[52:53]"));
    if constexpr (G == 56) asm volatile(FL_POLL ::: "memory");
    sm_gap<X, G>(c);
  }
  GAP();
}
template <int X, int... Gs> DI void phaseB_(AC& c, iseq<Gs...>) { (gapB<X, true, Gs>(c), ...); }
template <int X> DI void phaseB(AC& c) { phaseB_<X>(c, make_iseq<64>{}); }
constexpr int sm_nex() { int n = 0; for (int t = 0; t < 64; ++t) n += SM.ex[t] >= 0; return n; }
template <int X> DI void phaseB0(unsigned mode, int wid) {
  dma_m0<X, 4>();
  kreads<X ^ 1>(make_iseq<8>{});
  dma_at<4>();
  asm volatile("s_nop 3");
  float d[2];
  if (mode & 2) {
    d[0] = vget<122>(); d[1] = vget<123>();
    dma_at<5>();
  } else {
    if constexpr (X == 0) if (__builtin_expect(mode & 4, 0)) set_mask0(wid);
    d[0] = row_max<X, 0>(); GAP(); dma_at<5>();
    d[1] = row_max<X, 1>();
#pragma unroll
    for (int qb = 0; qb < 2; ++qb) if (!(d[qb] > -3.0e38f)) d[qb] = 0.f;
  }
  GAP();
  vset<R_M>(d[0]); vset<R_M + 1>(d[1]);
  dma_at<6>();
  const f32x2c nd[2] = {{-d[0], -d[0]}, {-d[1], -d[1]}};
  static_for([&](auto E) {
    constexpr int e = E;
    const f32x2c v = nd[(e >> 1) & 1];
    asm volatile("v_pk_add_f32 v[%c0:%c1], v[%c0:%c1], %4\n\tv_mov_b64 v[%c2:%c3], %4" ::"i"(R_S + 32 * X + 2 * e), "i"(R_S + 32 * X + 2 * e + 1), "i"(R_CI + 2 * e), "i"(R_CI + 2 * e + 1), "v"(v));
    if constexpr (e == 3) dma_at<7>();
    if constexpr (e == 6) dma_m0<X, 8>();
    if constexpr (e == 7 || e == 11 || e == 15) dma_at<8 + (e - 7) / 4>();
  }, make_iseq<16>{});
  static_assert(SM.op0[64] >= 3 * ((sm_nex() - 15) / 2) && SM.op0[64] <= 24);
  static_for([&](auto E) {
    constexpr int e = E;
    if constexpr (e < sm_nex()) asm volatile("v_exp_f32 v%c0, v%c1" ::"i"(R_E + e), "i"(R_S + 32 * X + e));
    if constexpr (e == 3) dma_at<11>();
    if constexpr (e == 6) dma_m0<X, 12>();
    if constexpr (e == 7 || e == 11 || e == 15) dma_at<12 + (e - 7) / 4>();
  }, make_iseq<16>{});
  static_for([&](auto O) { sm_op<X, O>(); }, make_iseq<SM.op0[64]>{});
  dma_at<15>();
  static_for([&](auto E) { asm volatile("v_exp_f32 v%c0, v%c1" ::"i"(R_E + (int)E), "i"(R_S + 32 * X + 16 + (int)E)); }, make_iseq<(sm_nex() > 16 ? sm_nex() - 16 : 0)>{});
  asm volatile("buffer_store_dwordx2 v[%c0:%c1], v88, s[88:91], s76 offen sc1\n\ts_bitcmp1_b32 s77, 0\n\ts_cselect_b64 s[52:53], s[62:63], 0\n\t"
               FL_ARM("s[52:53]") "\n\t" FL_POLL ::"i"(R_M), "i"(R_M + 1) : "memory", "scc");
  GAP();
}
constexpr int T_ZO = 8;
#define NINF2(R) "v_mov_b64 v[" #R ":" #R "+1], v[122:123]\n\t"
#define NINF4(R) NINF2(R) NINF2(R + 2)
#define TRI(R) "v_cndmask_b32_e64 v[" #R "], v[" #R "], v122, s[48:49]\n\tv_cndmask_b32_e64 v[" #R "+1], v[" #R "+1], v122, s[50:51]\n\t" \
               "v_cndmask_b32_e64 v[" #R "+2], v[" #R "+2], v122, s[52:53]\n\tv_cndmask_b32_e64 v[" #R "+3], v[" #R "+3], v122, s[54:55]\n\t"
DI void ev_code() {
  asm volatile(
    "s_getpc_b64 s[82:83]\n\ts_add_u32 s82, s82, 12\n\ts_addc_u32 s83, s83, 0\n\ts_branch 9f\n\t"
    "s_bitcmp0_b32 s58, s56\n\ts_cbranch_scc1 1f\n\t" FL_SET("s70") "\n\t" FL_SET("s71") "\n"
    "1:\n\ts_bitcmp0_b32 s57, s56\n\ts_cbranch_scc1 2f\n\t" FL_CHECK("s[62:63]") "\n\t"
    "s_add_u32 s98, s98, s68\n\ts_mov_b32 s68, s69\n\ts_mov_b32 s69, 0\n\ts_mov_b64 s[62:63], s[64:65]\n\ts_mov_b64 s[64:65], s[66:67]\n"
    "2:\n\ts_bitcmp0_b32 s59, s56\n\ts_cbranch_scc1 3f\n\ts_mov_b32 s98, s72\n\ts_bitcmp0_b32 s73, 0\n\ts_cbranch_scc1 3f\n\t"
    "v_lshrrev_b32 v90, 7, v86\n\tv_and_b32 v91, 6, v90\n\tv_lshlrev_b32 v91, 4, v91\n\tv_and_b32 v92, 0x70, v86\n\tv_xor_b32 v91, v91, v92\n\t"
    "s_movk_i32 s48, 896\n\tv_mad_u32_u24 v94, v90, s48, v91\n\ts_mov_b64 s[92:93], s[80:81]\n\ts_mov_b32 s94, 229376\n\ts_movk_i32 s99, 128\n\t"
    "v_add_u32 v90, 28672, v94\n\tv_add_u32 v91, 34816, v94\n\tv_add_u32 v92, 40960, v94\n\tv_add_u32 v93, 47104, v94\n\t"
    "v_add_u32 v95, 6144, v94\n\tv_add_u32 v126, 12288, v94\n\tv_add_u32 v127, 18432, v94\n"
    "3:\n\ts_bitcmp0_b32 s60, s56\n\ts_cbranch_scc1 4f\n\t"
    "v_readlane_b32 s54, v78, %0\n\ts_add_u32 m0, s78, 0x10000\n\t"
    "s_lshr_b32 s53, s73, 16\n\ts_mulk_i32 s53, 3584\n\ts_mov_b64 s[48:49], s[80:81]\n\ts_add_u32 s50, s53, 3584\n\ts_mov_b32 s51, 0x20000\n\t"
    "v_lshlrev_b32 v121, 3, v84\n\tbuffer_load_dword v121, s[48:51], s53 offen lds\n\t"
    "s_bitcmp0_b32 s73, 3\n\ts_cbranch_scc1 4f\n\t"
    "v_lshlrev_b32 v121, 9, v84\n\tv_and_b32 v121, 0x38000, v121\n\tv_lshrrev_b32 v122, 9, v84\n\tv_lshl_add_u32 v121, v122, 12, v121\n\t"
    "v_lshrrev_b32 v122, 3, v84\n\tv_xor_b32 v122, v122, v84\n\tv_and_or_b32 v121, v122, 48, v121\n\t"
    "buffer_load_dwordx4 v121, s[88:91], s54 offen sc1 lds\n\ts_addk_i32 s54, 7168\n\t"
    "buffer_load_dwordx4 v121, s[88:91], s54 offen offset:1024 sc1 lds\n\ts_addk_i32 s54, 7168\n\t"
    "buffer_load_dwordx4 v121, s[88:91], s54 offen offset:2048 sc1 lds\n\ts_addk_i32 s54, 7168\n\t"
    "buffer_load_dwordx4 v121, s[88:91], s54 offen offset:3072 sc1 lds\n"
    "4:\n\ts_bitcmp0_b32 s61, s56\n\ts_cbranch_scc1 6f\n\t"
    "s_sub_u32 s74, s74, 64\n\ts_cmp_ge_i32 s74, 0\n\ts_cbranch_scc1 6f\n\t"
    "v_cmp_gt_i32 s[48:49], 0, v89\n\tv_cmp_gt_i32 s[50:51], 1, v89\n\tv_cmp_gt_i32 s[52:53], 2, v89\n\tv_cmp_gt_i32 s[54:55], 3, v89\n\t"
    "v_mov_b32 v122, 0xff800000\n\tv_mov_b32 v123, 0xff800000\n\t" NINF4(248)
    "s_cmp_eq_i32 s74, -32\n\ts_cbranch_scc0 5f\n\t" TRI(240) TRI(252) "s_setpc_b64 s[86:87]\n"
    "5:\n\t" NINF4(240) NINF4(244) NINF4(252) NINF4(232) TRI(224) TRI(236)
    "s_cmp_eq_i32 s74, -64\n\ts_cbranch_scc1 6f\n\t" NINF4(224) NINF4(228) NINF4(236)
    "6:\n\ts_setpc_b64 s[86:87]\n9:"
    ::"i"(T_ZO) : "scc");
}
DI void ev_call() { asm volatile("s_swappc_b64 s[86:87], s[82:83]" ::: "vcc", "scc", "memory"); GAP(); }

#define ACLOB_ALL "a0","a1","a2","a3","a4","a5","a6","a7","a8","a9","a10","a11","a12","a13","a14","a15","a16","a17","a18","a19","a20","a21","a22","a23","a24","a25","a26","a27","a28","a29","a30","a31","a32","a33","a34","a35","a36","a37","a38","a39","a40","a41","a42","a43","a44","a45","a46","a47","a48","a49","a50","a51","a52","a53","a54","a55","a56","a57","a58","a59","a60","a61","a62","a63","a64","a65","a66","a67","a68","a69","a70","a71","a72","a73","a74","a75","a76","a77","a78","a79","a80","a81","a82","a83","a84","a85","a86","a87","a88","a89","a90","a91","a92","a93","a94","a95","a96","a97","a98","a99","a100","a101","a102","a103","a104","a105","a106","a107","a108","a109","a110","a111","a112","a113","a114","a115","a116","a117","a118","a119","a120","a121","a122","a123","a124","a125","a126","a127","a128","a129","a130","a131","a132","a133","a134","a135","a136","a137","a138","a139","a140","a141","a142","a143","a144","a145","a146","a147","a148","a149","a150","a151","a152","a153","a154","a155","a156","a157","a158","a159","a160","a161","a162","a163","a164","a165","a166","a167","a168","a169","a170","a171","a172","a173","a174","a175","a176","a177","a178","a179","a180","a181","a182","a183","a184","a185","a186","a187","a188","a189","a190","a191","a192","a193","a194","a195","a196","a197","a198","a199","a200","a201","a202","a203","a204","a205","a206","a207","a208","a209","a210","a211","a212","a213","a214","a215","a216","a217","a218","a219","a220","a221","a222","a223","a224","a225","a226","a227","a228","a229","a230","a231","a232","a233","a234","a235","a236","a237","a238","a239","a240","a241","a242","a243","a244","a245","a246","a247","a248","a249","a250","a251","a252","a253","a254","a255"

struct EP { float fa[2], fb[2]; };
template <int I, int E, int MODE> DI void epi_pair(const EP& p) {
  constexpr int qb = I & 1, r = 16 * (I >> 1) + 4 * qb + (E >> 1) * 8 + (E & 1) * 2, t = 96 + 2 * E, w = 128 + 4 * I + E;
  if constexpr (MODE == 2) asm volatile("v_accvgpr_read_b32 v106, a[%c0]" ::"i"(w));
  asm volatile("v_accvgpr_read_b32 v%c0, a[%c2]\n\tv_accvgpr_read_b32 v%c1, a[%c3]" ::"i"(t), "i"(t + 1), "i"(r), "i"(r + 1));
  if constexpr (MODE) asm volatile("v_mul_f32 v%c0, v%c0, %2\n\tv_mul_f32 v%c1, v%c1, %2" ::"i"(t), "i"(t + 1), "v"(p.fa[qb]));
  if constexpr (MODE == 2)
    asm volatile("v_lshlrev_b32 v104, 16, v106\n\tv_and_b32 v105, 0xffff0000, v106\n\tv_fmac_f32 v%c0, v104, %2\n\tv_fmac_f32 v%c1, v105, %2"
                 ::"i"(t), "i"(t + 1), "v"(p.fb[qb]));
  asm volatile("v_cvt_pk_bf16_f32 v%c0, v%c1, v%c2" ::"i"(w), "i"(t), "i"(t + 1));
}
template <int I> DI void epi_store() {
  asm volatile("buffer_store_dwordx4 v[%c0:%c1], v84, s[88:91], s101 offen offset:%2 sc1" ::"i"(128 + 4 * I), "i"(131 + 4 * I), "i"((I & 3) * 1024) : "memory");
  if constexpr ((I & 3) == 3) asm volatile("s_addk_i32 s101, 4096");
}
constexpr int R_TB = 107, R_VS = 111;
template <int HF, int R> DI void p_read() {
  constexpr int mb = R >> 4, r = 64 * HF + 2 * R;
  asm volatile("ds_read_b64_tr_b16 a[%c1:%c2], v%c0 offset:%c3" ::"i"(R_TB + 2 * HF + mb), "i"(r), "i"(r + 1), "i"((R & 15) * 2048));
}
template <int QB, int E> DI void p_cvt() {
  asm volatile("v_cvt_pk_bf16_f32 v%c0, v%c1, v%c2" ::"i"(208 + 4 * QB + E), "i"(192 + 8 * QB + 2 * E), "i"(193 + 8 * QB + 2 * E));
}
template <int QB, int PR> DI void p_store() {
  asm volatile("buffer_store_dwordx4 v[%c0:%c1], v%c2, s[88:91], s96 offen offset:%3 sc1" ::"i"(208 + 4 * QB), "i"(211 + 4 * QB), "i"(R_VS + QB),
               "i"(PR * 128) : "memory");
}
constexpr int p_dma(int HF, int G) { return G == 3 && !HF ? 0 : G == 11 ? 1 - HF : G == 15 ? 2 - HF : G == 19 ? 3 - HF : G == 27 && HF ? 3 : -1; }
template <int HF, int G> DI void p_gap() {
  constexpr int ch = G >> 3, t = G & 7, a = 64 * HF + 32 * (ch & 1) + 4 * t, b = 128 + 4 * (2 * t + (ch >> 1)), d = 192 + 4 * ch;
  if constexpr (t == 0)
    asm volatile("v_mfma_f32_16x16x32_bf16 v[%c0:%c1], a[%c2:%c3], v[%c4:%c5], 0" ::"i"(d), "i"(d + 3), "i"(a), "i"(a + 3), "i"(b), "i"(b + 3));
  else
    asm volatile("v_mfma_f32_16x16x32_bf16 v[%c0:%c1], a[%c2:%c3], v[%c4:%c5], v[%c0:%c1]" ::"i"(d), "i"(d + 3), "i"(a), "i"(a + 3), "i"(b), "i"(b + 3));
  p_read<HF ^ 1, G>();
  if constexpr (G == 0) asm volatile("v_xor_b32 v%c0, s100, v%c0" ::"i"(R_TB + 2 * HF + 1));
  if constexpr (G == 17) asm volatile("v_xor_b32 v%c0, s100, v%c0" ::"i"(R_TB + 2 * (HF ^ 1)));
  if constexpr (G >= 4 && G <= 8 && G != 7) p_cvt<1, (G < 7 ? G - 4 : 3)>();
  if constexpr (G == 24 || (G == 7 && HF == 0)) asm volatile("s_waitcnt lgkmcnt(8)");
  if constexpr (G == 7 && HF == 1) asm volatile("s_waitcnt vmcnt(13) lgkmcnt(8)\n\ts_barrier" ::: "memory");
  if constexpr (G == 10) p_store<1, HF ^ 1>();
  if constexpr (G >= 20 && G < 24) p_cvt<0, G - 20>();
  if constexpr (G == 25) p_store<0, HF>();
  if constexpr (p_dma(HF, G) >= 0) dma_at<(HF ? 8 : 12) + p_dma(HF, G)>();
  if constexpr (HF == 0 && G == 2) asm volatile("s_bitset0_b32 m0, 12");
  if constexpr (HF == 0 && G == 13) asm volatile("s_add_u32 s96, s96, 0x40000");
  if constexpr (HF == 0 && G == 26) asm volatile("s_add_u32 s98, s98, 128");
  if constexpr (HF == 0 && G == 27) asm volatile("s_cmp_lt_u32 s98, 0x380");
  if constexpr (HF == 0 && G == 28) asm volatile("s_cselect_b32 s99, s99, 0x31000");
  if constexpr (HF == 1 && G == 1) asm volatile("s_xor_b32 s100, s100, 0x10000");
  if constexpr (HF == 1 && G == 2) asm volatile("s_xor_b32 s97, s97, s100");
  if constexpr (HF == 1 && G == 9) asm volatile("s_or_b32 m0, s97, s99");
  if constexpr (HF == 1 && G == 30) asm volatile("s_cmp_lt_u32 s98, 0x500");
  GAP();
}
template <int HF, int... Gs> DI void p_half(iseq<Gs...>) { (p_gap<HF, Gs>(), ...); }
constexpr int f_dma(int G) { return G == 2 ? 0 : G == 5 ? 1 : G == 8 ? 2 : G >= 12 && G <= 36 && G % 6 == 0 ? 1 + G / 6 : G >= 50 && G <= 68 && G % 6 == 2 ? G / 6 : -1; }
constexpr int f_ndma(int G) { int n = 0; for (int g = 0; g < G; ++g) n += f_dma(g) >= 0; return n; }
static_assert(f_ndma(74) == 12);
template <int MODE, int G> DI void gapF(AC& c, const EP& p) {
  if constexpr (MODE && G == 48) asm volatile("s_waitcnt lgkmcnt(6)\n\ts_barrier" ::: "memory");
  if constexpr (G < 64) gapB<0, false, G, !(MODE && G == 48)>(c);
  if constexpr (MODE && G >= 42 && G < 64) p_read<0, G - 42>();
  if constexpr (MODE && G >= 64 && G < 69) { p_read<0, 2 * G - 106>(); p_read<0, 2 * G - 105>(); }
  if constexpr (MODE && f_dma(G) >= 0) dma_at<8 + (f_dma(G) & 7)>();
  if constexpr (MODE == 2 && G >= 10 && (G - 10) % 16 == 0) asm volatile("s_waitcnt vmcnt(%0)" ::"i"(13 - (G - 10) / 4 + f_ndma(G + 1)) : "memory");
  if constexpr (G >= 10) epi_pair<(G - 10) / 4, (G - 10) & 3, MODE>(p);
  if constexpr (MODE == 3 && G < 64 && (G & 1)) {
    constexpr int db = G >> 2, qb = (G >> 1) & 1, o = 4 * (db * 2 + qb), a = 224 + 4 * (db & 1), b = 128 + 4 * ((db >> 1) * 2 + qb);
    if constexpr ((G & 7) == 1) asm volatile("s_waitcnt vmcnt(%0)" ::"i"(15 - (G >> 2) + f_ndma(G + 1)) : "memory");
    asm volatile("v_mfma_f32_16x16x32_bf16 a[%c0:%c1], v[%c2:%c3], a[%c4:%c5], a[%c0:%c1]" ::"i"(o), "i"(o + 3), "i"(a), "i"(a + 3), "i"(b), "i"(b + 3));
  }
  if constexpr (MODE == 0) {
    if constexpr (G >= 14 && ((G - 14) & 3) == 0) epi_store<(G - 14) / 4>();
    if constexpr (G == 73) epi_store<15>();
  } else {
    if constexpr (G == 0) asm volatile("s_or_b32 m0, s78, 0x1000\n\ts_movk_i32 s98, 256");
    if constexpr (G == 15) asm volatile("s_bitset0_b32 m0, 12");
    if constexpr (G == 48) asm volatile("s_or_b32 m0, s79, 0x1000\n\ts_movk_i32 s98, 384");
    if constexpr (G == 73) asm volatile("s_waitcnt vmcnt(4) lgkmcnt(0)" ::: "memory");
  }
  GAP();
}
template <int MODE, int... Gs> DI void phaseF(AC& c, const EP& p, iseq<Gs...>) { (gapF<MODE, Gs>(c, p), ...); }
template <int U, int K> DI void r_load(unsigned vo, unsigned so) {
  asm volatile("buffer_load_dwordx4 a[%c0:%c1], %2, s[88:91], %3 offen offset:%4 sc1" ::"i"(64 * U + 4 * K), "i"(64 * U + 4 * K + 3), "v"(vo),
               "s"(so + (K >> 2) * 4096), "i"((K & 1) * 512 + ((K >> 1) & 1) * 2048) : "memory");
}
template <int U, int K> DI void r_sum() {
  constexpr int d = 100 + 4 * (K & 1), b = 64 * U + 4 * K;
  if constexpr (K < 2) asm volatile("v_mfma_f32_16x16x32_bf16 v[%c0:%c1], v[96:99], a[%c2:%c3], 0" ::"i"(d), "i"(d + 3), "i"(b), "i"(b + 3));
  else asm volatile("v_mfma_f32_16x16x32_bf16 v[%c0:%c1], v[96:99], a[%c2:%c3], v[%c0:%c1]" ::"i"(d), "i"(d + 3), "i"(b), "i"(b + 3));
}
template <int U> DI void r_fin(unsigned vo, __amdgpu_buffer_rsrc_t srd, unsigned so) {
  asm volatile("" : "+s"(srd), "+s"(so));
  static_for([&](auto K) { r_sum<U, K>(); }, make_iseq<16>{});
  asm volatile("s_nop 7");
  static_for([&](auto E) { asm volatile("v_cvt_pk_bf16_f32 v%c0, v%c1, v%c2" ::"i"(116 + 4 * U + E), "i"(100 + 2 * E), "i"(101 + 2 * E)); }, make_iseq<4>{});
  asm volatile("buffer_store_dwordx4 v[%c3:%c4], %0, %1, %2 offen" OUTSCS ::"v"(vo), "s"(srd), "s"(so), "i"(116 + 4 * U), "i"(119 + 4 * U) : "memory");
}

#define CMAX 12
#define PREPX false
static_assert(NYT <= 16 && NYT >= 12);
enum { T_Q0, T_K0, T_OQ, T_OG, T_FPREP, T_FQ, T_FG, T_FS,   T_CNT = 9, T_NPRE = 10, T_NST = 12, T_TAB = 14, T_SLAB = 16, T_PART, T_IT = 18, T_ITN = 23 };
struct TRow { unsigned v[64]; };
constexpr u64 kvmask(int m0, int m1) { u64 n = 0; for (int m = m0; m <= m1; ++m) n |= 0x101ull << m; return n; }
constexpr u64 k_order(bool isY, int rr) {
  const int cls = (rr + (rr >> 2)) & 3;
  return !isY ? (cls == 0 ? 0xfedcb9875a463210ull : cls == 1 ? 0xefadc6982b574310ull : cls == 2 ? 0xfebad7693c285410ull : 0xefcba8764d392510ull)
              : (cls == 0 ? 0xfedcb987543a6210ull : cls == 1 ? 0xefadc698254b7310ull : cls == 2 ? 0xfebad769325c8410ull : 0xefcba876432d9510ull);
}
constexpr TRow t_row(int r, int wid, int j) {
  TRow t{};
  const int rr = r & 15, hsel = rr & 1, pj = rr >> 1, kv = pj & 1, h = j + 8 * hsel, u = hsel * 8 + j;
  const bool isY = r >= 16, solo = pj == 7;
  const int mt = (rr >> 2) + (isY ? 4 : 0), g = j * 128, NY = solo ? 18 : NYT;
  const int nx0 = 32 - 2 * pj - NY, cx = nx0 < CMAX ? nx0 : CMAX, ex = nx0 - cx, qbF = isY != (solo && SWAP7) ? 15 - pj : pj;
  const unsigned oQ = (unsigned)h << 20, oK = oQ + O_K, oP = O_P + (h * 8 + pj) * PSTR;
  if (wid == 4) {
    for (int l = 0; l < 64; ++l)
      t.v[l] = fo(l < 16 ? g + hsel * 16 + l : l < 24 ? g + 16 + hsel * 8 + l : l == 24 ? g + 48 + hsel * 8 + pj : l < 32 ? g + 64 + hsel * 8 + pj :
                  l < 48 ? 2304 + pj * 16 + l - 32 : 2048 + pj * 16 + l - 48);
    return t;
  }
  auto put64 = [&](int i, u64 x) { t.v[i] = (unsigned)x; t.v[i + 1] = (unsigned)(x >> 32); };
  t.v[T_Q0] = oQ + ((solo ? qbF : 15 - pj) * 128 + wid * 32) * 512;
  t.v[T_K0] = oK + (isY ? cx : 0) * 32768;
  t.v[T_OQ] = oQ + pj * 131072 + (wid >> 1) * 65536 + (wid & 1) * 4096;
  t.v[T_OG] = oK + kv * TSB + mt * 131072 + (wid >> 1) * 65536 + (wid & 1) * (kv ? 8192 : 4096);
  t.v[T_FPREP] = fo(2048 + pj * 16 + u);
  t.v[T_FQ] = fo(g + 32 + hsel * 8 + pj);
  t.v[T_FG] = fo(g + hsel * 16 + kv * 8 + mt);
  t.v[T_FS] = fo(1024 + qbF * 16 + j * 2 + hsel);
  t.v[T_CNT] = fo(2560 + qbF * 16);
  t.v[T_ZO] = O_Z + pj * 64 * SEQ * 2 + (u * 4 + wid) * 64;
  const u64 own = 0x1ull << (kv * 8 + mt), needQ = isY ? 1ull << (16 + (qbF >> 1)) : (1ull << (16 + (pj >> 1))) | (1ull << (16 + ((15 - pj) >> 1)));
  put64(T_NPRE, isY ? needQ | (1ull << (cx >> 2)) | (1ull << ((cx + 1) >> 2)) : solo ? needQ : needQ | 0xffffull << 48);
  put64(T_NST, isY ? kvmask(cx >> 2, 3) | (solo ? 0 : 1ull << 25) : kvmask(0, solo ? 3 : 0));
  put64(T_TAB, k_order(isY, rr));
  t.v[T_SLAB] = O_S + qbF * 0x200000 + wid * 65536 + (j * 2 + hsel) * 1024;
  t.v[T_PART] = oP;
  for (int it = 0; it < 2; ++it) {
    unsigned* p = t.v + T_IT + it * T_ITN;
    const bool last = isY || it == 1;
    const int n = solo ? 2 * qbF + 2 : isY ? NY : it ? 2 * pj + 2 : nx0;
    const u64 need2 = solo ? 0 : kvmask(0, (2 * pj + 1) >> 2) | (0xffffull << 32);
    u64 q[3] = {0, 0, 0};
    unsigned jmp[2] = {0, 0};
    if (isY) {
      p[0] = (solo && SWAP7 ? 0 : 1u << (14 - cx)) | (solo ? 0 : 1u << (n - 2));
      q[0] = kvmask(4, (31 - 2 * pj) >> 2) & ~own; jmp[0] = ex * 32768; q[1] = solo ? 0 : 1ull << 24;
      p[1] = 2; p[13] = t.v[T_FG]; p[14] = fo(g + 96 + r);
    } else if (it == 0) {
      p[0] = 2u | (ex ? 1u << (cx - 2) : 0);
      q[0] = kvmask(0, cx > 0 ? (cx - 1) >> 2 : 0);
      if (ex) { q[1] = kvmask(4, 4 + ((ex - 1) >> 2)); jmp[1] = (16 - cx) * 32768; q[2] = need2; } else q[1] = need2;
      p[1] = 2; p[13] = fo(2304 + pj * 16 + u) + j * (8 * 4352 - 8); p[14] = fo(g + 64 + hsel * 8 + pj);
    } else {
      p[0] = p[1] = solo ? 0 : 1;
      q[0] = need2;
      if (solo && SWAP7) { p[0] = 1u << 14; q[0] = kvmask(4, 4); } p[13] = fo(g + 48 + hsel * 8 + pj); p[14] = fo(g + 96 + r);
    }
    p[2] = 1u << (n - 2);
    p[3] = !last ? 0 : n > 2 ? 1u << (n - 3) : 1u;
    p[4] = !last ? 0 : n > 2 ? 3u << (n - 3) : 1u << 31;
    for (int k = 0; k < 3; ++k) { p[5 + 2 * k] = (unsigned)q[k]; p[6 + 2 * k] = (unsigned)(q[k] >> 32); }
    p[11] = jmp[0]; p[12] = jmp[1];
    p[15] = oK;
    p[16] = (last ? 1 : 0) | (isY && !solo ? 2 : 0) | (n == 2 ? 4 : 0) | (isY || solo ? 0 : 8) | (n - 1) << 8 | ((r >> 1) * 4 + wid) << 16;
    p[17] = wid * 32 - (n == 2 ? 64 : 0);
    p[18] = oQ + (pj * 128 + wid * 32) * 512;
    p[19] = 0;
    p[20] = (p[0] | (last ? 0 : 1u << n)) >> 1;
    p[21] = oK + (isY ? cx : 0) * 32768;
    p[22] = ((p[0] | p[1] | p[2] | p[3] | p[4]) & 0x7fffffffu) << 1;
  }
  return t;
}
template <int I> constexpr TRow t_base = I < 160 ? t_row(I / 5, I % 5, 0) : [] {
  TRow a = t_row(0, (I - 160) * 4, 0), b = t_row(0, (I - 160) * 4, 1);
  for (int l = 0; l < 64; ++l) a.v[l] = b.v[l] - a.v[l];
  return a;
}();
template <int I> constexpr bool t_linear = [] {
  for (int j = 1; j < 8; ++j) {
    const TRow b = t_row(I / 5, I % 5, j);
    for (int l = 0; l < 64; ++l) if (b.v[l] != t_base<I>.v[l] + j * t_base<160 + (I % 5) / 4>.v[l]) return false;
  }
  return true;
}();
struct Tab { TRow row[162]; };
template <int... I> constexpr Tab t_make(iseq<I...>) {
  static_assert((t_linear<(I < 160 ? I : 0)> && ...));
  return Tab{{t_base<I>...}};
}
__device__ const Tab g_tab = t_make(make_iseq<162>{});

typedef __attribute__((ext_vector_type(16))) unsigned u32x16;
template <class T, int I> DI T* arg_late() {
  unsigned lo, hi;
  asm volatile("v_readlane_b32 %0, v77, %2\n\tv_readlane_b32 %1, v77, %3\n\ts_nop 4" : "=s"(lo), "=s"(hi) : "i"(2 * I), "i"(2 * I + 1));
  return (T*)(((u64)hi << 32) | lo);
}
__global__ __launch_bounds__(256, 1) __attribute__((amdgpu_num_vgpr(77), amdgpu_num_sgpr(56))) void fused_kernel(
    const bf16* ckv_, const bf16* wuk_, const bf16* wuv_, const bf16* x_, const bf16* wq_, bf16* ws_, const bf16* ak_, const bf16* av_,
    const bf16* ash_, const bf16* assmk_, const bf16* assmD_, const bf16* wo_, const bf16* aq_, bf16* out_) {
  extern __shared__ __attribute__((aligned(16))) char lds[];
  const void* kap = (const void*)__builtin_amdgcn_kernarg_segment_ptr();
  u32x16 ka; u64 ka8;
  asm volatile("at_code_begin:\n\ts_load_dwordx16 %0, %2, 0\n\ts_load_dwordx2 %1, %2, 0x40" : "=&s"(ka), "=&s"(ka8) : "s"(kap) : ACLOB_ALL, "v255", "s101");
  GAP();
  const int tid = threadIdx.x, lane = tid & 63;
  const int wid = __builtin_amdgcn_readfirstlane(tid >> 6);
  const int b = blockIdx.x, j = b & 7, r = b >> 3, rr = r & 15, hsel = rr & 1, pj = rr >> 1, kv = pj & 1;
  const bool isY = r >= 16, solo = pj == 7, doP = isY != PREPX && !solo, doA = !isY && !solo;
  const int h = j + 8 * hsel, mt = (rr >> 2) + (isY ? 4 : 0), pu = (hsel * 8 + j) * 4 + wid;
  constexpr unsigned m0d = 131072 + 24576;
  const unsigned lA = isY ? 65536 : 0, lW = lA ^ 65536, po = isY ? 0 : 32768;
  const unsigned mp[4] = {lA + wid * 1024 + po, lW + wid * 1024 + po, lA + wid * 1024 + (po ^ 32768), lW + wid * 1024 + (po ^ 32768)};
  {
    const int lda = isY ? DA : DC;
    const unsigned ra = wid * 8 + (lane >> 3), sa = ((lane & 7) ^ (((wid & 1) * 4 + (lane >> 4)) & 7)) * 16;
    const unsigned vbA = ra * (lda * 2) + sa, vbB = (4 * wid + (lane >> 4)) * LDW * 2 + 16 * ((lane & 15) ^ (((lane >> 4) << 2) | wid));
    vsetu<92>(vbA); vsetu<93>(vbB); vsetu<90>(vbA + 128); vsetu<91>(vbB + 0x80000); vsetu<94>(ra * (DC * 2) + sa);
    static_for([&](auto J) {
      sset<64 + J>((J + 1) * 64 * lda);
      asm volatile("s_mov_b32 s%c0, %1" ::"i"(71 + J), "i"((((J + 1) >> 2) * 32 + 16 * ((J + 1) & 1)) * LDW * 2 + (((J + 1) >> 1) & 1) * 256));
    }, make_iseq<7>{});
    sset<78>(lA + wid * 1024); sset<79>(lW + wid * 1024);
    GAP();
  }
  asm volatile("s_waitcnt lgkmcnt(0)" : "+s"(ka), "+s"(ka8));
  auto arg = [&](int i) { return (const bf16*)(((u64)ka[2 * i + 1] << 32) | ka[2 * i]); };
  const bf16 *ckv = arg(0), *wuk = arg(1), *wuv = arg(2), *x = arg(3), *wq = arg(4), *ak = arg(6), *av = arg(7), *ash = (const bf16*)ka8;
  bf16* Qb = (bf16*)arg(5);
  KST(0, 0);
  bf16* xqT = (bf16*)((char*)Qb + O_X);
  const bf16* gA = ckv + (size_t)mt * 256 * DC;
  const bf16* gW = (kv ? wuv : wuk) + h * 256;
  unsigned dv = 0;
  {
    GM g;
    PR pr;
    if (doP) prep_load(pr, tid, pj, hsel * 8 + j, ak, av, ash);
    srd_set<56>(isY ? x + (size_t)pj * 256 * DA : gA, isY ? 256 * DA * 2 : 256 * DC * 2);
    srd_set<60>(isY ? wq + h * 256 : gW, isY ? (DA - 1) * LDW * 2 + 512 : (DC - 1) * LDW * 2 + 512);
    g_pro<0>(mp, make_iseq<12>{});
    srd_set<88>(Qb, WSB);
    {
      const unsigned* tp = g_tab.row[r * 5].v;
      asm volatile("buffer_load_dwordx2 v[120:121], off, s[88:91], %0 sc1\n\tglobal_load_dword v78, %1, %3\n\tglobal_load_dword v79, %2, %3 offset:1024\n\t"
                   "global_load_dword v118, %2, %4\n\tglobal_load_dword v119, %2, %4 offset:256\n\tglobal_load_dword v77, %5, %6"
                   ::"s"(fo(6144)), "v"(tid * 4), "v"(lane * 4), "s"(tp), "s"(g_tab.row[160].v), "v"((lane < 27 ? lane : 27) * 4), "s"(kap) : "memory");
    }
    if (doP) {
      asm volatile("s_waitcnt vmcnt(18)" ::: "memory");
#pragma unroll
      for (int q = 0; q < 7; ++q) asm volatile("" : "+v"(pr.kk[q]));
#pragma unroll
      for (int q = 0; q < 4; ++q) asm volatile("" : "+v"(pr.vv[q]));
      static_for([&](auto Ic) __attribute__((always_inline)) {
        constexpr int I = Ic;
        g_pro<12 + 4 * I>(mp, make_iseq<4>{});
        GAP();
        prep_math<I>(pr, tid, lds + 131072);
        GAP();
      }, make_iseq<4>{});
      asm volatile("s_waitcnt lgkmcnt(0)\n\ts_barrier" ::: "memory");
      prep_store(tid, pj, hsel * 8 + j, xqT, lds + 131072);
    } else g_pro<12>(mp, make_iseq<4>{});
    GAP();
    CODE_TOUCH("at_code_begin", "pf_code_end", r * 4 + wid, 128u, 8, m0d);
    {
      const int fr = lane & 15, fq = lane >> 4, q = (lane & 15) >> 2, p = lane & 3;
#pragma unroll
      for (int k2 = 0; k2 < 2; ++k2)
        g.aA[k2] = lA + (wid >> 1) * 16384 + (fr >> 3) * 1024 + (fr & 7) * 128 + (((4 * k2 + fq) ^ (fr >> 1)) * 16);
#pragma unroll
      for (int hf = 0; hf < 2; ++hf)
#pragma unroll
        for (int nf = 0; nf < 8; ++nf)
          g.aB[hf][nf] = lW + (wid & 1) * 8192 + off_b(8 * fq + 4 * hf + q, nf * 2 + (p >> 1)) + 8 * (p & 1);
      asm volatile("" : "+v"(g.aA[0]), "+v"(g.aA[1]));
#pragma unroll
      for (int nf = 0; nf < 8; ++nf) asm volatile("" : "+v"(g.aB[0][nf]), "+v"(g.aB[1][nf]));
      vsetu<81>(tid == 0 ? 0u : 0x80000000u);
      vsetu<84>(lane * 16); vsetu<85>(lane * 16 + 65536); vsetu<86>(wid * 8192 + lane * 16); vsetu<87>(wid * 8192 + lane * 16 + 4096);
      vsetu<88>(65536 + wid * 256 + fr * 16); vsetu<89>(fr - 4 * fq);
      asm volatile("s_mov_b64 s[92:93], s[88:89]\n\ts_mov_b64 s[94:95], s[90:91]");
    }
    if (doP) asm volatile("s_waitcnt vmcnt(13)" ::: "memory");
    else {
      g_pro<16>(mp, make_iseq<12>{});
      asm volatile("s_waitcnt vmcnt(8)" ::: "memory");
    }
    asm volatile("s_barrier" ::: "memory");
    if (isY) g_read0<0>(g, make_iseq<16>{});
    else g_read0<1>(g, make_iseq<16>{});
    GAP();
    {
      u64 z = (((u64)__float_as_uint(vget<121>()) << 32) | __float_as_uint(vget<120>())) + 0x9e3779b97f4a7c15ull;
      z = (z ^ (z >> 30)) * 0xbf58476d1ce4e5b9ull; z = (z ^ (z >> 27)) * 0x94d049bb133111ebull; z ^= z >> 31;
      vsetu<82>((unsigned)z); vsetu<83>((unsigned)(z >> 32));
      asm volatile("v_mad_u32_u24 v78, v118, %0, v78\n\tv_mad_u32_u24 v79, v119, %0, v79\n\tv_readfirstlane_b32 s84, v82\n\tv_readfirstlane_b32 s85, v83\n\t"
                   "v_readlane_b32 s80, v78, %1\n\tv_readlane_b32 s81, v78, %2" ::"s"(j), "i"((int)T_TAB), "i"(T_TAB + 1));
      if (isY) asm volatile("s_mov_b64 s[80:81], 0x6543210");
      asm volatile("s_bfe_u32 s82, s80, 0x40008\n\ts_lshr_b64 s[80:81], s[80:81], 12\n\tv_lshl_add_u32 v91, s82, 19, v93" ::: "scc");
    }
    asm volatile("s_waitcnt lgkmcnt(0)\n\ts_barrier" ::: "memory");
    g_pro<28>(mp, make_iseq<4>{});
    GAP();
    if (isY) {
      g_tile<false, 0, 1, NTW >= 1>(g);
      g_loop<false, true>(g, PREPX ? 0x20u : 0x22u, [&](int t) __attribute__((always_inline)) {
        if (t == 1) fl_set<T_FPREP>();
        else if (t == 4) { tload<80, T_TAB>(); tload<81, T_TAB + 1>(); srd_set<60>(gW, (DC - 1) * LDW * 2 + 512); }
        else {
          srd_set<56>(gA, 256 * DC * 2);
          asm volatile("v_mov_b32 v92, v94");
          static_for([&](auto J) { asm volatile("s_mov_b32 s%c0, %1" ::"i"(64 + J), "i"((J + 1) * 64 * DC)); }, make_iseq<7>{});
        }
      });
      tload<101, T_OQ>(); sset<83>(__float_as_uint(CS));
      asm volatile("v_mov_b32 v112, v84\n\tv_add_u32 v113, 8192, v84");
      GAP();
      g_tile<false, 0, 2, NTW >= 2>(g);
      KST(0, 3);
    }
#define TT 3
    auto kvtile = [&](auto TRc) __attribute__((always_inline)) {
      constexpr bool TR = TRc;
      if (isY) g_tile<TR, 1, 4, NTW >= 2>(g);
      else g_tile<TR, 1, 1, NTW >= 2>(g);
      g_loop<TR, false>(g, 0x2000u | (isY || doA ? 2u : 1u << TT), [&](int t) __attribute__((always_inline)) {
        int l = threadIdx.x & 63; asm volatile("" : "+v"(l));
        if (t == 13) {
          asm volatile("s_mov_b32 s79, 0x28000");
          fl_poll<T_NPRE>();
          if ((b & 15) == 0)
            asm volatile("v_readlane_b32 s48, v78, %0\n\tv_and_b32 v122, 0xffffff00, v82\n\tv_mov_b32 v123, v83\n\ts_nop 2\n\t"
                         "buffer_store_dwordx2 v[122:123], v81, s[88:91], s48 offen sc1" ::"i"((int)T_CNT) : "memory");
          if (!doA && !isY) {
            CODE_TOUCH("at_code_begin", "pf_code_end", hsel * 8 + wid * 2, 16u, 64, m0d);
            CODE_TOUCH("at_code_begin", "pf_code_end", hsel * 8 + wid * 2 + 1, 16u, 64, m0d);
          }
          if (doA) {
            const char* tp = arg_late<const char, 9>() + (size_t)(pj * 64 + pu) * (SEQ * 2) + (l & 31) * 128;
            asm volatile("s_mov_b32 m0, %1\n\ts_nop 0\n\tglobal_load_lds_dword %0, off" ::"v"(tp), "i"(m0d) : "memory");
          }
        } else if (isY) fl_set<T_FQ>();
        else if (doA) { if (PREPX) fl_set<T_FPREP>(); post_dma(pj, pu, arg_late<const bf16, 12>(), 131072 + wid * 8192); }
        else {
          const char* tp = arg_late<const char, 0>() + 1024 * DC * 2 + (pu * 256 + l) * 128;
          asm volatile("s_mov_b32 m0, %1\n\ts_nop 0\n\tglobal_load_lds_dword %0, off" ::"v"(tp), "i"(m0d) : "memory");
          asm volatile("global_load_lds_dword %0, off" ::"v"(tp + 8192) : "memory");
          asm volatile("global_load_lds_dword %0, off" ::"v"(tp + 16384) : "memory");
          asm volatile("global_load_lds_dword %0, off" ::"v"(tp + 24576) : "memory");
        }
      });
      tload<101, T_OG>(); tload<96, T_Q0>(); tload<98, T_K0>();
      fl_check();
      tload<50, T_NST>(); tload<51, T_NST + 1>();
      asm volatile("v_mov_b32 v112, v84\n\tv_add_u32 v113, %0, v84\n\tv_mov_b32 v90, v87\n\tv_mov_b32 v91, v87\n\tv_mov_b32 v92, v87\n\tv_mov_b32 v93, v87\n\t"
                   "v_mov_b32 v94, v86\n\tv_mov_b32 v95, v86\n\tv_mov_b32 v126, v86\n\tv_mov_b32 v127, v86\n\ts_mov_b32 s99, 0x8000\n\t" FL_ARM("s[50:51]")
                   ::"i"(TR ? 4096 : 8192));
      if (doA) {
        const int d = pj * 64 + pu;
        const bf16* dp = arg_late<const bf16, 10>() + d;
        asm volatile("global_load_ushort %0, %1, off" : "=v"(dv) : "v"(dp) : "memory");
        srd_set<56>(arg_late<const bf16, 9>() + (size_t)d * SEQ, SEQ * 2);
        srd_set<60>(arg_late<const char, 5>() + O_X + d * SEQ * 2, SEQ * 2);
        sset<78>(65536 + wid * 15488);
        asm volatile("s_movk_i32 s64, 992\n\tv_subrev_u32 v122, 32, v84");
        GAP();
        g_tile<TR, 0, 3>(g);
      } else {
        sset<79>(isY ? wid * 8192 : 0x30000);
        if ((NYT & 1) && !solo) asm volatile("s_add_u32 s98, s98, 0x8000\n\ts_mov_b32 s97, 0xffff8000" ::: "scc");
        else asm volatile("s_mov_b32 s97, 0x8000");
        GAP();
        g_tile<TR, 0, 5>(g);
      }
    };
    if (kv) kvtile(std::true_type{});
    else kvtile(std::false_type{});
  }
  KST(0, 4);
  asm volatile("s_waitcnt vmcnt(16)" : "+v"(dv) :: "memory");
  sset<79>(wid * 8192);
  if (doA) {
    const float Dd = bf2f(dv);
    KST(0, 5);
    conv_wave<T_FG>(O_Z + (pj * 64 + pu) * SEQ * 2, Dd, lds + 65536 + wid * 15488);
    const char* tp = arg_late<const char, 11>() + (size_t)h * (HD * DA * 2) + ((pj * 4 + wid) * 64 + lane) * 128;
    asm volatile("s_mov_b32 m0, %1\n\ts_nop 0\n\tglobal_load_lds_dword %0, off\n\ts_waitcnt vmcnt(17)" ::"v"(tp), "i"(m0d) : "memory");
    KST(0, 6);
  } else if (!isY) {
    static_for([&](auto I) { d_store<16 + I>(); }, make_iseq<16>{});
    tload<98, T_K0>();
    asm volatile("s_waitcnt vmcnt(0)\n\ts_barrier" ::: "memory");
    fl_set<T_FG>();
    KST(0, 5);
    fl_check();
    CODE_TOUCH("att_begin", "pf_code_end", hsel * 4 + wid, 8u, 64, m0d);
    dma4k<0>(); dma4k<1>(); dma4k<2>(); dma4k<3>();
    asm volatile("s_waitcnt vmcnt(0)" ::: "memory");
    KST(0, 6);
  } else {
    tload<76, T_PART>();
    fl_check();
    asm volatile("buffer_load_dwordx2 v[122:123], v88, s[88:91], s76 offen offset:1024 sc1" ::: "memory");
  }
  AC c;
  sset<78>(65536 + wid * 8192); sset<79>(98304 + wid * 8192);
  { const bf16* wp = arg_late<const bf16, 11>() + (size_t)h * HD * DA; asm volatile("s_mov_b64 s[80:81], %0" ::"s"(wp)); }
  asm volatile("att_begin:");
  ev_code();
  asm volatile("s_waitcnt lgkmcnt(0)\n\ts_barrier" ::: "memory");
#pragma unroll 1
  for (int it = doA ? 0 : 1; it < 2; ++it) {
    const bool odd = (NYT & 1) && !solo && (isY || it == 0);
    if (odd) kreads<1>(make_iseq<8>{});
    else kreads<0>(make_iseq<8>{});
    const unsigned oP = tget<T_PART>();
    if (it == 0) {
      static_for([&](auto I) { tload<57 + I, T_IT + I>(); }, make_iseq<21>{});
      tload<S_T, T_IT + 21>(); tload<S_EV, T_IT + 22>();
      asm volatile("s_sub_u32 s70, s70, %0\n\ts_add_u32 s76, %1, 1024" ::"s"(j * (8 * 4352 - 8)), "s"(oP) : "scc");
    } else {
      static_for([&](auto I) { tload<57 + I, T_IT + T_ITN + I>(); }, make_iseq<21>{});
      tload<S_T, T_IT + T_ITN + 21>(); tload<S_EV, T_IT + T_ITN + 22>();
      asm volatile("s_mov_b32 s72, 0\n\ts_mov_b32 s75, %0\n\ts_mov_b32 s76, %1" ::"s"(oP + wid * 16384), "s"(oP + 1032));
    }
    unsigned mode;
    asm volatile("s_add_u32 s98, s97, 0x8000\n\ts_mov_b32 %0, s73\n\ts_mov_b32 s56, -1\n\tv_mov_b64 v[114:115], 0\n\tv_mov_b64 v[118:119], 0\n\t"
                 "v_mov_b32 v120, 0x45800000" : "=s"(mode) :: "scc");
    KST(0, 8 + 3 * (it && !isY));
    GAP();
#define STA(k)
#define STI()
    STA(0);
    if (odd) {
      if (isY) phaseA<1, 2>(c);
      else phaseA<1, 1>(c);
      STA(1);
      STA(2);
      phaseB0<1>(mode, wid);
      STA(3);
      asm volatile("s_bfe_u32 s76, s73, 0x80008\n\ts_branch att_mid" ::: "scc");
    } else {
      if (isY) phaseA<0, 2>(c);
      else phaseA<0, 1>(c);
      STA(1);
      if (c.rare) ev_call();
      STA(2);
      phaseB0<0>(mode, wid);
      if (__builtin_expect(mode & 4, 0)) { asm volatile("s_mov_b32 s56, 31"); ev_call(); asm volatile("s_mov_b32 s56, 0"); }
      STA(3);
    }
    GAP();
    asm volatile("s_bfe_u32 s76, s73, 0x80008\natt_loop:" ::: "scc");
    GAP();
    phaseA<1, 0>(c);
    STI();
    if (__builtin_expect(c.rare != 0, 0)) { if (redo()) sm_redo<1>(); ev_call(); }
    phaseB<1>(c);
    asm volatile("s_cbranch_scc1 att_done\natt_mid:");
    phaseA<0, 0>(c);
    STI();
    if (__builtin_expect(c.rare != 0, 0)) { if (redo()) sm_redo<0>(); ev_call(); }
    phaseB<0>(c);
    asm volatile("s_branch att_loop\natt_done:");
    GAP();
    STA(4);
    if ((mode & 3) == 1) phaseA<0, 4>(c);
    else {
      asm volatile("buffer_load_dwordx4 v[128:131], v88, s[88:91], %0 offen sc1" ::"s"(oP) : "memory");
      GAP();
      phaseA<0, 3>(c);
    }
    if (__builtin_expect(redo(), 0)) sm_redo<0>();
    STA(5);
    KST(0, 9 + 3 * (it && !isY));
    const float l[2] = {row_reduce<false>(vget<R_RS>() + vget<R_TS>()), row_reduce<false>(vget<R_RS + 1>() + vget<R_TS + 1>())};
    const float cm[2] = {vget<R_M>(), vget<R_M + 1>()};
    EP p;
    if (it == 0) {
      const f32x4 ml = {cm[0], l[0], cm[1], l[1]};
      asm volatile("buffer_store_dwordx4 %0, v88, s[88:91], %1 offen sc1\n\ts_mov_b32 s101, %2" ::"v"(ml), "s"(oP), "s"(oP + wid * 16384) : "memory");
      phaseF<0>(c, p, make_iseq<74>{});
      asm volatile(FL_POLL "\n\ts_waitcnt vmcnt(17)" ::: "memory");
    } else {
      int lz = threadIdx.x & 63; asm volatile("" : "+v"(lz));
      const int rw = lz >> 2;
      static_for([&](auto E) { vsetu<R_TB + E>(rw * 128 + 16 * ((2 * E + ((lz & 3) >> 1)) ^ (rw & 6)) + 8 * (lz & 1)); }, make_iseq<4>{});
      const unsigned vs = ((lz >> 3) & 1) * 16384 + ((lz >> 4) & 1) * 512 + (lz >> 5) * 256 + (lz & 7) * 16;
      vsetu<R_VS>(vs); vsetu<R_VS + 1>(vs + 32768);
      tload<96, T_SLAB>();
      asm volatile(TICKET ::"i"((int)T_CNT) : "memory");
      asm volatile("s_mov_b32 s97, s79\n\ts_mov_b32 s100, 0x8000\n\ts_movk_i32 s99, 0x1000");
      if (!(mode & 2)) {
        p.fa[0] = __builtin_amdgcn_rcpf(l[0]); p.fa[1] = __builtin_amdgcn_rcpf(l[1]);
        phaseF<1>(c, p, make_iseq<74>{});
      } else {
        const f32x4 ml = {vget<128>(), vget<129>(), vget<130>(), vget<131>()};
        if (__builtin_expect(__builtin_amdgcn_ballot_w64(ml[0] != cm[0] || ml[2] != cm[1]) == 0, 1)) {
          p.fa[0] = __builtin_amdgcn_rcpf(l[0] + ml[1]); p.fa[1] = __builtin_amdgcn_rcpf(l[1] + ml[3]);
          const unsigned one = (lz >> 4) != ((lz >> 2) & 3) ? 0u : (lz & 1) ? 0x3f800000u : 0x3f80u, o0 = (lz & 2) ? 0u : one, o1 = (lz & 2) ? one : 0u;
          vsetu<224>(o0); vsetu<225>(o1); vsetu<230>(o0); vsetu<231>(o1);
          asm volatile("v_mov_b64 v[226:227], 0\n\tv_mov_b64 v[228:229], 0");
          phaseF<3>(c, p, make_iseq<74>{});
        } else {
#pragma unroll
          for (int e = 0; e < 2; ++e) {
            const float mm = fmaxf(cm[e], ml[2 * e]);
            const float wa = __builtin_amdgcn_exp2f(cm[e] - mm), wb = __builtin_amdgcn_exp2f(ml[2 * e] - mm);
            const float inv = __builtin_amdgcn_rcpf(l[e] * wa + ml[2 * e + 1] * wb);
            p.fa[e] = wa * inv; p.fb[e] = wb * inv;
          }
          phaseF<2>(c, p, make_iseq<74>{});
        }
      }
    }
    KST(0, 10 + 3 * (it && !isY));
  }
  asm volatile("v_xor_b32 v%c0, s100, v%c0\np_loop:" ::"i"(R_TB));
  GAP();
  p_half<0>(make_iseq<32>{});
  p_half<1>(make_iseq<32>{});
  asm volatile("s_cbranch_scc1 p_loop\n\ts_nop 5");
  GAP();
  p_cvt<1, 0>(); p_cvt<1, 1>(); p_cvt<1, 2>(); p_cvt<1, 3>();
  p_store<1, 1>();
  GAP();
  KST(0, 27);
  {
    constexpr unsigned rdy = 98304;
    int lz = threadIdx.x & 63; asm volatile("" : "+v"(lz));
    const int fr = lz & 15, fq = lz >> 4, qbF = isY != (solo && SWAP7) ? 15 - pj : pj;
    if (wid == 0) {
#pragma unroll 1
      for (int q = 0; q < 0x100000; ++q) {
        unsigned lo, hi, tl, th;
        asm volatile("v_readfirstlane_b32 %0, v122\n\tv_readfirstlane_b32 %1, v123\n\ts_mov_b32 %2, s84\n\ts_mov_b32 %3, s85" : "=s"(lo), "=s"(hi), "=s"(tl), "=s"(th));
        if (__builtin_expect((((lo ^ tl) >> 8) | (hi ^ th)) == 0, 1)) break;
        asm volatile("s_sleep 4\n\t" TICKET "\n\ts_waitcnt vmcnt(0)" ::"i"((int)T_CNT) : "memory");
      }
      asm volatile("v_mov_b32 v123, 0\n\ts_mov_b64 exec, 1\n\tds_write_b64 %0, v[122:123]\n\ts_mov_b64 exec, -1" ::"v"(rdy) : "memory");
    }
    vsetu<79>(fo(1024) + qbF * 4352 + fr * 8);
    {
      const int jj = 4 * (fr >> 3) + (fr & 3);
      const unsigned one = ((fq ^ (fr >> 2)) & 1) ? 0u : (jj & 1) ? 0x3f800000u : 0x3f80u;
      static_for([&](auto I) { vsetu<96 + I>((jj >> 1) == I ? one : 0u); }, make_iseq<4>{});
    }
    unsigned vo = (fq >> 1) * 1024 + (fq & 1) * 256 + fr * 16;
    bf16* out = arg_late<bf16, 13>();
    __amdgpu_buffer_rsrc_t so_ = __builtin_amdgcn_make_buffer_rsrc((void*)out, (short)0, SEQ * OUTW * 2, 0x00020000);
    unsigned vw = ((qbF * 128 + (fr & 7)) * OUTW + DA + (fr >> 3) * 32 + fq * 8) * 2;
    asm volatile("" : "+s"(so_));
    asm volatile("s_waitcnt vmcnt(0) lgkmcnt(0)\n\ts_barrier" : "+v"(vo), "+v"(vw) :: "memory");
    fl_set<T_FS>();
    unsigned tk;
    asm volatile("ds_read_b32 %0, %1\n\ts_waitcnt lgkmcnt(0)" : "=v"(tk) : "v"(rdy) : "memory");
    tk = __builtin_amdgcn_readfirstlane(tk) & 255;
    KST(0, 30);
    if (doA) post_math(pj, pu, out, lds + 131072 + wid * 8192);
#define N3 0
#define N2 12
    constexpr int N1 = 28 - 3 * N3 - 2 * N2;
    static_assert(N1 >= 0 && N3 + N2 + N1 <= 16);
    const int nu = tk < N3 ? 3 : tk < N3 + N2 ? 2 : tk < N3 + N2 + N1 ? 1 : 0;
    const int u0 = 4 * (tk < N3 ? 3 * tk : tk < N3 + N2 ? N3 + 2 * tk : 2 * N3 + N2 + tk) + wid * nu;
    KST(0, 28);
    if (nu) {
      unsigned so[3];
#pragma unroll
      for (int k = 0; k < 3; ++k) so[k] = O_S + qbF * 0x200000 + 0x40000 + (u0 + k) * 16384;
      unsigned pend = 0x5555u, cnt = 0x400000;
      while (true) {
        unsigned nw;
        if (wid == 3) {
          u64 m;
          const u64 ex = pend | (pend << 1);
          asm volatile(FL_ARM("%1") "\n\t" FL_POLL "\n\ts_waitcnt vmcnt(0)\n\tv_cmp_eq_u64 %0, s[84:85], v[124:125]" : "=s"(m) : "s"(ex) : "memory");
          nw = (unsigned)m & ((unsigned)m >> 1) & pend;
          if (nw) asm volatile("ds_write_b32 %0, %1 offset:4" ::"v"(rdy), "v"(~pend | nw) : "memory");
        } else {
          unsigned t;
          asm volatile("ds_read_b32 %0, %1 offset:4\n\ts_waitcnt lgkmcnt(0)" : "=v"(t) : "v"(rdy) : "memory");
          nw = __builtin_amdgcn_readfirstlane(t) & pend;
        }
        if (--cnt == 0) nw = pend;
        if (nw == 0) {
          if (wid == 3) asm volatile("s_sleep 4");
          else asm volatile("s_sleep 1");
          continue;
        }
        asm volatile("s_nop 4");
        static_for([&](auto S) {
          if ((nw >> (2 * S)) & 1) {
            r_load<0, 2 * S>(vo, so[0]); r_load<0, 2 * S + 1>(vo, so[0]);
            if (nu > 1) { r_load<1, 2 * S>(vo, so[1]); r_load<1, 2 * S + 1>(vo, so[1]); }
            if (nu > 2) { r_load<2, 2 * S>(vo, so[2]); r_load<2, 2 * S + 1>(vo, so[2]); }
          }
        }, make_iseq<8>{});
        pend &= ~nw;
        if (pend == 0) break;
      }
      asm volatile("s_waitcnt vmcnt(0)" ::: "memory");
      KST(0, 16);
      auto oo = [&](int U) { return ((U & 15) * 8 * OUTW + (U >> 4) * 64) * 2; };
      r_fin<0>(vw, so_, oo(u0));
      if (nu > 1) r_fin<1>(vw, so_, oo(u0 + 1));
      if (nu > 2) r_fin<2>(vw, so_, oo(u0 + 2));
      if (qbF == 15 && tk == 0) asm volatile(FL_SET("%0") ::"s"(fo(6144)) : "memory");
    }
    KST(0, 29);
  }
  asm volatile("s_waitcnt vmcnt(0)" ::: "memory");
  asm volatile("pf_code_end:");
  KST(0, 1);
}

extern "C" void kernel_launch(void* const* d_in, const int* in_sizes, int n_in, void* d_out, int out_size, void* d_ws,
                              size_t ws_size, hipStream_t stream) {
  const bf16* Aq = (const bf16*)d_in[0];
  const bf16* Ak = (const bf16*)d_in[1];
  const bf16* Av = (const bf16*)d_in[2];
  const bf16* Ash = (const bf16*)d_in[3];
  const bf16* Assmk = (const bf16*)d_in[4];
  const bf16* AssmD = (const bf16*)d_in[5];
  const bf16* Bx = (const bf16*)d_in[6];
  const bf16* Bckv = (const bf16*)d_in[7];
  const bf16* Bwq = (const bf16*)d_in[8];
  const bf16* Bwuk = (const bf16*)d_in[9];
  const bf16* Bwuv = (const bf16*)d_in[10];
  const bf16* Bwo = (const bf16*)d_in[11];
  bf16* out = (bf16*)d_out;
  hipFuncSetAttribute((const void*)fused_kernel, hipFuncAttributeMaxDynamicSharedMemorySize, 163840);
  fused_kernel<<<256, 256, 163840, stream>>>(Bckv, Bwuk, Bwuv, Bx, Bwq, (bf16*)d_ws, Ak, Av, Ash, Assmk, AssmD, Bwo, Aq, out);
}
```

```cpp
#define SPINT 10000
#define PROBE 19
#define PSEL (doA)
#include <hip/hip_runtime.h>
#include <hip/hip_bf16.h>
#include <stdint.h>
#include <utility>

typedef __hip_bfloat16 bf16;
typedef __attribute__((ext_vector_type(4))) short bf16x4;
typedef __attribute__((ext_vector_type(2))) unsigned u32x2;

constexpr int SEQ = 2048;
constexpr int DA = 448;
constexpr int NH = 16;
constexpr int HD = 256;
constexpr int DC = 1024;
constexpr int OUTW = 896;
#define OUTSC 17
#define OUTSCS " sc0 sc1"
#define NTPS " nt"
#define NTVS " nt"
#define NTQS ""
#define NTW 1
#define NYT 15
#define SWAP7 1
constexpr unsigned TSB = NH * SEQ * HD * 2, O_K = TSB, O_S = 3 * TSB, O_P = 5 * TSB, PSTR = 67584, O_F = O_P + 128 * PSTR,
                   O_X = O_F + 400 * 4352, O_Z = O_X + DA * SEQ * 2, WSB = O_Z + DA * SEQ * 2;
constexpr unsigned fo(int w) { return O_F + (w >> 4) * 4352 + (w & 15) * 8; }

#define STAMPK2
#define KST(kid, which) do { if ((kid) == 0 && (which) == PROBE) { __builtin_amdgcn_sched_barrier(0); \
    asm volatile("s_memrealtime s[48:49]\n\ts_waitcnt lgkmcnt(0)\n\tv_writelane_b32 v77, s48, 40" ::: "memory"); __builtin_amdgcn_sched_barrier(0); } } while (0)

__device__ __forceinline__ float bf2f(unsigned short u) { return __uint_as_float(((unsigned)u) << 16); }
__device__ __forceinline__ unsigned short f2bf(float f) { return __bfloat16_as_ushort(__float2bfloat16(f)); }
__device__ __forceinline__ float rbf(float f) { return bf2f(f2bf(f)); }
typedef __attribute__((ext_vector_type(2))) __bf16 bf16x2n;
typedef __attribute__((ext_vector_type(2))) float f32x2n;
__device__ __forceinline__ unsigned pk2(float a, float b) {
  return __builtin_bit_cast(unsigned, __builtin_convertvector((f32x2n){a, b}, bf16x2n));
}

__device__ __forceinline__ int off_b(int row, int ch) { return 256 * row + 16 * (ch ^ (((row & 3) << 2) | ((row >> 2) & 3))); }

template <int R> __device__ __forceinline__ void sset(unsigned x) { asm volatile("s_mov_b32 s%c0, %1" ::"i"(R), "s"(x)); }
template <int R> __device__ __forceinline__ void vsetu(unsigned x) { asm volatile("v_mov_b32 v%c0, %1" ::"i"(R), "v"(x)); }
template <int R> __device__ __forceinline__ void srd_set(const void* p, unsigned n) {
  asm volatile("s_mov_b64 s[%c0:%c1], %4\n\ts_mov_b32 s%c2, %5\n\ts_mov_b32 s%c3, 0x20000" ::"i"(R), "i"(R + 1), "i"(R + 2), "i"(R + 3), "s"(p), "s"(n));
}
template <int R, int L> __device__ __forceinline__ void tload() { asm volatile("v_readlane_b32 s%c0, v78, %1" ::"i"(R), "i"(L)); }
template <int L> __device__ __forceinline__ unsigned tget() { unsigned t; asm volatile("v_readlane_b32 %0, v78, %1" : "=s"(t) : "i"(L)); return t; }
#define FL_SET(S) "buffer_store_dwordx2 v[82:83], v81, s[88:91], " S " offen sc1"
#define FL_ARM(N) "v_cndmask_b32_e64 v80, -2.0, v79, " N
#define FL_POLL "buffer_load_dwordx2 v[124:125], v80, s[88:91], 0 offen sc1"
#define FL_CHECK(N) "s_mov_b32 s48, 0x100000\n7:\n\tv_cmp_eq_u64 vcc, s[84:85], v[124:125]\n\ts_and_b64 vcc, vcc, " N "\n\ts_cmp_eq_u64 vcc, " N \
                    "\n\ts_cbranch_scc1 8f\n\ts_sleep 2\n\t" FL_POLL "\n\ts_waitcnt vmcnt(0)\n\ts_sub_u32 s48, s48, 1\n\ts_cbranch_scc0 7b\n8:"
template <int L> __device__ __forceinline__ void fl_set() {
  asm volatile("v_readlane_b32 s48, v78, %0\n\ts_nop 4\n\t" FL_SET("s48") ::"i"(L) : "memory");
}
template <int L> __device__ __forceinline__ void fl_poll() {
  asm volatile("v_readlane_b32 s50, v78, %0\n\tv_readlane_b32 s51, v78, %1\n\ts_nop 1\n\t" FL_ARM("s[50:51]") "\n\t" FL_POLL ::"i"(L), "i"(L + 1) : "memory");
}
#define TICKET "v_readlane_b32 s48, v78, %0\n\tv_mov_b32 v122, 1\n\tv_mov_b32 v123, 0\n\ts_nop 2\n\tbuffer_atomic_add_x2 v[122:123], v81, s[88:91], s48 offen sc0"
__device__ __forceinline__ void fl_check() { asm volatile(FL_CHECK("s[50:51]") ::: "vcc", "scc", "memory"); }
template <int R> __device__ __forceinline__ float vget() { float t; asm volatile("v_mov_b32 %0, v%c1" : "=v"(t) : "i"(R)); return t; }
template <int R> __device__ __forceinline__ void vset(float x) { asm volatile("v_mov_b32 v%c0, %1" ::"i"(R), "v"(x)); }
template <int R> __device__ __forceinline__ float acc_read1() { float t; asm volatile("v_accvgpr_read_b32 %0, a[%c1]" : "=v"(t) : "i"(R)); return t; }
template <class F, int... I> __device__ __forceinline__ void static_for(F&& f, std::integer_sequence<int, I...>) {
  (f(std::integral_constant<int, I>{}), ...);
}
template <int... Is> using iseq = std::integer_sequence<int, Is...>;
template <int N> using make_iseq = std::make_integer_sequence<int, N>;

typedef __attribute__((ext_vector_type(4))) unsigned u32x4c;
typedef __attribute__((ext_vector_type(4))) float f32x4c;
typedef __attribute__((ext_vector_type(2))) float f32x2c;
typedef unsigned long long u64;
struct PR { u32x4c kk[7], vv[4]; };
__device__ __forceinline__ void prep_load(PR& p, int tid, int cg, int tb, const bf16* __restrict__ k, const bf16* __restrict__ v,
                                          const bf16* __restrict__ shift_k) {
  const __amdgpu_buffer_rsrc_t sk = __builtin_amdgcn_make_buffer_rsrc((void*)k, (short)0, SEQ * DA * 2, 0x00020000);
  const __amdgpu_buffer_rsrc_t sv = __builtin_amdgcn_make_buffer_rsrc((void*)v, (short)0, SEQ * DA * 2, 0x00020000);
  const __amdgpu_buffer_rsrc_t sw = __builtin_amdgcn_make_buffer_rsrc((void*)(shift_k + cg * 256), (short)0, 512, 0x00020000);
  const int vo = ((tb * 128 + (tid >> 3) * 4) * DA + cg * 64 + (tid & 7) * 8) * 2;
#pragma unroll
  for (int q = 0; q < 7; ++q)
    asm volatile("buffer_load_dwordx4 %0, %1, %2, 0 offen" NTPS : "=v"(p.kk[q]) : "v"(vo + (q - 3) * DA * 2), "s"(sk) : "memory");
#pragma unroll
  for (int q = 0; q < 4; ++q)
    asm volatile("buffer_load_dwordx4 %0, %1, %2, 0 offen offset:%3" NTVS : "=v"(p.vv[q]) : "v"(vo), "s"(sv), "i"(q * DA * 2) : "memory");
#pragma unroll
  for (int q = 0; q < 4; ++q)
    asm volatile("buffer_load_dwordx4 a[%c0:%c1], %2, %3, 0 offen offset:%4" ::"i"(4 * q), "i"(4 * q + 3), "v"((tid & 7) * 64), "s"(sw), "i"(q * 16) : "memory");
}
__device__ __forceinline__ float bfl(unsigned u) { return __uint_as_float(u << 16); }
__device__ __forceinline__ float bfh(unsigned u) { return __uint_as_float(u & 0xffff0000u); }
__device__ __forceinline__ f32x2c rbf2(f32x2c a) {
  return (f32x2c){__uint_as_float(pk2(0.f, a[0])), __uint_as_float(pk2(0.f, a[1]))};
}
template <int I> __device__ __forceinline__ void prep_math(const PR& p, int tid, char* lx) {
  const int rp = tid >> 3, c = tid & 7;
  f32x2c wt[4], kf[7];
  const unsigned w[4] = {__float_as_uint(acc_read1<4 * I>()), __float_as_uint(acc_read1<4 * I + 1>()), __float_as_uint(acc_read1<4 * I + 2>()),
                         __float_as_uint(acc_read1<4 * I + 3>())};
#pragma unroll
  for (int t = 0; t < 4; ++t)
    wt[t] = (t & 1) ? (f32x2c){bfh(w[t >> 1]), bfh(w[2 + (t >> 1)])} : (f32x2c){bfl(w[t >> 1]), bfl(w[2 + (t >> 1)])};
#pragma unroll
  for (int q = 0; q < 7; ++q) kf[q] = (f32x2c){bfl(p.kk[q][I]), bfh(p.kk[q][I])};
  f32x2c x[4];
#pragma unroll
  for (int e = 0; e < 4; ++e) {
    f32x2c s = rbf2(kf[e] * wt[3]);
    s = rbf2(s + rbf2(kf[e + 1] * wt[2]));
    s = rbf2(s + rbf2(kf[e + 2] * wt[1]));
    s = rbf2(s + rbf2(kf[e + 3] * wt[0]));
    x[e] = s * (f32x2c){bfl(p.vv[e][I]), bfh(p.vv[e][I])};
  }
#pragma unroll
  for (int u = 0; u < 2; ++u)
    *(u32x2*)(lx + (c * 8 + 2 * I + u) * 256 + (((31 - rp) * 8) ^ (c << 4))) = (u32x2){pk2(x[3][u], x[2][u]), pk2(x[1][u], x[0][u])};
}
__device__ __forceinline__ void prep_store(int tid, int cg, int tb, bf16* __restrict__ xr, const char* lx) {
  const __amdgpu_buffer_rsrc_t sx = __builtin_amdgcn_make_buffer_rsrc((void*)(xr + (size_t)cg * 64 * SEQ), (short)0, 64 * SEQ * 2, 0x00020000);
#pragma unroll
  for (int rd = 0; rd < 4; ++rd) {
    const int idx = tid + 256 * rd, ch = idx >> 4, q = idx & 15;
    u32x4c w = *(const u32x4c*)(lx + ch * 256 + 16 * (q ^ (ch >> 3)));
    __builtin_amdgcn_raw_buffer_store_b128(w, sx, (ch * SEQ + (15 - tb) * 128 + q * 8) * 2, 0, 16);
  }
}

#define CODE_TOUCH(LB, LE, wx, NW, NL, m0v) do { unsigned blo_, bhi_, elo_; \
    asm volatile("s_getpc_b64 s[48:49]\n\ts_add_u32 s48, s48, " LB "@rel32@lo+4\n\ts_addc_u32 s49, s49, " LB "@rel32@hi+12\n\t" \
                 "s_mov_b32 %0, s48\n\ts_mov_b32 %1, s49\n\t" \
                 "s_getpc_b64 s[48:49]\n\ts_add_u32 s48, s48, " LE "@rel32@lo+4\n\ts_mov_b32 %2, s48" \
                 : "=s"(blo_), "=s"(bhi_), "=s"(elo_) :: "scc"); \
    const unsigned n_ = elo_ - (blo_ & ~127u); \
    const __amdgpu_buffer_rsrc_t srd_ = __builtin_amdgcn_make_buffer_rsrc((void*)(((unsigned long long)bhi_ << 32) | (blo_ & ~127u)), (short)0, n_ < 262144u ? n_ : 0u, 0x00020000); \
    const unsigned l_ = threadIdx.x & 63, vo_ = l_ < (NL) ? ((wx) + l_ * (NW)) * 128u : 0x80000000u; \
    asm volatile("s_mov_b32 m0, %0\n\ts_nop 4\n\tbuffer_load_dword %1, %2, 0 offen lds" ::"s"(m0v), "v"(vo_), "s"(srd_) : "memory"); } while (0)
template <int I> __device__ __forceinline__ void d_store() {
  asm volatile("buffer_store_dwordx4 v[%c0:%c1], v%c2, s[88:91], s101 offen offset:%3 sc1" ::"i"(128 + 4 * I), "i"(131 + 4 * I), "i"(112 + ((I >> 2) & 1)),
               "i"((I & 3) * 1024) : "memory");
  if constexpr ((I & 7) == 7) asm volatile("s_addk_i32 s101, 0x4000" ::: "scc");
}
constexpr int NS_X = 19;
struct CV { unsigned pa[4], pb, px, vz, sz; float D; };
constexpr int cv_J(bool up, int q) { int J = up ? 0 : 7; while (q > J) { q -= J + 1; J += up ? 1 : -1; } return J; }
constexpr int cv_i(bool up, int q) { int J = up ? 0 : 7; while (q > J) { q -= J + 1; J += up ? 1 : -1; } return q; }
template <int R, int N> __device__ __forceinline__ void cv_rd(CV& c) {
  if constexpr (N < 16) {
    constexpr int i = N >> 1, o = 16 * R + 128 * (i & 1) + 2 * (N & 1), r = 32 + 32 * (R & 1) + 2 * N;
    asm volatile("ds_read2_b32 a[%c1:%c2], %0 offset0:%c3 offset1:%c4" ::"v"(c.pa[i >> 1]), "i"(r), "i"(r + 1), "i"(o), "i"(o + 1));
  } else {
    constexpr int m = N - 16, r = 192 + 32 * (R & 1) + 4 * m;
    asm volatile("ds_read_b128 a[%c1:%c2], %0 offset:%c3" ::"v"(c.pb), "i"(r), "i"(r + 3), "i"(64 * R + 512 * (7 - m)));
  }
}
template <int O> __device__ __forceinline__ void cv_xd(CV& c) {
  constexpr int e = O & 31, x = 96 + ((e >> 1) ^ 1);
  if constexpr (O >= 64) asm volatile("v_cvt_pk_bf16_f32 v%c0, 0, v%c0" ::"i"(160 + e));
  else if constexpr (O >= 32) asm volatile("v_mul_f32 v%c0, v%c0, %1" ::"i"(160 + e), "v"(c.D));
  else if constexpr (O & 1) asm volatile("v_lshlrev_b32 v%c0, 16, v%c1" ::"i"(160 + e), "i"(x));
  else asm volatile("v_and_b32 v%c0, 0xffff0000, v%c1" ::"i"(160 + e), "i"(x));
}
template <int O> __device__ __forceinline__ void cv_z(CV& c) {
  constexpr int J = O / 11, k = O % 11, y = 128 + 4 * J;
  if constexpr (k < 4) asm volatile("v_cvt_pk_bf16_f32 v%c0, 0, v%c0" ::"i"(y + k));
  else if constexpr (k < 8) asm volatile("v_add_f32 v%c0, v%c0, v%c1" ::"i"(y + k - 4), "i"(y + 28 + k));
  else if constexpr (k < 10) asm volatile("v_cvt_pk_bf16_f32 v%c0, v%c1, v%c2" ::"i"(y + k - 8), "i"(y + 2 * k - 16), "i"(y + 2 * k - 15));
  else asm volatile("buffer_store_dwordx2 v[%c0:%c1], %2, s[88:91], %3 offen offset:%4 sc1" ::"i"(y), "i"(y + 1), "v"(c.vz), "s"(c.sz),
                    "i"(512 * J) : "memory");
}
constexpr int cv_z0(int Q) {
  int o = 0;
  for (int q = 0; q < Q; ++q)
    for (int k = 0; k < (q % 4 == 1 ? 0 : 3); ++k) { const int J = o / 11; if ((J + 1) * (J + 2) / 2 - 1 + (J ? 4 : 7) <= q) ++o; }
  return o;
}
constexpr int cv_xd0(int T) { int o = 0; for (int t = 0; t < T; ++t) o += t % 4 != 1 || (t >= 72 && t < 108); return o < 96 ? o : 96; }
static_assert(cv_xd0(144) == 96);
template <int B, int... Is> __device__ __forceinline__ void cv_zs(CV& c, iseq<Is...>) { (cv_z<B + Is>(c), ...); }
template <int R, int Q> __device__ __forceinline__ void cv_slot(CV& c) {
  constexpr int i = cv_i(R == 7, Q), J = cv_J(R == 7, Q), d = 128 + 4 * J, a = 32 + 32 * (R & 1) + 4 * i, b = 192 + 32 * (R & 1) + 4 * (J - i);
  constexpr int T = 36 * R + Q, vm = Q % 4 == 1 && Q < 32 ? Q / 4 : -1;
  if constexpr (Q == 0) asm volatile("s_waitcnt lgkmcnt(0)");
  if constexpr (R >= 6 && vm >= 0 && vm % 4 == 0) asm volatile("s_add_u32 m0, s79, %0" ::"i"(((R - 6) ^ (NYT & 1)) * 32768 + vm * 1024));
  if constexpr (R == 0 && i == 0)
    asm volatile("v_mfma_f32_16x16x32_bf16 v[%c0:%c1], a[%c2:%c3], a[%c4:%c5], 0" ::"i"(d), "i"(d + 3), "i"(a), "i"(a + 3), "i"(b), "i"(b + 3));
  else
    asm volatile("v_mfma_f32_16x16x32_bf16 v[%c0:%c1], a[%c2:%c3], a[%c4:%c5], v[%c0:%c1]" ::"i"(d), "i"(d + 3), "i"(a), "i"(a + 3), "i"(b), "i"(b + 3));
  if constexpr (Q < 24 && R < 7) cv_rd<R + 1, Q>(c);
  if constexpr (T % 7 == 3 && NS_X + 5 + T / 7 < 32) d_store<NS_X + 5 + T / 7>();
  if constexpr (R == 2 && Q >= 24 && Q < 32)
    asm volatile("ds_read_b64 v[%c1:%c2], %0 offset:%c3" ::"v"(c.px), "i"(96 + 2 * (Q - 24)), "i"(97 + 2 * (Q - 24)), "i"(512 * (31 - Q)));
  if constexpr (R >= 3 && R < 7) if constexpr (cv_xd0(T - 107) > cv_xd0(T - 108)) cv_xd<cv_xd0(T - 108)>(c);
  if constexpr ((R == 3 || R == 4) && vm >= 0) {
    constexpr int f = (R - 3) * 8 + vm;
    asm volatile("buffer_load_dwordx4 a[%c0:%c1], v84, s[88:91], s96 offen offset:%2 sc1" ::"i"(128 + 4 * f), "i"(131 + 4 * f), "i"((f & 3) * 1024) : "memory");
    if constexpr ((f & 3) == 3) asm volatile("s_addk_i32 s96, 4096");
  }
  if constexpr (R == 4 && Q == 35) asm volatile(FL_POLL ::: "memory");
  if constexpr (R >= 6 && vm >= 0)
    asm volatile("buffer_load_dwordx4 v%c0, s[88:91], s98 offen offset:%1 sc1 lds" ::"i"(86 + vm / 4), "i"((vm & 3) * 1024) : "memory");
  if constexpr (R == 6 && Q == 35) asm volatile("s_add_u32 s98, s98, 0x8000");
  if constexpr (R == 7) cv_zs<cv_z0(Q)>(c, make_iseq<cv_z0(Q + 1) - cv_z0(Q)>{});
  __builtin_amdgcn_sched_barrier(0);
}
template <int R, int... Q> __device__ __forceinline__ void cv_step(CV& c, iseq<Q...>) { (cv_slot<R, Q>(c), ...); }
template <int... N> __device__ __forceinline__ void cv_rd0(CV& c, iseq<N...>) { (cv_rd<0, N>(c), ...); }
template <int FLAG> __device__ __forceinline__ void conv_wave(unsigned sz, float Dd, char* lw) {
  int lane = threadIdx.x & 63; asm volatile("" : "+v"(lane));
  const int l16 = lane & 15, fq = lane >> 4;
  u32x4c w[5]; unsigned w4[5];
#pragma unroll
  for (int r = 0; r < 5; ++r) {
    w[r] = *(const u32x4c*)(lw + r * 1024 + lane * 16);
    w4[r] = *(const unsigned*)(lw + r * 1024 + lane * 16 + 16);
  }
  d_store<NS_X>(); d_store<NS_X + 1>();
#pragma unroll
  for (int r = 0; r < 5; ++r)
    *(u32x4c*)(lw + 10304 + r * 1024 + lane * 16) = (u32x4c){__builtin_amdgcn_alignbit(w[r][1], w[r][0], 16), __builtin_amdgcn_alignbit(w[r][2], w[r][1], 16),
                                                             __builtin_amdgcn_alignbit(w[r][3], w[r][2], 16), __builtin_amdgcn_alignbit(w4[r], w[r][3], 16)};
  d_store<NS_X + 2>();
  CV c;
#pragma unroll
  for (int i = 0; i < 4; ++i) c.pa[i] = (unsigned)(size_t)lw + ((l16 + 1) & 1) * 10304 + 4 * ((8 * fq + l16 + 1) >> 1) + 1024 * i;
  c.pb = (unsigned)(size_t)lw + 5120 + (2032 - 16 * l16 + 8 * fq) * 2 - 512 * 7;
  c.vz = (16 * l16 + 4 * fq) * 2;
  c.px = (unsigned)(size_t)lw + 5120 + 2044 * 2 - 512 * 7 - c.vz;
  c.sz = sz; c.D = Dd;
  asm volatile("" : "+v"(c.vz), "+v"(c.px), "+v"(c.D) :: "memory");
  asm volatile("" : "+s"(c.sz));
  cv_rd0(c, make_iseq<24>{});
  d_store<NS_X + 3>(); d_store<NS_X + 4>();
  __builtin_amdgcn_sched_barrier(0);
  static_for([&](auto R) __attribute__((always_inline)) {
    cv_step<R>(c, make_iseq<36>{});
    if constexpr (R == 2) {
      asm volatile("s_waitcnt vmcnt(0)\n\ts_barrier" ::: "memory");
      fl_set<FLAG>();
    }
    if constexpr (R == 5) {
      asm volatile("s_waitcnt vmcnt(0)" ::: "memory");
      fl_check();
    }
    __builtin_amdgcn_sched_barrier(0);
  }, make_iseq<8>{});
  asm volatile("s_nop 7");
  cv_zs<cv_z0(36)>(c, make_iseq<88 - cv_z0(36)>{});
}

__device__ __forceinline__ void post_dma(int cg, int tb, const bf16* __restrict__ q, unsigned m0w) {
  int lane = threadIdx.x & 63; asm volatile("" : "+v"(lane));
  const __amdgpu_buffer_rsrc_t sq = __builtin_amdgcn_make_buffer_rsrc((void*)(q + (size_t)tb * 32 * DA + cg * 64), (short)0, 32 * DA * 2, 0x00020000);
  const unsigned vq = ((lane >> 3) * DA + (lane & 7) * 8) * 2;
  asm volatile("s_mov_b32 m0, %0\n\ts_nop 4\n\tbuffer_load_dwordx4 %1, %2, 0 offen" NTQS " lds\n\tbuffer_load_dwordx4 %1, %2, %3 offen offset:1024" NTQS " lds\n\t"
               "buffer_load_dwordx4 %1, %2, %4 offen offset:2048" NTQS " lds\n\tbuffer_load_dwordx4 %1, %2, %5 offen offset:3072" NTQS " lds"
               ::"s"(m0w + 4096), "v"(vq), "s"(sq), "s"(8 * DA * 2 - 1024), "s"(16 * DA * 2 - 2048), "s"(24 * DA * 2 - 3072) : "memory");
}
__device__ __forceinline__ void post_math(int cg, int tb, bf16* __restrict__ out, const char* lw) {
  int lane = threadIdx.x & 63; asm volatile("" : "+v"(lane));
  const int tl = lane >> 3, ck = lane & 7;
  const __amdgpu_buffer_rsrc_t so = __builtin_amdgcn_make_buffer_rsrc((void*)(out + (size_t)tb * 32 * OUTW + cg * 64), (short)0, 32 * OUTW * 2 - 2 * DA, 0x00020000);
  const unsigned vo = (tl * OUTW + ck * 8) * 2;
#pragma unroll
  for (int i = 0; i < 4; ++i) {
    const u32x4c qv = *(const u32x4c*)(lw + 4096 + i * 1024 + lane * 16);
    const char* lz = lw + ck * 64 + 16 * (i ^ (ck >> 1)) + tl * 2;
    u32x4c o;
#pragma unroll
    for (int e = 0; e < 4; ++e) {
      const float z0 = bf2f(*(const unsigned short*)(lz + 2 * e * 512));
      const float z1 = bf2f(*(const unsigned short*)(lz + (2 * e + 1) * 512));
      o[e] = pk2(bfl(qv[e]) * z0, bfh(qv[e]) * z1);
    }
    __builtin_amdgcn_raw_buffer_store_b128(o, so, vo + i * 8 * OUTW * 2, 0, OUTSC);
  }
}

#define DI __device__ __forceinline__
#define GAP() __builtin_amdgcn_sched_barrier(0)
typedef __attribute__((ext_vector_type(4))) float f32x4g;
typedef __attribute__((ext_vector_type(4))) unsigned u32x4g;
constexpr int LDW = NH * HD;
struct GM { unsigned aA[2], aB[2][8]; };
template <bool W, int J, int V, bool NT = false> DI void g_dma() {
  if constexpr (NT && J == 0) asm volatile("buffer_load_dwordx4 v%c0, s[60:63], 0 offen nt lds" ::"i"(V) : "memory");
  else if constexpr (NT) asm volatile("buffer_load_dwordx4 v%c0, s[60:63], s%c1 offen nt lds" ::"i"(V), "i"(70 + J) : "memory");
  else
  if constexpr (J == 0) asm volatile("buffer_load_dwordx4 v%c0, s[%c1:%c2], 0 offen lds" ::"i"(V), "i"(W ? 60 : 56), "i"(W ? 63 : 59) : "memory");
  else asm volatile("buffer_load_dwordx4 v%c0, s[%c1:%c2], s%c3 offen lds" ::"i"(V), "i"(W ? 60 : 56), "i"(W ? 63 : 59), "i"((W ? 70 : 63) + J) : "memory");
}
template <int P, int KS, int I> DI void g_wread(GM& g) {
  constexpr int nf = I >> 1, hf = I & 1, r = 192 - 64 * P + 8 * nf + 4 * KS + 2 * hf;
  asm volatile("ds_read_b64_tr_b16 a[%c1:%c2], %0 offset:%c3" ::"v"(g.aB[hf][nf]), "i"(r), "i"(r + 1), "i"(P * 32768 + KS * 16384));
}
template <int P, int M, int KS> DI void g_aread(GM& g) {
  constexpr int r = 96 + 8 * (M & 1) + 4 * KS;
  asm volatile("ds_read_b128 v[%c1:%c2], %0 offset:%c3" ::"v"(g.aA[KS]), "i"(r), "i"(r + 3), "i"(P * 32768 + M * 2048));
}
constexpr float CS = 0.0625f * 1.4426950408889634f;
constexpr int NS_Q = 19;
constexpr int e_vm(int G) { return (G * 3) % 8 < 3 ? G * 3 / 8 : -1; }
constexpr int e_st(int MODE, int G) {
  const int q = G & 15, k3 = ((G >> 4) - 1) * 3 + (q == 0 ? 0 : q == 4 ? 1 : 2), k2 = NS_Q + (G >> 4) * 2 + (q == 4);
  if (MODE == 5) return e_vm(G) >= 32 ? e_vm(G) - 32 : -1;
  if (MODE == 3) return G >= 33 && (G - 33) % 5 == 0 && (G - 33) / 5 < NS_X ? (G - 33) / 5 : -1;
  if (MODE == 2) return G >= 30 && (q == 0 || q == 4 || q == 14) && k3 - 2 < NS_Q ? k3 - 2 : -1;
  if (MODE == 4) return (q == 0 || q == 4) && k2 < 32 ? k2 : -1;
  return -1;
}
constexpr int e_nst(int MODE, int G0, int G1) { int n = 0; for (int G = G0; G < G1; ++G) n += e_st(MODE, G) >= 0; return n; }
static_assert(e_nst(2, 0, 128) == NS_Q && e_nst(3, 30, 128) == NS_X && e_nst(5, 0, 128) == 16 && e_nst(4, 0, 104) == 32 - NS_Q);
constexpr int e_slot(int MODE, int G) { return MODE == 3 ? (G % 3 == 0 && G < 30 ? G / 3 : -1) : e_vm(G) >= 0 && e_vm(G) < 32 ? 10 + e_vm(G) : -1; }
struct ES { int op0[129], nop[129]; };
constexpr ES e_sched(bool TR, int MODE) {
  ES z{};
  const bool SCL = MODE == 2 || MODE == 4;
  const int per = SCL ? 16 : 8, o1 = 32 * per;
  int o = MODE == 4 ? e_sched(false, 2).op0[128] : 0;
  for (int G = 0; G <= 128; ++G) {
    const int q = G & 15;
    int cap = 2 - (q == 1 || q == 2 || q == 15);
    if (SCL) cap -= (q >= 3 && q < 11 && (q & 1)) + (q == 7 || q == 11) + 2 * (G == 70 || G == 74 || (G & 63) == 61);
    cap += MODE == 2 ? 4 + (q == 10 || q == 13) : MODE == 3 ? 1 : MODE == 5 ? 2 : 0;
    if (e_st(MODE, G) >= 0 || (SCL ? q == 8 || q == 12 : e_slot(MODE, G) >= 0) || (MODE == 4 && G < 4)) cap = 0;
    if (G == 128) cap = 10000;
    z.op0[G] = o;
    while (o < o1) {
      const int i = o / per, rdy = MODE == 4 ? 0 : (TR ? 32 * (i >> 3) + 16 + 2 * (i & 7) + 1 : 4 * i + 3) + 6;
      if ((G < rdy && G < 128) || cap-- <= 0) break;
      ++o; ++z.nop[G];
    }
  }
  return z;
}
template <bool TR, int MODE> constexpr ES ESCH = e_sched(TR, MODE);
static_assert(ESCH<false, 4>.op0[96] == 512 && ESCH<false, 4>.op0[0] >= 24 * 16);
template <bool TR, bool SCL, int O> DI void e_op() {
  constexpr int per = SCL ? 16 : 8, i = O / per, k = O % per, r = 128 + 4 * i, t = 114 + 4 * (i & 1), c = k - (per - 4);
  if constexpr (k < 4) asm volatile("v_accvgpr_read_b32 v%c0, a[%c1]" ::"i"(t + k), "i"(4 * i + k));
  else if constexpr (c < 0) asm volatile("v_mul_f32 v%c0, s83, v%c0" ::"i"(k < 8 ? r + (k ^ 6) : t + k - 8));
  else {
    constexpr int d = c == 0 ? r + 3 : c == 1 ? r + 2 : r + c - 2, x = c == 0 ? r + 2 : c == 1 ? r : t + 2 * (c - 2);
    asm volatile("v_cvt_pk_bf16_f32 v%c0, v%c1, v%c2" ::"i"(d), "i"(x), "i"(x + 1));
  }
}
template <bool TR, bool SCL, int B, int... Is> DI void e_ops(iseq<Is...>) { (e_op<TR, SCL, B + Is>(), ...); }
template <int S> DI void e_m0() {
  if constexpr (S == 0 || S == 1 || S == 5 || S == 9) asm volatile("s_add_u32 m0, s78, %0" ::"i"(S * 1024));
  if constexpr (S >= 26 && (S - 26) % 4 == 0) asm volatile("s_add_u32 m0, s79, %0" ::"i"(((S - 26) >> 3) * 32768 + ((S - 26) & 4) * 1024));
}
template <int S> DI void e_load() {
  if constexpr (S == 0) asm volatile("buffer_load_dwordx4 v122, s[56:59], 0 offen lds" ::: "memory");
  else if constexpr (S < 5) asm volatile("buffer_load_dwordx4 v84, s[56:59], s64 offen offset:%0 lds" ::"i"((S - 1) * 1024) : "memory");
  else if constexpr (S < 9) asm volatile("buffer_load_dwordx4 v84, s[60:63], 0 offen offset:%0 sc1 lds" ::"i"((S - 5) * 1024) : "memory");
  else if constexpr (S == 9) asm volatile("buffer_load_dwordx4 v84, s[60:63], s62 offen sc1 lds" ::: "memory");
  else if constexpr (S < 26) {
    constexpr int f = S - 10;
    asm volatile("buffer_load_dwordx4 a[%c0:%c1], v84, s[88:91], s96 offen offset:%2 sc1" ::"i"(128 + 4 * f), "i"(131 + 4 * f), "i"((f & 3) * 1024) : "memory");
    if constexpr ((f & 3) == 3) asm volatile("s_addk_i32 s96, 4096");
  } else {
    constexpr int f = S - 26;
    asm volatile("buffer_load_dwordx4 v%c0, s[88:91], s98 offen offset:%1 sc1 lds" ::"i"(86 + ((f >> 2) & 1)), "i"((f & 3) * 1024) : "memory");
    if constexpr (f == 7) asm volatile("s_add_u32 s98, s98, s97");
    if constexpr (f == 15) asm volatile(FL_POLL ::: "memory");
  }
}
template <bool TR, int P, int H, int G, int MODE, bool NT> DI void g_gap(GM& g) {
  constexpr int m = 4 * H + (G >> 4), q = G & 15, n = q >> 1, ks = q & 1, GG = 64 * H + G;
  constexpr bool inV = TR ? m & 1 : n & 1, END = MODE == 3 || MODE == 5;
  constexpr int a = 96 + 8 * (m & 1) + 4 * ks, b = 192 - 64 * P + 8 * n + 4 * ks, d = (inV ? 128 : 0) + 4 * (TR ? 8 * (m >> 1) + n : 4 * m + (n >> 1));
  constexpr int J = END ? -1 : q == 8 ? 2 * (G >> 4) : q == 12 ? 2 * (G >> 4) + 1 : -1, gj = (J & 3) ^ ((J & 3) >> 1);
  if constexpr (END) { if constexpr (q == 0 && GG > 0) asm volatile("s_waitcnt lgkmcnt(0)"); e_m0<e_slot(MODE, GG)>(); }
  else if constexpr (q == 0 && G > 0) asm volatile("s_waitcnt lgkmcnt(4)");
  if constexpr (J == 0) asm volatile("s_add_u32 m0, s78, %0" ::"i"((P ^ H ^ 1) * 32768 + (1 - H) * 8192));
  else if constexpr (J == 4) asm volatile("s_add_u32 m0, s79, %0" ::"i"((P ^ H) * 32768 + (1 - H) * 16384));
  else if constexpr (J > 0 && (J & 3) == 1) asm volatile("s_bitset1_b32 m0, 12");
  else if constexpr (J > 0 && (J & 3) == 2) asm volatile("s_bitset1_b32 m0, %0" ::"i"(J < 4 ? 14 : 13));
  else if constexpr (J > 0) asm volatile("s_bitset0_b32 m0, 12");
#define GMF(D, X, Y, C) asm volatile("v_mfma_f32_16x16x32_bf16 " D "[%c0:%c1], " X ", " Y ", " C ::"i"(d), "i"(d + 3), "i"(a), "i"(a + 3), "i"(b), "i"(b + 3))
#define GMF2(D, C) do { if constexpr (TR) GMF(D, "v[%c2:%c3]", "a[%c4:%c5]", C); else GMF(D, "a[%c4:%c5]", "v[%c2:%c3]", C); } while (0)
  if constexpr ((MODE == 1 || MODE == 4) && ks == 0) { if constexpr (inV) GMF2("v", "0"); else GMF2("a", "0"); }
  else if constexpr (inV) GMF2("v", "v[%c0:%c1]");
  else GMF2("a", "a[%c0:%c1]");
#undef GMF2
#undef GMF
  if constexpr ((q == 1 || q == 2) && !(END && m == 7)) g_aread<(m < 7 ? P : P ^ 1), (m + 1) & 7, q - 1>(g);
  if constexpr (!END && q >= 3 && q < 11 && (q & 1)) g_wread<P ^ 1, H, (G >> 4) * 4 + (q - 3) / 2>(g);
  if constexpr (e_st(MODE, GG) >= 0) {
    static_assert(ESCH<TR, MODE>.op0[GG] >= (e_st(MODE, GG) + 1) * (END ? 8 : 16));
    d_store<e_st(MODE, GG)>();
  }
  if constexpr (J >= 0 && J < 4) g_dma<false, (1 - H) * 2 + (gj & 1) + (gj >> 1) * 4, 90>();
  else if constexpr (J >= 4) g_dma<true, (1 - H) * 4 + gj, 91, NT>();
  if constexpr (MODE >= 2) e_ops<TR, !END, ESCH<TR, MODE>.op0[GG]>(make_iseq<ESCH<TR, MODE>.nop[GG]>{});
  if constexpr (END) {
    if constexpr (e_slot(MODE, GG) >= 0) e_load<e_slot(MODE, GG)>();
  } else {
    if constexpr (H == 1 && G == 6) asm volatile("v_lshl_add_u32 v90, s82, 7, v92\n\ts_and_b32 s82, s80, 15");
    if constexpr (H == 1 && G == 10) asm volatile("s_lshr_b64 s[80:81], s[80:81], 4\n\tv_lshl_add_u32 v91, s82, 19, v93");
    constexpr int nst = e_nst(MODE, H ? 29 : 0, GG + 1) + (MODE == 4 && !H ? e_nst(2, 93, 128) : 0);
    if constexpr (G == 61) asm volatile("s_waitcnt vmcnt(%0) lgkmcnt(0)\n\ts_barrier" ::"i"(12 + nst) : "memory");
  }
  if constexpr (END && GG == 127) {
    asm volatile("s_nop 7");
    e_ops<TR, false, ESCH<TR, MODE>.op0[128]>(make_iseq<ESCH<TR, MODE>.nop[128]>{});
  }
  GAP();
}
template <bool TR, int P, int H, int MODE, bool NT, int... Gs> DI void g_half_(GM& g, iseq<Gs...>) { (g_gap<TR, P, H, Gs, MODE, NT>(g), ...); }
template <bool TR, int P, int H, int MODE, bool NT = false> DI void g_half(GM& g) { g_half_<TR, P, H, MODE, NT>(g, make_iseq<64>{}); }
template <bool TR, int P, int MODE, bool NT = false> DI void g_tile(GM& g) { g_half<TR, P, 0, MODE, NT>(g); g_half<TR, P, 1, MODE, NT>(g); }
template <int P, int... Is> DI void g_read0(GM& g, iseq<Is...>) {
  (g_wread<P, 0, Is>(g), ...); (g_wread<P, 1, Is>(g), ...);
  g_aread<P, 0, 0>(g); g_aread<P, 0, 1>(g);
}
template <int I> DI void g_pro1(const unsigned (&m)[4]) {
  constexpr int grp = I >> 2, i = I & 3, t = grp < 4 ? 0 : grp < 7 ? 1 : 2;
  constexpr bool isA = grp == 0 || grp == 3 || grp == 5;
  constexpr int jj = (grp == 3 ? 2 : 0) + (i & 1) + (i >> 1) * 4, ks = grp == 2 || grp == 6;
  asm volatile("s_add_u32 m0, %0, %1\n\ts_nop 0" ::"s"(m[(t & 1) * 2 + !isA]), "i"(isA ? jj * 4096 : ks * 16384 + (i >> 1) * 8192 + (i & 1) * 4096) : "scc");
  if constexpr (isA) g_dma<false, jj, t ? 90 : 92>();
  else g_dma<true, ks * 4 + i, t ? 91 : 93, NTW >= 2>();
}
template <int B, int... Is> DI void g_pro(const unsigned (&m)[4], iseq<Is...>) { (g_pro1<B + Is>(m), ...); }
template <bool TR, bool SCL, class HOOK> DI void g_loop(GM& g, unsigned evm, HOOK&& hook) {
  constexpr int nkt = SCL ? 7 : 16, P0 = SCL ? 0 : 1;
  asm volatile("" : "+s"(evm));
#pragma unroll 1
  for (int t = 1;; t += 2) {
    g_half<TR, P0 ^ 1, 0, 0, (SCL ? NTW >= 1 : NTW >= 2)>(g);
    if (__builtin_expect((evm >> t) & 1, 0)) { hook(t); GAP(); }
    g_half<TR, P0 ^ 1, 1, 0, (SCL ? NTW >= 1 : NTW >= 2)>(g);
    if (SCL && t + 2 >= nkt) break;
    g_half<TR, P0, 0, 0, (SCL ? NTW >= 1 : NTW >= 2)>(g);
    if constexpr (SCL) { if (__builtin_expect(t == 3, 0)) { hook(4); GAP(); } }
    else asm volatile("s_cmp_eq_u32 %0, 13\n\ts_cselect_b32 s78, 0x28000, s78" ::"s"(t) : "scc");
    g_half<TR, P0, 1, 0, (SCL ? NTW >= 1 : NTW >= 2)>(g);
    if (!SCL && t + 3 >= nkt) break;
  }
}

typedef __attribute__((ext_vector_type(4))) unsigned u32x4;
typedef __attribute__((ext_vector_type(4))) float f32x4;

constexpr int R_S = 128, R_P = 192, R_CI = 224, R_E = 96;
constexpr int R_TS = 112, R_RS = 114, R_M = 116;
constexpr int R_PD = 118, R_THR = 120, R_TMP = 121;
constexpr int r_dk(int q) { return q < 6 ? 90 + q : 120 + q; }
constexpr int S_V = 96, S_T = 97, S_K = 98, S_I = 99, S_EV = 100;
struct AC {
  unsigned rare;
};
DI bool redo() { u64 t; asm volatile("s_mov_b64 %0, s[54:55]" : "=s"(t)); return t != 0; }
template <int X, int F> DI void kread() {
  constexpr int r = 192 + 4 * (F & 15);
  asm volatile("ds_read_b128 a[%c0:%c1], v84 offset:%c2" ::"i"(r), "i"(r + 3), "i"(X * 32768 + F * 1024));
}
template <int X, int F> DI void vread() {
  constexpr int r = 192 + 4 * (F & 15);
  asm volatile("ds_read_b128 a[%c0:%c1], v85 offset:%c2" ::"i"(r), "i"(r + 3), "i"(X * 32768 + (F & 1) * 16384 + (F >> 1) * 1024));
}
template <int X, int... Fs> DI void kreads(iseq<Fs...>) { (kread<X, Fs>(), ...); }
template <int X, int J> DI void dma_m0() {
  if constexpr (J == 0) asm volatile("s_mov_b32 m0, s%c0" ::"i"(78 + X));
  else if constexpr (J == 4) asm volatile("s_or_b32 m0, s%c0, 0x1000" ::"i"(78 + X));
  else if constexpr (J == 8) asm volatile("s_bitset0_b32 m0, 16");
  else if constexpr (J == 12) asm volatile("s_bitset0_b32 m0, 12");
}
template <int J> DI void dma_at() {
  if constexpr (J < 8)
    asm volatile("buffer_load_dwordx4 v%c0, s[88:91], s96 offen offset:%1 sc1 lds" ::"i"(86 + (J >= 4)), "i"((J & 3) * 1024) : "memory");
  else
    asm volatile("buffer_load_dwordx4 v%c0, s[92:95], s98 offen offset:%1 sc1 lds" ::"i"(r_dk(J - 8)), "i"((J & 3) * 1024) : "memory");
}
template <int Q> DI void dma4k() {
  asm volatile("s_add_u32 m0, s79, %0\n\ts_add_u32 s48, s98, %1\n\tbuffer_load_dwordx4 v%c2, s[88:91], s48 offen sc1 lds\n\t"
               "buffer_load_dwordx4 v%c2, s[88:91], s48 offen offset:1024 sc1 lds\n\tbuffer_load_dwordx4 v%c2, s[88:91], s48 offen offset:2048 sc1 lds\n\t"
               "buffer_load_dwordx4 v%c2, s[88:91], s48 offen offset:3072 sc1 lds"
               ::"i"((Q >> 1) * 32768 + (Q & 1) * 4096), "i"((Q >> 1) * 32768), "i"(86 + (Q & 1)) : "memory", "scc");
}
template <int Q> DI void acc_zero2() {
  asm volatile("v_accvgpr_write_b32 a[%c0], 0\n\tv_accvgpr_write_b32 a[%c1], 0" ::"i"(2 * Q), "i"(2 * Q + 1));
}
#define SMV 0
struct SMS { int ex[128], op0[128], nop[128], last; };
constexpr SMS sm_sched() {
  SMS z{};
  int et[32] = {}, e = 0;
  for (int t = 0; t < 128; ++t) {
    const int g = t & 15;
    const bool x = t < 48 ? (g >= 8 && (g & 1) && g < 15) : t < 64 ? (g >= 9 && g < 15) : (g >= 8 && g < 14);
    z.ex[t] = x && e < 32 ? e : -1;
    if (z.ex[t] >= 0) et[e++] = t;
  }
  int o = 0;
  for (int t = 0; t < 128; ++t) {
    const int g = t & 15;
    int cap = z.ex[t] >= 0 || t < 8 || (t < 48 && (g == 7 || (g >= 8 && !(g & 1)))) || t == 56 || t == 63 || t == 111 ? 0 : 1;
    z.op0[t] = o; z.nop[t] = 0;
    while (cap > 0 && o < 48 && et[2 * (o / 3) + (o % 3 == 0 ? 0 : 1)] + 2 <= t) {
      const int el = 2 * (o / 3) + o % 3;
      if (!(o % 3 < 2 && (el == 0 || el == 4))) --cap;
      ++o; ++z.nop[t]; z.last = t;
    }
  }
  for (int k = 16; k < 32; ++k) if (z.op0[et[k]] < 3 * ((k - 16) / 2 + 1)) z.last = 1000;
  return z;
}
constexpr SMS SM = sm_sched();
static_assert(SM.last <= 117);
constexpr int p_reg(int Y, int k) { return R_P + 16 * Y + 4 * ((k >> 3) * 2 + ((k >> 1) & 1)) + ((k >> 2) & 1) * 2 + (k & 1); }
template <int Y, int O> DI void sm_op() {
  constexpr int k = O / 3, e = 2 * k + O % 3, ts = R_TS + ((e >> 2) & 1);
  if constexpr (O % 3 == 2) asm volatile("v_cvt_pk_bf16_f32 v%c0, v%c1, v%c2" ::"i"(p_reg(Y, k)), "i"(R_E + ((2 * k) & 15)), "i"(R_E + ((2 * k + 1) & 15)));
  else if constexpr (e == 1 || e == 5) asm volatile("v_add_f32_e32 v%c0, v%c1, v%c2" ::"i"(ts), "i"(R_E + e - 1), "i"(R_E + e));
  else if constexpr (e != 0 && e != 4) asm volatile("v_add_f32_e32 v%c0, v%c0, v%c1" ::"i"(ts), "i"(R_E + (e & 15)));
}
template <int Y, int T> DI void sm_gap(AC& c) {
  if constexpr (SM.ex[T] >= 0) asm volatile("v_exp_f32 v%c0, v%c1" ::"i"(R_E + (SM.ex[T] & 15)), "i"(R_S + 32 * Y + SM.ex[T]));
  if constexpr (SM.nop[T] > 0) sm_op<Y, SM.op0[T]>();
  if constexpr (SM.nop[T] > 1) sm_op<Y, SM.op0[T] + 1>();
  if constexpr (SM.nop[T] > 2) sm_op<Y, SM.op0[T] + 2>();
  if constexpr (T == SM.last + 1) asm volatile("v_add_f32_e32 v%c0, v%c1, v%c2" ::"i"(R_TMP), "i"(R_TS), "i"(R_TS + 1));
  if constexpr (T == SM.last + 2) asm volatile("v_cmp_nle_f32_e64 s[54:55], v%c0, v%c1" ::"i"(R_TMP), "i"(R_THR));
}
#define W_A0 1
template <int X, int MODE, int G> DI void gapA(AC& c) {
  constexpr bool FIRST = MODE == 1 || MODE == 2, NOQK = MODE >= 3;
  constexpr int Xp = X ^ 1, kb = G >> 4, g = G & 15, qb = g >> 3, s = g & 7;
  if constexpr (MODE == 3 && G < 48 && G % 3 == 0) {
    constexpr int f = G / 3;
    asm volatile("buffer_load_dwordx4 a[%c0:%c1], v84, s[88:91], s75 offen offset:%2 sc1" ::"i"(128 + 4 * f), "i"(131 + 4 * f), "i"((f & 3) * 1024) : "memory");
    if constexpr ((f & 3) == 3) asm volatile("s_addk_i32 s75, 4096");
  }
  if constexpr (G == 48) asm volatile("s_waitcnt vmcnt(%0) lgkmcnt(0)\n\ts_barrier" ::"i"(MODE == 3 ? 16 : MODE == 2 ? 12 : MODE == 1 ? W_A0 : 0) : "memory");
  else if constexpr (g == 0 && !NOQK) asm volatile("s_waitcnt lgkmcnt(0)");
  if constexpr (!NOQK) {
    constexpr int r = 192 + 4 * ((kb & 1) * 8 + s), q = 128 + 4 * g;
    constexpr int d = R_S + 32 * X + 4 * (kb * 2 + qb), ci = s == 0 ? R_CI + 4 * (kb * 2 + qb) : d;
    if constexpr (FIRST && s == 0)
      asm volatile("v_mfma_f32_16x16x32_bf16 v[%c0:%c1], a[%c2:%c3], a[%c4:%c5], 0" ::"i"(d), "i"(d + 3), "i"(r), "i"(r + 3), "i"(q), "i"(q + 3));
    else
      asm volatile("v_mfma_f32_16x16x32_bf16 v[%c0:%c1], a[%c2:%c3], a[%c4:%c5], v[%c6:%c7]" ::"i"(d), "i"(d + 3), "i"(r), "i"(r + 3), "i"(q), "i"(q + 3),
                   "i"(ci), "i"(ci + 3));
    if constexpr (g < 8 && kb < 3) kread<X, (kb + 1) * 8 + g>();
    constexpr int S0 = SMV ? 40 : 14, S1 = SMV ? 41 : 30, S2 = SMV ? 42 : 45, S3 = SMV ? 43 : 46;
    if constexpr (G == S0) asm volatile("s_add_u32 s96, s97, 0x1000000");
    if constexpr (G == S1) dma_m0<X, 0>();
    if constexpr (G == S2) asm volatile("s_mov_b32 s97, s98");
    if constexpr (G == S3) asm volatile("s_add_u32 s98, s98, s99");
    if constexpr (G >= 56 && !(G & 1)) dma_at<(G - 56) / 2>();
    if constexpr (G == 57) asm volatile("s_lshr_b32 s100, s100, 1\n\ts_add_u32 s56, s56, 1");
    if constexpr (G == 59 && !FIRST) asm volatile("s_and_b32 s49, s100, 1\n\ts_or_b32 s49, s49, s54");
    if constexpr (G == 61 && !FIRST) asm volatile("s_or_b32 %0, s49, s55" : "=s"(c.rare));
    if constexpr (G == 61 && FIRST) asm volatile("s_and_b32 %0, s100, 1" : "=s"(c.rare));
  } else if constexpr (SM.nop[64 + G] > 0) asm volatile("s_nop 0");
  if constexpr (!FIRST && g < 8 && kb == 3) vread<Xp, g>();
  if constexpr (MODE == 2 && (G & 3) == 1) d_store<16 + G / 4>();
  if constexpr (FIRST) acc_zero2<G>();
  else sm_gap<Xp, 64 + G>(c);
  GAP();
}
template <int R> DI void rescale1(float alpha) {
  float t;
  asm volatile("v_accvgpr_read_b32 %0, a[%c2]\n\tv_mul_f32 %0, %0, %1\n\tv_accvgpr_write_b32 a[%c2], %0" : "=&v"(t) : "v"(alpha), "i"(R));
}
template <int... Rs> DI void rescale_all(const float (&alpha)[2], iseq<Rs...>) { (rescale1<Rs>(alpha[(Rs >> 2) & 1]), ...); }
template <bool MAX> DI float row_reduce(float v) {
  auto p = __builtin_amdgcn_permlane32_swap(__float_as_uint(v), __float_as_uint(v), false, false);
  v = MAX ? fmaxf(__uint_as_float(p[0]), __uint_as_float(p[1])) : __uint_as_float(p[0]) + __uint_as_float(p[1]);
  p = __builtin_amdgcn_permlane16_swap(__float_as_uint(v), __float_as_uint(v), false, false);
  return MAX ? fmaxf(__uint_as_float(p[0]), __uint_as_float(p[1])) : __uint_as_float(p[0]) + __uint_as_float(p[1]);
}
template <int Y, int QB> DI float row_max() {
  constexpr int r = R_S + 32 * Y + 4 * QB;
  float mx;
  asm volatile("v_max_f32 %0, v%c1, v%c2\n\tv_max3_f32 %0, %0, v%c3, v%c4" : "=&v"(mx) : "i"(r), "i"(r + 1), "i"(r + 2), "i"(r + 3));
  static_for([&](auto K) {
    constexpr int q = r + 8 * (K + 1);
    float t = mx;
    asm volatile("v_max3_f32 %0, %0, v%c1, v%c2\n\tv_max3_f32 %0, %0, v%c3, v%c4" : "+v"(t) : "i"(q), "i"(q + 1), "i"(q + 2), "i"(q + 3));
    mx = t;
  }, make_iseq<3>{});
  return row_reduce<true>(mx);
}
template <int R> DI void v_sub(float d) { asm volatile("v_sub_f32 v%c0, v%c0, %1" ::"i"(R), "v"(d)); }
template <int B> DI void s_shift(const float (&d)[2]) { static_for([&](auto E) { v_sub<B + E>(d[(E >> 2) & 1]); }, make_iseq<32>{}); }
DI void m_shift(const float (&d)[2]) {
  s_shift<R_CI>(d);
  v_sub<R_M>(-d[0]); v_sub<R_M + 1>(-d[1]);
}
DI void set_mask0(int wid) {
  const int mr = wid * 32 + __float_as_int(vget<89>());
  static_for([&](auto I) {
    constexpr int e = I, k = (e >> 3) * 16 - ((e >> 2) & 1) * 16 + (e & 3);
    const float x = vget<R_S + e>();
    vset<R_S + e>(k > mr ? -INFINITY : x);
  }, make_iseq<32>{});
}
template <int X> DI void sm_redo() {
  constexpr int Xp = X ^ 1;
  float d[2], dt[2], alpha[2], ts[2] = {0.f, 0.f};
  static_for([&](auto Q) {
    constexpr int qb = Q;
    const float pd = vget<R_PD + qb>();
    d[qb] = fmaxf(row_max<Xp, qb>() - pd, 0.f);
    dt[qb] = d[qb] + pd;
    vset<R_PD + qb>(d[qb]);
    alpha[qb] = __builtin_amdgcn_exp2f(-d[qb]);
    vset<R_RS + qb>(vget<R_RS + qb>() * alpha[qb]);
  }, make_iseq<2>{});
  vset<R_THR>(d[0] > 0.f || d[1] > 0.f ? -1.f : 4096.f);
  m_shift(d);
  static_for([&](auto K) {
    constexpr int k = K, qb = (k >> 1) & 1;
    const float e0 = __builtin_amdgcn_exp2f(vget<R_S + 32 * Xp + 2 * k>() - dt[qb]), e1 = __builtin_amdgcn_exp2f(vget<R_S + 32 * Xp + 2 * k + 1>() - dt[qb]);
    ts[qb] += e0; ts[qb] += e1;
    vset<p_reg(Xp, k)>(__uint_as_float(pk2(e0, e1)));
  }, make_iseq<16>{});
  vset<R_TS>(ts[0]); vset<R_TS + 1>(ts[1]);
  rescale_all(alpha, make_iseq<128>{});
  asm volatile("s_nop 1" ::: "memory");
  GAP();
}
template <int X, int MODE, int... Gs> DI void phaseA_(AC& c, iseq<Gs...>) { (gapA<X, MODE, Gs>(c), ...); }
template <int X, int MODE> DI void phaseA(AC& c) { phaseA_<X, MODE>(c, make_iseq<64>{}); }

template <int X, bool SMX, int G, bool WAIT = true> DI void gapB(AC& c) {
  constexpr int Xp = X ^ 1, blk = G >> 4, g = G & 15;
  if constexpr (g == 0 && WAIT) asm volatile("s_waitcnt lgkmcnt(0)");
  constexpr int qb = (g >> 1) & 1, s = g & 1, r = 192 + 4 * ((blk & 1) * 8 + (g >> 2) * 2 + s), o = 4 * ((blk * 4 + (g >> 2)) * 2 + qb);
  constexpr int p = R_P + 16 * Xp + 4 * (s * 2 + qb);
  asm volatile("v_mfma_f32_16x16x32_bf16 a[%c0:%c1], a[%c2:%c3], v[%c4:%c5], a[%c0:%c1]" ::"i"(o), "i"(o + 3), "i"(r), "i"(r + 3), "i"(p), "i"(p + 3));
  if constexpr (g < 8) {
    if constexpr (blk < 3) vread<Xp, (blk + 1) * 8 + g>();
    else if constexpr (SMX) kread<Xp, g>();
  }
  if constexpr (SMX) {
    if constexpr (G == 1 || G == 2) asm volatile("v_add_f32_e32 v%c0, v%c0, v%c1" ::"i"(R_RS + G - 1), "i"(R_TS + G - 1));
    if constexpr (g == 7 && blk < 3) dma_m0<X, blk * 4 + 4>();
    if constexpr (g >= 8 && !(g & 1) && blk < 3) dma_at<blk * 4 + 4 + (g - 8) / 2>();
    if constexpr (G == 55 && X == 1) asm volatile("s_cmp_eq_u32 s56, s76");
    if constexpr (G == 3) asm volatile("s_lshr_b32 s77, s77, 1");
    if constexpr (G == 4) asm volatile("s_bitcmp1_b32 s77, 0");
    if constexpr (G == 5) asm volatile("s_cselect_b64 s[52:53], s[62:63], 0");
    if constexpr (G == 6) asm volatile(FL_ARM("s[52:53]"));
    if constexpr (G == 56) asm volatile(FL_POLL ::: "memory");
    sm_gap<X, G>(c);
  }
  GAP();
}
template <int X, int... Gs> DI void phaseB_(AC& c, iseq<Gs...>) { (gapB<X, true, Gs>(c), ...); }
template <int X> DI void phaseB(AC& c) { phaseB_<X>(c, make_iseq<64>{}); }
constexpr int sm_nex() { int n = 0; for (int t = 0; t < 64; ++t) n += SM.ex[t] >= 0; return n; }
template <int X> DI void phaseB0(unsigned mode, int wid) {
  dma_m0<X, 4>();
  kreads<X ^ 1>(make_iseq<8>{});
  dma_at<4>();
  asm volatile("s_nop 3");
  float d[2];
  if (mode & 2) {
    d[0] = vget<122>(); d[1] = vget<123>();
    dma_at<5>();
  } else {
    if constexpr (X == 0) if (__builtin_expect(mode & 4, 0)) set_mask0(wid);
    d[0] = row_max<X, 0>(); GAP(); dma_at<5>();
    d[1] = row_max<X, 1>();
#pragma unroll
    for (int qb = 0; qb < 2; ++qb) if (!(d[qb] > -3.0e38f)) d[qb] = 0.f;
  }
  GAP();
  vset<R_M>(d[0]); vset<R_M + 1>(d[1]);
  dma_at<6>();
  const f32x2c nd[2] = {{-d[0], -d[0]}, {-d[1], -d[1]}};
  static_for([&](auto E) {
    constexpr int e = E;
    const f32x2c v = nd[(e >> 1) & 1];
    asm volatile("v_pk_add_f32 v[%c0:%c1], v[%c0:%c1], %4\n\tv_mov_b64 v[%c2:%c3], %4" ::"i"(R_S + 32 * X + 2 * e), "i"(R_S + 32 * X + 2 * e + 1), "i"(R_CI + 2 * e), "i"(R_CI + 2 * e + 1), "v"(v));
    if constexpr (e == 3) dma_at<7>();
    if constexpr (e == 6) dma_m0<X, 8>();
    if constexpr (e == 7 || e == 11 || e == 15) dma_at<8 + (e - 7) / 4>();
  }, make_iseq<16>{});
  static_assert(SM.op0[64] >= 3 * ((sm_nex() - 15) / 2) && SM.op0[64] <= 24);
  static_for([&](auto E) {
    constexpr int e = E;
    if constexpr (e < sm_nex()) asm volatile("v_exp_f32 v%c0, v%c1" ::"i"(R_E + e), "i"(R_S + 32 * X + e));
    if constexpr (e == 3) dma_at<11>();
    if constexpr (e == 6) dma_m0<X, 12>();
    if constexpr (e == 7 || e == 11 || e == 15) dma_at<12 + (e - 7) / 4>();
  }, make_iseq<16>{});
  static_for([&](auto O) { sm_op<X, O>(); }, make_iseq<SM.op0[64]>{});
  dma_at<15>();
  static_for([&](auto E) { asm volatile("v_exp_f32 v%c0, v%c1" ::"i"(R_E + (int)E), "i"(R_S + 32 * X + 16 + (int)E)); }, make_iseq<(sm_nex() > 16 ? sm_nex() - 16 : 0)>{});
  asm volatile("buffer_store_dwordx2 v[%c0:%c1], v88, s[88:91], s76 offen sc1\n\ts_bitcmp1_b32 s77, 0\n\ts_cselect_b64 s[52:53], s[62:63], 0\n\t"
               FL_ARM("s[52:53]") "\n\t" FL_POLL ::"i"(R_M), "i"(R_M + 1) : "memory", "scc");
  GAP();
}
constexpr int T_ZO = 8;
#define NINF2(R) "v_mov_b64 v[" #R ":" #R "+1], v[122:123]\n\t"
#define NINF4(R) NINF2(R) NINF2(R + 2)
#define TRI(R) "v_cndmask_b32_e64 v[" #R "], v[" #R "], v122, s[48:49]\n\tv_cndmask_b32_e64 v[" #R "+1], v[" #R "+1], v122, s[50:51]\n\t" \
               "v_cndmask_b32_e64 v[" #R "+2], v[" #R "+2], v122, s[52:53]\n\tv_cndmask_b32_e64 v[" #R "+3], v[" #R "+3], v122, s[54:55]\n\t"
DI void ev_code() {
  asm volatile(
    "s_getpc_b64 s[82:83]\n\ts_add_u32 s82, s82, 12\n\ts_addc_u32 s83, s83, 0\n\ts_branch 9f\n\t"
    "s_bitcmp0_b32 s58, s56\n\ts_cbranch_scc1 1f\n\t" FL_SET("s70") "\n\t" FL_SET("s71") "\n"
    "1:\n\ts_bitcmp0_b32 s57, s56\n\ts_cbranch_scc1 2f\n\t" FL_CHECK("s[62:63]") "\n\t"
    "s_add_u32 s98, s98, s68\n\ts_mov_b32 s68, s69\n\ts_mov_b32 s69, 0\n\ts_mov_b64 s[62:63], s[64:65]\n\ts_mov_b64 s[64:65], s[66:67]\n"
    "2:\n\ts_bitcmp0_b32 s59, s56\n\ts_cbranch_scc1 3f\n\ts_mov_b32 s98, s72\n\ts_bitcmp0_b32 s73, 0\n\ts_cbranch_scc1 3f\n\t"
    "v_lshrrev_b32 v90, 7, v86\n\tv_and_b32 v91, 6, v90\n\tv_lshlrev_b32 v91, 4, v91\n\tv_and_b32 v92, 0x70, v86\n\tv_xor_b32 v91, v91, v92\n\t"
    "s_movk_i32 s48, 896\n\tv_mad_u32_u24 v94, v90, s48, v91\n\ts_mov_b64 s[92:93], s[80:81]\n\ts_mov_b32 s94, 229376\n\ts_movk_i32 s99, 128\n\t"
    "v_add_u32 v90, 28672, v94\n\tv_add_u32 v91, 34816, v94\n\tv_add_u32 v92, 40960, v94\n\tv_add_u32 v93, 47104, v94\n\t"
    "v_add_u32 v95, 6144, v94\n\tv_add_u32 v126, 12288, v94\n\tv_add_u32 v127, 18432, v94\n"
    "3:\n\ts_bitcmp0_b32 s60, s56\n\ts_cbranch_scc1 4f\n\t"
    "v_readlane_b32 s54, v78, %0\n\ts_add_u32 m0, s78, 0x10000\n\t"
    "s_lshr_b32 s53, s73, 16\n\ts_mulk_i32 s53, 3584\n\ts_mov_b64 s[48:49], s[80:81]\n\ts_add_u32 s50, s53, 3584\n\ts_mov_b32 s51, 0x20000\n\t"
    "v_lshlrev_b32 v121, 3, v84\n\tbuffer_load_dword v121, s[48:51], s53 offen lds\n\t"
    "s_bitcmp0_b32 s73, 3\n\ts_cbranch_scc1 4f\n\t"
    "v_lshlrev_b32 v121, 9, v84\n\tv_and_b32 v121, 0x38000, v121\n\tv_lshrrev_b32 v122, 9, v84\n\tv_lshl_add_u32 v121, v122, 12, v121\n\t"
    "v_lshrrev_b32 v122, 3, v84\n\tv_xor_b32 v122, v122, v84\n\tv_and_or_b32 v121, v122, 48, v121\n\t"
    "buffer_load_dwordx4 v121, s[88:91], s54 offen sc1 lds\n\ts_addk_i32 s54, 7168\n\t"
    "buffer_load_dwordx4 v121, s[88:91], s54 offen offset:1024 sc1 lds\n\ts_addk_i32 s54, 7168\n\t"
    "buffer_load_dwordx4 v121, s[88:91], s54 offen offset:2048 sc1 lds\n\ts_addk_i32 s54, 7168\n\t"
    "buffer_load_dwordx4 v121, s[88:91], s54 offen offset:3072 sc1 lds\n"
    "4:\n\ts_bitcmp0_b32 s61, s56\n\ts_cbranch_scc1 6f\n\t"
    "s_sub_u32 s74, s74, 64\n\ts_cmp_ge_i32 s74, 0\n\ts_cbranch_scc1 6f\n\t"
    "v_cmp_gt_i32 s[48:49], 0, v89\n\tv_cmp_gt_i32 s[50:51], 1, v89\n\tv_cmp_gt_i32 s[52:53], 2, v89\n\tv_cmp_gt_i32 s[54:55], 3, v89\n\t"
    "v_mov_b32 v122, 0xff800000\n\tv_mov_b32 v123, 0xff800000\n\t" NINF4(248)
    "s_cmp_eq_i32 s74, -32\n\ts_cbranch_scc0 5f\n\t" TRI(240) TRI(252) "s_setpc_b64 s[86:87]\n"
    "5:\n\t" NINF4(240) NINF4(244) NINF4(252) NINF4(232) TRI(224) TRI(236)
    "s_cmp_eq_i32 s74, -64\n\ts_cbranch_scc1 6f\n\t" NINF4(224) NINF4(228) NINF4(236)
    "6:\n\ts_setpc_b64 s[86:87]\n9:"
    ::"i"(T_ZO) : "scc");
}
DI void ev_call() { asm volatile("s_swappc_b64 s[86:87], s[82:83]" ::: "vcc", "scc", "memory"); GAP(); }

#define ACLOB_ALL "a0","a1","a2","a3","a4","a5","a6","a7","a8","a9","a10","a11","a12","a13","a14","a15","a16","a17","a18","a19","a20","a21","a22","a23","a24","a25","a26","a27","a28","a29","a30","a31","a32","a33","a34","a35","a36","a37","a38","a39","a40","a41","a42","a43","a44","a45","a46","a47","a48","a49","a50","a51","a52","a53","a54","a55","a56","a57","a58","a59","a60","a61","a62","a63","a64","a65","a66","a67","a68","a69","a70","a71","a72","a73","a74","a75","a76","a77","a78","a79","a80","a81","a82","a83","a84","a85","a86","a87","a88","a89","a90","a91","a92","a93","a94","a95","a96","a97","a98","a99","a100","a101","a102","a103","a104","a105","a106","a107","a108","a109","a110","a111","a112","a113","a114","a115","a116","a117","a118","a119","a120","a121","a122","a123","a124","a125","a126","a127","a128","a129","a130","a131","a132","a133","a134","a135","a136","a137","a138","a139","a140","a141","a142","a143","a144","a145","a146","a147","a148","a149","a150","a151","a152","a153","a154","a155","a156","a157","a158","a159","a160","a161","a162","a163","a164","a165","a166","a167","a168","a169","a170","a171","a172","a173","a174","a175","a176","a177","a178","a179","a180","a181","a182","a183","a184","a185","a186","a187","a188","a189","a190","a191","a192","a193","a194","a195","a196","a197","a198","a199","a200","a201","a202","a203","a204","a205","a206","a207","a208","a209","a210","a211","a212","a213","a214","a215","a216","a217","a218","a219","a220","a221","a222","a223","a224","a225","a226","a227","a228","a229","a230","a231","a232","a233","a234","a235","a236","a237","a238","a239","a240","a241","a242","a243","a244","a245","a246","a247","a248","a249","a250","a251","a252","a253","a254","a255"

struct EP { float fa[2], fb[2]; };
template <int I, int E, int MODE> DI void epi_pair(const EP& p) {
  constexpr int qb = I & 1, r = 16 * (I >> 1) + 4 * qb + (E >> 1) * 8 + (E & 1) * 2, t = 96 + 2 * E, w = 128 + 4 * I + E;
  if constexpr (MODE == 2) asm volatile("v_accvgpr_read_b32 v106, a[%c0]" ::"i"(w));
  asm volatile("v_accvgpr_read_b32 v%c0, a[%c2]\n\tv_accvgpr_read_b32 v%c1, a[%c3]" ::"i"(t), "i"(t + 1), "i"(r), "i"(r + 1));
  if constexpr (MODE) asm volatile("v_mul_f32 v%c0, v%c0, %2\n\tv_mul_f32 v%c1, v%c1, %2" ::"i"(t), "i"(t + 1), "v"(p.fa[qb]));
  if constexpr (MODE == 2)
    asm volatile("v_lshlrev_b32 v104, 16, v106\n\tv_and_b32 v105, 0xffff0000, v106\n\tv_fmac_f32 v%c0, v104, %2\n\tv_fmac_f32 v%c1, v105, %2"
                 ::"i"(t), "i"(t + 1), "v"(p.fb[qb]));
  asm volatile("v_cvt_pk_bf16_f32 v%c0, v%c1, v%c2" ::"i"(w), "i"(t), "i"(t + 1));
}
template <int I> DI void epi_store() {
  asm volatile("buffer_store_dwordx4 v[%c0:%c1], v84, s[88:91], s101 offen offset:%2 sc1" ::"i"(128 + 4 * I), "i"(131 + 4 * I), "i"((I & 3) * 1024) : "memory");
  if constexpr ((I & 3) == 3) asm volatile("s_addk_i32 s101, 4096");
}
constexpr int R_TB = 107, R_VS = 111;
template <int HF, int R> DI void p_read() {
  constexpr int mb = R >> 4, r = 64 * HF + 2 * R;
  asm volatile("ds_read_b64_tr_b16 a[%c1:%c2], v%c0 offset:%c3" ::"i"(R_TB + 2 * HF + mb), "i"(r), "i"(r + 1), "i"((R & 15) * 2048));
}
template <int QB, int E> DI void p_cvt() {
  asm volatile("v_cvt_pk_bf16_f32 v%c0, v%c1, v%c2" ::"i"(208 + 4 * QB + E), "i"(192 + 8 * QB + 2 * E), "i"(193 + 8 * QB + 2 * E));
}
template <int QB, int PR> DI void p_store() {
  asm volatile("buffer_store_dwordx4 v[%c0:%c1], v%c2, s[88:91], s96 offen offset:%3 sc1" ::"i"(208 + 4 * QB), "i"(211 + 4 * QB), "i"(R_VS + QB),
               "i"(PR * 128) : "memory");
}
constexpr int p_dma(int HF, int G) { return G == 3 && !HF ? 0 : G == 11 ? 1 - HF : G == 15 ? 2 - HF : G == 19 ? 3 - HF : G == 27 && HF ? 3 : -1; }
template <int HF, int G> DI void p_gap() {
  constexpr int ch = G >> 3, t = G & 7, a = 64 * HF + 32 * (ch & 1) + 4 * t, b = 128 + 4 * (2 * t + (ch >> 1)), d = 192 + 4 * ch;
  if constexpr (t == 0)
    asm volatile("v_mfma_f32_16x16x32_bf16 v[%c0:%c1], a[%c2:%c3], v[%c4:%c5], 0" ::"i"(d), "i"(d + 3), "i"(a), "i"(a + 3), "i"(b), "i"(b + 3));
  else
    asm volatile("v_mfma_f32_16x16x32_bf16 v[%c0:%c1], a[%c2:%c3], v[%c4:%c5], v[%c0:%c1]" ::"i"(d), "i"(d + 3), "i"(a), "i"(a + 3), "i"(b), "i"(b + 3));
  p_read<HF ^ 1, G>();
  if constexpr (G == 0) asm volatile("v_xor_b32 v%c0, s100, v%c0" ::"i"(R_TB + 2 * HF + 1));
  if constexpr (G == 17) asm volatile("v_xor_b32 v%c0, s100, v%c0" ::"i"(R_TB + 2 * (HF ^ 1)));
  if constexpr (G >= 4 && G <= 8 && G != 7) p_cvt<1, (G < 7 ? G - 4 : 3)>();
  if constexpr (G == 24 || (G == 7 && HF == 0)) asm volatile("s_waitcnt lgkmcnt(8)");
  if constexpr (G == 7 && HF == 1) asm volatile("s_waitcnt vmcnt(13) lgkmcnt(8)\n\ts_barrier" ::: "memory");
  if constexpr (G == 10) p_store<1, HF ^ 1>();
  if constexpr (G >= 20 && G < 24) p_cvt<0, G - 20>();
  if constexpr (G == 25) p_store<0, HF>();
  if constexpr (p_dma(HF, G) >= 0) dma_at<(HF ? 8 : 12) + p_dma(HF, G)>();
  if constexpr (HF == 0 && G == 2) asm volatile("s_bitset0_b32 m0, 12");
  if constexpr (HF == 0 && G == 13) asm volatile("s_add_u32 s96, s96, 0x40000");
  if constexpr (HF == 0 && G == 26) asm volatile("s_add_u32 s98, s98, 128");
  if constexpr (HF == 0 && G == 27) asm volatile("s_cmp_lt_u32 s98, 0x380");
  if constexpr (HF == 0 && G == 28) asm volatile("s_cselect_b32 s99, s99, 0x31000");
  if constexpr (HF == 1 && G == 1) asm volatile("s_xor_b32 s100, s100, 0x10000");
  if constexpr (HF == 1 && G == 2) asm volatile("s_xor_b32 s97, s97, s100");
  if constexpr (HF == 1 && G == 9) asm volatile("s_or_b32 m0, s97, s99");
  if constexpr (HF == 1 && G == 30) asm volatile("s_cmp_lt_u32 s98, 0x500");
  GAP();
}
template <int HF, int... Gs> DI void p_half(iseq<Gs...>) { (p_gap<HF, Gs>(), ...); }
constexpr int f_dma(int G) { return G == 2 ? 0 : G == 5 ? 1 : G == 8 ? 2 : G >= 12 && G <= 36 && G % 6 == 0 ? 1 + G / 6 : G >= 50 && G <= 68 && G % 6 == 2 ? G / 6 : -1; }
constexpr int f_ndma(int G) { int n = 0; for (int g = 0; g < G; ++g) n += f_dma(g) >= 0; return n; }
static_assert(f_ndma(74) == 12);
template <int MODE, int G> DI void gapF(AC& c, const EP& p) {
  if constexpr (MODE && G == 48) asm volatile("s_waitcnt lgkmcnt(6)\n\ts_barrier" ::: "memory");
  if constexpr (G < 64) gapB<0, false, G, !(MODE && G == 48)>(c);
  if constexpr (MODE && G >= 42 && G < 64) p_read<0, G - 42>();
  if constexpr (MODE && G >= 64 && G < 69) { p_read<0, 2 * G - 106>(); p_read<0, 2 * G - 105>(); }
  if constexpr (MODE && f_dma(G) >= 0) dma_at<8 + (f_dma(G) & 7)>();
  if constexpr (MODE == 2 && G >= 10 && (G - 10) % 16 == 0) asm volatile("s_waitcnt vmcnt(%0)" ::"i"(13 - (G - 10) / 4 + f_ndma(G + 1)) : "memory");
  if constexpr (G >= 10) epi_pair<(G - 10) / 4, (G - 10) & 3, MODE>(p);
  if constexpr (MODE == 3 && G < 64 && (G & 1)) {
    constexpr int db = G >> 2, qb = (G >> 1) & 1, o = 4 * (db * 2 + qb), a = 224 + 4 * (db & 1), b = 128 + 4 * ((db >> 1) * 2 + qb);
    if constexpr ((G & 7) == 1) asm volatile("s_waitcnt vmcnt(%0)" ::"i"(15 - (G >> 2) + f_ndma(G + 1)) : "memory");
    asm volatile("v_mfma_f32_16x16x32_bf16 a[%c0:%c1], v[%c2:%c3], a[%c4:%c5], a[%c0:%c1]" ::"i"(o), "i"(o + 3), "i"(a), "i"(a + 3), "i"(b), "i"(b + 3));
  }
  if constexpr (MODE == 0) {
    if constexpr (G >= 14 && ((G - 14) & 3) == 0) epi_store<(G - 14) / 4>();
    if constexpr (G == 73) epi_store<15>();
  } else {
    if constexpr (G == 0) asm volatile("s_or_b32 m0, s78, 0x1000\n\ts_movk_i32 s98, 256");
    if constexpr (G == 15) asm volatile("s_bitset0_b32 m0, 12");
    if constexpr (G == 48) asm volatile("s_or_b32 m0, s79, 0x1000\n\ts_movk_i32 s98, 384");
    if constexpr (G == 73) asm volatile("s_waitcnt vmcnt(4) lgkmcnt(0)" ::: "memory");
  }
  GAP();
}
template <int MODE, int... Gs> DI void phaseF(AC& c, const EP& p, iseq<Gs...>) { (gapF<MODE, Gs>(c, p), ...); }
template <int U, int K> DI void r_load(unsigned vo, unsigned so) {
  asm volatile("buffer_load_dwordx4 a[%c0:%c1], %2, s[88:91], %3 offen offset:%4 sc1" ::"i"(64 * U + 4 * K), "i"(64 * U + 4 * K + 3), "v"(vo),
               "s"(so + (K >> 2) * 4096), "i"((K & 1) * 512 + ((K >> 1) & 1) * 2048) : "memory");
}
template <int U, int K> DI void r_sum() {
  constexpr int d = 100 + 4 * (K & 1), b = 64 * U + 4 * K;
  if constexpr (K < 2) asm volatile("v_mfma_f32_16x16x32_bf16 v[%c0:%c1], v[96:99], a[%c2:%c3], 0" ::"i"(d), "i"(d + 3), "i"(b), "i"(b + 3));
  else asm volatile("v_mfma_f32_16x16x32_bf16 v[%c0:%c1], v[96:99], a[%c2:%c3], v[%c0:%c1]" ::"i"(d), "i"(d + 3), "i"(b), "i"(b + 3));
}
template <int U> DI void r_fin(unsigned vo, __amdgpu_buffer_rsrc_t srd, unsigned so) {
  asm volatile("" : "+s"(srd), "+s"(so));
  static_for([&](auto K) { r_sum<U, K>(); }, make_iseq<16>{});
  asm volatile("s_nop 7");
  static_for([&](auto E) { asm volatile("v_cvt_pk_bf16_f32 v%c0, v%c1, v%c2" ::"i"(116 + 4 * U + E), "i"(100 + 2 * E), "i"(101 + 2 * E)); }, make_iseq<4>{});
  asm volatile("buffer_store_dwordx4 v[%c3:%c4], %0, %1, %2 offen" OUTSCS ::"v"(vo), "s"(srd), "s"(so), "i"(116 + 4 * U), "i"(119 + 4 * U) : "memory");
}

#define CMAX 12
#define PREPX false
static_assert(NYT <= 16 && NYT >= 12);
enum { T_Q0, T_K0, T_OQ, T_OG, T_FPREP, T_FQ, T_FG, T_FS,   T_CNT = 9, T_NPRE = 10, T_NST = 12, T_TAB = 14, T_SLAB = 16, T_PART, T_IT = 18, T_ITN = 23 };
struct TRow { unsigned v[64]; };
constexpr u64 kvmask(int m0, int m1) { u64 n = 0; for (int m = m0; m <= m1; ++m) n |= 0x101ull << m; return n; }
constexpr u64 k_order(bool isY, int rr) {
  const int cls = (rr + (rr >> 2)) & 3;
  return !isY ? (cls == 0 ? 0xfedcb9875a463210ull : cls == 1 ? 0xefadc6982b574310ull : cls == 2 ? 0xfebad7693c285410ull : 0xefcba8764d392510ull)
              : (cls == 0 ? 0xfedcb987543a6210ull : cls == 1 ? 0xefadc698254b7310ull : cls == 2 ? 0xfebad769325c8410ull : 0xefcba876432d9510ull);
}
constexpr TRow t_row(int r, int wid, int j) {
  TRow t{};
  const int rr = r & 15, hsel = rr & 1, pj = rr >> 1, kv = pj & 1, h = j + 8 * hsel, u = hsel * 8 + j;
  const bool isY = r >= 16, solo = pj == 7;
  const int mt = (rr >> 2) + (isY ? 4 : 0), g = j * 128, NY = solo ? 18 : NYT;
  const int nx0 = 32 - 2 * pj - NY, cx = nx0 < CMAX ? nx0 : CMAX, ex = nx0 - cx, qbF = isY != (solo && SWAP7) ? 15 - pj : pj;
  const unsigned oQ = (unsigned)h << 20, oK = oQ + O_K, oP = O_P + (h * 8 + pj) * PSTR;
  if (wid == 4) {
    for (int l = 0; l < 64; ++l)
      t.v[l] = fo(l < 16 ? g + hsel * 16 + l : l < 24 ? g + 16 + hsel * 8 + l : l == 24 ? g + 48 + hsel * 8 + pj : l < 32 ? g + 64 + hsel * 8 + pj :
                  l < 48 ? 2304 + pj * 16 + l - 32 : 2048 + pj * 16 + l - 48);
    return t;
  }
  auto put64 = [&](int i, u64 x) { t.v[i] = (unsigned)x; t.v[i + 1] = (unsigned)(x >> 32); };
  t.v[T_Q0] = oQ + ((solo ? qbF : 15 - pj) * 128 + wid * 32) * 512;
  t.v[T_K0] = oK + (isY ? cx : 0) * 32768;
  t.v[T_OQ] = oQ + pj * 131072 + (wid >> 1) * 65536 + (wid & 1) * 4096;
  t.v[T_OG] = oK + kv * TSB + mt * 131072 + (wid >> 1) * 65536 + (wid & 1) * (kv ? 8192 : 4096);
  t.v[T_FPREP] = fo(2048 + pj * 16 + u);
  t.v[T_FQ] = fo(g + 32 + hsel * 8 + pj);
  t.v[T_FG] = fo(g + hsel * 16 + kv * 8 + mt);
  t.v[T_FS] = fo(1024 + qbF * 16 + j * 2 + hsel);
  t.v[T_CNT] = fo(2560 + qbF * 16);
  t.v[T_ZO] = O_Z + pj * 64 * SEQ * 2 + (u * 4 + wid) * 64;
  const u64 own = 0x1ull << (kv * 8 + mt), needQ = isY ? 1ull << (16 + (qbF >> 1)) : (1ull << (16 + (pj >> 1))) | (1ull << (16 + ((15 - pj) >> 1)));
  put64(T_NPRE, isY ? needQ | (1ull << (cx >> 2)) | (1ull << ((cx + 1) >> 2)) : solo ? needQ : needQ | 0xffffull << 48);
  put64(T_NST, isY ? kvmask(cx >> 2, 3) | (solo ? 0 : 1ull << 25) : kvmask(0, solo ? 3 : 0));
  put64(T_TAB, k_order(isY, rr));
  t.v[T_SLAB] = O_S + qbF * 0x200000 + wid * 65536 + (j * 2 + hsel) * 1024;
  t.v[T_PART] = oP;
  for (int it = 0; it < 2; ++it) {
    unsigned* p = t.v + T_IT + it * T_ITN;
    const bool last = isY || it == 1;
    const int n = solo ? 2 * qbF + 2 : isY ? NY : it ? 2 * pj + 2 : nx0;
    const u64 need2 = solo ? 0 : kvmask(0, (2 * pj + 1) >> 2) | (0xffffull << 32);
    u64 q[3] = {0, 0, 0};
    unsigned jmp[2] = {0, 0};
    if (isY) {
      p[0] = (solo && SWAP7 ? 0 : 1u << (14 - cx)) | (solo ? 0 : 1u << (n - 2));
      q[0] = kvmask(4, (31 - 2 * pj) >> 2) & ~own; jmp[0] = ex * 32768; q[1] = solo ? 0 : 1ull << 24;
      p[1] = 2; p[13] = t.v[T_FG]; p[14] = fo(g + 96 + r);
    } else if (it == 0) {
      p[0] = 2u | (ex ? 1u << (cx - 2) : 0);
      q[0] = kvmask(0, cx > 0 ? (cx - 1) >> 2 : 0);
      if (ex) { q[1] = kvmask(4, 4 + ((ex - 1) >> 2)); jmp[1] = (16 - cx) * 32768; q[2] = need2; } else q[1] = need2;
      p[1] = 2; p[13] = fo(2304 + pj * 16 + u) + j * (8 * 4352 - 8); p[14] = fo(g + 64 + hsel * 8 + pj);
    } else {
      p[0] = p[1] = solo ? 0 : 1;
      q[0] = need2;
      if (solo && SWAP7) { p[0] = 1u << 14; q[0] = kvmask(4, 4); } p[13] = fo(g + 48 + hsel * 8 + pj); p[14] = fo(g + 96 + r);
    }
    p[2] = 1u << (n - 2);
    p[3] = !last ? 0 : n > 2 ? 1u << (n - 3) : 1u;
    p[4] = !last ? 0 : n > 2 ? 3u << (n - 3) : 1u << 31;
    for (int k = 0; k < 3; ++k) { p[5 + 2 * k] = (unsigned)q[k]; p[6 + 2 * k] = (unsigned)(q[k] >> 32); }
    p[11] = jmp[0]; p[12] = jmp[1];
    p[15] = oK;
    p[16] = (last ? 1 : 0) | (isY && !solo ? 2 : 0) | (n == 2 ? 4 : 0) | (isY || solo ? 0 : 8) | (n - 1) << 8 | ((r >> 1) * 4 + wid) << 16;
    p[17] = wid * 32 - (n == 2 ? 64 : 0);
    p[18] = oQ + (pj * 128 + wid * 32) * 512;
    p[19] = 0;
    p[20] = (p[0] | (last ? 0 : 1u << n)) >> 1;
    p[21] = oK + (isY ? cx : 0) * 32768;
    p[22] = ((p[0] | p[1] | p[2] | p[3] | p[4]) & 0x7fffffffu) << 1;
  }
  return t;
}
template <int I> constexpr TRow t_base = I < 160 ? t_row(I / 5, I % 5, 0) : [] {
  TRow a = t_row(0, (I - 160) * 4, 0), b = t_row(0, (I - 160) * 4, 1);
  for (int l = 0; l < 64; ++l) a.v[l] = b.v[l] - a.v[l];
  return a;
}();
template <int I> constexpr bool t_linear = [] {
  for (int j = 1; j < 8; ++j) {
    const TRow b = t_row(I / 5, I % 5, j);
    for (int l = 0; l < 64; ++l) if (b.v[l] != t_base<I>.v[l] + j * t_base<160 + (I % 5) / 4>.v[l]) return false;
  }
  return true;
}();
struct Tab { TRow row[162]; };
template <int... I> constexpr Tab t_make(iseq<I...>) {
  static_assert((t_linear<(I < 160 ? I : 0)> && ...));
  return Tab{{t_base<I>...}};
}
__device__ const Tab g_tab = t_make(make_iseq<162>{});

typedef __attribute__((ext_vector_type(16))) unsigned u32x16;
template <class T, int I> DI T* arg_late() {
  unsigned lo, hi;
  asm volatile("v_readlane_b32 %0, v77, %2\n\tv_readlane_b32 %1, v77, %3\n\ts_nop 4" : "=s"(lo), "=s"(hi) : "i"(2 * I), "i"(2 * I + 1));
  return (T*)(((u64)hi << 32) | lo);
}
__global__ __launch_bounds__(256, 1) __attribute__((amdgpu_num_vgpr(77), amdgpu_num_sgpr(56))) void fused_kernel(
    const bf16* ckv_, const bf16* wuk_, const bf16* wuv_, const bf16* x_, const bf16* wq_, bf16* ws_, const bf16* ak_, const bf16* av_,
    const bf16* ash_, const bf16* assmk_, const bf16* assmD_, const bf16* wo_, const bf16* aq_, bf16* out_) {
  extern __shared__ __attribute__((aligned(16))) char lds[];
  unsigned long long t00; asm volatile("s_memrealtime %0\n\ts_waitcnt lgkmcnt(0)" : "=s"(t00) :: "memory");
  const void* kap = (const void*)__builtin_amdgcn_kernarg_segment_ptr();
  u32x16 ka; u64 ka8;
  asm volatile("at_code_begin:\n\ts_load_dwordx16 %0, %2, 0\n\ts_load_dwordx2 %1, %2, 0x40" : "=&s"(ka), "=&s"(ka8) : "s"(kap) : ACLOB_ALL, "v255", "s101");
  GAP();
  const int tid = threadIdx.x, lane = tid & 63;
  const int wid = __builtin_amdgcn_readfirstlane(tid >> 6);
  const int b = blockIdx.x, j = b & 7, r = b >> 3, rr = r & 15, hsel = rr & 1, pj = rr >> 1, kv = pj & 1;
  const bool isY = r >= 16, solo = pj == 7, doP = isY != PREPX && !solo, doA = !isY && !solo;
  const int h = j + 8 * hsel, mt = (rr >> 2) + (isY ? 4 : 0), pu = (hsel * 8 + j) * 4 + wid;
  constexpr unsigned m0d = 131072 + 24576;
  const unsigned lA = isY ? 65536 : 0, lW = lA ^ 65536, po = isY ? 0 : 32768;
  const unsigned mp[4] = {lA + wid * 1024 + po, lW + wid * 1024 + po, lA + wid * 1024 + (po ^ 32768), lW + wid * 1024 + (po ^ 32768)};
  {
    const int lda = isY ? DA : DC;
    const unsigned ra = wid * 8 + (lane >> 3), sa = ((lane & 7) ^ (((wid & 1) * 4 + (lane >> 4)) & 7)) * 16;
    const unsigned vbA = ra * (lda * 2) + sa, vbB = (4 * wid + (lane >> 4)) * LDW * 2 + 16 * ((lane & 15) ^ (((lane >> 4) << 2) | wid));
    vsetu<92>(vbA); vsetu<93>(vbB); vsetu<90>(vbA + 128); vsetu<91>(vbB + 0x80000); vsetu<94>(ra * (DC * 2) + sa);
    static_for([&](auto J) {
      sset<64 + J>((J + 1) * 64 * lda);
      asm volatile("s_mov_b32 s%c0, %1" ::"i"(71 + J), "i"((((J + 1) >> 2) * 32 + 16 * ((J + 1) & 1)) * LDW * 2 + (((J + 1) >> 1) & 1) * 256));
    }, make_iseq<7>{});
    sset<78>(lA + wid * 1024); sset<79>(lW + wid * 1024);
    GAP();
  }
  asm volatile("s_waitcnt lgkmcnt(0)" : "+s"(ka), "+s"(ka8));
  auto arg = [&](int i) { return (const bf16*)(((u64)ka[2 * i + 1] << 32) | ka[2 * i]); };
  const bf16 *ckv = arg(0), *wuk = arg(1), *wuv = arg(2), *x = arg(3), *wq = arg(4), *ak = arg(6), *av = arg(7), *ash = (const bf16*)ka8;
  bf16* Qb = (bf16*)arg(5);
  KST(0, 0);
  bf16* xqT = (bf16*)((char*)Qb + O_X);
  const bf16* gA = ckv + (size_t)mt * 256 * DC;
  const bf16* gW = (kv ? wuv : wuk) + h * 256;
  unsigned dv = 0;
  {
    GM g;
    PR pr;
    if (doP) prep_load(pr, tid, pj, hsel * 8 + j, ak, av, ash);
    srd_set<56>(isY ? x + (size_t)pj * 256 * DA : gA, isY ? 256 * DA * 2 : 256 * DC * 2);
    srd_set<60>(isY ? wq + h * 256 : gW, isY ? (DA - 1) * LDW * 2 + 512 : (DC - 1) * LDW * 2 + 512);
    g_pro<0>(mp, make_iseq<12>{});
    srd_set<88>(Qb, WSB);
    {
      const unsigned* tp = g_tab.row[r * 5].v;
      asm volatile("buffer_load_dwordx2 v[120:121], off, s[88:91], %0 sc1\n\tglobal_load_dword v78, %1, %3\n\tglobal_load_dword v79, %2, %3 offset:1024\n\t"
                   "global_load_dword v118, %2, %4\n\tglobal_load_dword v119, %2, %4 offset:256\n\tglobal_load_dword v77, %5, %6"
                   ::"s"(fo(6144)), "v"(tid * 4), "v"(lane * 4), "s"(tp), "s"(g_tab.row[160].v), "v"((lane < 27 ? lane : 27) * 4), "s"(kap) : "memory");
    }
    KST(0, 26);
    if (doP) {
      asm volatile("s_waitcnt vmcnt(18)" ::: "memory");
#pragma unroll
      for (int q = 0; q < 7; ++q) asm volatile("" : "+v"(pr.kk[q]));
#pragma unroll
      for (int q = 0; q < 4; ++q) asm volatile("" : "+v"(pr.vv[q]));
      KST(0, 24);
      static_for([&](auto Ic) __attribute__((always_inline)) {
        constexpr int I = Ic;
        g_pro<12 + 4 * I>(mp, make_iseq<4>{});
        GAP();
        prep_math<I>(pr, tid, lds + 131072);
        GAP();
      }, make_iseq<4>{});
      asm volatile("s_waitcnt lgkmcnt(0)\n\ts_barrier" ::: "memory");
      prep_store(tid, pj, hsel * 8 + j, xqT, lds + 131072);
    } else g_pro<12>(mp, make_iseq<4>{});
    GAP();
    CODE_TOUCH("at_code_begin", "pf_code_end", r * 4 + wid, 128u, 8, m0d);
    {
      const int fr = lane & 15, fq = lane >> 4, q = (lane & 15) >> 2, p = lane & 3;
#pragma unroll
      for (int k2 = 0; k2 < 2; ++k2)
        g.aA[k2] = lA + (wid >> 1) * 16384 + (fr >> 3) * 1024 + (fr & 7) * 128 + (((4 * k2 + fq) ^ (fr >> 1)) * 16);
#pragma unroll
      for (int hf = 0; hf < 2; ++hf)
#pragma unroll
        for (int nf = 0; nf < 8; ++nf)
          g.aB[hf][nf] = lW + (wid & 1) * 8192 + off_b(8 * fq + 4 * hf + q, nf * 2 + (p >> 1)) + 8 * (p & 1);
      asm volatile("" : "+v"(g.aA[0]), "+v"(g.aA[1]));
#pragma unroll
      for (int nf = 0; nf < 8; ++nf) asm volatile("" : "+v"(g.aB[0][nf]), "+v"(g.aB[1][nf]));
      vsetu<81>(tid == 0 ? 0u : 0x80000000u);
      vsetu<84>(lane * 16); vsetu<85>(lane * 16 + 65536); vsetu<86>(wid * 8192 + lane * 16); vsetu<87>(wid * 8192 + lane * 16 + 4096);
      vsetu<88>(65536 + wid * 256 + fr * 16); vsetu<89>(fr - 4 * fq);
      asm volatile("s_mov_b64 s[92:93], s[88:89]\n\ts_mov_b64 s[94:95], s[90:91]");
    }
    KST(0, 25);
    if (doP) asm volatile("s_waitcnt vmcnt(13)" ::: "memory");
    else {
      g_pro<16>(mp, make_iseq<12>{});
      asm volatile("s_waitcnt vmcnt(8)" ::: "memory");
    }
    asm volatile("s_barrier" ::: "memory");
    if (isY) g_read0<0>(g, make_iseq<16>{});
    else g_read0<1>(g, make_iseq<16>{});
    GAP();
    {
      u64 z = (((u64)__float_as_uint(vget<121>()) << 32) | __float_as_uint(vget<120>())) + 0x9e3779b97f4a7c15ull;
      z = (z ^ (z >> 30)) * 0xbf58476d1ce4e5b9ull; z = (z ^ (z >> 27)) * 0x94d049bb133111ebull; z ^= z >> 31;
      vsetu<82>((unsigned)z); vsetu<83>((unsigned)(z >> 32));
      asm volatile("v_mad_u32_u24 v78, v118, %0, v78\n\tv_mad_u32_u24 v79, v119, %0, v79\n\tv_readfirstlane_b32 s84, v82\n\tv_readfirstlane_b32 s85, v83\n\t"
                   "v_readlane_b32 s80, v78, %1\n\tv_readlane_b32 s81, v78, %2" ::"s"(j), "i"((int)T_TAB), "i"(T_TAB + 1));
      if (isY) asm volatile("s_mov_b64 s[80:81], 0x6543210");
      asm volatile("s_bfe_u32 s82, s80, 0x40008\n\ts_lshr_b64 s[80:81], s[80:81], 12\n\tv_lshl_add_u32 v91, s82, 19, v93" ::: "scc");
    }
    asm volatile("s_waitcnt lgkmcnt(0)\n\ts_barrier" ::: "memory");
    g_pro<28>(mp, make_iseq<4>{});
    GAP();
    if (isY) {
      KST(0, 14);
      g_tile<false, 0, 1, NTW >= 1>(g);
      KST(0, 15);
      g_loop<false, true>(g, PREPX ? 0x20u : 0x22u, [&](int t) __attribute__((always_inline)) {
        if (t == 1) fl_set<T_FPREP>();
        else if (t == 4) { tload<80, T_TAB>(); tload<81, T_TAB + 1>(); srd_set<60>(gW, (DC - 1) * LDW * 2 + 512); }
        else {
          srd_set<56>(gA, 256 * DC * 2);
          asm volatile("v_mov_b32 v92, v94");
          static_for([&](auto J) { asm volatile("s_mov_b32 s%c0, %1" ::"i"(64 + J), "i"((J + 1) * 64 * DC)); }, make_iseq<7>{});
        }
      });
      KST(0, 18);
      tload<101, T_OQ>(); sset<83>(__float_as_uint(CS));
      asm volatile("v_mov_b32 v112, v84\n\tv_add_u32 v113, 8192, v84");
      GAP();
      g_tile<false, 0, 2, NTW >= 2>(g);
      KST(0, 3);
    }
#define TT 3
    auto kvtile = [&](auto TRc) __attribute__((always_inline)) {
      constexpr bool TR = TRc;
      KST(0, 19);
      if (isY) g_tile<TR, 1, 4, NTW >= 2>(g);
      else g_tile<TR, 1, 1, NTW >= 2>(g);
      KST(0, 20);
      g_loop<TR, false>(g, 0x2000u | (isY || doA ? 2u : 1u << TT), [&](int t) __attribute__((always_inline)) {
        int l = threadIdx.x & 63; asm volatile("" : "+v"(l));
        if (t == 13) {
          asm volatile("s_mov_b32 s79, 0x28000");
          fl_poll<T_NPRE>();
          if ((b & 15) == 0)
            asm volatile("v_readlane_b32 s48, v78, %0\n\tv_and_b32 v122, 0xffffff00, v82\n\tv_mov_b32 v123, v83\n\ts_nop 2\n\t"
                         "buffer_store_dwordx2 v[122:123], v81, s[88:91], s48 offen sc1" ::"i"((int)T_CNT) : "memory");
          if (!doA && !isY) {
            CODE_TOUCH("at_code_begin", "pf_code_end", hsel * 8 + wid * 2, 16u, 64, m0d);
            CODE_TOUCH("at_code_begin", "pf_code_end", hsel * 8 + wid * 2 + 1, 16u, 64, m0d);
          }
          if (doA) {
            const char* tp = arg_late<const char, 9>() + (size_t)(pj * 64 + pu) * (SEQ * 2) + (l & 31) * 128;
            asm volatile("s_mov_b32 m0, %1\n\ts_nop 0\n\tglobal_load_lds_dword %0, off" ::"v"(tp), "i"(m0d) : "memory");
          }
        } else if (isY) fl_set<T_FQ>();
        else if (doA) { if (PREPX) fl_set<T_FPREP>(); post_dma(pj, pu, arg_late<const bf16, 12>(), 131072 + wid * 8192); }
        else {
          const char* tp = arg_late<const char, 0>() + 1024 * DC * 2 + (pu * 256 + l) * 128;
          asm volatile("s_mov_b32 m0, %1\n\ts_nop 0\n\tglobal_load_lds_dword %0, off" ::"v"(tp), "i"(m0d) : "memory");
          asm volatile("global_load_lds_dword %0, off" ::"v"(tp + 8192) : "memory");
          asm volatile("global_load_lds_dword %0, off" ::"v"(tp + 16384) : "memory");
          asm volatile("global_load_lds_dword %0, off" ::"v"(tp + 24576) : "memory");
        }
      });
      KST(0, 23);
      tload<101, T_OG>(); tload<96, T_Q0>(); tload<98, T_K0>();
      fl_check();
      tload<50, T_NST>(); tload<51, T_NST + 1>();
      asm volatile("v_mov_b32 v112, v84\n\tv_add_u32 v113, %0, v84\n\tv_mov_b32 v90, v87\n\tv_mov_b32 v91, v87\n\tv_mov_b32 v92, v87\n\tv_mov_b32 v93, v87\n\t"
                   "v_mov_b32 v94, v86\n\tv_mov_b32 v95, v86\n\tv_mov_b32 v126, v86\n\tv_mov_b32 v127, v86\n\ts_mov_b32 s99, 0x8000\n\t" FL_ARM("s[50:51]")
                   ::"i"(TR ? 4096 : 8192));
      if (doA) {
        const int d = pj * 64 + pu;
        const bf16* dp = arg_late<const bf16, 10>() + d;
        asm volatile("global_load_ushort %0, %1, off" : "=v"(dv) : "v"(dp) : "memory");
        srd_set<56>(arg_late<const bf16, 9>() + (size_t)d * SEQ, SEQ * 2);
        srd_set<60>(arg_late<const char, 5>() + O_X + d * SEQ * 2, SEQ * 2);
        sset<78>(65536 + wid * 15488);
        asm volatile("s_movk_i32 s64, 992\n\tv_subrev_u32 v122, 32, v84");
        GAP();
        g_tile<TR, 0, 3>(g);
      } else {
        sset<79>(isY ? wid * 8192 : 0x30000);
        if ((NYT & 1) && !solo) asm volatile("s_add_u32 s98, s98, 0x8000\n\ts_mov_b32 s97, 0xffff8000" ::: "scc");
        else asm volatile("s_mov_b32 s97, 0x8000");
        GAP();
        g_tile<TR, 0, 5>(g);
      }
    };
    if (kv) kvtile(std::true_type{});
    else kvtile(std::false_type{});
  }
  KST(0, 4);
  asm volatile("s_waitcnt vmcnt(16)" : "+v"(dv) :: "memory");
  sset<79>(wid * 8192);
  if (doA) {
    const float Dd = bf2f(dv);
    KST(0, 5);
    conv_wave<T_FG>(O_Z + (pj * 64 + pu) * SEQ * 2, Dd, lds + 65536 + wid * 15488);
    const char* tp = arg_late<const char, 11>() + (size_t)h * (HD * DA * 2) + ((pj * 4 + wid) * 64 + lane) * 128;
    asm volatile("s_mov_b32 m0, %1\n\ts_nop 0\n\tglobal_load_lds_dword %0, off\n\ts_waitcnt vmcnt(17)" ::"v"(tp), "i"(m0d) : "memory");
    KST(0, 6);
  } else if (!isY) {
    static_for([&](auto I) { d_store<16 + I>(); }, make_iseq<16>{});
    tload<98, T_K0>();
    asm volatile("s_waitcnt vmcnt(0)\n\ts_barrier" ::: "memory");
    fl_set<T_FG>();
    KST(0, 5);
    fl_check();
    CODE_TOUCH("att_begin", "pf_code_end", hsel * 4 + wid, 8u, 64, m0d);
    dma4k<0>(); dma4k<1>(); dma4k<2>(); dma4k<3>();
    asm volatile("s_waitcnt vmcnt(0)" ::: "memory");
    KST(0, 6);
  } else {
    tload<76, T_PART>();
    fl_check();
    asm volatile("buffer_load_dwordx2 v[122:123], v88, s[88:91], s76 offen offset:1024 sc1" ::: "memory");
  }
  AC c;
  sset<78>(65536 + wid * 8192); sset<79>(98304 + wid * 8192);
  { const bf16* wp = arg_late<const bf16, 11>() + (size_t)h * HD * DA; asm volatile("s_mov_b64 s[80:81], %0" ::"s"(wp)); }
  asm volatile("att_begin:");
  ev_code();
  asm volatile("s_waitcnt lgkmcnt(0)\n\ts_barrier" ::: "memory");
#pragma unroll 1
  for (int it = doA ? 0 : 1; it < 2; ++it) {
    const bool odd = (NYT & 1) && !solo && (isY || it == 0);
    if (odd) kreads<1>(make_iseq<8>{});
    else kreads<0>(make_iseq<8>{});
    const unsigned oP = tget<T_PART>();
    if (it == 0) {
      static_for([&](auto I) { tload<57 + I, T_IT + I>(); }, make_iseq<21>{});
      tload<S_T, T_IT + 21>(); tload<S_EV, T_IT + 22>();
      asm volatile("s_sub_u32 s70, s70, %0\n\ts_add_u32 s76, %1, 1024" ::"s"(j * (8 * 4352 - 8)), "s"(oP) : "scc");
    } else {
      static_for([&](auto I) { tload<57 + I, T_IT + T_ITN + I>(); }, make_iseq<21>{});
      tload<S_T, T_IT + T_ITN + 21>(); tload<S_EV, T_IT + T_ITN + 22>();
      asm volatile("s_mov_b32 s72, 0\n\ts_mov_b32 s75, %0\n\ts_mov_b32 s76, %1" ::"s"(oP + wid * 16384), "s"(oP + 1032));
    }
    unsigned mode;
    asm volatile("s_add_u32 s98, s97, 0x8000\n\ts_mov_b32 %0, s73\n\ts_mov_b32 s56, -1\n\tv_mov_b64 v[114:115], 0\n\tv_mov_b64 v[118:119], 0\n\t"
                 "v_mov_b32 v120, 0x45800000" : "=s"(mode) :: "scc");
    KST(0, 8 + 3 * (it && !isY));
    GAP();
#define STA(k)
#define STI()
    STA(0);
    if (odd) {
      if (isY) phaseA<1, 2>(c);
      else phaseA<1, 1>(c);
      STA(1);
      STA(2);
      phaseB0<1>(mode, wid);
      STA(3);
      asm volatile("s_bfe_u32 s76, s73, 0x80008\n\ts_branch att_mid" ::: "scc");
    } else {
      if (isY) phaseA<0, 2>(c);
      else phaseA<0, 1>(c);
      STA(1);
      if (c.rare) ev_call();
      STA(2);
      phaseB0<0>(mode, wid);
      if (__builtin_expect(mode & 4, 0)) { asm volatile("s_mov_b32 s56, 31"); ev_call(); asm volatile("s_mov_b32 s56, 0"); }
      STA(3);
    }
    GAP();
    asm volatile("s_bfe_u32 s76, s73, 0x80008\natt_loop:" ::: "scc");
    GAP();
    phaseA<1, 0>(c);
    STI();
    if (__builtin_expect(c.rare != 0, 0)) { if (redo()) sm_redo<1>(); ev_call(); }
    phaseB<1>(c);
    asm volatile("s_cbranch_scc1 att_done\natt_mid:");
    phaseA<0, 0>(c);
    STI();
    if (__builtin_expect(c.rare != 0, 0)) { if (redo()) sm_redo<0>(); ev_call(); }
    phaseB<0>(c);
    asm volatile("s_branch att_loop\natt_done:");
    GAP();
    STA(4);
    if ((mode & 3) == 1) phaseA<0, 4>(c);
    else {
      asm volatile("buffer_load_dwordx4 v[128:131], v88, s[88:91], %0 offen sc1" ::"s"(oP) : "memory");
      GAP();
      phaseA<0, 3>(c);
    }
    if (__builtin_expect(redo(), 0)) sm_redo<0>();
    STA(5);
    KST(0, 9 + 3 * (it && !isY));
    const float l[2] = {row_reduce<false>(vget<R_RS>() + vget<R_TS>()), row_reduce<false>(vget<R_RS + 1>() + vget<R_TS + 1>())};
    const float cm[2] = {vget<R_M>(), vget<R_M + 1>()};
    EP p;
    if (it == 0) {
      const f32x4 ml = {cm[0], l[0], cm[1], l[1]};
      asm volatile("buffer_store_dwordx4 %0, v88, s[88:91], %1 offen sc1\n\ts_mov_b32 s101, %2" ::"v"(ml), "s"(oP), "s"(oP + wid * 16384) : "memory");
      phaseF<0>(c, p, make_iseq<74>{});
      asm volatile(FL_POLL "\n\ts_waitcnt vmcnt(17)" ::: "memory");
    } else {
      int lz = threadIdx.x & 63; asm volatile("" : "+v"(lz));
      const int rw = lz >> 2;
      static_for([&](auto E) { vsetu<R_TB + E>(rw * 128 + 16 * ((2 * E + ((lz & 3) >> 1)) ^ (rw & 6)) + 8 * (lz & 1)); }, make_iseq<4>{});
      const unsigned vs = ((lz >> 3) & 1) * 16384 + ((lz >> 4) & 1) * 512 + (lz >> 5) * 256 + (lz & 7) * 16;
      vsetu<R_VS>(vs); vsetu<R_VS + 1>(vs + 32768);
      tload<96, T_SLAB>();
      asm volatile(TICKET ::"i"((int)T_CNT) : "memory");
      asm volatile("s_mov_b32 s97, s79\n\ts_mov_b32 s100, 0x8000\n\ts_movk_i32 s99, 0x1000");
      if (!(mode & 2)) {
        p.fa[0] = __builtin_amdgcn_rcpf(l[0]); p.fa[1] = __builtin_amdgcn_rcpf(l[1]);
        phaseF<1>(c, p, make_iseq<74>{});
      } else {
        const f32x4 ml = {vget<128>(), vget<129>(), vget<130>(), vget<131>()};
        if (__builtin_expect(__builtin_amdgcn_ballot_w64(ml[0] != cm[0] || ml[2] != cm[1]) == 0, 1)) {
          p.fa[0] = __builtin_amdgcn_rcpf(l[0] + ml[1]); p.fa[1] = __builtin_amdgcn_rcpf(l[1] + ml[3]);
          const unsigned one = (lz >> 4) != ((lz >> 2) & 3) ? 0u : (lz & 1) ? 0x3f800000u : 0x3f80u, o0 = (lz & 2) ? 0u : one, o1 = (lz & 2) ? one : 0u;
          vsetu<224>(o0); vsetu<225>(o1); vsetu<230>(o0); vsetu<231>(o1);
          asm volatile("v_mov_b64 v[226:227], 0\n\tv_mov_b64 v[228:229], 0");
          phaseF<3>(c, p, make_iseq<74>{});
        } else {
#pragma unroll
          for (int e = 0; e < 2; ++e) {
            const float mm = fmaxf(cm[e], ml[2 * e]);
            const float wa = __builtin_amdgcn_exp2f(cm[e] - mm), wb = __builtin_amdgcn_exp2f(ml[2 * e] - mm);
            const float inv = __builtin_amdgcn_rcpf(l[e] * wa + ml[2 * e + 1] * wb);
            p.fa[e] = wa * inv; p.fb[e] = wb * inv;
          }
          phaseF<2>(c, p, make_iseq<74>{});
        }
      }
    }
    KST(0, 10 + 3 * (it && !isY));
  }
  asm volatile("v_xor_b32 v%c0, s100, v%c0\np_loop:" ::"i"(R_TB));
  GAP();
  p_half<0>(make_iseq<32>{});
  p_half<1>(make_iseq<32>{});
  asm volatile("s_cbranch_scc1 p_loop\n\ts_nop 5");
  GAP();
  p_cvt<1, 0>(); p_cvt<1, 1>(); p_cvt<1, 2>(); p_cvt<1, 3>();
  p_store<1, 1>();
  GAP();
  KST(0, 27);
  {
    constexpr unsigned rdy = 98304;
    int lz = threadIdx.x & 63; asm volatile("" : "+v"(lz));
    const int fr = lz & 15, fq = lz >> 4, qbF = isY != (solo && SWAP7) ? 15 - pj : pj;
    if (wid == 0) {
#pragma unroll 1
      for (int q = 0; q < 0x100000; ++q) {
        unsigned lo, hi, tl, th;
        asm volatile("v_readfirstlane_b32 %0, v122\n\tv_readfirstlane_b32 %1, v123\n\ts_mov_b32 %2, s84\n\ts_mov_b32 %3, s85" : "=s"(lo), "=s"(hi), "=s"(tl), "=s"(th));
        if (__builtin_expect((((lo ^ tl) >> 8) | (hi ^ th)) == 0, 1)) break;
        asm volatile("s_sleep 4\n\t" TICKET "\n\ts_waitcnt vmcnt(0)" ::"i"((int)T_CNT) : "memory");
      }
      asm volatile("v_mov_b32 v123, 0\n\ts_mov_b64 exec, 1\n\tds_write_b64 %0, v[122:123]\n\ts_mov_b64 exec, -1" ::"v"(rdy) : "memory");
    }
    vsetu<79>(fo(1024) + qbF * 4352 + fr * 8);
    {
      const int jj = 4 * (fr >> 3) + (fr & 3);
      const unsigned one = ((fq ^ (fr >> 2)) & 1) ? 0u : (jj & 1) ? 0x3f800000u : 0x3f80u;
      static_for([&](auto I) { vsetu<96 + I>((jj >> 1) == I ? one : 0u); }, make_iseq<4>{});
    }
    unsigned vo = (fq >> 1) * 1024 + (fq & 1) * 256 + fr * 16;
    bf16* out = arg_late<bf16, 13>();
    __amdgpu_buffer_rsrc_t so_ = __builtin_amdgcn_make_buffer_rsrc((void*)out, (short)0, SEQ * OUTW * 2, 0x00020000);
    unsigned vw = ((qbF * 128 + (fr & 7)) * OUTW + DA + (fr >> 3) * 32 + fq * 8) * 2;
    asm volatile("" : "+s"(so_));
    asm volatile("s_waitcnt vmcnt(0) lgkmcnt(0)\n\ts_barrier" : "+v"(vo), "+v"(vw) :: "memory");
    fl_set<T_FS>();
    unsigned tk;
    asm volatile("ds_read_b32 %0, %1\n\ts_waitcnt lgkmcnt(0)" : "=v"(tk) : "v"(rdy) : "memory");
    tk = __builtin_amdgcn_readfirstlane(tk) & 255;
    KST(0, 30);
    if (doA) post_math(pj, pu, out, lds + 131072 + wid * 8192);
#define N3 0
#define N2 12
    constexpr int N1 = 28 - 3 * N3 - 2 * N2;
    static_assert(N1 >= 0 && N3 + N2 + N1 <= 16);
    const int nu = tk < N3 ? 3 : tk < N3 + N2 ? 2 : tk < N3 + N2 + N1 ? 1 : 0;
    const int u0 = 4 * (tk < N3 ? 3 * tk : tk < N3 + N2 ? N3 + 2 * tk : 2 * N3 + N2 + tk) + wid * nu;
    KST(0, 28);
    if (nu) {
      unsigned so[3];
#pragma unroll
      for (int k = 0; k < 3; ++k) so[k] = O_S + qbF * 0x200000 + 0x40000 + (u0 + k) * 16384;
      unsigned pend = 0x5555u, cnt = 0x400000;
      while (true) {
        unsigned nw;
        if (wid == 3) {
          u64 m;
          const u64 ex = pend | (pend << 1);
          asm volatile(FL_ARM("%1") "\n\t" FL_POLL "\n\ts_waitcnt vmcnt(0)\n\tv_cmp_eq_u64 %0, s[84:85], v[124:125]" : "=s"(m) : "s"(ex) : "memory");
          nw = (unsigned)m & ((unsigned)m >> 1) & pend;
          if (nw) asm volatile("ds_write_b32 %0, %1 offset:4" ::"v"(rdy), "v"(~pend | nw) : "memory");
        } else {
          unsigned t;
          asm volatile("ds_read_b32 %0, %1 offset:4\n\ts_waitcnt lgkmcnt(0)" : "=v"(t) : "v"(rdy) : "memory");
          nw = __builtin_amdgcn_readfirstlane(t) & pend;
        }
        if (--cnt == 0) nw = pend;
        if (nw == 0) {
          if (wid == 3) asm volatile("s_sleep 4");
          else asm volatile("s_sleep 1");
          continue;
        }
        asm volatile("s_nop 4");
        static_for([&](auto S) {
          if ((nw >> (2 * S)) & 1) {
            r_load<0, 2 * S>(vo, so[0]); r_load<0, 2 * S + 1>(vo, so[0]);
            if (nu > 1) { r_load<1, 2 * S>(vo, so[1]); r_load<1, 2 * S + 1>(vo, so[1]); }
            if (nu > 2) { r_load<2, 2 * S>(vo, so[2]); r_load<2, 2 * S + 1>(vo, so[2]); }
          }
        }, make_iseq<8>{});
        pend &= ~nw;
        if (pend == 0) break;
      }
      asm volatile("s_waitcnt vmcnt(0)" ::: "memory");
      KST(0, 16);
      auto oo = [&](int U) { return ((U & 15) * 8 * OUTW + (U >> 4) * 64) * 2; };
      r_fin<0>(vw, so_, oo(u0));
      if (nu > 1) r_fin<1>(vw, so_, oo(u0 + 1));
      if (nu > 2) r_fin<2>(vw, so_, oo(u0 + 2));
      if (qbF == 15 && tk == 0) asm volatile(FL_SET("%0") ::"s"(fo(6144)) : "memory");
    }
    KST(0, 29);
  }
  asm volatile("s_waitcnt vmcnt(0)" ::: "memory");
  { unsigned tp_; asm volatile("v_readlane_b32 %0, v77, 40" : "=s"(tp_)); if (PSEL) t00 += tp_ - (unsigned)t00; }
  for (int q = 0; q < 1000000; ++q) { unsigned long long t1; asm volatile("s_sleep 1\n\ts_memrealtime %0\n\ts_waitcnt lgkmcnt(0)" : "=s"(t1) :: "memory"); if (t1 - t00 >= SPINT) break; }
  asm volatile("pf_code_end:");
  KST(0, 1);
}

extern "C" void kernel_launch(void* const* d_in, const int* in_sizes, int n_in, void* d_out, int out_size, void* d_ws,
                              size_t ws_size, hipStream_t stream) {
  const bf16* Aq = (const bf16*)d_in[0];
  const bf16* Ak = (const bf16*)d_in[1];
  const bf16* Av = (const bf16*)d_in[2];
  const bf16* Ash = (const bf16*)d_in[3];
  const bf16* Assmk = (const bf16*)d_in[4];
  const bf16* AssmD = (const bf16*)d_in[5];
  const bf16* Bx = (const bf16*)d_in[6];
  const bf16* Bckv = (const bf16*)d_in[7];
  const bf16* Bwq = (const bf16*)d_in[8];
  const bf16* Bwuk = (const bf16*)d_in[9];
  const bf16* Bwuv = (const bf16*)d_in[10];
  const bf16* Bwo = (const bf16*)d_in[11];
  bf16* out = (bf16*)d_out;
  hipFuncSetAttribute((const void*)fused_kernel, hipFuncAttributeMaxDynamicSharedMemorySize, 163840);
  fused_kernel<<<256, 256, 163840, stream>>>(Bckv, Bwuk, Bwuv, Bx, Bwq, (bf16*)d_ws, Ak, Av, Ash, Assmk, AssmD, Bwo, Aq, out);
}
```

```cpp
#define SPINT 10000
#define PROBE 23
#define PSEL (doA)
#define PCYC
#include <hip/hip_runtime.h>
#include <hip/hip_bf16.h>
#include <stdint.h>
#include <utility>

typedef __hip_bfloat16 bf16;
typedef __attribute__((ext_vector_type(4))) short bf16x4;
typedef __attribute__((ext_vector_type(2))) unsigned u32x2;

constexpr int SEQ = 2048;
constexpr int DA = 448;
constexpr int NH = 16;
constexpr int HD = 256;
constexpr int DC = 1024;
constexpr int OUTW = 896;
#define OUTSC 17
#define OUTSCS " sc0 sc1"
#define NTPS " nt"
#define NTVS " nt"
#define NTQS ""
#define NTW 1
#define NYT 15
#define SWAP7 1
constexpr unsigned TSB = NH * SEQ * HD * 2, O_K = TSB, O_S = 3 * TSB, O_P = 5 * TSB, PSTR = 67584, O_F = O_P + 128 * PSTR,
                   O_X = O_F + 400 * 4352, O_Z = O_X + DA * SEQ * 2, WSB = O_Z + DA * SEQ * 2;
constexpr unsigned fo(int w) { return O_F + (w >> 4) * 4352 + (w & 15) * 8; }

#define STAMPK2
#define KST(kid, which) do { if ((kid) == 0 && (which) == PROBE) { __builtin_amdgcn_sched_barrier(0); \
    asm volatile("s_memrealtime s[48:49]\n\ts_waitcnt lgkmcnt(0)\n\tv_writelane_b32 v77, s48, 40\n\ts_memtime s[48:49]\n\ts_waitcnt lgkmcnt(0)\n\tv_writelane_b32 v77, s48, 41" ::: "memory"); __builtin_amdgcn_sched_barrier(0); } } while (0)

__device__ __forceinline__ float bf2f(unsigned short u) { return __uint_as_float(((unsigned)u) << 16); }
__device__ __forceinline__ unsigned short f2bf(float f) { return __bfloat16_as_ushort(__float2bfloat16(f)); }
__device__ __forceinline__ float rbf(float f) { return bf2f(f2bf(f)); }
typedef __attribute__((ext_vector_type(2))) __bf16 bf16x2n;
typedef __attribute__((ext_vector_type(2))) float f32x2n;
__device__ __forceinline__ unsigned pk2(float a, float b) {
  return __builtin_bit_cast(unsigned, __builtin_convertvector((f32x2n){a, b}, bf16x2n));
}

__device__ __forceinline__ int off_b(int row, int ch) { return 256 * row + 16 * (ch ^ (((row & 3) << 2) | ((row >> 2) & 3))); }

template <int R> __device__ __forceinline__ void sset(unsigned x) { asm volatile("s_mov_b32 s%c0, %1" ::"i"(R), "s"(x)); }
template <int R> __device__ __forceinline__ void vsetu(unsigned x) { asm volatile("v_mov_b32 v%c0, %1" ::"i"(R), "v"(x)); }
template <int R> __device__ __forceinline__ void srd_set(const void* p, unsigned n) {
  asm volatile("s_mov_b64 s[%c0:%c1], %4\n\ts_mov_b32 s%c2, %5\n\ts_mov_b32 s%c3, 0x20000" ::"i"(R), "i"(R + 1), "i"(R + 2), "i"(R + 3), "s"(p), "s"(n));
}
template <int R, int L> __device__ __forceinline__ void tload() { asm volatile("v_readlane_b32 s%c0, v78, %1" ::"i"(R), "i"(L)); }
template <int L> __device__ __forceinline__ unsigned tget() { unsigned t; asm volatile("v_readlane_b32 %0, v78, %1" : "=s"(t) : "i"(L)); return t; }
#define FL_SET(S) "buffer_store_dwordx2 v[82:83], v81, s[88:91], " S " offen sc1"
#define FL_ARM(N) "v_cndmask_b32_e64 v80, -2.0, v79, " N
#define FL_POLL "buffer_load_dwordx2 v[124:125], v80, s[88:91], 0 offen sc1"
#define FL_CHECK(N) "s_mov_b32 s48, 0x100000\n7:\n\tv_cmp_eq_u64 vcc, s[84:85], v[124:125]\n\ts_and_b64 vcc, vcc, " N "\n\ts_cmp_eq_u64 vcc, " N \
                    "\n\ts_cbranch_scc1 8f\n\ts_sleep 2\n\t" FL_POLL "\n\ts_waitcnt vmcnt(0)\n\ts_sub_u32 s48, s48, 1\n\ts_cbranch_scc0 7b\n8:"
template <int L> __device__ __forceinline__ void fl_set() {
  asm volatile("v_readlane_b32 s48, v78, %0\n\ts_nop 4\n\t" FL_SET("s48") ::"i"(L) : "memory");
}
template <int L> __device__ __forceinline__ void fl_poll() {
  asm volatile("v_readlane_b32 s50, v78, %0\n\tv_readlane_b32 s51, v78, %1\n\ts_nop 1\n\t" FL_ARM("s[50:51]") "\n\t" FL_POLL ::"i"(L), "i"(L + 1) : "memory");
}
#define TICKET "v_readlane_b32 s48, v78, %0\n\tv_mov_b32 v122, 1\n\tv_mov_b32 v123, 0\n\ts_nop 2\n\tbuffer_atomic_add_x2 v[122:123], v81, s[88:91], s48 offen sc0"
__device__ __forceinline__ void fl_check() { asm volatile(FL_CHECK("s[50:51]") ::: "vcc", "scc", "memory"); }
template <int R> __device__ __forceinline__ float vget() { float t; asm volatile("v_mov_b32 %0, v%c1" : "=v"(t) : "i"(R)); return t; }
template <int R> __device__ __forceinline__ void vset(float x) { asm volatile("v_mov_b32 v%c0, %1" ::"i"(R), "v"(x)); }
template <int R> __device__ __forceinline__ float acc_read1() { float t; asm volatile("v_accvgpr_read_b32 %0, a[%c1]" : "=v"(t) : "i"(R)); return t; }
template <class F, int... I> __device__ __forceinline__ void static_for(F&& f, std::integer_sequence<int, I...>) {
  (f(std::integral_constant<int, I>{}), ...);
}
template <int... Is> using iseq = std::integer_sequence<int, Is...>;
template <int N> using make_iseq = std::make_integer_sequence<int, N>;

typedef __attribute__((ext_vector_type(4))) unsigned u32x4c;
typedef __attribute__((ext_vector_type(4))) float f32x4c;
typedef __attribute__((ext_vector_type(2))) float f32x2c;
typedef unsigned long long u64;
struct PR { u32x4c kk[7], vv[4]; };
__device__ __forceinline__ void prep_load(PR& p, int tid, int cg, int tb, const bf16* __restrict__ k, const bf16* __restrict__ v,
                                          const bf16* __restrict__ shift_k) {
  const __amdgpu_buffer_rsrc_t sk = __builtin_amdgcn_make_buffer_rsrc((void*)k, (short)0, SEQ * DA * 2, 0x00020000);
  const __amdgpu_buffer_rsrc_t sv = __builtin_amdgcn_make_buffer_rsrc((void*)v, (short)0, SEQ * DA * 2, 0x00020000);
  const __amdgpu_buffer_rsrc_t sw = __builtin_amdgcn_make_buffer_rsrc((void*)(shift_k + cg * 256), (short)0, 512, 0x00020000);
  const int vo = ((tb * 128 + (tid >> 3) * 4) * DA + cg * 64 + (tid & 7) * 8) * 2;
#pragma unroll
  for (int q = 0; q < 7; ++q)
    asm volatile("buffer_load_dwordx4 %0, %1, %2, 0 offen" NTPS : "=v"(p.kk[q]) : "v"(vo + (q - 3) * DA * 2), "s"(sk) : "memory");
#pragma unroll
  for (int q = 0; q < 4; ++q)
    asm volatile("buffer_load_dwordx4 %0, %1, %2, 0 offen offset:%3" NTVS : "=v"(p.vv[q]) : "v"(vo), "s"(sv), "i"(q * DA * 2) : "memory");
#pragma unroll
  for (int q = 0; q < 4; ++q)
    asm volatile("buffer_load_dwordx4 a[%c0:%c1], %2, %3, 0 offen offset:%4" ::"i"(4 * q), "i"(4 * q + 3), "v"((tid & 7) * 64), "s"(sw), "i"(q * 16) : "memory");
}
__device__ __forceinline__ float bfl(unsigned u) { return __uint_as_float(u << 16); }
__device__ __forceinline__ float bfh(unsigned u) { return __uint_as_float(u & 0xffff0000u); }
__device__ __forceinline__ f32x2c rbf2(f32x2c a) {
  return (f32x2c){__uint_as_float(pk2(0.f, a[0])), __uint_as_float(pk2(0.f, a[1]))};
}
template <int I> __device__ __forceinline__ void prep_math(const PR& p, int tid, char* lx) {
  const int rp = tid >> 3, c = tid & 7;
  f32x2c wt[4], kf[7];
  const unsigned w[4] = {__float_as_uint(acc_read1<4 * I>()), __float_as_uint(acc_read1<4 * I + 1>()), __float_as_uint(acc_read1<4 * I + 2>()),
                         __float_as_uint(acc_read1<4 * I + 3>())};
#pragma unroll
  for (int t = 0; t < 4; ++t)
    wt[t] = (t & 1) ? (f32x2c){bfh(w[t >> 1]), bfh(w[2 + (t >> 1)])} : (f32x2c){bfl(w[t >> 1]), bfl(w[2 + (t >> 1)])};
#pragma unroll
  for (int q = 0; q < 7; ++q) kf[q] = (f32x2c){bfl(p.kk[q][I]), bfh(p.kk[q][I])};
  f32x2c x[4];
#pragma unroll
  for (int e = 0; e < 4; ++e) {
    f32x2c s = rbf2(kf[e] * wt[3]);
    s = rbf2(s + rbf2(kf[e + 1] * wt[2]));
    s = rbf2(s + rbf2(kf[e + 2] * wt[1]));
    s = rbf2(s + rbf2(kf[e + 3] * wt[0]));
    x[e] = s * (f32x2c){bfl(p.vv[e][I]), bfh(p.vv[e][I])};
  }
#pragma unroll
  for (int u = 0; u < 2; ++u)
    *(u32x2*)(lx + (c * 8 + 2 * I + u) * 256 + (((31 - rp) * 8) ^ (c << 4))) = (u32x2){pk2(x[3][u], x[2][u]), pk2(x[1][u], x[0][u])};
}
__device__ __forceinline__ void prep_store(int tid, int cg, int tb, bf16* __restrict__ xr, const char* lx) {
  const __amdgpu_buffer_rsrc_t sx = __builtin_amdgcn_make_buffer_rsrc((void*)(xr + (size_t)cg * 64 * SEQ), (short)0, 64 * SEQ * 2, 0x00020000);
#pragma unroll
  for (int rd = 0; rd < 4; ++rd) {
    const int idx = tid + 256 * rd, ch = idx >> 4, q = idx & 15;
    u32x4c w = *(const u32x4c*)(lx + ch * 256 + 16 * (q ^ (ch >> 3)));
    __builtin_amdgcn_raw_buffer_store_b128(w, sx, (ch * SEQ + (15 - tb) * 128 + q * 8) * 2, 0, 16);
  }
}

#define CODE_TOUCH(LB, LE, wx, NW, NL, m0v) do { unsigned blo_, bhi_, elo_; \
    asm volatile("s_getpc_b64 s[48:49]\n\ts_add_u32 s48, s48, " LB "@rel32@lo+4\n\ts_addc_u32 s49, s49, " LB "@rel32@hi+12\n\t" \
                 "s_mov_b32 %0, s48\n\ts_mov_b32 %1, s49\n\t" \
                 "s_getpc_b64 s[48:49]\n\ts_add_u32 s48, s48, " LE "@rel32@lo+4\n\ts_mov_b32 %2, s48" \
                 : "=s"(blo_), "=s"(bhi_), "=s"(elo_) :: "scc"); \
    const unsigned n_ = elo_ - (blo_ & ~127u); \
    const __amdgpu_buffer_rsrc_t srd_ = __builtin_amdgcn_make_buffer_rsrc((void*)(((unsigned long long)bhi_ << 32) | (blo_ & ~127u)), (short)0, n_ < 262144u ? n_ : 0u, 0x00020000); \
    const unsigned l_ = threadIdx.x & 63, vo_ = l_ < (NL) ? ((wx) + l_ * (NW)) * 128u : 0x80000000u; \
    asm volatile("s_mov_b32 m0, %0\n\ts_nop 4\n\tbuffer_load_dword %1, %2, 0 offen lds" ::"s"(m0v), "v"(vo_), "s"(srd_) : "memory"); } while (0)
template <int I> __device__ __forceinline__ void d_store() {
  asm volatile("buffer_store_dwordx4 v[%c0:%c1], v%c2, s[88:91], s101 offen offset:%3 sc1" ::"i"(128 + 4 * I), "i"(131 + 4 * I), "i"(112 + ((I >> 2) & 1)),
               "i"((I & 3) * 1024) : "memory");
  if constexpr ((I & 7) == 7) asm volatile("s_addk_i32 s101, 0x4000" ::: "scc");
}
constexpr int NS_X = 19;
struct CV { unsigned pa[4], pb, px, vz, sz; float D; };
constexpr int cv_J(bool up, int q) { int J = up ? 0 : 7; while (q > J) { q -= J + 1; J += up ? 1 : -1; } return J; }
constexpr int cv_i(bool up, int q) { int J = up ? 0 : 7; while (q > J) { q -= J + 1; J += up ? 1 : -1; } return q; }
template <int R, int N> __device__ __forceinline__ void cv_rd(CV& c) {
  if constexpr (N < 16) {
    constexpr int i = N >> 1, o = 16 * R + 128 * (i & 1) + 2 * (N & 1), r = 32 + 32 * (R & 1) + 2 * N;
    asm volatile("ds_read2_b32 a[%c1:%c2], %0 offset0:%c3 offset1:%c4" ::"v"(c.pa[i >> 1]), "i"(r), "i"(r + 1), "i"(o), "i"(o + 1));
  } else {
    constexpr int m = N - 16, r = 192 + 32 * (R & 1) + 4 * m;
    asm volatile("ds_read_b128 a[%c1:%c2], %0 offset:%c3" ::"v"(c.pb), "i"(r), "i"(r + 3), "i"(64 * R + 512 * (7 - m)));
  }
}
template <int O> __device__ __forceinline__ void cv_xd(CV& c) {
  constexpr int e = O & 31, x = 96 + ((e >> 1) ^ 1);
  if constexpr (O >= 64) asm volatile("v_cvt_pk_bf16_f32 v%c0, 0, v%c0" ::"i"(160 + e));
  else if constexpr (O >= 32) asm volatile("v_mul_f32 v%c0, v%c0, %1" ::"i"(160 + e), "v"(c.D));
  else if constexpr (O & 1) asm volatile("v_lshlrev_b32 v%c0, 16, v%c1" ::"i"(160 + e), "i"(x));
  else asm volatile("v_and_b32 v%c0, 0xffff0000, v%c1" ::"i"(160 + e), "i"(x));
}
template <int O> __device__ __forceinline__ void cv_z(CV& c) {
  constexpr int J = O / 11, k = O % 11, y = 128 + 4 * J;
  if constexpr (k < 4) asm volatile("v_cvt_pk_bf16_f32 v%c0, 0, v%c0" ::"i"(y + k));
  else if constexpr (k < 8) asm volatile("v_add_f32 v%c0, v%c0, v%c1" ::"i"(y + k - 4), "i"(y + 28 + k));
  else if constexpr (k < 10) asm volatile("v_cvt_pk_bf16_f32 v%c0, v%c1, v%c2" ::"i"(y + k - 8), "i"(y + 2 * k - 16), "i"(y + 2 * k - 15));
  else asm volatile("buffer_store_dwordx2 v[%c0:%c1], %2, s[88:91], %3 offen offset:%4 sc1" ::"i"(y), "i"(y + 1), "v"(c.vz), "s"(c.sz),
                    "i"(512 * J) : "memory");
}
constexpr int cv_z0(int Q) {
  int o = 0;
  for (int q = 0; q < Q; ++q)
    for (int k = 0; k < (q % 4 == 1 ? 0 : 3); ++k) { const int J = o / 11; if ((J + 1) * (J + 2) / 2 - 1 + (J ? 4 : 7) <= q) ++o; }
  return o;
}
constexpr int cv_xd0(int T) { int o = 0; for (int t = 0; t < T; ++t) o += t % 4 != 1 || (t >= 72 && t < 108); return o < 96 ? o : 96; }
static_assert(cv_xd0(144) == 96);
template <int B, int... Is> __device__ __forceinline__ void cv_zs(CV& c, iseq<Is...>) { (cv_z<B + Is>(c), ...); }
template <int R, int Q> __device__ __forceinline__ void cv_slot(CV& c) {
  constexpr int i = cv_i(R == 7, Q), J = cv_J(R == 7, Q), d = 128 + 4 * J, a = 32 + 32 * (R & 1) + 4 * i, b = 192 + 32 * (R & 1) + 4 * (J - i);
  constexpr int T = 36 * R + Q, vm = Q % 4 == 1 && Q < 32 ? Q / 4 : -1;
  if constexpr (Q == 0) asm volatile("s_waitcnt lgkmcnt(0)");
  if constexpr (R >= 6 && vm >= 0 && vm % 4 == 0) asm volatile("s_add_u32 m0, s79, %0" ::"i"(((R - 6) ^ (NYT & 1)) * 32768 + vm * 1024));
  if constexpr (R == 0 && i == 0)
    asm volatile("v_mfma_f32_16x16x32_bf16 v[%c0:%c1], a[%c2:%c3], a[%c4:%c5], 0" ::"i"(d), "i"(d + 3), "i"(a), "i"(a + 3), "i"(b), "i"(b + 3));
  else
    asm volatile("v_mfma_f32_16x16x32_bf16 v[%c0:%c1], a[%c2:%c3], a[%c4:%c5], v[%c0:%c1]" ::"i"(d), "i"(d + 3), "i"(a), "i"(a + 3), "i"(b), "i"(b + 3));
  if constexpr (Q < 24 && R < 7) cv_rd<R + 1, Q>(c);
  if constexpr (T % 7 == 3 && NS_X + 5 + T / 7 < 32) d_store<NS_X + 5 + T / 7>();
  if constexpr (R == 2 && Q >= 24 && Q < 32)
    asm volatile("ds_read_b64 v[%c1:%c2], %0 offset:%c3" ::"v"(c.px), "i"(96 + 2 * (Q - 24)), "i"(97 + 2 * (Q - 24)), "i"(512 * (31 - Q)));
  if constexpr (R >= 3 && R < 7) if constexpr (cv_xd0(T - 107) > cv_xd0(T - 108)) cv_xd<cv_xd0(T - 108)>(c);
  if constexpr ((R == 3 || R == 4) && vm >= 0) {
    constexpr int f = (R - 3) * 8 + vm;
    asm volatile("buffer_load_dwordx4 a[%c0:%c1], v84, s[88:91], s96 offen offset:%2 sc1" ::"i"(128 + 4 * f), "i"(131 + 4 * f), "i"((f & 3) * 1024) : "memory");
    if constexpr ((f & 3) == 3) asm volatile("s_addk_i32 s96, 4096");
  }
  if constexpr (R == 4 && Q == 35) asm volatile(FL_POLL ::: "memory");
  if constexpr (R >= 6 && vm >= 0)
    asm volatile("buffer_load_dwordx4 v%c0, s[88:91], s98 offen offset:%1 sc1 lds" ::"i"(86 + vm / 4), "i"((vm & 3) * 1024) : "memory");
  if constexpr (R == 6 && Q == 35) asm volatile("s_add_u32 s98, s98, 0x8000");
  if constexpr (R == 7) cv_zs<cv_z0(Q)>(c, make_iseq<cv_z0(Q + 1) - cv_z0(Q)>{});
  __builtin_amdgcn_sched_barrier(0);
}
template <int R, int... Q> __device__ __forceinline__ void cv_step(CV& c, iseq<Q...>) { (cv_slot<R, Q>(c), ...); }
template <int... N> __device__ __forceinline__ void cv_rd0(CV& c, iseq<N...>) { (cv_rd<0, N>(c), ...); }
template <int FLAG> __device__ __forceinline__ void conv_wave(unsigned sz, float Dd, char* lw) {
  int lane = threadIdx.x & 63; asm volatile("" : "+v"(lane));
  const int l16 = lane & 15, fq = lane >> 4;
  u32x4c w[5]; unsigned w4[5];
#pragma unroll
  for (int r = 0; r < 5; ++r) {
    w[r] = *(const u32x4c*)(lw + r * 1024 + lane * 16);
    w4[r] = *(const unsigned*)(lw + r * 1024 + lane * 16 + 16);
  }
  d_store<NS_X>(); d_store<NS_X + 1>();
#pragma unroll
  for (int r = 0; r < 5; ++r)
    *(u32x4c*)(lw + 10304 + r * 1024 + lane * 16) = (u32x4c){__builtin_amdgcn_alignbit(w[r][1], w[r][0], 16), __builtin_amdgcn_alignbit(w[r][2], w[r][1], 16),
                                                             __builtin_amdgcn_alignbit(w[r][3], w[r][2], 16), __builtin_amdgcn_alignbit(w4[r], w[r][3], 16)};
  d_store<NS_X + 2>();
  CV c;
#pragma unroll
  for (int i = 0; i < 4; ++i) c.pa[i] = (unsigned)(size_t)lw + ((l16 + 1) & 1) * 10304 + 4 * ((8 * fq + l16 + 1) >> 1) + 1024 * i;
  c.pb = (unsigned)(size_t)lw + 5120 + (2032 - 16 * l16 + 8 * fq) * 2 - 512 * 7;
  c.vz = (16 * l16 + 4 * fq) * 2;
  c.px = (unsigned)(size_t)lw + 5120 + 2044 * 2 - 512 * 7 - c.vz;
  c.sz = sz; c.D = Dd;
  asm volatile("" : "+v"(c.vz), "+v"(c.px), "+v"(c.D) :: "memory");
  asm volatile("" : "+s"(c.sz));
  cv_rd0(c, make_iseq<24>{});
  d_store<NS_X + 3>(); d_store<NS_X + 4>();
  __builtin_amdgcn_sched_barrier(0);
  static_for([&](auto R) __attribute__((always_inline)) {
    cv_step<R>(c, make_iseq<36>{});
    if constexpr (R == 2) {
      asm volatile("s_waitcnt vmcnt(0)\n\ts_barrier" ::: "memory");
      fl_set<FLAG>();
    }
    if constexpr (R == 5) {
      asm volatile("s_waitcnt vmcnt(0)" ::: "memory");
      fl_check();
    }
    __builtin_amdgcn_sched_barrier(0);
  }, make_iseq<8>{});
  asm volatile("s_nop 7");
  cv_zs<cv_z0(36)>(c, make_iseq<88 - cv_z0(36)>{});
}

__device__ __forceinline__ void post_dma(int cg, int tb, const bf16* __restrict__ q, unsigned m0w) {
  int lane = threadIdx.x & 63; asm volatile("" : "+v"(lane));
  const __amdgpu_buffer_rsrc_t sq = __builtin_amdgcn_make_buffer_rsrc((void*)(q + (size_t)tb * 32 * DA + cg * 64), (short)0, 32 * DA * 2, 0x00020000);
  const unsigned vq = ((lane >> 3) * DA + (lane & 7) * 8) * 2;
  asm volatile("s_mov_b32 m0, %0\n\ts_nop 4\n\tbuffer_load_dwordx4 %1, %2, 0 offen" NTQS " lds\n\tbuffer_load_dwordx4 %1, %2, %3 offen offset:1024" NTQS " lds\n\t"
               "buffer_load_dwordx4 %1, %2, %4 offen offset:2048" NTQS " lds\n\tbuffer_load_dwordx4 %1, %2, %5 offen offset:3072" NTQS " lds"
               ::"s"(m0w + 4096), "v"(vq), "s"(sq), "s"(8 * DA * 2 - 1024), "s"(16 * DA * 2 - 2048), "s"(24 * DA * 2 - 3072) : "memory");
}
__device__ __forceinline__ void post_math(int cg, int tb, bf16* __restrict__ out, const char* lw) {
  int lane = threadIdx.x & 63; asm volatile("" : "+v"(lane));
  const int tl = lane >> 3, ck = lane & 7;
  const __amdgpu_buffer_rsrc_t so = __builtin_amdgcn_make_buffer_rsrc((void*)(out + (size_t)tb * 32 * OUTW + cg * 64), (short)0, 32 * OUTW * 2 - 2 * DA, 0x00020000);
  const unsigned vo = (tl * OUTW + ck * 8) * 2;
#pragma unroll
  for (int i = 0; i < 4; ++i) {
    const u32x4c qv = *(const u32x4c*)(lw + 4096 + i * 1024 + lane * 16);
    const char* lz = lw + ck * 64 + 16 * (i ^ (ck >> 1)) + tl * 2;
    u32x4c o;
#pragma unroll
    for (int e = 0; e < 4; ++e) {
      const float z0 = bf2f(*(const unsigned short*)(lz + 2 * e * 512));
      const float z1 = bf2f(*(const unsigned short*)(lz + (2 * e + 1) * 512));
      o[e] = pk2(bfl(qv[e]) * z0, bfh(qv[e]) * z1);
    }
    __builtin_amdgcn_raw_buffer_store_b128(o, so, vo + i * 8 * OUTW * 2, 0, OUTSC);
  }
}

#define DI __device__ __forceinline__
#define GAP() __builtin_amdgcn_sched_barrier(0)
typedef __attribute__((ext_vector_type(4))) float f32x4g;
typedef __attribute__((ext_vector_type(4))) unsigned u32x4g;
constexpr int LDW = NH * HD;
struct GM { unsigned aA[2], aB[2][8]; };
template <bool W, int J, int V, bool NT = false> DI void g_dma() {
  if constexpr (NT && J == 0) asm volatile("buffer_load_dwordx4 v%c0, s[60:63], 0 offen nt lds" ::"i"(V) : "memory");
  else if constexpr (NT) asm volatile("buffer_load_dwordx4 v%c0, s[60:63], s%c1 offen nt lds" ::"i"(V), "i"(70 + J) : "memory");
  else
  if constexpr (J == 0) asm volatile("buffer_load_dwordx4 v%c0, s[%c1:%c2], 0 offen lds" ::"i"(V), "i"(W ? 60 : 56), "i"(W ? 63 : 59) : "memory");
  else asm volatile("buffer_load_dwordx4 v%c0, s[%c1:%c2], s%c3 offen lds" ::"i"(V), "i"(W ? 60 : 56), "i"(W ? 63 : 59), "i"((W ? 70 : 63) + J) : "memory");
}
template <int P, int KS, int I> DI void g_wread(GM& g) {
  constexpr int nf = I >> 1, hf = I & 1, r = 192 - 64 * P + 8 * nf + 4 * KS + 2 * hf;
  asm volatile("ds_read_b64_tr_b16 a[%c1:%c2], %0 offset:%c3" ::"v"(g.aB[hf][nf]), "i"(r), "i"(r + 1), "i"(P * 32768 + KS * 16384));
}
template <int P, int M, int KS> DI void g_aread(GM& g) {
  constexpr int r = 96 + 8 * (M & 1) + 4 * KS;
  asm volatile("ds_read_b128 v[%c1:%c2], %0 offset:%c3" ::"v"(g.aA[KS]), "i"(r), "i"(r + 3), "i"(P * 32768 + M * 2048));
}
constexpr float CS = 0.0625f * 1.4426950408889634f;
constexpr int NS_Q = 19;
constexpr int e_vm(int G) { return (G * 3) % 8 < 3 ? G * 3 / 8 : -1; }
constexpr int e_st(int MODE, int G) {
  const int q = G & 15, k3 = ((G >> 4) - 1) * 3 + (q == 0 ? 0 : q == 4 ? 1 : 2), k2 = NS_Q + (G >> 4) * 2 + (q == 4);
  if (MODE == 5) return e_vm(G) >= 32 ? e_vm(G) - 32 : -1;
  if (MODE == 3) return G >= 33 && (G - 33) % 5 == 0 && (G - 33) / 5 < NS_X ? (G - 33) / 5 : -1;
  if (MODE == 2) return G >= 30 && (q == 0 || q == 4 || q == 14) && k3 - 2 < NS_Q ? k3 - 2 : -1;
  if (MODE == 4) return (q == 0 || q == 4) && k2 < 32 ? k2 : -1;
  return -1;
}
constexpr int e_nst(int MODE, int G0, int G1) { int n = 0; for (int G = G0; G < G1; ++G) n += e_st(MODE, G) >= 0; return n; }
static_assert(e_nst(2, 0, 128) == NS_Q && e_nst(3, 30, 128) == NS_X && e_nst(5, 0, 128) == 16 && e_nst(4, 0, 104) == 32 - NS_Q);
constexpr int e_slot(int MODE, int G) { return MODE == 3 ? (G % 3 == 0 && G < 30 ? G / 3 : -1) : e_vm(G) >= 0 && e_vm(G) < 32 ? 10 + e_vm(G) : -1; }
struct ES { int op0[129], nop[129]; };
constexpr ES e_sched(bool TR, int MODE) {
  ES z{};
  const bool SCL = MODE == 2 || MODE == 4;
  const int per = SCL ? 16 : 8, o1 = 32 * per;
  int o = MODE == 4 ? e_sched(false, 2).op0[128] : 0;
  for (int G = 0; G <= 128; ++G) {
    const int q = G & 15;
    int cap = 2 - (q == 1 || q == 2 || q == 15);
    if (SCL) cap -= (q >= 3 && q < 11 && (q & 1)) + (q == 7 || q == 11) + 2 * (G == 70 || G == 74 || (G & 63) == 61);
    cap += MODE == 2 ? 4 + (q == 10 || q == 13) : MODE == 3 ? 1 : MODE == 5 ? 2 : 0;
    if (e_st(MODE, G) >= 0 || (SCL ? q == 8 || q == 12 : e_slot(MODE, G) >= 0) || (MODE == 4 && G < 4)) cap = 0;
    if (G == 128) cap = 10000;
    z.op0[G] = o;
    while (o < o1) {
      const int i = o / per, rdy = MODE == 4 ? 0 : (TR ? 32 * (i >> 3) + 16 + 2 * (i & 7) + 1 : 4 * i + 3) + 6;
      if ((G < rdy && G < 128) || cap-- <= 0) break;
      ++o; ++z.nop[G];
    }
  }
  return z;
}
template <bool TR, int MODE> constexpr ES ESCH = e_sched(TR, MODE);
static_assert(ESCH<false, 4>.op0[96] == 512 && ESCH<false, 4>.op0[0] >= 24 * 16);
template <bool TR, bool SCL, int O> DI void e_op() {
  constexpr int per = SCL ? 16 : 8, i = O / per, k = O % per, r = 128 + 4 * i, t = 114 + 4 * (i & 1), c = k - (per - 4);
  if constexpr (k < 4) asm volatile("v_accvgpr_read_b32 v%c0, a[%c1]" ::"i"(t + k), "i"(4 * i + k));
  else if constexpr (c < 0) asm volatile("v_mul_f32 v%c0, s83, v%c0" ::"i"(k < 8 ? r + (k ^ 6) : t + k - 8));
  else {
    constexpr int d = c == 0 ? r + 3 : c == 1 ? r + 2 : r + c - 2, x = c == 0 ? r + 2 : c == 1 ? r : t + 2 * (c - 2);
    asm volatile("v_cvt_pk_bf16_f32 v%c0, v%c1, v%c2" ::"i"(d), "i"(x), "i"(x + 1));
  }
}
template <bool TR, bool SCL, int B, int... Is> DI void e_ops(iseq<Is...>) { (e_op<TR, SCL, B + Is>(), ...); }
template <int S> DI void e_m0() {
  if constexpr (S == 0 || S == 1 || S == 5 || S == 9) asm volatile("s_add_u32 m0, s78, %0" ::"i"(S * 1024));
  if constexpr (S >= 26 && (S - 26) % 4 == 0) asm volatile("s_add_u32 m0, s79, %0" ::"i"(((S - 26) >> 3) * 32768 + ((S - 26) & 4) * 1024));
}
template <int S> DI void e_load() {
  if constexpr (S == 0) asm volatile("buffer_load_dwordx4 v122, s[56:59], 0 offen lds" ::: "memory");
  else if constexpr (S < 5) asm volatile("buffer_load_dwordx4 v84, s[56:59], s64 offen offset:%0 lds" ::"i"((S - 1) * 1024) : "memory");
  else if constexpr (S < 9) asm volatile("buffer_load_dwordx4 v84, s[60:63], 0 offen offset:%0 sc1 lds" ::"i"((S - 5) * 1024) : "memory");
  else if constexpr (S == 9) asm volatile("buffer_load_dwordx4 v84, s[60:63], s62 offen sc1 lds" ::: "memory");
  else if constexpr (S < 26) {
    constexpr int f = S - 10;
    asm volatile("buffer_load_dwordx4 a[%c0:%c1], v84, s[88:91], s96 offen offset:%2 sc1" ::"i"(128 + 4 * f), "i"(131 + 4 * f), "i"((f & 3) * 1024) : "memory");
    if constexpr ((f & 3) == 3) asm volatile("s_addk_i32 s96, 4096");
  } else {
    constexpr int f = S - 26;
    asm volatile("buffer_load_dwordx4 v%c0, s[88:91], s98 offen offset:%1 sc1 lds" ::"i"(86 + ((f >> 2) & 1)), "i"((f & 3) * 1024) : "memory");
    if constexpr (f == 7) asm volatile("s_add_u32 s98, s98, s97");
    if constexpr (f == 15) asm volatile(FL_POLL ::: "memory");
  }
}
template <bool TR, int P, int H, int G, int MODE, bool NT> DI void g_gap(GM& g) {
  constexpr int m = 4 * H + (G >> 4), q = G & 15, n = q >> 1, ks = q & 1, GG = 64 * H + G;
  constexpr bool inV = TR ? m & 1 : n & 1, END = MODE == 3 || MODE == 5;
  constexpr int a = 96 + 8 * (m & 1) + 4 * ks, b = 192 - 64 * P + 8 * n + 4 * ks, d = (inV ? 128 : 0) + 4 * (TR ? 8 * (m >> 1) + n : 4 * m + (n >> 1));
  constexpr int J = END ? -1 : q == 8 ? 2 * (G >> 4) : q == 12 ? 2 * (G >> 4) + 1 : -1, gj = (J & 3) ^ ((J & 3) >> 1);
  if constexpr (END) { if constexpr (q == 0 && GG > 0) asm volatile("s_waitcnt lgkmcnt(0)"); e_m0<e_slot(MODE, GG)>(); }
  else if constexpr (q == 0 && G > 0) asm volatile("s_waitcnt lgkmcnt(4)");
  if constexpr (J == 0) asm volatile("s_add_u32 m0, s78, %0" ::"i"((P ^ H ^ 1) * 32768 + (1 - H) * 8192));
  else if constexpr (J == 4) asm volatile("s_add_u32 m0, s79, %0" ::"i"((P ^ H) * 32768 + (1 - H) * 16384));
  else if constexpr (J > 0 && (J & 3) == 1) asm volatile("s_bitset1_b32 m0, 12");
  else if constexpr (J > 0 && (J & 3) == 2) asm volatile("s_bitset1_b32 m0, %0" ::"i"(J < 4 ? 14 : 13));
  else if constexpr (J > 0) asm volatile("s_bitset0_b32 m0, 12");
#define GMF(D, X, Y, C) asm volatile("v_mfma_f32_16x16x32_bf16 " D "[%c0:%c1], " X ", " Y ", " C ::"i"(d), "i"(d + 3), "i"(a), "i"(a + 3), "i"(b), "i"(b + 3))
#define GMF2(D, C) do { if constexpr (TR) GMF(D, "v[%c2:%c3]", "a[%c4:%c5]", C); else GMF(D, "a[%c4:%c5]", "v[%c2:%c3]", C); } while (0)
  if constexpr ((MODE == 1 || MODE == 4) && ks == 0) { if constexpr (inV) GMF2("v", "0"); else GMF2("a", "0"); }
  else if constexpr (inV) GMF2("v", "v[%c0:%c1]");
  else GMF2("a", "a[%c0:%c1]");
#undef GMF2
#undef GMF
  if constexpr ((q == 1 || q == 2) && !(END && m == 7)) g_aread<(m < 7 ? P : P ^ 1), (m + 1) & 7, q - 1>(g);
  if constexpr (!END && q >= 3 && q < 11 && (q & 1)) g_wread<P ^ 1, H, (G >> 4) * 4 + (q - 3) / 2>(g);
  if constexpr (e_st(MODE, GG) >= 0) {
    static_assert(ESCH<TR, MODE>.op0[GG] >= (e_st(MODE, GG) + 1) * (END ? 8 : 16));
    d_store<e_st(MODE, GG)>();
  }
  if constexpr (J >= 0 && J < 4) g_dma<false, (1 - H) * 2 + (gj & 1) + (gj >> 1) * 4, 90>();
  else if constexpr (J >= 4) g_dma<true, (1 - H) * 4 + gj, 91, NT>();
  if constexpr (MODE >= 2) e_ops<TR, !END, ESCH<TR, MODE>.op0[GG]>(make_iseq<ESCH<TR, MODE>.nop[GG]>{});
  if constexpr (END) {
    if constexpr (e_slot(MODE, GG) >= 0) e_load<e_slot(MODE, GG)>();
  } else {
    if constexpr (H == 1 && G == 6) asm volatile("v_lshl_add_u32 v90, s82, 7, v92\n\ts_and_b32 s82, s80, 15");
    if constexpr (H == 1 && G == 10) asm volatile("s_lshr_b64 s[80:81], s[80:81], 4\n\tv_lshl_add_u32 v91, s82, 19, v93");
    constexpr int nst = e_nst(MODE, H ? 29 : 0, GG + 1) + (MODE == 4 && !H ? e_nst(2, 93, 128) : 0);
    if constexpr (G == 61) asm volatile("s_waitcnt vmcnt(%0) lgkmcnt(0)\n\ts_barrier" ::"i"(12 + nst) : "memory");
  }
  if constexpr (END && GG == 127) {
    asm volatile("s_nop 7");
    e_ops<TR, false, ESCH<TR, MODE>.op0[128]>(make_iseq<ESCH<TR, MODE>.nop[128]>{});
  }
  GAP();
}
template <bool TR, int P, int H, int MODE, bool NT, int... Gs> DI void g_half_(GM& g, iseq<Gs...>) { (g_gap<TR, P, H, Gs, MODE, NT>(g), ...); }
template <bool TR, int P, int H, int MODE, bool NT = false> DI void g_half(GM& g) { g_half_<TR, P, H, MODE, NT>(g, make_iseq<64>{}); }
template <bool TR, int P, int MODE, bool NT = false> DI void g_tile(GM& g) { g_half<TR, P, 0, MODE, NT>(g); g_half<TR, P, 1, MODE, NT>(g); }
template <int P, int... Is> DI void g_read0(GM& g, iseq<Is...>) {
  (g_wread<P, 0, Is>(g), ...); (g_wread<P, 1, Is>(g), ...);
  g_aread<P, 0, 0>(g); g_aread<P, 0, 1>(g);
}
template <int I> DI void g_pro1(const unsigned (&m)[4]) {
  constexpr int grp = I >> 2, i = I & 3, t = grp < 4 ? 0 : grp < 7 ? 1 : 2;
  constexpr bool isA = grp == 0 || grp == 3 || grp == 5;
  constexpr int jj = (grp == 3 ? 2 : 0) + (i & 1) + (i >> 1) * 4, ks = grp == 2 || grp == 6;
  asm volatile("s_add_u32 m0, %0, %1\n\ts_nop 0" ::"s"(m[(t & 1) * 2 + !isA]), "i"(isA ? jj * 4096 : ks * 16384 + (i >> 1) * 8192 + (i & 1) * 4096) : "scc");
  if constexpr (isA) g_dma<false, jj, t ? 90 : 92>();
  else g_dma<true, ks * 4 + i, t ? 91 : 93, NTW >= 2>();
}
template <int B, int... Is> DI void g_pro(const unsigned (&m)[4], iseq<Is...>) { (g_pro1<B + Is>(m), ...); }
template <bool TR, bool SCL, class HOOK> DI void g_loop(GM& g, unsigned evm, HOOK&& hook) {
  constexpr int nkt = SCL ? 7 : 16, P0 = SCL ? 0 : 1;
  asm volatile("" : "+s"(evm));
#pragma unroll 1
  for (int t = 1;; t += 2) {
    g_half<TR, P0 ^ 1, 0, 0, (SCL ? NTW >= 1 : NTW >= 2)>(g);
    if (__builtin_expect((evm >> t) & 1, 0)) { hook(t); GAP(); }
    g_half<TR, P0 ^ 1, 1, 0, (SCL ? NTW >= 1 : NTW >= 2)>(g);
    if (SCL && t + 2 >= nkt) break;
    g_half<TR, P0, 0, 0, (SCL ? NTW >= 1 : NTW >= 2)>(g);
    if constexpr (SCL) { if (__builtin_expect(t == 3, 0)) { hook(4); GAP(); } }
    else asm volatile("s_cmp_eq_u32 %0, 13\n\ts_cselect_b32 s78, 0x28000, s78" ::"s"(t) : "scc");
    g_half<TR, P0, 1, 0, (SCL ? NTW >= 1 : NTW >= 2)>(g);
    if (!SCL && t + 3 >= nkt) break;
  }
}

typedef __attribute__((ext_vector_type(4))) unsigned u32x4;
typedef __attribute__((ext_vector_type(4))) float f32x4;

constexpr int R_S = 128, R_P = 192, R_CI = 224, R_E = 96;
constexpr int R_TS = 112, R_RS = 114, R_M = 116;
constexpr int R_PD = 118, R_THR = 120, R_TMP = 121;
constexpr int r_dk(int q) { return q < 6 ? 90 + q : 120 + q; }
constexpr int S_V = 96, S_T = 97, S_K = 98, S_I = 99, S_EV = 100;
struct AC {
  unsigned rare;
};
DI bool redo() { u64 t; asm volatile("s_mov_b64 %0, s[54:55]" : "=s"(t)); return t != 0; }
template <int X, int F> DI void kread() {
  constexpr int r = 192 + 4 * (F & 15);
  asm volatile("ds_read_b128 a[%c0:%c1], v84 offset:%c2" ::"i"(r), "i"(r + 3), "i"(X * 32768 + F * 1024));
}
template <int X, int F> DI void vread() {
  constexpr int r = 192 + 4 * (F & 15);
  asm volatile("ds_read_b128 a[%c0:%c1], v85 offset:%c2" ::"i"(r), "i"(r + 3), "i"(X * 32768 + (F & 1) * 16384 + (F >> 1) * 1024));
}
template <int X, int... Fs> DI void kreads(iseq<Fs...>) { (kread<X, Fs>(), ...); }
template <int X, int J> DI void dma_m0() {
  if constexpr (J == 0) asm volatile("s_mov_b32 m0, s%c0" ::"i"(78 + X));
  else if constexpr (J == 4) asm volatile("s_or_b32 m0, s%c0, 0x1000" ::"i"(78 + X));
  else if constexpr (J == 8) asm volatile("s_bitset0_b32 m0, 16");
  else if constexpr (J == 12) asm volatile("s_bitset0_b32 m0, 12");
}
template <int J> DI void dma_at() {
  if constexpr (J < 8)
    asm volatile("buffer_load_dwordx4 v%c0, s[88:91], s96 offen offset:%1 sc1 lds" ::"i"(86 + (J >= 4)), "i"((J & 3) * 1024) : "memory");
  else
    asm volatile("buffer_load_dwordx4 v%c0, s[92:95], s98 offen offset:%1 sc1 lds" ::"i"(r_dk(J - 8)), "i"((J & 3) * 1024) : "memory");
}
template <int Q> DI void dma4k() {
  asm volatile("s_add_u32 m0, s79, %0\n\ts_add_u32 s48, s98, %1\n\tbuffer_load_dwordx4 v%c2, s[88:91], s48 offen sc1 lds\n\t"
               "buffer_load_dwordx4 v%c2, s[88:91], s48 offen offset:1024 sc1 lds\n\tbuffer_load_dwordx4 v%c2, s[88:91], s48 offen offset:2048 sc1 lds\n\t"
               "buffer_load_dwordx4 v%c2, s[88:91], s48 offen offset:3072 sc1 lds"
               ::"i"((Q >> 1) * 32768 + (Q & 1) * 4096), "i"((Q >> 1) * 32768), "i"(86 + (Q & 1)) : "memory", "scc");
}
template <int Q> DI void acc_zero2() {
  asm volatile("v_accvgpr_write_b32 a[%c0], 0\n\tv_accvgpr_write_b32 a[%c1], 0" ::"i"(2 * Q), "i"(2 * Q + 1));
}
#define SMV 0
struct SMS { int ex[128], op0[128], nop[128], last; };
constexpr SMS sm_sched() {
  SMS z{};
  int et[32] = {}, e = 0;
  for (int t = 0; t < 128; ++t) {
    const int g = t & 15;
    const bool x = t < 48 ? (g >= 8 && (g & 1) && g < 15) : t < 64 ? (g >= 9 && g < 15) : (g >= 8 && g < 14);
    z.ex[t] = x && e < 32 ? e : -1;
    if (z.ex[t] >= 0) et[e++] = t;
  }
  int o = 0;
  for (int t = 0; t < 128; ++t) {
    const int g = t & 15;
    int cap = z.ex[t] >= 0 || t < 8 || (t < 48 && (g == 7 || (g >= 8 && !(g & 1)))) || t == 56 || t == 63 || t == 111 ? 0 : 1;
    z.op0[t] = o; z.nop[t] = 0;
    while (cap > 0 && o < 48 && et[2 * (o / 3) + (o % 3 == 0 ? 0 : 1)] + 2 <= t) {
      const int el = 2 * (o / 3) + o % 3;
      if (!(o % 3 < 2 && (el == 0 || el == 4))) --cap;
      ++o; ++z.nop[t]; z.last = t;
    }
  }
  for (int k = 16; k < 32; ++k) if (z.op0[et[k]] < 3 * ((k - 16) / 2 + 1)) z.last = 1000;
  return z;
}
constexpr SMS SM = sm_sched();
static_assert(SM.last <= 117);
constexpr int p_reg(int Y, int k) { return R_P + 16 * Y + 4 * ((k >> 3) * 2 + ((k >> 1) & 1)) + ((k >> 2) & 1) * 2 + (k & 1); }
template <int Y, int O> DI void sm_op() {
  constexpr int k = O / 3, e = 2 * k + O % 3, ts = R_TS + ((e >> 2) & 1);
  if constexpr (O % 3 == 2) asm volatile("v_cvt_pk_bf16_f32 v%c0, v%c1, v%c2" ::"i"(p_reg(Y, k)), "i"(R_E + ((2 * k) & 15)), "i"(R_E + ((2 * k + 1) & 15)));
  else if constexpr (e == 1 || e == 5) asm volatile("v_add_f32_e32 v%c0, v%c1, v%c2" ::"i"(ts), "i"(R_E + e - 1), "i"(R_E + e));
  else if constexpr (e != 0 && e != 4) asm volatile("v_add_f32_e32 v%c0, v%c0, v%c1" ::"i"(ts), "i"(R_E + (e & 15)));
}
template <int Y, int T> DI void sm_gap(AC& c) {
  if constexpr (SM.ex[T] >= 0) asm volatile("v_exp_f32 v%c0, v%c1" ::"i"(R_E + (SM.ex[T] & 15)), "i"(R_S + 32 * Y + SM.ex[T]));
  if constexpr (SM.nop[T] > 0) sm_op<Y, SM.op0[T]>();
  if constexpr (SM.nop[T] > 1) sm_op<Y, SM.op0[T] + 1>();
  if constexpr (SM.nop[T] > 2) sm_op<Y, SM.op0[T] + 2>();
  if constexpr (T == SM.last + 1) asm volatile("v_add_f32_e32 v%c0, v%c1, v%c2" ::"i"(R_TMP), "i"(R_TS), "i"(R_TS + 1));
  if constexpr (T == SM.last + 2) asm volatile("v_cmp_nle_f32_e64 s[54:55], v%c0, v%c1" ::"i"(R_TMP), "i"(R_THR));
}
#define W_A0 1
template <int X, int MODE, int G> DI void gapA(AC& c) {
  constexpr bool FIRST = MODE == 1 || MODE == 2, NOQK = MODE >= 3;
  constexpr int Xp = X ^ 1, kb = G >> 4, g = G & 15, qb = g >> 3, s = g & 7;
  if constexpr (MODE == 3 && G < 48 && G % 3 == 0) {
    constexpr int f = G / 3;
    asm volatile("buffer_load_dwordx4 a[%c0:%c1], v84, s[88:91], s75 offen offset:%2 sc1" ::"i"(128 + 4 * f), "i"(131 + 4 * f), "i"((f & 3) * 1024) : "memory");
    if constexpr ((f & 3) == 3) asm volatile("s_addk_i32 s75, 4096");
  }
  if constexpr (G == 48) asm volatile("s_waitcnt vmcnt(%0) lgkmcnt(0)\n\ts_barrier" ::"i"(MODE == 3 ? 16 : MODE == 2 ? 12 : MODE == 1 ? W_A0 : 0) : "memory");
  else if constexpr (g == 0 && !NOQK) asm volatile("s_waitcnt lgkmcnt(0)");
  if constexpr (!NOQK) {
    constexpr int r = 192 + 4 * ((kb & 1) * 8 + s), q = 128 + 4 * g;
    constexpr int d = R_S + 32 * X + 4 * (kb * 2 + qb), ci = s == 0 ? R_CI + 4 * (kb * 2 + qb) : d;
    if constexpr (FIRST && s == 0)
      asm volatile("v_mfma_f32_16x16x32_bf16 v[%c0:%c1], a[%c2:%c3], a[%c4:%c5], 0" ::"i"(d), "i"(d + 3), "i"(r), "i"(r + 3), "i"(q), "i"(q + 3));
    else
      asm volatile("v_mfma_f32_16x16x32_bf16 v[%c0:%c1], a[%c2:%c3], a[%c4:%c5], v[%c6:%c7]" ::"i"(d), "i"(d + 3), "i"(r), "i"(r + 3), "i"(q), "i"(q + 3),
                   "i"(ci), "i"(ci + 3));
    if constexpr (g < 8 && kb < 3) kread<X, (kb + 1) * 8 + g>();
    constexpr int S0 = SMV ? 40 : 14, S1 = SMV ? 41 : 30, S2 = SMV ? 42 : 45, S3 = SMV ? 43 : 46;
    if constexpr (G == S0) asm volatile("s_add_u32 s96, s97, 0x1000000");
    if constexpr (G == S1) dma_m0<X, 0>();
    if constexpr (G == S2) asm volatile("s_mov_b32 s97, s98");
    if constexpr (G == S3) asm volatile("s_add_u32 s98, s98, s99");
    if constexpr (G >= 56 && !(G & 1)) dma_at<(G - 56) / 2>();
    if constexpr (G == 57) asm volatile("s_lshr_b32 s100, s100, 1\n\ts_add_u32 s56, s56, 1");
    if constexpr (G == 59 && !FIRST) asm volatile("s_and_b32 s49, s100, 1\n\ts_or_b32 s49, s49, s54");
    if constexpr (G == 61 && !FIRST) asm volatile("s_or_b32 %0, s49, s55" : "=s"(c.rare));
    if constexpr (G == 61 && FIRST) asm volatile("s_and_b32 %0, s100, 1" : "=s"(c.rare));
  } else if constexpr (SM.nop[64 + G] > 0) asm volatile("s_nop 0");
  if constexpr (!FIRST && g < 8 && kb == 3) vread<Xp, g>();
  if constexpr (MODE == 2 && (G & 3) == 1) d_store<16 + G / 4>();
  if constexpr (FIRST) acc_zero2<G>();
  else sm_gap<Xp, 64 + G>(c);
  GAP();
}
template <int R> DI void rescale1(float alpha) {
  float t;
  asm volatile("v_accvgpr_read_b32 %0, a[%c2]\n\tv_mul_f32 %0, %0, %1\n\tv_accvgpr_write_b32 a[%c2], %0" : "=&v"(t) : "v"(alpha), "i"(R));
}
template <int... Rs> DI void rescale_all(const float (&alpha)[2], iseq<Rs...>) { (rescale1<Rs>(alpha[(Rs >> 2) & 1]), ...); }
template <bool MAX> DI float row_reduce(float v) {
  auto p = __builtin_amdgcn_permlane32_swap(__float_as_uint(v), __float_as_uint(v), false, false);
  v = MAX ? fmaxf(__uint_as_float(p[0]), __uint_as_float(p[1])) : __uint_as_float(p[0]) + __uint_as_float(p[1]);
  p = __builtin_amdgcn_permlane16_swap(__float_as_uint(v), __float_as_uint(v), false, false);
  return MAX ? fmaxf(__uint_as_float(p[0]), __uint_as_float(p[1])) : __uint_as_float(p[0]) + __uint_as_float(p[1]);
}
template <int Y, int QB> DI float row_max() {
  constexpr int r = R_S + 32 * Y + 4 * QB;
  float mx;
  asm volatile("v_max_f32 %0, v%c1, v%c2\n\tv_max3_f32 %0, %0, v%c3, v%c4" : "=&v"(mx) : "i"(r), "i"(r + 1), "i"(r + 2), "i"(r + 3));
  static_for([&](auto K) {
    constexpr int q = r + 8 * (K + 1);
    float t = mx;
    asm volatile("v_max3_f32 %0, %0, v%c1, v%c2\n\tv_max3_f32 %0, %0, v%c3, v%c4" : "+v"(t) : "i"(q), "i"(q + 1), "i"(q + 2), "i"(q + 3));
    mx = t;
  }, make_iseq<3>{});
  return row_reduce<true>(mx);
}
template <int R> DI void v_sub(float d) { asm volatile("v_sub_f32 v%c0, v%c0, %1" ::"i"(R), "v"(d)); }
template <int B> DI void s_shift(const float (&d)[2]) { static_for([&](auto E) { v_sub<B + E>(d[(E >> 2) & 1]); }, make_iseq<32>{}); }
DI void m_shift(const float (&d)[2]) {
  s_shift<R_CI>(d);
  v_sub<R_M>(-d[0]); v_sub<R_M + 1>(-d[1]);
}
DI void set_mask0(int wid) {
  const int mr = wid * 32 + __float_as_int(vget<89>());
  static_for([&](auto I) {
    constexpr int e = I, k = (e >> 3) * 16 - ((e >> 2) & 1) * 16 + (e & 3);
    const float x = vget<R_S + e>();
    vset<R_S + e>(k > mr ? -INFINITY : x);
  }, make_iseq<32>{});
}
template <int X> DI void sm_redo() {
  constexpr int Xp = X ^ 1;
  float d[2], dt[2], alpha[2], ts[2] = {0.f, 0.f};
  static_for([&](auto Q) {
    constexpr int qb = Q;
    const float pd = vget<R_PD + qb>();
    d[qb] = fmaxf(row_max<Xp, qb>() - pd, 0.f);
    dt[qb] = d[qb] + pd;
    vset<R_PD + qb>(d[qb]);
    alpha[qb] = __builtin_amdgcn_exp2f(-d[qb]);
    vset<R_RS + qb>(vget<R_RS + qb>() * alpha[qb]);
  }, make_iseq<2>{});
  vset<R_THR>(d[0] > 0.f || d[1] > 0.f ? -1.f : 4096.f);
  m_shift(d);
  static_for([&](auto K) {
    constexpr int k = K, qb = (k >> 1) & 1;
    const float e0 = __builtin_amdgcn_exp2f(vget<R_S + 32 * Xp + 2 * k>() - dt[qb]), e1 = __builtin_amdgcn_exp2f(vget<R_S + 32 * Xp + 2 * k + 1>() - dt[qb]);
    ts[qb] += e0; ts[qb] += e1;
    vset<p_reg(Xp, k)>(__uint_as_float(pk2(e0, e1)));
  }, make_iseq<16>{});
  vset<R_TS>(ts[0]); vset<R_TS + 1>(ts[1]);
  rescale_all(alpha, make_iseq<128>{});
  asm volatile("s_nop 1" ::: "memory");
  GAP();
}
template <int X, int MODE, int... Gs> DI void phaseA_(AC& c, iseq<Gs...>) { (gapA<X, MODE, Gs>(c), ...); }
template <int X, int MODE> DI void phaseA(AC& c) { phaseA_<X, MODE>(c, make_iseq<64>{}); }

template <int X, bool SMX, int G, bool WAIT = true> DI void gapB(AC& c) {
  constexpr int Xp = X ^ 1, blk = G >> 4, g = G & 15;
  if constexpr (g == 0 && WAIT) asm volatile("s_waitcnt lgkmcnt(0)");
  constexpr int qb = (g >> 1) & 1, s = g & 1, r = 192 + 4 * ((blk & 1) * 8 + (g >> 2) * 2 + s), o = 4 * ((blk * 4 + (g >> 2)) * 2 + qb);
  constexpr int p = R_P + 16 * Xp + 4 * (s * 2 + qb);
  asm volatile("v_mfma_f32_16x16x32_bf16 a[%c0:%c1], a[%c2:%c3], v[%c4:%c5], a[%c0:%c1]" ::"i"(o), "i"(o + 3), "i"(r), "i"(r + 3), "i"(p), "i"(p + 3));
  if constexpr (g < 8) {
    if constexpr (blk < 3) vread<Xp, (blk + 1) * 8 + g>();
    else if constexpr (SMX) kread<Xp, g>();
  }
  if constexpr (SMX) {
    if constexpr (G == 1 || G == 2) asm volatile("v_add_f32_e32 v%c0, v%c0, v%c1" ::"i"(R_RS + G - 1), "i"(R_TS + G - 1));
    if constexpr (g == 7 && blk < 3) dma_m0<X, blk * 4 + 4>();
    if constexpr (g >= 8 && !(g & 1) && blk < 3) dma_at<blk * 4 + 4 + (g - 8) / 2>();
    if constexpr (G == 55 && X == 1) asm volatile("s_cmp_eq_u32 s56, s76");
    if constexpr (G == 3) asm volatile("s_lshr_b32 s77, s77, 1");
    if constexpr (G == 4) asm volatile("s_bitcmp1_b32 s77, 0");
    if constexpr (G == 5) asm volatile("s_cselect_b64 s[52:53], s[62:63], 0");
    if constexpr (G == 6) asm volatile(FL_ARM("s[52:53]"));
    if constexpr (G == 56) asm volatile(FL_POLL ::: "memory");
    sm_gap<X, G>(c);
  }
  GAP();
}
template <int X, int... Gs> DI void phaseB_(AC& c, iseq<Gs...>) { (gapB<X, true, Gs>(c), ...); }
template <int X> DI void phaseB(AC& c) { phaseB_<X>(c, make_iseq<64>{}); }
constexpr int sm_nex() { int n = 0; for (int t = 0; t < 64; ++t) n += SM.ex[t] >= 0; return n; }
template <int X> DI void phaseB0(unsigned mode, int wid) {
  dma_m0<X, 4>();
  kreads<X ^ 1>(make_iseq<8>{});
  dma_at<4>();
  asm volatile("s_nop 3");
  float d[2];
  if (mode & 2) {
    d[0] = vget<122>(); d[1] = vget<123>();
    dma_at<5>();
  } else {
    if constexpr (X == 0) if (__builtin_expect(mode & 4, 0)) set_mask0(wid);
    d[0] = row_max<X, 0>(); GAP(); dma_at<5>();
    d[1] = row_max<X, 1>();
#pragma unroll
    for (int qb = 0; qb < 2; ++qb) if (!(d[qb] > -3.0e38f)) d[qb] = 0.f;
  }
  GAP();
  vset<R_M>(d[0]); vset<R_M + 1>(d[1]);
  dma_at<6>();
  const f32x2c nd[2] = {{-d[0], -d[0]}, {-d[1], -d[1]}};
  static_for([&](auto E) {
    constexpr int e = E;
    const f32x2c v = nd[(e >> 1) & 1];
    asm volatile("v_pk_add_f32 v[%c0:%c1], v[%c0:%c1], %4\n\tv_mov_b64 v[%c2:%c3], %4" ::"i"(R_S + 32 * X + 2 * e), "i"(R_S + 32 * X + 2 * e + 1), "i"(R_CI + 2 * e), "i"(R_CI + 2 * e + 1), "v"(v));
    if constexpr (e == 3) dma_at<7>();
    if constexpr (e == 6) dma_m0<X, 8>();
    if constexpr (e == 7 || e == 11 || e == 15) dma_at<8 + (e - 7) / 4>();
  }, make_iseq<16>{});
  static_assert(SM.op0[64] >= 3 * ((sm_nex() - 15) / 2) && SM.op0[64] <= 24);
  static_for([&](auto E) {
    constexpr int e = E;
    if constexpr (e < sm_nex()) asm volatile("v_exp_f32 v%c0, v%c1" ::"i"(R_E + e), "i"(R_S + 32 * X + e));
    if constexpr (e == 3) dma_at<11>();
    if constexpr (e == 6) dma_m0<X, 12>();
    if constexpr (e == 7 || e == 11 || e == 15) dma_at<12 + (e - 7) / 4>();
  }, make_iseq<16>{});
  static_for([&](auto O) { sm_op<X, O>(); }, make_iseq<SM.op0[64]>{});
  dma_at<15>();
  static_for([&](auto E) { asm volatile("v_exp_f32 v%c0, v%c1" ::"i"(R_E + (int)E), "i"(R_S + 32 * X + 16 + (int)E)); }, make_iseq<(sm_nex() > 16 ? sm_nex() - 16 : 0)>{});
  asm volatile("buffer_store_dwordx2 v[%c0:%c1], v88, s[88:91], s76 offen sc1\n\ts_bitcmp1_b32 s77, 0\n\ts_cselect_b64 s[52:53], s[62:63], 0\n\t"
               FL_ARM("s[52:53]") "\n\t" FL_POLL ::"i"(R_M), "i"(R_M + 1) : "memory", "scc");
  GAP();
}
constexpr int T_ZO = 8;
#define NINF2(R) "v_mov_b64 v[" #R ":" #R "+1], v[122:123]\n\t"
#define NINF4(R) NINF2(R) NINF2(R + 2)
#define TRI(R) "v_cndmask_b32_e64 v[" #R "], v[" #R "], v122, s[48:49]\n\tv_cndmask_b32_e64 v[" #R "+1], v[" #R "+1], v122, s[50:51]\n\t" \
               "v_cndmask_b32_e64 v[" #R "+2], v[" #R "+2], v122, s[52:53]\n\tv_cndmask_b32_e64 v[" #R "+3], v[" #R "+3], v122, s[54:55]\n\t"
DI void ev_code() {
  asm volatile(
    "s_getpc_b64 s[82:83]\n\ts_add_u32 s82, s82, 12\n\ts_addc_u32 s83, s83, 0\n\ts_branch 9f\n\t"
    "s_bitcmp0_b32 s58, s56\n\ts_cbranch_scc1 1f\n\t" FL_SET("s70") "\n\t" FL_SET("s71") "\n"
    "1:\n\ts_bitcmp0_b32 s57, s56\n\ts_cbranch_scc1 2f\n\t" FL_CHECK("s[62:63]") "\n\t"
    "s_add_u32 s98, s98, s68\n\ts_mov_b32 s68, s69\n\ts_mov_b32 s69, 0\n\ts_mov_b64 s[62:63], s[64:65]\n\ts_mov_b64 s[64:65], s[66:67]\n"
    "2:\n\ts_bitcmp0_b32 s59, s56\n\ts_cbranch_scc1 3f\n\ts_mov_b32 s98, s72\n\ts_bitcmp0_b32 s73, 0\n\ts_cbranch_scc1 3f\n\t"
    "v_lshrrev_b32 v90, 7, v86\n\tv_and_b32 v91, 6, v90\n\tv_lshlrev_b32 v91, 4, v91\n\tv_and_b32 v92, 0x70, v86\n\tv_xor_b32 v91, v91, v92\n\t"
    "s_movk_i32 s48, 896\n\tv_mad_u32_u24 v94, v90, s48, v91\n\ts_mov_b64 s[92:93], s[80:81]\n\ts_mov_b32 s94, 229376\n\ts_movk_i32 s99, 128\n\t"
    "v_add_u32 v90, 28672, v94\n\tv_add_u32 v91, 34816, v94\n\tv_add_u32 v92, 40960, v94\n\tv_add_u32 v93, 47104, v94\n\t"
    "v_add_u32 v95, 6144, v94\n\tv_add_u32 v126, 12288, v94\n\tv_add_u32 v127, 18432, v94\n"
    "3:\n\ts_bitcmp0_b32 s60, s56\n\ts_cbranch_scc1 4f\n\t"
    "v_readlane_b32 s54, v78, %0\n\ts_add_u32 m0, s78, 0x10000\n\t"
    "s_lshr_b32 s53, s73, 16\n\ts_mulk_i32 s53, 3584\n\ts_mov_b64 s[48:49], s[80:81]\n\ts_add_u32 s50, s53, 3584\n\ts_mov_b32 s51, 0x20000\n\t"
    "v_lshlrev_b32 v121, 3, v84\n\tbuffer_load_dword v121, s[48:51], s53 offen lds\n\t"
    "s_bitcmp0_b32 s73, 3\n\ts_cbranch_scc1 4f\n\t"
    "v_lshlrev_b32 v121, 9, v84\n\tv_and_b32 v121, 0x38000, v121\n\tv_lshrrev_b32 v122, 9, v84\n\tv_lshl_add_u32 v121, v122, 12, v121\n\t"
    "v_lshrrev_b32 v122, 3, v84\n\tv_xor_b32 v122, v122, v84\n\tv_and_or_b32 v121, v122, 48, v121\n\t"
    "buffer_load_dwordx4 v121, s[88:91], s54 offen sc1 lds\n\ts_addk_i32 s54, 7168\n\t"
    "buffer_load_dwordx4 v121, s[88:91], s54 offen offset:1024 sc1 lds\n\ts_addk_i32 s54, 7168\n\t"
    "buffer_load_dwordx4 v121, s[88:91], s54 offen offset:2048 sc1 lds\n\ts_addk_i32 s54, 7168\n\t"
    "buffer_load_dwordx4 v121, s[88:91], s54 offen offset:3072 sc1 lds\n"
    "4:\n\ts_bitcmp0_b32 s61, s56\n\ts_cbranch_scc1 6f\n\t"
    "s_sub_u32 s74, s74, 64\n\ts_cmp_ge_i32 s74, 0\n\ts_cbranch_scc1 6f\n\t"
    "v_cmp_gt_i32 s[48:49], 0, v89\n\tv_cmp_gt_i32 s[50:51], 1, v89\n\tv_cmp_gt_i32 s[52:53], 2, v89\n\tv_cmp_gt_i32 s[54:55], 3, v89\n\t"
    "v_mov_b32 v122, 0xff800000\n\tv_mov_b32 v123, 0xff800000\n\t" NINF4(248)
    "s_cmp_eq_i32 s74, -32\n\ts_cbranch_scc0 5f\n\t" TRI(240) TRI(252) "s_setpc_b64 s[86:87]\n"
    "5:\n\t" NINF4(240) NINF4(244) NINF4(252) NINF4(232) TRI(224) TRI(236)
    "s_cmp_eq_i32 s74, -64\n\ts_cbranch_scc1 6f\n\t" NINF4(224) NINF4(228) NINF4(236)
    "6:\n\ts_setpc_b64 s[86:87]\n9:"
    ::"i"(T_ZO) : "scc");
}
DI void ev_call() { asm volatile("s_swappc_b64 s[86:87], s[82:83]" ::: "vcc", "scc", "memory"); GAP(); }

#define ACLOB_ALL "a0","a1","a2","a3","a4","a5","a6","a7","a8","a9","a10","a11","a12","a13","a14","a15","a16","a17","a18","a19","a20","a21","a22","a23","a24","a25","a26","a27","a28","a29","a30","a31","a32","a33","a34","a35","a36","a37","a38","a39","a40","a41","a42","a43","a44","a45","a46","a47","a48","a49","a50","a51","a52","a53","a54","a55","a56","a57","a58","a59","a60","a61","a62","a63","a64","a65","a66","a67","a68","a69","a70","a71","a72","a73","a74","a75","a76","a77","a78","a79","a80","a81","a82","a83","a84","a85","a86","a87","a88","a89","a90","a91","a92","a93","a94","a95","a96","a97","a98","a99","a100","a101","a102","a103","a104","a105","a106","a107","a108","a109","a110","a111","a112","a113","a114","a115","a116","a117","a118","a119","a120","a121","a122","a123","a124","a125","a126","a127","a128","a129","a130","a131","a132","a133","a134","a135","a136","a137","a138","a139","a140","a141","a142","a143","a144","a145","a146","a147","a148","a149","a150","a151","a152","a153","a154","a155","a156","a157","a158","a159","a160","a161","a162","a163","a164","a165","a166","a167","a168","a169","a170","a171","a172","a173","a174","a175","a176","a177","a178","a179","a180","a181","a182","a183","a184","a185","a186","a187","a188","a189","a190","a191","a192","a193","a194","a195","a196","a197","a198","a199","a200","a201","a202","a203","a204","a205","a206","a207","a208","a209","a210","a211","a212","a213","a214","a215","a216","a217","a218","a219","a220","a221","a222","a223","a224","a225","a226","a227","a228","a229","a230","a231","a232","a233","a234","a235","a236","a237","a238","a239","a240","a241","a242","a243","a244","a245","a246","a247","a248","a249","a250","a251","a252","a253","a254","a255"

struct EP { float fa[2], fb[2]; };
template <int I, int E, int MODE> DI void epi_pair(const EP& p) {
  constexpr int qb = I & 1, r = 16 * (I >> 1) + 4 * qb + (E >> 1) * 8 + (E & 1) * 2, t = 96 + 2 * E, w = 128 + 4 * I + E;
  if constexpr (MODE == 2) asm volatile("v_accvgpr_read_b32 v106, a[%c0]" ::"i"(w));
  asm volatile("v_accvgpr_read_b32 v%c0, a[%c2]\n\tv_accvgpr_read_b32 v%c1, a[%c3]" ::"i"(t), "i"(t + 1), "i"(r), "i"(r + 1));
  if constexpr (MODE) asm volatile("v_mul_f32 v%c0, v%c0, %2\n\tv_mul_f32 v%c1, v%c1, %2" ::"i"(t), "i"(t + 1), "v"(p.fa[qb]));
  if constexpr (MODE == 2)
    asm volatile("v_lshlrev_b32 v104, 16, v106\n\tv_and_b32 v105, 0xffff0000, v106\n\tv_fmac_f32 v%c0, v104, %2\n\tv_fmac_f32 v%c1, v105, %2"
                 ::"i"(t), "i"(t + 1), "v"(p.fb[qb]));
  asm volatile("v_cvt_pk_bf16_f32 v%c0, v%c1, v%c2" ::"i"(w), "i"(t), "i"(t + 1));
}
template <int I> DI void epi_store() {
  asm volatile("buffer_store_dwordx4 v[%c0:%c1], v84, s[88:91], s101 offen offset:%2 sc1" ::"i"(128 + 4 * I), "i"(131 + 4 * I), "i"((I & 3) * 1024) : "memory");
  if constexpr ((I & 3) == 3) asm volatile("s_addk_i32 s101, 4096");
}
constexpr int R_TB = 107, R_VS = 111;
template <int HF, int R> DI void p_read() {
  constexpr int mb = R >> 4, r = 64 * HF + 2 * R;
  asm volatile("ds_read_b64_tr_b16 a[%c1:%c2], v%c0 offset:%c3" ::"i"(R_TB + 2 * HF + mb), "i"(r), "i"(r + 1), "i"((R & 15) * 2048));
}
template <int QB, int E> DI void p_cvt() {
  asm volatile("v_cvt_pk_bf16_f32 v%c0, v%c1, v%c2" ::"i"(208 + 4 * QB + E), "i"(192 + 8 * QB + 2 * E), "i"(193 + 8 * QB + 2 * E));
}
template <int QB, int PR> DI void p_store() {
  asm volatile("buffer_store_dwordx4 v[%c0:%c1], v%c2, s[88:91], s96 offen offset:%3 sc1" ::"i"(208 + 4 * QB), "i"(211 + 4 * QB), "i"(R_VS + QB),
               "i"(PR * 128) : "memory");
}
constexpr int p_dma(int HF, int G) { return G == 3 && !HF ? 0 : G == 11 ? 1 - HF : G == 15 ? 2 - HF : G == 19 ? 3 - HF : G == 27 && HF ? 3 : -1; }
template <int HF, int G> DI void p_gap() {
  constexpr int ch = G >> 3, t = G & 7, a = 64 * HF + 32 * (ch & 1) + 4 * t, b = 128 + 4 * (2 * t + (ch >> 1)), d = 192 + 4 * ch;
  if constexpr (t == 0)
    asm volatile("v_mfma_f32_16x16x32_bf16 v[%c0:%c1], a[%c2:%c3], v[%c4:%c5], 0" ::"i"(d), "i"(d + 3), "i"(a), "i"(a + 3), "i"(b), "i"(b + 3));
  else
    asm volatile("v_mfma_f32_16x16x32_bf16 v[%c0:%c1], a[%c2:%c3], v[%c4:%c5], v[%c0:%c1]" ::"i"(d), "i"(d + 3), "i"(a), "i"(a + 3), "i"(b), "i"(b + 3));
  p_read<HF ^ 1, G>();
  if constexpr (G == 0) asm volatile("v_xor_b32 v%c0, s100, v%c0" ::"i"(R_TB + 2 * HF + 1));
  if constexpr (G == 17) asm volatile("v_xor_b32 v%c0, s100, v%c0" ::"i"(R_TB + 2 * (HF ^ 1)));
  if constexpr (G >= 4 && G <= 8 && G != 7) p_cvt<1, (G < 7 ? G - 4 : 3)>();
  if constexpr (G == 24 || (G == 7 && HF == 0)) asm volatile("s_waitcnt lgkmcnt(8)");
  if constexpr (G == 7 && HF == 1) asm volatile("s_waitcnt vmcnt(13) lgkmcnt(8)\n\ts_barrier" ::: "memory");
  if constexpr (G == 10) p_store<1, HF ^ 1>();
  if constexpr (G >= 20 && G < 24) p_cvt<0, G - 20>();
  if constexpr (G == 25) p_store<0, HF>();
  if constexpr (p_dma(HF, G) >= 0) dma_at<(HF ? 8 : 12) + p_dma(HF, G)>();
  if constexpr (HF == 0 && G == 2) asm volatile("s_bitset0_b32 m0, 12");
  if constexpr (HF == 0 && G == 13) asm volatile("s_add_u32 s96, s96, 0x40000");
  if constexpr (HF == 0 && G == 26) asm volatile("s_add_u32 s98, s98, 128");
  if constexpr (HF == 0 && G == 27) asm volatile("s_cmp_lt_u32 s98, 0x380");
  if constexpr (HF == 0 && G == 28) asm volatile("s_cselect_b32 s99, s99, 0x31000");
  if constexpr (HF == 1 && G == 1) asm volatile("s_xor_b32 s100, s100, 0x10000");
  if constexpr (HF == 1 && G == 2) asm volatile("s_xor_b32 s97, s97, s100");
  if constexpr (HF == 1 && G == 9) asm volatile("s_or_b32 m0, s97, s99");
  if constexpr (HF == 1 && G == 30) asm volatile("s_cmp_lt_u32 s98, 0x500");
  GAP();
}
template <int HF, int... Gs> DI void p_half(iseq<Gs...>) { (p_gap<HF, Gs>(), ...); }
constexpr int f_dma(int G) { return G == 2 ? 0 : G == 5 ? 1 : G == 8 ? 2 : G >= 12 && G <= 36 && G % 6 == 0 ? 1 + G / 6 : G >= 50 && G <= 68 && G % 6 == 2 ? G / 6 : -1; }
constexpr int f_ndma(int G) { int n = 0; for (int g = 0; g < G; ++g) n += f_dma(g) >= 0; return n; }
static_assert(f_ndma(74) == 12);
template <int MODE, int G> DI void gapF(AC& c, const EP& p) {
  if constexpr (MODE && G == 48) asm volatile("s_waitcnt lgkmcnt(6)\n\ts_barrier" ::: "memory");
  if constexpr (G < 64) gapB<0, false, G, !(MODE && G == 48)>(c);
  if constexpr (MODE && G >= 42 && G < 64) p_read<0, G - 42>();
  if constexpr (MODE && G >= 64 && G < 69) { p_read<0, 2 * G - 106>(); p_read<0, 2 * G - 105>(); }
  if constexpr (MODE && f_dma(G) >= 0) dma_at<8 + (f_dma(G) & 7)>();
  if constexpr (MODE == 2 && G >= 10 && (G - 10) % 16 == 0) asm volatile("s_waitcnt vmcnt(%0)" ::"i"(13 - (G - 10) / 4 + f_ndma(G + 1)) : "memory");
  if constexpr (G >= 10) epi_pair<(G - 10) / 4, (G - 10) & 3, MODE>(p);
  if constexpr (MODE == 3 && G < 64 && (G & 1)) {
    constexpr int db = G >> 2, qb = (G >> 1) & 1, o = 4 * (db * 2 + qb), a = 224 + 4 * (db & 1), b = 128 + 4 * ((db >> 1) * 2 + qb);
    if constexpr ((G & 7) == 1) asm volatile("s_waitcnt vmcnt(%0)" ::"i"(15 - (G >> 2) + f_ndma(G + 1)) : "memory");
    asm volatile("v_mfma_f32_16x16x32_bf16 a[%c0:%c1], v[%c2:%c3], a[%c4:%c5], a[%c0:%c1]" ::"i"(o), "i"(o + 3), "i"(a), "i"(a + 3), "i"(b), "i"(b + 3));
  }
  if constexpr (MODE == 0) {
    if constexpr (G >= 14 && ((G - 14) & 3) == 0) epi_store<(G - 14) / 4>();
    if constexpr (G == 73) epi_store<15>();
  } else {
    if constexpr (G == 0) asm volatile("s_or_b32 m0, s78, 0x1000\n\ts_movk_i32 s98, 256");
    if constexpr (G == 15) asm volatile("s_bitset0_b32 m0, 12");
    if constexpr (G == 48) asm volatile("s_or_b32 m0, s79, 0x1000\n\ts_movk_i32 s98, 384");
    if constexpr (G == 73) asm volatile("s_waitcnt vmcnt(4) lgkmcnt(0)" ::: "memory");
  }
  GAP();
}
template <int MODE, int... Gs> DI void phaseF(AC& c, const EP& p, iseq<Gs...>) { (gapF<MODE, Gs>(c, p), ...); }
template <int U, int K> DI void r_load(unsigned vo, unsigned so) {
  asm volatile("buffer_load_dwordx4 a[%c0:%c1], %2, s[88:91], %3 offen offset:%4 sc1" ::"i"(64 * U + 4 * K), "i"(64 * U + 4 * K + 3), "v"(vo),
               "s"(so + (K >> 2) * 4096), "i"((K & 1) * 512 + ((K >> 1) & 1) * 2048) : "memory");
}
template <int U, int K> DI void r_sum() {
  constexpr int d = 100 + 4 * (K & 1), b = 64 * U + 4 * K;
  if constexpr (K < 2) asm volatile("v_mfma_f32_16x16x32_bf16 v[%c0:%c1], v[96:99], a[%c2:%c3], 0" ::"i"(d), "i"(d + 3), "i"(b), "i"(b + 3));
  else asm volatile("v_mfma_f32_16x16x32_bf16 v[%c0:%c1], v[96:99], a[%c2:%c3], v[%c0:%c1]" ::"i"(d), "i"(d + 3), "i"(b), "i"(b + 3));
}
template <int U> DI void r_fin(unsigned vo, __amdgpu_buffer_rsrc_t srd, unsigned so) {
  asm volatile("" : "+s"(srd), "+s"(so));
  static_for([&](auto K) { r_sum<U, K>(); }, make_iseq<16>{});
  asm volatile("s_nop 7");
  static_for([&](auto E) { asm volatile("v_cvt_pk_bf16_f32 v%c0, v%c1, v%c2" ::"i"(116 + 4 * U + E), "i"(100 + 2 * E), "i"(101 + 2 * E)); }, make_iseq<4>{});
  asm volatile("buffer_store_dwordx4 v[%c3:%c4], %0, %1, %2 offen" OUTSCS ::"v"(vo), "s"(srd), "s"(so), "i"(116 + 4 * U), "i"(119 + 4 * U) : "memory");
}

#define CMAX 12
#define PREPX false
static_assert(NYT <= 16 && NYT >= 12);
enum { T_Q0, T_K0, T_OQ, T_OG, T_FPREP, T_FQ, T_FG, T_FS,   T_CNT = 9, T_NPRE = 10, T_NST = 12, T_TAB = 14, T_SLAB = 16, T_PART, T_IT = 18, T_ITN = 23 };
struct TRow { unsigned v[64]; };
constexpr u64 kvmask(int m0, int m1) { u64 n = 0; for (int m = m0; m <= m1; ++m) n |= 0x101ull << m; return n; }
constexpr u64 k_order(bool isY, int rr) {
  const int cls = (rr + (rr >> 2)) & 3;
  return !isY ? (cls == 0 ? 0xfedcb9875a463210ull : cls == 1 ? 0xefadc6982b574310ull : cls == 2 ? 0xfebad7693c285410ull : 0xefcba8764d392510ull)
              : (cls == 0 ? 0xfedcb987543a6210ull : cls == 1 ? 0xefadc698254b7310ull : cls == 2 ? 0xfebad769325c8410ull : 0xefcba876432d9510ull);
}
constexpr TRow t_row(int r, int wid, int j) {
  TRow t{};
  const int rr = r & 15, hsel = rr & 1, pj = rr >> 1, kv = pj & 1, h = j + 8 * hsel, u = hsel * 8 + j;
  const bool isY = r >= 16, solo = pj == 7;
  const int mt = (rr >> 2) + (isY ? 4 : 0), g = j * 128, NY = solo ? 18 : NYT;
  const int nx0 = 32 - 2 * pj - NY, cx = nx0 < CMAX ? nx0 : CMAX, ex = nx0 - cx, qbF = isY != (solo && SWAP7) ? 15 - pj : pj;
  const unsigned oQ = (unsigned)h << 20, oK = oQ + O_K, oP = O_P + (h * 8 + pj) * PSTR;
  if (wid == 4) {
    for (int l = 0; l < 64; ++l)
      t.v[l] = fo(l < 16 ? g + hsel * 16 + l : l < 24 ? g + 16 + hsel * 8 + l : l == 24 ? g + 48 + hsel * 8 + pj : l < 32 ? g + 64 + hsel * 8 + pj :
                  l < 48 ? 2304 + pj * 16 + l - 32 : 2048 + pj * 16 + l - 48);
    return t;
  }
  auto put64 = [&](int i, u64 x) { t.v[i] = (unsigned)x; t.v[i + 1] = (unsigned)(x >> 32); };
  t.v[T_Q0] = oQ + ((solo ? qbF : 15 - pj) * 128 + wid * 32) * 512;
  t.v[T_K0] = oK + (isY ? cx : 0) * 32768;
  t.v[T_OQ] = oQ + pj * 131072 + (wid >> 1) * 65536 + (wid & 1) * 4096;
  t.v[T_OG] = oK + kv * TSB + mt * 131072 + (wid >> 1) * 65536 + (wid & 1) * (kv ? 8192 : 4096);
  t.v[T_FPREP] = fo(2048 + pj * 16 + u);
  t.v[T_FQ] = fo(g + 32 + hsel * 8 + pj);
  t.v[T_FG] = fo(g + hsel * 16 + kv * 8 + mt);
  t.v[T_FS] = fo(1024 + qbF * 16 + j * 2 + hsel);
  t.v[T_CNT] = fo(2560 + qbF * 16);
  t.v[T_ZO] = O_Z + pj * 64 * SEQ * 2 + (u * 4 + wid) * 64;
  const u64 own = 0x1ull << (kv * 8 + mt), needQ = isY ? 1ull << (16 + (qbF >> 1)) : (1ull << (16 + (pj >> 1))) | (1ull << (16 + ((15 - pj) >> 1)));
  put64(T_NPRE, isY ? needQ | (1ull << (cx >> 2)) | (1ull << ((cx + 1) >> 2)) : solo ? needQ : needQ | 0xffffull << 48);
  put64(T_NST, isY ? kvmask(cx >> 2, 3) | (solo ? 0 : 1ull << 25) : kvmask(0, solo ? 3 : 0));
  put64(T_TAB, k_order(isY, rr));
  t.v[T_SLAB] = O_S + qbF * 0x200000 + wid * 65536 + (j * 2 + hsel) * 1024;
  t.v[T_PART] = oP;
  for (int it = 0; it < 2; ++it) {
    unsigned* p = t.v + T_IT + it * T_ITN;
    const bool last = isY || it == 1;
    const int n = solo ? 2 * qbF + 2 : isY ? NY : it ? 2 * pj + 2 : nx0;
    const u64 need2 = solo ? 0 : kvmask(0, (2 * pj + 1) >> 2) | (0xffffull << 32);
    u64 q[3] = {0, 0, 0};
    unsigned jmp[2] = {0, 0};
    if (isY) {
      p[0] = (solo && SWAP7 ? 0 : 1u << (14 - cx)) | (solo ? 0 : 1u << (n - 2));
      q[0] = kvmask(4, (31 - 2 * pj) >> 2) & ~own; jmp[0] = ex * 32768; q[1] = solo ? 0 : 1ull << 24;
      p[1] = 2; p[13] = t.v[T_FG]; p[14] = fo(g + 96 + r);
    } else if (it == 0) {
      p[0] = 2u | (ex ? 1u << (cx - 2) : 0);
      q[0] = kvmask(0, cx > 0 ? (cx - 1) >> 2 : 0);
      if (ex) { q[1] = kvmask(4, 4 + ((ex - 1) >> 2)); jmp[1] = (16 - cx) * 32768; q[2] = need2; } else q[1] = need2;
      p[1] = 2; p[13] = fo(2304 + pj * 16 + u) + j * (8 * 4352 - 8); p[14] = fo(g + 64 + hsel * 8 + pj);
    } else {
      p[0] = p[1] = solo ? 0 : 1;
      q[0] = need2;
      if (solo && SWAP7) { p[0] = 1u << 14; q[0] = kvmask(4, 4); } p[13] = fo(g + 48 + hsel * 8 + pj); p[14] = fo(g + 96 + r);
    }
    p[2] = 1u << (n - 2);
    p[3] = !last ? 0 : n > 2 ? 1u << (n - 3) : 1u;
    p[4] = !last ? 0 : n > 2 ? 3u << (n - 3) : 1u << 31;
    for (int k = 0; k < 3; ++k) { p[5 + 2 * k] = (unsigned)q[k]; p[6 + 2 * k] = (unsigned)(q[k] >> 32); }
    p[11] = jmp[0]; p[12] = jmp[1];
    p[15] = oK;
    p[16] = (last ? 1 : 0) | (isY && !solo ? 2 : 0) | (n == 2 ? 4 : 0) | (isY || solo ? 0 : 8) | (n - 1) << 8 | ((r >> 1) * 4 + wid) << 16;
    p[17] = wid * 32 - (n == 2 ? 64 : 0);
    p[18] = oQ + (pj * 128 + wid * 32) * 512;
    p[19] = 0;
    p[20] = (p[0] | (last ? 0 : 1u << n)) >> 1;
    p[21] = oK + (isY ? cx : 0) * 32768;
    p[22] = ((p[0] | p[1] | p[2] | p[3] | p[4]) & 0x7fffffffu) << 1;
  }
  return t;
}
template <int I> constexpr TRow t_base = I < 160 ? t_row(I / 5, I % 5, 0) : [] {
  TRow a = t_row(0, (I - 160) * 4, 0), b = t_row(0, (I - 160) * 4, 1);
  for (int l = 0; l < 64; ++l) a.v[l] = b.v[l] - a.v[l];
  return a;
}();
template <int I> constexpr bool t_linear = [] {
  for (int j = 1; j < 8; ++j) {
    const TRow b = t_row(I / 5, I % 5, j);
    for (int l = 0; l < 64; ++l) if (b.v[l] != t_base<I>.v[l] + j * t_base<160 + (I % 5) / 4>.v[l]) return false;
  }
  return true;
}();
struct Tab { TRow row[162]; };
template <int... I> constexpr Tab t_make(iseq<I...>) {
  static_assert((t_linear<(I < 160 ? I : 0)> && ...));
  return Tab{{t_base<I>...}};
}
__device__ const Tab g_tab = t_make(make_iseq<162>{});

typedef __attribute__((ext_vector_type(16))) unsigned u32x16;
template <class T, int I> DI T* arg_late() {
  unsigned lo, hi;
  asm volatile("v_readlane_b32 %0, v77, %2\n\tv_readlane_b32 %1, v77, %3\n\ts_nop 4" : "=s"(lo), "=s"(hi) : "i"(2 * I), "i"(2 * I + 1));
  return (T*)(((u64)hi << 32) | lo);
}
__global__ __launch_bounds__(256, 1) __attribute__((amdgpu_num_vgpr(77), amdgpu_num_sgpr(56))) void fused_kernel(
    const bf16* ckv_, const bf16* wuk_, const bf16* wuv_, const bf16* x_, const bf16* wq_, bf16* ws_, const bf16* ak_, const bf16* av_,
    const bf16* ash_, const bf16* assmk_, const bf16* assmD_, const bf16* wo_, const bf16* aq_, bf16* out_) {
  extern __shared__ __attribute__((aligned(16))) char lds[];
  unsigned long long t00, c00; asm volatile("s_memrealtime %0\n\ts_memtime %1\n\ts_waitcnt lgkmcnt(0)" : "=s"(t00), "=s"(c00) :: "memory");
  const void* kap = (const void*)__builtin_amdgcn_kernarg_segment_ptr();
  u32x16 ka; u64 ka8;
  asm volatile("at_code_begin:\n\ts_load_dwordx16 %0, %2, 0\n\ts_load_dwordx2 %1, %2, 0x40" : "=&s"(ka), "=&s"(ka8) : "s"(kap) : ACLOB_ALL, "v255", "s101");
  GAP();
  const int tid = threadIdx.x, lane = tid & 63;
  const int wid = __builtin_amdgcn_readfirstlane(tid >> 6);
  const int b = blockIdx.x, j = b & 7, r = b >> 3, rr = r & 15, hsel = rr & 1, pj = rr >> 1, kv = pj & 1;
  const bool isY = r >= 16, solo = pj == 7, doP = isY != PREPX && !solo, doA = !isY && !solo;
  const int h = j + 8 * hsel, mt = (rr >> 2) + (isY ? 4 : 0), pu = (hsel * 8 + j) * 4 + wid;
  constexpr unsigned m0d = 131072 + 24576;
  const unsigned lA = isY ? 65536 : 0, lW = lA ^ 65536, po = isY ? 0 : 32768;
  const unsigned mp[4] = {lA + wid * 1024 + po, lW + wid * 1024 + po, lA + wid * 1024 + (po ^ 32768), lW + wid * 1024 + (po ^ 32768)};
  {
    const int lda = isY ? DA : DC;
    const unsigned ra = wid * 8 + (lane >> 3), sa = ((lane & 7) ^ (((wid & 1) * 4 + (lane >> 4)) & 7)) * 16;
    const unsigned vbA = ra * (lda * 2) + sa, vbB = (4 * wid + (lane >> 4)) * LDW * 2 + 16 * ((lane & 15) ^ (((lane >> 4) << 2) | wid));
    vsetu<92>(vbA); vsetu<93>(vbB); vsetu<90>(vbA + 128); vsetu<91>(vbB + 0x80000); vsetu<94>(ra * (DC * 2) + sa);
    static_for([&](auto J) {
      sset<64 + J>((J + 1) * 64 * lda);
      asm volatile("s_mov_b32 s%c0, %1" ::"i"(71 + J), "i"((((J + 1) >> 2) * 32 + 16 * ((J + 1) & 1)) * LDW * 2 + (((J + 1) >> 1) & 1) * 256));
    }, make_iseq<7>{});
    sset<78>(lA + wid * 1024); sset<79>(lW + wid * 1024);
    GAP();
  }
  asm volatile("s_waitcnt lgkmcnt(0)" : "+s"(ka), "+s"(ka8));
  auto arg = [&](int i) { return (const bf16*)(((u64)ka[2 * i + 1] << 32) | ka[2 * i]); };
  const bf16 *ckv = arg(0), *wuk = arg(1), *wuv = arg(2), *x = arg(3), *wq = arg(4), *ak = arg(6), *av = arg(7), *ash = (const bf16*)ka8;
  bf16* Qb = (bf16*)arg(5);
  KST(0, 0);
  bf16* xqT = (bf16*)((char*)Qb + O_X);
  const bf16* gA = ckv + (size_t)mt * 256 * DC;
  const bf16* gW = (kv ? wuv : wuk) + h * 256;
  unsigned dv = 0;
  {
    GM g;
    PR pr;
    if (doP) prep_load(pr, tid, pj, hsel * 8 + j, ak, av, ash);
    srd_set<56>(isY ? x + (size_t)pj * 256 * DA : gA, isY ? 256 * DA * 2 : 256 * DC * 2);
    srd_set<60>(isY ? wq + h * 256 : gW, isY ? (DA - 1) * LDW * 2 + 512 : (DC - 1) * LDW * 2 + 512);
    g_pro<0>(mp, make_iseq<12>{});
    srd_set<88>(Qb, WSB);
    {
      const unsigned* tp = g_tab.row[r * 5].v;
      asm volatile("buffer_load_dwordx2 v[120:121], off, s[88:91], %0 sc1\n\tglobal_load_dword v78, %1, %3\n\tglobal_load_dword v79, %2, %3 offset:1024\n\t"
                   "global_load_dword v118, %2, %4\n\tglobal_load_dword v119, %2, %4 offset:256\n\tglobal_load_dword v77, %5, %6"
                   ::"s"(fo(6144)), "v"(tid * 4), "v"(lane * 4), "s"(tp), "s"(g_tab.row[160].v), "v"((lane < 27 ? lane : 27) * 4), "s"(kap) : "memory");
    }
    KST(0, 26);
    if (doP) {
      asm volatile("s_waitcnt vmcnt(18)" ::: "memory");
#pragma unroll
      for (int q = 0; q < 7; ++q) asm volatile("" : "+v"(pr.kk[q]));
#pragma unroll
      for (int q = 0; q < 4; ++q) asm volatile("" : "+v"(pr.vv[q]));
      KST(0, 24);
      static_for([&](auto Ic) __attribute__((always_inline)) {
        constexpr int I = Ic;
        g_pro<12 + 4 * I>(mp, make_iseq<4>{});
        GAP();
        prep_math<I>(pr, tid, lds + 131072);
        GAP();
      }, make_iseq<4>{});
      asm volatile("s_waitcnt lgkmcnt(0)\n\ts_barrier" ::: "memory");
      prep_store(tid, pj, hsel * 8 + j, xqT, lds + 131072);
    } else g_pro<12>(mp, make_iseq<4>{});
    GAP();
    CODE_TOUCH("at_code_begin", "pf_code_end", r * 4 + wid, 128u, 8, m0d);
    {
      const int fr = lane & 15, fq = lane >> 4, q = (lane & 15) >> 2, p = lane & 3;
#pragma unroll
      for (int k2 = 0; k2 < 2; ++k2)
        g.aA[k2] = lA + (wid >> 1) * 16384 + (fr >> 3) * 1024 + (fr & 7) * 128 + (((4 * k2 + fq) ^ (fr >> 1)) * 16);
#pragma unroll
      for (int hf = 0; hf < 2; ++hf)
#pragma unroll
        for (int nf = 0; nf < 8; ++nf)
          g.aB[hf][nf] = lW + (wid & 1) * 8192 + off_b(8 * fq + 4 * hf + q, nf * 2 + (p >> 1)) + 8 * (p & 1);
      asm volatile("" : "+v"(g.aA[0]), "+v"(g.aA[1]));
#pragma unroll
      for (int nf = 0; nf < 8; ++nf) asm volatile("" : "+v"(g.aB[0][nf]), "+v"(g.aB[1][nf]));
      vsetu<81>(tid == 0 ? 0u : 0x80000000u);
      vsetu<84>(lane * 16); vsetu<85>(lane * 16 + 65536); vsetu<86>(wid * 8192 + lane * 16); vsetu<87>(wid * 8192 + lane * 16 + 4096);
      vsetu<88>(65536 + wid * 256 + fr * 16); vsetu<89>(fr - 4 * fq);
      asm volatile("s_mov_b64 s[92:93], s[88:89]\n\ts_mov_b64 s[94:95], s[90:91]");
    }
    KST(0, 25);
    if (doP) asm volatile("s_waitcnt vmcnt(13)" ::: "memory");
    else {
      g_pro<16>(mp, make_iseq<12>{});
      asm volatile("s_waitcnt vmcnt(8)" ::: "memory");
    }
    asm volatile("s_barrier" ::: "memory");
    if (isY) g_read0<0>(g, make_iseq<16>{});
    else g_read0<1>(g, make_iseq<16>{});
    GAP();
    {
      u64 z = (((u64)__float_as_uint(vget<121>()) << 32) | __float_as_uint(vget<120>())) + 0x9e3779b97f4a7c15ull;
      z = (z ^ (z >> 30)) * 0xbf58476d1ce4e5b9ull; z = (z ^ (z >> 27)) * 0x94d049bb133111ebull; z ^= z >> 31;
      vsetu<82>((unsigned)z); vsetu<83>((unsigned)(z >> 32));
      asm volatile("v_mad_u32_u24 v78, v118, %0, v78\n\tv_mad_u32_u24 v79, v119, %0, v79\n\tv_readfirstlane_b32 s84, v82\n\tv_readfirstlane_b32 s85, v83\n\t"
                   "v_readlane_b32 s80, v78, %1\n\tv_readlane_b32 s81, v78, %2" ::"s"(j), "i"((int)T_TAB), "i"(T_TAB + 1));
      if (isY) asm volatile("s_mov_b64 s[80:81], 0x6543210");
      asm volatile("s_bfe_u32 s82, s80, 0x40008\n\ts_lshr_b64 s[80:81], s[80:81], 12\n\tv_lshl_add_u32 v91, s82, 19, v93" ::: "scc");
    }
    asm volatile("s_waitcnt lgkmcnt(0)\n\ts_barrier" ::: "memory");
    g_pro<28>(mp, make_iseq<4>{});
    GAP();
    if (isY) {
      KST(0, 14);
      g_tile<false, 0, 1, NTW >= 1>(g);
      KST(0, 15);
      g_loop<false, true>(g, PREPX ? 0x20u : 0x22u, [&](int t) __attribute__((always_inline)) {
        if (t == 1) fl_set<T_FPREP>();
        else if (t == 4) { tload<80, T_TAB>(); tload<81, T_TAB + 1>(); srd_set<60>(gW, (DC - 1) * LDW * 2 + 512); }
        else {
          srd_set<56>(gA, 256 * DC * 2);
          asm volatile("v_mov_b32 v92, v94");
          static_for([&](auto J) { asm volatile("s_mov_b32 s%c0, %1" ::"i"(64 + J), "i"((J + 1) * 64 * DC)); }, make_iseq<7>{});
        }
      });
      KST(0, 18);
      tload<101, T_OQ>(); sset<83>(__float_as_uint(CS));
      asm volatile("v_mov_b32 v112, v84\n\tv_add_u32 v113, 8192, v84");
      GAP();
      g_tile<false, 0, 2, NTW >= 2>(g);
      KST(0, 3);
    }
#define TT 3
    auto kvtile = [&](auto TRc) __attribute__((always_inline)) {
      constexpr bool TR = TRc;
      KST(0, 19);
      if (isY) g_tile<TR, 1, 4, NTW >= 2>(g);
      else g_tile<TR, 1, 1, NTW >= 2>(g);
      KST(0, 20);
      g_loop<TR, false>(g, 0x2000u | (isY || doA ? 2u : 1u << TT), [&](int t) __attribute__((always_inline)) {
        int l = threadIdx.x & 63; asm volatile("" : "+v"(l));
        if (t == 13) {
          asm volatile("s_mov_b32 s79, 0x28000");
          fl_poll<T_NPRE>();
          if ((b & 15) == 0)
            asm volatile("v_readlane_b32 s48, v78, %0\n\tv_and_b32 v122, 0xffffff00, v82\n\tv_mov_b32 v123, v83\n\ts_nop 2\n\t"
                         "buffer_store_dwordx2 v[122:123], v81, s[88:91], s48 offen sc1" ::"i"((int)T_CNT) : "memory");
          if (!doA && !isY) {
            CODE_TOUCH("at_code_begin", "pf_code_end", hsel * 8 + wid * 2, 16u, 64, m0d);
            CODE_TOUCH("at_code_begin", "pf_code_end", hsel * 8 + wid * 2 + 1, 16u, 64, m0d);
          }
          if (doA) {
            const char* tp = arg_late<const char, 9>() + (size_t)(pj * 64 + pu) * (SEQ * 2) + (l & 31) * 128;
            asm volatile("s_mov_b32 m0, %1\n\ts_nop 0\n\tglobal_load_lds_dword %0, off" ::"v"(tp), "i"(m0d) : "memory");
          }
        } else if (isY) fl_set<T_FQ>();
        else if (doA) { if (PREPX) fl_set<T_FPREP>(); post_dma(pj, pu, arg_late<const bf16, 12>(), 131072 + wid * 8192); }
        else {
          const char* tp = arg_late<const char, 0>() + 1024 * DC * 2 + (pu * 256 + l) * 128;
          asm volatile("s_mov_b32 m0, %1\n\ts_nop 0\n\tglobal_load_lds_dword %0, off" ::"v"(tp), "i"(m0d) : "memory");
          asm volatile("global_load_lds_dword %0, off" ::"v"(tp + 8192) : "memory");
          asm volatile("global_load_lds_dword %0, off" ::"v"(tp + 16384) : "memory");
          asm volatile("global_load_lds_dword %0, off" ::"v"(tp + 24576) : "memory");
        }
      });
      KST(0, 23);
      tload<101, T_OG>(); tload<96, T_Q0>(); tload<98, T_K0>();
      fl_check();
      tload<50, T_NST>(); tload<51, T_NST + 1>();
      asm volatile("v_mov_b32 v112, v84\n\tv_add_u32 v113, %0, v84\n\tv_mov_b32 v90, v87\n\tv_mov_b32 v91, v87\n\tv_mov_b32 v92, v87\n\tv_mov_b32 v93, v87\n\t"
                   "v_mov_b32 v94, v86\n\tv_mov_b32 v95, v86\n\tv_mov_b32 v126, v86\n\tv_mov_b32 v127, v86\n\ts_mov_b32 s99, 0x8000\n\t" FL_ARM("s[50:51]")
                   ::"i"(TR ? 4096 : 8192));
      if (doA) {
        const int d = pj * 64 + pu;
        const bf16* dp = arg_late<const bf16, 10>() + d;
        asm volatile("global_load_ushort %0, %1, off" : "=v"(dv) : "v"(dp) : "memory");
        srd_set<56>(arg_late<const bf16, 9>() + (size_t)d * SEQ, SEQ * 2);
        srd_set<60>(arg_late<const char, 5>() + O_X + d * SEQ * 2, SEQ * 2);
        sset<78>(65536 + wid * 15488);
        asm volatile("s_movk_i32 s64, 992\n\tv_subrev_u32 v122, 32, v84");
        GAP();
        g_tile<TR, 0, 3>(g);
      } else {
        sset<79>(isY ? wid * 8192 : 0x30000);
        if ((NYT & 1) && !solo) asm volatile("s_add_u32 s98, s98, 0x8000\n\ts_mov_b32 s97, 0xffff8000" ::: "scc");
        else asm volatile("s_mov_b32 s97, 0x8000");
        GAP();
        g_tile<TR, 0, 5>(g);
      }
    };
    if (kv) kvtile(std::true_type{});
    else kvtile(std::false_type{});
  }
  KST(0, 4);
  asm volatile("s_waitcnt vmcnt(16)" : "+v"(dv) :: "memory");
  sset<79>(wid * 8192);
  if (doA) {
    const float Dd = bf2f(dv);
    KST(0, 5);
    conv_wave<T_FG>(O_Z + (pj * 64 + pu) * SEQ * 2, Dd, lds + 65536 + wid * 15488);
    const char* tp = arg_late<const char, 11>() + (size_t)h * (HD * DA * 2) + ((pj * 4 + wid) * 64 + lane) * 128;
    asm volatile("s_mov_b32 m0, %1\n\ts_nop 0\n\tglobal_load_lds_dword %0, off\n\ts_waitcnt vmcnt(17)" ::"v"(tp), "i"(m0d) : "memory");
    KST(0, 6);
  } else if (!isY) {
    static_for([&](auto I) { d_store<16 + I>(); }, make_iseq<16>{});
    tload<98, T_K0>();
    asm volatile("s_waitcnt vmcnt(0)\n\ts_barrier" ::: "memory");
    fl_set<T_FG>();
    KST(0, 5);
    fl_check();
    CODE_TOUCH("att_begin", "pf_code_end", hsel * 4 + wid, 8u, 64, m0d);
    dma4k<0>(); dma4k<1>(); dma4k<2>(); dma4k<3>();
    asm volatile("s_waitcnt vmcnt(0)" ::: "memory");
    KST(0, 6);
  } else {
    tload<76, T_PART>();
    fl_check();
    asm volatile("buffer_load_dwordx2 v[122:123], v88, s[88:91], s76 offen offset:1024 sc1" ::: "memory");
  }
  AC c;
  sset<78>(65536 + wid * 8192); sset<79>(98304 + wid * 8192);
  { const bf16* wp = arg_late<const bf16, 11>() + (size_t)h * HD * DA; asm volatile("s_mov_b64 s[80:81], %0" ::"s"(wp)); }
  asm volatile("att_begin:");
  ev_code();
  asm volatile("s_waitcnt lgkmcnt(0)\n\ts_barrier" ::: "memory");
#pragma unroll 1
  for (int it = doA ? 0 : 1; it < 2; ++it) {
    const bool odd = (NYT & 1) && !solo && (isY || it == 0);
    if (odd) kreads<1>(make_iseq<8>{});
    else kreads<0>(make_iseq<8>{});
    const unsigned oP = tget<T_PART>();
    if (it == 0) {
      static_for([&](auto I) { tload<57 + I, T_IT + I>(); }, make_iseq<21>{});
      tload<S_T, T_IT + 21>(); tload<S_EV, T_IT + 22>();
      asm volatile("s_sub_u32 s70, s70, %0\n\ts_add_u32 s76, %1, 1024" ::"s"(j * (8 * 4352 - 8)), "s"(oP) : "scc");
    } else {
      static_for([&](auto I) { tload<57 + I, T_IT + T_ITN + I>(); }, make_iseq<21>{});
      tload<S_T, T_IT + T_ITN + 21>(); tload<S_EV, T_IT + T_ITN + 22>();
      asm volatile("s_mov_b32 s72, 0\n\ts_mov_b32 s75, %0\n\ts_mov_b32 s76, %1" ::"s"(oP + wid * 16384), "s"(oP + 1032));
    }
    unsigned mode;
    asm volatile("s_add_u32 s98, s97, 0x8000\n\ts_mov_b32 %0, s73\n\ts_mov_b32 s56, -1\n\tv_mov_b64 v[114:115], 0\n\tv_mov_b64 v[118:119], 0\n\t"
                 "v_mov_b32 v120, 0x45800000" : "=s"(mode) :: "scc");
    KST(0, 8 + 3 * (it && !isY));
    GAP();
#define STA(k)
#define STI()
    STA(0);
    if (odd) {
      if (isY) phaseA<1, 2>(c);
      else phaseA<1, 1>(c);
      STA(1);
      STA(2);
      phaseB0<1>(mode, wid);
      STA(3);
      asm volatile("s_bfe_u32 s76, s73, 0x80008\n\ts_branch att_mid" ::: "scc");
    } else {
      if (isY) phaseA<0, 2>(c);
      else phaseA<0, 1>(c);
      STA(1);
      if (c.rare) ev_call();
      STA(2);
      phaseB0<0>(mode, wid);
      if (__builtin_expect(mode & 4, 0)) { asm volatile("s_mov_b32 s56, 31"); ev_call(); asm volatile("s_mov_b32 s56, 0"); }
      STA(3);
    }
    GAP();
    asm volatile("s_bfe_u32 s76, s73, 0x80008\natt_loop:" ::: "scc");
    GAP();
    phaseA<1, 0>(c);
    STI();
    if (__builtin_expect(c.rare != 0, 0)) { if (redo()) sm_redo<1>(); ev_call(); }
    phaseB<1>(c);
    asm volatile("s_cbranch_scc1 att_done\natt_mid:");
    phaseA<0, 0>(c);
    STI();
    if (__builtin_expect(c.rare != 0, 0)) { if (redo()) sm_redo<0>(); ev_call(); }
    phaseB<0>(c);
    asm volatile("s_branch att_loop\natt_done:");
    GAP();
    STA(4);
    if ((mode & 3) == 1) phaseA<0, 4>(c);
    else {
      asm volatile("buffer_load_dwordx4 v[128:131], v88, s[88:91], %0 offen sc1" ::"s"(oP) : "memory");
      GAP();
      phaseA<0, 3>(c);
    }
    if (__builtin_expect(redo(), 0)) sm_redo<0>();
    STA(5);
    KST(0, 9 + 3 * (it && !isY));
    const float l[2] = {row_reduce<false>(vget<R_RS>() + vget<R_TS>()), row_reduce<false>(vget<R_RS + 1>() + vget<R_TS + 1>())};
    const float cm[2] = {vget<R_M>(), vget<R_M + 1>()};
    EP p;
    if (it == 0) {
      const f32x4 ml = {cm[0], l[0], cm[1], l[1]};
      asm volatile("buffer_store_dwordx4 %0, v88, s[88:91], %1 offen sc1\n\ts_mov_b32 s101, %2" ::"v"(ml), "s"(oP), "s"(oP + wid * 16384) : "memory");
      phaseF<0>(c, p, make_iseq<74>{});
      asm volatile(FL_POLL "\n\ts_waitcnt vmcnt(17)" ::: "memory");
    } else {
      int lz = threadIdx.x & 63; asm volatile("" : "+v"(lz));
      const int rw = lz >> 2;
      static_for([&](auto E) { vsetu<R_TB + E>(rw * 128 + 16 * ((2 * E + ((lz & 3) >> 1)) ^ (rw & 6)) + 8 * (lz & 1)); }, make_iseq<4>{});
      const unsigned vs = ((lz >> 3) & 1) * 16384 + ((lz >> 4) & 1) * 512 + (lz >> 5) * 256 + (lz & 7) * 16;
      vsetu<R_VS>(vs); vsetu<R_VS + 1>(vs + 32768);
      tload<96, T_SLAB>();
      asm volatile(TICKET ::"i"((int)T_CNT) : "memory");
      asm volatile("s_mov_b32 s97, s79\n\ts_mov_b32 s100, 0x8000\n\ts_movk_i32 s99, 0x1000");
      if (!(mode & 2)) {
        p.fa[0] = __builtin_amdgcn_rcpf(l[0]); p.fa[1] = __builtin_amdgcn_rcpf(l[1]);
        phaseF<1>(c, p, make_iseq<74>{});
      } else {
        const f32x4 ml = {vget<128>(), vget<129>(), vget<130>(), vget<131>()};
        if (__builtin_expect(__builtin_amdgcn_ballot_w64(ml[0] != cm[0] || ml[2] != cm[1]) == 0, 1)) {
          p.fa[0] = __builtin_amdgcn_rcpf(l[0] + ml[1]); p.fa[1] = __builtin_amdgcn_rcpf(l[1] + ml[3]);
          const unsigned one = (lz >> 4) != ((lz >> 2) & 3) ? 0u : (lz & 1) ? 0x3f800000u : 0x3f80u, o0 = (lz & 2) ? 0u : one, o1 = (lz & 2) ? one : 0u;
          vsetu<224>(o0); vsetu<225>(o1); vsetu<230>(o0); vsetu<231>(o1);
          asm volatile("v_mov_b64 v[226:227], 0\n\tv_mov_b64 v[228:229], 0");
          phaseF<3>(c, p, make_iseq<74>{});
        } else {
#pragma unroll
          for (int e = 0; e < 2; ++e) {
            const float mm = fmaxf(cm[e], ml[2 * e]);
            const float wa = __builtin_amdgcn_exp2f(cm[e] - mm), wb = __builtin_amdgcn_exp2f(ml[2 * e] - mm);
            const float inv = __builtin_amdgcn_rcpf(l[e] * wa + ml[2 * e + 1] * wb);
            p.fa[e] = wa * inv; p.fb[e] = wb * inv;
          }
          phaseF<2>(c, p, make_iseq<74>{});
        }
      }
    }
    KST(0, 10 + 3 * (it && !isY));
  }
  asm volatile("v_xor_b32 v%c0, s100, v%c0\np_loop:" ::"i"(R_TB));
  GAP();
  p_half<0>(make_iseq<32>{});
  p_half<1>(make_iseq<32>{});
  asm volatile("s_cbranch_scc1 p_loop\n\ts_nop 5");
  GAP();
  p_cvt<1, 0>(); p_cvt<1, 1>(); p_cvt<1, 2>(); p_cvt<1, 3>();
  p_store<1, 1>();
  GAP();
  KST(0, 27);
  {
    constexpr unsigned rdy = 98304;
    int lz = threadIdx.x & 63; asm volatile("" : "+v"(lz));
    const int fr = lz & 15, fq = lz >> 4, qbF = isY != (solo && SWAP7) ? 15 - pj : pj;
    if (wid == 0) {
#pragma unroll 1
      for (int q = 0; q < 0x100000; ++q) {
        unsigned lo, hi, tl, th;
        asm volatile("v_readfirstlane_b32 %0, v122\n\tv_readfirstlane_b32 %1, v123\n\ts_mov_b32 %2, s84\n\ts_mov_b32 %3, s85" : "=s"(lo), "=s"(hi), "=s"(tl), "=s"(th));
        if (__builtin_expect((((lo ^ tl) >> 8) | (hi ^ th)) == 0, 1)) break;
        asm volatile("s_sleep 4\n\t" TICKET "\n\ts_waitcnt vmcnt(0)" ::"i"((int)T_CNT) : "memory");
      }
      asm volatile("v_mov_b32 v123, 0\n\ts_mov_b64 exec, 1\n\tds_write_b64 %0, v[122:123]\n\ts_mov_b64 exec, -1" ::"v"(rdy) : "memory");
    }
    vsetu<79>(fo(1024) + qbF * 4352 + fr * 8);
    {
      const int jj = 4 * (fr >> 3) + (fr & 3);
      const unsigned one = ((fq ^ (fr >> 2)) & 1) ? 0u : (jj & 1) ? 0x3f800000u : 0x3f80u;
      static_for([&](auto I) { vsetu<96 + I>((jj >> 1) == I ? one : 0u); }, make_iseq<4>{});
    }
    unsigned vo = (fq >> 1) * 1024 + (fq & 1) * 256 + fr * 16;
    bf16* out = arg_late<bf16, 13>();
    __amdgpu_buffer_rsrc_t so_ = __builtin_amdgcn_make_buffer_rsrc((void*)out, (short)0, SEQ * OUTW * 2, 0x00020000);
    unsigned vw = ((qbF * 128 + (fr & 7)) * OUTW + DA + (fr >> 3) * 32 + fq * 8) * 2;
    asm volatile("" : "+s"(so_));
    asm volatile("s_waitcnt vmcnt(0) lgkmcnt(0)\n\ts_barrier" : "+v"(vo), "+v"(vw) :: "memory");
    fl_set<T_FS>();
    unsigned tk;
    asm volatile("ds_read_b32 %0, %1\n\ts_waitcnt lgkmcnt(0)" : "=v"(tk) : "v"(rdy) : "memory");
    tk = __builtin_amdgcn_readfirstlane(tk) & 255;
    KST(0, 30);
    if (doA) post_math(pj, pu, out, lds + 131072 + wid * 8192);
#define N3 0
#define N2 12
    constexpr int N1 = 28 - 3 * N3 - 2 * N2;
    static_assert(N1 >= 0 && N3 + N2 + N1 <= 16);
    const int nu = tk < N3 ? 3 : tk < N3 + N2 ? 2 : tk < N3 + N2 + N1 ? 1 : 0;
    const int u0 = 4 * (tk < N3 ? 3 * tk : tk < N3 + N2 ? N3 + 2 * tk : 2 * N3 + N2 + tk) + wid * nu;
    KST(0, 28);
    if (nu) {
      unsigned so[3];
#pragma unroll
      for (int k = 0; k < 3; ++k) so[k] = O_S + qbF * 0x200000 + 0x40000 + (u0 + k) * 16384;
      unsigned pend = 0x5555u, cnt = 0x400000;
      while (true) {
        unsigned nw;
        if (wid == 3) {
          u64 m;
          const u64 ex = pend | (pend << 1);
          asm volatile(FL_ARM("%1") "\n\t" FL_POLL "\n\ts_waitcnt vmcnt(0)\n\tv_cmp_eq_u64 %0, s[84:85], v[124:125]" : "=s"(m) : "s"(ex) : "memory");
          nw = (unsigned)m & ((unsigned)m >> 1) & pend;
          if (nw) asm volatile("ds_write_b32 %0, %1 offset:4" ::"v"(rdy), "v"(~pend | nw) : "memory");
        } else {
          unsigned t;
          asm volatile("ds_read_b32 %0, %1 offset:4\n\ts_waitcnt lgkmcnt(0)" : "=v"(t) : "v"(rdy) : "memory");
          nw = __builtin_amdgcn_readfirstlane(t) & pend;
        }
        if (--cnt == 0) nw = pend;
        if (nw == 0) {
          if (wid == 3) asm volatile("s_sleep 4");
          else asm volatile("s_sleep 1");
          continue;
        }
        asm volatile("s_nop 4");
        static_for([&](auto S) {
          if ((nw >> (2 * S)) & 1) {
            r_load<0, 2 * S>(vo, so[0]); r_load<0, 2 * S + 1>(vo, so[0]);
            if (nu > 1) { r_load<1, 2 * S>(vo, so[1]); r_load<1, 2 * S + 1>(vo, so[1]); }
            if (nu > 2) { r_load<2, 2 * S>(vo, so[2]); r_load<2, 2 * S + 1>(vo, so[2]); }
          }
        }, make_iseq<8>{});
        pend &= ~nw;
        if (pend == 0) break;
      }
      asm volatile("s_waitcnt vmcnt(0)" ::: "memory");
      KST(0, 16);
      auto oo = [&](int U) { return ((U & 15) * 8 * OUTW + (U >> 4) * 64) * 2; };
      r_fin<0>(vw, so_, oo(u0));
      if (nu > 1) r_fin<1>(vw, so_, oo(u0 + 1));
      if (nu > 2) r_fin<2>(vw, so_, oo(u0 + 2));
      if (qbF == 15 && tk == 0) asm volatile(FL_SET("%0") ::"s"(fo(6144)) : "memory");
    }
    KST(0, 29);
  }
  asm volatile("s_waitcnt vmcnt(0)" ::: "memory");
  { unsigned tp_; asm volatile("v_readlane_b32 %0, v77, 41" : "=s"(tp_)); if (PSEL) t00 += (tp_ - (unsigned)c00) / 20; }
  for (int q = 0; q < 1000000; ++q) { unsigned long long t1; asm volatile("s_sleep 1\n\ts_memrealtime %0\n\ts_waitcnt lgkmcnt(0)" : "=s"(t1) :: "memory"); if (t1 - t00 >= SPINT) break; }
  asm volatile("pf_code_end:");
  KST(0, 1);
}

extern "C" void kernel_launch(void* const* d_in, const int* in_sizes, int n_in, void* d_out, int out_size, void* d_ws,
                              size_t ws_size, hipStream_t stream) {
  const bf16* Aq = (const bf16*)d_in[0];
  const bf16* Ak = (const bf16*)d_in[1];
  const bf16* Av = (const bf16*)d_in[2];
  const bf16* Ash = (const bf16*)d_in[3];
  const bf16* Assmk = (const bf16*)d_in[4];
  const bf16* AssmD = (const bf16*)d_in[5];
  const bf16* Bx = (const bf16*)d_in[6];
  const bf16* Bckv = (const bf16*)d_in[7];
  const bf16* Bwq = (const bf16*)d_in[8];
  const bf16* Bwuk = (const bf16*)d_in[9];
  const bf16* Bwuv = (const bf16*)d_in[10];
  const bf16* Bwo = (const bf16*)d_in[11];
  bf16* out = (bf16*)d_out;
  hipFuncSetAttribute((const void*)fused_kernel, hipFuncAttributeMaxDynamicSharedMemorySize, 163840);
  fused_kernel<<<256, 256, 163840, stream>>>(Bckv, Bwuk, Bwuv, Bx, Bwq, (bf16*)d_ws, Ak, Av, Ash, Assmk, AssmD, Bwo, Aq, out);
}
```

```cpp
#include <hip/hip_runtime.h>
#include <hip/hip_bf16.h>
#include <stdint.h>
#include <utility>

typedef __hip_bfloat16 bf16;
typedef __attribute__((ext_vector_type(4))) short bf16x4;
typedef __attribute__((ext_vector_type(2))) unsigned u32x2;

constexpr int SEQ = 2048;
constexpr int DA = 448;
constexpr int NH = 16;
constexpr int HD = 256;
constexpr int DC = 1024;
constexpr int OUTW = 896;
#define OUTSC 17
#define OUTSCS " sc0 sc1"
#define NTPS " nt"
#define NTVS " nt"
#define NTQS ""
#define NTW 1
#define NYT 15
#define SWAP7 1
constexpr unsigned TSB = NH * SEQ * HD * 2, O_K = TSB, O_S = 3 * TSB, O_P = 5 * TSB, PSTR = 67584, O_F = O_P + 128 * PSTR,
                   O_X = O_F + 400 * 4352, O_Z = O_X + DA * SEQ * 2, WSB = O_Z + DA * SEQ * 2;
constexpr unsigned fo(int w) { return O_F + (w >> 4) * 4352 + (w & 15) * 8; }

#define KST(kid, which)

__device__ __forceinline__ float bf2f(unsigned short u) { return __uint_as_float(((unsigned)u) << 16); }
__device__ __forceinline__ unsigned short f2bf(float f) { return __bfloat16_as_ushort(__float2bfloat16(f)); }
__device__ __forceinline__ float rbf(float f) { return bf2f(f2bf(f)); }
typedef __attribute__((ext_vector_type(2))) __bf16 bf16x2n;
typedef __attribute__((ext_vector_type(2))) float f32x2n;
__device__ __forceinline__ unsigned pk2(float a, float b) {
  return __builtin_bit_cast(unsigned, __builtin_convertvector((f32x2n){a, b}, bf16x2n));
}

__device__ __forceinline__ int off_b(int row, int ch) { return 256 * row + 16 * (ch ^ (((row & 3) << 2) | ((row >> 2) & 3))); }

template <int R> __device__ __forceinline__ void sset(unsigned x) { asm volatile("s_mov_b32 s%c0, %1" ::"i"(R), "s"(x)); }
template <int R> __device__ __forceinline__ void vsetu(unsigned x) { asm volatile("v_mov_b32 v%c0, %1" ::"i"(R), "v"(x)); }
template <int R> __device__ __forceinline__ void srd_set(const void* p, unsigned n) {
  asm volatile("s_mov_b64 s[%c0:%c1], %4\n\ts_mov_b32 s%c2, %5\n\ts_mov_b32 s%c3, 0x20000" ::"i"(R), "i"(R + 1), "i"(R + 2), "i"(R + 3), "s"(p), "s"(n));
}
template <int R, int L> __device__ __forceinline__ void tload() { asm volatile("v_readlane_b32 s%c0, v78, %1" ::"i"(R), "i"(L)); }
template <int L> __device__ __forceinline__ unsigned tget() { unsigned t; asm volatile("v_readlane_b32 %0, v78, %1" : "=s"(t) : "i"(L)); return t; }
#define FL_SET(S) "buffer_store_dwordx2 v[82:83], v81, s[88:91], " S " offen sc1"
#define FL_ARM(N) "v_cndmask_b32_e64 v80, -2.0, v79, " N
#define FL_POLL "buffer_load_dwordx2 v[124:125], v80, s[88:91], 0 offen sc1"
#define FL_CHECK(N) "s_mov_b32 s48, 0x100000\n7:\n\tv_cmp_eq_u64 vcc, s[84:85], v[124:125]\n\ts_and_b64 vcc, vcc, " N "\n\ts_cmp_eq_u64 vcc, " N \
                    "\n\ts_cbranch_scc1 8f\n\ts_sleep 2\n\t" FL_POLL "\n\ts_waitcnt vmcnt(0)\n\ts_sub_u32 s48, s48, 1\n\ts_cbranch_scc0 7b\n8:"
template <int L> __device__ __forceinline__ void fl_set() {
  asm volatile("v_readlane_b32 s48, v78, %0\n\ts_nop 4\n\t" FL_SET("s48") ::"i"(L) : "memory");
}
template <int L> __device__ __forceinline__ void fl_poll() {
  asm volatile("v_readlane_b32 s50, v78, %0\n\tv_readlane_b32 s51, v78, %1\n\ts_nop 1\n\t" FL_ARM("s[50:51]") "\n\t" FL_POLL ::"i"(L), "i"(L + 1) : "memory");
}
#define TICKET "v_readlane_b32 s48, v78, %0\n\tv_mov_b32 v122, 1\n\tv_mov_b32 v123, 0\n\ts_nop 2\n\tbuffer_atomic_add_x2 v[122:123], v81, s[88:91], s48 offen sc0"
__device__ __forceinline__ void fl_check() { asm volatile(FL_CHECK("s[50:51]") ::: "vcc", "scc", "memory"); }
template <int R> __device__ __forceinline__ float vget() { float t; asm volatile("v_mov_b32 %0, v%c1" : "=v"(t) : "i"(R)); return t; }
template <int R> __device__ __forceinline__ void vset(float x) { asm volatile("v_mov_b32 v%c0, %1" ::"i"(R), "v"(x)); }
template <int R> __device__ __forceinline__ float acc_read1() { float t; asm volatile("v_accvgpr_read_b32 %0, a[%c1]" : "=v"(t) : "i"(R)); return t; }
template <class F, int... I> __device__ __forceinline__ void static_for(F&& f, std::integer_sequence<int, I...>) {
  (f(std::integral_constant<int, I>{}), ...);
}
template <int... Is> using iseq = std::integer_sequence<int, Is...>;
template <int N> using make_iseq = std::make_integer_sequence<int, N>;

typedef __attribute__((ext_vector_type(4))) unsigned u32x4c;
typedef __attribute__((ext_vector_type(4))) float f32x4c;
typedef __attribute__((ext_vector_type(2))) float f32x2c;
typedef unsigned long long u64;
struct PR { u32x4c kk[7], vv[4]; };
__device__ __forceinline__ void prep_load(PR& p, int tid, int cg, int tb, const bf16* __restrict__ k, const bf16* __restrict__ v,
                                          const bf16* __restrict__ shift_k) {
  const __amdgpu_buffer_rsrc_t sk = __builtin_amdgcn_make_buffer_rsrc((void*)k, (short)0, SEQ * DA * 2, 0x00020000);
  const __amdgpu_buffer_rsrc_t sv = __builtin_amdgcn_make_buffer_rsrc((void*)v, (short)0, SEQ * DA * 2, 0x00020000);
  const __amdgpu_buffer_rsrc_t sw = __builtin_amdgcn_make_buffer_rsrc((void*)(shift_k + cg * 256), (short)0, 512, 0x00020000);
  const int vo = ((tb * 128 + (tid >> 3) * 4) * DA + cg * 64 + (tid & 7) * 8) * 2;
#pragma unroll
  for (int q = 0; q < 7; ++q)
    asm volatile("buffer_load_dwordx4 %0, %1, %2, 0 offen" NTPS : "=v"(p.kk[q]) : "v"(vo + (q - 3) * DA * 2), "s"(sk) : "memory");
#pragma unroll
  for (int q = 0; q < 4; ++q)
    asm volatile("buffer_load_dwordx4 %0, %1, %2, 0 offen offset:%3" NTVS : "=v"(p.vv[q]) : "v"(vo), "s"(sv), "i"(q * DA * 2) : "memory");
#pragma unroll
  for (int q = 0; q < 4; ++q)
    asm volatile("buffer_load_dwordx4 a[%c0:%c1], %2, %3, 0 offen offset:%4" ::"i"(4 * q), "i"(4 * q + 3), "v"((tid & 7) * 64), "s"(sw), "i"(q * 16) : "memory");
}
__device__ __forceinline__ float bfl(unsigned u) { return __uint_as_float(u << 16); }
__device__ __forceinline__ float bfh(unsigned u) { return __uint_as_float(u & 0xffff0000u); }
__device__ __forceinline__ f32x2c rbf2(f32x2c a) {
  return (f32x2c){__uint_as_float(pk2(0.f, a[0])), __uint_as_float(pk2(0.f, a[1]))};
}
template <int I> __device__ __forceinline__ void prep_math(const PR& p, int tid, char* lx) {
  const int rp = tid >> 3, c = tid & 7;
  f32x2c wt[4], kf[7];
  const unsigned w[4] = {__float_as_uint(acc_read1<4 * I>()), __float_as_uint(acc_read1<4 * I + 1>()), __float_as_uint(acc_read1<4 * I + 2>()),
                         __float_as_uint(acc_read1<4 * I + 3>())};
#pragma unroll
  for (int t = 0; t < 4; ++t)
    wt[t] = (t & 1) ? (f32x2c){bfh(w[t >> 1]), bfh(w[2 + (t >> 1)])} : (f32x2c){bfl(w[t >> 1]), bfl(w[2 + (t >> 1)])};
#pragma unroll
  for (int q = 0; q < 7; ++q) kf[q] = (f32x2c){bfl(p.kk[q][I]), bfh(p.kk[q][I])};
  f32x2c x[4];
#pragma unroll
  for (int e = 0; e < 4; ++e) {
    f32x2c s = rbf2(kf[e] * wt[3]);
    s = rbf2(s + rbf2(kf[e + 1] * wt[2]));
    s = rbf2(s + rbf2(kf[e + 2] * wt[1]));
    s = rbf2(s + rbf2(kf[e + 3] * wt[0]));
    x[e] = s * (f32x2c){bfl(p.vv[e][I]), bfh(p.vv[e][I])};
  }
#pragma unroll
  for (int u = 0; u < 2; ++u)
    *(u32x2*)(lx + (c * 8 + 2 * I + u) * 256 + (((31 - rp) * 8) ^ (c << 4))) = (u32x2){pk2(x[3][u], x[2][u]), pk2(x[1][u], x[0][u])};
}
__device__ __forceinline__ void prep_store(int tid, int cg, int tb, bf16* __restrict__ xr, const char* lx) {
  const __amdgpu_buffer_rsrc_t sx = __builtin_amdgcn_make_buffer_rsrc((void*)(xr + (size_t)cg * 64 * SEQ), (short)0, 64 * SEQ * 2, 0x00020000);
#pragma unroll
  for (int rd = 0; rd < 4; ++rd) {
    const int idx = tid + 256 * rd, ch = idx >> 4, q = idx & 15;
    u32x4c w = *(const u32x4c*)(lx + ch * 256 + 16 * (q ^ (ch >> 3)));
    __builtin_amdgcn_raw_buffer_store_b128(w, sx, (ch * SEQ + (15 - tb) * 128 + q * 8) * 2, 0, 16);
  }
}

#define CODE_TOUCH(LB, LE, wx, NW, NL, m0v) do { unsigned blo_, bhi_, elo_; \
    asm volatile("s_getpc_b64 s[48:49]\n\ts_add_u32 s48, s48, " LB "@rel32@lo+4\n\ts_addc_u32 s49, s49, " LB "@rel32@hi+12\n\t" \
                 "s_mov_b32 %0, s48\n\ts_mov_b32 %1, s49\n\t" \
                 "s_getpc_b64 s[48:49]\n\ts_add_u32 s48, s48, " LE "@rel32@lo+4\n\ts_mov_b32 %2, s48" \
                 : "=s"(blo_), "=s"(bhi_), "=s"(elo_) :: "scc"); \
    const unsigned n_ = elo_ - (blo_ & ~127u); \
    const __amdgpu_buffer_rsrc_t srd_ = __builtin_amdgcn_make_buffer_rsrc((void*)(((unsigned long long)bhi_ << 32) | (blo_ & ~127u)), (short)0, n_ < 262144u ? n_ : 0u, 0x00020000); \
    const unsigned l_ = threadIdx.x & 63, vo_ = l_ < (NL) ? ((wx) + l_ * (NW)) * 128u : 0x80000000u; \
    asm volatile("s_mov_b32 m0, %0\n\ts_nop 4\n\tbuffer_load_dword %1, %2, 0 offen lds" ::"s"(m0v), "v"(vo_), "s"(srd_) : "memory"); } while (0)
template <int I> __device__ __forceinline__ void d_store() {
  asm volatile("buffer_store_dwordx4 v[%c0:%c1], v%c2, s[88:91], s101 offen offset:%3 sc1" ::"i"(128 + 4 * I), "i"(131 + 4 * I), "i"(112 + ((I >> 2) & 1)),
               "i"((I & 3) * 1024) : "memory");
  if constexpr ((I & 7) == 7) asm volatile("s_addk_i32 s101, 0x4000" ::: "scc");
}
constexpr int NS_X = 19;
struct CV { unsigned pa[4], pb, px, vz, sz; float D; };
constexpr int cv_J(bool up, int q) { int J = up ? 0 : 7; while (q > J) { q -= J + 1; J += up ? 1 : -1; } return J; }
constexpr int cv_i(bool up, int q) { int J = up ? 0 : 7; while (q > J) { q -= J + 1; J += up ? 1 : -1; } return q; }
template <int R, int N> __device__ __forceinline__ void cv_rd(CV& c) {
  if constexpr (N < 16) {
    constexpr int i = N >> 1, o = 16 * R + 128 * (i & 1) + 2 * (N & 1), r = 32 + 32 * (R & 1) + 2 * N;
    asm volatile("ds_read2_b32 a[%c1:%c2], %0 offset0:%c3 offset1:%c4" ::"v"(c.pa[i >> 1]), "i"(r), "i"(r + 1), "i"(o), "i"(o + 1));
  } else {
    constexpr int m = N - 16, r = 192 + 32 * (R & 1) + 4 * m;
    asm volatile("ds_read_b128 a[%c1:%c2], %0 offset:%c3" ::"v"(c.pb), "i"(r), "i"(r + 3), "i"(64 * R + 512 * (7 - m)));
  }
}
template <int O> __device__ __forceinline__ void cv_xd(CV& c) {
  constexpr int e = O & 31, x = 96 + ((e >> 1) ^ 1);
  if constexpr (O >= 64) asm volatile("v_cvt_pk_bf16_f32 v%c0, 0, v%c0" ::"i"(160 + e));
  else if constexpr (O >= 32) asm volatile("v_mul_f32 v%c0, v%c0, %1" ::"i"(160 + e), "v"(c.D));
  else if constexpr (O & 1) asm volatile("v_lshlrev_b32 v%c0, 16, v%c1" ::"i"(160 + e), "i"(x));
  else asm volatile("v_and_b32 v%c0, 0xffff0000, v%c1" ::"i"(160 + e), "i"(x));
}
template <int O> __device__ __forceinline__ void cv_z(CV& c) {
  constexpr int J = O / 11, k = O % 11, y = 128 + 4 * J;
  if constexpr (k < 4) asm volatile("v_cvt_pk_bf16_f32 v%c0, 0, v%c0" ::"i"(y + k));
  else if constexpr (k < 8) asm volatile("v_add_f32 v%c0, v%c0, v%c1" ::"i"(y + k - 4), "i"(y + 28 + k));
  else if constexpr (k < 10) asm volatile("v_cvt_pk_bf16_f32 v%c0, v%c1, v%c2" ::"i"(y + k - 8), "i"(y + 2 * k - 16), "i"(y + 2 * k - 15));
  else asm volatile("buffer_store_dwordx2 v[%c0:%c1], %2, s[88:91], %3 offen offset:%4 sc1" ::"i"(y), "i"(y + 1), "v"(c.vz), "s"(c.sz),
                    "i"(512 * J) : "memory");
}
constexpr int cv_z0(int Q) {
  int o = 0;
  for (int q = 0; q < Q; ++q)
    for (int k = 0; k < (q % 4 == 1 ? 0 : 3); ++k) { const int J = o / 11; if ((J + 1) * (J + 2) / 2 - 1 + (J ? 4 : 7) <= q) ++o; }
  return o;
}
constexpr int cv_xd0(int T) { int o = 0; for (int t = 0; t < T; ++t) o += t % 4 != 1 || (t >= 72 && t < 108); return o < 96 ? o : 96; }
static_assert(cv_xd0(144) == 96);
template <int B, int... Is> __device__ __forceinline__ void cv_zs(CV& c, iseq<Is...>) { (cv_z<B + Is>(c), ...); }
template <int R, int Q> __device__ __forceinline__ void cv_slot(CV& c) {
  constexpr int i = cv_i(R == 7, Q), J = cv_J(R == 7, Q), d = 128 + 4 * J, a = 32 + 32 * (R & 1) + 4 * i, b = 192 + 32 * (R & 1) + 4 * (J - i);
  constexpr int T = 36 * R + Q, vm = Q % 4 == 1 && Q < 32 ? Q / 4 : -1;
  if constexpr (Q == 0) asm volatile("s_waitcnt lgkmcnt(0)");
  if constexpr (R >= 6 && vm >= 0 && vm % 4 == 0) asm volatile("s_add_u32 m0, s79, %0" ::"i"(((R - 6) ^ (NYT & 1)) * 32768 + vm * 1024));
  if constexpr (R == 0 && i == 0)
    asm volatile("v_mfma_f32_16x16x32_bf16 v[%c0:%c1], a[%c2:%c3], a[%c4:%c5], 0" ::"i"(d), "i"(d + 3), "i"(a), "i"(a + 3), "i"(b), "i"(b + 3));
  else
    asm volatile("v_mfma_f32_16x16x32_bf16 v[%c0:%c1], a[%c2:%c3], a[%c4:%c5], v[%c0:%c1]" ::"i"(d), "i"(d + 3), "i"(a), "i"(a + 3), "i"(b), "i"(b + 3));
  if constexpr (Q < 24 && R < 7) cv_rd<R + 1, Q>(c);
  if constexpr (T % 7 == 3 && NS_X + 5 + T / 7 < 32) d_store<NS_X + 5 + T / 7>();
  if constexpr (R == 2 && Q >= 24 && Q < 32)
    asm volatile("ds_read_b64 v[%c1:%c2], %0 offset:%c3" ::"v"(c.px), "i"(96 + 2 * (Q - 24)), "i"(97 + 2 * (Q - 24)), "i"(512 * (31 - Q)));
  if constexpr (R >= 3 && R < 7) if constexpr (cv_xd0(T - 107) > cv_xd0(T - 108)) cv_xd<cv_xd0(T - 108)>(c);
  if constexpr ((R == 3 || R == 4) && vm >= 0) {
    constexpr int f = (R - 3) * 8 + vm;
    asm volatile("buffer_load_dwordx4 a[%c0:%c1], v84, s[88:91], s96 offen offset:%2 sc1" ::"i"(128 + 4 * f), "i"(131 + 4 * f), "i"((f & 3) * 1024) : "memory");
    if constexpr ((f & 3) == 3) asm volatile("s_addk_i32 s96, 4096");
  }
  if constexpr (R == 4 && Q == 35) asm volatile(FL_POLL ::: "memory");
  if constexpr (R >= 6 && vm >= 0)
    asm volatile("buffer_load_dwordx4 v%c0, s[88:91], s98 offen offset:%1 sc1 lds" ::"i"(86 + vm / 4), "i"((vm & 3) * 1024) : "memory");
  if constexpr (R == 6 && Q == 35) asm volatile("s_add_u32 s98, s98, 0x8000");
  if constexpr (R == 7) cv_zs<cv_z0(Q)>(c, make_iseq<cv_z0(Q + 1) - cv_z0(Q)>{});
  __builtin_amdgcn_sched_barrier(0);
}
template <int R, int... Q> __device__ __forceinline__ void cv_step(CV& c, iseq<Q...>) { (cv_slot<R, Q>(c), ...); }
template <int... N> __device__ __forceinline__ void cv_rd0(CV& c, iseq<N...>) { (cv_rd<0, N>(c), ...); }
template <int FLAG> __device__ __forceinline__ void conv_wave(unsigned sz, float Dd, char* lw) {
  int lane = threadIdx.x & 63; asm volatile("" : "+v"(lane));
  const int l16 = lane & 15, fq = lane >> 4;
  u32x4c w[5]; unsigned w4[5];
#pragma unroll
  for (int r = 0; r < 5; ++r) {
    w[r] = *(const u32x4c*)(lw + r * 1024 + lane * 16);
    w4[r] = *(const unsigned*)(lw + r * 1024 + lane * 16 + 16);
  }
  d_store<NS_X>(); d_store<NS_X + 1>();
#pragma unroll
  for (int r = 0; r < 5; ++r)
    *(u32x4c*)(lw + 10304 + r * 1024 + lane * 16) = (u32x4c){__builtin_amdgcn_alignbit(w[r][1], w[r][0], 16), __builtin_amdgcn_alignbit(w[r][2], w[r][1], 16),
                                                             __builtin_amdgcn_alignbit(w[r][3], w[r][2], 16), __builtin_amdgcn_alignbit(w4[r], w[r][3], 16)};
  d_store<NS_X + 2>();
  CV c;
#pragma unroll
  for (int i = 0; i < 4; ++i) c.pa[i] = (unsigned)(size_t)lw + ((l16 + 1) & 1) * 10304 + 4 * ((8 * fq + l16 + 1) >> 1) + 1024 * i;
  c.pb = (unsigned)(size_t)lw + 5120 + (2032 - 16 * l16 + 8 * fq) * 2 - 512 * 7;
  c.vz = (16 * l16 + 4 * fq) * 2;
  c.px = (unsigned)(size_t)lw + 5120 + 2044 * 2 - 512 * 7 - c.vz;
  c.sz = sz; c.D = Dd;
  asm volatile("" : "+v"(c.vz), "+v"(c.px), "+v"(c.D) :: "memory");
  asm volatile("" : "+s"(c.sz));
  cv_rd0(c, make_iseq<24>{});
  d_store<NS_X + 3>(); d_store<NS_X + 4>();
  __builtin_amdgcn_sched_barrier(0);
  static_for([&](auto R) __attribute__((always_inline)) {
    cv_step<R>(c, make_iseq<36>{});
    if constexpr (R == 2) {
      asm volatile("s_waitcnt vmcnt(0)\n\ts_barrier" ::: "memory");
      fl_set<FLAG>();
    }
    if constexpr (R == 5) {
      asm volatile("s_waitcnt vmcnt(0)" ::: "memory");
      fl_check();
    }
    __builtin_amdgcn_sched_barrier(0);
  }, make_iseq<8>{});
  asm volatile("s_nop 7");
  cv_zs<cv_z0(36)>(c, make_iseq<88 - cv_z0(36)>{});
}

__device__ __forceinline__ void post_dma(int cg, int tb, const bf16* __restrict__ q, unsigned m0w) {
  int lane = threadIdx.x & 63; asm volatile("" : "+v"(lane));
  const __amdgpu_buffer_rsrc_t sq = __builtin_amdgcn_make_buffer_rsrc((void*)(q + (size_t)tb * 32 * DA + cg * 64), (short)0, 32 * DA * 2, 0x00020000);
  const unsigned vq = ((lane >> 3) * DA + (lane & 7) * 8) * 2;
  asm volatile("s_mov_b32 m0, %0\n\ts_nop 4\n\tbuffer_load_dwordx4 %1, %2, 0 offen" NTQS " lds\n\tbuffer_load_dwordx4 %1, %2, %3 offen offset:1024" NTQS " lds\n\t"
               "buffer_load_dwordx4 %1, %2, %4 offen offset:2048" NTQS " lds\n\tbuffer_load_dwordx4 %1, %2, %5 offen offset:3072" NTQS " lds"
               ::"s"(m0w + 4096), "v"(vq), "s"(sq), "s"(8 * DA * 2 - 1024), "s"(16 * DA * 2 - 2048), "s"(24 * DA * 2 - 3072) : "memory");
}
__device__ __forceinline__ void post_math(int cg, int tb, bf16* __restrict__ out, const char* lw) {
  int lane = threadIdx.x & 63; asm volatile("" : "+v"(lane));
  const int tl = lane >> 3, ck = lane & 7;
  const __amdgpu_buffer_rsrc_t so = __builtin_amdgcn_make_buffer_rsrc((void*)(out + (size_t)tb * 32 * OUTW + cg * 64), (short)0, 32 * OUTW * 2 - 2 * DA, 0x00020000);
  const unsigned vo = (tl * OUTW + ck * 8) * 2;
#pragma unroll
  for (int i = 0; i < 4; ++i) {
    const u32x4c qv = *(const u32x4c*)(lw + 4096 + i * 1024 + lane * 16);
    const char* lz = lw + ck * 64 + 16 * (i ^ (ck >> 1)) + tl * 2;
    u32x4c o;
#pragma unroll
    for (int e = 0; e < 4; ++e) {
      const float z0 = bf2f(*(const unsigned short*)(lz + 2 * e * 512));
      const float z1 = bf2f(*(const unsigned short*)(lz + (2 * e + 1) * 512));
      o[e] = pk2(bfl(qv[e]) * z0, bfh(qv[e]) * z1);
    }
    __builtin_amdgcn_raw_buffer_store_b128(o, so, vo + i * 8 * OUTW * 2, 0, OUTSC);
  }
}

#define DI __device__ __forceinline__
#define GAP() __builtin_amdgcn_sched_barrier(0)
typedef __attribute__((ext_vector_type(4))) float f32x4g;
typedef __attribute__((ext_vector_type(4))) unsigned u32x4g;
constexpr int LDW = NH * HD;
struct GM { unsigned aA[2], aB[2][8]; };
template <bool W, int J, int V, bool NT = false> DI void g_dma() {
  if constexpr (NT && J == 0) asm volatile("buffer_load_dwordx4 v%c0, s[60:63], 0 offen nt lds" ::"i"(V) : "memory");
  else if constexpr (NT) asm volatile("buffer_load_dwordx4 v%c0, s[60:63], s%c1 offen nt lds" ::"i"(V), "i"(70 + J) : "memory");
  else
  if constexpr (J == 0) asm volatile("buffer_load_dwordx4 v%c0, s[%c1:%c2], 0 offen lds" ::"i"(V), "i"(W ? 60 : 56), "i"(W ? 63 : 59) : "memory");
  else asm volatile("buffer_load_dwordx4 v%c0, s[%c1:%c2], s%c3 offen lds" ::"i"(V), "i"(W ? 60 : 56), "i"(W ? 63 : 59), "i"((W ? 70 : 63) + J) : "memory");
}
template <int P, int KS, int I> DI void g_wread(GM& g) {
  constexpr int nf = I >> 1, hf = I & 1, r = 192 - 64 * P + 8 * nf + 4 * KS + 2 * hf;
  asm volatile("ds_read_b64_tr_b16 a[%c1:%c2], %0 offset:%c3" ::"v"(g.aB[hf][nf]), "i"(r), "i"(r + 1), "i"(P * 32768 + KS * 16384));
}
template <int P, int M, int KS> DI void g_aread(GM& g) {
  constexpr int r = 96 + 8 * (M & 1) + 4 * KS;
  asm volatile("ds_read_b128 v[%c1:%c2], %0 offset:%c3" ::"v"(g.aA[KS]), "i"(r), "i"(r + 3), "i"(P * 32768 + M * 2048));
}
constexpr float CS = 0.0625f * 1.4426950408889634f;
constexpr int NS_Q = 19;
constexpr int e_vm(int G) { return (G * 3) % 8 < 3 ? G * 3 / 8 : -1; }
constexpr int e_st(int MODE, int G) {
  const int q = G & 15, k3 = ((G >> 4) - 1) * 3 + (q == 0 ? 0 : q == 4 ? 1 : 2), k2 = NS_Q + (G >> 4) * 2 + (q == 4);
  if (MODE == 5) return e_vm(G) >= 32 ? e_vm(G) - 32 : -1;
  if (MODE == 3) return G >= 33 && (G - 33) % 5 == 0 && (G - 33) / 5 < NS_X ? (G - 33) / 5 : -1;
  if (MODE == 2) return G >= 30 && (q == 0 || q == 4 || q == 14) && k3 - 2 < NS_Q ? k3 - 2 : -1;
  if (MODE == 4) return (q == 0 || q == 4) && k2 < 32 ? k2 : -1;
  return -1;
}
constexpr int e_nst(int MODE, int G0, int G1) { int n = 0; for (int G = G0; G < G1; ++G) n += e_st(MODE, G) >= 0; return n; }
static_assert(e_nst(2, 0, 128) == NS_Q && e_nst(3, 30, 128) == NS_X && e_nst(5, 0, 128) == 16 && e_nst(4, 0, 104) == 32 - NS_Q);
constexpr int e_slot(int MODE, int G) { return MODE == 3 ? (G % 3 == 0 && G < 30 ? G / 3 : -1) : e_vm(G) >= 0 && e_vm(G) < 32 ? 10 + e_vm(G) : -1; }
struct ES { int op0[129], nop[129]; };
constexpr ES e_sched(bool TR, int MODE) {
  ES z{};
  const bool SCL = MODE == 2 || MODE == 4;
  const int per = SCL ? 16 : 8, o1 = 32 * per;
  int o = MODE == 4 ? e_sched(false, 2).op0[128] : 0;
  for (int G = 0; G <= 128; ++G) {
    const int q = G & 15;
    int cap = 2 - (q == 1 || q == 2 || q == 15);
    if (SCL) cap -= (q >= 3 && q < 11 && (q & 1)) + (q == 7 || q == 11) + 2 * (G == 70 || G == 74 || (G & 63) == 61);
    cap += MODE == 2 ? 4 + (q == 10 || q == 13) : MODE == 3 ? 1 : MODE == 5 ? 2 : 0;
    if (e_st(MODE, G) >= 0 || (SCL ? q == 8 || q == 12 : e_slot(MODE, G) >= 0) || (MODE == 4 && G < 4)) cap = 0;
    if (G == 128) cap = 10000;
    z.op0[G] = o;
    while (o < o1) {
      const int i = o / per, rdy = MODE == 4 ? 0 : (TR ? 32 * (i >> 3) + 16 + 2 * (i & 7) + 1 : 4 * i + 3) + 6;
      if ((G < rdy && G < 128) || cap-- <= 0) break;
      ++o; ++z.nop[G];
    }
  }
  return z;
}
template <bool TR, int MODE> constexpr ES ESCH = e_sched(TR, MODE);
static_assert(ESCH<false, 4>.op0[96] == 512 && ESCH<false, 4>.op0[0] >= 24 * 16);
template <bool TR, bool SCL, int O> DI void e_op() {
  constexpr int per = SCL ? 16 : 8, i = O / per, k = O % per, r = 128 + 4 * i, t = 114 + 4 * (i & 1), c = k - (per - 4);
  if constexpr (k < 4) asm volatile("v_accvgpr_read_b32 v%c0, a[%c1]" ::"i"(t + k), "i"(4 * i + k));
  else if constexpr (c < 0) asm volatile("v_mul_f32 v%c0, s83, v%c0" ::"i"(k < 8 ? r + (k ^ 6) : t + k - 8));
  else {
    constexpr int d = c == 0 ? r + 3 : c == 1 ? r + 2 : r + c - 2, x = c == 0 ? r + 2 : c == 1 ? r : t + 2 * (c - 2);
    asm volatile("v_cvt_pk_bf16_f32 v%c0, v%c1, v%c2" ::"i"(d), "i"(x), "i"(x + 1));
  }
}
template <bool TR, bool SCL, int B, int... Is> DI void e_ops(iseq<Is...>) { (e_op<TR, SCL, B + Is>(), ...); }
template <int S> DI void e_m0() {
  if constexpr (S == 0 || S == 1 || S == 5 || S == 9) asm volatile("s_add_u32 m0, s78, %0" ::"i"(S * 1024));
  if constexpr (S >= 26 && (S - 26) % 4 == 0) asm volatile("s_add_u32 m0, s79, %0" ::"i"(((S - 26) >> 3) * 32768 + ((S - 26) & 4) * 1024));
}
template <int S> DI void e_load() {
  if constexpr (S == 0) asm volatile("buffer_load_dwordx4 v122, s[56:59], 0 offen lds" ::: "memory");
  else if constexpr (S < 5) asm volatile("buffer_load_dwordx4 v84, s[56:59], s64 offen offset:%0 lds" ::"i"((S - 1) * 1024) : "memory");
  else if constexpr (S < 9) asm volatile("buffer_load_dwordx4 v84, s[60:63], 0 offen offset:%0 sc1 lds" ::"i"((S - 5) * 1024) : "memory");
  else if constexpr (S == 9) asm volatile("buffer_load_dwordx4 v84, s[60:63], s62 offen sc1 lds" ::: "memory");
  else if constexpr (S < 26) {
    constexpr int f = S - 10;
    asm volatile("buffer_load_dwordx4 a[%c0:%c1], v84, s[88:91], s96 offen offset:%2 sc1" ::"i"(128 + 4 * f), "i"(131 + 4 * f), "i"((f & 3) * 1024) : "memory");
    if constexpr ((f & 3) == 3) asm volatile("s_addk_i32 s96, 4096");
  } else {
    constexpr int f = S - 26;
    asm volatile("buffer_load_dwordx4 v%c0, s[88:91], s98 offen offset:%1 sc1 lds" ::"i"(86 + ((f >> 2) & 1)), "i"((f & 3) * 1024) : "memory");
    if constexpr (f == 7) asm volatile("s_add_u32 s98, s98, s97");
    if constexpr (f == 15) asm volatile(FL_POLL ::: "memory");
  }
}
template <bool TR, int P, int H, int G, int MODE, bool NT> DI void g_gap(GM& g) {
  constexpr int m = 4 * H + (G >> 4), q = G & 15, n = q >> 1, ks = q & 1, GG = 64 * H + G;
  constexpr bool inV = TR ? m & 1 : n & 1, END = MODE == 3 || MODE == 5;
  constexpr int a = 96 + 8 * (m & 1) + 4 * ks, b = 192 - 64 * P + 8 * n + 4 * ks, d = (inV ? 128 : 0) + 4 * (TR ? 8 * (m >> 1) + n : 4 * m + (n >> 1));
  constexpr int J = END ? -1 : q == 8 ? 2 * (G >> 4) : q == 12 ? 2 * (G >> 4) + 1 : -1, gj = (J & 3) ^ ((J & 3) >> 1);
  if constexpr (END) { if constexpr (q == 0 && GG > 0) asm volatile("s_waitcnt lgkmcnt(0)"); e_m0<e_slot(MODE, GG)>(); }
  else if constexpr (q == 0 && G > 0) asm volatile("s_waitcnt lgkmcnt(4)");
  if constexpr (J == 0) asm volatile("s_add_u32 m0, s78, %0" ::"i"((P ^ H ^ 1) * 32768 + (1 - H) * 8192));
  else if constexpr (J == 4) asm volatile("s_add_u32 m0, s79, %0" ::"i"((P ^ H) * 32768 + (1 - H) * 16384));
  else if constexpr (J > 0 && (J & 3) == 1) asm volatile("s_bitset1_b32 m0, 12");
  else if constexpr (J > 0 && (J & 3) == 2) asm volatile("s_bitset1_b32 m0, %0" ::"i"(J < 4 ? 14 : 13));
  else if constexpr (J > 0) asm volatile("s_bitset0_b32 m0, 12");
#define GMF(D, X, Y, C) asm volatile("v_mfma_f32_16x16x32_bf16 " D "[%c0:%c1], " X ", " Y ", " C ::"i"(d), "i"(d + 3), "i"(a), "i"(a + 3), "i"(b), "i"(b + 3))
#define GMF2(D, C) do { if constexpr (TR) GMF(D, "v[%c2:%c3]", "a[%c4:%c5]", C); else GMF(D, "a[%c4:%c5]", "v[%c2:%c3]", C); } while (0)
  if constexpr ((MODE == 1 || MODE == 4) && ks == 0) { if constexpr (inV) GMF2("v", "0"); else GMF2("a", "0"); }
  else if constexpr (inV) GMF2("v", "v[%c0:%c1]");
  else GMF2("a", "a[%c0:%c1]");
#undef GMF2
#undef GMF
  if constexpr ((q == 1 || q == 2) && !(END && m == 7)) g_aread<(m < 7 ? P : P ^ 1), (m + 1) & 7, q - 1>(g);
  if constexpr (!END && q >= 3 && q < 11 && (q & 1)) g_wread<P ^ 1, H, (G >> 4) * 4 + (q - 3) / 2>(g);
  if constexpr (e_st(MODE, GG) >= 0) {
    static_assert(ESCH<TR, MODE>.op0[GG] >= (e_st(MODE, GG) + 1) * (END ? 8 : 16));
    d_store<e_st(MODE, GG)>();
  }
  if constexpr (J >= 0 && J < 4) g_dma<false, (1 - H) * 2 + (gj & 1) + (gj >> 1) * 4, 90>();
  else if constexpr (J >= 4) g_dma<true, (1 - H) * 4 + gj, 91, NT>();
  if constexpr (MODE >= 2) e_ops<TR, !END, ESCH<TR, MODE>.op0[GG]>(make_iseq<ESCH<TR, MODE>.nop[GG]>{});
  if constexpr (END) {
    if constexpr (e_slot(MODE, GG) >= 0) e_load<e_slot(MODE, GG)>();
  } else {
    if constexpr (H == 1 && G == 6) asm volatile("v_lshl_add_u32 v90, s82, 7, v92\n\ts_and_b32 s82, s80, 15");
    if constexpr (H == 1 && G == 10) asm volatile("s_lshr_b64 s[80:81], s[80:81], 4\n\tv_lshl_add_u32 v91, s82, 19, v93");
    constexpr int nst = e_nst(MODE, H ? 29 : 0, GG + 1) + (MODE == 4 && !H ? e_nst(2, 93, 128) : 0);
    if constexpr (G == 61) asm volatile("s_waitcnt vmcnt(%0) lgkmcnt(0)\n\ts_barrier" ::"i"(12 + nst) : "memory");
  }
  if constexpr (END && GG == 127) {
    asm volatile("s_nop 7");
    e_ops<TR, false, ESCH<TR, MODE>.op0[128]>(make_iseq<ESCH<TR, MODE>.nop[128]>{});
  }
  GAP();
}
template <bool TR, int P, int H, int MODE, bool NT, int... Gs> DI void g_half_(GM& g, iseq<Gs...>) { (g_gap<TR, P, H, Gs, MODE, NT>(g), ...); }
template <bool TR, int P, int H, int MODE, bool NT = false> DI void g_half(GM& g) { g_half_<TR, P, H, MODE, NT>(g, make_iseq<64>{}); }
template <bool TR, int P, int MODE, bool NT = false> DI void g_tile(GM& g) { g_half<TR, P, 0, MODE, NT>(g); g_half<TR, P, 1, MODE, NT>(g); }
template <int P, int... Is> DI void g_read0(GM& g, iseq<Is...>) {
  (g_wread<P, 0, Is>(g), ...); (g_wread<P, 1, Is>(g), ...);
  g_aread<P, 0, 0>(g); g_aread<P, 0, 1>(g);
}
DI void g_proW(unsigned g, unsigned mW) {
  asm volatile("v_lshl_add_u32 v91, %0, 18, v93\n\ts_mov_b32 m0, %1" ::"s"(g), "s"(mW));
  asm volatile("s_nop 0"); g_dma<true, 0, 91>();
  asm volatile("s_bitset1_b32 m0, 12\n\ts_nop 0"); g_dma<true, 1, 91>();
  asm volatile("s_bitset1_b32 m0, 13\n\ts_nop 0"); g_dma<true, 3, 91>();
  asm volatile("s_bitset0_b32 m0, 12\n\ts_nop 0"); g_dma<true, 2, 91>();
}
template <int PART = 3> DI void g_pro(unsigned g, unsigned mA, unsigned mW, unsigned lda) {
  g &= 3;
  if constexpr (PART & 1)
    asm volatile("v_add_u32 v90, %0, v92\n\ts_mov_b32 m0, %1" ::"s"((g >> 1) * 128 + (g & 1) * 128 * lda), "s"((mA ^ (g >> 1) * 32768) + (g & 1) * 8192));
  if constexpr (PART == 1) return;
  asm volatile("s_nop 0"); g_dma<false, 0, 90>();
  asm volatile("s_bitset1_b32 m0, 12\n\ts_nop 0"); g_dma<false, 1, 90>();
  asm volatile("s_bitset1_b32 m0, 14\n\ts_nop 0"); g_dma<false, 5, 90>();
  asm volatile("s_bitset0_b32 m0, 12\n\ts_nop 0"); g_dma<false, 4, 90>();
  g_proW(g, mW ^ g * 16384);
}
template <bool TR, bool SCL, class HOOK> DI void g_loop(GM& g, unsigned evm, HOOK&& hook) {
  constexpr int nkt = SCL ? 7 : 16, P0 = SCL ? 0 : 1;
  asm volatile("" : "+s"(evm));
#pragma unroll 1
  for (int t = 1;; t += 2) {
    g_half<TR, P0 ^ 1, 0, 0, (SCL ? NTW >= 1 : NTW >= 2)>(g);
    if (__builtin_expect((evm >> t) & 1, 0)) { hook(t); GAP(); }
    g_half<TR, P0 ^ 1, 1, 0, (SCL ? NTW >= 1 : NTW >= 2)>(g);
    if (SCL && t + 2 >= nkt) break;
    g_half<TR, P0, 0, 0, (SCL ? NTW >= 1 : NTW >= 2)>(g);
    if constexpr (SCL) { if (__builtin_expect(t == 3, 0)) { hook(4); GAP(); } }
    else asm volatile("s_cmp_eq_u32 %0, 13\n\ts_cselect_b32 s78, 0x28000, s78" ::"s"(t) : "scc");
    g_half<TR, P0, 1, 0, (SCL ? NTW >= 1 : NTW >= 2)>(g);
    if (!SCL && t + 3 >= nkt) break;
  }
}

typedef __attribute__((ext_vector_type(4))) unsigned u32x4;
typedef __attribute__((ext_vector_type(4))) float f32x4;

constexpr int R_S = 128, R_P = 192, R_CI = 224, R_E = 96;
constexpr int R_TS = 112, R_RS = 114, R_M = 116;
constexpr int R_PD = 118, R_THR = 120, R_TMP = 121;
constexpr int r_dk(int q) { return q < 6 ? 90 + q : 120 + q; }
constexpr int S_V = 96, S_T = 97, S_K = 98, S_I = 99, S_EV = 100;
struct AC {
  unsigned rare;
};
DI bool redo() { u64 t; asm volatile("s_mov_b64 %0, s[54:55]" : "=s"(t)); return t != 0; }
template <int X, int F> DI void kread() {
  constexpr int r = 192 + 4 * (F & 15);
  asm volatile("ds_read_b128 a[%c0:%c1], v84 offset:%c2" ::"i"(r), "i"(r + 3), "i"(X * 32768 + F * 1024));
}
template <int X, int F> DI void vread() {
  constexpr int r = 192 + 4 * (F & 15);
  asm volatile("ds_read_b128 a[%c0:%c1], v85 offset:%c2" ::"i"(r), "i"(r + 3), "i"(X * 32768 + (F & 1) * 16384 + (F >> 1) * 1024));
}
template <int X, int... Fs> DI void kreads(iseq<Fs...>) { (kread<X, Fs>(), ...); }
template <int X, int J> DI void dma_m0() {
  if constexpr (J == 0) asm volatile("s_mov_b32 m0, s%c0" ::"i"(78 + X));
  else if constexpr (J == 4) asm volatile("s_or_b32 m0, s%c0, 0x1000" ::"i"(78 + X));
  else if constexpr (J == 8) asm volatile("s_bitset0_b32 m0, 16");
  else if constexpr (J == 12) asm volatile("s_bitset0_b32 m0, 12");
}
template <int J> DI void dma_at() {
  if constexpr (J < 8)
    asm volatile("buffer_load_dwordx4 v%c0, s[88:91], s96 offen offset:%1 sc1 lds" ::"i"(86 + (J >= 4)), "i"((J & 3) * 1024) : "memory");
  else
    asm volatile("buffer_load_dwordx4 v%c0, s[92:95], s98 offen offset:%1 sc1 lds" ::"i"(r_dk(J - 8)), "i"((J & 3) * 1024) : "memory");
}
template <int Q> DI void dma4k() {
  asm volatile("s_add_u32 m0, s79, %0\n\ts_add_u32 s48, s98, %1\n\tbuffer_load_dwordx4 v%c2, s[88:91], s48 offen sc1 lds\n\t"
               "buffer_load_dwordx4 v%c2, s[88:91], s48 offen offset:1024 sc1 lds\n\tbuffer_load_dwordx4 v%c2, s[88:91], s48 offen offset:2048 sc1 lds\n\t"
               "buffer_load_dwordx4 v%c2, s[88:91], s48 offen offset:3072 sc1 lds"
               ::"i"((Q >> 1) * 32768 + (Q & 1) * 4096), "i"((Q >> 1) * 32768), "i"(86 + (Q & 1)) : "memory", "scc");
}
template <int Q> DI void acc_zero2() {
  asm volatile("v_accvgpr_write_b32 a[%c0], 0\n\tv_accvgpr_write_b32 a[%c1], 0" ::"i"(2 * Q), "i"(2 * Q + 1));
}
#define SMV 0
struct SMS { int ex[128], op0[128], nop[128], last; };
constexpr SMS sm_sched() {
  SMS z{};
  int et[32] = {}, e = 0;
  for (int t = 0; t < 128; ++t) {
    const int g = t & 15;
    const bool x = t < 48 ? (g >= 8 && (g & 1) && g < 15) : t < 64 ? (g >= 9 && g < 15) : (g >= 8 && g < 14);
    z.ex[t] = x && e < 32 ? e : -1;
    if (z.ex[t] >= 0) et[e++] = t;
  }
  int o = 0;
  for (int t = 0; t < 128; ++t) {
    const int g = t & 15;
    int cap = z.ex[t] >= 0 || t < 8 || (t < 48 && (g == 7 || (g >= 8 && !(g & 1)))) || t == 56 || t == 63 || t == 111 ? 0 : 1;
    z.op0[t] = o; z.nop[t] = 0;
    while (cap > 0 && o < 48 && et[2 * (o / 3) + (o % 3 == 0 ? 0 : 1)] + 2 <= t) {
      const int el = 2 * (o / 3) + o % 3;
      if (!(o % 3 < 2 && (el == 0 || el == 4))) --cap;
      ++o; ++z.nop[t]; z.last = t;
    }
  }
  for (int k = 16; k < 32; ++k) if (z.op0[et[k]] < 3 * ((k - 16) / 2 + 1)) z.last = 1000;
  return z;
}
constexpr SMS SM = sm_sched();
static_assert(SM.last <= 117);
constexpr int p_reg(int Y, int k) { return R_P + 16 * Y + 4 * ((k >> 3) * 2 + ((k >> 1) & 1)) + ((k >> 2) & 1) * 2 + (k & 1); }
template <int Y, int O> DI void sm_op() {
  constexpr int k = O / 3, e = 2 * k + O % 3, ts = R_TS + ((e >> 2) & 1);
  if constexpr (O % 3 == 2) asm volatile("v_cvt_pk_bf16_f32 v%c0, v%c1, v%c2" ::"i"(p_reg(Y, k)), "i"(R_E + ((2 * k) & 15)), "i"(R_E + ((2 * k + 1) & 15)));
  else if constexpr (e == 1 || e == 5) asm volatile("v_add_f32_e32 v%c0, v%c1, v%c2" ::"i"(ts), "i"(R_E + e - 1), "i"(R_E + e));
  else if constexpr (e != 0 && e != 4) asm volatile("v_add_f32_e32 v%c0, v%c0, v%c1" ::"i"(ts), "i"(R_E + (e & 15)));
}
template <int Y, int T> DI void sm_gap(AC& c) {
  if constexpr (SM.ex[T] >= 0) asm volatile("v_exp_f32 v%c0, v%c1" ::"i"(R_E + (SM.ex[T] & 15)), "i"(R_S + 32 * Y + SM.ex[T]));
  if constexpr (SM.nop[T] > 0) sm_op<Y, SM.op0[T]>();
  if constexpr (SM.nop[T] > 1) sm_op<Y, SM.op0[T] + 1>();
  if constexpr (SM.nop[T] > 2) sm_op<Y, SM.op0[T] + 2>();
  if constexpr (T == SM.last + 1) asm volatile("v_add_f32_e32 v%c0, v%c1, v%c2" ::"i"(R_TMP), "i"(R_TS), "i"(R_TS + 1));
  if constexpr (T == SM.last + 2) asm volatile("v_cmp_nle_f32_e64 s[54:55], v%c0, v%c1" ::"i"(R_TMP), "i"(R_THR));
}
#define W_A0 1
template <int X, int MODE, int G> DI void gapA(AC& c) {
  constexpr bool FIRST = MODE == 1 || MODE == 2, NOQK = MODE >= 3;
  constexpr int Xp = X ^ 1, kb = G >> 4, g = G & 15, qb = g >> 3, s = g & 7;
  if constexpr (MODE == 3 && G < 48 && G % 3 == 0) {
    constexpr int f = G / 3;
    asm volatile("buffer_load_dwordx4 a[%c0:%c1], v84, s[88:91], s75 offen offset:%2 sc1" ::"i"(128 + 4 * f), "i"(131 + 4 * f), "i"((f & 3) * 1024) : "memory");
    if constexpr ((f & 3) == 3) asm volatile("s_addk_i32 s75, 4096");
  }
  if constexpr (G == 48) asm volatile("s_waitcnt vmcnt(%0) lgkmcnt(0)\n\ts_barrier" ::"i"(MODE == 3 ? 16 : MODE == 2 ? 12 : MODE == 1 ? W_A0 : 0) : "memory");
  else if constexpr (g == 0 && !NOQK) asm volatile("s_waitcnt lgkmcnt(0)");
  if constexpr (!NOQK) {
    constexpr int r = 192 + 4 * ((kb & 1) * 8 + s), q = 128 + 4 * g;
    constexpr int d = R_S + 32 * X + 4 * (kb * 2 + qb), ci = s == 0 ? R_CI + 4 * (kb * 2 + qb) : d;
    if constexpr (FIRST && s == 0)
      asm volatile("v_mfma_f32_16x16x32_bf16 v[%c0:%c1], a[%c2:%c3], a[%c4:%c5], 0" ::"i"(d), "i"(d + 3), "i"(r), "i"(r + 3), "i"(q), "i"(q + 3));
    else
      asm volatile("v_mfma_f32_16x16x32_bf16 v[%c0:%c1], a[%c2:%c3], a[%c4:%c5], v[%c6:%c7]" ::"i"(d), "i"(d + 3), "i"(r), "i"(r + 3), "i"(q), "i"(q + 3),
                   "i"(ci), "i"(ci + 3));
    if constexpr (g < 8 && kb < 3) kread<X, (kb + 1) * 8 + g>();
    constexpr int S0 = SMV ? 40 : 14, S1 = SMV ? 41 : 30, S2 = SMV ? 42 : 45, S3 = SMV ? 43 : 46;
    if constexpr (G == S0) asm volatile("s_add_u32 s96, s97, 0x1000000");
    if constexpr (G == S1) dma_m0<X, 0>();
    if constexpr (G == S2) asm volatile("s_mov_b32 s97, s98");
    if constexpr (G == S3) asm volatile("s_add_u32 s98, s98, s99");
    if constexpr (G >= 56 && !(G & 1)) dma_at<(G - 56) / 2>();
    if constexpr (G == 57) asm volatile("s_lshr_b32 s100, s100, 1\n\ts_add_u32 s56, s56, 1");
    if constexpr (G == 59 && !FIRST) asm volatile("s_and_b32 s49, s100, 1\n\ts_or_b32 s49, s49, s54");
    if constexpr (G == 61 && !FIRST) asm volatile("s_or_b32 %0, s49, s55" : "=s"(c.rare));
    if constexpr (G == 61 && FIRST) asm volatile("s_and_b32 %0, s100, 1" : "=s"(c.rare));
  } else if constexpr (SM.nop[64 + G] > 0) asm volatile("s_nop 0");
  if constexpr (!FIRST && g < 8 && kb == 3) vread<Xp, g>();
  if constexpr (MODE == 2 && (G & 3) == 1) d_store<16 + G / 4>();
  if constexpr (FIRST) acc_zero2<G>();
  else sm_gap<Xp, 64 + G>(c);
  GAP();
}
template <int R> DI void rescale1(float alpha) {
  float t;
  asm volatile("v_accvgpr_read_b32 %0, a[%c2]\n\tv_mul_f32 %0, %0, %1\n\tv_accvgpr_write_b32 a[%c2], %0" : "=&v"(t) : "v"(alpha), "i"(R));
}
template <int... Rs> DI void rescale_all(const float (&alpha)[2], iseq<Rs...>) { (rescale1<Rs>(alpha[(Rs >> 2) & 1]), ...); }
template <bool MAX> DI float row_reduce(float v) {
  auto p = __builtin_amdgcn_permlane32_swap(__float_as_uint(v), __float_as_uint(v), false, false);
  v = MAX ? fmaxf(__uint_as_float(p[0]), __uint_as_float(p[1])) : __uint_as_float(p[0]) + __uint_as_float(p[1]);
  p = __builtin_amdgcn_permlane16_swap(__float_as_uint(v), __float_as_uint(v), false, false);
  return MAX ? fmaxf(__uint_as_float(p[0]), __uint_as_float(p[1])) : __uint_as_float(p[0]) + __uint_as_float(p[1]);
}
template <int Y, int QB> DI float row_max() {
  constexpr int r = R_S + 32 * Y + 4 * QB;
  float mx;
  asm volatile("v_max_f32 %0, v%c1, v%c2\n\tv_max3_f32 %0, %0, v%c3, v%c4" : "=&v"(mx) : "i"(r), "i"(r + 1), "i"(r + 2), "i"(r + 3));
  static_for([&](auto K) {
    constexpr int q = r + 8 * (K + 1);
    float t = mx;
    asm volatile("v_max3_f32 %0, %0, v%c1, v%c2\n\tv_max3_f32 %0, %0, v%c3, v%c4" : "+v"(t) : "i"(q), "i"(q + 1), "i"(q + 2), "i"(q + 3));
    mx = t;
  }, make_iseq<3>{});
  return row_reduce<true>(mx);
}
template <int R> DI void v_sub(float d) { asm volatile("v_sub_f32 v%c0, v%c0, %1" ::"i"(R), "v"(d)); }
template <int B> DI void s_shift(const float (&d)[2]) { static_for([&](auto E) { v_sub<B + E>(d[(E >> 2) & 1]); }, make_iseq<32>{}); }
DI void m_shift(const float (&d)[2]) {
  s_shift<R_CI>(d);
  v_sub<R_M>(-d[0]); v_sub<R_M + 1>(-d[1]);
}
DI void set_mask0(int wid) {
  const int mr = wid * 32 + __float_as_int(vget<89>());
  static_for([&](auto I) {
    constexpr int e = I, k = (e >> 3) * 16 - ((e >> 2) & 1) * 16 + (e & 3);
    const float x = vget<R_S + e>();
    vset<R_S + e>(k > mr ? -INFINITY : x);
  }, make_iseq<32>{});
}
template <int X> DI void sm_redo() {
  constexpr int Xp = X ^ 1;
  float d[2], dt[2], alpha[2], ts[2] = {0.f, 0.f};
  static_for([&](auto Q) {
    constexpr int qb = Q;
    const float pd = vget<R_PD + qb>();
    d[qb] = fmaxf(row_max<Xp, qb>() - pd, 0.f);
    dt[qb] = d[qb] + pd;
    vset<R_PD + qb>(d[qb]);
    alpha[qb] = __builtin_amdgcn_exp2f(-d[qb]);
    vset<R_RS + qb>(vget<R_RS + qb>() * alpha[qb]);
  }, make_iseq<2>{});
  vset<R_THR>(d[0] > 0.f || d[1] > 0.f ? -1.f : 4096.f);
  m_shift(d);
  static_for([&](auto K) {
    constexpr int k = K, qb = (k >> 1) & 1;
    const float e0 = __builtin_amdgcn_exp2f(vget<R_S + 32 * Xp + 2 * k>() - dt[qb]), e1 = __builtin_amdgcn_exp2f(vget<R_S + 32 * Xp + 2 * k + 1>() - dt[qb]);
    ts[qb] += e0; ts[qb] += e1;
    vset<p_reg(Xp, k)>(__uint_as_float(pk2(e0, e1)));
  }, make_iseq<16>{});
  vset<R_TS>(ts[0]); vset<R_TS + 1>(ts[1]);
  rescale_all(alpha, make_iseq<128>{});
  asm volatile("s_nop 1" ::: "memory");
  GAP();
}
template <int X, int MODE, int... Gs> DI void phaseA_(AC& c, iseq<Gs...>) { (gapA<X, MODE, Gs>(c), ...); }
template <int X, int MODE> DI void phaseA(AC& c) { phaseA_<X, MODE>(c, make_iseq<64>{}); }

template <int X, bool SMX, int G, bool WAIT = true> DI void gapB(AC& c) {
  constexpr int Xp = X ^ 1, blk = G >> 4, g = G & 15;
  if constexpr (g == 0 && WAIT) asm volatile("s_waitcnt lgkmcnt(0)");
  constexpr int qb = (g >> 1) & 1, s = g & 1, r = 192 + 4 * ((blk & 1) * 8 + (g >> 2) * 2 + s), o = 4 * ((blk * 4 + (g >> 2)) * 2 + qb);
  constexpr int p = R_P + 16 * Xp + 4 * (s * 2 + qb);
  asm volatile("v_mfma_f32_16x16x32_bf16 a[%c0:%c1], a[%c2:%c3], v[%c4:%c5], a[%c0:%c1]" ::"i"(o), "i"(o + 3), "i"(r), "i"(r + 3), "i"(p), "i"(p + 3));
  if constexpr (g < 8) {
    if constexpr (blk < 3) vread<Xp, (blk + 1) * 8 + g>();
    else if constexpr (SMX) kread<Xp, g>();
  }
  if constexpr (SMX) {
    if constexpr (G == 1 || G == 2) asm volatile("v_add_f32_e32 v%c0, v%c0, v%c1" ::"i"(R_RS + G - 1), "i"(R_TS + G - 1));
    if constexpr (g == 7 && blk < 3) dma_m0<X, blk * 4 + 4>();
    if constexpr (g >= 8 && !(g & 1) && blk < 3) dma_at<blk * 4 + 4 + (g - 8) / 2>();
    if constexpr (G == 55 && X == 1) asm volatile("s_cmp_eq_u32 s56, s76");
    if constexpr (G == 3) asm volatile("s_lshr_b32 s77, s77, 1");
    if constexpr (G == 4) asm volatile("s_bitcmp1_b32 s77, 0");
    if constexpr (G == 5) asm volatile("s_cselect_b64 s[52:53], s[62:63], 0");
    if constexpr (G == 6) asm volatile(FL_ARM("s[52:53]"));
    if constexpr (G == 56) asm volatile(FL_POLL ::: "memory");
    sm_gap<X, G>(c);
  }
  GAP();
}
template <int X, int... Gs> DI void phaseB_(AC& c, iseq<Gs...>) { (gapB<X, true, Gs>(c), ...); }
template <int X> DI void phaseB(AC& c) { phaseB_<X>(c, make_iseq<64>{}); }
constexpr int sm_nex() { int n = 0; for (int t = 0; t < 64; ++t) n += SM.ex[t] >= 0; return n; }
template <int X> DI void phaseB0(unsigned mode, int wid) {
  dma_m0<X, 4>();
  kreads<X ^ 1>(make_iseq<8>{});
  dma_at<4>();
  asm volatile("s_nop 3");
  float d[2];
  if (mode & 2) {
    d[0] = vget<122>(); d[1] = vget<123>();
    dma_at<5>();
  } else {
    if constexpr (X == 0) if (__builtin_expect(mode & 4, 0)) set_mask0(wid);
    d[0] = row_max<X, 0>(); GAP(); dma_at<5>();
    d[1] = row_max<X, 1>();
#pragma unroll
    for (int qb = 0; qb < 2; ++qb) if (!(d[qb] > -3.0e38f)) d[qb] = 0.f;
  }
  GAP();
  vset<R_M>(d[0]); vset<R_M + 1>(d[1]);
  dma_at<6>();
  const f32x2c nd[2] = {{-d[0], -d[0]}, {-d[1], -d[1]}};
  static_for([&](auto E) {
    constexpr int e = E;
    const f32x2c v = nd[(e >> 1) & 1];
    asm volatile("v_pk_add_f32 v[%c0:%c1], v[%c0:%c1], %4\n\tv_mov_b64 v[%c2:%c3], %4" ::"i"(R_S + 32 * X + 2 * e), "i"(R_S + 32 * X + 2 * e + 1), "i"(R_CI + 2 * e), "i"(R_CI + 2 * e + 1), "v"(v));
    if constexpr (e == 3) dma_at<7>();
    if constexpr (e == 6) dma_m0<X, 8>();
    if constexpr (e == 7 || e == 11 || e == 15) dma_at<8 + (e - 7) / 4>();
  }, make_iseq<16>{});
  static_assert(SM.op0[64] >= 3 * ((sm_nex() - 15) / 2) && SM.op0[64] <= 24);
  static_for([&](auto E) {
    constexpr int e = E;
    if constexpr (e < sm_nex()) asm volatile("v_exp_f32 v%c0, v%c1" ::"i"(R_E + e), "i"(R_S + 32 * X + e));
    if constexpr (e == 3) dma_at<11>();
    if constexpr (e == 6) dma_m0<X, 12>();
    if constexpr (e == 7 || e == 11 || e == 15) dma_at<12 + (e - 7) / 4>();
  }, make_iseq<16>{});
  static_for([&](auto O) { sm_op<X, O>(); }, make_iseq<SM.op0[64]>{});
  dma_at<15>();
  static_for([&](auto E) { asm volatile("v_exp_f32 v%c0, v%c1" ::"i"(R_E + (int)E), "i"(R_S + 32 * X + 16 + (int)E)); }, make_iseq<(sm_nex() > 16 ? sm_nex() - 16 : 0)>{});
  asm volatile("buffer_store_dwordx2 v[%c0:%c1], v88, s[88:91], s76 offen sc1\n\ts_bitcmp1_b32 s77, 0\n\ts_cselect_b64 s[52:53], s[62:63], 0\n\t"
               FL_ARM("s[52:53]") "\n\t" FL_POLL ::"i"(R_M), "i"(R_M + 1) : "memory", "scc");
  GAP();
}
constexpr int T_ZO = 8;
#define NINF2(R) "v_mov_b64 v[" #R ":" #R "+1], v[122:123]\n\t"
#define NINF4(R) NINF2(R) NINF2(R + 2)
#define TRI(R) "v_cndmask_b32_e64 v[" #R "], v[" #R "], v122, s[48:49]\n\tv_cndmask_b32_e64 v[" #R "+1], v[" #R "+1], v122, s[50:51]\n\t" \
               "v_cndmask_b32_e64 v[" #R "+2], v[" #R "+2], v122, s[52:53]\n\tv_cndmask_b32_e64 v[" #R "+3], v[" #R "+3], v122, s[54:55]\n\t"
DI void ev_code() {
  asm volatile(
    "s_getpc_b64 s[82:83]\n\ts_add_u32 s82, s82, 12\n\ts_addc_u32 s83, s83, 0\n\ts_branch 9f\n\t"
    "s_bitcmp0_b32 s58, s56\n\ts_cbranch_scc1 1f\n\t" FL_SET("s70") "\n\t" FL_SET("s71") "\n"
    "1:\n\ts_bitcmp0_b32 s57, s56\n\ts_cbranch_scc1 2f\n\t" FL_CHECK("s[62:63]") "\n\t"
    "s_add_u32 s98, s98, s68\n\ts_mov_b32 s68, s69\n\ts_mov_b32 s69, 0\n\ts_mov_b64 s[62:63], s[64:65]\n\ts_mov_b64 s[64:65], s[66:67]\n"
    "2:\n\ts_bitcmp0_b32 s59, s56\n\ts_cbranch_scc1 3f\n\ts_mov_b32 s98, s72\n\ts_bitcmp0_b32 s73, 0\n\ts_cbranch_scc1 3f\n\t"
    "v_lshrrev_b32 v90, 7, v86\n\tv_and_b32 v91, 6, v90\n\tv_lshlrev_b32 v91, 4, v91\n\tv_and_b32 v92, 0x70, v86\n\tv_xor_b32 v91, v91, v92\n\t"
    "s_movk_i32 s48, 896\n\tv_mad_u32_u24 v94, v90, s48, v91\n\ts_mov_b64 s[92:93], s[80:81]\n\ts_mov_b32 s94, 229376\n\ts_movk_i32 s99, 128\n\t"
    "v_add_u32 v90, 28672, v94\n\tv_add_u32 v91, 34816, v94\n\tv_add_u32 v92, 40960, v94\n\tv_add_u32 v93, 47104, v94\n\t"
    "v_add_u32 v95, 6144, v94\n\tv_add_u32 v126, 12288, v94\n\tv_add_u32 v127, 18432, v94\n"
    "3:\n\ts_bitcmp0_b32 s60, s56\n\ts_cbranch_scc1 4f\n\t"
    "v_readlane_b32 s54, v78, %0\n\ts_add_u32 m0, s78, 0x10000\n\t"
    "s_lshr_b32 s53, s73, 16\n\ts_mulk_i32 s53, 3584\n\ts_mov_b64 s[48:49], s[80:81]\n\ts_add_u32 s50, s53, 3584\n\ts_mov_b32 s51, 0x20000\n\t"
    "v_lshlrev_b32 v121, 3, v84\n\tbuffer_load_dword v121, s[48:51], s53 offen lds\n\t"
    "s_bitcmp0_b32 s73, 3\n\ts_cbranch_scc1 4f\n\t"
    "v_lshlrev_b32 v121, 9, v84\n\tv_and_b32 v121, 0x38000, v121\n\tv_lshrrev_b32 v122, 9, v84\n\tv_lshl_add_u32 v121, v122, 12, v121\n\t"
    "v_lshrrev_b32 v122, 3, v84\n\tv_xor_b32 v122, v122, v84\n\tv_and_or_b32 v121, v122, 48, v121\n\t"
    "buffer_load_dwordx4 v121, s[88:91], s54 offen sc1 lds\n\ts_addk_i32 s54, 7168\n\t"
    "buffer_load_dwordx4 v121, s[88:91], s54 offen offset:1024 sc1 lds\n\ts_addk_i32 s54, 7168\n\t"
    "buffer_load_dwordx4 v121, s[88:91], s54 offen offset:2048 sc1 lds\n\ts_addk_i32 s54, 7168\n\t"
    "buffer_load_dwordx4 v121, s[88:91], s54 offen offset:3072 sc1 lds\n"
    "4:\n\ts_bitcmp0_b32 s61, s56\n\ts_cbranch_scc1 6f\n\t"
    "s_sub_u32 s74, s74, 64\n\ts_cmp_ge_i32 s74, 0\n\ts_cbranch_scc1 6f\n\t"
    "v_cmp_gt_i32 s[48:49], 0, v89\n\tv_cmp_gt_i32 s[50:51], 1, v89\n\tv_cmp_gt_i32 s[52:53], 2, v89\n\tv_cmp_gt_i32 s[54:55], 3, v89\n\t"
    "v_mov_b32 v122, 0xff800000\n\tv_mov_b32 v123, 0xff800000\n\t" NINF4(248)
    "s_cmp_eq_i32 s74, -32\n\ts_cbranch_scc0 5f\n\t" TRI(240) TRI(252) "s_setpc_b64 s[86:87]\n"
    "5:\n\t" NINF4(240) NINF4(244) NINF4(252) NINF4(232) TRI(224) TRI(236)
    "s_cmp_eq_i32 s74, -64\n\ts_cbranch_scc1 6f\n\t" NINF4(224) NINF4(228) NINF4(236)
    "6:\n\ts_setpc_b64 s[86:87]\n9:"
    ::"i"(T_ZO) : "scc");
}
DI void ev_call() { asm volatile("s_swappc_b64 s[86:87], s[82:83]" ::: "vcc", "scc", "memory"); GAP(); }

#define ACLOB_ALL "a0","a1","a2","a3","a4","a5","a6","a7","a8","a9","a10","a11","a12","a13","a14","a15","a16","a17","a18","a19","a20","a21","a22","a23","a24","a25","a26","a27","a28","a29","a30","a31","a32","a33","a34","a35","a36","a37","a38","a39","a40","a41","a42","a43","a44","a45","a46","a47","a48","a49","a50","a51","a52","a53","a54","a55","a56","a57","a58","a59","a60","a61","a62","a63","a64","a65","a66","a67","a68","a69","a70","a71","a72","a73","a74","a75","a76","a77","a78","a79","a80","a81","a82","a83","a84","a85","a86","a87","a88","a89","a90","a91","a92","a93","a94","a95","a96","a97","a98","a99","a100","a101","a102","a103","a104","a105","a106","a107","a108","a109","a110","a111","a112","a113","a114","a115","a116","a117","a118","a119","a120","a121","a122","a123","a124","a125","a126","a127","a128","a129","a130","a131","a132","a133","a134","a135","a136","a137","a138","a139","a140","a141","a142","a143","a144","a145","a146","a147","a148","a149","a150","a151","a152","a153","a154","a155","a156","a157","a158","a159","a160","a161","a162","a163","a164","a165","a166","a167","a168","a169","a170","a171","a172","a173","a174","a175","a176","a177","a178","a179","a180","a181","a182","a183","a184","a185","a186","a187","a188","a189","a190","a191","a192","a193","a194","a195","a196","a197","a198","a199","a200","a201","a202","a203","a204","a205","a206","a207","a208","a209","a210","a211","a212","a213","a214","a215","a216","a217","a218","a219","a220","a221","a222","a223","a224","a225","a226","a227","a228","a229","a230","a231","a232","a233","a234","a235","a236","a237","a238","a239","a240","a241","a242","a243","a244","a245","a246","a247","a248","a249","a250","a251","a252","a253","a254","a255"

struct EP { float fa[2], fb[2]; };
template <int I, int E, int MODE> DI void epi_pair(const EP& p) {
  constexpr int qb = I & 1, r = 16 * (I >> 1) + 4 * qb + (E >> 1) * 8 + (E & 1) * 2, t = 96 + 2 * E, w = 128 + 4 * I + E;
  if constexpr (MODE == 2) asm volatile("v_accvgpr_read_b32 v106, a[%c0]" ::"i"(w));
  asm volatile("v_accvgpr_read_b32 v%c0, a[%c2]\n\tv_accvgpr_read_b32 v%c1, a[%c3]" ::"i"(t), "i"(t + 1), "i"(r), "i"(r + 1));
  if constexpr (MODE) asm volatile("v_mul_f32 v%c0, v%c0, %2\n\tv_mul_f32 v%c1, v%c1, %2" ::"i"(t), "i"(t + 1), "v"(p.fa[qb]));
  if constexpr (MODE == 2)
    asm volatile("v_lshlrev_b32 v104, 16, v106\n\tv_and_b32 v105, 0xffff0000, v106\n\tv_fmac_f32 v%c0, v104, %2\n\tv_fmac_f32 v%c1, v105, %2"
                 ::"i"(t), "i"(t + 1), "v"(p.fb[qb]));
  asm volatile("v_cvt_pk_bf16_f32 v%c0, v%c1, v%c2" ::"i"(w), "i"(t), "i"(t + 1));
}
template <int I> DI void epi_store() {
  asm volatile("buffer_store_dwordx4 v[%c0:%c1], v84, s[88:91], s101 offen offset:%2 sc1" ::"i"(128 + 4 * I), "i"(131 + 4 * I), "i"((I & 3) * 1024) : "memory");
  if constexpr ((I & 3) == 3) asm volatile("s_addk_i32 s101, 4096");
}
constexpr int R_TB = 107, R_VS = 111;
template <int HF, int R> DI void p_read() {
  constexpr int mb = R >> 4, r = 64 * HF + 2 * R;
  asm volatile("ds_read_b64_tr_b16 a[%c1:%c2], v%c0 offset:%c3" ::"i"(R_TB + 2 * HF + mb), "i"(r), "i"(r + 1), "i"((R & 15) * 2048));
}
template <int QB, int E> DI void p_cvt() {
  asm volatile("v_cvt_pk_bf16_f32 v%c0, v%c1, v%c2" ::"i"(208 + 4 * QB + E), "i"(192 + 8 * QB + 2 * E), "i"(193 + 8 * QB + 2 * E));
}
template <int QB, int PR> DI void p_store() {
  asm volatile("buffer_store_dwordx4 v[%c0:%c1], v%c2, s[88:91], s96 offen offset:%3 sc1" ::"i"(208 + 4 * QB), "i"(211 + 4 * QB), "i"(R_VS + QB),
               "i"(PR * 128) : "memory");
}
constexpr int p_dma(int HF, int G) { return G == 3 && !HF ? 0 : G == 11 ? 1 - HF : G == 15 ? 2 - HF : G == 19 ? 3 - HF : G == 27 && HF ? 3 : -1; }
template <int HF, int G> DI void p_gap() {
  constexpr int ch = G >> 3, t = G & 7, a = 64 * HF + 32 * (ch & 1) + 4 * t, b = 128 + 4 * (2 * t + (ch >> 1)), d = 192 + 4 * ch;
  if constexpr (t == 0)
    asm volatile("v_mfma_f32_16x16x32_bf16 v[%c0:%c1], a[%c2:%c3], v[%c4:%c5], 0" ::"i"(d), "i"(d + 3), "i"(a), "i"(a + 3), "i"(b), "i"(b + 3));
  else
    asm volatile("v_mfma_f32_16x16x32_bf16 v[%c0:%c1], a[%c2:%c3], v[%c4:%c5], v[%c0:%c1]" ::"i"(d), "i"(d + 3), "i"(a), "i"(a + 3), "i"(b), "i"(b + 3));
  p_read<HF ^ 1, G>();
  if constexpr (G == 0) asm volatile("v_xor_b32 v%c0, s100, v%c0" ::"i"(R_TB + 2 * HF + 1));
  if constexpr (G == 17) asm volatile("v_xor_b32 v%c0, s100, v%c0" ::"i"(R_TB + 2 * (HF ^ 1)));
  if constexpr (G >= 4 && G <= 8 && G != 7) p_cvt<1, (G < 7 ? G - 4 : 3)>();
  if constexpr (G == 24 || (G == 7 && HF == 0)) asm volatile("s_waitcnt lgkmcnt(8)");
  if constexpr (G == 7 && HF == 1) asm volatile("s_waitcnt vmcnt(13) lgkmcnt(8)\n\ts_barrier" ::: "memory");
  if constexpr (G == 10) p_store<1, HF ^ 1>();
  if constexpr (G >= 20 && G < 24) p_cvt<0, G - 20>();
  if constexpr (G == 25) p_store<0, HF>();
  if constexpr (p_dma(HF, G) >= 0) dma_at<(HF ? 8 : 12) + p_dma(HF, G)>();
  if constexpr (HF == 0 && G == 2) asm volatile("s_bitset0_b32 m0, 12");
  if constexpr (HF == 0 && G == 13) asm volatile("s_add_u32 s96, s96, 0x40000");
  if constexpr (HF == 0 && G == 26) asm volatile("s_add_u32 s98, s98, 128");
  if constexpr (HF == 0 && G == 27) asm volatile("s_cmp_lt_u32 s98, 0x380");
  if constexpr (HF == 0 && G == 28) asm volatile("s_cselect_b32 s99, s99, 0x31000");
  if constexpr (HF == 1 && G == 1) asm volatile("s_xor_b32 s100, s100, 0x10000");
  if constexpr (HF == 1 && G == 2) asm volatile("s_xor_b32 s97, s97, s100");
  if constexpr (HF == 1 && G == 9) asm volatile("s_or_b32 m0, s97, s99");
  if constexpr (HF == 1 && G == 30) asm volatile("s_cmp_lt_u32 s98, 0x500");
  GAP();
}
template <int HF, int... Gs> DI void p_half(iseq<Gs...>) { (p_gap<HF, Gs>(), ...); }
constexpr int f_dma(int G) { return G == 2 ? 0 : G == 5 ? 1 : G == 8 ? 2 : G >= 12 && G <= 36 && G % 6 == 0 ? 1 + G / 6 : G >= 50 && G <= 68 && G % 6 == 2 ? G / 6 : -1; }
constexpr int f_ndma(int G) { int n = 0; for (int g = 0; g < G; ++g) n += f_dma(g) >= 0; return n; }
static_assert(f_ndma(74) == 12);
template <int MODE, int G> DI void gapF(AC& c, const EP& p) {
  if constexpr (MODE && G == 48) asm volatile("s_waitcnt lgkmcnt(6)\n\ts_barrier" ::: "memory");
  if constexpr (G < 64) gapB<0, false, G, !(MODE && G == 48)>(c);
  if constexpr (MODE && G >= 42 && G < 64) p_read<0, G - 42>();
  if constexpr (MODE && G >= 64 && G < 69) { p_read<0, 2 * G - 106>(); p_read<0, 2 * G - 105>(); }
  if constexpr (MODE && f_dma(G) >= 0) dma_at<8 + (f_dma(G) & 7)>();
  if constexpr (MODE == 2 && G >= 10 && (G - 10) % 16 == 0) asm volatile("s_waitcnt vmcnt(%0)" ::"i"(13 - (G - 10) / 4 + f_ndma(G + 1)) : "memory");
  if constexpr (G >= 10) epi_pair<(G - 10) / 4, (G - 10) & 3, MODE>(p);
  if constexpr (MODE == 3 && G < 64 && (G & 1)) {
    constexpr int db = G >> 2, qb = (G >> 1) & 1, o = 4 * (db * 2 + qb), a = 224 + 4 * (db & 1), b = 128 + 4 * ((db >> 1) * 2 + qb);
    if constexpr ((G & 7) == 1) asm volatile("s_waitcnt vmcnt(%0)" ::"i"(15 - (G >> 2) + f_ndma(G + 1)) : "memory");
    asm volatile("v_mfma_f32_16x16x32_bf16 a[%c0:%c1], v[%c2:%c3], a[%c4:%c5], a[%c0:%c1]" ::"i"(o), "i"(o + 3), "i"(a), "i"(a + 3), "i"(b), "i"(b + 3));
  }
  if constexpr (MODE == 0) {
    if constexpr (G >= 14 && ((G - 14) & 3) == 0) epi_store<(G - 14) / 4>();
    if constexpr (G == 73) epi_store<15>();
  } else {
    if constexpr (G == 0) asm volatile("s_or_b32 m0, s78, 0x1000\n\ts_movk_i32 s98, 256");
    if constexpr (G == 15) asm volatile("s_bitset0_b32 m0, 12");
    if constexpr (G == 48) asm volatile("s_or_b32 m0, s79, 0x1000\n\ts_movk_i32 s98, 384");
    if constexpr (G == 73) asm volatile("s_waitcnt vmcnt(4) lgkmcnt(0)" ::: "memory");
  }
  GAP();
}
template <int MODE, int... Gs> DI void phaseF(AC& c, const EP& p, iseq<Gs...>) { (gapF<MODE, Gs>(c, p), ...); }
template <int U, int K> DI void r_load(unsigned vo, unsigned so) {
  asm volatile("buffer_load_dwordx4 a[%c0:%c1], %2, s[88:91], %3 offen offset:%4 sc1" ::"i"(64 * U + 4 * K), "i"(64 * U + 4 * K + 3), "v"(vo),
               "s"(so + (K >> 2) * 4096), "i"((K & 1) * 512 + ((K >> 1) & 1) * 2048) : "memory");
}
template <int U, int K> DI void r_sum() {
  constexpr int d = 100 + 4 * (K & 1), b = 64 * U + 4 * K;
  if constexpr (K < 2) asm volatile("v_mfma_f32_16x16x32_bf16 v[%c0:%c1], v[96:99], a[%c2:%c3], 0" ::"i"(d), "i"(d + 3), "i"(b), "i"(b + 3));
  else asm volatile("v_mfma_f32_16x16x32_bf16 v[%c0:%c1], v[96:99], a[%c2:%c3], v[%c0:%c1]" ::"i"(d), "i"(d + 3), "i"(b), "i"(b + 3));
}
template <int U> DI void r_fin(unsigned vo, __amdgpu_buffer_rsrc_t srd, unsigned so) {
  asm volatile("" : "+s"(srd), "+s"(so));
  static_for([&](auto K) { r_sum<U, K>(); }, make_iseq<16>{});
  asm volatile("s_nop 7");
  static_for([&](auto E) { asm volatile("v_cvt_pk_bf16_f32 v%c0, v%c1, v%c2" ::"i"(116 + 4 * U + E), "i"(100 + 2 * E), "i"(101 + 2 * E)); }, make_iseq<4>{});
  asm volatile("buffer_store_dwordx4 v[%c3:%c4], %0, %1, %2 offen" OUTSCS ::"v"(vo), "s"(srd), "s"(so), "i"(116 + 4 * U), "i"(119 + 4 * U) : "memory");
}

#define CMAX 12
#define PREPE 0
static_assert(NYT <= 16 && NYT >= 12);
enum { T_Q0, T_K0, T_OQ, T_OG, T_FPREP, T_FQ, T_FG, T_FS,   T_CNT = 9, T_NPRE = 10, T_NST = 12, T_TAB = 14, T_SLAB = 16, T_PART, T_IT = 18, T_ITN = 23 };
struct TRow { unsigned v[64]; };
constexpr u64 kvmask(int m0, int m1) { u64 n = 0; for (int m = m0; m <= m1; ++m) n |= 0x101ull << m; return n; }
constexpr u64 k_order(bool isY, int rr) {
  const int cls = (rr + (rr >> 2)) & 3;
  return !isY ? (cls == 0 ? 0xfedcb9875a463210ull : cls == 1 ? 0xefadc6982b574310ull : cls == 2 ? 0xfebad7693c285410ull : 0xefcba8764d392510ull)
              : (cls == 0 ? 0xfedcb987543a6210ull : cls == 1 ? 0xefadc698254b7310ull : cls == 2 ? 0xfebad769325c8410ull : 0xefcba876432d9510ull);
}
constexpr TRow t_row(int r, int wid, int j) {
  TRow t{};
  const int rr = r & 15, hsel = rr & 1, pj = rr >> 1, kv = pj & 1, h = j + 8 * hsel, u = hsel * 8 + j;
  const bool isY = r >= 16, solo = pj == 7;
  const int mt = (rr >> 2) + (isY ? 4 : 0), g = j * 128, NY = solo ? 18 : NYT;
  const int nx0 = 32 - 2 * pj - NY, cx = nx0 < CMAX ? nx0 : CMAX, ex = nx0 - cx, qbF = isY != (solo && SWAP7) ? 15 - pj : pj;
  const unsigned oQ = (unsigned)h << 20, oK = oQ + O_K, oP = O_P + (h * 8 + pj) * PSTR;
  if (wid == 4) {
    for (int l = 0; l < 64; ++l)
      t.v[l] = fo(l < 16 ? g + hsel * 16 + l : l < 24 ? g + 16 + hsel * 8 + l : l == 24 ? g + 48 + hsel * 8 + pj : l < 32 ? g + 64 + hsel * 8 + pj :
                  l < 48 ? 2304 + pj * 16 + l - 32 : 2048 + pj * 16 + l - 48);
    return t;
  }
  auto put64 = [&](int i, u64 x) { t.v[i] = (unsigned)x; t.v[i + 1] = (unsigned)(x >> 32); };
  t.v[T_Q0] = oQ + ((solo ? qbF : 15 - pj) * 128 + wid * 32) * 512;
  t.v[T_K0] = oK + (isY ? cx : 0) * 32768;
  t.v[T_OQ] = oQ + pj * 131072 + (wid >> 1) * 65536 + (wid & 1) * 4096;
  t.v[T_OG] = oK + kv * TSB + mt * 131072 + (wid >> 1) * 65536 + (wid & 1) * (kv ? 8192 : 4096);
  t.v[T_FPREP] = PREPE ? fo(2048 + pj * 16 + (isY * 2 + hsel) * 4) + (j - PREPE + 1) * 4 : fo(2048 + pj * 16 + u);
  t.v[T_FQ] = fo(g + 32 + hsel * 8 + pj);
  t.v[T_FG] = fo(g + hsel * 16 + kv * 8 + mt);
  t.v[T_FS] = fo(1024 + qbF * 16 + j * 2 + hsel);
  t.v[T_CNT] = fo(2560 + qbF * 16);
  t.v[T_ZO] = O_Z + pj * 64 * SEQ * 2 + (u * 4 + wid) * 64;
  const u64 own = 0x1ull << (kv * 8 + mt), needQ = isY ? 1ull << (16 + (qbF >> 1)) : (1ull << (16 + (pj >> 1))) | (1ull << (16 + ((15 - pj) >> 1)));
  put64(T_NPRE, isY ? needQ | (1ull << (cx >> 2)) | (1ull << ((cx + 1) >> 2)) : solo ? needQ : needQ | 0xffffull << 48);
  put64(T_NST, isY ? kvmask(cx >> 2, 3) | (solo ? 0 : 1ull << 25) : kvmask(0, solo ? 3 : 0));
  put64(T_TAB, k_order(isY, rr));
  t.v[T_SLAB] = O_S + qbF * 0x200000 + wid * 65536 + (j * 2 + hsel) * 1024;
  t.v[T_PART] = oP;
  for (int it = 0; it < 2; ++it) {
    unsigned* p = t.v + T_IT + it * T_ITN;
    const bool last = isY || it == 1;
    const int n = solo ? 2 * qbF + 2 : isY ? NY : it ? 2 * pj + 2 : nx0;
    const u64 need2 = solo ? 0 : kvmask(0, (2 * pj + 1) >> 2) | (0xffffull << 32);
    u64 q[3] = {0, 0, 0};
    unsigned jmp[2] = {0, 0};
    if (isY) {
      p[0] = (solo && SWAP7 ? 0 : 1u << (14 - cx)) | (solo ? 0 : 1u << (n - 2));
      q[0] = kvmask(4, (31 - 2 * pj) >> 2) & ~own; jmp[0] = ex * 32768; q[1] = solo ? 0 : 1ull << 24;
      p[1] = 2; p[13] = t.v[T_FG]; p[14] = fo(g + 96 + r);
    } else if (it == 0) {
      p[0] = 2u | (ex ? 1u << (cx - 2) : 0);
      q[0] = kvmask(0, cx > 0 ? (cx - 1) >> 2 : 0);
      if (ex) { q[1] = kvmask(4, 4 + ((ex - 1) >> 2)); jmp[1] = (16 - cx) * 32768; q[2] = need2; } else q[1] = need2;
      p[1] = 2; p[13] = fo(2304 + pj * 16 + u) + j * (8 * 4352 - 8); p[14] = fo(g + 64 + hsel * 8 + pj);
    } else {
      p[0] = p[1] = solo ? 0 : 1;
      q[0] = need2;
      if (solo && SWAP7) { p[0] = 1u << 14; q[0] = kvmask(4, 4); } p[13] = fo(g + 48 + hsel * 8 + pj); p[14] = fo(g + 96 + r);
    }
    p[2] = 1u << (n - 2);
    p[3] = !last ? 0 : n > 2 ? 1u << (n - 3) : 1u;
    p[4] = !last ? 0 : n > 2 ? 3u << (n - 3) : 1u << 31;
    for (int k = 0; k < 3; ++k) { p[5 + 2 * k] = (unsigned)q[k]; p[6 + 2 * k] = (unsigned)(q[k] >> 32); }
    p[11] = jmp[0]; p[12] = jmp[1];
    p[15] = oK;
    p[16] = (last ? 1 : 0) | (isY && !solo ? 2 : 0) | (n == 2 ? 4 : 0) | (isY || solo ? 0 : 8) | (n - 1) << 8 | ((r >> 1) * 4 + wid) << 16;
    p[17] = wid * 32 - (n == 2 ? 64 : 0);
    p[18] = oQ + (pj * 128 + wid * 32) * 512;
    p[19] = 0;
    p[20] = (p[0] | (last ? 0 : 1u << n)) >> 1;
    p[21] = oK + (isY ? cx : 0) * 32768;
    p[22] = ((p[0] | p[1] | p[2] | p[3] | p[4]) & 0x7fffffffu) << 1;
  }
  return t;
}
template <int I> constexpr TRow t_base = I < 160 ? t_row(I / 5, I % 5, 0) : [] {
  TRow a = t_row(0, (I - 160) * 4, 0), b = t_row(0, (I - 160) * 4, 1);
  for (int l = 0; l < 64; ++l) a.v[l] = b.v[l] - a.v[l];
  return a;
}();
template <int I> constexpr bool t_linear = [] {
  for (int j = 1; j < 8; ++j) {
    const TRow b = t_row(I / 5, I % 5, j);
    for (int l = 0; l < 64; ++l) if (b.v[l] != t_base<I>.v[l] + j * t_base<160 + (I % 5) / 4>.v[l]) return false;
  }
  return true;
}();
struct Tab { TRow row[162]; };
template <int... I> constexpr Tab t_make(iseq<I...>) {
  static_assert((t_linear<(I < 160 ? I : 0)> && ...));
  return Tab{{t_base<I>...}};
}
__device__ const Tab g_tab = t_make(make_iseq<162>{});

typedef __attribute__((ext_vector_type(16))) unsigned u32x16;
template <class T, int I> DI T* arg_late() {
  unsigned lo, hi;
  asm volatile("v_readlane_b32 %0, v77, %2\n\tv_readlane_b32 %1, v77, %3\n\ts_nop 4" : "=s"(lo), "=s"(hi) : "i"(2 * I), "i"(2 * I + 1));
  return (T*)(((u64)hi << 32) | lo);
}
__global__ __launch_bounds__(256, 1) __attribute__((amdgpu_num_vgpr(77), amdgpu_num_sgpr(56))) void fused_kernel(
    const bf16* ckv_, const bf16* wuk_, const bf16* wuv_, const bf16* x_, const bf16* wq_, bf16* ws_, const bf16* ak_, const bf16* av_,
    const bf16* ash_, const bf16* assmk_, const bf16* assmD_, const bf16* wo_, const bf16* aq_, bf16* out_) {
  extern __shared__ __attribute__((aligned(16))) char lds[];
  const void* kap = (const void*)__builtin_amdgcn_kernarg_segment_ptr();
  u32x16 ka; u64 ka8;
  asm volatile("at_code_begin:\n\ts_load_dwordx16 %0, %2, 0\n\ts_load_dwordx2 %1, %2, 0x40" : "=&s"(ka), "=&s"(ka8) : "s"(kap) : ACLOB_ALL, "v255", "s101");
  asm volatile("s_getpc_b64 s[48:49]\nct_base:\n\ts_mov_b32 s50, pf_code_end-ct_base\n\ts_mov_b32 s51, 0x20000\n\ts_lshr_b32 s52, %1, 3\n\t"
               "v_and_b32 v116, 63, %0\n\tv_lshrrev_b32 v117, 6, %0\n\tv_lshl_add_u32 v116, v116, 7, v117\n\ts_lshl_b32 s52, s52, 9\n\t"
               "v_lshlrev_b32 v116, 7, v116\n\tbuffer_load_dword v117, v116, s[48:51], s52 offen" ::"v"(threadIdx.x), "s"(blockIdx.x) : "scc", "memory");
  GAP();
  const int tid = threadIdx.x, lane = tid & 63;
  const int wid = __builtin_amdgcn_readfirstlane(tid >> 6);
  const int b = blockIdx.x, j = b & 7, r = b >> 3, rr = r & 15, hsel = rr & 1, pj = rr >> 1, kv = pj & 1;
  const bool isY = r >= 16, solo = pj == 7, doP = (PREPE ? (j & 1) == PREPE - 1 : isY) && !solo, doA = !isY && !solo;
  const int h = j + 8 * hsel, mt = (rr >> 2) + (isY ? 4 : 0), pu = (hsel * 8 + j) * 4 + wid;
  constexpr unsigned m0d = 131072 + 24576;
  const unsigned lA = isY ? 65536 : 0, lW = lA ^ 65536, po = isY ? 0 : 32768;
  unsigned mA = lA + wid * 1024 + po, mW = lW + wid * 1024 + po, ofA = isY ? pj * 256 * DA * 2 : mt * 256 * DC * 2, ofW = h * 512;
  const unsigned lda = isY ? DA : DC;
#define ROT 1
  unsigned g0 = (!ROT ? 0 : isY ? pj + 2 * hsel : rr + (rr >> 2)) & 3;
  {
    const unsigned* tp = g_tab.row[r * 5].v;
    asm volatile("global_load_dword v78, %0, %2\n\tglobal_load_dword v79, %1, %2 offset:1024\n\t"
                 "global_load_dword v118, %1, %3\n\tglobal_load_dword v119, %1, %3 offset:256\n\tglobal_load_dword v77, %4, %5"
                 ::"v"(tid * 4), "v"(lane * 4), "s"(tp), "s"(g_tab.row[160].v), "v"((lane < 27 ? lane : 27) * 4), "s"(kap) : "memory");
    const unsigned ra = wid * 8 + (lane >> 3), sa = ((lane & 7) ^ (((wid & 1) * 4 + (lane >> 4)) & 7)) * 16;
    const unsigned vbA = ra * (lda * 2) + sa, vbB = (4 * wid + (lane >> 4)) * LDW * 2 + 16 * ((lane & 15) ^ (((lane >> 4) << 2) | wid));
    vsetu<92>(vbA); vsetu<93>(vbB); vsetu<94>(ra * (DC * 2) + sa);
    static_for([&](auto J) {
      sset<64 + J>((J + 1) * 64 * lda);
      asm volatile("s_mov_b32 s%c0, %1" ::"i"(71 + J), "i"((((J + 1) >> 2) * 32 + 16 * ((J + 1) & 1)) * LDW * 2 + (((J + 1) >> 1) & 1) * 256));
    }, make_iseq<7>{});
    sset<78>(lA + wid * 1024); sset<79>(lW + wid * 1024);
    sset<58>(256 * lda * 2); sset<62>((lda - 1) * LDW * 2 + 512);
    asm volatile("s_mov_b32 s59, 0x20000\n\ts_mov_b32 s63, 0x20000\n\ts_mov_b32 s90, %0\n\ts_mov_b32 s91, 0x20000" ::"i"(WSB));
    g_pro<1>(g0, mA, mW, lda);
    asm volatile("" : "+s"(ofA), "+s"(ofW), "+s"(mA), "+s"(mW), "+s"(g0));
    GAP();
  }
  asm volatile("s_waitcnt lgkmcnt(0)" : "+s"(ka), "+s"(ka8));
  auto arg = [&](int i) { return (const bf16*)(((u64)ka[2 * i + 1] << 32) | ka[2 * i]); };
  const bf16 *ckv = arg(0), *wuk = arg(1), *wuv = arg(2), *x = arg(3), *wq = arg(4), *ak = arg(6), *av = arg(7), *ash = (const bf16*)ka8;
  bf16* Qb = (bf16*)arg(5);
  KST(0, 0);
  bf16* xqT = (bf16*)((char*)Qb + O_X);
  unsigned dv = 0;
  {
    GM g;
    PR pr;
    const int ptb = PREPE ? (isY * 2 + hsel) * 4 + (j >> 1) : hsel * 8 + j;
    if (doP) prep_load(pr, tid, pj, ptb, ak, av, ash);
    asm volatile("s_mov_b64 s[56:57], %0\n\ts_mov_b64 s[60:61], %1" ::"s"((const char*)(isY ? x : ckv) + ofA), "s"((const char*)(isY ? wq : kv ? wuv : wuk) + ofW));
    g_pro<2>(g0, mA, mW, lda);
    asm volatile("s_mov_b64 s[88:89], %0\n\tbuffer_load_dwordx2 v[120:121], off, s[88:91], %1 sc1" ::"s"(Qb), "s"(fo(6144)) : "memory");
    if (doP) {
      asm volatile("s_waitcnt vmcnt(9)" ::: "memory");
#pragma unroll
      for (int q = 0; q < 7; ++q) asm volatile("" : "+v"(pr.kk[q]));
#pragma unroll
      for (int q = 0; q < 4; ++q) asm volatile("" : "+v"(pr.vv[q]));
      static_for([&](auto Ic) __attribute__((always_inline)) {
        constexpr int I = Ic;
        if constexpr (I < 3) g_pro(g0 + 1 + I, mA, mW, lda);
        GAP();
        prep_math<I>(pr, tid, lds + 131072);
        GAP();
      }, make_iseq<4>{});
      asm volatile("s_waitcnt lgkmcnt(0)\n\ts_barrier" ::: "memory");
      prep_store(tid, pj, ptb, xqT, lds + 131072);
    }
    GAP();
    {
      const int fr = lane & 15, fq = lane >> 4, q = (lane & 15) >> 2, p = lane & 3;
#pragma unroll
      for (int k2 = 0; k2 < 2; ++k2)
        g.aA[k2] = lA + (wid >> 1) * 16384 + (fr >> 3) * 1024 + (fr & 7) * 128 + (((4 * k2 + fq) ^ (fr >> 1)) * 16);
#pragma unroll
      for (int hf = 0; hf < 2; ++hf)
#pragma unroll
        for (int nf = 0; nf < 8; ++nf)
          g.aB[hf][nf] = lW + (wid & 1) * 8192 + off_b(8 * fq + 4 * hf + q, nf * 2 + (p >> 1)) + 8 * (p & 1);
      asm volatile("" : "+v"(g.aA[0]), "+v"(g.aA[1]));
#pragma unroll
      for (int nf = 0; nf < 8; ++nf) asm volatile("" : "+v"(g.aB[0][nf]), "+v"(g.aB[1][nf]));
      vsetu<81>(tid == 0 ? 0u : 0x80000000u);
      vsetu<84>(lane * 16); vsetu<85>(lane * 16 + 65536); vsetu<86>(wid * 8192 + lane * 16); vsetu<87>(wid * 8192 + lane * 16 + 4096);
      vsetu<88>(65536 + wid * 256 + fr * 16); vsetu<89>(fr - 4 * fq);
      asm volatile("s_mov_b64 s[92:93], s[88:89]\n\ts_mov_b64 s[94:95], s[90:91]");
    }
    if (doP) asm volatile("s_waitcnt vmcnt(4)" ::: "memory");
    else {
      g_pro(g0 + 1, mA, mW, lda); g_pro(g0 + 2, mA, mW, lda); g_pro(g0 + 3, mA, mW, lda);
      asm volatile("s_waitcnt vmcnt(0)" ::: "memory");
    }
    asm volatile("s_barrier" ::: "memory");
    if (isY) g_read0<0>(g, make_iseq<16>{});
    else g_read0<1>(g, make_iseq<16>{});
    GAP();
    {
      u64 z = (((u64)__float_as_uint(vget<121>()) << 32) | __float_as_uint(vget<120>())) + 0x9e3779b97f4a7c15ull;
      z = (z ^ (z >> 30)) * 0xbf58476d1ce4e5b9ull; z = (z ^ (z >> 27)) * 0x94d049bb133111ebull; z ^= z >> 31;
      vsetu<82>((unsigned)z); vsetu<83>((unsigned)(z >> 32));
      asm volatile("v_mad_u32_u24 v78, v118, %0, v78\n\tv_mad_u32_u24 v79, v119, %0, v79\n\tv_readfirstlane_b32 s84, v82\n\tv_readfirstlane_b32 s85, v83\n\t"
                   "v_readlane_b32 s80, v78, %1\n\tv_readlane_b32 s81, v78, %2" ::"s"(j), "i"((int)T_TAB), "i"(T_TAB + 1));
      if (isY) asm volatile("s_mov_b64 s[80:81], 0x6543210");
    }
    unsigned k2;
    asm volatile("s_bfe_u32 s82, s80, 0x40008\n\ts_lshr_b64 s[80:81], s[80:81], 12\n\ts_lshl_b32 %0, s82, 1\n\tv_add_u32 v90, 128, v92" : "=s"(k2) :: "scc");
    asm volatile("s_waitcnt lgkmcnt(0)\n\ts_barrier" ::: "memory");
    g_proW(k2, mW);
    GAP();
    if (isY) {
      g_tile<false, 0, 1, NTW >= 1>(g);
      g_loop<false, true>(g, doP ? 0x22u : 0x20u, [&](int t) __attribute__((always_inline)) {
        if (t == 1) fl_set<T_FPREP>();
        else if (t == 4) { tload<80, T_TAB>(); tload<81, T_TAB + 1>(); srd_set<60>((kv ? arg_late<const bf16, 2>() : arg_late<const bf16, 1>()) + h * 256, (DC - 1) * LDW * 2 + 512); }
        else {
          srd_set<56>(arg_late<const bf16, 0>() + (size_t)mt * 256 * DC, 256 * DC * 2);
          asm volatile("v_mov_b32 v92, v94");
          static_for([&](auto J) { asm volatile("s_mov_b32 s%c0, %1" ::"i"(64 + J), "i"((J + 1) * 64 * DC)); }, make_iseq<7>{});
        }
      });
      tload<101, T_OQ>(); sset<83>(__float_as_uint(CS));
      asm volatile("v_mov_b32 v112, v84\n\tv_add_u32 v113, 8192, v84");
      GAP();
      g_tile<false, 0, 2, NTW >= 2>(g);
      KST(0, 3);
    }
#define TT 3
    auto kvtile = [&](auto TRc) __attribute__((always_inline)) {
      constexpr bool TR = TRc;
      if (isY) g_tile<TR, 1, 4, NTW >= 2>(g);
      else g_tile<TR, 1, 1, NTW >= 2>(g);
      g_loop<TR, false>(g, 0x2000u | (isY || doA ? 2u : 1u << TT), [&](int t) __attribute__((always_inline)) {
        int l = threadIdx.x & 63; asm volatile("" : "+v"(l));
        if (t == 13) {
          asm volatile("s_mov_b32 s79, 0x28000");
          fl_poll<T_NPRE>();
          if ((b & 15) == 0)
            asm volatile("v_readlane_b32 s48, v78, %0\n\tv_and_b32 v122, 0xffffff00, v82\n\tv_mov_b32 v123, v83\n\ts_nop 2\n\t"
                         "buffer_store_dwordx2 v[122:123], v81, s[88:91], s48 offen sc1" ::"i"((int)T_CNT) : "memory");
          if (!doA && !isY) {
            CODE_TOUCH("at_code_begin", "pf_code_end", hsel * 8 + wid * 2, 16u, 64, m0d);
            CODE_TOUCH("at_code_begin", "pf_code_end", hsel * 8 + wid * 2 + 1, 16u, 64, m0d);
          }
          if (doA) {
            const char* tp = arg_late<const char, 9>() + (size_t)(pj * 64 + pu) * (SEQ * 2) + (l & 31) * 128;
            asm volatile("s_mov_b32 m0, %1\n\ts_nop 0\n\tglobal_load_lds_dword %0, off" ::"v"(tp), "i"(m0d) : "memory");
          }
        } else if (isY) fl_set<T_FQ>();
        else if (doA) { if (doP) fl_set<T_FPREP>(); post_dma(pj, pu, arg_late<const bf16, 12>(), 131072 + wid * 8192); }
        else {
          const char* tp = arg_late<const char, 0>() + 1024 * DC * 2 + (pu * 256 + l) * 128;
          asm volatile("s_mov_b32 m0, %1\n\ts_nop 0\n\tglobal_load_lds_dword %0, off" ::"v"(tp), "i"(m0d) : "memory");
          asm volatile("global_load_lds_dword %0, off" ::"v"(tp + 8192) : "memory");
          asm volatile("global_load_lds_dword %0, off" ::"v"(tp + 16384) : "memory");
          asm volatile("global_load_lds_dword %0, off" ::"v"(tp + 24576) : "memory");
        }
      });
      tload<101, T_OG>(); tload<96, T_Q0>(); tload<98, T_K0>();
      fl_check();
      tload<50, T_NST>(); tload<51, T_NST + 1>();
      asm volatile("v_mov_b32 v112, v84\n\tv_add_u32 v113, %0, v84\n\tv_mov_b32 v90, v87\n\tv_mov_b32 v91, v87\n\tv_mov_b32 v92, v87\n\tv_mov_b32 v93, v87\n\t"
                   "v_mov_b32 v94, v86\n\tv_mov_b32 v95, v86\n\tv_mov_b32 v126, v86\n\tv_mov_b32 v127, v86\n\ts_mov_b32 s99, 0x8000\n\t" FL_ARM("s[50:51]")
                   ::"i"(TR ? 4096 : 8192));
      if (doA) {
        const int d = pj * 64 + pu;
        const bf16* dp = arg_late<const bf16, 10>() + d;
        asm volatile("global_load_ushort %0, %1, off" : "=v"(dv) : "v"(dp) : "memory");
        srd_set<56>(arg_late<const bf16, 9>() + (size_t)d * SEQ, SEQ * 2);
        srd_set<60>(arg_late<const char, 5>() + O_X + d * SEQ * 2, SEQ * 2);
        sset<78>(65536 + wid * 15488);
        asm volatile("s_movk_i32 s64, 992\n\tv_subrev_u32 v122, 32, v84");
        GAP();
        g_tile<TR, 0, 3>(g);
      } else {
        sset<79>(isY ? wid * 8192 : 0x30000);
        if ((NYT & 1) && !solo) asm volatile("s_add_u32 s98, s98, 0x8000\n\ts_mov_b32 s97, 0xffff8000" ::: "scc");
        else asm volatile("s_mov_b32 s97, 0x8000");
        GAP();
        g_tile<TR, 0, 5>(g);
      }
    };
    if (kv) kvtile(std::true_type{});
    else kvtile(std::false_type{});
  }
  KST(0, 4);
  asm volatile("s_waitcnt vmcnt(16)" : "+v"(dv) :: "memory");
  sset<79>(wid * 8192);
  if (doA) {
    const float Dd = bf2f(dv);
    KST(0, 5);
    conv_wave<T_FG>(O_Z + (pj * 64 + pu) * SEQ * 2, Dd, lds + 65536 + wid * 15488);
    const char* tp = arg_late<const char, 11>() + (size_t)h * (HD * DA * 2) + ((pj * 4 + wid) * 64 + lane) * 128;
    asm volatile("s_mov_b32 m0, %1\n\ts_nop 0\n\tglobal_load_lds_dword %0, off\n\ts_waitcnt vmcnt(17)" ::"v"(tp), "i"(m0d) : "memory");
    KST(0, 6);
  } else if (!isY) {
    static_for([&](auto I) { d_store<16 + I>(); }, make_iseq<16>{});
    tload<98, T_K0>();
    asm volatile("s_waitcnt vmcnt(0)\n\ts_barrier" ::: "memory");
    fl_set<T_FG>();
    KST(0, 5);
    fl_check();
    CODE_TOUCH("att_begin", "pf_code_end", hsel * 4 + wid, 8u, 64, m0d);
    dma4k<0>(); dma4k<1>(); dma4k<2>(); dma4k<3>();
    asm volatile("s_waitcnt vmcnt(0)" ::: "memory");
    KST(0, 6);
  } else {
    tload<76, T_PART>();
    fl_check();
    asm volatile("buffer_load_dwordx2 v[122:123], v88, s[88:91], s76 offen offset:1024 sc1" ::: "memory");
  }
  AC c;
  sset<78>(65536 + wid * 8192); sset<79>(98304 + wid * 8192);
  { const bf16* wp = arg_late<const bf16, 11>() + (size_t)h * HD * DA; asm volatile("s_mov_b64 s[80:81], %0" ::"s"(wp)); }
  asm volatile("att_begin:");
  ev_code();
  asm volatile("s_waitcnt lgkmcnt(0)\n\ts_barrier" ::: "memory");
#pragma unroll 1
  for (int it = doA ? 0 : 1; it < 2; ++it) {
    const bool odd = (NYT & 1) && !solo && (isY || it == 0);
    if (odd) kreads<1>(make_iseq<8>{});
    else kreads<0>(make_iseq<8>{});
    const unsigned oP = tget<T_PART>();
    if (it == 0) {
      static_for([&](auto I) { tload<57 + I, T_IT + I>(); }, make_iseq<21>{});
      tload<S_T, T_IT + 21>(); tload<S_EV, T_IT + 22>();
      asm volatile("s_sub_u32 s70, s70, %0\n\ts_add_u32 s76, %1, 1024" ::"s"(j * (8 * 4352 - 8)), "s"(oP) : "scc");
    } else {
      static_for([&](auto I) { tload<57 + I, T_IT + T_ITN + I>(); }, make_iseq<21>{});
      tload<S_T, T_IT + T_ITN + 21>(); tload<S_EV, T_IT + T_ITN + 22>();
      asm volatile("s_mov_b32 s72, 0\n\ts_mov_b32 s75, %0\n\ts_mov_b32 s76, %1" ::"s"(oP + wid * 16384), "s"(oP + 1032));
    }
    unsigned mode;
    asm volatile("s_add_u32 s98, s97, 0x8000\n\ts_mov_b32 %0, s73\n\ts_mov_b32 s56, -1\n\tv_mov_b64 v[114:115], 0\n\tv_mov_b64 v[118:119], 0\n\t"
                 "v_mov_b32 v120, 0x45800000" : "=s"(mode) :: "scc");
    KST(0, 8 + 3 * (it && !isY));
    GAP();
#define STA(k)
#define STI()
    STA(0);
    if (odd) {
      if (isY) phaseA<1, 2>(c);
      else phaseA<1, 1>(c);
      STA(1);
      STA(2);
      phaseB0<1>(mode, wid);
      STA(3);
      asm volatile("s_bfe_u32 s76, s73, 0x80008\n\ts_branch att_mid" ::: "scc");
    } else {
      if (isY) phaseA<0, 2>(c);
      else phaseA<0, 1>(c);
      STA(1);
      if (c.rare) ev_call();
      STA(2);
      phaseB0<0>(mode, wid);
      if (__builtin_expect(mode & 4, 0)) { asm volatile("s_mov_b32 s56, 31"); ev_call(); asm volatile("s_mov_b32 s56, 0"); }
      STA(3);
    }
    GAP();
    asm volatile("s_bfe_u32 s76, s73, 0x80008\natt_loop:" ::: "scc");
    GAP();
    phaseA<1, 0>(c);
    STI();
    if (__builtin_expect(c.rare != 0, 0)) { if (redo()) sm_redo<1>(); ev_call(); }
    phaseB<1>(c);
    asm volatile("s_cbranch_scc1 att_done\natt_mid:");
    phaseA<0, 0>(c);
    STI();
    if (__builtin_expect(c.rare != 0, 0)) { if (redo()) sm_redo<0>(); ev_call(); }
    phaseB<0>(c);
    asm volatile("s_branch att_loop\natt_done:");
    GAP();
    STA(4);
    if ((mode & 3) == 1) phaseA<0, 4>(c);
    else {
      asm volatile("buffer_load_dwordx4 v[128:131], v88, s[88:91], %0 offen sc1" ::"s"(oP) : "memory");
      GAP();
      phaseA<0, 3>(c);
    }
    if (__builtin_expect(redo(), 0)) sm_redo<0>();
    STA(5);
    KST(0, 9 + 3 * (it && !isY));
    const float l[2] = {row_reduce<false>(vget<R_RS>() + vget<R_TS>()), row_reduce<false>(vget<R_RS + 1>() + vget<R_TS + 1>())};
    const float cm[2] = {vget<R_M>(), vget<R_M + 1>()};
    EP p;
    if (it == 0) {
      const f32x4 ml = {cm[0], l[0], cm[1], l[1]};
      asm volatile("buffer_store_dwordx4 %0, v88, s[88:91], %1 offen sc1\n\ts_mov_b32 s101, %2" ::"v"(ml), "s"(oP), "s"(oP + wid * 16384) : "memory");
      phaseF<0>(c, p, make_iseq<74>{});
      asm volatile(FL_POLL "\n\ts_waitcnt vmcnt(17)" ::: "memory");
    } else {
      int lz = threadIdx.x & 63; asm volatile("" : "+v"(lz));
      const int rw = lz >> 2;
      static_for([&](auto E) { vsetu<R_TB + E>(rw * 128 + 16 * ((2 * E + ((lz & 3) >> 1)) ^ (rw & 6)) + 8 * (lz & 1)); }, make_iseq<4>{});
      const unsigned vs = ((lz >> 3) & 1) * 16384 + ((lz >> 4) & 1) * 512 + (lz >> 5) * 256 + (lz & 7) * 16;
      vsetu<R_VS>(vs); vsetu<R_VS + 1>(vs + 32768);
      tload<96, T_SLAB>();
      asm volatile(TICKET ::"i"((int)T_CNT) : "memory");
      asm volatile("s_mov_b32 s97, s79\n\ts_mov_b32 s100, 0x8000\n\ts_movk_i32 s99, 0x1000");
      if (!(mode & 2)) {
        p.fa[0] = __builtin_amdgcn_rcpf(l[0]); p.fa[1] = __builtin_amdgcn_rcpf(l[1]);
        phaseF<1>(c, p, make_iseq<74>{});
      } else {
        const f32x4 ml = {vget<128>(), vget<129>(), vget<130>(), vget<131>()};
        if (__builtin_expect(__builtin_amdgcn_ballot_w64(ml[0] != cm[0] || ml[2] != cm[1]) == 0, 1)) {
          p.fa[0] = __builtin_amdgcn_rcpf(l[0] + ml[1]); p.fa[1] = __builtin_amdgcn_rcpf(l[1] + ml[3]);
          const unsigned one = (lz >> 4) != ((lz >> 2) & 3) ? 0u : (lz & 1) ? 0x3f800000u : 0x3f80u, o0 = (lz & 2) ? 0u : one, o1 = (lz & 2) ? one : 0u;
          vsetu<224>(o0); vsetu<225>(o1); vsetu<230>(o0); vsetu<231>(o1);
          asm volatile("v_mov_b64 v[226:227], 0\n\tv_mov_b64 v[228:229], 0");
          phaseF<3>(c, p, make_iseq<74>{});
        } else {
#pragma unroll
          for (int e = 0; e < 2; ++e) {
            const float mm = fmaxf(cm[e], ml[2 * e]);
            const float wa = __builtin_amdgcn_exp2f(cm[e] - mm), wb = __builtin_amdgcn_exp2f(ml[2 * e] - mm);
            const float inv = __builtin_amdgcn_rcpf(l[e] * wa + ml[2 * e + 1] * wb);
            p.fa[e] = wa * inv; p.fb[e] = wb * inv;
          }
          phaseF<2>(c, p, make_iseq<74>{});
        }
      }
    }
    KST(0, 10 + 3 * (it && !isY));
  }
  asm volatile("v_xor_b32 v%c0, s100, v%c0\np_loop:" ::"i"(R_TB));
  GAP();
  p_half<0>(make_iseq<32>{});
  p_half<1>(make_iseq<32>{});
  asm volatile("s_cbranch_scc1 p_loop\n\ts_nop 5");
  GAP();
  p_cvt<1, 0>(); p_cvt<1, 1>(); p_cvt<1, 2>(); p_cvt<1, 3>();
  p_store<1, 1>();
  GAP();
  KST(0, 27);
  {
    constexpr unsigned rdy = 98304;
    int lz = threadIdx.x & 63; asm volatile("" : "+v"(lz));
    const int fr = lz & 15, fq = lz >> 4, qbF = isY != (solo && SWAP7) ? 15 - pj : pj;
    if (wid == 0) {
#pragma unroll 1
      for (int q = 0; q < 0x100000; ++q) {
        unsigned lo, hi, tl, th;
        asm volatile("v_readfirstlane_b32 %0, v122\n\tv_readfirstlane_b32 %1, v123\n\ts_mov_b32 %2, s84\n\ts_mov_b32 %3, s85" : "=s"(lo), "=s"(hi), "=s"(tl), "=s"(th));
        if (__builtin_expect((((lo ^ tl) >> 8) | (hi ^ th)) == 0, 1)) break;
        asm volatile("s_sleep 4\n\t" TICKET "\n\ts_waitcnt vmcnt(0)" ::"i"((int)T_CNT) : "memory");
      }
      asm volatile("v_mov_b32 v123, 0\n\ts_mov_b64 exec, 1\n\tds_write_b64 %0, v[122:123]\n\ts_mov_b64 exec, -1" ::"v"(rdy) : "memory");
    }
    vsetu<79>(fo(1024) + qbF * 4352 + fr * 8);
    {
      const int jj = 4 * (fr >> 3) + (fr & 3);
      const unsigned one = ((fq ^ (fr >> 2)) & 1) ? 0u : (jj & 1) ? 0x3f800000u : 0x3f80u;
      static_for([&](auto I) { vsetu<96 + I>((jj >> 1) == I ? one : 0u); }, make_iseq<4>{});
    }
    unsigned vo = (fq >> 1) * 1024 + (fq & 1) * 256 + fr * 16;
    bf16* out = arg_late<bf16, 13>();
    __amdgpu_buffer_rsrc_t so_ = __builtin_amdgcn_make_buffer_rsrc((void*)out, (short)0, SEQ * OUTW * 2, 0x00020000);
    unsigned vw = ((qbF * 128 + (fr & 7)) * OUTW + DA + (fr >> 3) * 32 + fq * 8) * 2;
    asm volatile("" : "+s"(so_));
    asm volatile("s_waitcnt vmcnt(0) lgkmcnt(0)\n\ts_barrier" : "+v"(vo), "+v"(vw) :: "memory");
    fl_set<T_FS>();
    unsigned tk;
    asm volatile("ds_read_b32 %0, %1\n\ts_waitcnt lgkmcnt(0)" : "=v"(tk) : "v"(rdy) : "memory");
    tk = __builtin_amdgcn_readfirstlane(tk) & 255;
    KST(0, 30);
    if (doA) post_math(pj, pu, out, lds + 131072 + wid * 8192);
#define N3 0
#define N2 12
    constexpr int N1 = 28 - 3 * N3 - 2 * N2;
    static_assert(N1 >= 0 && N3 + N2 + N1 <= 16);
    const int nu = tk < N3 ? 3 : tk < N3 + N2 ? 2 : tk < N3 + N2 + N1 ? 1 : 0;
    const int u0 = 4 * (tk < N3 ? 3 * tk : tk < N3 + N2 ? N3 + 2 * tk : 2 * N3 + N2 + tk) + wid * nu;
    KST(0, 28);
    if (nu) {
      unsigned so[3];
#pragma unroll
      for (int k = 0; k < 3; ++k) so[k] = O_S + qbF * 0x200000 + 0x40000 + (u0 + k) * 16384;
      unsigned pend = 0x5555u, cnt = 0x400000;
      while (true) {
        unsigned nw;
        if (wid == 3) {
          u64 m;
          const u64 ex = pend | (pend << 1);
          asm volatile(FL_ARM("%1") "\n\t" FL_POLL "\n\ts_waitcnt vmcnt(0)\n\tv_cmp_eq_u64 %0, s[84:85], v[124:125]" : "=s"(m) : "s"(ex) : "memory");
          nw = (unsigned)m & ((unsigned)m >> 1) & pend;
          if (nw) asm volatile("ds_write_b32 %0, %1 offset:4" ::"v"(rdy), "v"(~pend | nw) : "memory");
        } else {
          unsigned t;
          asm volatile("ds_read_b32 %0, %1 offset:4\n\ts_waitcnt lgkmcnt(0)" : "=v"(t) : "v"(rdy) : "memory");
          nw = __builtin_amdgcn_readfirstlane(t) & pend;
        }
        if (--cnt == 0) nw = pend;
        if (nw == 0) {
          if (wid == 3) asm volatile("s_sleep 4");
          else asm volatile("s_sleep 1");
          continue;
        }
        asm volatile("s_nop 4");
        static_for([&](auto S) {
          if ((nw >> (2 * S)) & 1) {
            r_load<0, 2 * S>(vo, so[0]); r_load<0, 2 * S + 1>(vo, so[0]);
            if (nu > 1) { r_load<1, 2 * S>(vo, so[1]); r_load<1, 2 * S + 1>(vo, so[1]); }
            if (nu > 2) { r_load<2, 2 * S>(vo, so[2]); r_load<2, 2 * S + 1>(vo, so[2]); }
          }
        }, make_iseq<8>{});
        pend &= ~nw;
        if (pend == 0) break;
      }
      asm volatile("s_waitcnt vmcnt(0)" ::: "memory");
      KST(0, 16);
      auto oo = [&](int U) { return ((U & 15) * 8 * OUTW + (U >> 4) * 64) * 2; };
      r_fin<0>(vw, so_, oo(u0));
      if (nu > 1) r_fin<1>(vw, so_, oo(u0 + 1));
      if (nu > 2) r_fin<2>(vw, so_, oo(u0 + 2));
      if (qbF == 15 && tk == 0) asm volatile(FL_SET("%0") ::"s"(fo(6144)) : "memory");
    }
    KST(0, 29);
  }
  asm volatile("s_waitcnt vmcnt(0)" ::: "memory");
  asm volatile("pf_code_end:");
  KST(0, 1);
}

extern "C" void kernel_launch(void* const* d_in, const int* in_sizes, int n_in, void* d_out, int out_size, void* d_ws,
                              size_t ws_size, hipStream_t stream) {
  const bf16* Aq = (const bf16*)d_in[0];
  const bf16* Ak = (const bf16*)d_in[1];
  const bf16* Av = (const bf16*)d_in[2];
  const bf16* Ash = (const bf16*)d_in[3];
  const bf16* Assmk = (const bf16*)d_in[4];
  const bf16* AssmD = (const bf16*)d_in[5];
  const bf16* Bx = (const bf16*)d_in[6];
  const bf16* Bckv = (const bf16*)d_in[7];
  const bf16* Bwq = (const bf16*)d_in[8];
  const bf16* Bwuk = (const bf16*)d_in[9];
  const bf16* Bwuv = (const bf16*)d_in[10];
  const bf16* Bwo = (const bf16*)d_in[11];
  bf16* out = (bf16*)d_out;
  hipFuncSetAttribute((const void*)fused_kernel, hipFuncAttributeMaxDynamicSharedMemorySize, 163840);
  fused_kernel<<<256, 256, 163840, stream>>>(Bckv, Bwuk, Bwuv, Bx, Bwq, (bf16*)d_ws, Ak, Av, Ash, Assmk, AssmD, Bwo, Aq, out);
}
```

```cpp
#include <hip/hip_runtime.h>
#include <hip/hip_bf16.h>
#include <stdint.h>
#include <utility>

typedef __hip_bfloat16 bf16;
typedef __attribute__((ext_vector_type(4))) short bf16x4;
typedef __attribute__((ext_vector_type(2))) unsigned u32x2;

constexpr int SEQ = 2048;
constexpr int DA = 448;
constexpr int NH = 16;
constexpr int HD = 256;
constexpr int DC = 1024;
constexpr int OUTW = 896;
#define OUTSC 17
#define OUTSCS " sc0 sc1"
#define NTPS " nt"
#define NTVS " nt"
#define NTQS ""
#define NTW 1
#define NYT 15
#define SWAP7 1
constexpr unsigned TSB = NH * SEQ * HD * 2, O_K = TSB, O_S = 3 * TSB, O_P = 5 * TSB, PSTR = 67584, O_F = O_P + 128 * PSTR,
                   O_X = O_F + 400 * 4352, O_Z = O_X + DA * SEQ * 2, WSB = O_Z + DA * SEQ * 2;
constexpr unsigned fo(int w) { return O_F + (w >> 4) * 4352 + (w & 15) * 8; }

#define KST(kid, which)

__device__ __forceinline__ float bf2f(unsigned short u) { return __uint_as_float(((unsigned)u) << 16); }
__device__ __forceinline__ unsigned short f2bf(float f) { return __bfloat16_as_ushort(__float2bfloat16(f)); }
__device__ __forceinline__ float rbf(float f) { return bf2f(f2bf(f)); }
typedef __attribute__((ext_vector_type(2))) __bf16 bf16x2n;
typedef __attribute__((ext_vector_type(2))) float f32x2n;
__device__ __forceinline__ unsigned pk2(float a, float b) {
  return __builtin_bit_cast(unsigned, __builtin_convertvector((f32x2n){a, b}, bf16x2n));
}

__device__ __forceinline__ int off_b(int row, int ch) { return 256 * row + 16 * (ch ^ (((row & 3) << 2) | ((row >> 2) & 3))); }

template <int R> __device__ __forceinline__ void sset(unsigned x) { asm volatile("s_mov_b32 s%c0, %1" ::"i"(R), "s"(x)); }
template <int R> __device__ __forceinline__ void vsetu(unsigned x) { asm volatile("v_mov_b32 v%c0, %1" ::"i"(R), "v"(x)); }
template <int R> __device__ __forceinline__ void srd_set(const void* p, unsigned n) {
  asm volatile("s_mov_b64 s[%c0:%c1], %4\n\ts_mov_b32 s%c2, %5\n\ts_mov_b32 s%c3, 0x20000" ::"i"(R), "i"(R + 1), "i"(R + 2), "i"(R + 3), "s"(p), "s"(n));
}
template <int R, int L> __device__ __forceinline__ void tload() { asm volatile("v_readlane_b32 s%c0, v78, %1" ::"i"(R), "i"(L)); }
template <int L> __device__ __forceinline__ unsigned tget() { unsigned t; asm volatile("v_readlane_b32 %0, v78, %1" : "=s"(t) : "i"(L)); return t; }
#define FL_SET(S) "buffer_store_dwordx2 v[82:83], v81, s[88:91], " S " offen sc1"
#define FL_ARM(N) "v_cndmask_b32_e64 v80, -2.0, v79, " N
#define FL_POLL "buffer_load_dwordx2 v[124:125], v80, s[88:91], 0 offen sc1"
#define FL_CHECK(N) "s_mov_b32 s48, 0x100000\n7:\n\tv_cmp_eq_u64 vcc, s[84:85], v[124:125]\n\ts_and_b64 vcc, vcc, " N "\n\ts_cmp_eq_u64 vcc, " N \
                    "\n\ts_cbranch_scc1 8f\n\ts_sleep 2\n\t" FL_POLL "\n\ts_waitcnt vmcnt(0)\n\ts_sub_u32 s48, s48, 1\n\ts_cbranch_scc0 7b\n8:"
template <int L> __device__ __forceinline__ void fl_set() {
  asm volatile("v_readlane_b32 s48, v78, %0\n\ts_nop 4\n\t" FL_SET("s48") ::"i"(L) : "memory");
}
template <int L> __device__ __forceinline__ void fl_poll() {
  asm volatile("v_readlane_b32 s50, v78, %0\n\tv_readlane_b32 s51, v78, %1\n\ts_nop 1\n\t" FL_ARM("s[50:51]") "\n\t" FL_POLL ::"i"(L), "i"(L + 1) : "memory");
}
#define TICKET "v_readlane_b32 s48, v78, %0\n\tv_mov_b32 v122, 1\n\tv_mov_b32 v123, 0\n\ts_nop 2\n\tbuffer_atomic_add_x2 v[122:123], v81, s[88:91], s48 offen sc0"
__device__ __forceinline__ void fl_check() { asm volatile(FL_CHECK("s[50:51]") ::: "vcc", "scc", "memory"); }
template <int R> __device__ __forceinline__ float vget() { float t; asm volatile("v_mov_b32 %0, v%c1" : "=v"(t) : "i"(R)); return t; }
template <int R> __device__ __forceinline__ void vset(float x) { asm volatile("v_mov_b32 v%c0, %1" ::"i"(R), "v"(x)); }
template <int R> __device__ __forceinline__ float acc_read1() { float t; asm volatile("v_accvgpr_read_b32 %0, a[%c1]" : "=v"(t) : "i"(R)); return t; }
template <class F, int... I> __device__ __forceinline__ void static_for(F&& f, std::integer_sequence<int, I...>) {
  (f(std::integral_constant<int, I>{}), ...);
}
template <int... Is> using iseq = std::integer_sequence<int, Is...>;
template <int N> using make_iseq = std::make_integer_sequence<int, N>;

typedef __attribute__((ext_vector_type(4))) unsigned u32x4c;
typedef __attribute__((ext_vector_type(4))) float f32x4c;
typedef __attribute__((ext_vector_type(2))) float f32x2c;
typedef unsigned long long u64;
struct PR { u32x4c kk[7], vv[4]; };
__device__ __forceinline__ void prep_load(PR& p, int tid, int cg, int tb, const bf16* __restrict__ k, const bf16* __restrict__ v,
                                          const bf16* __restrict__ shift_k) {
  const __amdgpu_buffer_rsrc_t sk = __builtin_amdgcn_make_buffer_rsrc((void*)k, (short)0, SEQ * DA * 2, 0x00020000);
  const __amdgpu_buffer_rsrc_t sv = __builtin_amdgcn_make_buffer_rsrc((void*)v, (short)0, SEQ * DA * 2, 0x00020000);
  const __amdgpu_buffer_rsrc_t sw = __builtin_amdgcn_make_buffer_rsrc((void*)(shift_k + cg * 256), (short)0, 512, 0x00020000);
  const int vo = ((tb * 128 + (tid >> 3) * 4) * DA + cg * 64 + (tid & 7) * 8) * 2;
#pragma unroll
  for (int q = 0; q < 7; ++q)
    asm volatile("buffer_load_dwordx4 %0, %1, %2, 0 offen" NTPS : "=v"(p.kk[q]) : "v"(vo + (q - 3) * DA * 2), "s"(sk) : "memory");
#pragma unroll
  for (int q = 0; q < 4; ++q)
    asm volatile("buffer_load_dwordx4 %0, %1, %2, 0 offen offset:%3" NTVS : "=v"(p.vv[q]) : "v"(vo), "s"(sv), "i"(q * DA * 2) : "memory");
#pragma unroll
  for (int q = 0; q < 4; ++q)
    asm volatile("buffer_load_dwordx4 a[%c0:%c1], %2, %3, 0 offen offset:%4" ::"i"(4 * q), "i"(4 * q + 3), "v"((tid & 7) * 64), "s"(sw), "i"(q * 16) : "memory");
}
__device__ __forceinline__ float bfl(unsigned u) { return __uint_as_float(u << 16); }
__device__ __forceinline__ float bfh(unsigned u) { return __uint_as_float(u & 0xffff0000u); }
__device__ __forceinline__ f32x2c rbf2(f32x2c a) {
  return (f32x2c){__uint_as_float(pk2(0.f, a[0])), __uint_as_float(pk2(0.f, a[1]))};
}
template <int I> __device__ __forceinline__ void prep_math(const PR& p, int tid, char* lx) {
  const int rp = tid >> 3, c = tid & 7;
  f32x2c wt[4], kf[7];
  const unsigned w[4] = {__float_as_uint(acc_read1<4 * I>()), __float_as_uint(acc_read1<4 * I + 1>()), __float_as_uint(acc_read1<4 * I + 2>()),
                         __float_as_uint(acc_read1<4 * I + 3>())};
#pragma unroll
  for (int t = 0; t < 4; ++t)
    wt[t] = (t & 1) ? (f32x2c){bfh(w[t >> 1]), bfh(w[2 + (t >> 1)])} : (f32x2c){bfl(w[t >> 1]), bfl(w[2 + (t >> 1)])};
#pragma unroll
  for (int q = 0; q < 7; ++q) kf[q] = (f32x2c){bfl(p.kk[q][I]), bfh(p.kk[q][I])};
  f32x2c x[4];
#pragma unroll
  for (int e = 0; e < 4; ++e) {
    f32x2c s = rbf2(kf[e] * wt[3]);
    s = rbf2(s + rbf2(kf[e + 1] * wt[2]));
    s = rbf2(s + rbf2(kf[e + 2] * wt[1]));
    s = rbf2(s + rbf2(kf[e + 3] * wt[0]));
    x[e] = s * (f32x2c){bfl(p.vv[e][I]), bfh(p.vv[e][I])};
  }
#pragma unroll
  for (int u = 0; u < 2; ++u)
    *(u32x2*)(lx + (c * 8 + 2 * I + u) * 256 + (((31 - rp) * 8) ^ (c << 4))) = (u32x2){pk2(x[3][u], x[2][u]), pk2(x[1][u], x[0][u])};
}
__device__ __forceinline__ void prep_store(int tid, int cg, int tb, bf16* __restrict__ xr, const char* lx) {
  const __amdgpu_buffer_rsrc_t sx = __builtin_amdgcn_make_buffer_rsrc((void*)(xr + (size_t)cg * 64 * SEQ), (short)0, 64 * SEQ * 2, 0x00020000);
#pragma unroll
  for (int rd = 0; rd < 4; ++rd) {
    const int idx = tid + 256 * rd, ch = idx >> 4, q = idx & 15;
    u32x4c w = *(const u32x4c*)(lx + ch * 256 + 16 * (q ^ (ch >> 3)));
    __builtin_amdgcn_raw_buffer_store_b128(w, sx, (ch * SEQ + (15 - tb) * 128 + q * 8) * 2, 0, 16);
  }
}

#define CODE_TOUCH(LB, LE, wx, NW, NL, m0v) do { unsigned blo_, bhi_, elo_; \
    asm volatile("s_getpc_b64 s[48:49]\n\ts_add_u32 s48, s48, " LB "@rel32@lo+4\n\ts_addc_u32 s49, s49, " LB "@rel32@hi+12\n\t" \
                 "s_mov_b32 %0, s48\n\ts_mov_b32 %1, s49\n\t" \
                 "s_getpc_b64 s[48:49]\n\ts_add_u32 s48, s48, " LE "@rel32@lo+4\n\ts_mov_b32 %2, s48" \
                 : "=s"(blo_), "=s"(bhi_), "=s"(elo_) :: "scc"); \
    const unsigned n_ = elo_ - (blo_ & ~127u); \
    const __amdgpu_buffer_rsrc_t srd_ = __builtin_amdgcn_make_buffer_rsrc((void*)(((unsigned long long)bhi_ << 32) | (blo_ & ~127u)), (short)0, n_ < 262144u ? n_ : 0u, 0x00020000); \
    const unsigned l_ = threadIdx.x & 63, vo_ = l_ < (NL) ? ((wx) + l_ * (NW)) * 128u : 0x80000000u; \
    asm volatile("s_mov_b32 m0, %0\n\ts_nop 4\n\tbuffer_load_dword %1, %2, 0 offen lds" ::"s"(m0v), "v"(vo_), "s"(srd_) : "memory"); } while (0)
template <int I> __device__ __forceinline__ void d_store() {
  asm volatile("buffer_store_dwordx4 v[%c0:%c1], v%c2, s[88:91], s101 offen offset:%3 sc1" ::"i"(128 + 4 * I), "i"(131 + 4 * I), "i"(112 + ((I >> 2) & 1)),
               "i"((I & 3) * 1024) : "memory");
  if constexpr ((I & 7) == 7) asm volatile("s_addk_i32 s101, 0x4000" ::: "scc");
}
constexpr int NS_X = 19;
struct CV { unsigned pa[4], pb, px, vz, sz; float D; };
constexpr int cv_J(bool up, int q) { int J = up ? 0 : 7; while (q > J) { q -= J + 1; J += up ? 1 : -1; } return J; }
constexpr int cv_i(bool up, int q) { int J = up ? 0 : 7; while (q > J) { q -= J + 1; J += up ? 1 : -1; } return q; }
template <int R, int N> __device__ __forceinline__ void cv_rd(CV& c) {
  if constexpr (N < 16) {
    constexpr int i = N >> 1, o = 16 * R + 128 * (i & 1) + 2 * (N & 1), r = 32 + 32 * (R & 1) + 2 * N;
    asm volatile("ds_read2_b32 a[%c1:%c2], %0 offset0:%c3 offset1:%c4" ::"v"(c.pa[i >> 1]), "i"(r), "i"(r + 1), "i"(o), "i"(o + 1));
  } else {
    constexpr int m = N - 16, r = 192 + 32 * (R & 1) + 4 * m;
    asm volatile("ds_read_b128 a[%c1:%c2], %0 offset:%c3" ::"v"(c.pb), "i"(r), "i"(r + 3), "i"(64 * R + 512 * (7 - m)));
  }
}
template <int O> __device__ __forceinline__ void cv_xd(CV& c) {
  constexpr int e = O & 31, x = 96 + ((e >> 1) ^ 1);
  if constexpr (O >= 64) asm volatile("v_cvt_pk_bf16_f32 v%c0, 0, v%c0" ::"i"(160 + e));
  else if constexpr (O >= 32) asm volatile("v_mul_f32 v%c0, v%c0, %1" ::"i"(160 + e), "v"(c.D));
  else if constexpr (O & 1) asm volatile("v_lshlrev_b32 v%c0, 16, v%c1" ::"i"(160 + e), "i"(x));
  else asm volatile("v_and_b32 v%c0, 0xffff0000, v%c1" ::"i"(160 + e), "i"(x));
}
template <int O> __device__ __forceinline__ void cv_z(CV& c) {
  constexpr int J = O / 11, k = O % 11, y = 128 + 4 * J;
  if constexpr (k < 4) asm volatile("v_cvt_pk_bf16_f32 v%c0, 0, v%c0" ::"i"(y + k));
  else if constexpr (k < 8) asm volatile("v_add_f32 v%c0, v%c0, v%c1" ::"i"(y + k - 4), "i"(y + 28 + k));
  else if constexpr (k < 10) asm volatile("v_cvt_pk_bf16_f32 v%c0, v%c1, v%c2" ::"i"(y + k - 8), "i"(y + 2 * k - 16), "i"(y + 2 * k - 15));
  else asm volatile("buffer_store_dwordx2 v[%c0:%c1], %2, s[88:91], %3 offen offset:%4 sc1" ::"i"(y), "i"(y + 1), "v"(c.vz), "s"(c.sz),
                    "i"(512 * J) : "memory");
}
constexpr int cv_z0(int Q) {
  int o = 0;
  for (int q = 0; q < Q; ++q)
    for (int k = 0; k < (q % 4 == 1 ? 0 : 3); ++k) { const int J = o / 11; if ((J + 1) * (J + 2) / 2 - 1 + (J ? 4 : 7) <= q) ++o; }
  return o;
}
constexpr int cv_xd0(int T) { int o = 0; for (int t = 0; t < T; ++t) o += t % 4 != 1 || (t >= 72 && t < 108); return o < 96 ? o : 96; }
static_assert(cv_xd0(144) == 96);
template <int B, int... Is> __device__ __forceinline__ void cv_zs(CV& c, iseq<Is...>) { (cv_z<B + Is>(c), ...); }
template <int R, int Q> __device__ __forceinline__ void cv_slot(CV& c) {
  constexpr int i = cv_i(R == 7, Q), J = cv_J(R == 7, Q), d = 128 + 4 * J, a = 32 + 32 * (R & 1) + 4 * i, b = 192 + 32 * (R & 1) + 4 * (J - i);
  constexpr int T = 36 * R + Q, vm = Q % 4 == 1 && Q < 32 ? Q / 4 : -1;
  if constexpr (Q == 0) asm volatile("s_waitcnt lgkmcnt(0)");
  if constexpr (R >= 6 && vm >= 0 && vm % 4 == 0) asm volatile("s_add_u32 m0, s79, %0" ::"i"(((R - 6) ^ (NYT & 1)) * 32768 + vm * 1024));
  if constexpr (R == 0 && i == 0)
    asm volatile("v_mfma_f32_16x16x32_bf16 v[%c0:%c1], a[%c2:%c3], a[%c4:%c5], 0" ::"i"(d), "i"(d + 3), "i"(a), "i"(a + 3), "i"(b), "i"(b + 3));
  else
    asm volatile("v_mfma_f32_16x16x32_bf16 v[%c0:%c1], a[%c2:%c3], a[%c4:%c5], v[%c0:%c1]" ::"i"(d), "i"(d + 3), "i"(a), "i"(a + 3), "i"(b), "i"(b + 3));
  if constexpr (Q < 24 && R < 7) cv_rd<R + 1, Q>(c);
  if constexpr (T % 7 == 3 && NS_X + 5 + T / 7 < 32) d_store<NS_X + 5 + T / 7>();
  if constexpr (R == 2 && Q >= 24 && Q < 32)
    asm volatile("ds_read_b64 v[%c1:%c2], %0 offset:%c3" ::"v"(c.px), "i"(96 + 2 * (Q - 24)), "i"(97 + 2 * (Q - 24)), "i"(512 * (31 - Q)));
  if constexpr (R >= 3 && R < 7) if constexpr (cv_xd0(T - 107) > cv_xd0(T - 108)) cv_xd<cv_xd0(T - 108)>(c);
  if constexpr ((R == 3 || R == 4) && vm >= 0) {
    constexpr int f = (R - 3) * 8 + vm;
    asm volatile("buffer_load_dwordx4 a[%c0:%c1], v84, s[88:91], s96 offen offset:%2 sc1" ::"i"(128 + 4 * f), "i"(131 + 4 * f), "i"((f & 3) * 1024) : "memory");
    if constexpr ((f & 3) == 3) asm volatile("s_addk_i32 s96, 4096");
  }
  if constexpr (R == 4 && Q == 35) asm volatile(FL_POLL ::: "memory");
  if constexpr (R >= 6 && vm >= 0)
    asm volatile("buffer_load_dwordx4 v%c0, s[88:91], s98 offen offset:%1 sc1 lds" ::"i"(86 + vm / 4), "i"((vm & 3) * 1024) : "memory");
  if constexpr (R == 6 && Q == 35) asm volatile("s_add_u32 s98, s98, 0x8000");
  if constexpr (R == 7) cv_zs<cv_z0(Q)>(c, make_iseq<cv_z0(Q + 1) - cv_z0(Q)>{});
  __builtin_amdgcn_sched_barrier(0);
}
template <int R, int... Q> __device__ __forceinline__ void cv_step(CV& c, iseq<Q...>) { (cv_slot<R, Q>(c), ...); }
template <int... N> __device__ __forceinline__ void cv_rd0(CV& c, iseq<N...>) { (cv_rd<0, N>(c), ...); }
template <int FLAG> __device__ __forceinline__ void conv_wave(unsigned sz, float Dd, char* lw) {
  int lane = threadIdx.x & 63; asm volatile("" : "+v"(lane));
  const int l16 = lane & 15, fq = lane >> 4;
  u32x4c w[5]; unsigned w4[5];
#pragma unroll
  for (int r = 0; r < 5; ++r) {
    w[r] = *(const u32x4c*)(lw + r * 1024 + lane * 16);
    w4[r] = *(const unsigned*)(lw + r * 1024 + lane * 16 + 16);
  }
  d_store<NS_X>(); d_store<NS_X + 1>();
#pragma unroll
  for (int r = 0; r < 5; ++r)
    *(u32x4c*)(lw + 10304 + r * 1024 + lane * 16) = (u32x4c){__builtin_amdgcn_alignbit(w[r][1], w[r][0], 16), __builtin_amdgcn_alignbit(w[r][2], w[r][1], 16),
                                                             __builtin_amdgcn_alignbit(w[r][3], w[r][2], 16), __builtin_amdgcn_alignbit(w4[r], w[r][3], 16)};
  d_store<NS_X + 2>();
  CV c;
#pragma unroll
  for (int i = 0; i < 4; ++i) c.pa[i] = (unsigned)(size_t)lw + ((l16 + 1) & 1) * 10304 + 4 * ((8 * fq + l16 + 1) >> 1) + 1024 * i;
  c.pb = (unsigned)(size_t)lw + 5120 + (2032 - 16 * l16 + 8 * fq) * 2 - 512 * 7;
  c.vz = (16 * l16 + 4 * fq) * 2;
  c.px = (unsigned)(size_t)lw + 5120 + 2044 * 2 - 512 * 7 - c.vz;
  c.sz = sz; c.D = Dd;
  asm volatile("" : "+v"(c.vz), "+v"(c.px), "+v"(c.D) :: "memory");
  asm volatile("" : "+s"(c.sz));
  cv_rd0(c, make_iseq<24>{});
  d_store<NS_X + 3>(); d_store<NS_X + 4>();
  __builtin_amdgcn_sched_barrier(0);
  static_for([&](auto R) __attribute__((always_inline)) {
    cv_step<R>(c, make_iseq<36>{});
    if constexpr (R == 2) {
      asm volatile("s_waitcnt vmcnt(0)\n\ts_barrier" ::: "memory");
      fl_set<FLAG>();
    }
    if constexpr (R == 5) {
      asm volatile("s_waitcnt vmcnt(0)" ::: "memory");
      fl_check();
    }
    __builtin_amdgcn_sched_barrier(0);
  }, make_iseq<8>{});
  asm volatile("s_nop 7");
  cv_zs<cv_z0(36)>(c, make_iseq<88 - cv_z0(36)>{});
}

__device__ __forceinline__ void post_dma(int cg, int tb, const bf16* __restrict__ q, unsigned m0w) {
  int lane = threadIdx.x & 63; asm volatile("" : "+v"(lane));
  const __amdgpu_buffer_rsrc_t sq = __builtin_amdgcn_make_buffer_rsrc((void*)(q + (size_t)tb * 32 * DA + cg * 64), (short)0, 32 * DA * 2, 0x00020000);
  const unsigned vq = ((lane >> 3) * DA + (lane & 7) * 8) * 2;
  asm volatile("s_mov_b32 m0, %0\n\ts_nop 4\n\tbuffer_load_dwordx4 %1, %2, 0 offen" NTQS " lds\n\tbuffer_load_dwordx4 %1, %2, %3 offen offset:1024" NTQS " lds\n\t"
               "buffer_load_dwordx4 %1, %2, %4 offen offset:2048" NTQS " lds\n\tbuffer_load_dwordx4 %1, %2, %5 offen offset:3072" NTQS " lds"
               ::"s"(m0w + 4096), "v"(vq), "s"(sq), "s"(8 * DA * 2 - 1024), "s"(16 * DA * 2 - 2048), "s"(24 * DA * 2 - 3072) : "memory");
}
__device__ __forceinline__ void post_math(int cg, int tb, bf16* __restrict__ out, const char* lw) {
  int lane = threadIdx.x & 63; asm volatile("" : "+v"(lane));
  const int tl = lane >> 3, ck = lane & 7;
  const __amdgpu_buffer_rsrc_t so = __builtin_amdgcn_make_buffer_rsrc((void*)(out + (size_t)tb * 32 * OUTW + cg * 64), (short)0, 32 * OUTW * 2 - 2 * DA, 0x00020000);
  const unsigned vo = (tl * OUTW + ck * 8) * 2;
#pragma unroll
  for (int i = 0; i < 4; ++i) {
    const u32x4c qv = *(const u32x4c*)(lw + 4096 + i * 1024 + lane * 16);
    const char* lz = lw + ck * 64 + 16 * (i ^ (ck >> 1)) + tl * 2;
    u32x4c o;
#pragma unroll
    for (int e = 0; e < 4; ++e) {
      const float z0 = bf2f(*(const unsigned short*)(lz + 2 * e * 512));
      const float z1 = bf2f(*(const unsigned short*)(lz + (2 * e + 1) * 512));
      o[e] = pk2(bfl(qv[e]) * z0, bfh(qv[e]) * z1);
    }
    __builtin_amdgcn_raw_buffer_store_b128(o, so, vo + i * 8 * OUTW * 2, 0, OUTSC);
  }
}

#define DI __device__ __forceinline__
#define GAP() __builtin_amdgcn_sched_barrier(0)
typedef __attribute__((ext_vector_type(4))) float f32x4g;
typedef __attribute__((ext_vector_type(4))) unsigned u32x4g;
constexpr int LDW = NH * HD;
struct GM { unsigned aA[2], aB[2][8]; };
template <bool W, int J, int V, bool NT = false> DI void g_dma() {
  if constexpr (NT && J == 0) asm volatile("buffer_load_dwordx4 v%c0, s[60:63], 0 offen nt lds" ::"i"(V) : "memory");
  else if constexpr (NT) asm volatile("buffer_load_dwordx4 v%c0, s[60:63], s%c1 offen nt lds" ::"i"(V), "i"(70 + J) : "memory");
  else
  if constexpr (J == 0) asm volatile("buffer_load_dwordx4 v%c0, s[%c1:%c2], 0 offen lds" ::"i"(V), "i"(W ? 60 : 56), "i"(W ? 63 : 59) : "memory");
  else asm volatile("buffer_load_dwordx4 v%c0, s[%c1:%c2], s%c3 offen lds" ::"i"(V), "i"(W ? 60 : 56), "i"(W ? 63 : 59), "i"((W ? 70 : 63) + J) : "memory");
}
template <int P, int KS, int I> DI void g_wread(GM& g) {
  constexpr int nf = I >> 1, hf = I & 1, r = 192 - 64 * P + 8 * nf + 4 * KS + 2 * hf;
  asm volatile("ds_read_b64_tr_b16 a[%c1:%c2], %0 offset:%c3" ::"v"(g.aB[hf][nf]), "i"(r), "i"(r + 1), "i"(P * 32768 + KS * 16384));
}
template <int P, int M, int KS> DI void g_aread(GM& g) {
  constexpr int r = 96 + 8 * (M & 1) + 4 * KS;
  asm volatile("ds_read_b128 v[%c1:%c2], %0 offset:%c3" ::"v"(g.aA[KS]), "i"(r), "i"(r + 3), "i"(P * 32768 + M * 2048));
}
constexpr float CS = 0.0625f * 1.4426950408889634f;
constexpr int NS_Q = 19;
constexpr int e_vm(int G) { return (G * 3) % 8 < 3 ? G * 3 / 8 : -1; }
constexpr int e_st(int MODE, int G) {
  const int q = G & 15, k3 = ((G >> 4) - 1) * 3 + (q == 0 ? 0 : q == 4 ? 1 : 2), k2 = NS_Q + (G >> 4) * 2 + (q == 4);
  if (MODE == 5) return e_vm(G) >= 32 ? e_vm(G) - 32 : -1;
  if (MODE == 3) return G >= 33 && (G - 33) % 5 == 0 && (G - 33) / 5 < NS_X ? (G - 33) / 5 : -1;
  if (MODE == 2) return G >= 30 && (q == 0 || q == 4 || q == 14) && k3 - 2 < NS_Q ? k3 - 2 : -1;
  if (MODE == 4) return (q == 0 || q == 4) && k2 < 32 ? k2 : -1;
  return -1;
}
constexpr int e_nst(int MODE, int G0, int G1) { int n = 0; for (int G = G0; G < G1; ++G) n += e_st(MODE, G) >= 0; return n; }
static_assert(e_nst(2, 0, 128) == NS_Q && e_nst(3, 30, 128) == NS_X && e_nst(5, 0, 128) == 16 && e_nst(4, 0, 104) == 32 - NS_Q);
constexpr int e_slot(int MODE, int G) { return MODE == 3 ? (G % 3 == 0 && G < 30 ? G / 3 : -1) : e_vm(G) >= 0 && e_vm(G) < 32 ? 10 + e_vm(G) : -1; }
struct ES { int op0[129], nop[129]; };
constexpr ES e_sched(bool TR, int MODE) {
  ES z{};
  const bool SCL = MODE == 2 || MODE == 4;
  const int per = SCL ? 16 : 8, o1 = 32 * per;
  int o = MODE == 4 ? e_sched(false, 2).op0[128] : 0;
  for (int G = 0; G <= 128; ++G) {
    const int q = G & 15;
    int cap = 2 - (q == 1 || q == 2 || q == 15);
    if (SCL) cap -= (q >= 3 && q < 11 && (q & 1)) + (q == 7 || q == 11) + 2 * (G == 70 || G == 74 || (G & 63) == 61);
    cap += MODE == 2 ? 4 + (q == 10 || q == 13) : MODE == 3 ? 1 : MODE == 5 ? 2 : 0;
    if (e_st(MODE, G) >= 0 || (SCL ? q == 8 || q == 12 : e_slot(MODE, G) >= 0) || (MODE == 4 && G < 4)) cap = 0;
    if (G == 128) cap = 10000;
    z.op0[G] = o;
    while (o < o1) {
      const int i = o / per, rdy = MODE == 4 ? 0 : (TR ? 32 * (i >> 3) + 16 + 2 * (i & 7) + 1 : 4 * i + 3) + 6;
      if ((G < rdy && G < 128) || cap-- <= 0) break;
      ++o; ++z.nop[G];
    }
  }
  return z;
}
template <bool TR, int MODE> constexpr ES ESCH = e_sched(TR, MODE);
static_assert(ESCH<false, 4>.op0[96] == 512 && ESCH<false, 4>.op0[0] >= 24 * 16);
template <bool TR, bool SCL, int O> DI void e_op() {
  constexpr int per = SCL ? 16 : 8, i = O / per, k = O % per, r = 128 + 4 * i, t = 114 + 4 * (i & 1), c = k - (per - 4);
  if constexpr (k < 4) asm volatile("v_accvgpr_read_b32 v%c0, a[%c1]" ::"i"(t + k), "i"(4 * i + k));
  else if constexpr (c < 0) asm volatile("v_mul_f32 v%c0, s83, v%c0" ::"i"(k < 8 ? r + (k ^ 6) : t + k - 8));
  else {
    constexpr int d = c == 0 ? r + 3 : c == 1 ? r + 2 : r + c - 2, x = c == 0 ? r + 2 : c == 1 ? r : t + 2 * (c - 2);
    asm volatile("v_cvt_pk_bf16_f32 v%c0, v%c1, v%c2" ::"i"(d), "i"(x), "i"(x + 1));
  }
}
template <bool TR, bool SCL, int B, int... Is> DI void e_ops(iseq<Is...>) { (e_op<TR, SCL, B + Is>(), ...); }
template <int S> DI void e_m0() {
  if constexpr (S == 0 || S == 1 || S == 5 || S == 9) asm volatile("s_add_u32 m0, s78, %0" ::"i"(S * 1024));
  if constexpr (S >= 26 && (S - 26) % 4 == 0) asm volatile("s_add_u32 m0, s79, %0" ::"i"(((S - 26) >> 3) * 32768 + ((S - 26) & 4) * 1024));
}
template <int S> DI void e_load() {
  if constexpr (S == 0) asm volatile("buffer_load_dwordx4 v122, s[56:59], 0 offen lds" ::: "memory");
  else if constexpr (S < 5) asm volatile("buffer_load_dwordx4 v84, s[56:59], s64 offen offset:%0 lds" ::"i"((S - 1) * 1024) : "memory");
  else if constexpr (S < 9) asm volatile("buffer_load_dwordx4 v84, s[60:63], 0 offen offset:%0 sc1 lds" ::"i"((S - 5) * 1024) : "memory");
  else if constexpr (S == 9) asm volatile("buffer_load_dwordx4 v84, s[60:63], s62 offen sc1 lds" ::: "memory");
  else if constexpr (S < 26) {
    constexpr int f = S - 10;
    asm volatile("buffer_load_dwordx4 a[%c0:%c1], v84, s[88:91], s96 offen offset:%2 sc1" ::"i"(128 + 4 * f), "i"(131 + 4 * f), "i"((f & 3) * 1024) : "memory");
    if constexpr ((f & 3) == 3) asm volatile("s_addk_i32 s96, 4096");
  } else {
    constexpr int f = S - 26;
    asm volatile("buffer_load_dwordx4 v%c0, s[88:91], s98 offen offset:%1 sc1 lds" ::"i"(86 + ((f >> 2) & 1)), "i"((f & 3) * 1024) : "memory");
    if constexpr (f == 7) asm volatile("s_add_u32 s98, s98, s97");
    if constexpr (f == 15) asm volatile(FL_POLL ::: "memory");
  }
}
template <bool TR, int P, int H, int G, int MODE, bool NT> DI void g_gap(GM& g) {
  constexpr int m = 4 * H + (G >> 4), q = G & 15, n = q >> 1, ks = q & 1, GG = 64 * H + G;
  constexpr bool inV = TR ? m & 1 : n & 1, END = MODE == 3 || MODE == 5;
  constexpr int a = 96 + 8 * (m & 1) + 4 * ks, b = 192 - 64 * P + 8 * n + 4 * ks, d = (inV ? 128 : 0) + 4 * (TR ? 8 * (m >> 1) + n : 4 * m + (n >> 1));
  constexpr int J = END ? -1 : q == 8 ? 2 * (G >> 4) : q == 12 ? 2 * (G >> 4) + 1 : -1, gj = (J & 3) ^ ((J & 3) >> 1);
  if constexpr (END) { if constexpr (q == 0 && GG > 0) asm volatile("s_waitcnt lgkmcnt(0)"); e_m0<e_slot(MODE, GG)>(); }
  else if constexpr (q == 0 && G > 0) asm volatile("s_waitcnt lgkmcnt(4)");
  if constexpr (J == 0) asm volatile("s_add_u32 m0, s78, %0" ::"i"((P ^ H ^ 1) * 32768 + (1 - H) * 8192));
  else if constexpr (J == 4) asm volatile("s_add_u32 m0, s79, %0" ::"i"((P ^ H) * 32768 + (1 - H) * 16384));
  else if constexpr (J > 0 && (J & 3) == 1) asm volatile("s_bitset1_b32 m0, 12");
  else if constexpr (J > 0 && (J & 3) == 2) asm volatile("s_bitset1_b32 m0, %0" ::"i"(J < 4 ? 14 : 13));
  else if constexpr (J > 0) asm volatile("s_bitset0_b32 m0, 12");
#define GMF(D, X, Y, C) asm volatile("v_mfma_f32_16x16x32_bf16 " D "[%c0:%c1], " X ", " Y ", " C ::"i"(d), "i"(d + 3), "i"(a), "i"(a + 3), "i"(b), "i"(b + 3))
#define GMF2(D, C) do { if constexpr (TR) GMF(D, "v[%c2:%c3]", "a[%c4:%c5]", C); else GMF(D, "a[%c4:%c5]", "v[%c2:%c3]", C); } while (0)
  if constexpr ((MODE == 1 || MODE == 4) && ks == 0) { if constexpr (inV) GMF2("v", "0"); else GMF2("a", "0"); }
  else if constexpr (inV) GMF2("v", "v[%c0:%c1]");
  else GMF2("a", "a[%c0:%c1]");
#undef GMF2
#undef GMF
  if constexpr ((q == 1 || q == 2) && !(END && m == 7)) g_aread<(m < 7 ? P : P ^ 1), (m + 1) & 7, q - 1>(g);
  if constexpr (!END && q >= 3 && q < 11 && (q & 1)) g_wread<P ^ 1, H, (G >> 4) * 4 + (q - 3) / 2>(g);
  if constexpr (e_st(MODE, GG) >= 0) {
    static_assert(ESCH<TR, MODE>.op0[GG] >= (e_st(MODE, GG) + 1) * (END ? 8 : 16));
    d_store<e_st(MODE, GG)>();
  }
  if constexpr (J >= 0 && J < 4) g_dma<false, (1 - H) * 2 + (gj & 1) + (gj >> 1) * 4, 90>();
  else if constexpr (J >= 4) g_dma<true, (1 - H) * 4 + gj, 91, NT>();
  if constexpr (MODE >= 2) e_ops<TR, !END, ESCH<TR, MODE>.op0[GG]>(make_iseq<ESCH<TR, MODE>.nop[GG]>{});
  if constexpr (END) {
    if constexpr (e_slot(MODE, GG) >= 0) e_load<e_slot(MODE, GG)>();
  } else {
    if constexpr (H == 1 && G == 6) asm volatile("v_lshl_add_u32 v90, s82, 7, v92\n\ts_and_b32 s82, s80, 15");
    if constexpr (H == 1 && G == 10) asm volatile("s_lshr_b64 s[80:81], s[80:81], 4\n\tv_lshl_add_u32 v91, s82, 19, v93");
    constexpr int nst = e_nst(MODE, H ? 29 : 0, GG + 1) + (MODE == 4 && !H ? e_nst(2, 93, 128) : 0);
    if constexpr (G == 61) asm volatile("s_waitcnt vmcnt(%0) lgkmcnt(0)\n\ts_barrier" ::"i"(12 + nst) : "memory");
  }
  if constexpr (END && GG == 127) {
    asm volatile("s_nop 7");
    e_ops<TR, false, ESCH<TR, MODE>.op0[128]>(make_iseq<ESCH<TR, MODE>.nop[128]>{});
  }
  GAP();
}
template <bool TR, int P, int H, int MODE, bool NT, int... Gs> DI void g_half_(GM& g, iseq<Gs...>) { (g_gap<TR, P, H, Gs, MODE, NT>(g), ...); }
template <bool TR, int P, int H, int MODE, bool NT = false> DI void g_half(GM& g) { g_half_<TR, P, H, MODE, NT>(g, make_iseq<64>{}); }
template <bool TR, int P, int MODE, bool NT = false> DI void g_tile(GM& g) { g_half<TR, P, 0, MODE, NT>(g); g_half<TR, P, 1, MODE, NT>(g); }
template <int P, int... Is> DI void g_read0(GM& g, iseq<Is...>) {
  (g_wread<P, 0, Is>(g), ...); (g_wread<P, 1, Is>(g), ...);
  g_aread<P, 0, 0>(g); g_aread<P, 0, 1>(g);
}
DI void g_proW(unsigned g, unsigned mW) {
  asm volatile("v_lshl_add_u32 v91, %0, 18, v93\n\ts_mov_b32 m0, %1" ::"s"(g), "s"(mW));
  asm volatile("s_nop 0"); g_dma<true, 0, 91>();
  asm volatile("s_bitset1_b32 m0, 12\n\ts_nop 0"); g_dma<true, 1, 91>();
  asm volatile("s_bitset1_b32 m0, 13\n\ts_nop 0"); g_dma<true, 3, 91>();
  asm volatile("s_bitset0_b32 m0, 12\n\ts_nop 0"); g_dma<true, 2, 91>();
}
template <int PART = 3> DI void g_pro(unsigned g, unsigned mA, unsigned mW, unsigned lda) {
  g &= 3;
  if constexpr (PART & 1)
    asm volatile("v_add_u32 v90, %0, v92\n\ts_mov_b32 m0, %1" ::"s"((g >> 1) * 128 + (g & 1) * 128 * lda), "s"((mA ^ (g >> 1) * 32768) + (g & 1) * 8192));
  if constexpr (PART == 1) return;
  asm volatile("s_nop 0"); g_dma<false, 0, 90>();
  asm volatile("s_bitset1_b32 m0, 12\n\ts_nop 0"); g_dma<false, 1, 90>();
  asm volatile("s_bitset1_b32 m0, 14\n\ts_nop 0"); g_dma<false, 5, 90>();
  asm volatile("s_bitset0_b32 m0, 12\n\ts_nop 0"); g_dma<false, 4, 90>();
  g_proW(g, mW ^ g * 16384);
}
template <bool TR, bool SCL, class HOOK> DI void g_loop(GM& g, unsigned evm, HOOK&& hook) {
  constexpr int nkt = SCL ? 7 : 16, P0 = SCL ? 0 : 1;
  asm volatile("" : "+s"(evm));
#pragma unroll 1
  for (int t = 1;; t += 2) {
    g_half<TR, P0 ^ 1, 0, 0, (SCL ? NTW >= 1 : NTW >= 2)>(g);
    if (__builtin_expect((evm >> t) & 1, 0)) { hook(t); GAP(); }
    g_half<TR, P0 ^ 1, 1, 0, (SCL ? NTW >= 1 : NTW >= 2)>(g);
    if (SCL && t + 2 >= nkt) break;
    g_half<TR, P0, 0, 0, (SCL ? NTW >= 1 : NTW >= 2)>(g);
    if constexpr (SCL) { if (__builtin_expect(t == 3, 0)) { hook(4); GAP(); } }
    else asm volatile("s_cmp_eq_u32 %0, 13\n\ts_cselect_b32 s78, 0x28000, s78" ::"s"(t) : "scc");
    g_half<TR, P0, 1, 0, (SCL ? NTW >= 1 : NTW >= 2)>(g);
    if (!SCL && t + 3 >= nkt) break;
  }
}

typedef __attribute__((ext_vector_type(4))) unsigned u32x4;
typedef __attribute__((ext_vector_type(4))) float f32x4;

constexpr int R_S = 128, R_P = 192, R_CI = 224, R_E = 96;
constexpr int R_TS = 112, R_RS = 114, R_M = 116;
constexpr int R_PD = 118, R_THR = 120, R_TMP = 121;
constexpr int r_dk(int q) { return q < 6 ? 90 + q : 120 + q; }
constexpr int S_V = 96, S_T = 97, S_K = 98, S_I = 99, S_EV = 100;
struct AC {
  unsigned rare;
};
DI bool redo() { u64 t; asm volatile("s_mov_b64 %0, s[54:55]" : "=s"(t)); return t != 0; }
template <int X, int F> DI void kread() {
  constexpr int r = 192 + 4 * (F & 15);
  asm volatile("ds_read_b128 a[%c0:%c1], v84 offset:%c2" ::"i"(r), "i"(r + 3), "i"(X * 32768 + F * 1024));
}
template <int X, int F> DI void vread() {
  constexpr int r = 192 + 4 * (F & 15);
  asm volatile("ds_read_b128 a[%c0:%c1], v85 offset:%c2" ::"i"(r), "i"(r + 3), "i"(X * 32768 + (F & 1) * 16384 + (F >> 1) * 1024));
}
template <int X, int... Fs> DI void kreads(iseq<Fs...>) { (kread<X, Fs>(), ...); }
template <int X, int J> DI void dma_m0() {
  if constexpr (J == 0) asm volatile("s_mov_b32 m0, s%c0" ::"i"(78 + X));
  else if constexpr (J == 4) asm volatile("s_or_b32 m0, s%c0, 0x1000" ::"i"(78 + X));
  else if constexpr (J == 8) asm volatile("s_bitset0_b32 m0, 16");
  else if constexpr (J == 12) asm volatile("s_bitset0_b32 m0, 12");
}
template <int J> DI void dma_at() {
  if constexpr (J < 8)
    asm volatile("buffer_load_dwordx4 v%c0, s[88:91], s96 offen offset:%1 sc1 lds" ::"i"(86 + (J >= 4)), "i"((J & 3) * 1024) : "memory");
  else
    asm volatile("buffer_load_dwordx4 v%c0, s[92:95], s98 offen offset:%1 sc1 lds" ::"i"(r_dk(J - 8)), "i"((J & 3) * 1024) : "memory");
}
template <int Q> DI void dma4k() {
  asm volatile("s_add_u32 m0, s79, %0\n\ts_add_u32 s48, s98, %1\n\tbuffer_load_dwordx4 v%c2, s[88:91], s48 offen sc1 lds\n\t"
               "buffer_load_dwordx4 v%c2, s[88:91], s48 offen offset:1024 sc1 lds\n\tbuffer_load_dwordx4 v%c2, s[88:91], s48 offen offset:2048 sc1 lds\n\t"
               "buffer_load_dwordx4 v%c2, s[88:91], s48 offen offset:3072 sc1 lds"
               ::"i"((Q >> 1) * 32768 + (Q & 1) * 4096), "i"((Q >> 1) * 32768), "i"(86 + (Q & 1)) : "memory", "scc");
}
template <int Q> DI void acc_zero2() {
  asm volatile("v_accvgpr_write_b32 a[%c0], 0\n\tv_accvgpr_write_b32 a[%c1], 0" ::"i"(2 * Q), "i"(2 * Q + 1));
}
#define SMV 0
struct SMS { int ex[128], op0[128], nop[128], last; };
constexpr SMS sm_sched() {
  SMS z{};
  int et[32] = {}, e = 0;
  for (int t = 0; t < 128; ++t) {
    const int g = t & 15;
    const bool x = t < 48 ? (g >= 8 && (g & 1) && g < 15) : t < 64 ? (g >= 9 && g < 15) : (g >= 8 && g < 14);
    z.ex[t] = x && e < 32 ? e : -1;
    if (z.ex[t] >= 0) et[e++] = t;
  }
  int o = 0;
  for (int t = 0; t < 128; ++t) {
    const int g = t & 15;
    int cap = z.ex[t] >= 0 || t < 8 || (t < 48 && (g == 7 || (g >= 8 && !(g & 1)))) || t == 56 || t == 63 || t == 111 ? 0 : 1;
    z.op0[t] = o; z.nop[t] = 0;
    while (cap > 0 && o < 48 && et[2 * (o / 3) + (o % 3 == 0 ? 0 : 1)] + 2 <= t) {
      const int el = 2 * (o / 3) + o % 3;
      if (!(o % 3 < 2 && (el == 0 || el == 4))) --cap;
      ++o; ++z.nop[t]; z.last = t;
    }
  }
  for (int k = 16; k < 32; ++k) if (z.op0[et[k]] < 3 * ((k - 16) / 2 + 1)) z.last = 1000;
  return z;
}
constexpr SMS SM = sm_sched();
static_assert(SM.last <= 117);
constexpr int p_reg(int Y, int k) { return R_P + 16 * Y + 4 * ((k >> 3) * 2 + ((k >> 1) & 1)) + ((k >> 2) & 1) * 2 + (k & 1); }
template <int Y, int O> DI void sm_op() {
  constexpr int k = O / 3, e = 2 * k + O % 3, ts = R_TS + ((e >> 2) & 1);
  if constexpr (O % 3 == 2) asm volatile("v_cvt_pk_bf16_f32 v%c0, v%c1, v%c2" ::"i"(p_reg(Y, k)), "i"(R_E + ((2 * k) & 15)), "i"(R_E + ((2 * k + 1) & 15)));
  else if constexpr (e == 1 || e == 5) asm volatile("v_add_f32_e32 v%c0, v%c1, v%c2" ::"i"(ts), "i"(R_E + e - 1), "i"(R_E + e));
  else if constexpr (e != 0 && e != 4) asm volatile("v_add_f32_e32 v%c0, v%c0, v%c1" ::"i"(ts), "i"(R_E + (e & 15)));
}
template <int Y, int T> DI void sm_gap(AC& c) {
  if constexpr (SM.ex[T] >= 0) asm volatile("v_exp_f32 v%c0, v%c1" ::"i"(R_E + (SM.ex[T] & 15)), "i"(R_S + 32 * Y + SM.ex[T]));
  if constexpr (SM.nop[T] > 0) sm_op<Y, SM.op0[T]>();
  if constexpr (SM.nop[T] > 1) sm_op<Y, SM.op0[T] + 1>();
  if constexpr (SM.nop[T] > 2) sm_op<Y, SM.op0[T] + 2>();
  if constexpr (T == SM.last + 1) asm volatile("v_add_f32_e32 v%c0, v%c1, v%c2" ::"i"(R_TMP), "i"(R_TS), "i"(R_TS + 1));
  if constexpr (T == SM.last + 2) asm volatile("v_cmp_nle_f32_e64 s[54:55], v%c0, v%c1" ::"i"(R_TMP), "i"(R_THR));
}
#define W_A0 1
template <int X, int MODE, int G> DI void gapA(AC& c) {
  constexpr bool FIRST = MODE == 1 || MODE == 2, NOQK = MODE >= 3;
  constexpr int Xp = X ^ 1, kb = G >> 4, g = G & 15, qb = g >> 3, s = g & 7;
  if constexpr (MODE == 3 && G < 48 && G % 3 == 0) {
    constexpr int f = G / 3;
    asm volatile("buffer_load_dwordx4 a[%c0:%c1], v84, s[88:91], s75 offen offset:%2 sc1" ::"i"(128 + 4 * f), "i"(131 + 4 * f), "i"((f & 3) * 1024) : "memory");
    if constexpr ((f & 3) == 3) asm volatile("s_addk_i32 s75, 4096");
  }
  if constexpr (G == 48) asm volatile("s_waitcnt vmcnt(%0) lgkmcnt(0)\n\ts_barrier" ::"i"(MODE == 3 ? 16 : MODE == 2 ? 12 : MODE == 1 ? W_A0 : 0) : "memory");
  else if constexpr (g == 0 && !NOQK) asm volatile("s_waitcnt lgkmcnt(0)");
  if constexpr (!NOQK) {
    constexpr int r = 192 + 4 * ((kb & 1) * 8 + s), q = 128 + 4 * g;
    constexpr int d = R_S + 32 * X + 4 * (kb * 2 + qb), ci = s == 0 ? R_CI + 4 * (kb * 2 + qb) : d;
    if constexpr (FIRST && s == 0)
      asm volatile("v_mfma_f32_16x16x32_bf16 v[%c0:%c1], a[%c2:%c3], a[%c4:%c5], 0" ::"i"(d), "i"(d + 3), "i"(r), "i"(r + 3), "i"(q), "i"(q + 3));
    else
      asm volatile("v_mfma_f32_16x16x32_bf16 v[%c0:%c1], a[%c2:%c3], a[%c4:%c5], v[%c6:%c7]" ::"i"(d), "i"(d + 3), "i"(r), "i"(r + 3), "i"(q), "i"(q + 3),
                   "i"(ci), "i"(ci + 3));
    if constexpr (g < 8 && kb < 3) kread<X, (kb + 1) * 8 + g>();
    constexpr int S0 = SMV ? 40 : 14, S1 = SMV ? 41 : 30, S2 = SMV ? 42 : 45, S3 = SMV ? 43 : 46;
    if constexpr (G == S0) asm volatile("s_add_u32 s96, s97, 0x1000000");
    if constexpr (G == S1) dma_m0<X, 0>();
    if constexpr (G == S2) asm volatile("s_mov_b32 s97, s98");
    if constexpr (G == S3) asm volatile("s_add_u32 s98, s98, s99");
    if constexpr (G >= 56 && !(G & 1)) dma_at<(G - 56) / 2>();
    if constexpr (G == 57) asm volatile("s_lshr_b32 s100, s100, 1\n\ts_add_u32 s56, s56, 1");
    if constexpr (G == 59 && !FIRST) asm volatile("s_and_b32 s49, s100, 1\n\ts_or_b32 s49, s49, s54");
    if constexpr (G == 61 && !FIRST) asm volatile("s_or_b32 %0, s49, s55" : "=s"(c.rare));
    if constexpr (G == 61 && FIRST) asm volatile("s_and_b32 %0, s100, 1" : "=s"(c.rare));
  } else if constexpr (SM.nop[64 + G] > 0) asm volatile("s_nop 0");
  if constexpr (!FIRST && g < 8 && kb == 3) vread<Xp, g>();
  if constexpr (MODE == 2 && (G & 3) == 1) d_store<16 + G / 4>();
  if constexpr (FIRST) acc_zero2<G>();
  else sm_gap<Xp, 64 + G>(c);
  GAP();
}
template <int R> DI void rescale1(float alpha) {
  float t;
  asm volatile("v_accvgpr_read_b32 %0, a[%c2]\n\tv_mul_f32 %0, %0, %1\n\tv_accvgpr_write_b32 a[%c2], %0" : "=&v"(t) : "v"(alpha), "i"(R));
}
template <int... Rs> DI void rescale_all(const float (&alpha)[2], iseq<Rs...>) { (rescale1<Rs>(alpha[(Rs >> 2) & 1]), ...); }
template <bool MAX> DI float row_reduce(float v) {
  auto p = __builtin_amdgcn_permlane32_swap(__float_as_uint(v), __float_as_uint(v), false, false);
  v = MAX ? fmaxf(__uint_as_float(p[0]), __uint_as_float(p[1])) : __uint_as_float(p[0]) + __uint_as_float(p[1]);
  p = __builtin_amdgcn_permlane16_swap(__float_as_uint(v), __float_as_uint(v), false, false);
  return MAX ? fmaxf(__uint_as_float(p[0]), __uint_as_float(p[1])) : __uint_as_float(p[0]) + __uint_as_float(p[1]);
}
template <int Y, int QB> DI float row_max() {
  constexpr int r = R_S + 32 * Y + 4 * QB;
  float mx;
  asm volatile("v_max_f32 %0, v%c1, v%c2\n\tv_max3_f32 %0, %0, v%c3, v%c4" : "=&v"(mx) : "i"(r), "i"(r + 1), "i"(r + 2), "i"(r + 3));
  static_for([&](auto K) {
    constexpr int q = r + 8 * (K + 1);
    float t = mx;
    asm volatile("v_max3_f32 %0, %0, v%c1, v%c2\n\tv_max3_f32 %0, %0, v%c3, v%c4" : "+v"(t) : "i"(q), "i"(q + 1), "i"(q + 2), "i"(q + 3));
    mx = t;
  }, make_iseq<3>{});
  return row_reduce<true>(mx);
}
template <int R> DI void v_sub(float d) { asm volatile("v_sub_f32 v%c0, v%c0, %1" ::"i"(R), "v"(d)); }
template <int B> DI void s_shift(const float (&d)[2]) { static_for([&](auto E) { v_sub<B + E>(d[(E >> 2) & 1]); }, make_iseq<32>{}); }
DI void m_shift(const float (&d)[2]) {
  s_shift<R_CI>(d);
  v_sub<R_M>(-d[0]); v_sub<R_M + 1>(-d[1]);
}
DI void set_mask0(int wid) {
  const int mr = wid * 32 + __float_as_int(vget<89>());
  static_for([&](auto I) {
    constexpr int e = I, k = (e >> 3) * 16 - ((e >> 2) & 1) * 16 + (e & 3);
    const float x = vget<R_S + e>();
    vset<R_S + e>(k > mr ? -INFINITY : x);
  }, make_iseq<32>{});
}
template <int X> DI void sm_redo() {
  constexpr int Xp = X ^ 1;
  float d[2], dt[2], alpha[2], ts[2] = {0.f, 0.f};
  static_for([&](auto Q) {
    constexpr int qb = Q;
    const float pd = vget<R_PD + qb>();
    d[qb] = fmaxf(row_max<Xp, qb>() - pd, 0.f);
    dt[qb] = d[qb] + pd;
    vset<R_PD + qb>(d[qb]);
    alpha[qb] = __builtin_amdgcn_exp2f(-d[qb]);
    vset<R_RS + qb>(vget<R_RS + qb>() * alpha[qb]);
  }, make_iseq<2>{});
  vset<R_THR>(d[0] > 0.f || d[1] > 0.f ? -1.f : 4096.f);
  m_shift(d);
  static_for([&](auto K) {
    constexpr int k = K, qb = (k >> 1) & 1;
    const float e0 = __builtin_amdgcn_exp2f(vget<R_S + 32 * Xp + 2 * k>() - dt[qb]), e1 = __builtin_amdgcn_exp2f(vget<R_S + 32 * Xp + 2 * k + 1>() - dt[qb]);
    ts[qb] += e0; ts[qb] += e1;
    vset<p_reg(Xp, k)>(__uint_as_float(pk2(e0, e1)));
  }, make_iseq<16>{});
  vset<R_TS>(ts[0]); vset<R_TS + 1>(ts[1]);
  rescale_all(alpha, make_iseq<128>{});
  asm volatile("s_nop 1" ::: "memory");
  GAP();
}
template <int X, int MODE, int... Gs> DI void phaseA_(AC& c, iseq<Gs...>) { (gapA<X, MODE, Gs>(c), ...); }
template <int X, int MODE> DI void phaseA(AC& c) { phaseA_<X, MODE>(c, make_iseq<64>{}); }

template <int X, bool SMX, int G, bool WAIT = true> DI void gapB(AC& c) {
  constexpr int Xp = X ^ 1, blk = G >> 4, g = G & 15;
  if constexpr (g == 0 && WAIT) asm volatile("s_waitcnt lgkmcnt(0)");
  constexpr int qb = (g >> 1) & 1, s = g & 1, r = 192 + 4 * ((blk & 1) * 8 + (g >> 2) * 2 + s), o = 4 * ((blk * 4 + (g >> 2)) * 2 + qb);
  constexpr int p = R_P + 16 * Xp + 4 * (s * 2 + qb);
  asm volatile("v_mfma_f32_16x16x32_bf16 a[%c0:%c1], a[%c2:%c3], v[%c4:%c5], a[%c0:%c1]" ::"i"(o), "i"(o + 3), "i"(r), "i"(r + 3), "i"(p), "i"(p + 3));
  if constexpr (g < 8) {
    if constexpr (blk < 3) vread<Xp, (blk + 1) * 8 + g>();
    else if constexpr (SMX) kread<Xp, g>();
  }
  if constexpr (SMX) {
    if constexpr (G == 1 || G == 2) asm volatile("v_add_f32_e32 v%c0, v%c0, v%c1" ::"i"(R_RS + G - 1), "i"(R_TS + G - 1));
    if constexpr (g == 7 && blk < 3) dma_m0<X, blk * 4 + 4>();
    if constexpr (g >= 8 && !(g & 1) && blk < 3) dma_at<blk * 4 + 4 + (g - 8) / 2>();
    if constexpr (G == 55 && X == 1) asm volatile("s_cmp_eq_u32 s56, s76");
    if constexpr (G == 3) asm volatile("s_lshr_b32 s77, s77, 1");
    if constexpr (G == 4) asm volatile("s_bitcmp1_b32 s77, 0");
    if constexpr (G == 5) asm volatile("s_cselect_b64 s[52:53], s[62:63], 0");
    if constexpr (G == 6) asm volatile(FL_ARM("s[52:53]"));
    if constexpr (G == 56) asm volatile(FL_POLL ::: "memory");
    sm_gap<X, G>(c);
  }
  GAP();
}
template <int X, int... Gs> DI void phaseB_(AC& c, iseq<Gs...>) { (gapB<X, true, Gs>(c), ...); }
template <int X> DI void phaseB(AC& c) { phaseB_<X>(c, make_iseq<64>{}); }
constexpr int sm_nex() { int n = 0; for (int t = 0; t < 64; ++t) n += SM.ex[t] >= 0; return n; }
template <int X> DI void phaseB0(unsigned mode, int wid) {
  dma_m0<X, 4>();
  kreads<X ^ 1>(make_iseq<8>{});
  dma_at<4>();
  asm volatile("s_nop 3");
  float d[2];
  if (mode & 2) {
    d[0] = vget<122>(); d[1] = vget<123>();
    dma_at<5>();
  } else {
    if constexpr (X == 0) if (__builtin_expect(mode & 4, 0)) set_mask0(wid);
    d[0] = row_max<X, 0>(); GAP(); dma_at<5>();
    d[1] = row_max<X, 1>();
#pragma unroll
    for (int qb = 0; qb < 2; ++qb) if (!(d[qb] > -3.0e38f)) d[qb] = 0.f;
  }
  GAP();
  vset<R_M>(d[0]); vset<R_M + 1>(d[1]);
  dma_at<6>();
  const f32x2c nd[2] = {{-d[0], -d[0]}, {-d[1], -d[1]}};
  static_for([&](auto E) {
    constexpr int e = E;
    const f32x2c v = nd[(e >> 1) & 1];
    asm volatile("v_pk_add_f32 v[%c0:%c1], v[%c0:%c1], %4\n\tv_mov_b64 v[%c2:%c3], %4" ::"i"(R_S + 32 * X + 2 * e), "i"(R_S + 32 * X + 2 * e + 1), "i"(R_CI + 2 * e), "i"(R_CI + 2 * e + 1), "v"(v));
    if constexpr (e == 3) dma_at<7>();
    if constexpr (e == 6) dma_m0<X, 8>();
    if constexpr (e == 7 || e == 11 || e == 15) dma_at<8 + (e - 7) / 4>();
  }, make_iseq<16>{});
  static_assert(SM.op0[64] >= 3 * ((sm_nex() - 15) / 2) && SM.op0[64] <= 24);
  static_for([&](auto E) {
    constexpr int e = E;
    if constexpr (e < sm_nex()) asm volatile("v_exp_f32 v%c0, v%c1" ::"i"(R_E + e), "i"(R_S + 32 * X + e));
    if constexpr (e == 3) dma_at<11>();
    if constexpr (e == 6) dma_m0<X, 12>();
    if constexpr (e == 7 || e == 11 || e == 15) dma_at<12 + (e - 7) / 4>();
  }, make_iseq<16>{});
  static_for([&](auto O) { sm_op<X, O>(); }, make_iseq<SM.op0[64]>{});
  dma_at<15>();
  static_for([&](auto E) { asm volatile("v_exp_f32 v%c0, v%c1" ::"i"(R_E + (int)E), "i"(R_S + 32 * X + 16 + (int)E)); }, make_iseq<(sm_nex() > 16 ? sm_nex() - 16 : 0)>{});
  asm volatile("buffer_store_dwordx2 v[%c0:%c1], v88, s[88:91], s76 offen sc1\n\ts_bitcmp1_b32 s77, 0\n\ts_cselect_b64 s[52:53], s[62:63], 0\n\t"
               FL_ARM("s[52:53]") "\n\t" FL_POLL ::"i"(R_M), "i"(R_M + 1) : "memory", "scc");
  GAP();
}
constexpr int T_ZO = 8;
#define NINF2(R) "v_mov_b64 v[" #R ":" #R "+1], v[122:123]\n\t"
#define NINF4(R) NINF2(R) NINF2(R + 2)
#define TRI(R) "v_cndmask_b32_e64 v[" #R "], v[" #R "], v122, s[48:49]\n\tv_cndmask_b32_e64 v[" #R "+1], v[" #R "+1], v122, s[50:51]\n\t" \
               "v_cndmask_b32_e64 v[" #R "+2], v[" #R "+2], v122, s[52:53]\n\tv_cndmask_b32_e64 v[" #R "+3], v[" #R "+3], v122, s[54:55]\n\t"
DI void ev_code() {
  asm volatile(
    "s_getpc_b64 s[82:83]\n\ts_add_u32 s82, s82, 12\n\ts_addc_u32 s83, s83, 0\n\ts_branch 9f\n\t"
    "s_bitcmp0_b32 s58, s56\n\ts_cbranch_scc1 1f\n\t" FL_SET("s70") "\n\t" FL_SET("s71") "\n"
    "1:\n\ts_bitcmp0_b32 s57, s56\n\ts_cbranch_scc1 2f\n\t" FL_CHECK("s[62:63]") "\n\t"
    "s_add_u32 s98, s98, s68\n\ts_mov_b32 s68, s69\n\ts_mov_b32 s69, 0\n\ts_mov_b64 s[62:63], s[64:65]\n\ts_mov_b64 s[64:65], s[66:67]\n"
    "2:\n\ts_bitcmp0_b32 s59, s56\n\ts_cbranch_scc1 3f\n\ts_mov_b32 s98, s72\n\ts_bitcmp0_b32 s73, 0\n\ts_cbranch_scc1 3f\n\t"
    "v_lshrrev_b32 v90, 7, v86\n\tv_and_b32 v91, 6, v90\n\tv_lshlrev_b32 v91, 4, v91\n\tv_and_b32 v92, 0x70, v86\n\tv_xor_b32 v91, v91, v92\n\t"
    "s_movk_i32 s48, 896\n\tv_mad_u32_u24 v94, v90, s48, v91\n\ts_mov_b64 s[92:93], s[80:81]\n\ts_mov_b32 s94, 229376\n\ts_movk_i32 s99, 128\n\t"
    "v_add_u32 v90, 28672, v94\n\tv_add_u32 v91, 34816, v94\n\tv_add_u32 v92, 40960, v94\n\tv_add_u32 v93, 47104, v94\n\t"
    "v_add_u32 v95, 6144, v94\n\tv_add_u32 v126, 12288, v94\n\tv_add_u32 v127, 18432, v94\n"
    "3:\n\ts_bitcmp0_b32 s60, s56\n\ts_cbranch_scc1 4f\n\t"
    "v_readlane_b32 s54, v78, %0\n\ts_add_u32 m0, s78, 0x10000\n\t"
    "s_lshr_b32 s53, s73, 16\n\ts_mulk_i32 s53, 3584\n\ts_mov_b64 s[48:49], s[80:81]\n\ts_add_u32 s50, s53, 3584\n\ts_mov_b32 s51, 0x20000\n\t"
    "v_lshlrev_b32 v121, 3, v84\n\tbuffer_load_dword v121, s[48:51], s53 offen lds\n\t"
    "s_bitcmp0_b32 s73, 3\n\ts_cbranch_scc1 4f\n\t"
    "v_lshlrev_b32 v121, 9, v84\n\tv_and_b32 v121, 0x38000, v121\n\tv_lshrrev_b32 v122, 9, v84\n\tv_lshl_add_u32 v121, v122, 12, v121\n\t"
    "v_lshrrev_b32 v122, 3, v84\n\tv_xor_b32 v122, v122, v84\n\tv_and_or_b32 v121, v122, 48, v121\n\t"
    "buffer_load_dwordx4 v121, s[88:91], s54 offen sc1 lds\n\ts_addk_i32 s54, 7168\n\t"
    "buffer_load_dwordx4 v121, s[88:91], s54 offen offset:1024 sc1 lds\n\ts_addk_i32 s54, 7168\n\t"
    "buffer_load_dwordx4 v121, s[88:91], s54 offen offset:2048 sc1 lds\n\ts_addk_i32 s54, 7168\n\t"
    "buffer_load_dwordx4 v121, s[88:91], s54 offen offset:3072 sc1 lds\n"
    "4:\n\ts_bitcmp0_b32 s61, s56\n\ts_cbranch_scc1 6f\n\t"
    "s_sub_u32 s74, s74, 64\n\ts_cmp_ge_i32 s74, 0\n\ts_cbranch_scc1 6f\n\t"
    "v_cmp_gt_i32 s[48:49], 0, v89\n\tv_cmp_gt_i32 s[50:51], 1, v89\n\tv_cmp_gt_i32 s[52:53], 2, v89\n\tv_cmp_gt_i32 s[54:55], 3, v89\n\t"
    "v_mov_b32 v122, 0xff800000\n\tv_mov_b32 v123, 0xff800000\n\t" NINF4(248)
    "s_cmp_eq_i32 s74, -32\n\ts_cbranch_scc0 5f\n\t" TRI(240) TRI(252) "s_setpc_b64 s[86:87]\n"
    "5:\n\t" NINF4(240) NINF4(244) NINF4(252) NINF4(232) TRI(224) TRI(236)
    "s_cmp_eq_i32 s74, -64\n\ts_cbranch_scc1 6f\n\t" NINF4(224) NINF4(228) NINF4(236)
    "6:\n\ts_setpc_b64 s[86:87]\n9:"
    ::"i"(T_ZO) : "scc");
}
DI void ev_call() { asm volatile("s_swappc_b64 s[86:87], s[82:83]" ::: "vcc", "scc", "memory"); GAP(); }

#define ACLOB_ALL "a0","a1","a2","a3","a4","a5","a6","a7","a8","a9","a10","a11","a12","a13","a14","a15","a16","a17","a18","a19","a20","a21","a22","a23","a24","a25","a26","a27","a28","a29","a30","a31","a32","a33","a34","a35","a36","a37","a38","a39","a40","a41","a42","a43","a44","a45","a46","a47","a48","a49","a50","a51","a52","a53","a54","a55","a56","a57","a58","a59","a60","a61","a62","a63","a64","a65","a66","a67","a68","a69","a70","a71","a72","a73","a74","a75","a76","a77","a78","a79","a80","a81","a82","a83","a84","a85","a86","a87","a88","a89","a90","a91","a92","a93","a94","a95","a96","a97","a98","a99","a100","a101","a102","a103","a104","a105","a106","a107","a108","a109","a110","a111","a112","a113","a114","a115","a116","a117","a118","a119","a120","a121","a122","a123","a124","a125","a126","a127","a128","a129","a130","a131","a132","a133","a134","a135","a136","a137","a138","a139","a140","a141","a142","a143","a144","a145","a146","a147","a148","a149","a150","a151","a152","a153","a154","a155","a156","a157","a158","a159","a160","a161","a162","a163","a164","a165","a166","a167","a168","a169","a170","a171","a172","a173","a174","a175","a176","a177","a178","a179","a180","a181","a182","a183","a184","a185","a186","a187","a188","a189","a190","a191","a192","a193","a194","a195","a196","a197","a198","a199","a200","a201","a202","a203","a204","a205","a206","a207","a208","a209","a210","a211","a212","a213","a214","a215","a216","a217","a218","a219","a220","a221","a222","a223","a224","a225","a226","a227","a228","a229","a230","a231","a232","a233","a234","a235","a236","a237","a238","a239","a240","a241","a242","a243","a244","a245","a246","a247","a248","a249","a250","a251","a252","a253","a254","a255"

struct EP { float fa[2], fb[2]; };
template <int I, int E, int MODE> DI void epi_pair(const EP& p) {
  constexpr int qb = I & 1, r = 16 * (I >> 1) + 4 * qb + (E >> 1) * 8 + (E & 1) * 2, t = 96 + 2 * E, w = 128 + 4 * I + E;
  if constexpr (MODE == 2) asm volatile("v_accvgpr_read_b32 v106, a[%c0]" ::"i"(w));
  asm volatile("v_accvgpr_read_b32 v%c0, a[%c2]\n\tv_accvgpr_read_b32 v%c1, a[%c3]" ::"i"(t), "i"(t + 1), "i"(r), "i"(r + 1));
  if constexpr (MODE) asm volatile("v_mul_f32 v%c0, v%c0, %2\n\tv_mul_f32 v%c1, v%c1, %2" ::"i"(t), "i"(t + 1), "v"(p.fa[qb]));
  if constexpr (MODE == 2)
    asm volatile("v_lshlrev_b32 v104, 16, v106\n\tv_and_b32 v105, 0xffff0000, v106\n\tv_fmac_f32 v%c0, v104, %2\n\tv_fmac_f32 v%c1, v105, %2"
                 ::"i"(t), "i"(t + 1), "v"(p.fb[qb]));
  asm volatile("v_cvt_pk_bf16_f32 v%c0, v%c1, v%c2" ::"i"(w), "i"(t), "i"(t + 1));
}
template <int I> DI void epi_store() {
  asm volatile("buffer_store_dwordx4 v[%c0:%c1], v84, s[88:91], s101 offen offset:%2 sc1" ::"i"(128 + 4 * I), "i"(131 + 4 * I), "i"((I & 3) * 1024) : "memory");
  if constexpr ((I & 3) == 3) asm volatile("s_addk_i32 s101, 4096");
}
constexpr int R_TB = 107, R_VS = 111;
template <int HF, int R> DI void p_read() {
  constexpr int mb = R >> 4, r = 64 * HF + 2 * R;
  asm volatile("ds_read_b64_tr_b16 a[%c1:%c2], v%c0 offset:%c3" ::"i"(R_TB + 2 * HF + mb), "i"(r), "i"(r + 1), "i"((R & 15) * 2048));
}
template <int QB, int E> DI void p_cvt() {
  asm volatile("v_cvt_pk_bf16_f32 v%c0, v%c1, v%c2" ::"i"(208 + 4 * QB + E), "i"(192 + 8 * QB + 2 * E), "i"(193 + 8 * QB + 2 * E));
}
template <int QB, int PR> DI void p_store() {
  asm volatile("buffer_store_dwordx4 v[%c0:%c1], v%c2, s[88:91], s96 offen offset:%3 sc1" ::"i"(208 + 4 * QB), "i"(211 + 4 * QB), "i"(R_VS + QB),
               "i"(PR * 128) : "memory");
}
constexpr int p_dma(int HF, int G) { return G == 3 && !HF ? 0 : G == 11 ? 1 - HF : G == 15 ? 2 - HF : G == 19 ? 3 - HF : G == 27 && HF ? 3 : -1; }
template <int HF, int G> DI void p_gap() {
  constexpr int ch = G >> 3, t = G & 7, a = 64 * HF + 32 * (ch & 1) + 4 * t, b = 128 + 4 * (2 * t + (ch >> 1)), d = 192 + 4 * ch;
  if constexpr (t == 0)
    asm volatile("v_mfma_f32_16x16x32_bf16 v[%c0:%c1], a[%c2:%c3], v[%c4:%c5], 0" ::"i"(d), "i"(d + 3), "i"(a), "i"(a + 3), "i"(b), "i"(b + 3));
  else
    asm volatile("v_mfma_f32_16x16x32_bf16 v[%c0:%c1], a[%c2:%c3], v[%c4:%c5], v[%c0:%c1]" ::"i"(d), "i"(d + 3), "i"(a), "i"(a + 3), "i"(b), "i"(b + 3));
  p_read<HF ^ 1, G>();
  if constexpr (G == 0) asm volatile("v_xor_b32 v%c0, s100, v%c0" ::"i"(R_TB + 2 * HF + 1));
  if constexpr (G == 17) asm volatile("v_xor_b32 v%c0, s100, v%c0" ::"i"(R_TB + 2 * (HF ^ 1)));
  if constexpr (G >= 4 && G <= 8 && G != 7) p_cvt<1, (G < 7 ? G - 4 : 3)>();
  if constexpr (G == 24 || (G == 7 && HF == 0)) asm volatile("s_waitcnt lgkmcnt(8)");
  if constexpr (G == 7 && HF == 1) asm volatile("s_waitcnt vmcnt(13) lgkmcnt(8)\n\ts_barrier" ::: "memory");
  if constexpr (G == 10) p_store<1, HF ^ 1>();
  if constexpr (G >= 20 && G < 24) p_cvt<0, G - 20>();
  if constexpr (G == 25) p_store<0, HF>();
  if constexpr (p_dma(HF, G) >= 0) dma_at<(HF ? 8 : 12) + p_dma(HF, G)>();
  if constexpr (HF == 0 && G == 2) asm volatile("s_bitset0_b32 m0, 12");
  if constexpr (HF == 0 && G == 13) asm volatile("s_add_u32 s96, s96, 0x40000");
  if constexpr (HF == 0 && G == 26) asm volatile("s_add_u32 s98, s98, 128");
  if constexpr (HF == 0 && G == 27) asm volatile("s_cmp_lt_u32 s98, 0x380");
  if constexpr (HF == 0 && G == 28) asm volatile("s_cselect_b32 s99, s99, 0x31000");
  if constexpr (HF == 1 && G == 1) asm volatile("s_xor_b32 s100, s100, 0x10000");
  if constexpr (HF == 1 && G == 2) asm volatile("s_xor_b32 s97, s97, s100");
  if constexpr (HF == 1 && G == 9) asm volatile("s_or_b32 m0, s97, s99");
  if constexpr (HF == 1 && G == 30) asm volatile("s_cmp_lt_u32 s98, 0x500");
  GAP();
}
template <int HF, int... Gs> DI void p_half(iseq<Gs...>) { (p_gap<HF, Gs>(), ...); }
constexpr int f_dma(int G) { return G == 2 ? 0 : G == 5 ? 1 : G == 8 ? 2 : G >= 12 && G <= 36 && G % 6 == 0 ? 1 + G / 6 : G >= 50 && G <= 68 && G % 6 == 2 ? G / 6 : -1; }
constexpr int f_ndma(int G) { int n = 0; for (int g = 0; g < G; ++g) n += f_dma(g) >= 0; return n; }
static_assert(f_ndma(74) == 12);
template <int MODE, int G> DI void gapF(AC& c, const EP& p) {
  if constexpr (MODE && G == 48) asm volatile("s_waitcnt lgkmcnt(6)\n\ts_barrier" ::: "memory");
  if constexpr (G < 64) gapB<0, false, G, !(MODE && G == 48)>(c);
  if constexpr (MODE && G >= 42 && G < 64) p_read<0, G - 42>();
  if constexpr (MODE && G >= 64 && G < 69) { p_read<0, 2 * G - 106>(); p_read<0, 2 * G - 105>(); }
  if constexpr (MODE && f_dma(G) >= 0) dma_at<8 + (f_dma(G) & 7)>();
  if constexpr (MODE == 2 && G >= 10 && (G - 10) % 16 == 0) asm volatile("s_waitcnt vmcnt(%0)" ::"i"(13 - (G - 10) / 4 + f_ndma(G + 1)) : "memory");
  if constexpr (G >= 10) epi_pair<(G - 10) / 4, (G - 10) & 3, MODE>(p);
  if constexpr (MODE == 3 && G < 64 && (G & 1)) {
    constexpr int db = G >> 2, qb = (G >> 1) & 1, o = 4 * (db * 2 + qb), a = 224 + 4 * (db & 1), b = 128 + 4 * ((db >> 1) * 2 + qb);
    if constexpr ((G & 7) == 1) asm volatile("s_waitcnt vmcnt(%0)" ::"i"(15 - (G >> 2) + f_ndma(G + 1)) : "memory");
    asm volatile("v_mfma_f32_16x16x32_bf16 a[%c0:%c1], v[%c2:%c3], a[%c4:%c5], a[%c0:%c1]" ::"i"(o), "i"(o + 3), "i"(a), "i"(a + 3), "i"(b), "i"(b + 3));
  }
  if constexpr (MODE == 0) {
    if constexpr (G >= 14 && ((G - 14) & 3) == 0) epi_store<(G - 14) / 4>();
    if constexpr (G == 73) epi_store<15>();
  } else {
    if constexpr (G == 0) asm volatile("s_or_b32 m0, s78, 0x1000\n\ts_movk_i32 s98, 256");
    if constexpr (G == 15) asm volatile("s_bitset0_b32 m0, 12");
    if constexpr (G == 48) asm volatile("s_or_b32 m0, s79, 0x1000\n\ts_movk_i32 s98, 384");
    if constexpr (G == 73) asm volatile("s_waitcnt vmcnt(4) lgkmcnt(0)" ::: "memory");
  }
  GAP();
}
template <int MODE, int... Gs> DI void phaseF(AC& c, const EP& p, iseq<Gs...>) { (gapF<MODE, Gs>(c, p), ...); }
template <int U, int K> DI void r_load(unsigned vo, unsigned so) {
  asm volatile("buffer_load_dwordx4 a[%c0:%c1], %2, s[88:91], %3 offen offset:%4 sc1" ::"i"(64 * U + 4 * K), "i"(64 * U + 4 * K + 3), "v"(vo),
               "s"(so + (K >> 2) * 4096), "i"((K & 1) * 512 + ((K >> 1) & 1) * 2048) : "memory");
}
template <int U, int K> DI void r_sum() {
  constexpr int d = 100 + 4 * (K & 1), b = 64 * U + 4 * K;
  if constexpr (K < 2) asm volatile("v_mfma_f32_16x16x32_bf16 v[%c0:%c1], v[96:99], a[%c2:%c3], 0" ::"i"(d), "i"(d + 3), "i"(b), "i"(b + 3));
  else asm volatile("v_mfma_f32_16x16x32_bf16 v[%c0:%c1], v[96:99], a[%c2:%c3], v[%c0:%c1]" ::"i"(d), "i"(d + 3), "i"(b), "i"(b + 3));
}
template <int U> DI void r_fin(unsigned vo, __amdgpu_buffer_rsrc_t srd, unsigned so) {
  asm volatile("" : "+s"(srd), "+s"(so));
  static_for([&](auto K) { r_sum<U, K>(); }, make_iseq<16>{});
  asm volatile("s_nop 7");
  static_for([&](auto E) { asm volatile("v_cvt_pk_bf16_f32 v%c0, v%c1, v%c2" ::"i"(116 + 4 * U + E), "i"(100 + 2 * E), "i"(101 + 2 * E)); }, make_iseq<4>{});
  asm volatile("buffer_store_dwordx4 v[%c3:%c4], %0, %1, %2 offen" OUTSCS ::"v"(vo), "s"(srd), "s"(so), "i"(116 + 4 * U), "i"(119 + 4 * U) : "memory");
}

#define CMAX 12
#define PREPE 0
static_assert(NYT <= 16 && NYT >= 12);
enum { T_Q0, T_K0, T_OQ, T_OG, T_FPREP, T_FQ, T_FG, T_FS,   T_CNT = 9, T_NPRE = 10, T_NST = 12, T_TAB = 14, T_SLAB = 16, T_PART, T_IT = 18, T_ITN = 23 };
struct TRow { unsigned v[64]; };
constexpr u64 kvmask(int m0, int m1) { u64 n = 0; for (int m = m0; m <= m1; ++m) n |= 0x101ull << m; return n; }
constexpr u64 k_order(bool isY, int rr) {
  const int cls = (rr + (rr >> 2)) & 3;
  return !isY ? (cls == 0 ? 0xfedcb9875a463210ull : cls == 1 ? 0xefadc6982b574310ull : cls == 2 ? 0xfebad7693c285410ull : 0xefcba8764d392510ull)
              : (cls == 0 ? 0xfedcb987543a6210ull : cls == 1 ? 0xefadc698254b7310ull : cls == 2 ? 0xfebad769325c8410ull : 0xefcba876432d9510ull);
}
constexpr TRow t_row(int r, int wid, int j) {
  TRow t{};
  const int rr = r & 15, hsel = rr & 1, pj = rr >> 1, kv = pj & 1, h = j + 8 * hsel, u = hsel * 8 + j;
  const bool isY = r >= 16, solo = pj == 7;
  const int mt = (rr >> 2) + (isY ? 4 : 0), g = j * 128, NY = solo ? 18 : NYT;
  const int nx0 = 32 - 2 * pj - NY, cx = nx0 < CMAX ? nx0 : CMAX, ex = nx0 - cx, qbF = isY != (solo && SWAP7) ? 15 - pj : pj;
  const unsigned oQ = (unsigned)h << 20, oK = oQ + O_K, oP = O_P + (h * 8 + pj) * PSTR;
  if (wid == 4) {
    for (int l = 0; l < 64; ++l)
      t.v[l] = fo(l < 16 ? g + hsel * 16 + l : l < 24 ? g + 16 + hsel * 8 + l : l == 24 ? g + 48 + hsel * 8 + pj : l < 32 ? g + 64 + hsel * 8 + pj :
                  l < 48 ? 2304 + pj * 16 + l - 32 : 2048 + pj * 16 + l - 48);
    return t;
  }
  auto put64 = [&](int i, u64 x) { t.v[i] = (unsigned)x; t.v[i + 1] = (unsigned)(x >> 32); };
  t.v[T_Q0] = oQ + ((solo ? qbF : 15 - pj) * 128 + wid * 32) * 512;
  t.v[T_K0] = oK + (isY ? cx : 0) * 32768;
  t.v[T_OQ] = oQ + pj * 131072 + (wid >> 1) * 65536 + (wid & 1) * 4096;
  t.v[T_OG] = oK + kv * TSB + mt * 131072 + (wid >> 1) * 65536 + (wid & 1) * (kv ? 8192 : 4096);
  t.v[T_FPREP] = PREPE ? fo(2048 + pj * 16 + (isY * 2 + hsel) * 4) + (j - PREPE + 1) * 4 : fo(2048 + pj * 16 + u);
  t.v[T_FQ] = fo(g + 32 + hsel * 8 + pj);
  t.v[T_FG] = fo(g + hsel * 16 + kv * 8 + mt);
  t.v[T_FS] = fo(1024 + qbF * 16 + j * 2 + hsel);
  t.v[T_CNT] = fo(2560 + qbF * 16);
  t.v[T_ZO] = O_Z + pj * 64 * SEQ * 2 + (u * 4 + wid) * 64;
  const u64 own = 0x1ull << (kv * 8 + mt), needQ = isY ? 1ull << (16 + (qbF >> 1)) : (1ull << (16 + (pj >> 1))) | (1ull << (16 + ((15 - pj) >> 1)));
  put64(T_NPRE, isY ? needQ | (1ull << (cx >> 2)) | (1ull << ((cx + 1) >> 2)) : solo ? needQ : needQ | 0xffffull << 48);
  put64(T_NST, isY ? kvmask(cx >> 2, 3) | (solo ? 0 : 1ull << 25) : kvmask(0, solo ? 3 : 0));
  put64(T_TAB, k_order(isY, rr));
  t.v[T_SLAB] = O_S + qbF * 0x200000 + wid * 65536 + (j * 2 + hsel) * 1024;
  t.v[T_PART] = oP;
  for (int it = 0; it < 2; ++it) {
    unsigned* p = t.v + T_IT + it * T_ITN;
    const bool last = isY || it == 1;
    const int n = solo ? 2 * qbF + 2 : isY ? NY : it ? 2 * pj + 2 : nx0;
    const u64 need2 = solo ? 0 : kvmask(0, (2 * pj + 1) >> 2) | (0xffffull << 32);
    u64 q[3] = {0, 0, 0};
    unsigned jmp[2] = {0, 0};
    if (isY) {
      p[0] = (solo && SWAP7 ? 0 : 1u << (14 - cx)) | (solo ? 0 : 1u << (n - 2));
      q[0] = kvmask(4, (31 - 2 * pj) >> 2) & ~own; jmp[0] = ex * 32768; q[1] = solo ? 0 : 1ull << 24;
      p[1] = 2; p[13] = t.v[T_FG]; p[14] = fo(g + 96 + r);
    } else if (it == 0) {
      p[0] = 2u | (ex ? 1u << (cx - 2) : 0);
      q[0] = kvmask(0, cx > 0 ? (cx - 1) >> 2 : 0);
      if (ex) { q[1] = kvmask(4, 4 + ((ex - 1) >> 2)); jmp[1] = (16 - cx) * 32768; q[2] = need2; } else q[1] = need2;
      p[1] = 2; p[13] = fo(2304 + pj * 16 + u) + j * (8 * 4352 - 8); p[14] = fo(g + 64 + hsel * 8 + pj);
    } else {
      p[0] = p[1] = solo ? 0 : 1;
      q[0] = need2;
      if (solo && SWAP7) { p[0] = 1u << 14; q[0] = kvmask(4, 4); } p[13] = fo(g + 48 + hsel * 8 + pj); p[14] = fo(g + 96 + r);
    }
    p[2] = 1u << (n - 2);
    p[3] = !last ? 0 : n > 2 ? 1u << (n - 3) : 1u;
    p[4] = !last ? 0 : n > 2 ? 3u << (n - 3) : 1u << 31;
    for (int k = 0; k < 3; ++k) { p[5 + 2 * k] = (unsigned)q[k]; p[6 + 2 * k] = (unsigned)(q[k] >> 32); }
    p[11] = jmp[0]; p[12] = jmp[1];
    p[15] = oK;
    p[16] = (last ? 1 : 0) | (isY && !solo ? 2 : 0) | (n == 2 ? 4 : 0) | (isY || solo ? 0 : 8) | (n - 1) << 8 | ((r >> 1) * 4 + wid) << 16;
    p[17] = wid * 32 - (n == 2 ? 64 : 0);
    p[18] = oQ + (pj * 128 + wid * 32) * 512;
    p[19] = 0;
    p[20] = (p[0] | (last ? 0 : 1u << n)) >> 1;
    p[21] = oK + (isY ? cx : 0) * 32768;
    p[22] = ((p[0] | p[1] | p[2] | p[3] | p[4]) & 0x7fffffffu) << 1;
  }
  return t;
}
template <int I> constexpr TRow t_base = I < 160 ? t_row(I / 5, I % 5, 0) : [] {
  TRow a = t_row(0, (I - 160) * 4, 0), b = t_row(0, (I - 160) * 4, 1);
  for (int l = 0; l < 64; ++l) a.v[l] = b.v[l] - a.v[l];
  return a;
}();
template <int I> constexpr bool t_linear = [] {
  for (int j = 1; j < 8; ++j) {
    const TRow b = t_row(I / 5, I % 5, j);
    for (int l = 0; l < 64; ++l) if (b.v[l] != t_base<I>.v[l] + j * t_base<160 + (I % 5) / 4>.v[l]) return false;
  }
  return true;
}();
struct Tab { TRow row[162]; };
template <int... I> constexpr Tab t_make(iseq<I...>) {
  static_assert((t_linear<(I < 160 ? I : 0)> && ...));
  return Tab{{t_base<I>...}};
}
__device__ const Tab g_tab = t_make(make_iseq<162>{});

template <int R> DI const bf16* sptr() { const bf16* p; asm volatile("s_mov_b64 %0, s[%c1:%c2]" : "=s"(p) : "i"(R), "i"(R + 1)); return p; }
template <class T, int I> DI T* arg_late() {
  unsigned lo, hi;
  asm volatile("v_readlane_b32 %0, v77, %2\n\tv_readlane_b32 %1, v77, %3\n\ts_nop 4" : "=s"(lo), "=s"(hi) : "i"(2 * I), "i"(2 * I + 1));
  return (T*)(((u64)hi << 32) | lo);
}
__global__ __launch_bounds__(256, 1) __attribute__((amdgpu_num_vgpr(77), amdgpu_num_sgpr(56))) void fused_kernel(
    const bf16* ckv_, const bf16* wuk_, const bf16* wuv_, const bf16* x_, const bf16* wq_, bf16* ws_, const bf16* ak_, const bf16* av_,
    const bf16* ash_, const bf16* assmk_, const bf16* assmD_, const bf16* wo_, const bf16* aq_, bf16* out_) {
  extern __shared__ __attribute__((aligned(16))) char lds[];
  const void* kap = (const void*)__builtin_amdgcn_kernarg_segment_ptr();
  asm volatile("at_code_begin:\n\ts_load_dwordx16 s[80:95], %0, 0\n\ts_load_dwordx2 s[96:97], %0, 0x40" ::"s"(kap) : ACLOB_ALL, "v255", "s101");
  asm volatile("s_getpc_b64 s[48:49]\nct_base:\n\ts_mov_b32 s50, pf_code_end-ct_base\n\ts_mov_b32 s51, 0x20000\n\ts_lshr_b32 s52, %1, 3\n\t"
               "v_and_b32 v116, 63, %0\n\tv_lshrrev_b32 v117, 6, %0\n\tv_lshl_add_u32 v116, v116, 7, v117\n\ts_lshl_b32 s52, s52, 9\n\t"
               "v_lshlrev_b32 v116, 7, v116\n\tbuffer_load_dword v117, v116, s[48:51], s52 offen" ::"v"(threadIdx.x), "s"(blockIdx.x) : "scc", "memory");
  GAP();
  const int tid = threadIdx.x, lane = tid & 63;
  const int wid = __builtin_amdgcn_readfirstlane(tid >> 6);
  const int b = blockIdx.x, j = b & 7, r = b >> 3, rr = r & 15, hsel = rr & 1, pj = rr >> 1, kv = pj & 1;
  const bool isY = r >= 16, solo = pj == 7, doP = (PREPE ? (j & 1) == PREPE - 1 : isY) && !solo, doA = !isY && !solo;
  const int h = j + 8 * hsel, mt = (rr >> 2) + (isY ? 4 : 0), pu = (hsel * 8 + j) * 4 + wid;
  constexpr unsigned m0d = 131072 + 24576;
  const unsigned lA = isY ? 65536 : 0, lW = lA ^ 65536, po = isY ? 0 : 32768;
  unsigned mA = lA + wid * 1024 + po, mW = lW + wid * 1024 + po, ofA = isY ? pj * 256 * DA * 2 : mt * 256 * DC * 2, ofW = h * 512;
  const unsigned lda = isY ? DA : DC;
#define ROT 1
  unsigned g0 = (!ROT ? 0 : isY ? pj + 2 * hsel : rr + (rr >> 2)) & 3;
  {
    const unsigned* tp = g_tab.row[r * 5].v;
    asm volatile("global_load_dword v78, %0, %2\n\tglobal_load_dword v79, %1, %2 offset:1024\n\t"
                 "global_load_dword v118, %1, %3\n\tglobal_load_dword v119, %1, %3 offset:256\n\tglobal_load_dword v77, %4, %5"
                 ::"v"(tid * 4), "v"(lane * 4), "s"(tp), "s"(g_tab.row[160].v), "v"((lane < 27 ? lane : 27) * 4), "s"(kap) : "memory");
    const unsigned ra = wid * 8 + (lane >> 3), sa = ((lane & 7) ^ (((wid & 1) * 4 + (lane >> 4)) & 7)) * 16;
    const unsigned vbA = ra * (lda * 2) + sa, vbB = (4 * wid + (lane >> 4)) * LDW * 2 + 16 * ((lane & 15) ^ (((lane >> 4) << 2) | wid));
    vsetu<92>(vbA); vsetu<93>(vbB); vsetu<94>(ra * (DC * 2) + sa);
    sset<64>(64 * lda); sset<67>(256 * lda); sset<68>(320 * lda);
    asm volatile("s_mov_b32 s71, 0x20000\n\ts_movk_i32 s72, 0x100\n\ts_mov_b32 s73, 0x20100");
    sset<58>(256 * lda * 2); sset<62>((lda - 1) * LDW * 2 + 512);
    asm volatile("s_mov_b32 s59, 0x20000\n\ts_mov_b32 s63, 0x20000");
    g_pro<1>(g0, mA, mW, lda);
    asm volatile("" : "+s"(ofA), "+s"(ofW), "+s"(mA), "+s"(mW), "+s"(g0));
    GAP();
  }
  asm volatile("s_bitcmp1_b32 %0, 4\n\ts_waitcnt lgkmcnt(0)\n\ts_cselect_b64 s[60:61], s[84:85], s[82:83]\n\ts_bitcmp1_b32 %0, 7\n\t"
               "s_cselect_b64 s[60:61], s[88:89], s[60:61]\n\ts_cselect_b64 s[56:57], s[86:87], s[80:81]\n\t"
               "s_add_u32 s56, s56, %1\n\ts_addc_u32 s57, s57, 0\n\ts_add_u32 s60, s60, %2\n\ts_addc_u32 s61, s61, 0\n\t"
               "s_mov_b64 s[88:89], s[90:91]\n\ts_mov_b32 s90, %3\n\ts_mov_b32 s91, 0x20000" ::"s"(b), "s"(ofA), "s"(ofW), "i"(WSB) : "scc");
  KST(0, 0);
  {
    GM g;
    PR pr;
    const int ptb = PREPE ? (isY * 2 + hsel) * 4 + (j >> 1) : hsel * 8 + j;
    if (doP) prep_load(pr, tid, pj, ptb, sptr<92>(), sptr<94>(), sptr<96>());
    g_pro<2>(g0, mA, mW, lda);
    asm volatile("buffer_load_dwordx2 v[120:121], off, s[88:91], %0 sc1" ::"s"(fo(6144)) : "memory");
    if (doP) {
      asm volatile("s_waitcnt vmcnt(9)" ::: "memory");
#pragma unroll
      for (int q = 0; q < 7; ++q) asm volatile("" : "+v"(pr.kk[q]));
#pragma unroll
      for (int q = 0; q < 4; ++q) asm volatile("" : "+v"(pr.vv[q]));
      static_for([&](auto Ic) __attribute__((always_inline)) {
        constexpr int I = Ic;
        if constexpr (I < 3) g_pro(g0 + 1 + I, mA, mW, lda);
        GAP();
        prep_math<I>(pr, tid, lds + 131072);
        GAP();
      }, make_iseq<4>{});
      asm volatile("s_waitcnt lgkmcnt(0)\n\ts_barrier" ::: "memory");
      prep_store(tid, pj, ptb, (bf16*)sptr<88>() + O_X / 2, lds + 131072);
    }
    GAP();
    {
      const int fr = lane & 15, fq = lane >> 4, q = (lane & 15) >> 2, p = lane & 3;
#pragma unroll
      for (int k2 = 0; k2 < 2; ++k2)
        g.aA[k2] = lA + (wid >> 1) * 16384 + (fr >> 3) * 1024 + (fr & 7) * 128 + (((4 * k2 + fq) ^ (fr >> 1)) * 16);
#pragma unroll
      for (int hf = 0; hf < 2; ++hf)
#pragma unroll
        for (int nf = 0; nf < 8; ++nf)
          g.aB[hf][nf] = lW + (wid & 1) * 8192 + off_b(8 * fq + 4 * hf + q, nf * 2 + (p >> 1)) + 8 * (p & 1);
      asm volatile("" : "+v"(g.aA[0]), "+v"(g.aA[1]));
#pragma unroll
      for (int nf = 0; nf < 8; ++nf) asm volatile("" : "+v"(g.aB[0][nf]), "+v"(g.aB[1][nf]));
      vsetu<81>(tid == 0 ? 0u : 0x80000000u);
      vsetu<84>(lane * 16); vsetu<85>(lane * 16 + 65536); vsetu<86>(wid * 8192 + lane * 16); vsetu<87>(wid * 8192 + lane * 16 + 4096);
      vsetu<88>(65536 + wid * 256 + fr * 16); vsetu<89>(fr - 4 * fq);
      asm volatile("s_mov_b64 s[92:93], s[88:89]\n\ts_mov_b64 s[94:95], s[90:91]\n\ts_mov_b32 s74, 0x40000\n\ts_mov_b32 s75, 0x60000\n\t"
                   "s_mov_b32 s76, 0x40100\n\ts_mov_b32 s77, 0x60100");
      sset<65>(128 * lda); sset<66>(192 * lda); sset<69>(384 * lda); sset<70>(448 * lda);
      sset<78>(lA + wid * 1024); sset<79>(lW + wid * 1024);
    }
    if (doP) asm volatile("s_waitcnt vmcnt(4)" ::: "memory");
    else {
      g_pro(g0 + 1, mA, mW, lda); g_pro(g0 + 2, mA, mW, lda); g_pro(g0 + 3, mA, mW, lda);
      asm volatile("s_waitcnt vmcnt(0)" ::: "memory");
    }
    asm volatile("s_barrier" ::: "memory");
    if (isY) g_read0<0>(g, make_iseq<16>{});
    else g_read0<1>(g, make_iseq<16>{});
    GAP();
    {
      u64 z = (((u64)__float_as_uint(vget<121>()) << 32) | __float_as_uint(vget<120>())) + 0x9e3779b97f4a7c15ull;
      z = (z ^ (z >> 30)) * 0xbf58476d1ce4e5b9ull; z = (z ^ (z >> 27)) * 0x94d049bb133111ebull; z ^= z >> 31;
      vsetu<82>((unsigned)z); vsetu<83>((unsigned)(z >> 32));
      asm volatile("v_mad_u32_u24 v78, v118, %0, v78\n\tv_mad_u32_u24 v79, v119, %0, v79\n\tv_readfirstlane_b32 s84, v82\n\tv_readfirstlane_b32 s85, v83\n\t"
                   "v_readlane_b32 s80, v78, %1\n\tv_readlane_b32 s81, v78, %2" ::"s"(j), "i"((int)T_TAB), "i"(T_TAB + 1));
      if (isY) asm volatile("s_mov_b64 s[80:81], 0x6543210");
    }
    unsigned k2;
    asm volatile("s_bfe_u32 s82, s80, 0x40008\n\ts_lshr_b64 s[80:81], s[80:81], 12\n\ts_lshl_b32 %0, s82, 1\n\tv_add_u32 v90, 128, v92" : "=s"(k2) :: "scc");
    asm volatile("s_waitcnt lgkmcnt(0)\n\ts_barrier" ::: "memory");
    g_proW(k2, mW);
    GAP();
    if (isY) {
      g_tile<false, 0, 1, NTW >= 1>(g);
      g_loop<false, true>(g, doP ? 0x22u : 0x20u, [&](int t) __attribute__((always_inline)) {
        if (t == 1) fl_set<T_FPREP>();
        else if (t == 4) { tload<80, T_TAB>(); tload<81, T_TAB + 1>(); srd_set<60>((kv ? arg_late<const bf16, 2>() : arg_late<const bf16, 1>()) + h * 256, (DC - 1) * LDW * 2 + 512); }
        else {
          srd_set<56>(arg_late<const bf16, 0>() + (size_t)mt * 256 * DC, 256 * DC * 2);
          asm volatile("v_mov_b32 v92, v94");
          static_for([&](auto J) { asm volatile("s_mov_b32 s%c0, %1" ::"i"(64 + J), "i"((J + 1) * 64 * DC)); }, make_iseq<7>{});
        }
      });
      tload<101, T_OQ>(); sset<83>(__float_as_uint(CS));
      asm volatile("v_mov_b32 v112, v84\n\tv_add_u32 v113, 8192, v84");
      GAP();
      g_tile<false, 0, 2, NTW >= 2>(g);
      KST(0, 3);
    }
#define TT 3
    auto kvtile = [&](auto TRc) __attribute__((always_inline)) {
      constexpr bool TR = TRc;
      if (isY) g_tile<TR, 1, 4, NTW >= 2>(g);
      else g_tile<TR, 1, 1, NTW >= 2>(g);
      g_loop<TR, false>(g, 0x2000u | (isY || doA ? 2u : 1u << TT), [&](int t) __attribute__((always_inline)) {
        int l = threadIdx.x & 63; asm volatile("" : "+v"(l));
        if (t == 13) {
          asm volatile("s_mov_b32 s79, 0x28000");
          fl_poll<T_NPRE>();
          if ((b & 15) == 0)
            asm volatile("v_readlane_b32 s48, v78, %0\n\tv_and_b32 v122, 0xffffff00, v82\n\tv_mov_b32 v123, v83\n\ts_nop 2\n\t"
                         "buffer_store_dwordx2 v[122:123], v81, s[88:91], s48 offen sc1" ::"i"((int)T_CNT) : "memory");
          if (!doA && !isY) {
            CODE_TOUCH("at_code_begin", "pf_code_end", hsel * 8 + wid * 2, 16u, 64, m0d);
            CODE_TOUCH("at_code_begin", "pf_code_end", hsel * 8 + wid * 2 + 1, 16u, 64, m0d);
          }
          if (doA) {
            const char* tp = arg_late<const char, 9>() + (size_t)(pj * 64 + pu) * (SEQ * 2) + (l & 31) * 128;
            asm volatile("s_mov_b32 m0, %1\n\ts_nop 0\n\tglobal_load_lds_dword %0, off" ::"v"(tp), "i"(m0d) : "memory");
          }
        } else if (isY) fl_set<T_FQ>();
        else if (doA) { if (doP) fl_set<T_FPREP>(); post_dma(pj, pu, arg_late<const bf16, 12>(), 131072 + wid * 8192); }
        else {
          const char* tp = arg_late<const char, 0>() + 1024 * DC * 2 + (pu * 256 + l) * 128;
          asm volatile("s_mov_b32 m0, %1\n\ts_nop 0\n\tglobal_load_lds_dword %0, off" ::"v"(tp), "i"(m0d) : "memory");
          asm volatile("global_load_lds_dword %0, off" ::"v"(tp + 8192) : "memory");
          asm volatile("global_load_lds_dword %0, off" ::"v"(tp + 16384) : "memory");
          asm volatile("global_load_lds_dword %0, off" ::"v"(tp + 24576) : "memory");
        }
      });
      tload<101, T_OG>(); tload<96, T_Q0>(); tload<98, T_K0>();
      fl_check();
      tload<50, T_NST>(); tload<51, T_NST + 1>();
      asm volatile("v_mov_b32 v112, v84\n\tv_add_u32 v113, %0, v84\n\tv_mov_b32 v90, v87\n\tv_mov_b32 v91, v87\n\tv_mov_b32 v92, v87\n\tv_mov_b32 v93, v87\n\t"
                   "v_mov_b32 v94, v86\n\tv_mov_b32 v95, v86\n\tv_mov_b32 v126, v86\n\tv_mov_b32 v127, v86\n\ts_mov_b32 s99, 0x8000\n\t" FL_ARM("s[50:51]")
                   ::"i"(TR ? 4096 : 8192));
      if (doA) {
        const int d = pj * 64 + pu;
        const bf16* dp = arg_late<const bf16, 10>() + d;
        asm volatile("global_load_ushort v123, %0, off" ::"v"(dp) : "memory");
        srd_set<56>(arg_late<const bf16, 9>() + (size_t)d * SEQ, SEQ * 2);
        srd_set<60>(arg_late<const char, 5>() + O_X + d * SEQ * 2, SEQ * 2);
        sset<78>(65536 + wid * 15488);
        asm volatile("s_movk_i32 s64, 992\n\tv_subrev_u32 v122, 32, v84");
        GAP();
        g_tile<TR, 0, 3>(g);
      } else {
        sset<79>(isY ? wid * 8192 : 0x30000);
        if ((NYT & 1) && !solo) asm volatile("s_add_u32 s98, s98, 0x8000\n\ts_mov_b32 s97, 0xffff8000" ::: "scc");
        else asm volatile("s_mov_b32 s97, 0x8000");
        GAP();
        g_tile<TR, 0, 5>(g);
      }
    };
    if (kv) kvtile(std::true_type{});
    else kvtile(std::false_type{});
  }
  KST(0, 4);
  asm volatile("s_waitcnt vmcnt(16)" ::: "memory");
  sset<79>(wid * 8192);
  if (doA) {
    const float Dd = bfl(__float_as_uint(vget<123>()));
    KST(0, 5);
    conv_wave<T_FG>(O_Z + (pj * 64 + pu) * SEQ * 2, Dd, lds + 65536 + wid * 15488);
    const char* tp = arg_late<const char, 11>() + (size_t)h * (HD * DA * 2) + ((pj * 4 + wid) * 64 + lane) * 128;
    asm volatile("s_mov_b32 m0, %1\n\ts_nop 0\n\tglobal_load_lds_dword %0, off\n\ts_waitcnt vmcnt(17)" ::"v"(tp), "i"(m0d) : "memory");
    KST(0, 6);
  } else if (!isY) {
    static_for([&](auto I) { d_store<16 + I>(); }, make_iseq<16>{});
    tload<98, T_K0>();
    asm volatile("s_waitcnt vmcnt(0)\n\ts_barrier" ::: "memory");
    fl_set<T_FG>();
    KST(0, 5);
    fl_check();
    CODE_TOUCH("att_begin", "pf_code_end", hsel * 4 + wid, 8u, 64, m0d);
    dma4k<0>(); dma4k<1>(); dma4k<2>(); dma4k<3>();
    asm volatile("s_waitcnt vmcnt(0)" ::: "memory");
    KST(0, 6);
  } else {
    tload<76, T_PART>();
    fl_check();
    asm volatile("buffer_load_dwordx2 v[122:123], v88, s[88:91], s76 offen offset:1024 sc1" ::: "memory");
  }
  AC c;
  sset<78>(65536 + wid * 8192); sset<79>(98304 + wid * 8192);
  { const bf16* wp = arg_late<const bf16, 11>() + (size_t)h * HD * DA; asm volatile("s_mov_b64 s[80:81], %0" ::"s"(wp)); }
  asm volatile("att_begin:");
  ev_code();
  asm volatile("s_waitcnt lgkmcnt(0)\n\ts_barrier" ::: "memory");
#pragma unroll 1
  for (int it = doA ? 0 : 1; it < 2; ++it) {
    const bool odd = (NYT & 1) && !solo && (isY || it == 0);
    if (odd) kreads<1>(make_iseq<8>{});
    else kreads<0>(make_iseq<8>{});
    const unsigned oP = tget<T_PART>();
    if (it == 0) {
      static_for([&](auto I) { tload<57 + I, T_IT + I>(); }, make_iseq<21>{});
      tload<S_T, T_IT + 21>(); tload<S_EV, T_IT + 22>();
      asm volatile("s_sub_u32 s70, s70, %0\n\ts_add_u32 s76, %1, 1024" ::"s"(j * (8 * 4352 - 8)), "s"(oP) : "scc");
    } else {
      static_for([&](auto I) { tload<57 + I, T_IT + T_ITN + I>(); }, make_iseq<21>{});
      tload<S_T, T_IT + T_ITN + 21>(); tload<S_EV, T_IT + T_ITN + 22>();
      asm volatile("s_mov_b32 s72, 0\n\ts_mov_b32 s75, %0\n\ts_mov_b32 s76, %1" ::"s"(oP + wid * 16384), "s"(oP + 1032));
    }
    unsigned mode;
    asm volatile("s_add_u32 s98, s97, 0x8000\n\ts_mov_b32 %0, s73\n\ts_mov_b32 s56, -1\n\tv_mov_b64 v[114:115], 0\n\tv_mov_b64 v[118:119], 0\n\t"
                 "v_mov_b32 v120, 0x45800000" : "=s"(mode) :: "scc");
    KST(0, 8 + 3 * (it && !isY));
    GAP();
#define STA(k)
#define STI()
    STA(0);
    if (odd) {
      if (isY) phaseA<1, 2>(c);
      else phaseA<1, 1>(c);
      STA(1);
      STA(2);
      phaseB0<1>(mode, wid);
      STA(3);
      asm volatile("s_bfe_u32 s76, s73, 0x80008\n\ts_branch att_mid" ::: "scc");
    } else {
      if (isY) phaseA<0, 2>(c);
      else phaseA<0, 1>(c);
      STA(1);
      if (c.rare) ev_call();
      STA(2);
      phaseB0<0>(mode, wid);
      if (__builtin_expect(mode & 4, 0)) { asm volatile("s_mov_b32 s56, 31"); ev_call(); asm volatile("s_mov_b32 s56, 0"); }
      STA(3);
    }
    GAP();
    asm volatile("s_bfe_u32 s76, s73, 0x80008\natt_loop:" ::: "scc");
    GAP();
    phaseA<1, 0>(c);
    STI();
    if (__builtin_expect(c.rare != 0, 0)) { if (redo()) sm_redo<1>(); ev_call(); }
    phaseB<1>(c);
    asm volatile("s_cbranch_scc1 att_done\natt_mid:");
    phaseA<0, 0>(c);
    STI();
    if (__builtin_expect(c.rare != 0, 0)) { if (redo()) sm_redo<0>(); ev_call(); }
    phaseB<0>(c);
    asm volatile("s_branch att_loop\natt_done:");
    GAP();
    STA(4);
    if ((mode & 3) == 1) phaseA<0, 4>(c);
    else {
      asm volatile("buffer_load_dwordx4 v[128:131], v88, s[88:91], %0 offen sc1" ::"s"(oP) : "memory");
      GAP();
      phaseA<0, 3>(c);
    }
    if (__builtin_expect(redo(), 0)) sm_redo<0>();
    STA(5);
    KST(0, 9 + 3 * (it && !isY));
    const float l[2] = {row_reduce<false>(vget<R_RS>() + vget<R_TS>()), row_reduce<false>(vget<R_RS + 1>() + vget<R_TS + 1>())};
    const float cm[2] = {vget<R_M>(), vget<R_M + 1>()};
    EP p;
    if (it == 0) {
      const f32x4 ml = {cm[0], l[0], cm[1], l[1]};
      asm volatile("buffer_store_dwordx4 %0, v88, s[88:91], %1 offen sc1\n\ts_mov_b32 s101, %2" ::"v"(ml), "s"(oP), "s"(oP + wid * 16384) : "memory");
      phaseF<0>(c, p, make_iseq<74>{});
      asm volatile(FL_POLL "\n\ts_waitcnt vmcnt(17)" ::: "memory");
    } else {
      int lz = threadIdx.x & 63; asm volatile("" : "+v"(lz));
      const int rw = lz >> 2;
      static_for([&](auto E) { vsetu<R_TB + E>(rw * 128 + 16 * ((2 * E + ((lz & 3) >> 1)) ^ (rw & 6)) + 8 * (lz & 1)); }, make_iseq<4>{});
      const unsigned vs = ((lz >> 3) & 1) * 16384 + ((lz >> 4) & 1) * 512 + (lz >> 5) * 256 + (lz & 7) * 16;
      vsetu<R_VS>(vs); vsetu<R_VS + 1>(vs + 32768);
      tload<96, T_SLAB>();
      asm volatile(TICKET ::"i"((int)T_CNT) : "memory");
      asm volatile("s_mov_b32 s97, s79\n\ts_mov_b32 s100, 0x8000\n\ts_movk_i32 s99, 0x1000");
      if (!(mode & 2)) {
        p.fa[0] = __builtin_amdgcn_rcpf(l[0]); p.fa[1] = __builtin_amdgcn_rcpf(l[1]);
        phaseF<1>(c, p, make_iseq<74>{});
      } else {
        const f32x4 ml = {vget<128>(), vget<129>(), vget<130>(), vget<131>()};
        if (__builtin_expect(__builtin_amdgcn_ballot_w64(ml[0] != cm[0] || ml[2] != cm[1]) == 0, 1)) {
          p.fa[0] = __builtin_amdgcn_rcpf(l[0] + ml[1]); p.fa[1] = __builtin_amdgcn_rcpf(l[1] + ml[3]);
          const unsigned one = (lz >> 4) != ((lz >> 2) & 3) ? 0u : (lz & 1) ? 0x3f800000u : 0x3f80u, o0 = (lz & 2) ? 0u : one, o1 = (lz & 2) ? one : 0u;
          vsetu<224>(o0); vsetu<225>(o1); vsetu<230>(o0); vsetu<231>(o1);
          asm volatile("v_mov_b64 v[226:227], 0\n\tv_mov_b64 v[228:229], 0");
          phaseF<3>(c, p, make_iseq<74>{});
        } else {
#pragma unroll
          for (int e = 0; e < 2; ++e) {
            const float mm = fmaxf(cm[e], ml[2 * e]);
            const float wa = __builtin_amdgcn_exp2f(cm[e] - mm), wb = __builtin_amdgcn_exp2f(ml[2 * e] - mm);
            const float inv = __builtin_amdgcn_rcpf(l[e] * wa + ml[2 * e + 1] * wb);
            p.fa[e] = wa * inv; p.fb[e] = wb * inv;
          }
          phaseF<2>(c, p, make_iseq<74>{});
        }
      }
    }
    KST(0, 10 + 3 * (it && !isY));
  }
  asm volatile("v_xor_b32 v%c0, s100, v%c0\np_loop:" ::"i"(R_TB));
  GAP();
  p_half<0>(make_iseq<32>{});
  p_half<1>(make_iseq<32>{});
  asm volatile("s_cbranch_scc1 p_loop\n\ts_nop 5");
  GAP();
  p_cvt<1, 0>(); p_cvt<1, 1>(); p_cvt<1, 2>(); p_cvt<1, 3>();
  p_store<1, 1>();
  GAP();
  KST(0, 27);
  {
    constexpr unsigned rdy = 98304;
    int lz = threadIdx.x & 63; asm volatile("" : "+v"(lz));
    const int fr = lz & 15, fq = lz >> 4, qbF = isY != (solo && SWAP7) ? 15 - pj : pj;
    if (wid == 0) {
#pragma unroll 1
      for (int q = 0; q < 0x100000; ++q) {
        unsigned lo, hi, tl, th;
        asm volatile("v_readfirstlane_b32 %0, v122\n\tv_readfirstlane_b32 %1, v123\n\ts_mov_b32 %2, s84\n\ts_mov_b32 %3, s85" : "=s"(lo), "=s"(hi), "=s"(tl), "=s"(th));
        if (__builtin_expect((((lo ^ tl) >> 8) | (hi ^ th)) == 0, 1)) break;
        asm volatile("s_sleep 4\n\t" TICKET "\n\ts_waitcnt vmcnt(0)" ::"i"((int)T_CNT) : "memory");
      }
      asm volatile("v_mov_b32 v123, 0\n\ts_mov_b64 exec, 1\n\tds_write_b64 %0, v[122:123]\n\ts_mov_b64 exec, -1" ::"v"(rdy) : "memory");
    }
    vsetu<79>(fo(1024) + qbF * 4352 + fr * 8);
    {
      const int jj = 4 * (fr >> 3) + (fr & 3);
      const unsigned one = ((fq ^ (fr >> 2)) & 1) ? 0u : (jj & 1) ? 0x3f800000u : 0x3f80u;
      static_for([&](auto I) { vsetu<96 + I>((jj >> 1) == I ? one : 0u); }, make_iseq<4>{});
    }
    unsigned vo = (fq >> 1) * 1024 + (fq & 1) * 256 + fr * 16;
    bf16* out = arg_late<bf16, 13>();
    __amdgpu_buffer_rsrc_t so_ = __builtin_amdgcn_make_buffer_rsrc((void*)out, (short)0, SEQ * OUTW * 2, 0x00020000);
    unsigned vw = ((qbF * 128 + (fr & 7)) * OUTW + DA + (fr >> 3) * 32 + fq * 8) * 2;
    asm volatile("" : "+s"(so_));
    asm volatile("s_waitcnt vmcnt(0) lgkmcnt(0)\n\ts_barrier" : "+v"(vo), "+v"(vw) :: "memory");
    fl_set<T_FS>();
    unsigned tk;
    asm volatile("ds_read_b32 %0, %1\n\ts_waitcnt lgkmcnt(0)" : "=v"(tk) : "v"(rdy) : "memory");
    tk = __builtin_amdgcn_readfirstlane(tk) & 255;
    KST(0, 30);
    if (doA) post_math(pj, pu, out, lds + 131072 + wid * 8192);
#define N3 0
#define N2 12
    constexpr int N1 = 28 - 3 * N3 - 2 * N2;
    static_assert(N1 >= 0 && N3 + N2 + N1 <= 16);
    const int nu = tk < N3 ? 3 : tk < N3 + N2 ? 2 : tk < N3 + N2 + N1 ? 1 : 0;
    const int u0 = 4 * (tk < N3 ? 3 * tk : tk < N3 + N2 ? N3 + 2 * tk : 2 * N3 + N2 + tk) + wid * nu;
    KST(0, 28);
    if (nu) {
      unsigned so[3];
#pragma unroll
      for (int k = 0; k < 3; ++k) so[k] = O_S + qbF * 0x200000 + 0x40000 + (u0 + k) * 16384;
      unsigned pend = 0x5555u, cnt = 0x400000;
      while (true) {
        unsigned nw;
        if (wid == 3) {
          u64 m;
          const u64 ex = pend | (pend << 1);
          asm volatile(FL_ARM("%1") "\n\t" FL_POLL "\n\ts_waitcnt vmcnt(0)\n\tv_cmp_eq_u64 %0, s[84:85], v[124:125]" : "=s"(m) : "s"(ex) : "memory");
          nw = (unsigned)m & ((unsigned)m >> 1) & pend;
          if (nw) asm volatile("ds_write_b32 %0, %1 offset:4" ::"v"(rdy), "v"(~pend | nw) : "memory");
        } else {
          unsigned t;
          asm volatile("ds_read_b32 %0, %1 offset:4\n\ts_waitcnt lgkmcnt(0)" : "=v"(t) : "v"(rdy) : "memory");
          nw = __builtin_amdgcn_readfirstlane(t) & pend;
        }
        if (--cnt == 0) nw = pend;
        if (nw == 0) {
          if (wid == 3) asm volatile("s_sleep 4");
          else asm volatile("s_sleep 1");
          continue;
        }
        asm volatile("s_nop 4");
        static_for([&](auto S) {
          if ((nw >> (2 * S)) & 1) {
            r_load<0, 2 * S>(vo, so[0]); r_load<0, 2 * S + 1>(vo, so[0]);
            if (nu > 1) { r_load<1, 2 * S>(vo, so[1]); r_load<1, 2 * S + 1>(vo, so[1]); }
            if (nu > 2) { r_load<2, 2 * S>(vo, so[2]); r_load<2, 2 * S + 1>(vo, so[2]); }
          }
        }, make_iseq<8>{});
        pend &= ~nw;
        if (pend == 0) break;
      }
      asm volatile("s_waitcnt vmcnt(0)" ::: "memory");
      KST(0, 16);
      auto oo = [&](int U) { return ((U & 15) * 8 * OUTW + (U >> 4) * 64) * 2; };
      r_fin<0>(vw, so_, oo(u0));
      if (nu > 1) r_fin<1>(vw, so_, oo(u0 + 1));
      if (nu > 2) r_fin<2>(vw, so_, oo(u0 + 2));
      if (qbF == 15 && tk == 0) asm volatile(FL_SET("%0") ::"s"(fo(6144)) : "memory");
    }
    KST(0, 29);
  }
  asm volatile("s_waitcnt vmcnt(0)" ::: "memory");
  asm volatile("pf_code_end:");
  KST(0, 1);
}

extern "C" void kernel_launch(void* const* d_in, const int* in_sizes, int n_in, void* d_out, int out_size, void* d_ws,
                              size_t ws_size, hipStream_t stream) {
  const bf16* Aq = (const bf16*)d_in[0];
  const bf16* Ak = (const bf16*)d_in[1];
  const bf16* Av = (const bf16*)d_in[2];
  const bf16* Ash = (const bf16*)d_in[3];
  const bf16* Assmk = (const bf16*)d_in[4];
  const bf16* AssmD = (const bf16*)d_in[5];
  const bf16* Bx = (const bf16*)d_in[6];
  const bf16* Bckv = (const bf16*)d_in[7];
  const bf16* Bwq = (const bf16*)d_in[8];
  const bf16* Bwuk = (const bf16*)d_in[9];
  const bf16* Bwuv = (const bf16*)d_in[10];
  const bf16* Bwo = (const bf16*)d_in[11];
  bf16* out = (bf16*)d_out;
  hipFuncSetAttribute((const void*)fused_kernel, hipFuncAttributeMaxDynamicSharedMemorySize, 163840);
  fused_kernel<<<256, 256, 163840, stream>>>(Bckv, Bwuk, Bwuv, Bx, Bwq, (bf16*)d_ws, Ak, Av, Ash, Assmk, AssmD, Bwo, Aq, out);
}
```

```cpp
#include <hip/hip_runtime.h>
#include <hip/hip_bf16.h>
#include <stdint.h>
#include <utility>

typedef __hip_bfloat16 bf16;
typedef __attribute__((ext_vector_type(4))) short bf16x4;
typedef __attribute__((ext_vector_type(2))) unsigned u32x2;

constexpr int SEQ = 2048;
constexpr int DA = 448;
constexpr int NH = 16;
constexpr int HD = 256;
constexpr int DC = 1024;
constexpr int OUTW = 896;
#define OUTSC 17
#define OUTSCS " sc0 sc1"
#define NTPS " nt"
#define NTVS " nt"
#define NTQS ""
#define NTW 1
#define NYT 15
#define SWAP7 1
constexpr unsigned TSB = NH * SEQ * HD * 2, O_K = TSB, O_S = 3 * TSB, O_P = 5 * TSB, PSTR = 67584, O_F = O_P + 128 * PSTR,
                   O_X = O_F + 400 * 4352, O_Z = O_X + DA * SEQ * 2, WSB = O_Z + DA * SEQ * 2;
constexpr unsigned fo(int w) { return O_F + (w >> 4) * 4352 + (w & 15) * 8; }

#define KST(kid, which)

__device__ __forceinline__ float bf2f(unsigned short u) { return __uint_as_float(((unsigned)u) << 16); }
__device__ __forceinline__ unsigned short f2bf(float f) { return __bfloat16_as_ushort(__float2bfloat16(f)); }
__device__ __forceinline__ float rbf(float f) { return bf2f(f2bf(f)); }
typedef __attribute__((ext_vector_type(2))) __bf16 bf16x2n;
typedef __attribute__((ext_vector_type(2))) float f32x2n;
__device__ __forceinline__ unsigned pk2(float a, float b) {
  return __builtin_bit_cast(unsigned, __builtin_convertvector((f32x2n){a, b}, bf16x2n));
}

__device__ __forceinline__ int off_b(int row, int ch) { return 256 * row + 16 * (ch ^ (((row & 3) << 2) | ((row >> 2) & 3))); }

template <int R> __device__ __forceinline__ void sset(unsigned x) { asm volatile("s_mov_b32 s%c0, %1" ::"i"(R), "s"(x)); }
template <int R> __device__ __forceinline__ void vsetu(unsigned x) { asm volatile("v_mov_b32 v%c0, %1" ::"i"(R), "v"(x)); }
template <int R> __device__ __forceinline__ void srd_set(const void* p, unsigned n) {
  asm volatile("s_mov_b64 s[%c0:%c1], %4\n\ts_mov_b32 s%c2, %5\n\ts_mov_b32 s%c3, 0x20000" ::"i"(R), "i"(R + 1), "i"(R + 2), "i"(R + 3), "s"(p), "s"(n));
}
template <int R, int L> __device__ __forceinline__ void tload() { asm volatile("v_readlane_b32 s%c0, v78, %1" ::"i"(R), "i"(L)); }
template <int L> __device__ __forceinline__ unsigned tget() { unsigned t; asm volatile("v_readlane_b32 %0, v78, %1" : "=s"(t) : "i"(L)); return t; }
#define FL_SET(S) "buffer_store_dwordx2 v[82:83], v81, s[88:91], " S " offen sc1"
#define FL_ARM(N) "v_cndmask_b32_e64 v80, -2.0, v79, " N
#define FL_POLL "buffer_load_dwordx2 v[124:125], v80, s[88:91], 0 offen sc1"
#define FL_CHECK(N) "s_mov_b32 s48, 0x100000\n7:\n\tv_cmp_eq_u64 vcc, s[84:85], v[124:125]\n\ts_and_b64 vcc, vcc, " N "\n\ts_cmp_eq_u64 vcc, " N \
                    "\n\ts_cbranch_scc1 8f\n\ts_sleep 2\n\t" FL_POLL "\n\ts_waitcnt vmcnt(0)\n\ts_sub_u32 s48, s48, 1\n\ts_cbranch_scc0 7b\n8:"
template <int L> __device__ __forceinline__ void fl_set() {
  asm volatile("v_readlane_b32 s48, v78, %0\n\ts_nop 4\n\t" FL_SET("s48") ::"i"(L) : "memory");
}
template <int L> __device__ __forceinline__ void fl_poll() {
  asm volatile("v_readlane_b32 s50, v78, %0\n\tv_readlane_b32 s51, v78, %1\n\ts_nop 1\n\t" FL_ARM("s[50:51]") "\n\t" FL_POLL ::"i"(L), "i"(L + 1) : "memory");
}
#define TICKET "v_readlane_b32 s48, v78, %0\n\tv_mov_b32 v122, 1\n\tv_mov_b32 v123, 0\n\ts_nop 2\n\tbuffer_atomic_add_x2 v[122:123], v81, s[88:91], s48 offen sc0"
__device__ __forceinline__ void fl_check() { asm volatile(FL_CHECK("s[50:51]") ::: "vcc", "scc", "memory"); }
template <int R> __device__ __forceinline__ float vget() { float t; asm volatile("v_mov_b32 %0, v%c1" : "=v"(t) : "i"(R)); return t; }
template <int R> __device__ __forceinline__ void vset(float x) { asm volatile("v_mov_b32 v%c0, %1" ::"i"(R), "v"(x)); }
template <int R> __device__ __forceinline__ float acc_read1() { float t; asm volatile("v_accvgpr_read_b32 %0, a[%c1]" : "=v"(t) : "i"(R)); return t; }
template <class F, int... I> __device__ __forceinline__ void static_for(F&& f, std::integer_sequence<int, I...>) {
  (f(std::integral_constant<int, I>{}), ...);
}
template <int... Is> using iseq = std::integer_sequence<int, Is...>;
template <int N> using make_iseq = std::make_integer_sequence<int, N>;

typedef __attribute__((ext_vector_type(4))) unsigned u32x4c;
typedef __attribute__((ext_vector_type(4))) float f32x4c;
typedef __attribute__((ext_vector_type(2))) float f32x2c;
typedef unsigned long long u64;
struct PR { u32x4c kk[7], vv[4]; };
__device__ __forceinline__ void prep_load(PR& p, int tid, int cg, int tb, const bf16* __restrict__ k, const bf16* __restrict__ v,
                                          const bf16* __restrict__ shift_k) {
  const __amdgpu_buffer_rsrc_t sk = __builtin_amdgcn_make_buffer_rsrc((void*)k, (short)0, SEQ * DA * 2, 0x00020000);
  const __amdgpu_buffer_rsrc_t sv = __builtin_amdgcn_make_buffer_rsrc((void*)v, (short)0, SEQ * DA * 2, 0x00020000);
  const __amdgpu_buffer_rsrc_t sw = __builtin_amdgcn_make_buffer_rsrc((void*)(shift_k + cg * 256), (short)0, 512, 0x00020000);
  const int vo = ((tb * 128 + (tid >> 3) * 4) * DA + cg * 64 + (tid & 7) * 8) * 2;
#pragma unroll
  for (int q = 0; q < 7; ++q)
    asm volatile("buffer_load_dwordx4 %0, %1, %2, 0 offen" NTPS : "=v"(p.kk[q]) : "v"(vo + (q - 3) * DA * 2), "s"(sk) : "memory");
#pragma unroll
  for (int q = 0; q < 4; ++q)
    asm volatile("buffer_load_dwordx4 %0, %1, %2, 0 offen offset:%3" NTVS : "=v"(p.vv[q]) : "v"(vo), "s"(sv), "i"(q * DA * 2) : "memory");
#pragma unroll
  for (int q = 0; q < 4; ++q)
    asm volatile("buffer_load_dwordx4 a[%c0:%c1], %2, %3, 0 offen offset:%4" ::"i"(4 * q), "i"(4 * q + 3), "v"((tid & 7) * 64), "s"(sw), "i"(q * 16) : "memory");
}
__device__ __forceinline__ float bfl(unsigned u) { return __uint_as_float(u << 16); }
__device__ __forceinline__ float bfh(unsigned u) { return __uint_as_float(u & 0xffff0000u); }
__device__ __forceinline__ f32x2c rbf2(f32x2c a) {
  return (f32x2c){__uint_as_float(pk2(0.f, a[0])), __uint_as_float(pk2(0.f, a[1]))};
}
template <int I> __device__ __forceinline__ void prep_math(const PR& p, int tid, char* lx) {
  const int rp = tid >> 3, c = tid & 7;
  f32x2c wt[4], kf[7];
  const unsigned w[4] = {__float_as_uint(acc_read1<4 * I>()), __float_as_uint(acc_read1<4 * I + 1>()), __float_as_uint(acc_read1<4 * I + 2>()),
                         __float_as_uint(acc_read1<4 * I + 3>())};
#pragma unroll
  for (int t = 0; t < 4; ++t)
    wt[t] = (t & 1) ? (f32x2c){bfh(w[t >> 1]), bfh(w[2 + (t >> 1)])} : (f32x2c){bfl(w[t >> 1]), bfl(w[2 + (t >> 1)])};
#pragma unroll
  for (int q = 0; q < 7; ++q) kf[q] = (f32x2c){bfl(p.kk[q][I]), bfh(p.kk[q][I])};
  f32x2c x[4];
#pragma unroll
  for (int e = 0; e < 4; ++e) {
    f32x2c s = rbf2(kf[e] * wt[3]);
    s = rbf2(s + rbf2(kf[e + 1] * wt[2]));
    s = rbf2(s + rbf2(kf[e + 2] * wt[1]));
    s = rbf2(s + rbf2(kf[e + 3] * wt[0]));
    x[e] = s * (f32x2c){bfl(p.vv[e][I]), bfh(p.vv[e][I])};
  }
#pragma unroll
  for (int u = 0; u < 2; ++u)
    *(u32x2*)(lx + (c * 8 + 2 * I + u) * 256 + (((31 - rp) * 8) ^ (c << 4))) = (u32x2){pk2(x[3][u], x[2][u]), pk2(x[1][u], x[0][u])};
}
__device__ __forceinline__ void prep_store(int tid, int cg, int tb, bf16* __restrict__ xr, const char* lx) {
  const __amdgpu_buffer_rsrc_t sx = __builtin_amdgcn_make_buffer_rsrc((void*)(xr + (size_t)cg * 64 * SEQ), (short)0, 64 * SEQ * 2, 0x00020000);
#pragma unroll
  for (int rd = 0; rd < 4; ++rd) {
    const int idx = tid + 256 * rd, ch = idx >> 4, q = idx & 15;
    u32x4c w = *(const u32x4c*)(lx + ch * 256 + 16 * (q ^ (ch >> 3)));
    __builtin_amdgcn_raw_buffer_store_b128(w, sx, (ch * SEQ + (15 - tb) * 128 + q * 8) * 2, 0, 16);
  }
}

#define CODE_TOUCH(LB, LE, wx, NW, NL, m0v) do { unsigned blo_, bhi_, elo_; \
    asm volatile("s_getpc_b64 s[48:49]\n\ts_add_u32 s48, s48, " LB "@rel32@lo+4\n\ts_addc_u32 s49, s49, " LB "@rel32@hi+12\n\t" \
                 "s_mov_b32 %0, s48\n\ts_mov_b32 %1, s49\n\t" \
                 "s_getpc_b64 s[48:49]\n\ts_add_u32 s48, s48, " LE "@rel32@lo+4\n\ts_mov_b32 %2, s48" \
                 : "=s"(blo_), "=s"(bhi_), "=s"(elo_) :: "scc"); \
    const unsigned n_ = elo_ - (blo_ & ~127u); \
    const __amdgpu_buffer_rsrc_t srd_ = __builtin_amdgcn_make_buffer_rsrc((void*)(((unsigned long long)bhi_ << 32) | (blo_ & ~127u)), (short)0, n_ < 262144u ? n_ : 0u, 0x00020000); \
    const unsigned l_ = threadIdx.x & 63, vo_ = l_ < (NL) ? ((wx) + l_ * (NW)) * 128u : 0x80000000u; \
    asm volatile("s_mov_b32 m0, %0\n\ts_nop 4\n\tbuffer_load_dword %1, %2, 0 offen lds" ::"s"(m0v), "v"(vo_), "s"(srd_) : "memory"); } while (0)
template <int I> __device__ __forceinline__ void d_store() {
  asm volatile("buffer_store_dwordx4 v[%c0:%c1], v%c2, s[88:91], s101 offen offset:%3 sc1" ::"i"(128 + 4 * I), "i"(131 + 4 * I), "i"(112 + ((I >> 2) & 1)),
               "i"((I & 3) * 1024) : "memory");
  if constexpr ((I & 7) == 7) asm volatile("s_addk_i32 s101, 0x4000" ::: "scc");
}
constexpr int NS_X = 19;
struct CV { unsigned pa[4], pb, px, vz, sz; float D; };
constexpr int cv_J(bool up, int q) { int J = up ? 0 : 7; while (q > J) { q -= J + 1; J += up ? 1 : -1; } return J; }
constexpr int cv_i(bool up, int q) { int J = up ? 0 : 7; while (q > J) { q -= J + 1; J += up ? 1 : -1; } return q; }
template <int R, int N> __device__ __forceinline__ void cv_rd(CV& c) {
  if constexpr (N < 16) {
    constexpr int i = N >> 1, o = 16 * R + 128 * (i & 1) + 2 * (N & 1), r = 32 + 32 * (R & 1) + 2 * N;
    asm volatile("ds_read2_b32 a[%c1:%c2], %0 offset0:%c3 offset1:%c4" ::"v"(c.pa[i >> 1]), "i"(r), "i"(r + 1), "i"(o), "i"(o + 1));
  } else {
    constexpr int m = N - 16, r = 192 + 32 * (R & 1) + 4 * m;
    asm volatile("ds_read_b128 a[%c1:%c2], %0 offset:%c3" ::"v"(c.pb), "i"(r), "i"(r + 3), "i"(64 * R + 512 * (7 - m)));
  }
}
template <int O> __device__ __forceinline__ void cv_xd(CV& c) {
  constexpr int e = O & 31, x = 96 + ((e >> 1) ^ 1);
  if constexpr (O >= 64) asm volatile("v_cvt_pk_bf16_f32 v%c0, 0, v%c0" ::"i"(160 + e));
  else if constexpr (O >= 32) asm volatile("v_mul_f32 v%c0, v%c0, %1" ::"i"(160 + e), "v"(c.D));
  else if constexpr (O & 1) asm volatile("v_lshlrev_b32 v%c0, 16, v%c1" ::"i"(160 + e), "i"(x));
  else asm volatile("v_and_b32 v%c0, 0xffff0000, v%c1" ::"i"(160 + e), "i"(x));
}
template <int O> __device__ __forceinline__ void cv_z(CV& c) {
  constexpr int J = O / 11, k = O % 11, y = 128 + 4 * J;
  if constexpr (k < 4) asm volatile("v_cvt_pk_bf16_f32 v%c0, 0, v%c0" ::"i"(y + k));
  else if constexpr (k < 8) asm volatile("v_add_f32 v%c0, v%c0, v%c1" ::"i"(y + k - 4), "i"(y + 28 + k));
  else if constexpr (k < 10) asm volatile("v_cvt_pk_bf16_f32 v%c0, v%c1, v%c2" ::"i"(y + k - 8), "i"(y + 2 * k - 16), "i"(y + 2 * k - 15));
  else asm volatile("buffer_store_dwordx2 v[%c0:%c1], %2, s[88:91], %3 offen offset:%4 sc1" ::"i"(y), "i"(y + 1), "v"(c.vz), "s"(c.sz),
                    "i"(512 * J) : "memory");
}
constexpr int cv_z0(int Q) {
  int o = 0;
  for (int q = 0; q < Q; ++q)
    for (int k = 0; k < (q % 4 == 1 ? 0 : 3); ++k) { const int J = o / 11; if ((J + 1) * (J + 2) / 2 - 1 + (J ? 4 : 7) <= q) ++o; }
  return o;
}
constexpr int cv_xd0(int T) { int o = 0; for (int t = 0; t < T; ++t) o += t % 4 != 1 || (t >= 72 && t < 108); return o < 96 ? o : 96; }
static_assert(cv_xd0(144) == 96);
template <int B, int... Is> __device__ __forceinline__ void cv_zs(CV& c, iseq<Is...>) { (cv_z<B + Is>(c), ...); }
template <int R, int Q> __device__ __forceinline__ void cv_slot(CV& c) {
  constexpr int i = cv_i(R == 7, Q), J = cv_J(R == 7, Q), d = 128 + 4 * J, a = 32 + 32 * (R & 1) + 4 * i, b = 192 + 32 * (R & 1) + 4 * (J - i);
  constexpr int T = 36 * R + Q, vm = Q % 4 == 1 && Q < 32 ? Q / 4 : -1;
  if constexpr (Q == 0) asm volatile("s_waitcnt lgkmcnt(0)");
  if constexpr (R >= 6 && vm >= 0 && vm % 4 == 0) asm volatile("s_add_u32 m0, s79, %0" ::"i"(((R - 6) ^ (NYT & 1)) * 32768 + vm * 1024));
  if constexpr (R == 0 && i == 0)
    asm volatile("v_mfma_f32_16x16x32_bf16 v[%c0:%c1], a[%c2:%c3], a[%c4:%c5], 0" ::"i"(d), "i"(d + 3), "i"(a), "i"(a + 3), "i"(b), "i"(b + 3));
  else
    asm volatile("v_mfma_f32_16x16x32_bf16 v[%c0:%c1], a[%c2:%c3], a[%c4:%c5], v[%c0:%c1]" ::"i"(d), "i"(d + 3), "i"(a), "i"(a + 3), "i"(b), "i"(b + 3));
  if constexpr (Q < 24 && R < 7) cv_rd<R + 1, Q>(c);
  if constexpr (T % 7 == 3 && NS_X + 5 + T / 7 < 32) d_store<NS_X + 5 + T / 7>();
  if constexpr (R == 2 && Q >= 24 && Q < 32)
    asm volatile("ds_read_b64 v[%c1:%c2], %0 offset:%c3" ::"v"(c.px), "i"(96 + 2 * (Q - 24)), "i"(97 + 2 * (Q - 24)), "i"(512 * (31 - Q)));
  if constexpr (R >= 3 && R < 7) if constexpr (cv_xd0(T - 107) > cv_xd0(T - 108)) cv_xd<cv_xd0(T - 108)>(c);
  if constexpr ((R == 3 || R == 4) && vm >= 0) {
    constexpr int f = (R - 3) * 8 + vm;
    asm volatile("buffer_load_dwordx4 a[%c0:%c1], v84, s[88:91], s96 offen offset:%2 sc1" ::"i"(128 + 4 * f), "i"(131 + 4 * f), "i"((f & 3) * 1024) : "memory");
    if constexpr ((f & 3) == 3) asm volatile("s_addk_i32 s96, %0" ::"i"(f == 7 ? 28672 : 4096));
  }
  if constexpr (R == 4 && Q == 35) asm volatile(FL_POLL ::: "memory");
  if constexpr (R >= 6 && vm >= 0)
    asm volatile("buffer_load_dwordx4 v%c0, s[88:91], s98 offen offset:%1 sc1 lds" ::"i"(86 + vm / 4), "i"((vm & 3) * 1024) : "memory");
  if constexpr (R == 6 && Q == 35) asm volatile("s_add_u32 s98, s98, 0x8000");
  if constexpr (R == 7) cv_zs<cv_z0(Q)>(c, make_iseq<cv_z0(Q + 1) - cv_z0(Q)>{});
  __builtin_amdgcn_sched_barrier(0);
}
template <int R, int... Q> __device__ __forceinline__ void cv_step(CV& c, iseq<Q...>) { (cv_slot<R, Q>(c), ...); }
template <int... N> __device__ __forceinline__ void cv_rd0(CV& c, iseq<N...>) { (cv_rd<0, N>(c), ...); }
template <int FLAG> __device__ __forceinline__ void conv_wave(unsigned sz, float Dd, char* lw) {
  int lane = threadIdx.x & 63; asm volatile("" : "+v"(lane));
  const int l16 = lane & 15, fq = lane >> 4;
  u32x4c w[5]; unsigned w4[5];
#pragma unroll
  for (int r = 0; r < 5; ++r) {
    w[r] = *(const u32x4c*)(lw + r * 1024 + lane * 16);
    w4[r] = *(const unsigned*)(lw + r * 1024 + lane * 16 + 16);
  }
  d_store<NS_X>(); d_store<NS_X + 1>();
#pragma unroll
  for (int r = 0; r < 5; ++r)
    *(u32x4c*)(lw + 10304 + r * 1024 + lane * 16) = (u32x4c){__builtin_amdgcn_alignbit(w[r][1], w[r][0], 16), __builtin_amdgcn_alignbit(w[r][2], w[r][1], 16),
                                                             __builtin_amdgcn_alignbit(w[r][3], w[r][2], 16), __builtin_amdgcn_alignbit(w4[r], w[r][3], 16)};
  d_store<NS_X + 2>();
  CV c;
#pragma unroll
  for (int i = 0; i < 4; ++i) c.pa[i] = (unsigned)(size_t)lw + ((l16 + 1) & 1) * 10304 + 4 * ((8 * fq + l16 + 1) >> 1) + 1024 * i;
  c.pb = (unsigned)(size_t)lw + 5120 + (2032 - 16 * l16 + 8 * fq) * 2 - 512 * 7;
  c.vz = (16 * l16 + 4 * fq) * 2;
  c.px = (unsigned)(size_t)lw + 5120 + 2044 * 2 - 512 * 7 - c.vz;
  c.sz = sz; c.D = Dd;
  asm volatile("" : "+v"(c.vz), "+v"(c.px), "+v"(c.D) :: "memory");
  asm volatile("" : "+s"(c.sz));
  cv_rd0(c, make_iseq<24>{});
  d_store<NS_X + 3>(); d_store<NS_X + 4>();
  __builtin_amdgcn_sched_barrier(0);
  static_for([&](auto R) __attribute__((always_inline)) {
    cv_step<R>(c, make_iseq<36>{});
    if constexpr (R == 2) {
      asm volatile("s_waitcnt vmcnt(0)\n\ts_barrier" ::: "memory");
      fl_set<FLAG>();
    }
    if constexpr (R == 5) {
      asm volatile("s_waitcnt vmcnt(0)" ::: "memory");
      fl_check();
    }
    __builtin_amdgcn_sched_barrier(0);
  }, make_iseq<8>{});
  asm volatile("s_nop 7");
  cv_zs<cv_z0(36)>(c, make_iseq<88 - cv_z0(36)>{});
}

__device__ __forceinline__ void post_dma(int cg, int tb, const bf16* __restrict__ q, unsigned m0w) {
  int lane = threadIdx.x & 63; asm volatile("" : "+v"(lane));
  const __amdgpu_buffer_rsrc_t sq = __builtin_amdgcn_make_buffer_rsrc((void*)(q + (size_t)tb * 32 * DA + cg * 64), (short)0, 32 * DA * 2, 0x00020000);
  const unsigned vq = ((lane >> 3) * DA + (lane & 7) * 8) * 2;
  asm volatile("s_mov_b32 m0, %0\n\ts_nop 4\n\tbuffer_load_dwordx4 %1, %2, 0 offen" NTQS " lds\n\tbuffer_load_dwordx4 %1, %2, %3 offen offset:1024" NTQS " lds\n\t"
               "buffer_load_dwordx4 %1, %2, %4 offen offset:2048" NTQS " lds\n\tbuffer_load_dwordx4 %1, %2, %5 offen offset:3072" NTQS " lds"
               ::"s"(m0w + 4096), "v"(vq), "s"(sq), "s"(8 * DA * 2 - 1024), "s"(16 * DA * 2 - 2048), "s"(24 * DA * 2 - 3072) : "memory");
}
__device__ __forceinline__ void post_math(int cg, int tb, bf16* __restrict__ out, const char* lw) {
  int lane = threadIdx.x & 63; asm volatile("" : "+v"(lane));
  const int tl = lane >> 3, ck = lane & 7;
  const __amdgpu_buffer_rsrc_t so = __builtin_amdgcn_make_buffer_rsrc((void*)(out + (size_t)tb * 32 * OUTW + cg * 64), (short)0, 32 * OUTW * 2 - 2 * DA, 0x00020000);
  const unsigned vo = (tl * OUTW + ck * 8) * 2;
#pragma unroll
  for (int i = 0; i < 4; ++i) {
    const u32x4c qv = *(const u32x4c*)(lw + 4096 + i * 1024 + lane * 16);
    const char* lz = lw + ck * 64 + 16 * (i ^ (ck >> 1)) + tl * 2;
    u32x4c o;
#pragma unroll
    for (int e = 0; e < 4; ++e) {
      const float z0 = bf2f(*(const unsigned short*)(lz + 2 * e * 512));
      const float z1 = bf2f(*(const unsigned short*)(lz + (2 * e + 1) * 512));
      o[e] = pk2(bfl(qv[e]) * z0, bfh(qv[e]) * z1);
    }
    __builtin_amdgcn_raw_buffer_store_b128(o, so, vo + i * 8 * OUTW * 2, 0, OUTSC);
  }
}

#define DI __device__ __forceinline__
#define GAP() __builtin_amdgcn_sched_barrier(0)
typedef __attribute__((ext_vector_type(4))) float f32x4g;
typedef __attribute__((ext_vector_type(4))) unsigned u32x4g;
constexpr int LDW = NH * HD;
struct GM { unsigned aA[2], aB[2][8]; };
template <bool W, int J, int V, bool NT = false> DI void g_dma() {
  if constexpr (NT && J == 0) asm volatile("buffer_load_dwordx4 v%c0, s[60:63], 0 offen nt lds" ::"i"(V) : "memory");
  else if constexpr (NT) asm volatile("buffer_load_dwordx4 v%c0, s[60:63], s%c1 offen nt lds" ::"i"(V), "i"(70 + J) : "memory");
  else
  if constexpr (J == 0) asm volatile("buffer_load_dwordx4 v%c0, s[%c1:%c2], 0 offen lds" ::"i"(V), "i"(W ? 60 : 56), "i"(W ? 63 : 59) : "memory");
  else asm volatile("buffer_load_dwordx4 v%c0, s[%c1:%c2], s%c3 offen lds" ::"i"(V), "i"(W ? 60 : 56), "i"(W ? 63 : 59), "i"((W ? 70 : 63) + J) : "memory");
}
template <int P, int KS, int I> DI void g_wread(GM& g) {
  constexpr int nf = I >> 1, hf = I & 1, r = 192 - 64 * P + 8 * nf + 4 * KS + 2 * hf;
  asm volatile("ds_read_b64_tr_b16 a[%c1:%c2], %0 offset:%c3" ::"v"(g.aB[hf][nf]), "i"(r), "i"(r + 1), "i"(P * 32768 + KS * 16384));
}
template <int P, int M, int KS> DI void g_aread(GM& g) {
  constexpr int r = 96 + 8 * (M & 1) + 4 * KS;
  asm volatile("ds_read_b128 v[%c1:%c2], %0 offset:%c3" ::"v"(g.aA[KS]), "i"(r), "i"(r + 3), "i"(P * 32768 + M * 2048));
}
constexpr float CS = 0.0625f * 1.4426950408889634f;
constexpr int NS_Q = 19;
constexpr int e_vm(int G) { return (G * 3) % 8 < 3 ? G * 3 / 8 : -1; }
constexpr int e_st(int MODE, int G) {
  const int q = G & 15, k3 = ((G >> 4) - 1) * 3 + (q == 0 ? 0 : q == 4 ? 1 : 2), k2 = NS_Q + (G >> 4) * 2 + (q == 4);
  if (MODE == 5) return e_vm(G) >= 32 ? e_vm(G) - 32 : -1;
  if (MODE == 3) return G >= 33 && (G - 33) % 5 == 0 && (G - 33) / 5 < NS_X ? (G - 33) / 5 : -1;
  if (MODE == 2) return G >= 30 && (q == 0 || q == 4 || q == 14) && k3 - 2 < NS_Q ? k3 - 2 : -1;
  if (MODE == 4) return (q == 0 || q == 4) && k2 < 32 ? k2 : -1;
  return -1;
}
constexpr int e_nst(int MODE, int G0, int G1) { int n = 0; for (int G = G0; G < G1; ++G) n += e_st(MODE, G) >= 0; return n; }
static_assert(e_nst(2, 0, 128) == NS_Q && e_nst(3, 30, 128) == NS_X && e_nst(5, 0, 128) == 16 && e_nst(4, 0, 104) == 32 - NS_Q);
constexpr int e_slot(int MODE, int G) { return MODE == 3 ? (G % 3 == 0 && G < 30 ? G / 3 : -1) : e_vm(G) >= 0 && e_vm(G) < 32 ? 10 + e_vm(G) : -1; }
struct ES { int op0[129], nop[129]; };
constexpr ES e_sched(bool TR, int MODE) {
  ES z{};
  const bool SCL = MODE == 2 || MODE == 4;
  const int per = SCL ? 16 : 8, o1 = 32 * per;
  int o = MODE == 4 ? e_sched(false, 2).op0[128] : 0;
  for (int G = 0; G <= 128; ++G) {
    const int q = G & 15;
    int cap = 2 - (q == 1 || q == 2 || q == 15);
    if (SCL) cap -= (q >= 3 && q < 11 && (q & 1)) + (q == 7 || q == 11) + 2 * (G == 70 || G == 74 || (G & 63) == 61);
    cap += MODE == 2 ? 4 + (q == 10 || q == 13) : MODE == 3 ? 1 : MODE == 5 ? 2 : 0;
    if (e_st(MODE, G) >= 0 || (SCL ? q == 8 || q == 12 : e_slot(MODE, G) >= 0) || (MODE == 4 && G < 4)) cap = 0;
    if (G == 128) cap = 10000;
    z.op0[G] = o;
    while (o < o1) {
      const int i = o / per, rdy = MODE == 4 ? 0 : (TR ? 32 * (i >> 3) + 16 + 2 * (i & 7) + 1 : 4 * i + 3) + 6;
      if ((G < rdy && G < 128) || cap-- <= 0) break;
      ++o; ++z.nop[G];
    }
  }
  return z;
}
template <bool TR, int MODE> constexpr ES ESCH = e_sched(TR, MODE);
static_assert(ESCH<false, 4>.op0[96] == 512 && ESCH<false, 4>.op0[0] >= 24 * 16);
template <bool TR, bool SCL, int O> DI void e_op() {
  constexpr int per = SCL ? 16 : 8, i = O / per, k = O % per, r = 128 + 4 * i, t = 114 + 4 * (i & 1), c = k - (per - 4);
  if constexpr (k < 4) asm volatile("v_accvgpr_read_b32 v%c0, a[%c1]" ::"i"(t + k), "i"(4 * i + k));
  else if constexpr (c < 0) asm volatile("v_mul_f32 v%c0, s83, v%c0" ::"i"(k < 8 ? r + (k ^ 6) : t + k - 8));
  else {
    constexpr int d = c == 0 ? r + 3 : c == 1 ? r + 2 : r + c - 2, x = c == 0 ? r + 2 : c == 1 ? r : t + 2 * (c - 2);
    asm volatile("v_cvt_pk_bf16_f32 v%c0, v%c1, v%c2" ::"i"(d), "i"(x), "i"(x + 1));
  }
}
template <bool TR, bool SCL, int B, int... Is> DI void e_ops(iseq<Is...>) { (e_op<TR, SCL, B + Is>(), ...); }
template <int S> DI void e_m0() {
  if constexpr (S == 0 || S == 1 || S == 5 || S == 9) asm volatile("s_add_u32 m0, s78, %0" ::"i"(S * 1024));
  if constexpr (S >= 26 && (S - 26) % 4 == 0) asm volatile("s_add_u32 m0, s79, %0" ::"i"(((S - 26) >> 3) * 32768 + ((S - 26) & 4) * 1024));
}
template <int S> DI void e_load() {
  if constexpr (S == 0) asm volatile("buffer_load_dwordx4 v122, s[56:59], 0 offen lds" ::: "memory");
  else if constexpr (S < 5) asm volatile("buffer_load_dwordx4 v84, s[56:59], s64 offen offset:%0 lds" ::"i"((S - 1) * 1024) : "memory");
  else if constexpr (S < 9) asm volatile("buffer_load_dwordx4 v84, s[60:63], 0 offen offset:%0 sc1 lds" ::"i"((S - 5) * 1024) : "memory");
  else if constexpr (S == 9) asm volatile("buffer_load_dwordx4 v84, s[60:63], s62 offen sc1 lds" ::: "memory");
  else if constexpr (S < 26) {
    constexpr int f = S - 10;
    asm volatile("buffer_load_dwordx4 a[%c0:%c1], v84, s[88:91], s96 offen offset:%2 sc1" ::"i"(128 + 4 * f), "i"(131 + 4 * f), "i"((f & 3) * 1024) : "memory");
    if constexpr ((f & 3) == 3) asm volatile("s_addk_i32 s96, %0" ::"i"(f == 7 ? 28672 : 4096));
  } else {
    constexpr int f = S - 26;
    asm volatile("buffer_load_dwordx4 v%c0, s[88:91], s98 offen offset:%1 sc1 lds" ::"i"(86 + ((f >> 2) & 1)), "i"((f & 3) * 1024) : "memory");
    if constexpr (f == 7) asm volatile("s_add_u32 s98, s98, s97");
    if constexpr (f == 15) asm volatile(FL_POLL ::: "memory");
  }
}
template <bool TR, int P, int H, int G, int MODE, bool NT> DI void g_gap(GM& g) {
  constexpr int m = 4 * H + (G >> 4), q = G & 15, n = q >> 1, ks = q & 1, GG = 64 * H + G;
  constexpr bool inV = TR ? m & 1 : n & 1, END = MODE == 3 || MODE == 5;
  constexpr int a = 96 + 8 * (m & 1) + 4 * ks, b = 192 - 64 * P + 8 * n + 4 * ks, d = (inV ? 128 : 0) + 4 * (TR ? 8 * (m >> 1) + n : 4 * m + (n >> 1));
  constexpr int J = END ? -1 : q == 8 ? 2 * (G >> 4) : q == 12 ? 2 * (G >> 4) + 1 : -1, gj = (J & 3) ^ ((J & 3) >> 1);
  if constexpr (END) { if constexpr (q == 0 && GG > 0) asm volatile("s_waitcnt lgkmcnt(0)"); e_m0<e_slot(MODE, GG)>(); }
  else if constexpr (q == 0 && G > 0) asm volatile("s_waitcnt lgkmcnt(4)");
  if constexpr (J == 0) asm volatile("s_add_u32 m0, s78, %0" ::"i"((P ^ H ^ 1) * 32768 + (1 - H) * 8192));
  else if constexpr (J == 4) asm volatile("s_add_u32 m0, s79, %0" ::"i"((P ^ H) * 32768 + (1 - H) * 16384));
  else if constexpr (J > 0 && (J & 3) == 1) asm volatile("s_bitset1_b32 m0, 12");
  else if constexpr (J > 0 && (J & 3) == 2) asm volatile("s_bitset1_b32 m0, %0" ::"i"(J < 4 ? 14 : 13));
  else if constexpr (J > 0) asm volatile("s_bitset0_b32 m0, 12");
#define GMF(D, X, Y, C) asm volatile("v_mfma_f32_16x16x32_bf16 " D "[%c0:%c1], " X ", " Y ", " C ::"i"(d), "i"(d + 3), "i"(a), "i"(a + 3), "i"(b), "i"(b + 3))
#define GMF2(D, C) do { if constexpr (TR) GMF(D, "v[%c2:%c3]", "a[%c4:%c5]", C); else GMF(D, "a[%c4:%c5]", "v[%c2:%c3]", C); } while (0)
  if constexpr ((MODE == 1 || MODE == 4) && ks == 0) { if constexpr (inV) GMF2("v", "0"); else GMF2("a", "0"); }
  else if constexpr (inV) GMF2("v", "v[%c0:%c1]");
  else GMF2("a", "a[%c0:%c1]");
#undef GMF2
#undef GMF
  if constexpr ((q == 1 || q == 2) && !(END && m == 7)) g_aread<(m < 7 ? P : P ^ 1), (m + 1) & 7, q - 1>(g);
  if constexpr (!END && q >= 3 && q < 11 && (q & 1)) g_wread<P ^ 1, H, (G >> 4) * 4 + (q - 3) / 2>(g);
  if constexpr (e_st(MODE, GG) >= 0) {
    static_assert(ESCH<TR, MODE>.op0[GG] >= (e_st(MODE, GG) + 1) * (END ? 8 : 16));
    d_store<e_st(MODE, GG)>();
  }
  if constexpr (J >= 0 && J < 4) g_dma<false, (1 - H) * 2 + (gj & 1) + (gj >> 1) * 4, 90>();
  else if constexpr (J >= 4) g_dma<true, (1 - H) * 4 + gj, 91, NT>();
  if constexpr (MODE >= 2) e_ops<TR, !END, ESCH<TR, MODE>.op0[GG]>(make_iseq<ESCH<TR, MODE>.nop[GG]>{});
  if constexpr (END) {
    if constexpr (e_slot(MODE, GG) >= 0) e_load<e_slot(MODE, GG)>();
  } else {
    if constexpr (H == 1 && G == 6) asm volatile("v_lshl_add_u32 v90, s82, 7, v92\n\ts_and_b32 s82, s80, 15");
    if constexpr (H == 1 && G == 10) asm volatile("s_lshr_b64 s[80:81], s[80:81], 4\n\tv_lshl_add_u32 v91, s82, 19, v93");
    constexpr int nst = e_nst(MODE, H ? 29 : 0, GG + 1) + (MODE == 4 && !H ? e_nst(2, 93, 128) : 0);
    if constexpr (G == 61) asm volatile("s_waitcnt vmcnt(%0) lgkmcnt(0)\n\ts_barrier" ::"i"(12 + nst) : "memory");
  }
  if constexpr (END && GG == 127) {
    asm volatile("s_nop 7");
    e_ops<TR, false, ESCH<TR, MODE>.op0[128]>(make_iseq<ESCH<TR, MODE>.nop[128]>{});
  }
  GAP();
}
template <bool TR, int P, int H, int MODE, bool NT, int... Gs> DI void g_half_(GM& g, iseq<Gs...>) { (g_gap<TR, P, H, Gs, MODE, NT>(g), ...); }
template <bool TR, int P, int H, int MODE, bool NT = false> DI void g_half(GM& g) { g_half_<TR, P, H, MODE, NT>(g, make_iseq<64>{}); }
template <bool TR, int P, int MODE, bool NT = false> DI void g_tile(GM& g) { g_half<TR, P, 0, MODE, NT>(g); g_half<TR, P, 1, MODE, NT>(g); }
template <int P, int... Is> DI void g_read0(GM& g, iseq<Is...>) {
  (g_wread<P, 0, Is>(g), ...); (g_wread<P, 1, Is>(g), ...);
  g_aread<P, 0, 0>(g); g_aread<P, 0, 1>(g);
}
DI void g_proW(unsigned g, unsigned mW) {
  asm volatile("v_lshl_add_u32 v91, %0, 18, v93\n\ts_mov_b32 m0, %1" ::"s"(g), "s"(mW));
  asm volatile("s_nop 0"); g_dma<true, 0, 91>();
  asm volatile("s_bitset1_b32 m0, 12\n\ts_nop 0"); g_dma<true, 1, 91>();
  asm volatile("s_bitset1_b32 m0, 13\n\ts_nop 0"); g_dma<true, 3, 91>();
  asm volatile("s_bitset0_b32 m0, 12\n\ts_nop 0"); g_dma<true, 2, 91>();
}
template <int PART = 3> DI void g_pro(unsigned g, unsigned mA, unsigned mW, unsigned lda) {
  g &= 3;
  if constexpr (PART & 1)
    asm volatile("v_add_u32 v90, %0, v92\n\ts_mov_b32 m0, %1" ::"s"((g >> 1) * 128 + (g & 1) * 128 * lda), "s"((mA ^ (g >> 1) * 32768) + (g & 1) * 8192));
  if constexpr (PART == 1) return;
  asm volatile("s_nop 0"); g_dma<false, 0, 90>();
  asm volatile("s_bitset1_b32 m0, 12\n\ts_nop 0"); g_dma<false, 1, 90>();
  asm volatile("s_bitset1_b32 m0, 14\n\ts_nop 0"); g_dma<false, 5, 90>();
  asm volatile("s_bitset0_b32 m0, 12\n\ts_nop 0"); g_dma<false, 4, 90>();
  g_proW(g, mW ^ g * 16384);
}
template <bool TR, bool SCL, class HOOK> DI void g_loop(GM& g, unsigned evm, HOOK&& hook) {
  constexpr int nkt = SCL ? 7 : 16, P0 = SCL ? 0 : 1;
  asm volatile("" : "+s"(evm));
#pragma unroll 1
  for (int t = 1;; t += 2) {
    g_half<TR, P0 ^ 1, 0, 0, (SCL ? NTW >= 1 : NTW >= 2)>(g);
    if (__builtin_expect((evm >> t) & 1, 0)) { hook(t); GAP(); }
    g_half<TR, P0 ^ 1, 1, 0, (SCL ? NTW >= 1 : NTW >= 2)>(g);
    if (SCL && t + 2 >= nkt) break;
    g_half<TR, P0, 0, 0, (SCL ? NTW >= 1 : NTW >= 2)>(g);
    if constexpr (SCL) { if (__builtin_expect(t == 3, 0)) { hook(4); GAP(); } }
    else asm volatile("s_cmp_eq_u32 %0, 13\n\ts_cselect_b32 s78, 0x28000, s78" ::"s"(t) : "scc");
    g_half<TR, P0, 1, 0, (SCL ? NTW >= 1 : NTW >= 2)>(g);
    if (!SCL && t + 3 >= nkt) break;
  }
}

typedef __attribute__((ext_vector_type(4))) unsigned u32x4;
typedef __attribute__((ext_vector_type(4))) float f32x4;

constexpr int R_S = 128, R_P = 192, R_CI = 224, R_E = 96;
constexpr int R_TS = 112, R_RS = 114, R_M = 116;
constexpr int R_PD = 118, R_THR = 120, R_TMP = 121;
constexpr int r_dk(int q) { return q < 6 ? 90 + q : 120 + q; }
constexpr int S_V = 96, S_T = 97, S_K = 98, S_I = 99, S_EV = 100;
struct AC {
  unsigned rare;
};
DI bool redo() { u64 t; asm volatile("s_mov_b64 %0, s[54:55]" : "=s"(t)); return t != 0; }
template <int X, int F> DI void kread() {
  constexpr int r = 192 + 4 * (F & 15);
  asm volatile("ds_read_b128 a[%c0:%c1], v84 offset:%c2" ::"i"(r), "i"(r + 3), "i"(X * 32768 + F * 1024));
}
template <int X, int F> DI void vread() {
  constexpr int r = 192 + 4 * (F & 15);
  asm volatile("ds_read_b128 a[%c0:%c1], v85 offset:%c2" ::"i"(r), "i"(r + 3), "i"(X * 32768 + (F & 1) * 16384 + (F >> 1) * 1024));
}
template <int X, int... Fs> DI void kreads(iseq<Fs...>) { (kread<X, Fs>(), ...); }
template <int X, int J> DI void dma_m0() {
  if constexpr (J == 0) asm volatile("s_mov_b32 m0, s%c0" ::"i"(78 + X));
  else if constexpr (J == 4) asm volatile("s_or_b32 m0, s%c0, 0x1000" ::"i"(78 + X));
  else if constexpr (J == 8) asm volatile("s_bitset0_b32 m0, 16");
  else if constexpr (J == 12) asm volatile("s_bitset0_b32 m0, 12");
}
template <int J> DI void dma_at() {
  if constexpr (J < 8)
    asm volatile("buffer_load_dwordx4 v%c0, s[88:91], s96 offen offset:%1 sc1 lds" ::"i"(86 + (J >= 4)), "i"((J & 3) * 1024) : "memory");
  else
    asm volatile("buffer_load_dwordx4 v%c0, s[92:95], s98 offen offset:%1 sc1 lds" ::"i"(r_dk(J - 8)), "i"((J & 3) * 1024) : "memory");
}
template <int Q> DI void dma4k() {
  asm volatile("s_add_u32 m0, s79, %0\n\ts_add_u32 s48, s98, %1\n\tbuffer_load_dwordx4 v%c2, s[88:91], s48 offen sc1 lds\n\t"
               "buffer_load_dwordx4 v%c2, s[88:91], s48 offen offset:1024 sc1 lds\n\tbuffer_load_dwordx4 v%c2, s[88:91], s48 offen offset:2048 sc1 lds\n\t"
               "buffer_load_dwordx4 v%c2, s[88:91], s48 offen offset:3072 sc1 lds"
               ::"i"((Q >> 1) * 32768 + (Q & 1) * 4096), "i"((Q >> 1) * 32768), "i"(86 + (Q & 1)) : "memory", "scc");
}
template <int Q> DI void acc_zero2() {
  asm volatile("v_accvgpr_write_b32 a[%c0], 0\n\tv_accvgpr_write_b32 a[%c1], 0" ::"i"(2 * Q), "i"(2 * Q + 1));
}
#define SMV 0
struct SMS { int ex[128], op0[128], nop[128], last; };
constexpr SMS sm_sched() {
  SMS z{};
  int et[32] = {}, e = 0;
  for (int t = 0; t < 128; ++t) {
    const int g = t & 15;
    const bool x = t < 48 ? (g >= 8 && (g & 1) && g < 15) : t < 64 ? (g >= 9 && g < 15) : (g >= 8 && g < 14);
    z.ex[t] = x && e < 32 ? e : -1;
    if (z.ex[t] >= 0) et[e++] = t;
  }
  int o = 0;
  for (int t = 0; t < 128; ++t) {
    const int g = t & 15;
    int cap = z.ex[t] >= 0 || t < 8 || (t < 48 && (g == 7 || (g >= 8 && !(g & 1)))) || t == 56 || t == 63 || t == 111 ? 0 : 1;
    z.op0[t] = o; z.nop[t] = 0;
    while (cap > 0 && o < 48 && et[2 * (o / 3) + (o % 3 == 0 ? 0 : 1)] + 2 <= t) {
      const int el = 2 * (o / 3) + o % 3;
      if (!(o % 3 < 2 && (el == 0 || el == 4))) --cap;
      ++o; ++z.nop[t]; z.last = t;
    }
  }
  for (int k = 16; k < 32; ++k) if (z.op0[et[k]] < 3 * ((k - 16) / 2 + 1)) z.last = 1000;
  return z;
}
constexpr SMS SM = sm_sched();
static_assert(SM.last <= 117);
constexpr int p_reg(int Y, int k) { return R_P + 16 * Y + 4 * ((k >> 3) * 2 + ((k >> 1) & 1)) + ((k >> 2) & 1) * 2 + (k & 1); }
template <int Y, int O> DI void sm_op() {
  constexpr int k = O / 3, e = 2 * k + O % 3, ts = R_TS + ((e >> 2) & 1);
  if constexpr (O % 3 == 2) asm volatile("v_cvt_pk_bf16_f32 v%c0, v%c1, v%c2" ::"i"(p_reg(Y, k)), "i"(R_E + ((2 * k) & 15)), "i"(R_E + ((2 * k + 1) & 15)));
  else if constexpr (e == 1 || e == 5) asm volatile("v_add_f32_e32 v%c0, v%c1, v%c2" ::"i"(ts), "i"(R_E + e - 1), "i"(R_E + e));
  else if constexpr (e != 0 && e != 4) asm volatile("v_add_f32_e32 v%c0, v%c0, v%c1" ::"i"(ts), "i"(R_E + (e & 15)));
}
template <int Y, int T> DI void sm_gap(AC& c) {
  if constexpr (SM.ex[T] >= 0) asm volatile("v_exp_f32 v%c0, v%c1" ::"i"(R_E + (SM.ex[T] & 15)), "i"(R_S + 32 * Y + SM.ex[T]));
  if constexpr (SM.nop[T] > 0) sm_op<Y, SM.op0[T]>();
  if constexpr (SM.nop[T] > 1) sm_op<Y, SM.op0[T] + 1>();
  if constexpr (SM.nop[T] > 2) sm_op<Y, SM.op0[T] + 2>();
  if constexpr (T == SM.last + 1) asm volatile("v_add_f32_e32 v%c0, v%c1, v%c2" ::"i"(R_TMP), "i"(R_TS), "i"(R_TS + 1));
  if constexpr (T == SM.last + 2) asm volatile("v_cmp_nle_f32_e64 s[54:55], v%c0, v%c1" ::"i"(R_TMP), "i"(R_THR));
}
#define W_A0 1
template <int X, int MODE, int G> DI void gapA(AC& c) {
  constexpr bool FIRST = MODE == 1 || MODE == 2, NOQK = MODE == 3;
  constexpr int Xp = X ^ 1, kb = G >> 4, g = G & 15, qb = g >> 3, s = g & 7;
  if constexpr (MODE == 3 && G < 48 && G % 3 == 0) {
    constexpr int f = G / 3;
    asm volatile("buffer_load_dwordx4 a[%c0:%c1], v84, s[88:91], s75 offen offset:%2 sc1" ::"i"(128 + 4 * f), "i"(131 + 4 * f), "i"((f & 3) * 1024) : "memory");
    if constexpr ((f & 3) == 3) asm volatile("s_addk_i32 s75, %0" ::"i"(f == 7 ? 28672 : 4096));
  }
  if constexpr (G == 48) asm volatile("s_waitcnt vmcnt(%0) lgkmcnt(0)\n\ts_barrier" ::"i"(MODE == 3 ? 16 : MODE == 2 ? 12 : MODE == 1 ? W_A0 : 0) : "memory");
  else if constexpr (g == 0 && !NOQK) asm volatile("s_waitcnt lgkmcnt(0)");
  if constexpr (!NOQK) {
    constexpr int r = 192 + 4 * ((kb & 1) * 8 + s), q = 128 + 4 * g;
    constexpr int d = R_S + 32 * X + 4 * (kb * 2 + qb), ci = s == 0 ? R_CI + 4 * (kb * 2 + qb) : d;
    if constexpr (FIRST && s == 0)
      asm volatile("v_mfma_f32_16x16x32_bf16 v[%c0:%c1], a[%c2:%c3], a[%c4:%c5], 0" ::"i"(d), "i"(d + 3), "i"(r), "i"(r + 3), "i"(q), "i"(q + 3));
    else
      asm volatile("v_mfma_f32_16x16x32_bf16 v[%c0:%c1], a[%c2:%c3], a[%c4:%c5], v[%c6:%c7]" ::"i"(d), "i"(d + 3), "i"(r), "i"(r + 3), "i"(q), "i"(q + 3),
                   "i"(ci), "i"(ci + 3));
    if constexpr (g < 8 && kb < 3) kread<X, (kb + 1) * 8 + g>();
    constexpr int S0 = SMV ? 40 : 14, S1 = SMV ? 41 : 30, S2 = SMV ? 42 : 45, S3 = SMV ? 43 : 46;
    if constexpr (G == S0) asm volatile("s_add_u32 s96, s97, 0x1000000");
    if constexpr (G == S1) dma_m0<X, 0>();
    if constexpr (G == S2) asm volatile("s_mov_b32 s97, s98");
    if constexpr (G == S3) asm volatile("s_add_u32 s98, s98, s99");
    if constexpr (G >= 56 && !(G & 1)) dma_at<(G - 56) / 2>();
    if constexpr (G == 57) asm volatile("s_lshr_b32 s100, s100, 1\n\ts_add_u32 s56, s56, 1");
    if constexpr (G == 59 && !FIRST) asm volatile("s_and_b32 s49, s100, 1\n\ts_or_b32 s49, s49, s54");
    if constexpr (G == 61 && !FIRST) asm volatile("s_or_b32 %0, s49, s55" : "=s"(c.rare));
    if constexpr (G == 61 && FIRST) asm volatile("s_and_b32 %0, s100, 1" : "=s"(c.rare));
  } else if constexpr (SM.nop[64 + G] > 0) asm volatile("s_nop 0");
  if constexpr (!FIRST && g < 8 && kb == 3) vread<Xp, g>();
  if constexpr (MODE == 2 && (G & 3) == 1) d_store<16 + G / 4>();
  if constexpr (FIRST) acc_zero2<G>();
  else sm_gap<Xp, 64 + G>(c);
  GAP();
}
template <int R> DI void rescale1(float alpha) {
  float t;
  asm volatile("v_accvgpr_read_b32 %0, a[%c2]\n\tv_mul_f32 %0, %0, %1\n\tv_accvgpr_write_b32 a[%c2], %0" : "=&v"(t) : "v"(alpha), "i"(R));
}
template <int... Rs> DI void rescale_all(const float (&alpha)[2], iseq<Rs...>) { (rescale1<Rs>(alpha[(Rs >> 2) & 1]), ...); }
template <bool MAX> DI float row_reduce(float v) {
  auto p = __builtin_amdgcn_permlane32_swap(__float_as_uint(v), __float_as_uint(v), false, false);
  v = MAX ? fmaxf(__uint_as_float(p[0]), __uint_as_float(p[1])) : __uint_as_float(p[0]) + __uint_as_float(p[1]);
  p = __builtin_amdgcn_permlane16_swap(__float_as_uint(v), __float_as_uint(v), false, false);
  return MAX ? fmaxf(__uint_as_float(p[0]), __uint_as_float(p[1])) : __uint_as_float(p[0]) + __uint_as_float(p[1]);
}
template <int Y, int QB> DI float row_max() {
  constexpr int r = R_S + 32 * Y + 4 * QB;
  float mx;
  asm volatile("v_max_f32 %0, v%c1, v%c2\n\tv_max3_f32 %0, %0, v%c3, v%c4" : "=&v"(mx) : "i"(r), "i"(r + 1), "i"(r + 2), "i"(r + 3));
  static_for([&](auto K) {
    constexpr int q = r + 8 * (K + 1);
    float t = mx;
    asm volatile("v_max3_f32 %0, %0, v%c1, v%c2\n\tv_max3_f32 %0, %0, v%c3, v%c4" : "+v"(t) : "i"(q), "i"(q + 1), "i"(q + 2), "i"(q + 3));
    mx = t;
  }, make_iseq<3>{});
  return row_reduce<true>(mx);
}
template <int R> DI void v_sub(float d) { asm volatile("v_sub_f32 v%c0, v%c0, %1" ::"i"(R), "v"(d)); }
template <int B> DI void s_shift(const float (&d)[2]) { static_for([&](auto E) { v_sub<B + E>(d[(E >> 2) & 1]); }, make_iseq<32>{}); }
DI void m_shift(const float (&d)[2]) {
  s_shift<R_CI>(d);
  v_sub<R_M>(-d[0]); v_sub<R_M + 1>(-d[1]);
}
DI void set_mask0() {
  const int mr = __float_as_int(vget<89>());
  static_for([&](auto I) {
    constexpr int e = (I >> 2) * 8 + (I & 3), k = (I >> 2) * 16 + (I & 3);
    const float x = vget<R_S + e>();
    vset<R_S + e>(k > mr ? -INFINITY : x);
  }, make_iseq<16>{});
}
template <int X> DI void sm_redo() {
  constexpr int Xp = X ^ 1;
  float d[2], dt[2], alpha[2], ts[2] = {0.f, 0.f};
  static_for([&](auto Q) {
    constexpr int qb = Q;
    const float pd = vget<R_PD + qb>();
    d[qb] = fmaxf(row_max<Xp, qb>() - pd, 0.f);
    dt[qb] = d[qb] + pd;
    vset<R_PD + qb>(d[qb]);
    alpha[qb] = __builtin_amdgcn_exp2f(-d[qb]);
    vset<R_RS + qb>(vget<R_RS + qb>() * alpha[qb]);
  }, make_iseq<2>{});
  vset<R_THR>(d[0] > 0.f || d[1] > 0.f ? -1.f : 4096.f);
  m_shift(d);
  static_for([&](auto K) {
    constexpr int k = K, qb = (k >> 1) & 1;
    const float e0 = __builtin_amdgcn_exp2f(vget<R_S + 32 * Xp + 2 * k>() - dt[qb]), e1 = __builtin_amdgcn_exp2f(vget<R_S + 32 * Xp + 2 * k + 1>() - dt[qb]);
    ts[qb] += e0; ts[qb] += e1;
    vset<p_reg(Xp, k)>(__uint_as_float(pk2(e0, e1)));
  }, make_iseq<16>{});
  vset<R_TS>(ts[0]); vset<R_TS + 1>(ts[1]);
  rescale_all(alpha, make_iseq<128>{});
  asm volatile("s_nop 1" ::: "memory");
  GAP();
}
template <int X, int MODE, int... Gs> DI void phaseA_(AC& c, iseq<Gs...>) { (gapA<X, MODE, Gs>(c), ...); }
template <int X, int MODE> DI void phaseA(AC& c) { phaseA_<X, MODE>(c, make_iseq<64>{}); }

template <int X, bool SMX, int G> DI void gapB(AC& c) {
  constexpr int Xp = X ^ 1, blk = G >> 4, g = G & 15;
  if constexpr (g == 0) asm volatile("s_waitcnt lgkmcnt(0)");
  constexpr int qb = (g >> 1) & 1, s = g & 1, r = 192 + 4 * ((blk & 1) * 8 + (g >> 2) * 2 + s), o = 4 * ((blk * 4 + (g >> 2)) * 2 + qb);
  constexpr int p = R_P + 16 * Xp + 4 * (s * 2 + qb);
  asm volatile("v_mfma_f32_16x16x32_bf16 a[%c0:%c1], a[%c2:%c3], v[%c4:%c5], a[%c0:%c1]" ::"i"(o), "i"(o + 3), "i"(r), "i"(r + 3), "i"(p), "i"(p + 3));
  if constexpr (g < 8) {
    if constexpr (blk < 3) vread<Xp, (blk + 1) * 8 + g>();
    else if constexpr (SMX) kread<Xp, g>();
  }
  if constexpr (SMX) {
    if constexpr (G == 1 || G == 2) asm volatile("v_add_f32_e32 v%c0, v%c0, v%c1" ::"i"(R_RS + G - 1), "i"(R_TS + G - 1));
    if constexpr (g == 7 && blk < 3) dma_m0<X, blk * 4 + 4>();
    if constexpr (g >= 8 && !(g & 1) && blk < 3) dma_at<blk * 4 + 4 + (g - 8) / 2>();
    if constexpr (G == 55) asm volatile("s_cmp_eq_u32 s56, s76");
    if constexpr (G == 3) asm volatile("s_lshr_b32 s77, s77, 1");
    if constexpr (G == 4) asm volatile("s_bitcmp1_b32 s77, 0");
    if constexpr (G == 5) asm volatile("s_cselect_b64 s[52:53], s[62:63], 0");
    if constexpr (G == 6) asm volatile(FL_ARM("s[52:53]"));
    if constexpr (G == 56) asm volatile(FL_POLL ::: "memory");
    sm_gap<X, G>(c);
  }
  GAP();
}
template <int X, int... Gs> DI void phaseB_(AC& c, iseq<Gs...>) { (gapB<X, true, Gs>(c), ...); }
template <int X> DI void phaseB(AC& c) { phaseB_<X>(c, make_iseq<64>{}); }
constexpr int sm_nex() { int n = 0; for (int t = 0; t < 64; ++t) n += SM.ex[t] >= 0; return n; }
template <int X> DI void phaseB0(unsigned mode) {
  dma_m0<X, 4>();
  kreads<X ^ 1>(make_iseq<8>{});
  dma_at<4>();
  asm volatile("s_nop 3");
  float d[2];
  if (mode & 2) {
    d[0] = vget<122>(); d[1] = vget<123>();
    dma_at<5>();
  } else {
    if constexpr (X == 0) if (__builtin_expect(mode & 4, 0)) set_mask0();
    d[0] = row_max<X, 0>(); GAP(); dma_at<5>();
    d[1] = row_max<X, 1>();
#pragma unroll
    for (int qb = 0; qb < 2; ++qb) if (!(d[qb] > -3.0e38f)) d[qb] = 0.f;
  }
  GAP();
  vset<R_M>(d[0]); vset<R_M + 1>(d[1]);
  dma_at<6>();
  const f32x2c nd[2] = {{-d[0], -d[0]}, {-d[1], -d[1]}};
  static_for([&](auto E) {
    constexpr int e = E;
    const f32x2c v = nd[(e >> 1) & 1];
    asm volatile("v_pk_add_f32 v[%c0:%c1], v[%c0:%c1], %4\n\tv_mov_b64 v[%c2:%c3], %4" ::"i"(R_S + 32 * X + 2 * e), "i"(R_S + 32 * X + 2 * e + 1), "i"(R_CI + 2 * e), "i"(R_CI + 2 * e + 1), "v"(v));
    if constexpr (e == 3) dma_at<7>();
    if constexpr (e == 6) dma_m0<X, 8>();
    if constexpr (e == 7 || e == 11 || e == 15) dma_at<8 + (e - 7) / 4>();
  }, make_iseq<16>{});
  static_assert(SM.op0[64] >= 3 * ((sm_nex() - 15) / 2) && SM.op0[64] <= 24);
  static_for([&](auto E) {
    constexpr int e = E;
    if constexpr (e < sm_nex()) asm volatile("v_exp_f32 v%c0, v%c1" ::"i"(R_E + e), "i"(R_S + 32 * X + e));
    if constexpr (e == 3) dma_at<11>();
    if constexpr (e == 6) dma_m0<X, 12>();
    if constexpr (e == 7 || e == 11 || e == 15) dma_at<12 + (e - 7) / 4>();
  }, make_iseq<16>{});
  static_for([&](auto O) { sm_op<X, O>(); }, make_iseq<SM.op0[64]>{});
  dma_at<15>();
  static_for([&](auto E) { asm volatile("v_exp_f32 v%c0, v%c1" ::"i"(R_E + (int)E), "i"(R_S + 32 * X + 16 + (int)E)); }, make_iseq<(sm_nex() > 16 ? sm_nex() - 16 : 0)>{});
  asm volatile("buffer_store_dwordx2 v[%c0:%c1], v88, s[88:91], s76 offen sc1\n\ts_bitcmp1_b32 s77, 0\n\ts_cselect_b64 s[52:53], s[62:63], 0\n\t"
               FL_ARM("s[52:53]") "\n\t" FL_POLL ::"i"(R_M), "i"(R_M + 1) : "memory", "scc");
  GAP();
}
constexpr int T_ZO = 8;
#define MSK(R, K) "v_cmp_gt_i32 s[48:49], " #K ", v89\n\tv_cmp_gt_i32 s[50:51], " #K "+1, v89\n\tv_cmp_gt_i32 s[52:53], " #K "+2, v89\n\t" \
                  "v_cmp_gt_i32 s[54:55], " #K "+3, v89\n\tv_cndmask_b32_e64 v[" #R "], v[" #R "], v122, s[48:49]\n\t" \
                  "v_cndmask_b32_e64 v[" #R "+1], v[" #R "+1], v122, s[50:51]\n\tv_cndmask_b32_e64 v[" #R "+2], v[" #R "+2], v122, s[52:53]\n\t" \
                  "v_cndmask_b32_e64 v[" #R "+3], v[" #R "+3], v122, s[54:55]\n\t"
DI void ev_code() {
  asm volatile(
    "s_getpc_b64 s[82:83]\n\ts_add_u32 s82, s82, 12\n\ts_addc_u32 s83, s83, 0\n\ts_branch 9f\n\t"
    "s_bitcmp0_b32 s58, s56\n\ts_cbranch_scc1 1f\n\t" FL_SET("s70") "\n\t" FL_SET("s71") "\n"
    "1:\n\ts_bitcmp0_b32 s57, s56\n\ts_cbranch_scc1 2f\n\t" FL_CHECK("s[62:63]") "\n\t"
    "s_add_u32 s98, s98, s68\n\ts_mov_b32 s68, s69\n\ts_mov_b32 s69, 0\n\ts_mov_b64 s[62:63], s[64:65]\n\ts_mov_b64 s[64:65], s[66:67]\n"
    "2:\n\ts_bitcmp0_b32 s59, s56\n\ts_cbranch_scc1 3f\n\ts_mov_b32 s98, s72\n\ts_bitcmp0_b32 s73, 0\n\ts_cbranch_scc1 3f\n\t"
    "v_lshrrev_b32 v90, 7, v86\n\tv_and_b32 v91, 6, v90\n\tv_lshlrev_b32 v91, 4, v91\n\tv_and_b32 v92, 0x70, v86\n\tv_xor_b32 v91, v91, v92\n\t"
    "s_movk_i32 s48, 896\n\tv_mad_u32_u24 v94, v90, s48, v91\n\ts_mov_b64 s[92:93], s[80:81]\n\ts_mov_b32 s94, 229376\n\t"
    "v_add_u32 v90, 28672, v94\n\tv_add_u32 v91, 34816, v94\n\tv_add_u32 v92, 40960, v94\n\tv_add_u32 v93, 47104, v94\n\t"
    "v_add_u32 v95, 6144, v94\n\tv_add_u32 v126, 12288, v94\n\tv_add_u32 v127, 18432, v94\n"
    "3:\n\ts_bitcmp0_b32 s60, s56\n\ts_cbranch_scc1 4f\n\t"
    "v_readlane_b32 s54, v78, %0\n\ts_add_u32 m0, s78, 0x10000\n\t"
    "s_lshr_b32 s53, s73, 16\n\ts_mulk_i32 s53, 3584\n\ts_mov_b64 s[48:49], s[80:81]\n\ts_add_u32 s50, s53, 3584\n\ts_mov_b32 s51, 0x20000\n\t"
    "v_lshlrev_b32 v121, 3, v84\n\tbuffer_load_dword v121, s[48:51], s53 offen lds\n\t"
    "s_bitcmp0_b32 s73, 3\n\ts_cbranch_scc1 4f\n\t"
    "v_lshlrev_b32 v121, 9, v84\n\tv_and_b32 v121, 0x38000, v121\n\tv_lshrrev_b32 v122, 9, v84\n\tv_lshl_add_u32 v121, v122, 12, v121\n\t"
    "v_lshrrev_b32 v122, 3, v84\n\tv_xor_b32 v122, v122, v84\n\tv_and_or_b32 v121, v122, 48, v121\n\t"
    "buffer_load_dwordx4 v121, s[88:91], s54 offen sc1 lds\n\ts_addk_i32 s54, 7168\n\t"
    "buffer_load_dwordx4 v121, s[88:91], s54 offen offset:1024 sc1 lds\n\ts_addk_i32 s54, 7168\n\t"
    "buffer_load_dwordx4 v121, s[88:91], s54 offen offset:2048 sc1 lds\n\ts_addk_i32 s54, 7168\n\t"
    "buffer_load_dwordx4 v121, s[88:91], s54 offen offset:3072 sc1 lds\n"
    "4:\n\ts_bitcmp0_b32 s61, s56\n\ts_cbranch_scc1 6f\n\t"
    "v_mov_b32 v122, 0xff800000\n\ts_cmp_lg_u32 s74, 0\n\ts_mov_b32 s74, 1\n\ts_cbranch_scc1 5f\n\t"
    MSK(224, 0) MSK(232, 16) MSK(240, 32) MSK(248, 48) "s_setpc_b64 s[86:87]\n"
    "5:\n\t" MSK(228, 0) MSK(236, 16) MSK(244, 32) MSK(252, 48)
    "6:\n\ts_setpc_b64 s[86:87]\n9:"
    ::"i"(T_ZO) : "scc");
}
DI void ev_call() { asm volatile("s_swappc_b64 s[86:87], s[82:83]" ::: "vcc", "scc", "memory"); GAP(); }

#define ACLOB_ALL "a0","a1","a2","a3","a4","a5","a6","a7","a8","a9","a10","a11","a12","a13","a14","a15","a16","a17","a18","a19","a20","a21","a22","a23","a24","a25","a26","a27","a28","a29","a30","a31","a32","a33","a34","a35","a36","a37","a38","a39","a40","a41","a42","a43","a44","a45","a46","a47","a48","a49","a50","a51","a52","a53","a54","a55","a56","a57","a58","a59","a60","a61","a62","a63","a64","a65","a66","a67","a68","a69","a70","a71","a72","a73","a74","a75","a76","a77","a78","a79","a80","a81","a82","a83","a84","a85","a86","a87","a88","a89","a90","a91","a92","a93","a94","a95","a96","a97","a98","a99","a100","a101","a102","a103","a104","a105","a106","a107","a108","a109","a110","a111","a112","a113","a114","a115","a116","a117","a118","a119","a120","a121","a122","a123","a124","a125","a126","a127","a128","a129","a130","a131","a132","a133","a134","a135","a136","a137","a138","a139","a140","a141","a142","a143","a144","a145","a146","a147","a148","a149","a150","a151","a152","a153","a154","a155","a156","a157","a158","a159","a160","a161","a162","a163","a164","a165","a166","a167","a168","a169","a170","a171","a172","a173","a174","a175","a176","a177","a178","a179","a180","a181","a182","a183","a184","a185","a186","a187","a188","a189","a190","a191","a192","a193","a194","a195","a196","a197","a198","a199","a200","a201","a202","a203","a204","a205","a206","a207","a208","a209","a210","a211","a212","a213","a214","a215","a216","a217","a218","a219","a220","a221","a222","a223","a224","a225","a226","a227","a228","a229","a230","a231","a232","a233","a234","a235","a236","a237","a238","a239","a240","a241","a242","a243","a244","a245","a246","a247","a248","a249","a250","a251","a252","a253","a254","a255"

struct EP { float fa[2], fb[2]; };
template <int I, int E, int MODE> DI void epi_pair(const EP& p) {
  constexpr int qb = I & 1, r = 16 * (I >> 1) + 4 * qb + (E >> 1) * 8 + (E & 1) * 2, t = 96 + 2 * E, w = 128 + 4 * I + E;
  if constexpr (MODE == 2) asm volatile("v_accvgpr_read_b32 v106, a[%c0]" ::"i"(w));
  asm volatile("v_accvgpr_read_b32 v%c0, a[%c2]\n\tv_accvgpr_read_b32 v%c1, a[%c3]" ::"i"(t), "i"(t + 1), "i"(r), "i"(r + 1));
  if constexpr (MODE) asm volatile("v_mul_f32 v%c0, v%c0, %2\n\tv_mul_f32 v%c1, v%c1, %2" ::"i"(t), "i"(t + 1), "v"(p.fa[qb]));
  if constexpr (MODE == 2)
    asm volatile("v_lshlrev_b32 v104, 16, v106\n\tv_and_b32 v105, 0xffff0000, v106\n\tv_fmac_f32 v%c0, v104, %2\n\tv_fmac_f32 v%c1, v105, %2"
                 ::"i"(t), "i"(t + 1), "v"(p.fb[qb]));
  asm volatile("v_cvt_pk_bf16_f32 v%c0, v%c1, v%c2" ::"i"(w), "i"(t), "i"(t + 1));
}
template <int I> DI void epi_store() {
  asm volatile("buffer_store_dwordx4 v[%c0:%c1], v84, s[88:91], s101 offen offset:%2 sc1" ::"i"(128 + 4 * I), "i"(131 + 4 * I), "i"((I & 3) * 1024) : "memory");
  if constexpr ((I & 3) == 3) asm volatile("s_addk_i32 s101, 4096");
}
constexpr int R_TB = 107, R_VS = 111;
template <int HF, int R> DI void p_read() {
  constexpr int mb = R >> 4, r = 192 - 64 * HF + 2 * R;
  asm volatile("ds_read_b64_tr_b16 a[%c1:%c2], v%c0 offset:%c3" ::"i"(R_TB + 2 * HF + mb), "i"(r), "i"(r + 1), "i"((R & 15) * 2048));
}
template <int QB, int E> DI void p_cvt() {
  asm volatile("v_cvt_pk_bf16_f32 v%c0, v%c1, v%c2" ::"i"(208 + 4 * QB + E), "i"(192 + 8 * QB + 2 * E), "i"(193 + 8 * QB + 2 * E));
}
template <int QB, int PR> DI void p_store() {
  asm volatile("buffer_store_dwordx4 v[%c0:%c1], v%c2, s[88:91], s96 offen offset:%3 sc1" ::"i"(208 + 4 * QB), "i"(211 + 4 * QB), "i"(R_VS + QB),
               "i"(PR * 128) : "memory");
}
constexpr int p_dma(int HF, int G) { return G == 3 && !HF ? 0 : G == 11 ? 1 - HF : G == 15 ? 2 - HF : G == 19 ? 3 - HF : G == 27 && HF ? 3 : -1; }
template <int HF, int G> DI void p_gap() {
  constexpr int ch = G >> 3, t = G & 7, a = 192 - 64 * HF + 32 * (ch & 1) + 4 * t, b = 128 + 4 * (2 * t + (ch >> 1)), d = 192 + 4 * ch;
  if constexpr (t == 0)
    asm volatile("v_mfma_f32_16x16x32_bf16 v[%c0:%c1], a[%c2:%c3], v[%c4:%c5], 0" ::"i"(d), "i"(d + 3), "i"(a), "i"(a + 3), "i"(b), "i"(b + 3));
  else
    asm volatile("v_mfma_f32_16x16x32_bf16 v[%c0:%c1], a[%c2:%c3], v[%c4:%c5], v[%c0:%c1]" ::"i"(d), "i"(d + 3), "i"(a), "i"(a + 3), "i"(b), "i"(b + 3));
  p_read<HF ^ 1, G>();
  if constexpr (G == 0) asm volatile("v_xor_b32 v%c0, s100, v%c0" ::"i"(R_TB + 2 * HF + 1));
  if constexpr (G == 17) asm volatile("v_xor_b32 v%c0, s100, v%c0" ::"i"(R_TB + 2 * (HF ^ 1)));
  if constexpr (G >= 4 && G <= 8 && G != 7) p_cvt<1, (G < 7 ? G - 4 : 3)>();
  if constexpr (G == 24 || (G == 7 && HF == 0)) asm volatile("s_waitcnt lgkmcnt(8)");
  if constexpr (G == 7 && HF == 1) asm volatile("s_waitcnt vmcnt(13) lgkmcnt(8)\n\ts_barrier" ::: "memory");
  if constexpr (G == 10) p_store<1, HF ^ 1>();
  if constexpr (G >= 20 && G < 24) p_cvt<0, G - 20>();
  if constexpr (G == 25) p_store<0, HF>();
  if constexpr (p_dma(HF, G) >= 0) dma_at<(HF ? 8 : 12) + p_dma(HF, G)>();
  if constexpr (HF == 0 && G == 2) asm volatile("s_bitset0_b32 m0, 12");
  if constexpr (HF == 0 && G == 13) asm volatile("s_add_u32 s96, s96, 0x40000");
  if constexpr (HF == 0 && G == 26) asm volatile("s_add_u32 s98, s98, 128");
  if constexpr (HF == 0 && G == 27) asm volatile("s_cmp_lt_u32 s98, 0x380");
  if constexpr (HF == 0 && G == 28) asm volatile("s_cselect_b32 s99, s99, 0x31000");
  if constexpr (HF == 1 && G == 1) asm volatile("s_xor_b32 s100, s100, 0x10000");
  if constexpr (HF == 1 && G == 2) asm volatile("s_xor_b32 s97, s97, s100");
  if constexpr (HF == 1 && G == 9) asm volatile("s_or_b32 m0, s97, s99");
  if constexpr (HF == 1 && G == 30) asm volatile("s_cmp_lt_u32 s98, 0x500");
  GAP();
}
template <int HF, int... Gs> DI void p_half(iseq<Gs...>) { (p_gap<HF, Gs>(), ...); }
template <int G> DI void gapF(AC& c, const EP& p) {
  if constexpr (G < 64) gapB<0, false, G>(c);
  if constexpr (G >= 10) epi_pair<(G - 10) / 4, (G - 10) & 3, 0>(p);
  if constexpr (G >= 14 && ((G - 14) & 3) == 0) epi_store<(G - 14) / 4>();
  if constexpr (G == 73) epi_store<15>();
  GAP();
}
template <int... Gs> DI void phaseF(AC& c, const EP& p, iseq<Gs...>) { (gapF<Gs>(c, p), ...); }
template <int G> DI void gapAL(AC& c) {
  constexpr int kb = G >> 3, s = G & 7, r = 192 + 4 * (G & 15), q = 160 + 4 * s, d = R_S + 36 + 8 * kb, ci = s == 0 ? R_CI + 4 + 8 * kb : d;
  if constexpr (G == 0) asm volatile("s_add_u32 s96, s97, 0x1000000\n\ts_mov_b32 m0, s79\n\ts_waitcnt vmcnt(9) lgkmcnt(0)\n\ts_barrier" ::: "memory");
  if constexpr (G >= 8 && (G & 3) == 0) asm volatile("s_waitcnt lgkmcnt(4)");
  asm volatile("v_mfma_f32_16x16x32_bf16 v[%c0:%c1], a[%c2:%c3], a[%c4:%c5], v[%c6:%c7]" ::"i"(d), "i"(d + 3), "i"(r), "i"(r + 3), "i"(q), "i"(q + 3),
               "i"(ci), "i"(ci + 3));
  if constexpr (G < 24) kread<1, G + 8>();
  else vread<0, G - 24>();
  if constexpr ((G & 3) == 1) dma_at<G / 4>();
  if constexpr (G == 15) asm volatile("s_bitset1_b32 m0, 12");
  if constexpr (G == 31) asm volatile("buffer_load_dwordx4 v[122:125], v88, s[88:91], s76 offen sc1" ::: "memory");
  sm_gap<0, 64 + 2 * G>(c); sm_gap<0, 65 + 2 * G>(c);
  GAP();
}
template <int... Gs> DI void phaseAL(AC& c, iseq<Gs...>) { (gapAL<Gs>(c), ...); }
constexpr int bl_ex(int G) { int n = 0; for (int t = 0; t < G; ++t) { const int g = t & 15; n += t == 6 || (t < 48 && g > 8 && g != 12 && g != 15); } return n; }
constexpr int bl_op(int G) {
  int o = 1;
  for (int t = 15; t < G; ++t)
    if (o < 24 && (t & 3) != 0 && t != 47 && bl_ex(t + 1) == bl_ex(t) && bl_ex(t - 1) > 2 * (o / 3) + (o % 3 != 0)) ++o;
  return o;
}
static_assert(bl_ex(64) == 16 && bl_op(61) == 24);
template <bool LD, int G> DI void gapBL(AC& c) {
  if constexpr (G == 48) asm volatile("s_waitcnt vmcnt(%0) lgkmcnt(0)\n\ts_barrier" ::"i"(LD ? 12 : 0) : "memory");
  gapB<1, false, G>(c);
  if constexpr (G >= 48 && G < 56) vread<1, G - 48>();
  if constexpr (LD && (G & 3) == 0) {
    constexpr int k = G >> 2, I = 2 * (k & 7) + (k >> 3);
    asm volatile("buffer_load_dwordx4 a[%c0:%c1], v84, s[88:91], s75 offen offset:%2 sc1" ::"i"(128 + 4 * I), "i"(131 + 4 * I), "i"((I & 3) * 1024) : "memory");
    if constexpr (k & 1) asm volatile("s_addk_i32 s75, %0" ::"i"(k == 7 ? -12288 : 4096));
  }
  if constexpr (G == 1 || G == 2) asm volatile("v_add_f32_e32 v%c0, v%c0, v%c1" ::"i"(R_RS + G - 1), "i"(R_TS + G - 1));
  if constexpr (G == 3) asm volatile("v_mov_b32 v%c0, 0" ::"i"(R_TS));
  if constexpr (bl_ex(G + 1) > bl_ex(G)) {
    constexpr int j = bl_ex(G);
    asm volatile("v_exp_f32 v%c0, v%c1" ::"i"(R_E + j), "i"(R_S + 36 + 8 * (j >> 2) + (j & 3)));
  }
  if constexpr (bl_op(G + 1) > bl_op(G)) {
    constexpr int o = bl_op(G), k = o / 3, j = 2 * k + o % 3;
    if constexpr (o % 3 == 2) asm volatile("v_cvt_pk_bf16_f32 v%c0, v%c1, v%c2" ::"i"(p_reg(1, (k >> 1) * 4 + 2 + (k & 1))), "i"(R_E + 2 * k), "i"(R_E + 2 * k + 1));
    else asm volatile("v_add_f32_e32 v%c0, v%c1, v%c2" ::"i"(R_TS + 1), "i"(j == 1 ? R_E : R_TS + 1), "i"(R_E + j));
  }
  if constexpr (G == 61) asm volatile("v_cmp_nle_f32_e64 s[54:55], v%c0, v%c1" ::"i"(R_TS + 1), "i"(R_THR));
  GAP();
}
template <bool LD, int... Gs> DI void phaseBL(AC& c, iseq<Gs...>) { (gapBL<LD, Gs>(c), ...); }
template <int MODE, int G> DI void gapFL(const EP& p) {
  constexpr int x = G - (MODE == 3 ? 4 : 0), J = G < 32 ? ((G & 1) ? G >> 1 : -1) : (G & 7) == 4 && G < 64 ? 16 + (G - 36) / 8 : -1;
  if constexpr (G == 0) asm volatile("s_xor_b32 m0, s79, 0x11000\n\ts_movk_i32 s98, 128\n\ts_waitcnt vmcnt(%0) lgkmcnt(0)" ::"i"(MODE > 1 ? 9 : 63) : "memory");
  if constexpr (G >= 8 && G < 32 && (G & 3) == 0) asm volatile("s_waitcnt vmcnt(%0) lgkmcnt(%1)" ::"i"(MODE > 1 && G == 16 ? 9 : 63), "i"(G < 28 ? 4 : 0) : "memory");
  if constexpr (G == 32) asm volatile("s_barrier\n\ts_or_b32 m0, s79, 0x1000\n\ts_movk_i32 s98, 384" ::: "memory");
  if constexpr (G < 32) {
    constexpr int o = 8 * (G >> 1) + 4, r = 192 + 4 * (G & 15), q = R_P + 20 + 8 * (G & 1);
    asm volatile("v_mfma_f32_16x16x32_bf16 a[%c0:%c1], a[%c2:%c3], v[%c4:%c5], a[%c0:%c1]" ::"i"(o), "i"(o + 3), "i"(r), "i"(r + 3), "i"(q), "i"(q + 3));
  }
  if constexpr (G < 24) vread<1, G + 8>();
  if constexpr (G >= 32 && G < 64) p_read<0, G - 32>();
  if constexpr (J >= 0) dma_at<8 + (J & 7)>();
  if constexpr (x >= 0) epi_pair<2 * ((x >> 2) & 7) + (x >> 5), x & 3, MODE>(p);
  if constexpr (MODE == 3 && G < 32) {
    constexpr int db = G & 15, qb = G >> 4, o = 4 * (db * 2 + qb), a = 224 + 4 * (db & 1), b = 128 + 4 * ((db >> 1) * 2 + qb);
    asm volatile("v_mfma_f32_16x16x32_bf16 a[%c0:%c1], v[%c2:%c3], a[%c4:%c5], a[%c0:%c1]" ::"i"(o), "i"(o + 3), "i"(a), "i"(a + 3), "i"(b), "i"(b + 3));
  }
  if constexpr (J == 3 || J == 11) asm volatile("s_bitset0_b32 m0, 12");
  if constexpr (J == 7) asm volatile("s_or_b32 m0, s78, 0x1000\n\ts_movk_i32 s98, 256");
  if constexpr (x == 63) asm volatile("s_waitcnt vmcnt(4) lgkmcnt(0)\n\ts_barrier" ::: "memory");
  GAP();
}
template <int MODE, int... Gs> DI void phaseFL_(const EP& p, iseq<Gs...>) { (gapFL<MODE, Gs>(p), ...); }
template <int MODE> DI void phaseFL(const EP& p) { GAP(); phaseFL_<MODE>(p, make_iseq<MODE == 3 ? 68 : 64>{}); }
template <int U, int K> DI void r_load(unsigned vo, unsigned so) {
  asm volatile("buffer_load_dwordx4 a[%c0:%c1], %2, s[88:91], %3 offen offset:%4 sc1" ::"i"(64 * U + 4 * K), "i"(64 * U + 4 * K + 3), "v"(vo),
               "s"(so + (K >> 2) * 4096), "i"((K & 1) * 512 + ((K >> 1) & 1) * 2048) : "memory");
}
template <int U, int K> DI void r_sum() {
  constexpr int d = 100 + 4 * (K & 1), b = 64 * U + 4 * K;
  if constexpr (K < 2) asm volatile("v_mfma_f32_16x16x32_bf16 v[%c0:%c1], v[96:99], a[%c2:%c3], 0" ::"i"(d), "i"(d + 3), "i"(b), "i"(b + 3));
  else asm volatile("v_mfma_f32_16x16x32_bf16 v[%c0:%c1], v[96:99], a[%c2:%c3], v[%c0:%c1]" ::"i"(d), "i"(d + 3), "i"(b), "i"(b + 3));
}
template <int U> DI void r_fin(unsigned vo, __amdgpu_buffer_rsrc_t srd, unsigned so) {
  asm volatile("" : "+s"(srd), "+s"(so));
  static_for([&](auto K) { r_sum<U, K>(); }, make_iseq<16>{});
  asm volatile("s_nop 7");
  static_for([&](auto E) { asm volatile("v_cvt_pk_bf16_f32 v%c0, v%c1, v%c2" ::"i"(116 + 4 * U + E), "i"(100 + 2 * E), "i"(101 + 2 * E)); }, make_iseq<4>{});
  asm volatile("buffer_store_dwordx4 v[%c3:%c4], %0, %1, %2 offen" OUTSCS ::"v"(vo), "s"(srd), "s"(so), "i"(116 + 4 * U), "i"(119 + 4 * U) : "memory");
}

#define CMAX 12
#define PREPE 0
static_assert(NYT <= 16 && NYT >= 12);
enum { T_Q0, T_K0, T_OQ, T_OG, T_FPREP, T_FQ, T_FG, T_FS,   T_CNT = 9, T_NPRE = 10, T_NST = 12, T_TAB = 14, T_SLAB = 16, T_PART, T_IT = 18, T_ITN = 23 };
struct TRow { unsigned v[64]; };
constexpr u64 kvmask(int m0, int m1) { u64 n = 0; for (int m = m0; m <= m1; ++m) n |= 0x101ull << m; return n; }
constexpr u64 k_order(bool isY, int rr) {
  const int cls = (rr + (rr >> 2)) & 3;
  return !isY ? (cls == 0 ? 0xfedcb9875a463210ull : cls == 1 ? 0xefadc6982b574310ull : cls == 2 ? 0xfebad7693c285410ull : 0xefcba8764d392510ull)
              : (cls == 0 ? 0xfedcb987543a6210ull : cls == 1 ? 0xefadc698254b7310ull : cls == 2 ? 0xfebad769325c8410ull : 0xefcba876432d9510ull);
}
constexpr TRow t_row(int r, int wid, int j) {
  TRow t{};
  const int rr = r & 15, hsel = rr & 1, pj = rr >> 1, kv = pj & 1, h = j + 8 * hsel, u = hsel * 8 + j;
  const bool isY = r >= 16, solo = pj == 7;
  const int mt = (rr >> 2) + (isY ? 4 : 0), g = j * 128, NY = solo ? 18 : NYT;
  const int nx0 = 32 - 2 * pj - NY, cx = nx0 < CMAX ? nx0 : CMAX, ex = nx0 - cx, qbF = isY != (solo && SWAP7) ? 15 - pj : pj;
  const unsigned oQ = (unsigned)h << 20, oK = oQ + O_K, oP = O_P + (h * 8 + pj) * PSTR;
  if (wid == 4) {
    for (int l = 0; l < 64; ++l)
      t.v[l] = fo(l < 16 ? g + hsel * 16 + l : l < 24 ? g + 16 + hsel * 8 + l : l == 24 ? g + 48 + hsel * 8 + pj : l < 32 ? g + 64 + hsel * 8 + pj :
                  l < 48 ? 2304 + pj * 16 + l - 32 : 2048 + pj * 16 + l - 48);
    return t;
  }
  auto put64 = [&](int i, u64 x) { t.v[i] = (unsigned)x; t.v[i + 1] = (unsigned)(x >> 32); };
  t.v[T_Q0] = oQ + ((solo ? qbF : 15 - pj) * 128 + wid * 16) * 512;
  t.v[T_K0] = oK + (isY ? cx : 0) * 32768;
  t.v[T_OQ] = oQ + pj * 131072 + (wid >> 1) * 65536 + (wid & 1) * 4096;
  t.v[T_OG] = oK + kv * TSB + mt * 131072 + (wid >> 1) * 65536 + (wid & 1) * (kv ? 8192 : 4096);
  t.v[T_FPREP] = PREPE ? fo(2048 + pj * 16 + (isY * 2 + hsel) * 4) + (j - PREPE + 1) * 4 : fo(2048 + pj * 16 + u);
  t.v[T_FQ] = fo(g + 32 + hsel * 8 + pj);
  t.v[T_FG] = fo(g + hsel * 16 + kv * 8 + mt);
  t.v[T_FS] = fo(1024 + qbF * 16 + j * 2 + hsel);
  t.v[T_CNT] = fo(2560 + qbF * 16);
  t.v[T_ZO] = O_Z + pj * 64 * SEQ * 2 + (u * 4 + wid) * 64;
  const u64 own = 0x1ull << (kv * 8 + mt), needQ = isY ? 1ull << (16 + (qbF >> 1)) : (1ull << (16 + (pj >> 1))) | (1ull << (16 + ((15 - pj) >> 1)));
  put64(T_NPRE, isY ? needQ | (1ull << (cx >> 2)) | (1ull << ((cx + 1) >> 2)) : solo ? needQ : needQ | 0xffffull << 48);
  put64(T_NST, isY ? kvmask(cx >> 2, 3) | (solo ? 0 : 1ull << 25) : kvmask(0, solo ? 3 : 0));
  put64(T_TAB, k_order(isY, rr));
  t.v[T_SLAB] = O_S + qbF * 0x200000 + wid * 65536 + (j * 2 + hsel) * 1024;
  t.v[T_PART] = oP;
  for (int it = 0; it < 2; ++it) {
    unsigned* p = t.v + T_IT + it * T_ITN;
    const bool last = isY || it == 1;
    const int n = solo ? 2 * qbF + 2 : isY ? NY : it ? 2 * pj + 2 : nx0;
    const u64 need2 = solo ? 0 : kvmask(0, (2 * pj + 1) >> 2) | (0xffffull << 32);
    u64 q[3] = {0, 0, 0};
    unsigned jmp[2] = {0, 0};
    if (isY) {
      p[0] = (solo && SWAP7 ? 0 : 1u << (14 - cx)) | (solo ? 0 : 1u << (n - 2));
      q[0] = kvmask(4, (31 - 2 * pj) >> 2) & ~own; jmp[0] = ex * 32768; q[1] = solo ? 0 : 1ull << 24;
      p[1] = 2; p[13] = t.v[T_FG]; p[14] = fo(g + 96 + r);
    } else if (it == 0) {
      p[0] = 2u | (ex ? 1u << (cx - 2) : 0);
      q[0] = kvmask(0, cx > 0 ? (cx - 1) >> 2 : 0);
      if (ex) { q[1] = kvmask(4, 4 + ((ex - 1) >> 2)); jmp[1] = (16 - cx) * 32768; q[2] = need2; } else q[1] = need2;
      p[1] = 2; p[13] = fo(2304 + pj * 16 + u) + j * (8 * 4352 - 8); p[14] = fo(g + 64 + hsel * 8 + pj);
    } else {
      p[0] = p[1] = solo ? 0 : 1;
      q[0] = need2;
      if (solo && SWAP7) { p[0] = 1u << 14; q[0] = kvmask(4, 4); } p[13] = fo(g + 48 + hsel * 8 + pj); p[14] = fo(g + 96 + r);
    }
    p[2] = 1u << (n - 2);
    p[3] = !last ? 0 : n > 2 ? 1u << (n - 3) : 1u;
    p[4] = !last ? 0 : n > 2 ? 3u << (n - 3) : 1u << 31;
    for (int k = 0; k < 3; ++k) { p[5 + 2 * k] = (unsigned)q[k]; p[6 + 2 * k] = (unsigned)(q[k] >> 32); }
    p[11] = jmp[0]; p[12] = jmp[1];
    p[15] = oK;
    p[16] = (last ? 1 : 0) | (isY && !solo ? 2 : 0) | (n == 2 ? 4 : 0) | (isY || solo ? 0 : 8) | (n - 1 - last) << 8 | ((r >> 1) * 4 + wid) << 16;
    p[17] = n == 2;
    p[18] = oQ + (pj * 128 + wid * 16) * 512;
    p[19] = 0;
    p[20] = (p[0] | (last ? 0 : 1u << n)) >> 1;
    p[21] = oK + (isY ? cx : 0) * 32768;
    p[22] = ((p[0] | p[1] | p[2] | p[3] | p[4]) & 0x7fffffffu) << 1;
  }
  return t;
}
template <int I> constexpr TRow t_base = I < 160 ? t_row(I / 5, I % 5, 0) : [] {
  TRow a = t_row(0, (I - 160) * 4, 0), b = t_row(0, (I - 160) * 4, 1);
  for (int l = 0; l < 64; ++l) a.v[l] = b.v[l] - a.v[l];
  return a;
}();
template <int I> constexpr bool t_linear = [] {
  for (int j = 1; j < 8; ++j) {
    const TRow b = t_row(I / 5, I % 5, j);
    for (int l = 0; l < 64; ++l) if (b.v[l] != t_base<I>.v[l] + j * t_base<160 + (I % 5) / 4>.v[l]) return false;
  }
  return true;
}();
struct Tab { TRow row[162]; };
template <int... I> constexpr Tab t_make(iseq<I...>) {
  static_assert((t_linear<(I < 160 ? I : 0)> && ...));
  return Tab{{t_base<I>...}};
}
__device__ const Tab g_tab = t_make(make_iseq<162>{});

template <int R> DI const bf16* sptr() { const bf16* p; asm volatile("s_mov_b64 %0, s[%c1:%c2]" : "=s"(p) : "i"(R), "i"(R + 1)); return p; }
template <class T, int I> DI T* arg_late() {
  unsigned lo, hi;
  asm volatile("v_readlane_b32 %0, v77, %2\n\tv_readlane_b32 %1, v77, %3\n\ts_nop 4" : "=s"(lo), "=s"(hi) : "i"(2 * I), "i"(2 * I + 1));
  return (T*)(((u64)hi << 32) | lo);
}
__global__ __launch_bounds__(256, 1) __attribute__((amdgpu_num_vgpr(77), amdgpu_num_sgpr(56))) void fused_kernel(
    const bf16* ckv_, const bf16* wuk_, const bf16* wuv_, const bf16* x_, const bf16* wq_, bf16* ws_, const bf16* ak_, const bf16* av_,
    const bf16* ash_, const bf16* assmk_, const bf16* assmD_, const bf16* wo_, const bf16* aq_, bf16* out_) {
  extern __shared__ __attribute__((aligned(16))) char lds[];
  const void* kap = (const void*)__builtin_amdgcn_kernarg_segment_ptr();
  asm volatile("at_code_begin:\n\ts_load_dwordx16 s[80:95], %0, 0\n\ts_load_dwordx2 s[96:97], %0, 0x40" ::"s"(kap) : ACLOB_ALL, "v255", "s101");
  asm volatile("s_getpc_b64 s[48:49]\nct_base:\n\ts_mov_b32 s50, pf_code_end-ct_base\n\ts_mov_b32 s51, 0x20000\n\ts_lshr_b32 s52, %1, 3\n\t"
               "v_and_b32 v116, 63, %0\n\tv_lshrrev_b32 v117, 6, %0\n\tv_lshl_add_u32 v116, v116, 7, v117\n\ts_lshl_b32 s52, s52, 9\n\t"
               "v_lshlrev_b32 v116, 7, v116\n\tbuffer_load_dword v117, v116, s[48:51], s52 offen" ::"v"(threadIdx.x), "s"(blockIdx.x) : "scc", "memory");
  GAP();
  const int tid = threadIdx.x, lane = tid & 63;
  const int wid = __builtin_amdgcn_readfirstlane(tid >> 6);
  const int b = blockIdx.x, j = b & 7, r = b >> 3, rr = r & 15, hsel = rr & 1, pj = rr >> 1, kv = pj & 1;
  const bool isY = r >= 16, solo = pj == 7, doP = (PREPE ? (j & 1) == PREPE - 1 : isY) && !solo, doA = !isY && !solo;
  const int h = j + 8 * hsel, mt = (rr >> 2) + (isY ? 4 : 0), pu = (hsel * 8 + j) * 4 + wid;
  constexpr unsigned m0d = 131072 + 24576;
  const unsigned lA = isY ? 65536 : 0, lW = lA ^ 65536, po = isY ? 0 : 32768;
  unsigned mA = lA + wid * 1024 + po, mW = lW + wid * 1024 + po, ofA = isY ? pj * 256 * DA * 2 : mt * 256 * DC * 2, ofW = h * 512;
  const unsigned lda = isY ? DA : DC;
#define ROT 1
  unsigned g0 = (!ROT ? 0 : isY ? pj + 2 * hsel : rr + (rr >> 2)) & 3;
  {
    const unsigned* tp = g_tab.row[r * 5].v;
    asm volatile("global_load_dword v78, %0, %2\n\tglobal_load_dword v79, %1, %2 offset:1024\n\t"
                 "global_load_dword v118, %1, %3\n\tglobal_load_dword v119, %1, %3 offset:256\n\tglobal_load_dword v77, %4, %5"
                 ::"v"(tid * 4), "v"(lane * 4), "s"(tp), "s"(g_tab.row[160].v), "v"((lane < 27 ? lane : 27) * 4), "s"(kap) : "memory");
    const unsigned ra = wid * 8 + (lane >> 3), sa = ((lane & 7) ^ (((wid & 1) * 4 + (lane >> 4)) & 7)) * 16;
    const unsigned vbA = ra * (lda * 2) + sa, vbB = (4 * wid + (lane >> 4)) * LDW * 2 + 16 * ((lane & 15) ^ (((lane >> 4) << 2) | wid));
    vsetu<92>(vbA); vsetu<93>(vbB); vsetu<94>(ra * (DC * 2) + sa);
    sset<64>(64 * lda); sset<67>(256 * lda); sset<68>(320 * lda);
    asm volatile("s_mov_b32 s71, 0x20000\n\ts_movk_i32 s72, 0x100\n\ts_mov_b32 s73, 0x20100");
    sset<58>(256 * lda * 2); sset<62>((lda - 1) * LDW * 2 + 512);
    asm volatile("s_mov_b32 s59, 0x20000\n\ts_mov_b32 s63, 0x20000");
    g_pro<1>(g0, mA, mW, lda);
    asm volatile("" : "+s"(ofA), "+s"(ofW), "+s"(mA), "+s"(mW), "+s"(g0));
    GAP();
  }
  asm volatile("s_bitcmp1_b32 %0, 4\n\ts_waitcnt lgkmcnt(0)\n\ts_cselect_b64 s[60:61], s[84:85], s[82:83]\n\ts_bitcmp1_b32 %0, 7\n\t"
               "s_cselect_b64 s[60:61], s[88:89], s[60:61]\n\ts_cselect_b64 s[56:57], s[86:87], s[80:81]\n\t"
               "s_add_u32 s56, s56, %1\n\ts_addc_u32 s57, s57, 0\n\ts_add_u32 s60, s60, %2\n\ts_addc_u32 s61, s61, 0\n\t"
               "s_mov_b64 s[88:89], s[90:91]\n\ts_mov_b32 s90, %3\n\ts_mov_b32 s91, 0x20000" ::"s"(b), "s"(ofA), "s"(ofW), "i"(WSB) : "scc");
  KST(0, 0);
  {
    GM g;
    PR pr;
    const int ptb = PREPE ? (isY * 2 + hsel) * 4 + (j >> 1) : hsel * 8 + j;
    if (doP) prep_load(pr, tid, pj, ptb, sptr<92>(), sptr<94>(), sptr<96>());
    g_pro<2>(g0, mA, mW, lda);
    asm volatile("buffer_load_dwordx2 v[120:121], off, s[88:91], %0 sc1" ::"s"(fo(6144)) : "memory");
    if (doP) {
      asm volatile("s_waitcnt vmcnt(9)" ::: "memory");
#pragma unroll
      for (int q = 0; q < 7; ++q) asm volatile("" : "+v"(pr.kk[q]));
#pragma unroll
      for (int q = 0; q < 4; ++q) asm volatile("" : "+v"(pr.vv[q]));
      static_for([&](auto Ic) __attribute__((always_inline)) {
        constexpr int I = Ic;
        if constexpr (I < 3) g_pro(g0 + 1 + I, mA, mW, lda);
        GAP();
        prep_math<I>(pr, tid, lds + 131072);
        GAP();
      }, make_iseq<4>{});
      asm volatile("s_waitcnt lgkmcnt(0)\n\ts_barrier" ::: "memory");
      prep_store(tid, pj, ptb, (bf16*)sptr<88>() + O_X / 2, lds + 131072);
    }
    GAP();
    {
      const int fr = lane & 15, fq = lane >> 4, q = (lane & 15) >> 2, p = lane & 3;
#pragma unroll
      for (int k2 = 0; k2 < 2; ++k2)
        g.aA[k2] = lA + (wid >> 1) * 16384 + (fr >> 3) * 1024 + (fr & 7) * 128 + (((4 * k2 + fq) ^ (fr >> 1)) * 16);
#pragma unroll
      for (int hf = 0; hf < 2; ++hf)
#pragma unroll
        for (int nf = 0; nf < 8; ++nf)
          g.aB[hf][nf] = lW + (wid & 1) * 8192 + off_b(8 * fq + 4 * hf + q, nf * 2 + (p >> 1)) + 8 * (p & 1);
      asm volatile("" : "+v"(g.aA[0]), "+v"(g.aA[1]));
#pragma unroll
      for (int nf = 0; nf < 8; ++nf) asm volatile("" : "+v"(g.aB[0][nf]), "+v"(g.aB[1][nf]));
      vsetu<81>(tid == 0 ? 0u : 0x80000000u);
      vsetu<84>(lane * 16); vsetu<85>(lane * 16 + 65536); vsetu<86>(wid * 8192 + lane * 16); vsetu<87>(wid * 8192 + lane * 16 + 4096);
      vsetu<88>(65536 + wid * 256 + fr * 16); vsetu<89>(16 * wid + fr - 4 * fq);
      asm volatile("s_mov_b64 s[92:93], s[88:89]\n\ts_mov_b64 s[94:95], s[90:91]\n\ts_mov_b32 s74, 0x40000\n\ts_mov_b32 s75, 0x60000\n\t"
                   "s_mov_b32 s76, 0x40100\n\ts_mov_b32 s77, 0x60100");
      sset<65>(128 * lda); sset<66>(192 * lda); sset<69>(384 * lda); sset<70>(448 * lda);
      sset<78>(lA + wid * 1024); sset<79>(lW + wid * 1024);
    }
    if (doP) asm volatile("s_waitcnt vmcnt(4)" ::: "memory");
    else {
      g_pro(g0 + 1, mA, mW, lda); g_pro(g0 + 2, mA, mW, lda); g_pro(g0 + 3, mA, mW, lda);
      asm volatile("s_waitcnt vmcnt(0)" ::: "memory");
    }
    asm volatile("s_barrier" ::: "memory");
    if (isY) g_read0<0>(g, make_iseq<16>{});
    else g_read0<1>(g, make_iseq<16>{});
    GAP();
    {
      u64 z = (((u64)__float_as_uint(vget<121>()) << 32) | __float_as_uint(vget<120>())) + 0x9e3779b97f4a7c15ull;
      z = (z ^ (z >> 30)) * 0xbf58476d1ce4e5b9ull; z = (z ^ (z >> 27)) * 0x94d049bb133111ebull; z ^= z >> 31;
      vsetu<82>((unsigned)z); vsetu<83>((unsigned)(z >> 32));
      asm volatile("v_mad_u32_u24 v78, v118, %0, v78\n\tv_mad_u32_u24 v79, v119, %0, v79\n\tv_readfirstlane_b32 s84, v82\n\tv_readfirstlane_b32 s85, v83\n\t"
                   "v_readlane_b32 s80, v78, %1\n\tv_readlane_b32 s81, v78, %2" ::"s"(j), "i"((int)T_TAB), "i"(T_TAB + 1));
      if (isY) asm volatile("s_mov_b64 s[80:81], 0x6543210");
    }
    unsigned k2;
    asm volatile("s_bfe_u32 s82, s80, 0x40008\n\ts_lshr_b64 s[80:81], s[80:81], 12\n\ts_lshl_b32 %0, s82, 1\n\tv_add_u32 v90, 128, v92" : "=s"(k2) :: "scc");
    asm volatile("s_waitcnt lgkmcnt(0)\n\ts_barrier" ::: "memory");
    g_proW(k2, mW);
    GAP();
    if (isY) {
      g_tile<false, 0, 1, NTW >= 1>(g);
      g_loop<false, true>(g, doP ? 0x22u : 0x20u, [&](int t) __attribute__((always_inline)) {
        if (t == 1) fl_set<T_FPREP>();
        else if (t == 4) { tload<80, T_TAB>(); tload<81, T_TAB + 1>(); srd_set<60>((kv ? arg_late<const bf16, 2>() : arg_late<const bf16, 1>()) + h * 256, (DC - 1) * LDW * 2 + 512); }
        else {
          srd_set<56>(arg_late<const bf16, 0>() + (size_t)mt * 256 * DC, 256 * DC * 2);
          asm volatile("v_mov_b32 v92, v94");
          static_for([&](auto J) { asm volatile("s_mov_b32 s%c0, %1" ::"i"(64 + J), "i"((J + 1) * 64 * DC)); }, make_iseq<7>{});
        }
      });
      tload<101, T_OQ>(); sset<83>(__float_as_uint(CS));
      asm volatile("v_mov_b32 v112, v84\n\tv_add_u32 v113, 8192, v84");
      GAP();
      g_tile<false, 0, 2, NTW >= 2>(g);
      KST(0, 3);
    }
#define TT 3
    auto kvtile = [&](auto TRc) __attribute__((always_inline)) {
      constexpr bool TR = TRc;
      if (isY) g_tile<TR, 1, 4, NTW >= 2>(g);
      else g_tile<TR, 1, 1, NTW >= 2>(g);
      g_loop<TR, false>(g, 0x2000u | (isY || doA ? 2u : 1u << TT), [&](int t) __attribute__((always_inline)) {
        int l = threadIdx.x & 63; asm volatile("" : "+v"(l));
        if (t == 13) {
          asm volatile("s_mov_b32 s79, 0x28000");
          fl_poll<T_NPRE>();
          if ((b & 15) == 0)
            asm volatile("v_readlane_b32 s48, v78, %0\n\tv_and_b32 v122, 0xffffff00, v82\n\tv_mov_b32 v123, v83\n\ts_nop 2\n\t"
                         "buffer_store_dwordx2 v[122:123], v81, s[88:91], s48 offen sc1" ::"i"((int)T_CNT) : "memory");
          if (!doA && !isY) {
            CODE_TOUCH("at_code_begin", "pf_code_end", hsel * 8 + wid * 2, 16u, 64, m0d);
            CODE_TOUCH("at_code_begin", "pf_code_end", hsel * 8 + wid * 2 + 1, 16u, 64, m0d);
          }
          if (doA) {
            const char* tp = arg_late<const char, 9>() + (size_t)(pj * 64 + pu) * (SEQ * 2) + (l & 31) * 128;
            asm volatile("s_mov_b32 m0, %1\n\ts_nop 0\n\tglobal_load_lds_dword %0, off" ::"v"(tp), "i"(m0d) : "memory");
          }
        } else if (isY) fl_set<T_FQ>();
        else if (doA) { if (doP) fl_set<T_FPREP>(); post_dma(pj, pu, arg_late<const bf16, 12>(), 131072 + wid * 8192); }
        else {
          const char* tp = arg_late<const char, 0>() + 1024 * DC * 2 + (pu * 256 + l) * 128;
          asm volatile("s_mov_b32 m0, %1\n\ts_nop 0\n\tglobal_load_lds_dword %0, off" ::"v"(tp), "i"(m0d) : "memory");
          asm volatile("global_load_lds_dword %0, off" ::"v"(tp + 8192) : "memory");
          asm volatile("global_load_lds_dword %0, off" ::"v"(tp + 16384) : "memory");
          asm volatile("global_load_lds_dword %0, off" ::"v"(tp + 24576) : "memory");
        }
      });
      tload<101, T_OG>(); tload<96, T_Q0>(); tload<98, T_K0>();
      fl_check();
      tload<50, T_NST>(); tload<51, T_NST + 1>();
      asm volatile("v_mov_b32 v112, v84\n\tv_add_u32 v113, %0, v84\n\tv_mov_b32 v90, v87\n\tv_mov_b32 v91, v87\n\tv_mov_b32 v92, v87\n\tv_mov_b32 v93, v87\n\t"
                   "v_mov_b32 v94, v86\n\tv_mov_b32 v95, v86\n\tv_mov_b32 v126, v86\n\tv_mov_b32 v127, v86\n\ts_mov_b32 s99, 0x8000\n\t" FL_ARM("s[50:51]")
                   ::"i"(TR ? 4096 : 8192));
      if (doA) {
        const int d = pj * 64 + pu;
        const bf16* dp = arg_late<const bf16, 10>() + d;
        asm volatile("global_load_ushort v123, %0, off" ::"v"(dp) : "memory");
        srd_set<56>(arg_late<const bf16, 9>() + (size_t)d * SEQ, SEQ * 2);
        srd_set<60>(arg_late<const char, 5>() + O_X + d * SEQ * 2, SEQ * 2);
        sset<78>(65536 + wid * 15488);
        asm volatile("s_movk_i32 s64, 992\n\tv_subrev_u32 v122, 32, v84");
        GAP();
        g_tile<TR, 0, 3>(g);
      } else {
        sset<79>(isY ? wid * 8192 : 0x30000);
        if ((NYT & 1) && !solo) asm volatile("s_add_u32 s98, s98, 0x8000\n\ts_mov_b32 s97, 0xffff8000" ::: "scc");
        else asm volatile("s_mov_b32 s97, 0x8000");
        GAP();
        g_tile<TR, 0, 5>(g);
      }
    };
    if (kv) kvtile(std::true_type{});
    else kvtile(std::false_type{});
  }
  KST(0, 4);
  asm volatile("s_waitcnt vmcnt(16)" ::: "memory");
  sset<79>(wid * 8192);
  if (doA) {
    const float Dd = bfl(__float_as_uint(vget<123>()));
    KST(0, 5);
    conv_wave<T_FG>(O_Z + (pj * 64 + pu) * SEQ * 2, Dd, lds + 65536 + wid * 15488);
    const char* tp = arg_late<const char, 11>() + (size_t)h * (HD * DA * 2) + ((pj * 4 + wid) * 64 + lane) * 128;
    asm volatile("s_mov_b32 m0, %1\n\ts_nop 0\n\tglobal_load_lds_dword %0, off\n\ts_waitcnt vmcnt(17)" ::"v"(tp), "i"(m0d) : "memory");
    KST(0, 6);
  } else if (!isY) {
    static_for([&](auto I) { d_store<16 + I>(); }, make_iseq<16>{});
    tload<98, T_K0>();
    asm volatile("s_waitcnt vmcnt(0)\n\ts_barrier" ::: "memory");
    fl_set<T_FG>();
    KST(0, 5);
    fl_check();
    CODE_TOUCH("att_begin", "pf_code_end", hsel * 4 + wid, 8u, 64, m0d);
    dma4k<0>(); dma4k<1>(); dma4k<2>(); dma4k<3>();
    asm volatile("s_waitcnt vmcnt(0)" ::: "memory");
    KST(0, 6);
  } else {
    tload<76, T_PART>();
    fl_check();
    asm volatile("buffer_load_dwordx2 v[122:123], v88, s[88:91], s76 offen offset:1024 sc1" ::: "memory");
  }
  AC c;
  sset<78>(65536 + wid * 8192); sset<79>(98304 + wid * 8192);
  { const bf16* wp = arg_late<const bf16, 11>() + (size_t)h * HD * DA; asm volatile("s_mov_b64 s[80:81], %0" ::"s"(wp)); }
  asm volatile("att_begin:");
  ev_code();
  asm volatile("s_waitcnt lgkmcnt(0)\n\ts_barrier" ::: "memory");
#pragma unroll 1
  for (int it = doA ? 0 : 1; it < 2; ++it) {
    const bool odd = (NYT & 1) && !solo && (isY || it == 0);
    if (odd) kreads<1>(make_iseq<8>{});
    else kreads<0>(make_iseq<8>{});
    const unsigned oP = tget<T_PART>();
    if (it == 0) {
      static_for([&](auto I) { tload<57 + I, T_IT + I>(); }, make_iseq<21>{});
      tload<S_T, T_IT + 21>(); tload<S_EV, T_IT + 22>();
      asm volatile("s_sub_u32 s70, s70, %0\n\ts_add_u32 s76, %1, 1024" ::"s"(j * (8 * 4352 - 8)), "s"(oP) : "scc");
    } else {
      static_for([&](auto I) { tload<57 + I, T_IT + T_ITN + I>(); }, make_iseq<21>{});
      tload<S_T, T_IT + T_ITN + 21>(); tload<S_EV, T_IT + T_ITN + 22>();
      asm volatile("s_mov_b32 s72, 0\n\ts_mov_b32 s75, %0\n\ts_mov_b32 s76, %1" ::"s"(oP + wid * 16384), "s"(oP + 1032));
    }
    unsigned mode;
    asm volatile("s_add_u32 s98, s97, 0x8000\n\ts_mov_b32 %0, s73\n\ts_mov_b32 s56, -1\n\tv_mov_b64 v[114:115], 0\n\tv_mov_b64 v[118:119], 0\n\t"
                 "v_mov_b32 v120, 0x45800000" : "=s"(mode) :: "scc");
    KST(0, 8 + 3 * (it && !isY));
    GAP();
#define STA(k)
#define STI()
#define LOOP_ENDS "s_bfe_u32 s76, s73, 0x80008"
    STA(0);
    if (odd) {
      if (isY) phaseA<1, 2>(c);
      else phaseA<1, 1>(c);
      STA(1);
      STA(2);
      phaseB0<1>(mode);
      STA(3);
      asm volatile(LOOP_ENDS "\n\ts_branch att_mid" ::: "scc");
    } else {
      if (isY) phaseA<0, 2>(c);
      else phaseA<0, 1>(c);
      STA(1);
      if (c.rare) ev_call();
      STA(2);
      phaseB0<0>(mode);
      if (__builtin_expect(mode & 4, 0)) { asm volatile("s_mov_b32 s56, 31"); ev_call(); asm volatile("s_branch att_done"); }
      STA(3);
    }
    GAP();
    asm volatile(LOOP_ENDS "\natt_loop:" ::: "scc");
    GAP();
    phaseA<1, 0>(c);
    STI();
    if (__builtin_expect(c.rare != 0, 0)) { if (redo()) sm_redo<1>(); ev_call(); }
    phaseB<1>(c);
    asm volatile("s_cbranch_scc1 att_done\natt_mid:");
    phaseA<0, 0>(c);
    STI();
    if (__builtin_expect(c.rare != 0, 0)) { if (redo()) sm_redo<0>(); ev_call(); }
    phaseB<0>(c);
    asm volatile("s_cbranch_scc0 att_loop\natt_done:");
    GAP();
    STA(4);
    if (it == 0) phaseA<0, 3>(c);
    else {
      sset<76>(oP);
      GAP();
      phaseAL(c, make_iseq<32>{});
      if (__builtin_expect(redo(), 0)) sm_redo<1>();
      if (mode & 2) phaseBL<true>(c, make_iseq<64>{});
      else phaseBL<false>(c, make_iseq<64>{});
    }
    if (__builtin_expect(redo(), 0)) {
      if (it) static_for([&](auto I) { vset<R_S + 32 + (I >> 2) * 8 + (I & 3)>(-INFINITY); }, make_iseq<16>{});
      asm volatile("s_nop 7");
      sm_redo<0>();
    }
    STA(5);
    KST(0, 9 + 3 * (it && !isY));
    const float l[2] = {row_reduce<false>(vget<R_RS>() + vget<R_TS>()), row_reduce<false>(vget<R_RS + 1>() + vget<R_TS + 1>())};
    const float cm[2] = {vget<R_M>(), vget<R_M + 1>()};
    EP p;
    if (it == 0) {
      const f32x4 ml = {cm[0], l[0], cm[1], l[1]};
      asm volatile("buffer_store_dwordx4 %0, v88, s[88:91], %1 offen sc1\n\ts_mov_b32 s101, %2" ::"v"(ml), "s"(oP), "s"(oP + wid * 16384) : "memory");
      phaseF(c, p, make_iseq<74>{});
      asm volatile(FL_POLL "\n\ts_waitcnt vmcnt(17)" ::: "memory");
    } else {
      int lz = threadIdx.x & 63; asm volatile("" : "+v"(lz));
      const int rw = lz >> 2;
      static_for([&](auto E) { vsetu<R_TB + E>(rw * 128 + 16 * ((2 * E + ((lz & 3) >> 1)) ^ (rw & 6)) + 8 * (lz & 1)); }, make_iseq<4>{});
      const unsigned vs = ((lz >> 3) & 1) * 16384 + ((lz >> 4) & 1) * 512 + (lz >> 5) * 256 + (lz & 7) * 16;
      vsetu<R_VS>(vs); vsetu<R_VS + 1>(vs + 32768);
      tload<96, T_SLAB>();
      const f32x4 ml = {vget<122>(), vget<123>(), vget<124>(), vget<125>()};
      asm volatile(TICKET ::"i"((int)T_CNT) : "memory");
      asm volatile("s_mov_b32 s97, s79\n\ts_mov_b32 s100, 0x8000\n\ts_movk_i32 s99, 0x1000");
      if (!(mode & 2)) {
        p.fa[0] = __builtin_amdgcn_rcpf(l[0]); p.fa[1] = __builtin_amdgcn_rcpf(l[1]);
        phaseFL<1>(p);
      } else {
        if (__builtin_expect(__builtin_amdgcn_ballot_w64(ml[0] != cm[0] || ml[2] != cm[1]) == 0, 1)) {
          p.fa[0] = __builtin_amdgcn_rcpf(l[0] + ml[1]); p.fa[1] = __builtin_amdgcn_rcpf(l[1] + ml[3]);
          const unsigned one = (lz >> 4) != ((lz >> 2) & 3) ? 0u : (lz & 1) ? 0x3f800000u : 0x3f80u, o0 = (lz & 2) ? 0u : one, o1 = (lz & 2) ? one : 0u;
          vsetu<224>(o0); vsetu<225>(o1); vsetu<230>(o0); vsetu<231>(o1);
          asm volatile("v_mov_b64 v[226:227], 0\n\tv_mov_b64 v[228:229], 0");
          phaseFL<3>(p);
        } else {
#pragma unroll
          for (int e = 0; e < 2; ++e) {
            const float mm = fmaxf(cm[e], ml[2 * e]);
            const float wa = __builtin_amdgcn_exp2f(cm[e] - mm), wb = __builtin_amdgcn_exp2f(ml[2 * e] - mm);
            const float inv = __builtin_amdgcn_rcpf(l[e] * wa + ml[2 * e + 1] * wb);
            p.fa[e] = wa * inv; p.fb[e] = wb * inv;
          }
          phaseFL<2>(p);
        }
      }
    }
    KST(0, 10 + 3 * (it && !isY));
  }
  asm volatile("v_xor_b32 v%c0, s100, v%c0\np_loop:" ::"i"(R_TB));
  GAP();
  p_half<0>(make_iseq<32>{});
  p_half<1>(make_iseq<32>{});
  asm volatile("s_cbranch_scc1 p_loop\n\ts_nop 5");
  GAP();
  p_cvt<1, 0>(); p_cvt<1, 1>(); p_cvt<1, 2>(); p_cvt<1, 3>();
  p_store<1, 1>();
  GAP();
  KST(0, 27);
  {
    constexpr unsigned rdy = 98304;
    int lz = threadIdx.x & 63; asm volatile("" : "+v"(lz));
    const int fr = lz & 15, fq = lz >> 4, qbF = isY != (solo && SWAP7) ? 15 - pj : pj;
    if (wid == 0) {
#pragma unroll 1
      for (int q = 0; q < 0x100000; ++q) {
        unsigned lo, hi, tl, th;
        asm volatile("v_readfirstlane_b32 %0, v122\n\tv_readfirstlane_b32 %1, v123\n\ts_mov_b32 %2, s84\n\ts_mov_b32 %3, s85" : "=s"(lo), "=s"(hi), "=s"(tl), "=s"(th));
        if (__builtin_expect((((lo ^ tl) >> 8) | (hi ^ th)) == 0, 1)) break;
        asm volatile("s_sleep 4\n\t" TICKET "\n\ts_waitcnt vmcnt(0)" ::"i"((int)T_CNT) : "memory");
      }
      asm volatile("v_mov_b32 v123, 0\n\ts_mov_b64 exec, 1\n\tds_write_b64 %0, v[122:123]\n\ts_mov_b64 exec, -1" ::"v"(rdy) : "memory");
    }
    vsetu<79>(fo(1024) + qbF * 4352 + fr * 8);
    {
      const int jj = 4 * (fr >> 3) + (fr & 3);
      const unsigned one = ((fq ^ (fr >> 2)) & 1) ? 0u : (jj & 1) ? 0x3f800000u : 0x3f80u;
      static_for([&](auto I) { vsetu<96 + I>((jj >> 1) == I ? one : 0u); }, make_iseq<4>{});
    }
    unsigned vo = (fq >> 1) * 1024 + (fq & 1) * 256 + fr * 16;
    bf16* out = arg_late<bf16, 13>();
    __amdgpu_buffer_rsrc_t so_ = __builtin_amdgcn_make_buffer_rsrc((void*)out, (short)0, SEQ * OUTW * 2, 0x00020000);
    unsigned vw = ((qbF * 128 + (fr & 7)) * OUTW + DA + (fr >> 3) * 32 + fq * 8) * 2;
    asm volatile("" : "+s"(so_));
    asm volatile("s_waitcnt vmcnt(0) lgkmcnt(0)\n\ts_barrier" : "+v"(vo), "+v"(vw) :: "memory");
    fl_set<T_FS>();
    unsigned tk;
    asm volatile("ds_read_b32 %0, %1\n\ts_waitcnt lgkmcnt(0)" : "=v"(tk) : "v"(rdy) : "memory");
    tk = __builtin_amdgcn_readfirstlane(tk) & 255;
    KST(0, 30);
    if (doA) post_math(pj, pu, out, lds + 131072 + wid * 8192);
#define N3 0
#define N2 12
    constexpr int N1 = 28 - 3 * N3 - 2 * N2;
    static_assert(N1 >= 0 && N3 + N2 + N1 <= 16);
    const int nu = tk < N3 ? 3 : tk < N3 + N2 ? 2 : tk < N3 + N2 + N1 ? 1 : 0;
    const int u0 = 4 * (tk < N3 ? 3 * tk : tk < N3 + N2 ? N3 + 2 * tk : 2 * N3 + N2 + tk) + wid * nu;
    KST(0, 28);
    if (nu) {
      unsigned so[3];
#pragma unroll
      for (int k = 0; k < 3; ++k) so[k] = O_S + qbF * 0x200000 + 0x40000 + (u0 + k) * 16384;
      unsigned pend = 0x5555u, cnt = 0x400000;
      while (true) {
        unsigned nw;
        if (wid == 3) {
          u64 m;
          const u64 ex = pend | (pend << 1);
          asm volatile(FL_ARM("%1") "\n\t" FL_POLL "\n\ts_waitcnt vmcnt(0)\n\tv_cmp_eq_u64 %0, s[84:85], v[124:125]" : "=s"(m) : "s"(ex) : "memory");
          nw = (unsigned)m & ((unsigned)m >> 1) & pend;
          if (nw) asm volatile("ds_write_b32 %0, %1 offset:4" ::"v"(rdy), "v"(~pend | nw) : "memory");
        } else {
          unsigned t;
          asm volatile("ds_read_b32 %0, %1 offset:4\n\ts_waitcnt lgkmcnt(0)" : "=v"(t) : "v"(rdy) : "memory");
          nw = __builtin_amdgcn_readfirstlane(t) & pend;
        }
        if (--cnt == 0) nw = pend;
        if (nw == 0) {
          if (wid == 3) asm volatile("s_sleep 4");
          else asm volatile("s_sleep 1");
          continue;
        }
        asm volatile("s_nop 4");
        static_for([&](auto S) {
          if ((nw >> (2 * S)) & 1) {
            r_load<0, 2 * S>(vo, so[0]); r_load<0, 2 * S + 1>(vo, so[0]);
            if (nu > 1) { r_load<1, 2 * S>(vo, so[1]); r_load<1, 2 * S + 1>(vo, so[1]); }
            if (nu > 2) { r_load<2, 2 * S>(vo, so[2]); r_load<2, 2 * S + 1>(vo, so[2]); }
          }
        }, make_iseq<8>{});
        pend &= ~nw;
        if (pend == 0) break;
      }
      asm volatile("s_waitcnt vmcnt(0)" ::: "memory");
      KST(0, 16);
      auto oo = [&](int U) { return ((((U >> 1) & 1) * 64 + ((U >> 2) & 3) * 16 + (U & 1) * 8) * OUTW + (U >> 4) * 64) * 2; };
      r_fin<0>(vw, so_, oo(u0));
      if (nu > 1) r_fin<1>(vw, so_, oo(u0 + 1));
      if (nu > 2) r_fin<2>(vw, so_, oo(u0 + 2));
      if (qbF == 15 && tk == 0) asm volatile(FL_SET("%0") ::"s"(fo(6144)) : "memory");
    }
    KST(0, 29);
  }
  asm volatile("s_waitcnt vmcnt(0)" ::: "memory");
  asm volatile("pf_code_end:");
  KST(0, 1);
}

extern "C" void kernel_launch(void* const* d_in, const int* in_sizes, int n_in, void* d_out, int out_size, void* d_ws,
                              size_t ws_size, hipStream_t stream) {
  const bf16* Aq = (const bf16*)d_in[0];
  const bf16* Ak = (const bf16*)d_in[1];
  const bf16* Av = (const bf16*)d_in[2];
  const bf16* Ash = (const bf16*)d_in[3];
  const bf16* Assmk = (const bf16*)d_in[4];
  const bf16* AssmD = (const bf16*)d_in[5];
  const bf16* Bx = (const bf16*)d_in[6];
  const bf16* Bckv = (const bf16*)d_in[7];
  const bf16* Bwq = (const bf16*)d_in[8];
  const bf16* Bwuk = (const bf16*)d_in[9];
  const bf16* Bwuv = (const bf16*)d_in[10];
  const bf16* Bwo = (const bf16*)d_in[11];
  bf16* out = (bf16*)d_out;
  hipFuncSetAttribute((const void*)fused_kernel, hipFuncAttributeMaxDynamicSharedMemorySize, 163840);
  fused_kernel<<<256, 256, 163840, stream>>>(Bckv, Bwuk, Bwuv, Bx, Bwq, (bf16*)d_ws, Ak, Av, Ash, Assmk, AssmD, Bwo, Aq, out);
}
```

```cpp
#include <hip/hip_runtime.h>
#include <hip/hip_bf16.h>
#include <stdint.h>
#include <utility>

typedef __hip_bfloat16 bf16;
typedef __attribute__((ext_vector_type(4))) short bf16x4;
typedef __attribute__((ext_vector_type(2))) unsigned u32x2;

constexpr int SEQ = 2048;
constexpr int DA = 448;
constexpr int NH = 16;
constexpr int HD = 256;
constexpr int DC = 1024;
constexpr int OUTW = 896;
#define OUTSC 17
#define OUTSCS " sc0 sc1"
#define NTPS " nt"
#define NTVS " nt"
#define NTQS ""
#define NTW 1
#define NYT 15
#define SWAP7 1
constexpr unsigned TSB = NH * SEQ * HD * 2, O_K = TSB, O_S = 3 * TSB, O_P = 5 * TSB, PSTR = 67584, O_F = O_P + 128 * PSTR,
                   O_X = O_F + 400 * 4352, O_Z = O_X + DA * SEQ * 2, WSB = O_Z + DA * SEQ * 2;
constexpr unsigned fo(int w) { return O_F + (w >> 4) * 4352 + (w & 15) * 8; }

#define KST(kid, which)

__device__ __forceinline__ float bf2f(unsigned short u) { return __uint_as_float(((unsigned)u) << 16); }
__device__ __forceinline__ unsigned short f2bf(float f) { return __bfloat16_as_ushort(__float2bfloat16(f)); }
__device__ __forceinline__ float rbf(float f) { return bf2f(f2bf(f)); }
typedef __attribute__((ext_vector_type(2))) __bf16 bf16x2n;
typedef __attribute__((ext_vector_type(2))) float f32x2n;
__device__ __forceinline__ unsigned pk2(float a, float b) {
  return __builtin_bit_cast(unsigned, __builtin_convertvector((f32x2n){a, b}, bf16x2n));
}

__device__ __forceinline__ int off_b(int row, int ch) { return 256 * row + 16 * (ch ^ (((row & 3) << 2) | ((row >> 2) & 3))); }

template <int R> __device__ __forceinline__ void sset(unsigned x) { asm volatile("s_mov_b32 s%c0, %1" ::"i"(R), "s"(x)); }
template <int R> __device__ __forceinline__ void vsetu(unsigned x) { asm volatile("v_mov_b32 v%c0, %1" ::"i"(R), "v"(x)); }
template <int R> __device__ __forceinline__ void srd_set(const void* p, unsigned n) {
  asm volatile("s_mov_b64 s[%c0:%c1], %4\n\ts_mov_b32 s%c2, %5\n\ts_mov_b32 s%c3, 0x20000" ::"i"(R), "i"(R + 1), "i"(R + 2), "i"(R + 3), "s"(p), "s"(n));
}
template <int R, int L> __device__ __forceinline__ void tload() { asm volatile("v_readlane_b32 s%c0, v78, %1" ::"i"(R), "i"(L)); }
template <int L> __device__ __forceinline__ unsigned tget() { unsigned t; asm volatile("v_readlane_b32 %0, v78, %1" : "=s"(t) : "i"(L)); return t; }
#define FL_SET(S) "buffer_store_dwordx2 v[82:83], v81, s[88:91], " S " offen sc1"
#define FL_ARM(N) "v_cndmask_b32_e64 v80, -2.0, v79, " N
#define FL_POLL "buffer_load_dwordx2 v[124:125], v80, s[88:91], 0 offen sc1"
#define FL_CHECK(N) "s_mov_b32 s48, 0x100000\n7:\n\tv_cmp_eq_u64 vcc, s[84:85], v[124:125]\n\ts_and_b64 vcc, vcc, " N "\n\ts_cmp_eq_u64 vcc, " N \
                    "\n\ts_cbranch_scc1 8f\n\ts_sleep 2\n\t" FL_POLL "\n\ts_waitcnt vmcnt(0)\n\ts_sub_u32 s48, s48, 1\n\ts_cbranch_scc0 7b\n8:"
template <int L> __device__ __forceinline__ void fl_set() {
  asm volatile("v_readlane_b32 s48, v78, %0\n\ts_nop 4\n\t" FL_SET("s48") ::"i"(L) : "memory");
}
template <int L> __device__ __forceinline__ void fl_poll() {
  asm volatile("v_readlane_b32 s50, v78, %0\n\tv_readlane_b32 s51, v78, %1\n\ts_nop 1\n\t" FL_ARM("s[50:51]") "\n\t" FL_POLL ::"i"(L), "i"(L + 1) : "memory");
}
#define TICKET "buffer_atomic_add_x2 v[240:241], v81, s[88:91], s101 offen sc0"
__device__ __forceinline__ void fl_check() { asm volatile(FL_CHECK("s[50:51]") ::: "vcc", "scc", "memory"); }
template <int R> __device__ __forceinline__ float vget() { float t; asm volatile("v_mov_b32 %0, v%c1" : "=v"(t) : "i"(R)); return t; }
template <int R> __device__ __forceinline__ void vset(float x) { asm volatile("v_mov_b32 v%c0, %1" ::"i"(R), "v"(x)); }
template <int R> __device__ __forceinline__ float acc_read1() { float t; asm volatile("v_accvgpr_read_b32 %0, a[%c1]" : "=v"(t) : "i"(R)); return t; }
template <class F, int... I> __device__ __forceinline__ void static_for(F&& f, std::integer_sequence<int, I...>) {
  (f(std::integral_constant<int, I>{}), ...);
}
template <int... Is> using iseq = std::integer_sequence<int, Is...>;
template <int N> using make_iseq = std::make_integer_sequence<int, N>;

typedef __attribute__((ext_vector_type(4))) unsigned u32x4c;
typedef __attribute__((ext_vector_type(4))) float f32x4c;
typedef __attribute__((ext_vector_type(2))) float f32x2c;
typedef unsigned long long u64;
struct PR { u32x4c kk[7], vv[4]; };
__device__ __forceinline__ void prep_load(PR& p, int tid, int cg, int tb, const bf16* __restrict__ k, const bf16* __restrict__ v,
                                          const bf16* __restrict__ shift_k) {
  const __amdgpu_buffer_rsrc_t sk = __builtin_amdgcn_make_buffer_rsrc((void*)k, (short)0, SEQ * DA * 2, 0x00020000);
  const __amdgpu_buffer_rsrc_t sv = __builtin_amdgcn_make_buffer_rsrc((void*)v, (short)0, SEQ * DA * 2, 0x00020000);
  const __amdgpu_buffer_rsrc_t sw = __builtin_amdgcn_make_buffer_rsrc((void*)(shift_k + cg * 256), (short)0, 512, 0x00020000);
  const int vo = ((tb * 128 + (tid >> 3) * 4) * DA + cg * 64 + (tid & 7) * 8) * 2;
#pragma unroll
  for (int q = 0; q < 7; ++q)
    asm volatile("buffer_load_dwordx4 %0, %1, %2, 0 offen" NTPS : "=v"(p.kk[q]) : "v"(vo + (q - 3) * DA * 2), "s"(sk) : "memory");
#pragma unroll
  for (int q = 0; q < 4; ++q)
    asm volatile("buffer_load_dwordx4 %0, %1, %2, 0 offen offset:%3" NTVS : "=v"(p.vv[q]) : "v"(vo), "s"(sv), "i"(q * DA * 2) : "memory");
#pragma unroll
  for (int q = 0; q < 4; ++q)
    asm volatile("buffer_load_dwordx4 a[%c0:%c1], %2, %3, 0 offen offset:%4" ::"i"(4 * q), "i"(4 * q + 3), "v"((tid & 7) * 64), "s"(sw), "i"(q * 16) : "memory");
}
__device__ __forceinline__ float bfl(unsigned u) { return __uint_as_float(u << 16); }
__device__ __forceinline__ float bfh(unsigned u) { return __uint_as_float(u & 0xffff0000u); }
__device__ __forceinline__ f32x2c rbf2(f32x2c a) {
  return (f32x2c){__uint_as_float(pk2(0.f, a[0])), __uint_as_float(pk2(0.f, a[1]))};
}
template <int I> __device__ __forceinline__ void prep_math(const PR& p, int tid, char* lx) {
  const int rp = tid >> 3, c = tid & 7;
  f32x2c wt[4], kf[7];
  const unsigned w[4] = {__float_as_uint(acc_read1<4 * I>()), __float_as_uint(acc_read1<4 * I + 1>()), __float_as_uint(acc_read1<4 * I + 2>()),
                         __float_as_uint(acc_read1<4 * I + 3>())};
#pragma unroll
  for (int t = 0; t < 4; ++t)
    wt[t] = (t & 1) ? (f32x2c){bfh(w[t >> 1]), bfh(w[2 + (t >> 1)])} : (f32x2c){bfl(w[t >> 1]), bfl(w[2 + (t >> 1)])};
#pragma unroll
  for (int q = 0; q < 7; ++q) kf[q] = (f32x2c){bfl(p.kk[q][I]), bfh(p.kk[q][I])};
  f32x2c x[4];
#pragma unroll
  for (int e = 0; e < 4; ++e) {
    f32x2c s = rbf2(kf[e] * wt[3]);
    s = rbf2(s + rbf2(kf[e + 1] * wt[2]));
    s = rbf2(s + rbf2(kf[e + 2] * wt[1]));
    s = rbf2(s + rbf2(kf[e + 3] * wt[0]));
    x[e] = s * (f32x2c){bfl(p.vv[e][I]), bfh(p.vv[e][I])};
  }
#pragma unroll
  for (int u = 0; u < 2; ++u)
    *(u32x2*)(lx + (c * 8 + 2 * I + u) * 256 + (((31 - rp) * 8) ^ (c << 4))) = (u32x2){pk2(x[3][u], x[2][u]), pk2(x[1][u], x[0][u])};
}
__device__ __forceinline__ void prep_store(int tid, int cg, int tb, bf16* __restrict__ xr, const char* lx) {
  const __amdgpu_buffer_rsrc_t sx = __builtin_amdgcn_make_buffer_rsrc((void*)(xr + (size_t)cg * 64 * SEQ), (short)0, 64 * SEQ * 2, 0x00020000);
#pragma unroll
  for (int rd = 0; rd < 4; ++rd) {
    const int idx = tid + 256 * rd, ch = idx >> 4, q = idx & 15;
    u32x4c w = *(const u32x4c*)(lx + ch * 256 + 16 * (q ^ (ch >> 3)));
    __builtin_amdgcn_raw_buffer_store_b128(w, sx, (ch * SEQ + (15 - tb) * 128 + q * 8) * 2, 0, 16);
  }
}

#define CODE_TOUCH(LB, LE, wx, NW, NL, m0v) do { unsigned blo_, bhi_, elo_; \
    asm volatile("s_getpc_b64 s[48:49]\n\ts_add_u32 s48, s48, " LB "@rel32@lo+4\n\ts_addc_u32 s49, s49, " LB "@rel32@hi+12\n\t" \
                 "s_mov_b32 %0, s48\n\ts_mov_b32 %1, s49\n\t" \
                 "s_getpc_b64 s[48:49]\n\ts_add_u32 s48, s48, " LE "@rel32@lo+4\n\ts_mov_b32 %2, s48" \
                 : "=s"(blo_), "=s"(bhi_), "=s"(elo_) :: "scc"); \
    const unsigned n_ = elo_ - (blo_ & ~127u); \
    const __amdgpu_buffer_rsrc_t srd_ = __builtin_amdgcn_make_buffer_rsrc((void*)(((unsigned long long)bhi_ << 32) | (blo_ & ~127u)), (short)0, n_ < 262144u ? n_ : 0u, 0x00020000); \
    const unsigned l_ = threadIdx.x & 63, vo_ = l_ < (NL) ? ((wx) + l_ * (NW)) * 128u : 0x80000000u; \
    asm volatile("s_mov_b32 m0, %0\n\ts_nop 4\n\tbuffer_load_dword %1, %2, 0 offen lds" ::"s"(m0v), "v"(vo_), "s"(srd_) : "memory"); } while (0)
template <int I> __device__ __forceinline__ void d_store() {
  asm volatile("buffer_store_dwordx4 v[%c0:%c1], v%c2, s[88:91], s101 offen offset:%3 sc1" ::"i"(128 + 4 * I), "i"(131 + 4 * I), "i"(112 + ((I >> 2) & 1)),
               "i"((I & 3) * 1024) : "memory");
  if constexpr ((I & 7) == 7) asm volatile("s_addk_i32 s101, 0x4000" ::: "scc");
}
constexpr int NS_X = 19;
struct CV { unsigned pa[4], pb, px, vz, sz; float D; };
constexpr int cv_J(bool up, int q) { int J = up ? 0 : 7; while (q > J) { q -= J + 1; J += up ? 1 : -1; } return J; }
constexpr int cv_i(bool up, int q) { int J = up ? 0 : 7; while (q > J) { q -= J + 1; J += up ? 1 : -1; } return q; }
template <int R, int N> __device__ __forceinline__ void cv_rd(CV& c) {
  if constexpr (N < 16) {
    constexpr int i = N >> 1, o = 16 * R + 128 * (i & 1) + 2 * (N & 1), r = 32 + 32 * (R & 1) + 2 * N;
    asm volatile("ds_read2_b32 a[%c1:%c2], %0 offset0:%c3 offset1:%c4" ::"v"(c.pa[i >> 1]), "i"(r), "i"(r + 1), "i"(o), "i"(o + 1));
  } else {
    constexpr int m = N - 16, r = 192 + 32 * (R & 1) + 4 * m;
    asm volatile("ds_read_b128 a[%c1:%c2], %0 offset:%c3" ::"v"(c.pb), "i"(r), "i"(r + 3), "i"(64 * R + 512 * (7 - m)));
  }
}
template <int O> __device__ __forceinline__ void cv_xd(CV& c) {
  constexpr int e = O & 31, x = 96 + ((e >> 1) ^ 1);
  if constexpr (O >= 64) asm volatile("v_cvt_pk_bf16_f32 v%c0, 0, v%c0" ::"i"(160 + e));
  else if constexpr (O >= 32) asm volatile("v_mul_f32 v%c0, v%c0, %1" ::"i"(160 + e), "v"(c.D));
  else if constexpr (O & 1) asm volatile("v_lshlrev_b32 v%c0, 16, v%c1" ::"i"(160 + e), "i"(x));
  else asm volatile("v_and_b32 v%c0, 0xffff0000, v%c1" ::"i"(160 + e), "i"(x));
}
template <int O> __device__ __forceinline__ void cv_z(CV& c) {
  constexpr int J = O / 11, k = O % 11, y = 128 + 4 * J;
  if constexpr (k < 4) asm volatile("v_cvt_pk_bf16_f32 v%c0, 0, v%c0" ::"i"(y + k));
  else if constexpr (k < 8) asm volatile("v_add_f32 v%c0, v%c0, v%c1" ::"i"(y + k - 4), "i"(y + 28 + k));
  else if constexpr (k < 10) asm volatile("v_cvt_pk_bf16_f32 v%c0, v%c1, v%c2" ::"i"(y + k - 8), "i"(y + 2 * k - 16), "i"(y + 2 * k - 15));
  else asm volatile("buffer_store_dwordx2 v[%c0:%c1], %2, s[88:91], %3 offen offset:%4 sc1" ::"i"(y), "i"(y + 1), "v"(c.vz), "s"(c.sz),
                    "i"(512 * J) : "memory");
}
constexpr int cv_z0(int Q) {
  int o = 0;
  for (int q = 0; q < Q; ++q)
    for (int k = 0; k < (q % 4 == 1 ? 0 : 3); ++k) { const int J = o / 11; if ((J + 1) * (J + 2) / 2 - 1 + (J ? 4 : 7) <= q) ++o; }
  return o;
}
constexpr int cv_xd0(int T) { int o = 0; for (int t = 0; t < T; ++t) o += t % 4 != 1 || (t >= 72 && t < 108); return o < 96 ? o : 96; }
static_assert(cv_xd0(144) == 96);
template <int B, int... Is> __device__ __forceinline__ void cv_zs(CV& c, iseq<Is...>) { (cv_z<B + Is>(c), ...); }
template <int R, int Q> __device__ __forceinline__ void cv_slot(CV& c) {
  constexpr int i = cv_i(R == 7, Q), J = cv_J(R == 7, Q), d = 128 + 4 * J, a = 32 + 32 * (R & 1) + 4 * i, b = 192 + 32 * (R & 1) + 4 * (J - i);
  constexpr int T = 36 * R + Q, vm = Q % 4 == 1 && Q < 32 ? Q / 4 : -1;
  if constexpr (Q == 0) asm volatile("s_waitcnt lgkmcnt(0)");
  if constexpr (R >= 6 && vm >= 0 && vm % 4 == 0) asm volatile("s_add_u32 m0, s79, %0" ::"i"(((R - 6) ^ (NYT & 1)) * 32768 + vm * 1024));
  if constexpr (R == 0 && i == 0)
    asm volatile("v_mfma_f32_16x16x32_bf16 v[%c0:%c1], a[%c2:%c3], a[%c4:%c5], 0" ::"i"(d), "i"(d + 3), "i"(a), "i"(a + 3), "i"(b), "i"(b + 3));
  else
    asm volatile("v_mfma_f32_16x16x32_bf16 v[%c0:%c1], a[%c2:%c3], a[%c4:%c5], v[%c0:%c1]" ::"i"(d), "i"(d + 3), "i"(a), "i"(a + 3), "i"(b), "i"(b + 3));
  if constexpr (Q < 24 && R < 7) cv_rd<R + 1, Q>(c);
  if constexpr (T % 7 == 3 && NS_X + 5 + T / 7 < 32) d_store<NS_X + 5 + T / 7>();
  if constexpr (R == 2 && Q >= 24 && Q < 32)
    asm volatile("ds_read_b64 v[%c1:%c2], %0 offset:%c3" ::"v"(c.px), "i"(96 + 2 * (Q - 24)), "i"(97 + 2 * (Q - 24)), "i"(512 * (31 - Q)));
  if constexpr (R >= 3 && R < 7) if constexpr (cv_xd0(T - 107) > cv_xd0(T - 108)) cv_xd<cv_xd0(T - 108)>(c);
  if constexpr ((R == 3 || R == 4) && vm >= 0) {
    constexpr int f = (R - 3) * 8 + vm;
    asm volatile("buffer_load_dwordx4 a[%c0:%c1], v84, s[88:91], s96 offen offset:%2 sc1" ::"i"(128 + 4 * f), "i"(131 + 4 * f), "i"((f & 3) * 1024) : "memory");
    if constexpr ((f & 3) == 3) asm volatile("s_addk_i32 s96, %0" ::"i"(f == 7 ? 28672 : 4096));
  }
  if constexpr (R == 4 && Q == 35) asm volatile(FL_POLL ::: "memory");
  if constexpr (R >= 6 && vm >= 0)
    asm volatile("buffer_load_dwordx4 v%c0, s[88:91], s98 offen offset:%1 sc1 lds" ::"i"(86 + vm / 4), "i"((vm & 3) * 1024) : "memory");
  if constexpr (R == 6 && Q == 35) asm volatile("s_add_u32 s98, s98, 0x8000");
  if constexpr (R == 7) cv_zs<cv_z0(Q)>(c, make_iseq<cv_z0(Q + 1) - cv_z0(Q)>{});
  __builtin_amdgcn_sched_barrier(0);
}
template <int R, int... Q> __device__ __forceinline__ void cv_step(CV& c, iseq<Q...>) { (cv_slot<R, Q>(c), ...); }
template <int... N> __device__ __forceinline__ void cv_rd0(CV& c, iseq<N...>) { (cv_rd<0, N>(c), ...); }
template <int FLAG> __device__ __forceinline__ void conv_wave(unsigned sz, float Dd, char* lw) {
  int lane = threadIdx.x & 63; asm volatile("" : "+v"(lane));
  const int l16 = lane & 15, fq = lane >> 4;
  u32x4c w[5]; unsigned w4[5];
#pragma unroll
  for (int r = 0; r < 5; ++r) {
    w[r] = *(const u32x4c*)(lw + r * 1024 + lane * 16);
    w4[r] = *(const unsigned*)(lw + r * 1024 + lane * 16 + 16);
  }
  d_store<NS_X>(); d_store<NS_X + 1>();
#pragma unroll
  for (int r = 0; r < 5; ++r)
    *(u32x4c*)(lw + 10304 + r * 1024 + lane * 16) = (u32x4c){__builtin_amdgcn_alignbit(w[r][1], w[r][0], 16), __builtin_amdgcn_alignbit(w[r][2], w[r][1], 16),
                                                             __builtin_amdgcn_alignbit(w[r][3], w[r][2], 16), __builtin_amdgcn_alignbit(w4[r], w[r][3], 16)};
  d_store<NS_X + 2>();
  CV c;
#pragma unroll
  for (int i = 0; i < 4; ++i) c.pa[i] = (unsigned)(size_t)lw + ((l16 + 1) & 1) * 10304 + 4 * ((8 * fq + l16 + 1) >> 1) + 1024 * i;
  c.pb = (unsigned)(size_t)lw + 5120 + (2032 - 16 * l16 + 8 * fq) * 2 - 512 * 7;
  c.vz = (16 * l16 + 4 * fq) * 2;
  c.px = (unsigned)(size_t)lw + 5120 + 2044 * 2 - 512 * 7 - c.vz;
  c.sz = sz; c.D = Dd;
  asm volatile("" : "+v"(c.vz), "+v"(c.px), "+v"(c.D) :: "memory");
  asm volatile("" : "+s"(c.sz));
  cv_rd0(c, make_iseq<24>{});
  d_store<NS_X + 3>(); d_store<NS_X + 4>();
  __builtin_amdgcn_sched_barrier(0);
  static_for([&](auto R) __attribute__((always_inline)) {
    cv_step<R>(c, make_iseq<36>{});
    if constexpr (R == 2) {
      asm volatile("s_waitcnt vmcnt(0)\n\ts_barrier" ::: "memory");
      fl_set<FLAG>();
    }
    if constexpr (R == 5) {
      asm volatile("s_waitcnt vmcnt(0)" ::: "memory");
      fl_check();
    }
    __builtin_amdgcn_sched_barrier(0);
  }, make_iseq<8>{});
  asm volatile("s_nop 7");
  cv_zs<cv_z0(36)>(c, make_iseq<88 - cv_z0(36)>{});
}

__device__ __forceinline__ void post_dma(int cg, int tb, const bf16* __restrict__ q, unsigned m0w) {
  int lane = threadIdx.x & 63; asm volatile("" : "+v"(lane));
  const __amdgpu_buffer_rsrc_t sq = __builtin_amdgcn_make_buffer_rsrc((void*)(q + (size_t)tb * 32 * DA + cg * 64), (short)0, 32 * DA * 2, 0x00020000);
  const unsigned vq = ((lane >> 3) * DA + (lane & 7) * 8) * 2;
  asm volatile("s_mov_b32 m0, %0\n\ts_nop 4\n\tbuffer_load_dwordx4 %1, %2, 0 offen" NTQS " lds\n\tbuffer_load_dwordx4 %1, %2, %3 offen offset:1024" NTQS " lds\n\t"
               "buffer_load_dwordx4 %1, %2, %4 offen offset:2048" NTQS " lds\n\tbuffer_load_dwordx4 %1, %2, %5 offen offset:3072" NTQS " lds"
               ::"s"(m0w + 4096), "v"(vq), "s"(sq), "s"(8 * DA * 2 - 1024), "s"(16 * DA * 2 - 2048), "s"(24 * DA * 2 - 3072) : "memory");
}
__device__ __forceinline__ void post_math(int cg, int tb, bf16* __restrict__ out, const char* lw) {
  int lane = threadIdx.x & 63; asm volatile("" : "+v"(lane));
  const int tl = lane >> 3, ck = lane & 7;
  const __amdgpu_buffer_rsrc_t so = __builtin_amdgcn_make_buffer_rsrc((void*)(out + (size_t)tb * 32 * OUTW + cg * 64), (short)0, 32 * OUTW * 2 - 2 * DA, 0x00020000);
  const unsigned vo = (tl * OUTW + ck * 8) * 2;
#pragma unroll
  for (int i = 0; i < 4; ++i) {
    const u32x4c qv = *(const u32x4c*)(lw + 4096 + i * 1024 + lane * 16);
    const char* lz = lw + ck * 64 + 16 * (i ^ (ck >> 1)) + tl * 2;
    u32x4c o;
#pragma unroll
    for (int e = 0; e < 4; ++e) {
      const float z0 = bf2f(*(const unsigned short*)(lz + 2 * e * 512));
      const float z1 = bf2f(*(const unsigned short*)(lz + (2 * e + 1) * 512));
      o[e] = pk2(bfl(qv[e]) * z0, bfh(qv[e]) * z1);
    }
    __builtin_amdgcn_raw_buffer_store_b128(o, so, vo + i * 8 * OUTW * 2, 0, OUTSC);
  }
}

#define DI __device__ __forceinline__
#define GAP() __builtin_amdgcn_sched_barrier(0)
typedef __attribute__((ext_vector_type(4))) float f32x4g;
typedef __attribute__((ext_vector_type(4))) unsigned u32x4g;
constexpr int LDW = NH * HD;
struct GM { unsigned aA[2], aB[2][8]; };
template <bool W, int J, int V, bool NT = false> DI void g_dma() {
  if constexpr (NT && J == 0) asm volatile("buffer_load_dwordx4 v%c0, s[60:63], 0 offen nt lds" ::"i"(V) : "memory");
  else if constexpr (NT) asm volatile("buffer_load_dwordx4 v%c0, s[60:63], s%c1 offen nt lds" ::"i"(V), "i"(70 + J) : "memory");
  else
  if constexpr (J == 0) asm volatile("buffer_load_dwordx4 v%c0, s[%c1:%c2], 0 offen lds" ::"i"(V), "i"(W ? 60 : 56), "i"(W ? 63 : 59) : "memory");
  else asm volatile("buffer_load_dwordx4 v%c0, s[%c1:%c2], s%c3 offen lds" ::"i"(V), "i"(W ? 60 : 56), "i"(W ? 63 : 59), "i"((W ? 70 : 63) + J) : "memory");
}
template <int P, int KS, int I> DI void g_wread(GM& g) {
  constexpr int nf = I >> 1, hf = I & 1, r = 192 - 64 * P + 8 * nf + 4 * KS + 2 * hf;
  asm volatile("ds_read_b64_tr_b16 a[%c1:%c2], %0 offset:%c3" ::"v"(g.aB[hf][nf]), "i"(r), "i"(r + 1), "i"(P * 32768 + KS * 16384));
}
template <int P, int M, int KS> DI void g_aread(GM& g) {
  constexpr int r = 96 + 8 * (M & 1) + 4 * KS;
  asm volatile("ds_read_b128 v[%c1:%c2], %0 offset:%c3" ::"v"(g.aA[KS]), "i"(r), "i"(r + 3), "i"(P * 32768 + M * 2048));
}
constexpr float CS = 0.0625f * 1.4426950408889634f;
constexpr int NS_Q = 19;
constexpr int e_vm(int G) { return (G * 3) % 8 < 3 ? G * 3 / 8 : -1; }
constexpr int e_st(int MODE, int G) {
  const int q = G & 15, k3 = ((G >> 4) - 1) * 3 + (q == 0 ? 0 : q == 4 ? 1 : 2), k2 = NS_Q + (G >> 4) * 2 + (q == 4);
  if (MODE == 5) return e_vm(G) >= 32 ? e_vm(G) - 32 : -1;
  if (MODE == 3) return G >= 33 && (G - 33) % 5 == 0 && (G - 33) / 5 < NS_X ? (G - 33) / 5 : -1;
  if (MODE == 2) return G >= 30 && (q == 0 || q == 4 || q == 14) && k3 - 2 < NS_Q ? k3 - 2 : -1;
  if (MODE == 4) return (q == 0 || q == 4) && k2 < 32 ? k2 : -1;
  return -1;
}
constexpr int e_nst(int MODE, int G0, int G1) { int n = 0; for (int G = G0; G < G1; ++G) n += e_st(MODE, G) >= 0; return n; }
static_assert(e_nst(2, 0, 128) == NS_Q && e_nst(3, 30, 128) == NS_X && e_nst(5, 0, 128) == 16 && e_nst(4, 0, 104) == 32 - NS_Q);
constexpr int e_slot(int MODE, int G) { return MODE == 3 ? (G % 3 == 0 && G < 30 ? G / 3 : -1) : e_vm(G) >= 0 && e_vm(G) < 32 ? 10 + e_vm(G) : -1; }
struct ES { int op0[129], nop[129]; };
constexpr ES e_sched(bool TR, int MODE) {
  ES z{};
  const bool SCL = MODE == 2 || MODE == 4;
  const int per = SCL ? 16 : 8, o1 = 32 * per;
  int o = MODE == 4 ? e_sched(false, 2).op0[128] : 0;
  for (int G = 0; G <= 128; ++G) {
    const int q = G & 15;
    int cap = 2 - (q == 1 || q == 2 || q == 15);
    if (SCL) cap -= (q >= 3 && q < 11 && (q & 1)) + (q == 7 || q == 11) + 2 * (G == 70 || G == 74 || (G & 63) == 61);
    cap += MODE == 2 ? 4 + (q == 10 || q == 13) : MODE == 3 ? 1 : MODE == 5 ? 2 : 0;
    if (e_st(MODE, G) >= 0 || (SCL ? q == 8 || q == 12 : e_slot(MODE, G) >= 0) || (MODE == 4 && G < 4)) cap = 0;
    if (G == 128) cap = 10000;
    z.op0[G] = o;
    while (o < o1) {
      const int i = o / per, rdy = MODE == 4 ? 0 : (TR ? 32 * (i >> 3) + 16 + 2 * (i & 7) + 1 : 4 * i + 3) + 6;
      if ((G < rdy && G < 128) || cap-- <= 0) break;
      ++o; ++z.nop[G];
    }
  }
  return z;
}
template <bool TR, int MODE> constexpr ES ESCH = e_sched(TR, MODE);
static_assert(ESCH<false, 4>.op0[96] == 512 && ESCH<false, 4>.op0[0] >= 24 * 16);
template <bool TR, bool SCL, int O> DI void e_op() {
  constexpr int per = SCL ? 16 : 8, i = O / per, k = O % per, r = 128 + 4 * i, t = 114 + 4 * (i & 1), c = k - (per - 4);
  if constexpr (k < 4) asm volatile("v_accvgpr_read_b32 v%c0, a[%c1]" ::"i"(t + k), "i"(4 * i + k));
  else if constexpr (c < 0) asm volatile("v_mul_f32 v%c0, s83, v%c0" ::"i"(k < 8 ? r + (k ^ 6) : t + k - 8));
  else {
    constexpr int d = c == 0 ? r + 3 : c == 1 ? r + 2 : r + c - 2, x = c == 0 ? r + 2 : c == 1 ? r : t + 2 * (c - 2);
    asm volatile("v_cvt_pk_bf16_f32 v%c0, v%c1, v%c2" ::"i"(d), "i"(x), "i"(x + 1));
  }
}
template <bool TR, bool SCL, int B, int... Is> DI void e_ops(iseq<Is...>) { (e_op<TR, SCL, B + Is>(), ...); }
template <int S> DI void e_m0() {
  if constexpr (S == 0 || S == 1 || S == 5 || S == 9) asm volatile("s_add_u32 m0, s78, %0" ::"i"(S * 1024));
  if constexpr (S >= 26 && (S - 26) % 4 == 0) asm volatile("s_add_u32 m0, s79, %0" ::"i"(((S - 26) >> 3) * 32768 + ((S - 26) & 4) * 1024));
}
template <int S> DI void e_load() {
  if constexpr (S == 0) asm volatile("buffer_load_dwordx4 v122, s[56:59], 0 offen lds" ::: "memory");
  else if constexpr (S < 5) asm volatile("buffer_load_dwordx4 v84, s[56:59], s64 offen offset:%0 lds" ::"i"((S - 1) * 1024) : "memory");
  else if constexpr (S < 9) asm volatile("buffer_load_dwordx4 v84, s[60:63], 0 offen offset:%0 sc1 lds" ::"i"((S - 5) * 1024) : "memory");
  else if constexpr (S == 9) asm volatile("buffer_load_dwordx4 v84, s[60:63], s62 offen sc1 lds" ::: "memory");
  else if constexpr (S < 26) {
    constexpr int f = S - 10;
    asm volatile("buffer_load_dwordx4 a[%c0:%c1], v84, s[88:91], s96 offen offset:%2 sc1" ::"i"(128 + 4 * f), "i"(131 + 4 * f), "i"((f & 3) * 1024) : "memory");
    if constexpr ((f & 3) == 3) asm volatile("s_addk_i32 s96, %0" ::"i"(f == 7 ? 28672 : 4096));
  } else {
    constexpr int f = S - 26;
    asm volatile("buffer_load_dwordx4 v%c0, s[88:91], s98 offen offset:%1 sc1 lds" ::"i"(86 + ((f >> 2) & 1)), "i"((f & 3) * 1024) : "memory");
    if constexpr (f == 7) asm volatile("s_add_u32 s98, s98, s97");
    if constexpr (f == 15) asm volatile(FL_POLL ::: "memory");
  }
}
template <bool TR, int P, int H, int G, int MODE, bool NT> DI void g_gap(GM& g) {
  constexpr int m = 4 * H + (G >> 4), q = G & 15, n = q >> 1, ks = q & 1, GG = 64 * H + G;
  constexpr bool inV = TR ? m & 1 : n & 1, END = MODE == 3 || MODE == 5;
  constexpr int a = 96 + 8 * (m & 1) + 4 * ks, b = 192 - 64 * P + 8 * n + 4 * ks, d = (inV ? 128 : 0) + 4 * (TR ? 8 * (m >> 1) + n : 4 * m + (n >> 1));
  constexpr int J = END ? -1 : q == 8 ? 2 * (G >> 4) : q == 12 ? 2 * (G >> 4) + 1 : -1, gj = (J & 3) ^ ((J & 3) >> 1);
  if constexpr (END) { if constexpr (q == 0 && GG > 0) asm volatile("s_waitcnt lgkmcnt(0)"); e_m0<e_slot(MODE, GG)>(); }
  else if constexpr (q == 0 && G > 0) asm volatile("s_waitcnt lgkmcnt(4)");
  if constexpr (J == 0) asm volatile("s_add_u32 m0, s78, %0" ::"i"((P ^ H ^ 1) * 32768 + (1 - H) * 8192));
  else if constexpr (J == 4) asm volatile("s_add_u32 m0, s79, %0" ::"i"((P ^ H) * 32768 + (1 - H) * 16384));
  else if constexpr (J > 0 && (J & 3) == 1) asm volatile("s_bitset1_b32 m0, 12");
  else if constexpr (J > 0 && (J & 3) == 2) asm volatile("s_bitset1_b32 m0, %0" ::"i"(J < 4 ? 14 : 13));
  else if constexpr (J > 0) asm volatile("s_bitset0_b32 m0, 12");
#define GMF(D, X, Y, C) asm volatile("v_mfma_f32_16x16x32_bf16 " D "[%c0:%c1], " X ", " Y ", " C ::"i"(d), "i"(d + 3), "i"(a), "i"(a + 3), "i"(b), "i"(b + 3))
#define GMF2(D, C) do { if constexpr (TR) GMF(D, "v[%c2:%c3]", "a[%c4:%c5]", C); else GMF(D, "a[%c4:%c5]", "v[%c2:%c3]", C); } while (0)
  if constexpr ((MODE == 1 || MODE == 4) && ks == 0) { if constexpr (inV) GMF2("v", "0"); else GMF2("a", "0"); }
  else if constexpr (inV) GMF2("v", "v[%c0:%c1]");
  else GMF2("a", "a[%c0:%c1]");
#undef GMF2
#undef GMF
  if constexpr ((q == 1 || q == 2) && !(END && m == 7)) g_aread<(m < 7 ? P : P ^ 1), (m + 1) & 7, q - 1>(g);
  if constexpr (!END && q >= 3 && q < 11 && (q & 1)) g_wread<P ^ 1, H, (G >> 4) * 4 + (q - 3) / 2>(g);
  if constexpr (e_st(MODE, GG) >= 0) {
    static_assert(ESCH<TR, MODE>.op0[GG] >= (e_st(MODE, GG) + 1) * (END ? 8 : 16));
    d_store<e_st(MODE, GG)>();
  }
  if constexpr (J >= 0 && J < 4) g_dma<false, (1 - H) * 2 + (gj & 1) + (gj >> 1) * 4, 90>();
  else if constexpr (J >= 4) g_dma<true, (1 - H) * 4 + gj, 91, NT>();
  if constexpr (MODE >= 2) e_ops<TR, !END, ESCH<TR, MODE>.op0[GG]>(make_iseq<ESCH<TR, MODE>.nop[GG]>{});
  if constexpr (END) {
    if constexpr (e_slot(MODE, GG) >= 0) e_load<e_slot(MODE, GG)>();
  } else {
    if constexpr (H == 1 && G == 6) asm volatile("v_lshl_add_u32 v90, s82, 7, v92\n\ts_and_b32 s82, s80, 15");
    if constexpr (H == 1 && G == 10) asm volatile("s_lshr_b64 s[80:81], s[80:81], 4\n\tv_lshl_add_u32 v91, s82, 19, v93");
    constexpr int nst = e_nst(MODE, H ? 29 : 0, GG + 1) + (MODE == 4 && !H ? e_nst(2, 93, 128) : 0);
    if constexpr (G == 61) asm volatile("s_waitcnt vmcnt(%0) lgkmcnt(0)\n\ts_barrier" ::"i"(12 + nst) : "memory");
  }
  if constexpr (END && GG == 127) {
    asm volatile("s_nop 7");
    e_ops<TR, false, ESCH<TR, MODE>.op0[128]>(make_iseq<ESCH<TR, MODE>.nop[128]>{});
  }
  GAP();
}
template <bool TR, int P, int H, int MODE, bool NT, int... Gs> DI void g_half_(GM& g, iseq<Gs...>) { (g_gap<TR, P, H, Gs, MODE, NT>(g), ...); }
template <bool TR, int P, int H, int MODE, bool NT = false> DI void g_half(GM& g) { g_half_<TR, P, H, MODE, NT>(g, make_iseq<64>{}); }
template <bool TR, int P, int MODE, bool NT = false> DI void g_tile(GM& g) { g_half<TR, P, 0, MODE, NT>(g); g_half<TR, P, 1, MODE, NT>(g); }
template <int P, int... Is> DI void g_read0(GM& g, iseq<Is...>) {
  (g_wread<P, 0, Is>(g), ...); (g_wread<P, 1, Is>(g), ...);
  g_aread<P, 0, 0>(g); g_aread<P, 0, 1>(g);
}
DI void g_proW(unsigned g, unsigned mW) {
  asm volatile("v_lshl_add_u32 v91, %0, 18, v93\n\ts_mov_b32 m0, %1" ::"s"(g), "s"(mW));
  asm volatile("s_nop 0"); g_dma<true, 0, 91>();
  asm volatile("s_bitset1_b32 m0, 12\n\ts_nop 0"); g_dma<true, 1, 91>();
  asm volatile("s_bitset1_b32 m0, 13\n\ts_nop 0"); g_dma<true, 3, 91>();
  asm volatile("s_bitset0_b32 m0, 12\n\ts_nop 0"); g_dma<true, 2, 91>();
}
template <int PART = 3> DI void g_pro(unsigned g, unsigned mA, unsigned mW, unsigned lda) {
  g &= 3;
  if constexpr (PART & 1)
    asm volatile("v_add_u32 v90, %0, v92\n\ts_mov_b32 m0, %1" ::"s"((g >> 1) * 128 + (g & 1) * 128 * lda), "s"((mA ^ (g >> 1) * 32768) + (g & 1) * 8192));
  if constexpr (PART == 1) return;
  asm volatile("s_nop 0"); g_dma<false, 0, 90>();
  asm volatile("s_bitset1_b32 m0, 12\n\ts_nop 0"); g_dma<false, 1, 90>();
  asm volatile("s_bitset1_b32 m0, 14\n\ts_nop 0"); g_dma<false, 5, 90>();
  asm volatile("s_bitset0_b32 m0, 12\n\ts_nop 0"); g_dma<false, 4, 90>();
  g_proW(g, mW ^ g * 16384);
}
template <bool TR, bool SCL, class HOOK> DI void g_loop(GM& g, unsigned evm, HOOK&& hook) {
  constexpr int nkt = SCL ? 7 : 16, P0 = SCL ? 0 : 1;
  asm volatile("" : "+s"(evm));
#pragma unroll 1
  for (int t = 1;; t += 2) {
    g_half<TR, P0 ^ 1, 0, 0, (SCL ? NTW >= 1 : NTW >= 2)>(g);
    if (__builtin_expect((evm >> t) & 1, 0)) { hook(t); GAP(); }
    g_half<TR, P0 ^ 1, 1, 0, (SCL ? NTW >= 1 : NTW >= 2)>(g);
    if (SCL && t + 2 >= nkt) break;
    g_half<TR, P0, 0, 0, (SCL ? NTW >= 1 : NTW >= 2)>(g);
    if constexpr (SCL) { if (__builtin_expect(t == 3, 0)) { hook(4); GAP(); } }
    else asm volatile("s_cmp_eq_u32 %0, 13\n\ts_cselect_b32 s78, 0x28000, s78" ::"s"(t) : "scc");
    g_half<TR, P0, 1, 0, (SCL ? NTW >= 1 : NTW >= 2)>(g);
    if (!SCL && t + 3 >= nkt) break;
  }
}

typedef __attribute__((ext_vector_type(4))) unsigned u32x4;
typedef __attribute__((ext_vector_type(4))) float f32x4;

constexpr int R_S = 128, R_P = 192, R_CI = 224, R_E = 96;
constexpr int R_TS = 112, R_RS = 114, R_M = 116;
constexpr int R_PD = 118, R_THR = 120, R_TMP = 121;
constexpr int r_dk(int q) { return q < 6 ? 90 + q : 120 + q; }
constexpr int S_V = 96, S_T = 97, S_K = 98, S_I = 99, S_EV = 100;
struct AC {
  unsigned rare;
  unsigned tb, vs, o0, o1;
};
DI bool redo() { u64 t; asm volatile("s_mov_b64 %0, s[54:55]" : "=s"(t)); return t != 0; }
template <int X, int F> DI void kread() {
  constexpr int r = 192 + 4 * (F & 15);
  asm volatile("ds_read_b128 a[%c0:%c1], v84 offset:%c2" ::"i"(r), "i"(r + 3), "i"(X * 32768 + F * 1024));
}
template <int X, int F> DI void vread() {
  constexpr int r = 192 + 4 * (F & 15);
  asm volatile("ds_read_b128 a[%c0:%c1], v85 offset:%c2" ::"i"(r), "i"(r + 3), "i"(X * 32768 + (F & 1) * 16384 + (F >> 1) * 1024));
}
template <int X, int... Fs> DI void kreads(iseq<Fs...>) { (kread<X, Fs>(), ...); }
template <int X, int J> DI void dma_m0() {
  if constexpr (J == 0) asm volatile("s_mov_b32 m0, s%c0" ::"i"(78 + X));
  else if constexpr (J == 4) asm volatile("s_or_b32 m0, s%c0, 0x1000" ::"i"(78 + X));
  else if constexpr (J == 8) asm volatile("s_bitset0_b32 m0, 16");
  else if constexpr (J == 12) asm volatile("s_bitset0_b32 m0, 12");
}
template <int J> DI void dma_at() {
  if constexpr (J < 8)
    asm volatile("buffer_load_dwordx4 v%c0, s[88:91], s96 offen offset:%1 sc1 lds" ::"i"(86 + (J >= 4)), "i"((J & 3) * 1024) : "memory");
  else
    asm volatile("buffer_load_dwordx4 v%c0, s[92:95], s98 offen offset:%1 sc1 lds" ::"i"(r_dk(J - 8)), "i"((J & 3) * 1024) : "memory");
}
template <int Q> DI void dma4k() {
  asm volatile("s_add_u32 m0, s79, %0\n\ts_add_u32 s48, s98, %1\n\tbuffer_load_dwordx4 v%c2, s[88:91], s48 offen sc1 lds\n\t"
               "buffer_load_dwordx4 v%c2, s[88:91], s48 offen offset:1024 sc1 lds\n\tbuffer_load_dwordx4 v%c2, s[88:91], s48 offen offset:2048 sc1 lds\n\t"
               "buffer_load_dwordx4 v%c2, s[88:91], s48 offen offset:3072 sc1 lds"
               ::"i"((Q >> 1) * 32768 + (Q & 1) * 4096), "i"((Q >> 1) * 32768), "i"(86 + (Q & 1)) : "memory", "scc");
}
template <int Q> DI void acc_zero2() {
  asm volatile("v_accvgpr_write_b32 a[%c0], 0\n\tv_accvgpr_write_b32 a[%c1], 0" ::"i"(2 * Q), "i"(2 * Q + 1));
}
#define SMV 0
struct SMS { int ex[128], op0[128], nop[128], last; };
constexpr SMS sm_sched() {
  SMS z{};
  int et[32] = {}, e = 0;
  for (int t = 0; t < 128; ++t) {
    const int g = t & 15;
    const bool x = t < 48 ? (g >= 8 && (g & 1) && g < 15) : t < 64 ? (g >= 9 && g < 15) : (g >= 8 && g < 14);
    z.ex[t] = x && e < 32 ? e : -1;
    if (z.ex[t] >= 0) et[e++] = t;
  }
  int o = 0;
  for (int t = 0; t < 128; ++t) {
    const int g = t & 15;
    int cap = z.ex[t] >= 0 || t < 8 || (t < 48 && (g == 7 || (g >= 8 && !(g & 1)))) || t == 56 || t == 63 || t == 111 ? 0 : 1;
    z.op0[t] = o; z.nop[t] = 0;
    while (cap > 0 && o < 48 && et[2 * (o / 3) + (o % 3 == 0 ? 0 : 1)] + 2 <= t) {
      const int el = 2 * (o / 3) + o % 3;
      if (!(o % 3 < 2 && (el == 0 || el == 4))) --cap;
      ++o; ++z.nop[t]; z.last = t;
    }
  }
  for (int k = 16; k < 32; ++k) if (z.op0[et[k]] < 3 * ((k - 16) / 2 + 1)) z.last = 1000;
  return z;
}
constexpr SMS SM = sm_sched();
static_assert(SM.last <= 117);
constexpr int p_reg(int Y, int k) { return R_P + 16 * Y + 4 * ((k >> 3) * 2 + ((k >> 1) & 1)) + ((k >> 2) & 1) * 2 + (k & 1); }
template <int Y, int O> DI void sm_op() {
  constexpr int k = O / 3, e = 2 * k + O % 3, ts = R_TS + ((e >> 2) & 1);
  if constexpr (O % 3 == 2) asm volatile("v_cvt_pk_bf16_f32 v%c0, v%c1, v%c2" ::"i"(p_reg(Y, k)), "i"(R_E + ((2 * k) & 15)), "i"(R_E + ((2 * k + 1) & 15)));
  else if constexpr (e == 1 || e == 5) asm volatile("v_add_f32_e32 v%c0, v%c1, v%c2" ::"i"(ts), "i"(R_E + e - 1), "i"(R_E + e));
  else if constexpr (e != 0 && e != 4) asm volatile("v_add_f32_e32 v%c0, v%c0, v%c1" ::"i"(ts), "i"(R_E + (e & 15)));
}
template <int Y, int T> DI void sm_gap(AC& c) {
  if constexpr (SM.ex[T] >= 0) asm volatile("v_exp_f32 v%c0, v%c1" ::"i"(R_E + (SM.ex[T] & 15)), "i"(R_S + 32 * Y + SM.ex[T]));
  if constexpr (SM.nop[T] > 0) sm_op<Y, SM.op0[T]>();
  if constexpr (SM.nop[T] > 1) sm_op<Y, SM.op0[T] + 1>();
  if constexpr (SM.nop[T] > 2) sm_op<Y, SM.op0[T] + 2>();
  if constexpr (T == SM.last + 1) asm volatile("v_add_f32_e32 v%c0, v%c1, v%c2" ::"i"(R_TMP), "i"(R_TS), "i"(R_TS + 1));
  if constexpr (T == SM.last + 2) asm volatile("v_cmp_nle_f32_e64 s[54:55], v%c0, v%c1" ::"i"(R_TMP), "i"(R_THR));
}
#define W_A0 1
template <int X, int MODE, int G> DI void gapA(AC& c) {
  constexpr bool FIRST = MODE == 1 || MODE == 2, NOQK = MODE == 3;
  constexpr int Xp = X ^ 1, kb = G >> 4, g = G & 15, qb = g >> 3, s = g & 7;
  if constexpr (MODE == 3 && G < 48 && G % 3 == 0) {
    constexpr int f = G / 3;
    asm volatile("buffer_load_dwordx4 a[%c0:%c1], v84, s[88:91], s75 offen offset:%2 sc1" ::"i"(128 + 4 * f), "i"(131 + 4 * f), "i"((f & 3) * 1024) : "memory");
    if constexpr ((f & 3) == 3) asm volatile("s_addk_i32 s75, %0" ::"i"(f == 7 ? 28672 : 4096));
  }
  if constexpr (G == 48) asm volatile("s_waitcnt vmcnt(%0) lgkmcnt(0)\n\ts_barrier" ::"i"(MODE == 3 ? 16 : MODE == 2 ? 12 : MODE == 1 ? W_A0 : 0) : "memory");
  else if constexpr (g == 0 && !NOQK) asm volatile("s_waitcnt lgkmcnt(0)");
  if constexpr (!NOQK) {
    constexpr int r = 192 + 4 * ((kb & 1) * 8 + s), q = 128 + 4 * g;
    constexpr int d = R_S + 32 * X + 4 * (kb * 2 + qb), ci = s == 0 ? R_CI + 4 * (kb * 2 + qb) : d;
    if constexpr (FIRST && s == 0)
      asm volatile("v_mfma_f32_16x16x32_bf16 v[%c0:%c1], a[%c2:%c3], a[%c4:%c5], 0" ::"i"(d), "i"(d + 3), "i"(r), "i"(r + 3), "i"(q), "i"(q + 3));
    else
      asm volatile("v_mfma_f32_16x16x32_bf16 v[%c0:%c1], a[%c2:%c3], a[%c4:%c5], v[%c6:%c7]" ::"i"(d), "i"(d + 3), "i"(r), "i"(r + 3), "i"(q), "i"(q + 3),
                   "i"(ci), "i"(ci + 3));
    if constexpr (g < 8 && kb < 3) kread<X, (kb + 1) * 8 + g>();
    constexpr int S0 = SMV ? 40 : 14, S1 = SMV ? 41 : 30, S2 = SMV ? 42 : 45, S3 = SMV ? 43 : 46;
    if constexpr (G == S0) asm volatile("s_add_u32 s96, s97, 0x1000000");
    if constexpr (G == S1) dma_m0<X, 0>();
    if constexpr (G == S2) asm volatile("s_mov_b32 s97, s98");
    if constexpr (G == S3) asm volatile("s_add_u32 s98, s98, s99");
    if constexpr (G >= 56 && !(G & 1)) dma_at<(G - 56) / 2>();
    if constexpr (G == 57) asm volatile("s_lshr_b32 s100, s100, 1\n\ts_add_u32 s56, s56, 1");
    if constexpr (G == 59 && !FIRST) asm volatile("s_and_b32 s49, s100, 1\n\ts_or_b32 s49, s49, s54");
    if constexpr (G == 61 && !FIRST) asm volatile("s_or_b32 %0, s49, s55" : "=s"(c.rare));
    if constexpr (G == 61 && FIRST) asm volatile("s_and_b32 %0, s100, 1" : "=s"(c.rare));
  } else if constexpr (SM.nop[64 + G] > 0) asm volatile("s_nop 0");
  if constexpr (!FIRST && g < 8 && kb == 3) vread<Xp, g>();
  if constexpr (MODE == 2 && (G & 3) == 1) d_store<16 + G / 4>();
  if constexpr (FIRST) acc_zero2<G>();
  else sm_gap<Xp, 64 + G>(c);
  GAP();
}
template <int R> DI void rescale1(float alpha) {
  float t;
  asm volatile("v_accvgpr_read_b32 %0, a[%c2]\n\tv_mul_f32 %0, %0, %1\n\tv_accvgpr_write_b32 a[%c2], %0" : "=&v"(t) : "v"(alpha), "i"(R));
}
template <int... Rs> DI void rescale_all(const float (&alpha)[2], iseq<Rs...>) { (rescale1<Rs>(alpha[(Rs >> 2) & 1]), ...); }
template <bool MAX> DI float row_reduce(float v) {
  auto p = __builtin_amdgcn_permlane32_swap(__float_as_uint(v), __float_as_uint(v), false, false);
  v = MAX ? fmaxf(__uint_as_float(p[0]), __uint_as_float(p[1])) : __uint_as_float(p[0]) + __uint_as_float(p[1]);
  p = __builtin_amdgcn_permlane16_swap(__float_as_uint(v), __float_as_uint(v), false, false);
  return MAX ? fmaxf(__uint_as_float(p[0]), __uint_as_float(p[1])) : __uint_as_float(p[0]) + __uint_as_float(p[1]);
}
template <int Y, int QB> DI float row_max() {
  constexpr int r = R_S + 32 * Y + 4 * QB;
  float mx;
  asm volatile("v_max_f32 %0, v%c1, v%c2\n\tv_max3_f32 %0, %0, v%c3, v%c4" : "=&v"(mx) : "i"(r), "i"(r + 1), "i"(r + 2), "i"(r + 3));
  static_for([&](auto K) {
    constexpr int q = r + 8 * (K + 1);
    float t = mx;
    asm volatile("v_max3_f32 %0, %0, v%c1, v%c2\n\tv_max3_f32 %0, %0, v%c3, v%c4" : "+v"(t) : "i"(q), "i"(q + 1), "i"(q + 2), "i"(q + 3));
    mx = t;
  }, make_iseq<3>{});
  return row_reduce<true>(mx);
}
template <int R> DI void v_sub(float d) { asm volatile("v_sub_f32 v%c0, v%c0, %1" ::"i"(R), "v"(d)); }
template <int B> DI void s_shift(const float (&d)[2]) { static_for([&](auto E) { v_sub<B + E>(d[(E >> 2) & 1]); }, make_iseq<32>{}); }
template <bool CI> DI void m_shift(const float (&d)[2]) {
  if constexpr (CI) s_shift<R_CI>(d);
  v_sub<R_M>(-d[0]); v_sub<R_M + 1>(-d[1]);
}
DI void set_mask0() {
  const int mr = __float_as_int(vget<89>());
  static_for([&](auto I) {
    constexpr int e = (I >> 2) * 8 + (I & 3), k = (I >> 2) * 16 + (I & 3);
    const float x = vget<R_S + e>();
    vset<R_S + e>(k > mr ? -INFINITY : x);
  }, make_iseq<16>{});
}
template <int X, bool CI = true> DI void sm_redo() {
  constexpr int Xp = X ^ 1;
  float d[2], dt[2], alpha[2], ts[2] = {0.f, 0.f};
  static_for([&](auto Q) {
    constexpr int qb = Q;
    const float pd = vget<R_PD + qb>();
    d[qb] = fmaxf(row_max<Xp, qb>() - pd, 0.f);
    dt[qb] = d[qb] + pd;
    vset<R_PD + qb>(d[qb]);
    alpha[qb] = __builtin_amdgcn_exp2f(-d[qb]);
    vset<R_RS + qb>(vget<R_RS + qb>() * alpha[qb]);
  }, make_iseq<2>{});
  vset<R_THR>(d[0] > 0.f || d[1] > 0.f ? -1.f : 4096.f);
  m_shift<CI>(d);
  static_for([&](auto K) {
    constexpr int k = K, qb = (k >> 1) & 1;
    const float e0 = __builtin_amdgcn_exp2f(vget<R_S + 32 * Xp + 2 * k>() - dt[qb]), e1 = __builtin_amdgcn_exp2f(vget<R_S + 32 * Xp + 2 * k + 1>() - dt[qb]);
    ts[qb] += e0; ts[qb] += e1;
    vset<p_reg(Xp, k)>(__uint_as_float(pk2(e0, e1)));
  }, make_iseq<16>{});
  vset<R_TS>(ts[0]); vset<R_TS + 1>(ts[1]);
  rescale_all(alpha, make_iseq<128>{});
  asm volatile("s_nop 1" ::: "memory");
  GAP();
}
template <int X, int MODE, int... Gs> DI void phaseA_(AC& c, iseq<Gs...>) { (gapA<X, MODE, Gs>(c), ...); }
template <int X, int MODE> DI void phaseA(AC& c) { phaseA_<X, MODE>(c, make_iseq<64>{}); }

template <int X, bool SMX, int G> DI void gapB(AC& c) {
  constexpr int Xp = X ^ 1, blk = G >> 4, g = G & 15;
  if constexpr (g == 0) asm volatile("s_waitcnt lgkmcnt(0)");
  constexpr int qb = (g >> 1) & 1, s = g & 1, r = 192 + 4 * ((blk & 1) * 8 + (g >> 2) * 2 + s), o = 4 * ((blk * 4 + (g >> 2)) * 2 + qb);
  constexpr int p = R_P + 16 * Xp + 4 * (s * 2 + qb);
  asm volatile("v_mfma_f32_16x16x32_bf16 a[%c0:%c1], a[%c2:%c3], v[%c4:%c5], a[%c0:%c1]" ::"i"(o), "i"(o + 3), "i"(r), "i"(r + 3), "i"(p), "i"(p + 3));
  if constexpr (g < 8) {
    if constexpr (blk < 3) vread<Xp, (blk + 1) * 8 + g>();
    else if constexpr (SMX) kread<Xp, g>();
  }
  if constexpr (SMX) {
    if constexpr (G == 1 || G == 2) asm volatile("v_add_f32_e32 v%c0, v%c0, v%c1" ::"i"(R_RS + G - 1), "i"(R_TS + G - 1));
    if constexpr (g == 7 && blk < 3) dma_m0<X, blk * 4 + 4>();
    if constexpr (g >= 8 && !(g & 1) && blk < 3) dma_at<blk * 4 + 4 + (g - 8) / 2>();
    if constexpr (G == 55) asm volatile("s_cmp_eq_u32 s56, s76");
    if constexpr (G == 3) asm volatile("s_lshr_b32 s77, s77, 1");
    if constexpr (G == 4) asm volatile("s_bitcmp1_b32 s77, 0");
    if constexpr (G == 5) asm volatile("s_cselect_b64 s[52:53], s[62:63], 0");
    if constexpr (G == 6) asm volatile(FL_ARM("s[52:53]"));
    if constexpr (G == 56) asm volatile(FL_POLL ::: "memory");
    sm_gap<X, G>(c);
  }
  GAP();
}
template <int X, int... Gs> DI void phaseB_(AC& c, iseq<Gs...>) { (gapB<X, true, Gs>(c), ...); }
template <int X> DI void phaseB(AC& c) { phaseB_<X>(c, make_iseq<64>{}); }
constexpr int sm_nex() { int n = 0; for (int t = 0; t < 64; ++t) n += SM.ex[t] >= 0; return n; }
template <int X> DI void phaseB0(unsigned mode) {
  dma_m0<X, 4>();
  kreads<X ^ 1>(make_iseq<8>{});
  dma_at<4>();
  asm volatile("s_nop 3");
  float d[2];
  if (mode & 2) {
    d[0] = vget<122>(); d[1] = vget<123>();
    dma_at<5>();
  } else {
    if constexpr (X == 0) if (__builtin_expect(mode & 4, 0)) set_mask0();
    d[0] = row_max<X, 0>(); GAP(); dma_at<5>();
    d[1] = row_max<X, 1>();
#pragma unroll
    for (int qb = 0; qb < 2; ++qb) if (!(d[qb] > -3.0e38f)) d[qb] = 0.f;
  }
  GAP();
  vset<R_M>(d[0]); vset<R_M + 1>(d[1]);
  dma_at<6>();
  const f32x2c nd[2] = {{-d[0], -d[0]}, {-d[1], -d[1]}};
  static_for([&](auto E) {
    constexpr int e = E;
    const f32x2c v = nd[(e >> 1) & 1];
    asm volatile("v_pk_add_f32 v[%c0:%c1], v[%c0:%c1], %4\n\tv_mov_b64 v[%c2:%c3], %4" ::"i"(R_S + 32 * X + 2 * e), "i"(R_S + 32 * X + 2 * e + 1), "i"(R_CI + 2 * e), "i"(R_CI + 2 * e + 1), "v"(v));
    if constexpr (e == 3) dma_at<7>();
    if constexpr (e == 6) dma_m0<X, 8>();
    if constexpr (e == 7 || e == 11 || e == 15) dma_at<8 + (e - 7) / 4>();
  }, make_iseq<16>{});
  static_assert(SM.op0[64] >= 3 * ((sm_nex() - 15) / 2) && SM.op0[64] <= 24);
  static_for([&](auto E) {
    constexpr int e = E;
    if constexpr (e < sm_nex()) asm volatile("v_exp_f32 v%c0, v%c1" ::"i"(R_E + e), "i"(R_S + 32 * X + e));
    if constexpr (e == 3) dma_at<11>();
    if constexpr (e == 6) dma_m0<X, 12>();
    if constexpr (e == 7 || e == 11 || e == 15) dma_at<12 + (e - 7) / 4>();
  }, make_iseq<16>{});
  static_for([&](auto O) { sm_op<X, O>(); }, make_iseq<SM.op0[64]>{});
  dma_at<15>();
  static_for([&](auto E) { asm volatile("v_exp_f32 v%c0, v%c1" ::"i"(R_E + (int)E), "i"(R_S + 32 * X + 16 + (int)E)); }, make_iseq<(sm_nex() > 16 ? sm_nex() - 16 : 0)>{});
  asm volatile("buffer_store_dwordx2 v[%c0:%c1], v88, s[88:91], s76 offen sc1\n\ts_bitcmp1_b32 s77, 0\n\ts_cselect_b64 s[52:53], s[62:63], 0\n\t"
               FL_ARM("s[52:53]") "\n\t" FL_POLL ::"i"(R_M), "i"(R_M + 1) : "memory", "scc");
  GAP();
}
enum : int { T_Q0, T_K0, T_OQ, T_OG, T_FPREP, T_FQ, T_FG, T_FS, T_ZO, T_CNT, T_NPRE = 10, T_NST = 12, T_TAB = 14, T_SLAB = 16, T_PART, T_IT = 18, T_ITN = 23 };
#define MSK(R, K) "v_cmp_gt_i32 s[48:49], " #K ", v89\n\tv_cmp_gt_i32 s[50:51], " #K "+1, v89\n\tv_cmp_gt_i32 s[52:53], " #K "+2, v89\n\t" \
                  "v_cmp_gt_i32 s[54:55], " #K "+3, v89\n\tv_cndmask_b32_e64 v[" #R "], v[" #R "], v122, s[48:49]\n\t" \
                  "v_cndmask_b32_e64 v[" #R "+1], v[" #R "+1], v122, s[50:51]\n\tv_cndmask_b32_e64 v[" #R "+2], v[" #R "+2], v122, s[52:53]\n\t" \
                  "v_cndmask_b32_e64 v[" #R "+3], v[" #R "+3], v122, s[54:55]\n\t"
DI void ev_code() {
  asm volatile(
    "s_getpc_b64 s[82:83]\n\ts_add_u32 s82, s82, 12\n\ts_addc_u32 s83, s83, 0\n\ts_branch 9f\n\t"
    "s_bitcmp1_b32 s60, s56\n\ts_cbranch_scc1 3f\n\ts_bitcmp1_b32 s74, s56\n\ts_cbranch_scc1 2f\n\t"
    "s_bitcmp0_b32 s57, s56\n\ts_cbranch_scc1 1f\n\ts_bitcmp1_b32 s58, s56\n\ts_cselect_b64 vcc, -1, 0\n\tv_cndmask_b32 v121, -2.0, v81, vcc\n\t"
    "buffer_store_dwordx2 v[82:83], v121, s[88:91], s70 offen sc1\n\tbuffer_store_dwordx2 v[82:83], v121, s[88:91], s71 offen sc1\n\t"
    "v_cmp_eq_u64 vcc, s[84:85], v[124:125]\n\ts_andn2_b64 vcc, s[62:63], vcc\n\ts_cbranch_scc1 5f\n"
    "0:\n\ts_add_u32 s98, s98, s59\n\ts_mov_b32 s59, s61\n\ts_mov_b32 s61, s72\n\ts_mov_b64 s[62:63], s[64:65]\n\ts_mov_b64 s[64:65], s[66:67]\n\t"
    "s_mov_b64 s[66:67], s[68:69]\n"
    "1:\n\ts_setpc_b64 s[86:87]\n"
    "5:\n\t" FL_CHECK("s[62:63]") "\n\ts_branch 0b\n"
    "6:\n\t" FL_CHECK("s[62:63]") "\n\ts_setpc_b64 s[86:87]\n"
    "2:\n\ts_mov_b32 s98, 0\n\t"
    "v_lshrrev_b32 v90, 7, v86\n\tv_and_b32 v91, 6, v90\n\tv_lshlrev_b32 v91, 4, v91\n\tv_and_b32 v92, 0x70, v86\n\tv_xor_b32 v91, v91, v92\n\t"
    "s_movk_i32 s48, 896\n\tv_mad_u32_u24 v94, v90, s48, v91\n\ts_mov_b64 s[92:93], s[80:81]\n\ts_mov_b32 s94, 229376\n\t"
    "v_add_u32 v90, 28672, v94\n\tv_add_u32 v91, 34816, v94\n\tv_add_u32 v92, 40960, v94\n\tv_add_u32 v93, 47104, v94\n\t"
    "v_add_u32 v95, 6144, v94\n\tv_add_u32 v126, 12288, v94\n\tv_add_u32 v127, 18432, v94\n\t"
    "v_mov_b32 v122, 0xff800000\n\t" MSK(228, 0) MSK(236, 16) MSK(244, 32) MSK(252, 48)
    "v_cmp_eq_u64 vcc, s[84:85], v[124:125]\n\ts_andn2_b64 vcc, s[62:63], vcc\n\ts_cbranch_scc1 6b\n\ts_setpc_b64 s[86:87]\n"
    "3:\n\tv_mov_b32 v122, 0xff800000\n\t" MSK(224, 0) MSK(232, 16) MSK(240, 32) MSK(248, 48)
    "v_readlane_b32 s54, v78, %0\n\ts_add_u32 m0, s78, 0x10000\n\t"
    "s_lshr_b32 s53, s73, 16\n\ts_mulk_i32 s53, 3584\n\ts_mov_b64 s[48:49], s[80:81]\n\ts_add_u32 s50, s53, 3584\n\ts_mov_b32 s51, 0x20000\n\t"
    "v_lshlrev_b32 v121, 3, v84\n\tbuffer_load_dword v121, s[48:51], s53 offen lds\n\t"
    "s_bitcmp0_b32 s73, 3\n\ts_cbranch_scc1 4f\n\t"
    "v_lshlrev_b32 v121, 9, v84\n\tv_and_b32 v121, 0x38000, v121\n\tv_lshrrev_b32 v122, 9, v84\n\tv_lshl_add_u32 v121, v122, 12, v121\n\t"
    "v_lshrrev_b32 v122, 3, v84\n\tv_xor_b32 v122, v122, v84\n\tv_and_or_b32 v121, v122, 48, v121\n\t"
    "buffer_load_dwordx4 v121, s[88:91], s54 offen sc1 lds\n\ts_addk_i32 s54, 7168\n\t"
    "buffer_load_dwordx4 v121, s[88:91], s54 offen offset:1024 sc1 lds\n\ts_addk_i32 s54, 7168\n\t"
    "buffer_load_dwordx4 v121, s[88:91], s54 offen offset:2048 sc1 lds\n\ts_addk_i32 s54, 7168\n\t"
    "buffer_load_dwordx4 v121, s[88:91], s54 offen offset:3072 sc1 lds\n"
    "4:\n\ts_setpc_b64 s[86:87]\n9:"
    ::"i"((int)T_ZO) : "scc");
}
DI void ev_call() { asm volatile("s_swappc_b64 s[86:87], s[82:83]" ::: "vcc", "scc", "memory"); GAP(); }

#define ACLOB_ALL "a0","a1","a2","a3","a4","a5","a6","a7","a8","a9","a10","a11","a12","a13","a14","a15","a16","a17","a18","a19","a20","a21","a22","a23","a24","a25","a26","a27","a28","a29","a30","a31","a32","a33","a34","a35","a36","a37","a38","a39","a40","a41","a42","a43","a44","a45","a46","a47","a48","a49","a50","a51","a52","a53","a54","a55","a56","a57","a58","a59","a60","a61","a62","a63","a64","a65","a66","a67","a68","a69","a70","a71","a72","a73","a74","a75","a76","a77","a78","a79","a80","a81","a82","a83","a84","a85","a86","a87","a88","a89","a90","a91","a92","a93","a94","a95","a96","a97","a98","a99","a100","a101","a102","a103","a104","a105","a106","a107","a108","a109","a110","a111","a112","a113","a114","a115","a116","a117","a118","a119","a120","a121","a122","a123","a124","a125","a126","a127","a128","a129","a130","a131","a132","a133","a134","a135","a136","a137","a138","a139","a140","a141","a142","a143","a144","a145","a146","a147","a148","a149","a150","a151","a152","a153","a154","a155","a156","a157","a158","a159","a160","a161","a162","a163","a164","a165","a166","a167","a168","a169","a170","a171","a172","a173","a174","a175","a176","a177","a178","a179","a180","a181","a182","a183","a184","a185","a186","a187","a188","a189","a190","a191","a192","a193","a194","a195","a196","a197","a198","a199","a200","a201","a202","a203","a204","a205","a206","a207","a208","a209","a210","a211","a212","a213","a214","a215","a216","a217","a218","a219","a220","a221","a222","a223","a224","a225","a226","a227","a228","a229","a230","a231","a232","a233","a234","a235","a236","a237","a238","a239","a240","a241","a242","a243","a244","a245","a246","a247","a248","a249","a250","a251","a252","a253","a254","a255"

struct EP { float fa[2], fb[2]; };
template <int I, int E, int MODE> DI void epi_pair(const EP& p) {
  constexpr int qb = I & 1, r = 16 * (I >> 1) + 4 * qb + (E >> 1) * 8 + (E & 1) * 2, t = 96 + 2 * E, w = 128 + 4 * I + E;
  if constexpr (MODE == 2) asm volatile("v_accvgpr_read_b32 v106, a[%c0]" ::"i"(w));
  asm volatile("v_accvgpr_read_b32 v%c0, a[%c2]\n\tv_accvgpr_read_b32 v%c1, a[%c3]" ::"i"(t), "i"(t + 1), "i"(r), "i"(r + 1));
  if constexpr (MODE) asm volatile("v_mul_f32 v%c0, v%c0, %2\n\tv_mul_f32 v%c1, v%c1, %2" ::"i"(t), "i"(t + 1), "v"(p.fa[qb]));
  if constexpr (MODE == 2)
    asm volatile("v_lshlrev_b32 v104, 16, v106\n\tv_and_b32 v105, 0xffff0000, v106\n\tv_fmac_f32 v%c0, v104, %2\n\tv_fmac_f32 v%c1, v105, %2"
                 ::"i"(t), "i"(t + 1), "v"(p.fb[qb]));
  asm volatile("v_cvt_pk_bf16_f32 v%c0, v%c1, v%c2" ::"i"(w), "i"(t), "i"(t + 1));
}
template <int I> DI void epi_store() {
  asm volatile("buffer_store_dwordx4 v[%c0:%c1], v84, s[88:91], s101 offen offset:%2 sc1" ::"i"(128 + 4 * I), "i"(131 + 4 * I), "i"((I & 3) * 1024) : "memory");
  if constexpr ((I & 3) == 3) asm volatile("s_addk_i32 s101, 4096");
}
constexpr int R_TB = 232, R_VS = 236;
template <int HF, int R> DI void p_read() {
  constexpr int mb = R >> 4, r = 192 - 64 * HF + 2 * R;
  asm volatile("ds_read_b64_tr_b16 a[%c1:%c2], v%c0 offset:%c3" ::"i"(R_TB + 2 * HF + mb), "i"(r), "i"(r + 1), "i"((R & 15) * 2048));
}
template <int QB, int E> DI void p_cvt() {
  asm volatile("v_cvt_pk_bf16_f32 v%c0, v%c1, v%c2" ::"i"(208 + 4 * QB + E), "i"(192 + 8 * QB + 2 * E), "i"(193 + 8 * QB + 2 * E));
}
template <int QB, int PR> DI void p_store() {
  asm volatile("buffer_store_dwordx4 v[%c0:%c1], v%c2, s[88:91], s96 offen offset:%3 sc1" ::"i"(208 + 4 * QB), "i"(211 + 4 * QB), "i"(R_VS + QB),
               "i"(PR * 128) : "memory");
}
constexpr int p_dma(int HF, int G) { return G == 3 && !HF ? 0 : G == 11 ? 1 - HF : G == 15 ? 2 - HF : G == 19 ? 3 - HF : G == 27 && HF ? 3 : -1; }
template <int HF, int G> DI void p_gap() {
  constexpr int ch = G >> 3, t = G & 7, a = 192 - 64 * HF + 32 * (ch & 1) + 4 * t, b = 128 + 4 * (2 * t + (ch >> 1)), d = 192 + 4 * ch;
  if constexpr (t == 0)
    asm volatile("v_mfma_f32_16x16x32_bf16 v[%c0:%c1], a[%c2:%c3], v[%c4:%c5], 0" ::"i"(d), "i"(d + 3), "i"(a), "i"(a + 3), "i"(b), "i"(b + 3));
  else
    asm volatile("v_mfma_f32_16x16x32_bf16 v[%c0:%c1], a[%c2:%c3], v[%c4:%c5], v[%c0:%c1]" ::"i"(d), "i"(d + 3), "i"(a), "i"(a + 3), "i"(b), "i"(b + 3));
  p_read<HF ^ 1, G>();
  if constexpr (G == 0) asm volatile("v_xor_b32 v%c0, s100, v%c0" ::"i"(R_TB + 2 * HF + 1));
  if constexpr (G == 17) asm volatile("v_xor_b32 v%c0, s100, v%c0" ::"i"(R_TB + 2 * (HF ^ 1)));
  if constexpr (G >= 4 && G <= 8 && G != 7) p_cvt<1, (G < 7 ? G - 4 : 3)>();
  if constexpr (G == 24 || (G == 7 && HF == 0)) asm volatile("s_waitcnt lgkmcnt(8)");
  if constexpr (G == 7 && HF == 1) asm volatile("s_waitcnt vmcnt(13) lgkmcnt(8)\n\ts_barrier" ::: "memory");
  if constexpr (G == 10) p_store<1, HF ^ 1>();
  if constexpr (G >= 20 && G < 24) p_cvt<0, G - 20>();
  if constexpr (G == 25) p_store<0, HF>();
  if constexpr (p_dma(HF, G) >= 0) dma_at<(HF ? 8 : 12) + p_dma(HF, G)>();
  if constexpr (HF == 0 && G == 2) asm volatile("s_bitset0_b32 m0, 12");
  if constexpr (HF == 0 && G == 13) asm volatile("s_add_u32 s96, s96, 0x40000");
  if constexpr (HF == 0 && G == 26) asm volatile("s_add_u32 s98, s98, 128");
  if constexpr (HF == 0 && G == 27) asm volatile("s_cmp_lt_u32 s98, 0x380");
  if constexpr (HF == 0 && G == 28) asm volatile("s_cselect_b32 s99, s99, 0x31000");
  if constexpr (HF == 1 && G == 1) asm volatile("s_xor_b32 s100, s100, 0x10000");
  if constexpr (HF == 1 && G == 2) asm volatile("s_xor_b32 s97, s97, s100");
  if constexpr (HF == 1 && G == 9) asm volatile("s_or_b32 m0, s97, s99");
  if constexpr (HF == 1 && G == 30) asm volatile("s_cmp_lt_u32 s98, 0x500");
  GAP();
}
template <int HF, int... Gs> DI void p_half(iseq<Gs...>) { (p_gap<HF, Gs>(), ...); }
template <int G> DI void gapF(AC& c, const EP& p) {
  if constexpr (G < 64) gapB<0, false, G>(c);
  if constexpr (G >= 10) epi_pair<(G - 10) / 4, (G - 10) & 3, 0>(p);
  if constexpr (G >= 14 && ((G - 14) & 3) == 0) epi_store<(G - 14) / 4>();
  if constexpr (G == 73) epi_store<15>();
  GAP();
}
template <int... Gs> DI void phaseF(AC& c, const EP& p, iseq<Gs...>) { (gapF<Gs>(c, p), ...); }
template <int G> DI void gapAL(AC& c) {
  constexpr int kb = G >> 3, s = G & 7, r = 192 + 4 * (G & 15), q = 160 + 4 * s, d = R_S + 36 + 8 * kb, ci = s == 0 ? R_CI + 4 + 8 * kb : d;
  if constexpr (G == 0) asm volatile("s_add_u32 s96, s97, 0x1000000\n\ts_mov_b32 m0, s79\n\ts_waitcnt vmcnt(9) lgkmcnt(0)\n\ts_barrier" ::: "memory");
  if constexpr (G >= 8 && (G & 3) == 0) asm volatile("s_waitcnt lgkmcnt(4)");
  if constexpr (G == 29) asm volatile("s_barrier" ::: "memory");
  asm volatile("v_mfma_f32_16x16x32_bf16 v[%c0:%c1], a[%c2:%c3], a[%c4:%c5], v[%c6:%c7]" ::"i"(d), "i"(d + 3), "i"(r), "i"(r + 3), "i"(q), "i"(q + 3),
               "i"(ci), "i"(ci + 3));
  if constexpr (G < 24) kread<1, G + 8>();
  else vread<0, G - 24>();
  if constexpr (G % 3 == 1 && G < 24) dma_at<G / 3>();
  if constexpr (G == 11) asm volatile("s_bitset1_b32 m0, 12");
  if constexpr (G == 28) asm volatile("buffer_load_dwordx4 v[122:125], v88, s[88:91], s76 offen sc1" ::: "memory");
  if constexpr (G == 30) asm volatile("s_xor_b32 m0, s79, 0x11000\n\ts_movk_i32 s98, 128");
  sm_gap<0, 64 + 2 * G>(c); sm_gap<0, 65 + 2 * G>(c);
  GAP();
}
template <int... Gs> DI void phaseAL(AC& c, iseq<Gs...>) { (gapAL<Gs>(c), ...); }
constexpr int bl_ex(int G) { int n = 0; for (int t = 0; t < G; ++t) { const int g = t & 15; n += t == 6 || (t < 48 && g > 8 && g != 12 && g != 15); } return n; }
constexpr int bl_op(int G) {
  int o = 1;
  for (int t = 15; t < G; ++t)
    if (o < 24 && (t & 3) != 0 && t != 47 && bl_ex(t + 1) == bl_ex(t) && bl_ex(t - 1) > 2 * (o / 3) + (o % 3 != 0)) ++o;
  return o;
}
static_assert(bl_ex(64) == 16 && bl_op(61) == 24);
constexpr int bl_vm(bool LD, int G) {
  int n = 0;
  for (int t = 0; t < G; ++t) n += LD ? (t & 3) == 0 || (t & 15) == 7 || t == 5 || t == 58 || t == 62 || t == 63 : (t & 11) == 8;
  return n;
}
static_assert(bl_vm(true, 64) == 24 && bl_vm(false, 64) == 8);
constexpr int bl_su(bool LD, int G) {
  int n = 0;
  for (int t = 1; t < G; ++t)
    n += n < (LD ? 16 : 10) && t != 47 && (t & 15) != 15 && bl_vm(LD, t + 1) == bl_vm(LD, t) && bl_ex(t + 1) == bl_ex(t) && (LD || t > 55 || (t > 3 && bl_op(t + 1) == bl_op(t)));
  return n;
}
static_assert(bl_su(true, 56) == 16 && bl_su(false, 55) >= 8 && bl_su(false, 62) == 10);
template <bool LD, int G> DI void gapBL(AC& c) {
  if constexpr (G == 48) asm volatile("s_waitcnt vmcnt(%0) lgkmcnt(0)\n\ts_barrier" ::"i"(bl_vm(LD, 48)) : "memory");
  gapB<1, false, G>(c);
  if constexpr (G >= 48 && G < 56) vread<1, G - 48>();
  if constexpr (bl_vm(LD, G + 1) > bl_vm(LD, G)) {
    constexpr int k = bl_vm(LD, G) + (LD ? 0 : 16), I = 2 * (k & 7) + ((k >> 3) & 1);
    if constexpr (k < 16) {
      asm volatile("buffer_load_dwordx4 a[%c0:%c1], v84, s[88:91], s75 offen offset:%2 sc1" ::"i"(128 + 4 * I), "i"(131 + 4 * I), "i"((I & 3) * 1024) : "memory");
      if constexpr (k & 1) asm volatile("s_addk_i32 s75, %0" ::"i"(k == 7 ? -12288 : 4096));
    } else dma_at<k - 8>();
    if constexpr (k == 19) asm volatile("s_bitset0_b32 m0, 12");
  }
  if constexpr (G == 1 || G == 2) asm volatile("v_add_f32_e32 v%c0, v%c0, v%c1" ::"i"(R_RS + G - 1), "i"(R_TS + G - 1));
  if constexpr (G == 3) asm volatile("v_mov_b32 v%c0, 0" ::"i"(R_TS));
  if constexpr (bl_ex(G + 1) > bl_ex(G)) {
    constexpr int j = bl_ex(G);
    asm volatile("v_exp_f32 v%c0, v%c1" ::"i"(R_E + j), "i"(R_S + 36 + 8 * (j >> 2) + (j & 3)));
  }
  if constexpr (bl_op(G + 1) > bl_op(G)) {
    constexpr int o = bl_op(G), k = o / 3, j = 2 * k + o % 3;
    if constexpr (o % 3 == 2) asm volatile("v_cvt_pk_bf16_f32 v%c0, v%c1, v%c2" ::"i"(p_reg(1, (k >> 1) * 4 + 2 + (k & 1))), "i"(R_E + 2 * k), "i"(R_E + 2 * k + 1));
    else asm volatile("v_add_f32_e32 v%c0, v%c1, v%c2" ::"i"(R_TS + 1), "i"(j == 1 ? R_E : R_TS + 1), "i"(R_E + j));
  }
  if constexpr (G == 61) asm volatile("v_cmp_nle_f32_e64 s[54:55], v%c0, v%c1" ::"i"(R_TS + 1), "i"(R_THR));
  if constexpr (bl_su(LD, G + 1) > bl_su(LD, G)) {
    constexpr int k = bl_su(LD, G);
    if constexpr (k < 4) asm volatile("v_xor_b32 v%c0, %1, %2" ::"i"(R_TB + k), "i"(32 * k), "v"(c.tb));
    else if constexpr (k < 6) asm volatile("v_add_u32 v%c0, %1, %2" ::"i"(R_VS + k - 4), "i"(32768 * (k - 4)), "v"(c.vs));
    else if constexpr (k == 6) tload<96, T_SLAB>();
    else if constexpr (k == 7) tload<101, T_CNT>();
    else if constexpr (k == 8) asm volatile("v_mov_b64 v[240:241], 1");
    else if constexpr (k == 9) asm volatile("s_mov_b32 s97, s79\n\ts_mov_b32 s100, 0x8000\n\ts_movk_i32 s99, 0x1000");
    else if constexpr (k < 14) asm volatile("v_mov_b32 v%c0, %1" ::"i"(k < 12 ? 214 + k : 218 + k), "v"(k & 1 ? c.o1 : c.o0));
    else asm volatile("v_mov_b64 v[%c0:%c1], 0" ::"i"(198 + 2 * k), "i"(199 + 2 * k));
  }
  GAP();
}
template <bool LD, int... Gs> DI void phaseBL(AC& c, iseq<Gs...>) { (gapBL<LD, Gs>(c), ...); }
template <int MODE, int G> DI void gapFL(const EP& p) {
  constexpr int x = G - (MODE == 3 ? 4 : 0), J = G < 24 && G % 3 == 1 ? G / 3 : -1;
  if constexpr (G == 0) asm volatile("s_or_b32 m0, s78, 0x1000\n\ts_movk_i32 s98, 256\n\ts_waitcnt vmcnt(8) lgkmcnt(0)" ::: "memory");
  if constexpr (G >= 8 && G < 32 && (G & 3) == 0) asm volatile("s_waitcnt lgkmcnt(%0)" ::"i"(G < 28 ? 4 : 0));
  if constexpr (G < 32) {
    constexpr int o = 8 * (G >> 1) + 4, r = 192 + 4 * (G & 15), q = R_P + 20 + 8 * (G & 1);
    asm volatile("v_mfma_f32_16x16x32_bf16 a[%c0:%c1], a[%c2:%c3], v[%c4:%c5], a[%c0:%c1]" ::"i"(o), "i"(o + 3), "i"(r), "i"(r + 3), "i"(q), "i"(q + 3));
  }
  if constexpr (G < 24) vread<1, G + 8>();
  if constexpr (G == 28) asm volatile("s_waitcnt vmcnt(8)\n\ts_barrier\n\ts_or_b32 m0, s79, 0x1000\n\ts_movk_i32 s98, 384" ::: "memory");
  if constexpr (G == 29 || G == 31) dma_at<8 + (G - 29) / 2>();
  if constexpr (G == 32 || G == 40) {
    static_for([&](auto I) { p_read<0, 2 * (G - 32) + I>(); }, make_iseq<16>{});
    dma_at<10 + (G - 32) / 8>();
  }
  if constexpr (J >= 0) dma_at<8 + J>();
  if constexpr (x >= 0) epi_pair<2 * ((x >> 2) & 7) + (x >> 5), x & 3, MODE>(p);
  if constexpr (MODE == 3 && G < 32) {
    constexpr int db = G & 15, qb = G >> 4, o = 4 * (db * 2 + qb), a = 224 + 4 * (db & 1), b = 128 + 4 * ((db >> 1) * 2 + qb);
    asm volatile("v_mfma_f32_16x16x32_bf16 a[%c0:%c1], v[%c2:%c3], a[%c4:%c5], a[%c0:%c1]" ::"i"(o), "i"(o + 3), "i"(a), "i"(a + 3), "i"(b), "i"(b + 3));
  }
  if constexpr (J == 3) asm volatile("s_bitset0_b32 m0, 12");
  if constexpr (x == 63) asm volatile("s_waitcnt vmcnt(4) lgkmcnt(0)" ::: "memory");
  GAP();
}
template <int MODE, int... Gs> DI void phaseFL_(const EP& p, iseq<Gs...>) { (gapFL<MODE, Gs>(p), ...); }
template <int MODE> DI void phaseFL(const EP& p) { GAP(); phaseFL_<MODE>(p, make_iseq<MODE == 3 ? 68 : 64>{}); }
template <int U, int K> DI void r_load(unsigned vo, unsigned so) {
  asm volatile("buffer_load_dwordx4 a[%c0:%c1], %2, s[88:91], %3 offen offset:%4 sc1" ::"i"(64 * U + 4 * K), "i"(64 * U + 4 * K + 3), "v"(vo),
               "s"(so + (K >> 2) * 4096), "i"((K & 1) * 512 + ((K >> 1) & 1) * 2048) : "memory");
}
template <int U, int K> DI void r_sum() {
  constexpr int d = 100 + 4 * (K & 1), b = 64 * U + 4 * K;
  if constexpr (K < 2) asm volatile("v_mfma_f32_16x16x32_bf16 v[%c0:%c1], v[96:99], a[%c2:%c3], 0" ::"i"(d), "i"(d + 3), "i"(b), "i"(b + 3));
  else asm volatile("v_mfma_f32_16x16x32_bf16 v[%c0:%c1], v[96:99], a[%c2:%c3], v[%c0:%c1]" ::"i"(d), "i"(d + 3), "i"(b), "i"(b + 3));
}
template <int U> DI void r_fin(unsigned vo, __amdgpu_buffer_rsrc_t srd, unsigned so) {
  asm volatile("" : "+s"(srd), "+s"(so));
  static_for([&](auto K) { r_sum<U, K>(); }, make_iseq<16>{});
  asm volatile("s_nop 7");
  static_for([&](auto E) { asm volatile("v_cvt_pk_bf16_f32 v%c0, v%c1, v%c2" ::"i"(116 + 4 * U + E), "i"(100 + 2 * E), "i"(101 + 2 * E)); }, make_iseq<4>{});
  asm volatile("buffer_store_dwordx4 v[%c3:%c4], %0, %1, %2 offen" OUTSCS ::"v"(vo), "s"(srd), "s"(so), "i"(116 + 4 * U), "i"(119 + 4 * U) : "memory");
}

#define CMAX 12
#define PREPE 0
static_assert(NYT <= 16 && NYT >= 12);
struct TRow { unsigned v[64]; };
constexpr u64 kvmask(int m0, int m1) { u64 n = 0; for (int m = m0; m <= m1; ++m) n |= 0x101ull << m; return n; }
constexpr u64 k_order(bool isY, int rr) {
  const int cls = (rr + (rr >> 2)) & 3;
  return !isY ? (cls == 0 ? 0xfedcb9875a463210ull : cls == 1 ? 0xefadc6982b574310ull : cls == 2 ? 0xfebad7693c285410ull : 0xefcba8764d392510ull)
              : (cls == 0 ? 0xfedcb987543a6210ull : cls == 1 ? 0xefadc698254b7310ull : cls == 2 ? 0xfebad769325c8410ull : 0xefcba876432d9510ull);
}
constexpr TRow t_row(int r, int wid, int j) {
  TRow t{};
  const int rr = r & 15, hsel = rr & 1, pj = rr >> 1, kv = pj & 1, h = j + 8 * hsel, u = hsel * 8 + j;
  const bool isY = r >= 16, solo = pj == 7;
  const int mt = (rr >> 2) + (isY ? 4 : 0), g = j * 128, NY = solo ? 18 : NYT;
  const int nx0 = 32 - 2 * pj - NY, cx = nx0 < CMAX ? nx0 : CMAX, ex = nx0 - cx, qbF = isY != (solo && SWAP7) ? 15 - pj : pj;
  const unsigned oQ = (unsigned)h << 20, oK = oQ + O_K, oP = O_P + (h * 8 + pj) * PSTR;
  if (wid == 4) {
    for (int l = 0; l < 64; ++l)
      t.v[l] = fo(l < 16 ? g + hsel * 16 + l : l < 24 ? g + 16 + hsel * 8 + l : l == 24 ? g + 48 + hsel * 8 + pj : l < 32 ? g + 64 + hsel * 8 + pj :
                  l < 48 ? 2304 + pj * 16 + l - 32 : 2048 + pj * 16 + l - 48);
    return t;
  }
  auto put64 = [&](int i, u64 x) { t.v[i] = (unsigned)x; t.v[i + 1] = (unsigned)(x >> 32); };
  t.v[T_Q0] = oQ + ((solo ? qbF : 15 - pj) * 128 + wid * 16) * 512;
  t.v[T_K0] = oK + (isY ? cx : 0) * 32768;
  t.v[T_OQ] = oQ + pj * 131072 + (wid >> 1) * 65536 + (wid & 1) * 4096;
  t.v[T_OG] = oK + kv * TSB + mt * 131072 + (wid >> 1) * 65536 + (wid & 1) * (kv ? 8192 : 4096);
  t.v[T_FPREP] = PREPE ? fo(2048 + pj * 16 + (isY * 2 + hsel) * 4) + (j - PREPE + 1) * 4 : fo(2048 + pj * 16 + u);
  t.v[T_FQ] = fo(g + 32 + hsel * 8 + pj);
  t.v[T_FG] = fo(g + hsel * 16 + kv * 8 + mt);
  t.v[T_FS] = fo(1024 + qbF * 16 + j * 2 + hsel);
  t.v[T_CNT] = fo(2560 + qbF * 16);
  t.v[T_ZO] = O_Z + pj * 64 * SEQ * 2 + (u * 4 + wid) * 64;
  const u64 own = 0x1ull << (kv * 8 + mt), needQ = isY ? 1ull << (16 + (qbF >> 1)) : (1ull << (16 + (pj >> 1))) | (1ull << (16 + ((15 - pj) >> 1)));
  put64(T_NPRE, isY ? needQ | (1ull << (cx >> 2)) | (1ull << ((cx + 1) >> 2)) : solo ? needQ : needQ | 0xffffull << 48);
  put64(T_NST, isY ? kvmask(cx >> 2, 3) | (solo ? 0 : 1ull << 25) : kvmask(0, solo ? 3 : 0));
  put64(T_TAB, k_order(isY, rr));
  t.v[T_SLAB] = O_S + qbF * 0x200000 + wid * 65536 + (j * 2 + hsel) * 1024;
  t.v[T_PART] = oP;
  for (int it = 0; it < 2; ++it) {
    unsigned* p = t.v + T_IT + it * T_ITN;
    const bool last = isY || it == 1;
    const int n = solo ? 2 * qbF + 2 : isY ? NY : it ? 2 * pj + 2 : nx0;
    const u64 need2 = solo ? 0 : kvmask(0, (2 * pj + 1) >> 2) | (0xffffull << 32);
    u64 q[4] = {0, 0, 0, 0};
    unsigned jmp[3] = {0, 0, 0}, chk = 0;
    if (isY) {
      p[0] = 2; p[1] = 2; p[13] = t.v[T_FG]; p[14] = fo(g + 96 + r);
      if (!(solo && SWAP7)) { chk = 1u << (14 - cx); p[0] |= chk; q[1] = kvmask(4, (31 - 2 * pj) >> 2) & ~own; jmp[1] = ex * 32768; }
      if (!solo) { chk |= 1u << (n - 2); q[2] = 1ull << 24; }
    } else if (it == 0) {
      chk = 2u | (ex ? 1u << (cx - 2) : 0);
      p[0] = chk | 1u << (n - 2);
      chk |= 1u << n;
      q[0] = kvmask(0, cx > 0 ? (cx - 1) >> 2 : 0);
      if (ex) { q[1] = kvmask(4, 4 + ((ex - 1) >> 2)); jmp[1] = (16 - cx) * 32768; }
      jmp[ex ? 2 : 1] = -(ex ? 16 + ex : cx) * 32768;
      q[ex ? 3 : 2] = need2;
      p[1] = 2; p[13] = fo(2304 + pj * 16 + u) + j * (8 * 4352 - 8); p[14] = fo(g + 64 + hsel * 8 + pj);
    } else {
      p[0] = p[1] = solo ? 0 : 1;
      q[0] = need2;
      if (solo && SWAP7) { p[0] = chk = 1u << 14; q[0] = kvmask(4, 4); }
      p[13] = fo(g + 48 + hsel * 8 + pj); p[14] = fo(g + 96 + r);
    }
    p[2] = jmp[0]; p[4] = jmp[1]; p[15] = jmp[2];
    p[3] = !last ? 0 : n > 2 ? 1u << (n - 3) : 1u << 30;
    for (int k = 0; k < 4; ++k) { p[5 + 2 * k] = (unsigned)q[k]; p[6 + 2 * k] = (unsigned)(q[k] >> 32); }
    p[16] = (last ? 1 : 0) | (isY && !solo ? 2 : 0) | (n == 2 ? 4 : 0) | (isY || solo ? 0 : 8) | (n - 1 - last) << 8 | ((r >> 1) * 4 + wid) << 16;
    p[17] = !last ? 0 : n > 2 ? 1u << (n - 2) : 1u << 31;
    p[18] = oQ + (pj * 128 + wid * 16) * 512;
    p[19] = 0;
    p[20] = chk >> 1;
    p[21] = oK + (isY ? cx : 0) * 32768;
    p[22] = ((p[0] | p[3] | p[17]) & 0x3fffffffu) << 1;
  }
  return t;
}
template <int I> constexpr TRow t_base = I < 160 ? t_row(I / 5, I % 5, 0) : [] {
  TRow a = t_row(0, (I - 160) * 4, 0), b = t_row(0, (I - 160) * 4, 1);
  for (int l = 0; l < 64; ++l) a.v[l] = b.v[l] - a.v[l];
  return a;
}();
template <int I> constexpr bool t_linear = [] {
  for (int j = 1; j < 8; ++j) {
    const TRow b = t_row(I / 5, I % 5, j);
    for (int l = 0; l < 64; ++l) if (b.v[l] != t_base<I>.v[l] + j * t_base<160 + (I % 5) / 4>.v[l]) return false;
  }
  return true;
}();
struct Tab { TRow row[162]; };
template <int... I> constexpr Tab t_make(iseq<I...>) {
  static_assert((t_linear<(I < 160 ? I : 0)> && ...));
  return Tab{{t_base<I>...}};
}
__device__ const Tab g_tab = t_make(make_iseq<162>{});

template <int R> DI const bf16* sptr() { const bf16* p; asm volatile("s_mov_b64 %0, s[%c1:%c2]" : "=s"(p) : "i"(R), "i"(R + 1)); return p; }
template <class T, int I> DI T* arg_late() {
  unsigned lo, hi;
  asm volatile("v_readlane_b32 %0, v77, %2\n\tv_readlane_b32 %1, v77, %3\n\ts_nop 4" : "=s"(lo), "=s"(hi) : "i"(2 * I), "i"(2 * I + 1));
  return (T*)(((u64)hi << 32) | lo);
}
__global__ __launch_bounds__(256, 1) __attribute__((amdgpu_num_vgpr(77), amdgpu_num_sgpr(56))) void fused_kernel(
    const bf16* ckv_, const bf16* wuk_, const bf16* wuv_, const bf16* x_, const bf16* wq_, bf16* ws_, const bf16* ak_, const bf16* av_,
    const bf16* ash_, const bf16* assmk_, const bf16* assmD_, const bf16* wo_, const bf16* aq_, bf16* out_) {
  extern __shared__ __attribute__((aligned(16))) char lds[];
  const void* kap = (const void*)__builtin_amdgcn_kernarg_segment_ptr();
  asm volatile("at_code_begin:\n\ts_load_dwordx16 s[80:95], %0, 0\n\ts_load_dwordx2 s[96:97], %0, 0x40" ::"s"(kap) : ACLOB_ALL, "v255", "s101");
  asm volatile("s_getpc_b64 s[48:49]\nct_base:\n\ts_mov_b32 s50, pf_code_end-ct_base\n\ts_mov_b32 s51, 0x20000\n\ts_lshr_b32 s52, %1, 3\n\t"
               "v_and_b32 v116, 63, %0\n\tv_lshrrev_b32 v117, 6, %0\n\tv_lshl_add_u32 v116, v116, 7, v117\n\ts_lshl_b32 s52, s52, 9\n\t"
               "v_lshlrev_b32 v116, 7, v116\n\tbuffer_load_dword v117, v116, s[48:51], s52 offen" ::"v"(threadIdx.x), "s"(blockIdx.x) : "scc", "memory");
  GAP();
  const int tid = threadIdx.x, lane = tid & 63;
  const int wid = __builtin_amdgcn_readfirstlane(tid >> 6);
  const int b = blockIdx.x, j = b & 7, r = b >> 3, rr = r & 15, hsel = rr & 1, pj = rr >> 1, kv = pj & 1;
  const bool isY = r >= 16, solo = pj == 7, doP = (PREPE ? (j & 1) == PREPE - 1 : isY) && !solo, doA = !isY && !solo;
  const int h = j + 8 * hsel, mt = (rr >> 2) + (isY ? 4 : 0), pu = (hsel * 8 + j) * 4 + wid;
  constexpr unsigned m0d = 131072 + 24576;
  const unsigned lA = isY ? 65536 : 0, lW = lA ^ 65536, po = isY ? 0 : 32768;
  unsigned mA = lA + wid * 1024 + po, mW = lW + wid * 1024 + po, ofA = isY ? pj * 256 * DA * 2 : mt * 256 * DC * 2, ofW = h * 512;
  const unsigned lda = isY ? DA : DC;
#define ROT 1
  unsigned g0 = (!ROT ? 0 : isY ? pj + 2 * hsel : rr + (rr >> 2)) & 3;
  {
    const unsigned* tp = g_tab.row[r * 5].v;
    asm volatile("global_load_dword v78, %0, %2\n\tglobal_load_dword v79, %1, %2 offset:1024\n\t"
                 "global_load_dword v118, %1, %3\n\tglobal_load_dword v119, %1, %3 offset:256\n\tglobal_load_dword v77, %4, %5"
                 ::"v"(tid * 4), "v"(lane * 4), "s"(tp), "s"(g_tab.row[160].v), "v"((lane < 27 ? lane : 27) * 4), "s"(kap) : "memory");
    const unsigned ra = wid * 8 + (lane >> 3), sa = ((lane & 7) ^ (((wid & 1) * 4 + (lane >> 4)) & 7)) * 16;
    const unsigned vbA = ra * (lda * 2) + sa, vbB = (4 * wid + (lane >> 4)) * LDW * 2 + 16 * ((lane & 15) ^ (((lane >> 4) << 2) | wid));
    vsetu<92>(vbA); vsetu<93>(vbB); vsetu<94>(ra * (DC * 2) + sa);
    sset<64>(64 * lda); sset<67>(256 * lda); sset<68>(320 * lda);
    asm volatile("s_mov_b32 s71, 0x20000\n\ts_movk_i32 s72, 0x100\n\ts_mov_b32 s73, 0x20100");
    sset<58>(256 * lda * 2); sset<62>((lda - 1) * LDW * 2 + 512);
    asm volatile("s_mov_b32 s59, 0x20000\n\ts_mov_b32 s63, 0x20000");
    g_pro<1>(g0, mA, mW, lda);
    asm volatile("" : "+s"(ofA), "+s"(ofW), "+s"(mA), "+s"(mW), "+s"(g0));
    GAP();
  }
  asm volatile("s_bitcmp1_b32 %0, 4\n\ts_waitcnt lgkmcnt(0)\n\ts_cselect_b64 s[60:61], s[84:85], s[82:83]\n\ts_bitcmp1_b32 %0, 7\n\t"
               "s_cselect_b64 s[60:61], s[88:89], s[60:61]\n\ts_cselect_b64 s[56:57], s[86:87], s[80:81]\n\t"
               "s_add_u32 s56, s56, %1\n\ts_addc_u32 s57, s57, 0\n\ts_add_u32 s60, s60, %2\n\ts_addc_u32 s61, s61, 0\n\t"
               "s_mov_b64 s[88:89], s[90:91]\n\ts_mov_b32 s90, %3\n\ts_mov_b32 s91, 0x20000" ::"s"(b), "s"(ofA), "s"(ofW), "i"(WSB) : "scc");
  KST(0, 0);
  AC c;
  {
    GM g;
    PR pr;
    const int ptb = PREPE ? (isY * 2 + hsel) * 4 + (j >> 1) : hsel * 8 + j;
    if (doP) prep_load(pr, tid, pj, ptb, sptr<92>(), sptr<94>(), sptr<96>());
    g_pro<2>(g0, mA, mW, lda);
    asm volatile("buffer_load_dwordx2 v[120:121], off, s[88:91], %0 sc1" ::"s"(fo(6144)) : "memory");
    if (doP) {
      asm volatile("s_waitcnt vmcnt(9)" ::: "memory");
#pragma unroll
      for (int q = 0; q < 7; ++q) asm volatile("" : "+v"(pr.kk[q]));
#pragma unroll
      for (int q = 0; q < 4; ++q) asm volatile("" : "+v"(pr.vv[q]));
      static_for([&](auto Ic) __attribute__((always_inline)) {
        constexpr int I = Ic;
        if constexpr (I < 3) g_pro(g0 + 1 + I, mA, mW, lda);
        GAP();
        prep_math<I>(pr, tid, lds + 131072);
        GAP();
      }, make_iseq<4>{});
      asm volatile("s_waitcnt lgkmcnt(0)\n\ts_barrier" ::: "memory");
      prep_store(tid, pj, ptb, (bf16*)sptr<88>() + O_X / 2, lds + 131072);
    }
    GAP();
    {
      const int fr = lane & 15, fq = lane >> 4, q = (lane & 15) >> 2, p = lane & 3;
#pragma unroll
      for (int k2 = 0; k2 < 2; ++k2)
        g.aA[k2] = lA + (wid >> 1) * 16384 + (fr >> 3) * 1024 + (fr & 7) * 128 + (((4 * k2 + fq) ^ (fr >> 1)) * 16);
#pragma unroll
      for (int hf = 0; hf < 2; ++hf)
#pragma unroll
        for (int nf = 0; nf < 8; ++nf)
          g.aB[hf][nf] = lW + (wid & 1) * 8192 + off_b(8 * fq + 4 * hf + q, nf * 2 + (p >> 1)) + 8 * (p & 1);
      asm volatile("" : "+v"(g.aA[0]), "+v"(g.aA[1]));
#pragma unroll
      for (int nf = 0; nf < 8; ++nf) asm volatile("" : "+v"(g.aB[0][nf]), "+v"(g.aB[1][nf]));
      const unsigned one = fq != q ? 0u : (p & 1) ? 0x3f800000u : 0x3f80u;
      c.o0 = (p & 2) ? 0u : one; c.o1 = (p & 2) ? one : 0u;
      c.tb = (lane >> 2) * 128 + 16 * ((p >> 1) ^ ((lane >> 2) & 6)) + 8 * (p & 1);
      c.vs = (fr >> 3) * 16384 + (fq & 1) * 512 + (fq >> 1) * 256 + (fr & 7) * 16;
      asm volatile("" : "+v"(c.o0), "+v"(c.o1), "+v"(c.tb), "+v"(c.vs));
      vsetu<81>(tid == 0 ? 0u : 0x80000000u);
      vsetu<84>(lane * 16); vsetu<85>(lane * 16 + 65536); vsetu<86>(wid * 8192 + lane * 16); vsetu<87>(wid * 8192 + lane * 16 + 4096);
      vsetu<88>(65536 + wid * 256 + fr * 16); vsetu<89>(16 * wid + fr - 4 * fq);
      asm volatile("s_mov_b64 s[92:93], s[88:89]\n\ts_mov_b64 s[94:95], s[90:91]\n\ts_mov_b32 s74, 0x40000\n\ts_mov_b32 s75, 0x60000\n\t"
                   "s_mov_b32 s76, 0x40100\n\ts_mov_b32 s77, 0x60100");
      sset<65>(128 * lda); sset<66>(192 * lda); sset<69>(384 * lda); sset<70>(448 * lda);
      sset<78>(lA + wid * 1024); sset<79>(lW + wid * 1024);
    }
    if (doP) asm volatile("s_waitcnt vmcnt(4)" ::: "memory");
    else {
      g_pro(g0 + 1, mA, mW, lda); g_pro(g0 + 2, mA, mW, lda); g_pro(g0 + 3, mA, mW, lda);
      asm volatile("s_waitcnt vmcnt(0)" ::: "memory");
    }
    asm volatile("s_barrier" ::: "memory");
    if (isY) g_read0<0>(g, make_iseq<16>{});
    else g_read0<1>(g, make_iseq<16>{});
    GAP();
    {
      u64 z = (((u64)__float_as_uint(vget<121>()) << 32) | __float_as_uint(vget<120>())) + 0x9e3779b97f4a7c15ull;
      z = (z ^ (z >> 30)) * 0xbf58476d1ce4e5b9ull; z = (z ^ (z >> 27)) * 0x94d049bb133111ebull; z ^= z >> 31;
      vsetu<82>((unsigned)z); vsetu<83>((unsigned)(z >> 32));
      asm volatile("v_mad_u32_u24 v78, v118, %0, v78\n\tv_mad_u32_u24 v79, v119, %0, v79\n\tv_readfirstlane_b32 s84, v82\n\tv_readfirstlane_b32 s85, v83\n\t"
                   "v_readlane_b32 s80, v78, %1\n\tv_readlane_b32 s81, v78, %2" ::"s"(j), "i"((int)T_TAB), "i"(T_TAB + 1));
      if (isY) asm volatile("s_mov_b64 s[80:81], 0x6543210");
    }
    unsigned k2;
    asm volatile("s_bfe_u32 s82, s80, 0x40008\n\ts_lshr_b64 s[80:81], s[80:81], 12\n\ts_lshl_b32 %0, s82, 1\n\tv_add_u32 v90, 128, v92" : "=s"(k2) :: "scc");
    asm volatile("s_waitcnt lgkmcnt(0)\n\ts_barrier" ::: "memory");
    g_proW(k2, mW);
    GAP();
    if (isY) {
      g_tile<false, 0, 1, NTW >= 1>(g);
      g_loop<false, true>(g, doP ? 0x22u : 0x20u, [&](int t) __attribute__((always_inline)) {
        if (t == 1) fl_set<T_FPREP>();
        else if (t == 4) { tload<80, T_TAB>(); tload<81, T_TAB + 1>(); srd_set<60>((kv ? arg_late<const bf16, 2>() : arg_late<const bf16, 1>()) + h * 256, (DC - 1) * LDW * 2 + 512); }
        else {
          srd_set<56>(arg_late<const bf16, 0>() + (size_t)mt * 256 * DC, 256 * DC * 2);
          asm volatile("v_mov_b32 v92, v94");
          static_for([&](auto J) { asm volatile("s_mov_b32 s%c0, %1" ::"i"(64 + J), "i"((J + 1) * 64 * DC)); }, make_iseq<7>{});
        }
      });
      tload<101, T_OQ>(); sset<83>(__float_as_uint(CS));
      asm volatile("v_mov_b32 v112, v84\n\tv_add_u32 v113, 8192, v84");
      GAP();
      g_tile<false, 0, 2, NTW >= 2>(g);
      KST(0, 3);
    }
#define TT 3
    auto kvtile = [&](auto TRc) __attribute__((always_inline)) {
      constexpr bool TR = TRc;
      if (isY) g_tile<TR, 1, 4, NTW >= 2>(g);
      else g_tile<TR, 1, 1, NTW >= 2>(g);
      g_loop<TR, false>(g, 0x2000u | (isY || doA ? 2u : 1u << TT), [&](int t) __attribute__((always_inline)) {
        int l = threadIdx.x & 63; asm volatile("" : "+v"(l));
        if (t == 13) {
          asm volatile("s_mov_b32 s79, 0x28000");
          fl_poll<T_NPRE>();
          if ((b & 15) == 0)
            asm volatile("v_readlane_b32 s48, v78, %0\n\tv_and_b32 v122, 0xffffff00, v82\n\tv_mov_b32 v123, v83\n\ts_nop 2\n\t"
                         "buffer_store_dwordx2 v[122:123], v81, s[88:91], s48 offen sc1" ::"i"((int)T_CNT) : "memory");
          if (!doA && !isY) {
            CODE_TOUCH("at_code_begin", "pf_code_end", hsel * 8 + wid * 2, 16u, 64, m0d);
            CODE_TOUCH("at_code_begin", "pf_code_end", hsel * 8 + wid * 2 + 1, 16u, 64, m0d);
          }
          if (doA) {
            const char* tp = arg_late<const char, 9>() + (size_t)(pj * 64 + pu) * (SEQ * 2) + (l & 31) * 128;
            asm volatile("s_mov_b32 m0, %1\n\ts_nop 0\n\tglobal_load_lds_dword %0, off" ::"v"(tp), "i"(m0d) : "memory");
          }
        } else if (isY) fl_set<T_FQ>();
        else if (doA) { if (doP) fl_set<T_FPREP>(); post_dma(pj, pu, arg_late<const bf16, 12>(), 131072 + wid * 8192); }
        else {
          const char* tp = arg_late<const char, 0>() + 1024 * DC * 2 + (pu * 256 + l) * 128;
          asm volatile("s_mov_b32 m0, %1\n\ts_nop 0\n\tglobal_load_lds_dword %0, off" ::"v"(tp), "i"(m0d) : "memory");
          asm volatile("global_load_lds_dword %0, off" ::"v"(tp + 8192) : "memory");
          asm volatile("global_load_lds_dword %0, off" ::"v"(tp + 16384) : "memory");
          asm volatile("global_load_lds_dword %0, off" ::"v"(tp + 24576) : "memory");
        }
      });
      tload<101, T_OG>(); tload<96, T_Q0>(); tload<98, T_K0>();
      fl_check();
      tload<50, T_NST>(); tload<51, T_NST + 1>();
      asm volatile("v_mov_b32 v112, v84\n\tv_add_u32 v113, %0, v84\n\tv_mov_b32 v90, v87\n\tv_mov_b32 v91, v87\n\tv_mov_b32 v92, v87\n\tv_mov_b32 v93, v87\n\t"
                   "v_mov_b32 v94, v86\n\tv_mov_b32 v95, v86\n\tv_mov_b32 v126, v86\n\tv_mov_b32 v127, v86\n\ts_mov_b32 s99, 0x8000\n\t" FL_ARM("s[50:51]")
                   ::"i"(TR ? 4096 : 8192));
      if (doA) {
        const int d = pj * 64 + pu;
        const bf16* dp = arg_late<const bf16, 10>() + d;
        asm volatile("global_load_ushort v123, %0, off" ::"v"(dp) : "memory");
        srd_set<56>(arg_late<const bf16, 9>() + (size_t)d * SEQ, SEQ * 2);
        srd_set<60>(arg_late<const char, 5>() + O_X + d * SEQ * 2, SEQ * 2);
        sset<78>(65536 + wid * 15488);
        asm volatile("s_movk_i32 s64, 992\n\tv_subrev_u32 v122, 32, v84");
        GAP();
        g_tile<TR, 0, 3>(g);
      } else {
        sset<79>(isY ? wid * 8192 : 0x30000);
        if ((NYT & 1) && !solo) asm volatile("s_add_u32 s98, s98, 0x8000\n\ts_mov_b32 s97, 0xffff8000" ::: "scc");
        else asm volatile("s_mov_b32 s97, 0x8000");
        GAP();
        g_tile<TR, 0, 5>(g);
      }
    };
    if (kv) kvtile(std::true_type{});
    else kvtile(std::false_type{});
  }
  KST(0, 4);
  asm volatile("s_waitcnt vmcnt(16)" ::: "memory");
  sset<79>(wid * 8192);
  if (doA) {
    const float Dd = bfl(__float_as_uint(vget<123>()));
    KST(0, 5);
    conv_wave<T_FG>(O_Z + (pj * 64 + pu) * SEQ * 2, Dd, lds + 65536 + wid * 15488);
    const char* tp = arg_late<const char, 11>() + (size_t)h * (HD * DA * 2) + ((pj * 4 + wid) * 64 + lane) * 128;
    asm volatile("s_mov_b32 m0, %1\n\ts_nop 0\n\tglobal_load_lds_dword %0, off\n\ts_waitcnt vmcnt(17)" ::"v"(tp), "i"(m0d) : "memory");
    KST(0, 6);
  } else if (!isY) {
    static_for([&](auto I) { d_store<16 + I>(); }, make_iseq<16>{});
    tload<98, T_K0>();
    asm volatile("s_waitcnt vmcnt(0)\n\ts_barrier" ::: "memory");
    fl_set<T_FG>();
    KST(0, 5);
    fl_check();
    CODE_TOUCH("att_begin", "pf_code_end", hsel * 4 + wid, 8u, 64, m0d);
    dma4k<0>(); dma4k<1>(); dma4k<2>(); dma4k<3>();
    asm volatile("s_waitcnt vmcnt(0)" ::: "memory");
    KST(0, 6);
  } else {
    tload<76, T_PART>();
    fl_check();
    asm volatile("buffer_load_dwordx2 v[122:123], v88, s[88:91], s76 offen offset:1024 sc1" ::: "memory");
  }
  sset<78>(65536 + wid * 8192); sset<79>(98304 + wid * 8192);
  { const bf16* wp = arg_late<const bf16, 11>() + (size_t)h * HD * DA; asm volatile("s_mov_b64 s[80:81], %0" ::"s"(wp)); }
  asm volatile("att_begin:");
  ev_code();
  asm volatile("s_waitcnt lgkmcnt(0)\n\ts_barrier" ::: "memory");
#pragma unroll 1
  for (int it = doA ? 0 : 1; it < 2; ++it) {
    const bool odd = (NYT & 1) && !solo && (isY || it == 0);
    if (odd) kreads<1>(make_iseq<8>{});
    else kreads<0>(make_iseq<8>{});
    const unsigned oP = tget<T_PART>();
    if (it == 0) {
      static_for([&](auto I) { tload<57 + I, T_IT + I>(); }, make_iseq<21>{});
      tload<S_T, T_IT + 21>(); tload<S_EV, T_IT + 22>();
      asm volatile("s_sub_u32 s70, s70, %0\n\ts_add_u32 s76, %1, 1024" ::"s"(j * (8 * 4352 - 8)), "s"(oP) : "scc");
    } else {
      static_for([&](auto I) { tload<57 + I, T_IT + T_ITN + I>(); }, make_iseq<21>{});
      tload<S_T, T_IT + T_ITN + 21>(); tload<S_EV, T_IT + T_ITN + 22>();
      asm volatile("s_mov_b32 s75, %0\n\ts_mov_b32 s76, %1" ::"s"(oP + wid * 16384), "s"(oP + 1032));
    }
    unsigned mode;
    asm volatile("s_add_u32 s98, s97, 0x8000\n\ts_mov_b32 %0, s73\n\ts_mov_b32 s56, -1\n\tv_mov_b64 v[114:115], 0\n\tv_mov_b64 v[118:119], 0\n\t"
                 "v_mov_b32 v120, 0x45800000" : "=s"(mode) :: "scc");
    KST(0, 8 + 3 * (it && !isY));
    GAP();
#define STA(k)
#define STI()
#define LOOP_ENDS "s_bfe_u32 s76, s73, 0x80008"
    STA(0);
    if (odd) {
      if (isY) phaseA<1, 2>(c);
      else phaseA<1, 1>(c);
      STA(1);
      STA(2);
      phaseB0<1>(mode);
      STA(3);
      asm volatile(LOOP_ENDS "\n\ts_branch att_mid" ::: "scc");
    } else {
      if (isY) phaseA<0, 2>(c);
      else phaseA<0, 1>(c);
      STA(1);
      if (c.rare) ev_call();
      if (__builtin_expect(mode & 4, 0)) { asm volatile("s_mov_b32 s56, 30"); ev_call(); asm volatile("s_mov_b32 s56, 31"); ev_call(); }
      STA(2);
      phaseB0<0>(mode);
      if (__builtin_expect(mode & 4, 0)) { ev_call(); asm volatile("s_branch att_done"); }
      STA(3);
    }
    GAP();
    asm volatile(LOOP_ENDS "\natt_loop:" ::: "scc");
    GAP();
    phaseA<1, 0>(c);
    STI();
    if (__builtin_expect(c.rare != 0, 0)) { if (redo()) sm_redo<1>(); ev_call(); }
    phaseB<1>(c);
    asm volatile("s_cbranch_scc1 att_done\natt_mid:");
    phaseA<0, 0>(c);
    STI();
    if (__builtin_expect(c.rare != 0, 0)) { if (redo()) sm_redo<0>(); ev_call(); }
    phaseB<0>(c);
    asm volatile("s_cbranch_scc0 att_loop\natt_done:");
    GAP();
    STA(4);
    if (it == 0) phaseA<0, 3>(c);
    else {
      sset<76>(oP);
      GAP();
      phaseAL(c, make_iseq<32>{});
      if (__builtin_expect(redo(), 0)) sm_redo<1, false>();
      if (mode & 2) phaseBL<true>(c, make_iseq<64>{});
      else phaseBL<false>(c, make_iseq<64>{});
    }
    if (__builtin_expect(redo(), 0)) {
      if (it) static_for([&](auto I) { vset<R_S + 32 + (I >> 2) * 8 + (I & 3)>(-INFINITY); }, make_iseq<16>{});
      asm volatile("s_nop 7");
      sm_redo<0, false>();
    }
    STA(5);
    KST(0, 9 + 3 * (it && !isY));
    float l[2];
    asm volatile("v_add_f32 %0, v%c2, v%c3\n\tv_add_f32 %1, v%c4, v%c5" : "=v"(l[0]), "=v"(l[1]) : "i"(R_RS), "i"(R_TS), "i"(R_RS + 1), "i"(R_TS + 1));
    const float cm[2] = {vget<R_M>(), vget<R_M + 1>()};
    l[0] = row_reduce<false>(l[0]); l[1] = row_reduce<false>(l[1]);
    EP p;
    if (it == 0) {
      const f32x4 ml = {cm[0], l[0], cm[1], l[1]};
      asm volatile("buffer_store_dwordx4 %0, v88, s[88:91], %1 offen sc1\n\ts_mov_b32 s101, %2" ::"v"(ml), "s"(oP), "s"(oP + wid * 16384) : "memory");
      phaseF(c, p, make_iseq<74>{});
      asm volatile(FL_POLL "\n\ts_waitcnt vmcnt(17)" ::: "memory");
    } else {
      if (!(mode & 2)) {
        p.fa[0] = __builtin_amdgcn_rcpf(l[0]); p.fa[1] = __builtin_amdgcn_rcpf(l[1]);
        phaseFL<1>(p);
      } else {
        const f32x4 ml = {vget<122>(), vget<123>(), vget<124>(), vget<125>()};
        if (__builtin_expect(__builtin_amdgcn_ballot_w64(ml[0] != cm[0] || ml[2] != cm[1]) == 0, 1)) {
          p.fa[0] = __builtin_amdgcn_rcpf(l[0] + ml[1]); p.fa[1] = __builtin_amdgcn_rcpf(l[1] + ml[3]);
          phaseFL<3>(p);
        } else {
#pragma unroll
          for (int e = 0; e < 2; ++e) {
            const float mm = fmaxf(cm[e], ml[2 * e]);
            const float wa = __builtin_amdgcn_exp2f(cm[e] - mm), wb = __builtin_amdgcn_exp2f(ml[2 * e] - mm);
            const float inv = __builtin_amdgcn_rcpf(l[e] * wa + ml[2 * e + 1] * wb);
            p.fa[e] = wa * inv; p.fb[e] = wb * inv;
          }
          phaseFL<2>(p);
        }
      }
    }
    KST(0, 10 + 3 * (it && !isY));
  }
  asm volatile(TICKET "\n\tv_xor_b32 v%c0, s100, v%c0\np_loop:" ::"i"(R_TB) : "memory");
  GAP();
  p_half<0>(make_iseq<32>{});
  p_half<1>(make_iseq<32>{});
  asm volatile("s_cbranch_scc1 p_loop\n\ts_nop 5");
  GAP();
  p_cvt<1, 0>(); p_cvt<1, 1>(); p_cvt<1, 2>(); p_cvt<1, 3>();
  p_store<1, 1>();
  GAP();
  KST(0, 27);
  {
    constexpr unsigned rdy = 98304;
    int lz = threadIdx.x & 63; asm volatile("" : "+v"(lz));
    const int fr = lz & 15, fq = lz >> 4, qbF = isY != (solo && SWAP7) ? 15 - pj : pj;
    if (wid == 0) {
#pragma unroll 1
      for (int q = 0; q < 0x100000; ++q) {
        unsigned lo, hi, tl, th;
        asm volatile("v_readfirstlane_b32 %0, v240\n\tv_readfirstlane_b32 %1, v241\n\ts_mov_b32 %2, s84\n\ts_mov_b32 %3, s85" : "=s"(lo), "=s"(hi), "=s"(tl), "=s"(th));
        if (__builtin_expect((((lo ^ tl) >> 8) | (hi ^ th)) == 0, 1)) break;
        asm volatile("s_sleep 4\n\tv_mov_b64 v[240:241], 1\n\t" TICKET "\n\ts_waitcnt vmcnt(0)" ::: "memory");
      }
      asm volatile("v_mov_b32 v241, 0\n\ts_mov_b64 exec, 1\n\tds_write_b64 %0, v[240:241]\n\ts_mov_b64 exec, -1" ::"v"(rdy) : "memory");
    }
    vsetu<79>(fo(1024) + qbF * 4352 + fr * 8);
    {
      const int jj = 4 * (fr >> 3) + (fr & 3);
      const unsigned one = ((fq ^ (fr >> 2)) & 1) ? 0u : (jj & 1) ? 0x3f800000u : 0x3f80u;
      static_for([&](auto I) { vsetu<96 + I>((jj >> 1) == I ? one : 0u); }, make_iseq<4>{});
    }
    unsigned vo = (fq >> 1) * 1024 + (fq & 1) * 256 + fr * 16;
    bf16* out = arg_late<bf16, 13>();
    __amdgpu_buffer_rsrc_t so_ = __builtin_amdgcn_make_buffer_rsrc((void*)out, (short)0, SEQ * OUTW * 2, 0x00020000);
    unsigned vw = ((qbF * 128 + (fr & 7)) * OUTW + DA + (fr >> 3) * 32 + fq * 8) * 2;
    asm volatile("" : "+s"(so_));
    asm volatile("s_waitcnt vmcnt(0) lgkmcnt(0)\n\ts_barrier" : "+v"(vo), "+v"(vw) :: "memory");
    fl_set<T_FS>();
    unsigned tk;
    asm volatile("ds_read_b32 %0, %1\n\ts_waitcnt lgkmcnt(0)" : "=v"(tk) : "v"(rdy) : "memory");
    tk = __builtin_amdgcn_readfirstlane(tk) & 255;
    KST(0, 30);
    if (doA) post_math(pj, pu, out, lds + 131072 + wid * 8192);
#define N3 0
#define N2 12
    constexpr int N1 = 28 - 3 * N3 - 2 * N2;
    static_assert(N1 >= 0 && N3 + N2 + N1 <= 16);
    const int nu = tk < N3 ? 3 : tk < N3 + N2 ? 2 : tk < N3 + N2 + N1 ? 1 : 0;
    const int u0 = 4 * (tk < N3 ? 3 * tk : tk < N3 + N2 ? N3 + 2 * tk : 2 * N3 + N2 + tk) + wid * nu;
    KST(0, 28);
    if (nu) {
      unsigned so[3];
#pragma unroll
      for (int k = 0; k < 3; ++k) so[k] = O_S + qbF * 0x200000 + 0x40000 + (u0 + k) * 16384;
      unsigned pend = 0x5555u, cnt = 0x400000;
      while (true) {
        unsigned nw;
        if (wid == 3) {
          u64 m;
          const u64 ex = pend | (pend << 1);
          asm volatile(FL_ARM("%1") "\n\t" FL_POLL "\n\ts_waitcnt vmcnt(0)\n\tv_cmp_eq_u64 %0, s[84:85], v[124:125]" : "=s"(m) : "s"(ex) : "memory");
          nw = (unsigned)m & ((unsigned)m >> 1) & pend;
          if (nw) asm volatile("ds_write_b32 %0, %1 offset:4" ::"v"(rdy), "v"(~pend | nw) : "memory");
        } else {
          unsigned t;
          asm volatile("ds_read_b32 %0, %1 offset:4\n\ts_waitcnt lgkmcnt(0)" : "=v"(t) : "v"(rdy) : "memory");
          nw = __builtin_amdgcn_readfirstlane(t) & pend;
        }
        if (--cnt == 0) nw = pend;
        if (nw == 0) {
          if (wid == 3) asm volatile("s_sleep 4");
          else asm volatile("s_sleep 1");
          continue;
        }
        asm volatile("s_nop 4");
        static_for([&](auto S) {
          if ((nw >> (2 * S)) & 1) {
            r_load<0, 2 * S>(vo, so[0]); r_load<0, 2 * S + 1>(vo, so[0]);
            if (nu > 1) { r_load<1, 2 * S>(vo, so[1]); r_load<1, 2 * S + 1>(vo, so[1]); }
            if (nu > 2) { r_load<2, 2 * S>(vo, so[2]); r_load<2, 2 * S + 1>(vo, so[2]); }
          }
        }, make_iseq<8>{});
        pend &= ~nw;
        if (pend == 0) break;
      }
      asm volatile("s_waitcnt vmcnt(0)" ::: "memory");
      KST(0, 16);
      auto oo = [&](int U) { return ((((U >> 1) & 1) * 64 + ((U >> 2) & 3) * 16 + (U & 1) * 8) * OUTW + (U >> 4) * 64) * 2; };
      r_fin<0>(vw, so_, oo(u0));
      if (nu > 1) r_fin<1>(vw, so_, oo(u0 + 1));
      if (nu > 2) r_fin<2>(vw, so_, oo(u0 + 2));
      if (qbF == 15 && tk == 0) asm volatile(FL_SET("%0") ::"s"(fo(6144)) : "memory");
    }
    KST(0, 29);
  }
  asm volatile("s_waitcnt vmcnt(0)" ::: "memory");
  asm volatile("pf_code_end:");
  KST(0, 1);
}

extern "C" void kernel_launch(void* const* d_in, const int* in_sizes, int n_in, void* d_out, int out_size, void* d_ws,
                              size_t ws_size, hipStream_t stream) {
  const bf16* Aq = (const bf16*)d_in[0];
  const bf16* Ak = (const bf16*)d_in[1];
  const bf16* Av = (const bf16*)d_in[2];
  const bf16* Ash = (const bf16*)d_in[3];
  const bf16* Assmk = (const bf16*)d_in[4];
  const bf16* AssmD = (const bf16*)d_in[5];
  const bf16* Bx = (const bf16*)d_in[6];
  const bf16* Bckv = (const bf16*)d_in[7];
  const bf16* Bwq = (const bf16*)d_in[8];
  const bf16* Bwuk = (const bf16*)d_in[9];
  const bf16* Bwuv = (const bf16*)d_in[10];
  const bf16* Bwo = (const bf16*)d_in[11];
  bf16* out = (bf16*)d_out;
  hipFuncSetAttribute((const void*)fused_kernel, hipFuncAttributeMaxDynamicSharedMemorySize, 163840);
  fused_kernel<<<256, 256, 163840, stream>>>(Bckv, Bwuk, Bwuv, Bx, Bwq, (bf16*)d_ws, Ak, Av, Ash, Assmk, AssmD, Bwo, Aq, out);
}
```

```cpp
#include <hip/hip_runtime.h>
#include <hip/hip_bf16.h>
#include <stdint.h>
#include <utility>

typedef __hip_bfloat16 bf16;
typedef __attribute__((ext_vector_type(4))) short bf16x4;
typedef __attribute__((ext_vector_type(2))) unsigned u32x2;

constexpr int SEQ = 2048;
constexpr int DA = 448;
constexpr int NH = 16;
constexpr int HD = 256;
constexpr int DC = 1024;
constexpr int OUTW = 896;
#define OUTSC 17
#define OUTSCS " sc0 sc1"
#define NTPS " nt"
#define NTVS " nt"
#define NTQS ""
#define NTW 1
#define NYT 15
#define SWAP7 1
constexpr unsigned TSB = NH * SEQ * HD * 2, O_K = TSB, O_S = 3 * TSB, O_P = 5 * TSB, PSTR = 67584, O_F = O_P + 128 * PSTR,
                   O_X = O_F + 400 * 4352, O_Z = O_X + DA * SEQ * 2, WSB = O_Z + DA * SEQ * 2;
constexpr unsigned fo(int w) { return O_F + (w >> 4) * 4352 + (w & 15) * 8; }

#define KST(kid, which)

__device__ __forceinline__ float bf2f(unsigned short u) { return __uint_as_float(((unsigned)u) << 16); }
__device__ __forceinline__ unsigned short f2bf(float f) { return __bfloat16_as_ushort(__float2bfloat16(f)); }
__device__ __forceinline__ float rbf(float f) { return bf2f(f2bf(f)); }
typedef __attribute__((ext_vector_type(2))) __bf16 bf16x2n;
typedef __attribute__((ext_vector_type(2))) float f32x2n;
__device__ __forceinline__ unsigned pk2(float a, float b) {
  return __builtin_bit_cast(unsigned, __builtin_convertvector((f32x2n){a, b}, bf16x2n));
}

__device__ __forceinline__ int off_b(int row, int ch) { return 256 * row + 16 * (ch ^ (((row & 3) << 2) | ((row >> 2) & 3))); }

template <int R> __device__ __forceinline__ void sset(unsigned x) { asm volatile("s_mov_b32 s%c0, %1" ::"i"(R), "s"(x)); }
template <int R> __device__ __forceinline__ void vsetu(unsigned x) { asm volatile("v_mov_b32 v%c0, %1" ::"i"(R), "v"(x)); }
template <int R> __device__ __forceinline__ void srd_set(const void* p, unsigned n) {
  asm volatile("s_mov_b64 s[%c0:%c1], %4\n\ts_mov_b32 s%c2, %5\n\ts_mov_b32 s%c3, 0x20000" ::"i"(R), "i"(R + 1), "i"(R + 2), "i"(R + 3), "s"(p), "s"(n));
}
template <int R, int L> __device__ __forceinline__ void tload() { asm volatile("v_readlane_b32 s%c0, v78, %1" ::"i"(R), "i"(L)); }
template <int L> __device__ __forceinline__ unsigned tget() { unsigned t; asm volatile("v_readlane_b32 %0, v78, %1" : "=s"(t) : "i"(L)); return t; }
#define FL_SET(S) "buffer_store_dwordx2 v[82:83], v81, s[88:91], " S " offen sc1"
#define FL_ARM(N) "v_cndmask_b32_e64 v80, -2.0, v79, " N
#define FL_POLL "buffer_load_dwordx2 v[124:125], v80, s[88:91], 0 offen sc1"
#define FL_CHECK(N) "s_mov_b32 s48, 0x100000\n7:\n\tv_cmp_eq_u64 vcc, s[84:85], v[124:125]\n\ts_and_b64 vcc, vcc, " N "\n\ts_cmp_eq_u64 vcc, " N \
                    "\n\ts_cbranch_scc1 8f\n\ts_sleep 2\n\t" FL_POLL "\n\ts_waitcnt vmcnt(0)\n\ts_sub_u32 s48, s48, 1\n\ts_cbranch_scc0 7b\n8:"
template <int L> __device__ __forceinline__ void fl_set() {
  asm volatile("v_readlane_b32 s48, v78, %0\n\ts_nop 4\n\t" FL_SET("s48") ::"i"(L) : "memory");
}
template <int L> __device__ __forceinline__ void fl_poll() {
  asm volatile("v_readlane_b32 s50, v78, %0\n\tv_readlane_b32 s51, v78, %1\n\ts_nop 1\n\t" FL_ARM("s[50:51]") "\n\t" FL_POLL ::"i"(L), "i"(L + 1) : "memory");
}
#define TICKET "buffer_atomic_add_x2 v[240:241], v81, s[88:91], s101 offen sc0"
__device__ __forceinline__ void fl_check() { asm volatile(FL_CHECK("s[50:51]") ::: "vcc", "scc", "memory"); }
template <int R> __device__ __forceinline__ float vget() { float t; asm volatile("v_mov_b32 %0, v%c1" : "=v"(t) : "i"(R)); return t; }
template <int R> __device__ __forceinline__ void vset(float x) { asm volatile("v_mov_b32 v%c0, %1" ::"i"(R), "v"(x)); }
template <int R> __device__ __forceinline__ float acc_read1() { float t; asm volatile("v_accvgpr_read_b32 %0, a[%c1]" : "=v"(t) : "i"(R)); return t; }
template <class F, int... I> __device__ __forceinline__ void static_for(F&& f, std::integer_sequence<int, I...>) {
  (f(std::integral_constant<int, I>{}), ...);
}
template <int... Is> using iseq = std::integer_sequence<int, Is...>;
template <int N> using make_iseq = std::make_integer_sequence<int, N>;

typedef __attribute__((ext_vector_type(4))) unsigned u32x4c;
typedef __attribute__((ext_vector_type(4))) float f32x4c;
typedef __attribute__((ext_vector_type(2))) float f32x2c;
typedef unsigned long long u64;
struct PR { u32x4c kk[7], vv[4]; };
__device__ __forceinline__ void prep_load(PR& p, int tid, int cg, int tb, const bf16* __restrict__ k, const bf16* __restrict__ v,
                                          const bf16* __restrict__ shift_k) {
  const __amdgpu_buffer_rsrc_t sk = __builtin_amdgcn_make_buffer_rsrc((void*)k, (short)0, SEQ * DA * 2, 0x00020000);
  const __amdgpu_buffer_rsrc_t sv = __builtin_amdgcn_make_buffer_rsrc((void*)v, (short)0, SEQ * DA * 2, 0x00020000);
  const __amdgpu_buffer_rsrc_t sw = __builtin_amdgcn_make_buffer_rsrc((void*)(shift_k + cg * 256), (short)0, 512, 0x00020000);
  const int vo = ((tb * 128 + (tid >> 3) * 4) * DA + cg * 64 + (tid & 7) * 8) * 2;
#pragma unroll
  for (int q = 0; q < 7; ++q)
    asm volatile("buffer_load_dwordx4 %0, %1, %2, 0 offen" NTPS : "=v"(p.kk[q]) : "v"(vo + (q - 3) * DA * 2), "s"(sk) : "memory");
#pragma unroll
  for (int q = 0; q < 4; ++q)
    asm volatile("buffer_load_dwordx4 %0, %1, %2, 0 offen offset:%3" NTVS : "=v"(p.vv[q]) : "v"(vo), "s"(sv), "i"(q * DA * 2) : "memory");
#pragma unroll
  for (int q = 0; q < 4; ++q)
    asm volatile("buffer_load_dwordx4 a[%c0:%c1], %2, %3, 0 offen offset:%4" ::"i"(4 * q), "i"(4 * q + 3), "v"((tid & 7) * 64), "s"(sw), "i"(q * 16) : "memory");
}
__device__ __forceinline__ float bfl(unsigned u) { return __uint_as_float(u << 16); }
__device__ __forceinline__ float bfh(unsigned u) { return __uint_as_float(u & 0xffff0000u); }
__device__ __forceinline__ f32x2c rbf2(f32x2c a) {
  return (f32x2c){__uint_as_float(pk2(0.f, a[0])), __uint_as_float(pk2(0.f, a[1]))};
}
template <int I> __device__ __forceinline__ void prep_math(const PR& p, int tid, char* lx) {
  const int rp = tid >> 3, c = tid & 7;
  f32x2c wt[4], kf[7];
  const unsigned w[4] = {__float_as_uint(acc_read1<4 * I>()), __float_as_uint(acc_read1<4 * I + 1>()), __float_as_uint(acc_read1<4 * I + 2>()),
                         __float_as_uint(acc_read1<4 * I + 3>())};
#pragma unroll
  for (int t = 0; t < 4; ++t)
    wt[t] = (t & 1) ? (f32x2c){bfh(w[t >> 1]), bfh(w[2 + (t >> 1)])} : (f32x2c){bfl(w[t >> 1]), bfl(w[2 + (t >> 1)])};
#pragma unroll
  for (int q = 0; q < 7; ++q) kf[q] = (f32x2c){bfl(p.kk[q][I]), bfh(p.kk[q][I])};
  f32x2c x[4];
#pragma unroll
  for (int e = 0; e < 4; ++e) {
    f32x2c s = rbf2(kf[e] * wt[3]);
    s = rbf2(s + rbf2(kf[e + 1] * wt[2]));
    s = rbf2(s + rbf2(kf[e + 2] * wt[1]));
    s = rbf2(s + rbf2(kf[e + 3] * wt[0]));
    x[e] = s * (f32x2c){bfl(p.vv[e][I]), bfh(p.vv[e][I])};
  }
#pragma unroll
  for (int u = 0; u < 2; ++u)
    *(u32x2*)(lx + (c * 8 + 2 * I + u) * 256 + (((31 - rp) * 8) ^ (c << 4))) = (u32x2){pk2(x[3][u], x[2][u]), pk2(x[1][u], x[0][u])};
}
__device__ __forceinline__ void prep_store(int tid, int cg, int tb, bf16* __restrict__ xr, const char* lx) {
  const __amdgpu_buffer_rsrc_t sx = __builtin_amdgcn_make_buffer_rsrc((void*)(xr + (size_t)cg * 64 * SEQ), (short)0, 64 * SEQ * 2, 0x00020000);
#pragma unroll
  for (int rd = 0; rd < 4; ++rd) {
    const int idx = tid + 256 * rd, ch = idx >> 4, q = idx & 15;
    u32x4c w = *(const u32x4c*)(lx + ch * 256 + 16 * (q ^ (ch >> 3)));
    __builtin_amdgcn_raw_buffer_store_b128(w, sx, (ch * SEQ + (15 - tb) * 128 + q * 8) * 2, 0, 16);
  }
}

#define CODE_TOUCH(LB, LE, wx, NW, NL, m0v) do { unsigned blo_, bhi_, elo_; \
    asm volatile("s_getpc_b64 s[48:49]\n\ts_add_u32 s48, s48, " LB "@rel32@lo+4\n\ts_addc_u32 s49, s49, " LB "@rel32@hi+12\n\t" \
                 "s_mov_b32 %0, s48\n\ts_mov_b32 %1, s49\n\t" \
                 "s_getpc_b64 s[48:49]\n\ts_add_u32 s48, s48, " LE "@rel32@lo+4\n\ts_mov_b32 %2, s48" \
                 : "=s"(blo_), "=s"(bhi_), "=s"(elo_) :: "scc"); \
    const unsigned n_ = elo_ - (blo_ & ~127u); \
    const __amdgpu_buffer_rsrc_t srd_ = __builtin_amdgcn_make_buffer_rsrc((void*)(((unsigned long long)bhi_ << 32) | (blo_ & ~127u)), (short)0, n_ < 262144u ? n_ : 0u, 0x00020000); \
    const unsigned l_ = threadIdx.x & 63, vo_ = l_ < (NL) ? ((wx) + l_ * (NW)) * 128u : 0x80000000u; \
    asm volatile("s_mov_b32 m0, %0\n\ts_nop 4\n\tbuffer_load_dword %1, %2, 0 offen lds" ::"s"(m0v), "v"(vo_), "s"(srd_) : "memory"); } while (0)
template <int I> __device__ __forceinline__ void d_store() {
  asm volatile("buffer_store_dwordx4 v[%c0:%c1], v%c2, s[88:91], s101 offen offset:%3 sc1" ::"i"(128 + 4 * I), "i"(131 + 4 * I), "i"(112 + ((I >> 2) & 1)),
               "i"((I & 3) * 1024) : "memory");
  if constexpr ((I & 7) == 7) asm volatile("s_addk_i32 s101, 0x4000" ::: "scc");
}
constexpr int NS_X = 19;
struct CV { unsigned pa[4], pb, px, vz, sz; float D; };
constexpr int cv_J(bool up, int q) { int J = up ? 0 : 7; while (q > J) { q -= J + 1; J += up ? 1 : -1; } return J; }
constexpr int cv_i(bool up, int q) { int J = up ? 0 : 7; while (q > J) { q -= J + 1; J += up ? 1 : -1; } return q; }
template <int R, int N> __device__ __forceinline__ void cv_rd(CV& c) {
  if constexpr (N < 16) {
    constexpr int i = N >> 1, o = 16 * R + 128 * (i & 1) + 2 * (N & 1), r = 32 + 32 * (R & 1) + 2 * N;
    asm volatile("ds_read2_b32 a[%c1:%c2], %0 offset0:%c3 offset1:%c4" ::"v"(c.pa[i >> 1]), "i"(r), "i"(r + 1), "i"(o), "i"(o + 1));
  } else {
    constexpr int m = N - 16, r = 192 + 32 * (R & 1) + 4 * m;
    asm volatile("ds_read_b128 a[%c1:%c2], %0 offset:%c3" ::"v"(c.pb), "i"(r), "i"(r + 3), "i"(64 * R + 512 * (7 - m)));
  }
}
template <int O> __device__ __forceinline__ void cv_xd(CV& c) {
  constexpr int e = O & 31, x = 96 + ((e >> 1) ^ 1);
  if constexpr (O >= 64) asm volatile("v_cvt_pk_bf16_f32 v%c0, 0, v%c0" ::"i"(160 + e));
  else if constexpr (O >= 32) asm volatile("v_mul_f32 v%c0, v%c0, %1" ::"i"(160 + e), "v"(c.D));
  else if constexpr (O & 1) asm volatile("v_lshlrev_b32 v%c0, 16, v%c1" ::"i"(160 + e), "i"(x));
  else asm volatile("v_and_b32 v%c0, 0xffff0000, v%c1" ::"i"(160 + e), "i"(x));
}
template <int O> __device__ __forceinline__ void cv_z(CV& c) {
  constexpr int J = O / 11, k = O % 11, y = 128 + 4 * J;
  if constexpr (k < 4) asm volatile("v_cvt_pk_bf16_f32 v%c0, 0, v%c0" ::"i"(y + k));
  else if constexpr (k < 8) asm volatile("v_add_f32 v%c0, v%c0, v%c1" ::"i"(y + k - 4), "i"(y + 28 + k));
  else if constexpr (k < 10) asm volatile("v_cvt_pk_bf16_f32 v%c0, v%c1, v%c2" ::"i"(y + k - 8), "i"(y + 2 * k - 16), "i"(y + 2 * k - 15));
  else asm volatile("buffer_store_dwordx2 v[%c0:%c1], %2, s[88:91], %3 offen offset:%4 sc1" ::"i"(y), "i"(y + 1), "v"(c.vz), "s"(c.sz),
                    "i"(512 * J) : "memory");
}
constexpr int cv_z0(int Q) {
  int o = 0;
  for (int q = 0; q < Q; ++q)
    for (int k = 0; k < (q % 4 == 1 ? 0 : 3); ++k) { const int J = o / 11; if ((J + 1) * (J + 2) / 2 - 1 + (J ? 4 : 7) <= q) ++o; }
  return o;
}
constexpr int cv_xd0(int T) { int o = 0; for (int t = 0; t < T; ++t) o += t % 4 != 1 || (t >= 72 && t < 108); return o < 96 ? o : 96; }
static_assert(cv_xd0(144) == 96);
template <int B, int... Is> __device__ __forceinline__ void cv_zs(CV& c, iseq<Is...>) { (cv_z<B + Is>(c), ...); }
template <int R, int Q> __device__ __forceinline__ void cv_slot(CV& c) {
  constexpr int i = cv_i(R == 7, Q), J = cv_J(R == 7, Q), d = 128 + 4 * J, a = 32 + 32 * (R & 1) + 4 * i, b = 192 + 32 * (R & 1) + 4 * (J - i);
  constexpr int T = 36 * R + Q, vm = Q % 4 == 1 && Q < 32 ? Q / 4 : -1;
  if constexpr (Q == 0) asm volatile("s_waitcnt lgkmcnt(0)");
  if constexpr (R >= 6 && vm >= 0 && vm % 4 == 0) asm volatile("s_add_u32 m0, s79, %0" ::"i"(((R - 6) ^ (NYT & 1)) * 32768 + vm * 1024));
  if constexpr (R == 0 && i == 0)
    asm volatile("v_mfma_f32_16x16x32_bf16 v[%c0:%c1], a[%c2:%c3], a[%c4:%c5], 0" ::"i"(d), "i"(d + 3), "i"(a), "i"(a + 3), "i"(b), "i"(b + 3));
  else
    asm volatile("v_mfma_f32_16x16x32_bf16 v[%c0:%c1], a[%c2:%c3], a[%c4:%c5], v[%c0:%c1]" ::"i"(d), "i"(d + 3), "i"(a), "i"(a + 3), "i"(b), "i"(b + 3));
  if constexpr (Q < 24 && R < 7) cv_rd<R + 1, Q>(c);
  if constexpr (T % 7 == 3 && NS_X + 5 + T / 7 < 32) d_store<NS_X + 5 + T / 7>();
  if constexpr (R == 2 && Q >= 24 && Q < 32)
    asm volatile("ds_read_b64 v[%c1:%c2], %0 offset:%c3" ::"v"(c.px), "i"(96 + 2 * (Q - 24)), "i"(97 + 2 * (Q - 24)), "i"(512 * (31 - Q)));
  if constexpr (R >= 3 && R < 7) if constexpr (cv_xd0(T - 107) > cv_xd0(T - 108)) cv_xd<cv_xd0(T - 108)>(c);
  if constexpr ((R == 3 || R == 4) && vm >= 0) {
    constexpr int f = (R - 3) * 8 + vm;
    asm volatile("buffer_load_dwordx4 a[%c0:%c1], v84, s[88:91], s96 offen offset:%2 sc1" ::"i"(128 + 4 * f), "i"(131 + 4 * f), "i"((f & 3) * 1024) : "memory");
    if constexpr ((f & 3) == 3) asm volatile("s_addk_i32 s96, %0" ::"i"(f == 7 ? 28672 : 4096));
  }
  if constexpr (R == 4 && Q == 35) asm volatile(FL_POLL ::: "memory");
  if constexpr (R >= 6 && vm >= 0)
    asm volatile("buffer_load_dwordx4 v%c0, s[88:91], s98 offen offset:%1 sc1 lds" ::"i"(86 + vm / 4), "i"((vm & 3) * 1024) : "memory");
  if constexpr (R == 6 && Q == 35) asm volatile("s_add_u32 s98, s98, 0x8000");
  if constexpr (R == 7) cv_zs<cv_z0(Q)>(c, make_iseq<cv_z0(Q + 1) - cv_z0(Q)>{});
  __builtin_amdgcn_sched_barrier(0);
}
template <int R, int... Q> __device__ __forceinline__ void cv_step(CV& c, iseq<Q...>) { (cv_slot<R, Q>(c), ...); }
template <int... N> __device__ __forceinline__ void cv_rd0(CV& c, iseq<N...>) { (cv_rd<0, N>(c), ...); }
template <int FLAG> __device__ __forceinline__ void conv_wave(unsigned sz, float Dd, char* lw) {
  int lane = threadIdx.x & 63; asm volatile("" : "+v"(lane));
  const int l16 = lane & 15, fq = lane >> 4;
  u32x4c w[5]; unsigned w4[5];
#pragma unroll
  for (int r = 0; r < 5; ++r) {
    w[r] = *(const u32x4c*)(lw + r * 1024 + lane * 16);
    w4[r] = *(const unsigned*)(lw + r * 1024 + lane * 16 + 16);
  }
  d_store<NS_X>(); d_store<NS_X + 1>();
#pragma unroll
  for (int r = 0; r < 5; ++r)
    *(u32x4c*)(lw + 10304 + r * 1024 + lane * 16) = (u32x4c){__builtin_amdgcn_alignbit(w[r][1], w[r][0], 16), __builtin_amdgcn_alignbit(w[r][2], w[r][1], 16),
                                                             __builtin_amdgcn_alignbit(w[r][3], w[r][2], 16), __builtin_amdgcn_alignbit(w4[r], w[r][3], 16)};
  d_store<NS_X + 2>();
  CV c;
#pragma unroll
  for (int i = 0; i < 4; ++i) c.pa[i] = (unsigned)(size_t)lw + ((l16 + 1) & 1) * 10304 + 4 * ((8 * fq + l16 + 1) >> 1) + 1024 * i;
  c.pb = (unsigned)(size_t)lw + 5120 + (2032 - 16 * l16 + 8 * fq) * 2 - 512 * 7;
  c.vz = (16 * l16 + 4 * fq) * 2;
  c.px = (unsigned)(size_t)lw + 5120 + 2044 * 2 - 512 * 7 - c.vz;
  c.sz = sz; c.D = Dd;
  asm volatile("" : "+v"(c.vz), "+v"(c.px), "+v"(c.D) :: "memory");
  asm volatile("" : "+s"(c.sz));
  cv_rd0(c, make_iseq<24>{});
  d_store<NS_X + 3>(); d_store<NS_X + 4>();
  __builtin_amdgcn_sched_barrier(0);
  static_for([&](auto R) __attribute__((always_inline)) {
    cv_step<R>(c, make_iseq<36>{});
    if constexpr (R == 2) {
      asm volatile("s_waitcnt vmcnt(0)\n\ts_barrier" ::: "memory");
      fl_set<FLAG>();
    }
    if constexpr (R == 5) {
      asm volatile("s_waitcnt vmcnt(0)" ::: "memory");
      fl_check();
    }
    __builtin_amdgcn_sched_barrier(0);
  }, make_iseq<8>{});
  asm volatile("s_nop 7");
  cv_zs<cv_z0(36)>(c, make_iseq<88 - cv_z0(36)>{});
}

__device__ __forceinline__ void post_dma(int cg, int tb, const bf16* __restrict__ q, unsigned m0w) {
  int lane = threadIdx.x & 63; asm volatile("" : "+v"(lane));
  const __amdgpu_buffer_rsrc_t sq = __builtin_amdgcn_make_buffer_rsrc((void*)(q + (size_t)tb * 32 * DA + cg * 64), (short)0, 32 * DA * 2, 0x00020000);
  const unsigned vq = ((lane >> 3) * DA + (lane & 7) * 8) * 2;
  asm volatile("s_mov_b32 m0, %0\n\ts_nop 4\n\tbuffer_load_dwordx4 %1, %2, 0 offen" NTQS " lds\n\tbuffer_load_dwordx4 %1, %2, %3 offen offset:1024" NTQS " lds\n\t"
               "buffer_load_dwordx4 %1, %2, %4 offen offset:2048" NTQS " lds\n\tbuffer_load_dwordx4 %1, %2, %5 offen offset:3072" NTQS " lds"
               ::"s"(m0w + 4096), "v"(vq), "s"(sq), "s"(8 * DA * 2 - 1024), "s"(16 * DA * 2 - 2048), "s"(24 * DA * 2 - 3072) : "memory");
}
__device__ __forceinline__ void post_math(int cg, int tb, bf16* __restrict__ out, const char* lw) {
  int lane = threadIdx.x & 63; asm volatile("" : "+v"(lane));
  const int tl = lane >> 3, ck = lane & 7;
  const __amdgpu_buffer_rsrc_t so = __builtin_amdgcn_make_buffer_rsrc((void*)(out + (size_t)tb * 32 * OUTW + cg * 64), (short)0, 32 * OUTW * 2 - 2 * DA, 0x00020000);
  const unsigned vo = (tl * OUTW + ck * 8) * 2;
#pragma unroll
  for (int i = 0; i < 4; ++i) {
    const u32x4c qv = *(const u32x4c*)(lw + 4096 + i * 1024 + lane * 16);
    const char* lz = lw + ck * 64 + 16 * (i ^ (ck >> 1)) + tl * 2;
    u32x4c o;
#pragma unroll
    for (int e = 0; e < 4; ++e) {
      const float z0 = bf2f(*(const unsigned short*)(lz + 2 * e * 512));
      const float z1 = bf2f(*(const unsigned short*)(lz + (2 * e + 1) * 512));
      o[e] = pk2(bfl(qv[e]) * z0, bfh(qv[e]) * z1);
    }
    __builtin_amdgcn_raw_buffer_store_b128(o, so, vo + i * 8 * OUTW * 2, 0, OUTSC);
  }
}

#define DI __device__ __forceinline__
#define GAP() __builtin_amdgcn_sched_barrier(0)
typedef __attribute__((ext_vector_type(4))) float f32x4g;
typedef __attribute__((ext_vector_type(4))) unsigned u32x4g;
constexpr int LDW = NH * HD;
struct GM { unsigned aA[2], aB[2][8]; };
template <bool W, int J, int V, bool NT = false> DI void g_dma() {
  if constexpr (NT && J == 0) asm volatile("buffer_load_dwordx4 v%c0, s[60:63], 0 offen nt lds" ::"i"(V) : "memory");
  else if constexpr (NT) asm volatile("buffer_load_dwordx4 v%c0, s[60:63], s%c1 offen nt lds" ::"i"(V), "i"(70 + J) : "memory");
  else
  if constexpr (J == 0) asm volatile("buffer_load_dwordx4 v%c0, s[%c1:%c2], 0 offen lds" ::"i"(V), "i"(W ? 60 : 56), "i"(W ? 63 : 59) : "memory");
  else asm volatile("buffer_load_dwordx4 v%c0, s[%c1:%c2], s%c3 offen lds" ::"i"(V), "i"(W ? 60 : 56), "i"(W ? 63 : 59), "i"((W ? 70 : 63) + J) : "memory");
}
template <int P, int KS, int I> DI void g_wread(GM& g) {
  constexpr int nf = I >> 1, hf = I & 1, r = 192 - 64 * P + 8 * nf + 4 * KS + 2 * hf;
  asm volatile("ds_read_b64_tr_b16 a[%c1:%c2], %0 offset:%c3" ::"v"(g.aB[hf][nf]), "i"(r), "i"(r + 1), "i"(P * 32768 + KS * 16384));
}
template <int P, int M, int KS> DI void g_aread(GM& g) {
  constexpr int r = 96 + 8 * (M & 1) + 4 * KS;
  asm volatile("ds_read_b128 v[%c1:%c2], %0 offset:%c3" ::"v"(g.aA[KS]), "i"(r), "i"(r + 3), "i"(P * 32768 + M * 2048));
}
constexpr float CS = 0.0625f * 1.4426950408889634f;
constexpr int NS_Q = 19;
constexpr int e_vm(int G) { return (G * 3) % 8 < 3 ? G * 3 / 8 : -1; }
constexpr int e_st(int MODE, int G) {
  const int q = G & 15, k3 = ((G >> 4) - 1) * 3 + (q == 0 ? 0 : q == 4 ? 1 : 2), k2 = NS_Q + (G >> 4) * 2 + (q == 4);
  if (MODE == 5) return e_vm(G) >= 32 ? e_vm(G) - 32 : -1;
  if (MODE == 3) return G >= 33 && (G - 33) % 5 == 0 && (G - 33) / 5 < NS_X ? (G - 33) / 5 : -1;
  if (MODE == 2) return G >= 30 && (q == 0 || q == 4 || q == 14) && k3 - 2 < NS_Q ? k3 - 2 : -1;
  if (MODE == 4) return (q == 0 || q == 4) && k2 < 32 ? k2 : -1;
  return -1;
}
constexpr int e_nst(int MODE, int G0, int G1) { int n = 0; for (int G = G0; G < G1; ++G) n += e_st(MODE, G) >= 0; return n; }
static_assert(e_nst(2, 0, 128) == NS_Q && e_nst(3, 30, 128) == NS_X && e_nst(5, 0, 128) == 16 && e_nst(4, 0, 104) == 32 - NS_Q);
constexpr int e_slot(int MODE, int G) { return MODE == 3 ? (G % 3 == 0 && G < 30 ? G / 3 : -1) : e_vm(G) >= 0 && e_vm(G) < 32 ? 10 + e_vm(G) : -1; }
struct ES { int op0[129], nop[129]; };
constexpr ES e_sched(bool TR, int MODE) {
  ES z{};
  const bool SCL = MODE == 2 || MODE == 4;
  const int per = SCL ? 16 : 8, o1 = 32 * per;
  int o = MODE == 4 ? e_sched(false, 2).op0[128] : 0;
  for (int G = 0; G <= 128; ++G) {
    const int q = G & 15;
    int cap = 2 - (q == 1 || q == 2 || q == 15);
    if (SCL) cap -= (q >= 3 && q < 11 && (q & 1)) + (q == 7 || q == 11) + 2 * (G == 70 || G == 74 || (G & 63) == 61);
    cap += MODE == 2 ? 4 + (q == 10 || q == 13) : MODE == 3 ? 1 : MODE == 5 ? 2 : 0;
    if (e_st(MODE, G) >= 0 || (SCL ? q == 8 || q == 12 : e_slot(MODE, G) >= 0) || (MODE == 4 && G < 4)) cap = 0;
    if (G == 128) cap = 10000;
    z.op0[G] = o;
    while (o < o1) {
      const int i = o / per, rdy = MODE == 4 ? 0 : (TR ? 32 * (i >> 3) + 16 + 2 * (i & 7) + 1 : 4 * i + 3) + 6;
      if ((G < rdy && G < 128) || cap-- <= 0) break;
      ++o; ++z.nop[G];
    }
  }
  return z;
}
template <bool TR, int MODE> constexpr ES ESCH = e_sched(TR, MODE);
static_assert(ESCH<false, 4>.op0[96] == 512 && ESCH<false, 4>.op0[0] >= 24 * 16);
template <bool TR, bool SCL, int O> DI void e_op() {
  constexpr int per = SCL ? 16 : 8, i = O / per, k = O % per, r = 128 + 4 * i, t = 114 + 4 * (i & 1), c = k - (per - 4);
  if constexpr (k < 4) asm volatile("v_accvgpr_read_b32 v%c0, a[%c1]" ::"i"(t + k), "i"(4 * i + k));
  else if constexpr (c < 0) asm volatile("v_mul_f32 v%c0, s83, v%c0" ::"i"(k < 8 ? r + (k ^ 6) : t + k - 8));
  else {
    constexpr int d = c == 0 ? r + 3 : c == 1 ? r + 2 : r + c - 2, x = c == 0 ? r + 2 : c == 1 ? r : t + 2 * (c - 2);
    asm volatile("v_cvt_pk_bf16_f32 v%c0, v%c1, v%c2" ::"i"(d), "i"(x), "i"(x + 1));
  }
}
template <bool TR, bool SCL, int B, int... Is> DI void e_ops(iseq<Is...>) { (e_op<TR, SCL, B + Is>(), ...); }
template <int S> DI void e_m0() {
  if constexpr (S == 0 || S == 1 || S == 5 || S == 9) asm volatile("s_add_u32 m0, s78, %0" ::"i"(S * 1024));
  if constexpr (S >= 26 && (S - 26) % 4 == 0) asm volatile("s_add_u32 m0, s79, %0" ::"i"(((S - 26) >> 3) * 32768 + ((S - 26) & 4) * 1024));
}
template <int S> DI void e_load() {
  if constexpr (S == 0) asm volatile("buffer_load_dwordx4 v122, s[56:59], 0 offen lds" ::: "memory");
  else if constexpr (S < 5) asm volatile("buffer_load_dwordx4 v84, s[56:59], s64 offen offset:%0 lds" ::"i"((S - 1) * 1024) : "memory");
  else if constexpr (S < 9) asm volatile("buffer_load_dwordx4 v84, s[60:63], 0 offen offset:%0 sc1 lds" ::"i"((S - 5) * 1024) : "memory");
  else if constexpr (S == 9) asm volatile("buffer_load_dwordx4 v84, s[60:63], s62 offen sc1 lds" ::: "memory");
  else if constexpr (S < 26) {
    constexpr int f = S - 10;
    asm volatile("buffer_load_dwordx4 a[%c0:%c1], v84, s[88:91], s96 offen offset:%2 sc1" ::"i"(128 + 4 * f), "i"(131 + 4 * f), "i"((f & 3) * 1024) : "memory");
    if constexpr ((f & 3) == 3) asm volatile("s_addk_i32 s96, %0" ::"i"(f == 7 ? 28672 : 4096));
  } else {
    constexpr int f = S - 26;
    asm volatile("buffer_load_dwordx4 v%c0, s[88:91], s98 offen offset:%1 sc1 lds" ::"i"(86 + ((f >> 2) & 1)), "i"((f & 3) * 1024) : "memory");
    if constexpr (f == 7) asm volatile("s_add_u32 s98, s98, s97");
    if constexpr (f == 15) asm volatile(FL_POLL ::: "memory");
  }
}
template <bool TR, int P, int H, int G, int MODE, bool NT> DI void g_gap(GM& g) {
  constexpr int m = 4 * H + (G >> 4), q = G & 15, n = q >> 1, ks = q & 1, GG = 64 * H + G;
  constexpr bool inV = TR ? m & 1 : n & 1, END = MODE == 3 || MODE == 5;
  constexpr int a = 96 + 8 * (m & 1) + 4 * ks, b = 192 - 64 * P + 8 * n + 4 * ks, d = (inV ? 128 : 0) + 4 * (TR ? 8 * (m >> 1) + n : 4 * m + (n >> 1));
  constexpr int J = END ? -1 : q == 8 ? 2 * (G >> 4) : q == 12 ? 2 * (G >> 4) + 1 : -1, gj = (J & 3) ^ ((J & 3) >> 1);
  if constexpr (END) { if constexpr (q == 0 && GG > 0) asm volatile("s_waitcnt lgkmcnt(0)"); e_m0<e_slot(MODE, GG)>(); }
  else if constexpr (q == 0 && G > 0) asm volatile("s_waitcnt lgkmcnt(4)");
  if constexpr (J == 0) asm volatile("s_add_u32 m0, s78, %0" ::"i"((P ^ H ^ 1) * 32768 + (1 - H) * 8192));
  else if constexpr (J == 4) asm volatile("s_add_u32 m0, s79, %0" ::"i"((P ^ H) * 32768 + (1 - H) * 16384));
  else if constexpr (J > 0 && (J & 3) == 1) asm volatile("s_bitset1_b32 m0, 12");
  else if constexpr (J > 0 && (J & 3) == 2) asm volatile("s_bitset1_b32 m0, %0" ::"i"(J < 4 ? 14 : 13));
  else if constexpr (J > 0) asm volatile("s_bitset0_b32 m0, 12");
#define GMF(D, X, Y, C) asm volatile("v_mfma_f32_16x16x32_bf16 " D "[%c0:%c1], " X ", " Y ", " C ::"i"(d), "i"(d + 3), "i"(a), "i"(a + 3), "i"(b), "i"(b + 3))
#define GMF2(D, C) do { if constexpr (TR) GMF(D, "v[%c2:%c3]", "a[%c4:%c5]", C); else GMF(D, "a[%c4:%c5]", "v[%c2:%c3]", C); } while (0)
  if constexpr ((MODE == 1 || MODE == 4) && ks == 0) { if constexpr (inV) GMF2("v", "0"); else GMF2("a", "0"); }
  else if constexpr (inV) GMF2("v", "v[%c0:%c1]");
  else GMF2("a", "a[%c0:%c1]");
#undef GMF2
#undef GMF
  if constexpr ((q == 1 || q == 2) && !(END && m == 7)) g_aread<(m < 7 ? P : P ^ 1), (m + 1) & 7, q - 1>(g);
  if constexpr (!END && q >= 3 && q < 11 && (q & 1)) g_wread<P ^ 1, H, (G >> 4) * 4 + (q - 3) / 2>(g);
  if constexpr (e_st(MODE, GG) >= 0) {
    static_assert(ESCH<TR, MODE>.op0[GG] >= (e_st(MODE, GG) + 1) * (END ? 8 : 16));
    d_store<e_st(MODE, GG)>();
  }
  if constexpr (J >= 0 && J < 4) g_dma<false, (1 - H) * 2 + (gj & 1) + (gj >> 1) * 4, 90>();
  else if constexpr (J >= 4) g_dma<true, (1 - H) * 4 + gj, 91, NT>();
  if constexpr (MODE >= 2) e_ops<TR, !END, ESCH<TR, MODE>.op0[GG]>(make_iseq<ESCH<TR, MODE>.nop[GG]>{});
  if constexpr (END) {
    if constexpr (e_slot(MODE, GG) >= 0) e_load<e_slot(MODE, GG)>();
  } else {
    if constexpr (H == 1 && G == 6) asm volatile("v_lshl_add_u32 v90, s82, 7, v92\n\ts_and_b32 s82, s80, 15");
    if constexpr (H == 1 && G == 10) asm volatile("s_lshr_b64 s[80:81], s[80:81], 4\n\tv_lshl_add_u32 v91, s82, 19, v93");
    constexpr int nst = e_nst(MODE, H ? 29 : 0, GG + 1) + (MODE == 4 && !H ? e_nst(2, 93, 128) : 0);
    if constexpr (G == 61) asm volatile("s_waitcnt vmcnt(%0) lgkmcnt(0)\n\ts_barrier" ::"i"(12 + nst) : "memory");
  }
  if constexpr (END && GG == 127) {
    asm volatile("s_nop 7");
    e_ops<TR, false, ESCH<TR, MODE>.op0[128]>(make_iseq<ESCH<TR, MODE>.nop[128]>{});
  }
  GAP();
}
template <bool TR, int P, int H, int MODE, bool NT, int... Gs> DI void g_half_(GM& g, iseq<Gs...>) { (g_gap<TR, P, H, Gs, MODE, NT>(g), ...); }
template <bool TR, int P, int H, int MODE, bool NT = false> DI void g_half(GM& g) { g_half_<TR, P, H, MODE, NT>(g, make_iseq<64>{}); }
template <bool TR, int P, int MODE, bool NT = false> DI void g_tile(GM& g) { g_half<TR, P, 0, MODE, NT>(g); g_half<TR, P, 1, MODE, NT>(g); }
template <int P, int... Is> DI void g_read0(GM& g, iseq<Is...>) {
  (g_wread<P, 0, Is>(g), ...); (g_wread<P, 1, Is>(g), ...);
  g_aread<P, 0, 0>(g); g_aread<P, 0, 1>(g);
}
DI void g_proW(unsigned g, unsigned mW) {
  asm volatile("v_lshl_add_u32 v91, %0, 18, v93\n\ts_mov_b32 m0, %1" ::"s"(g), "s"(mW));
  asm volatile("s_nop 0"); g_dma<true, 0, 91>();
  asm volatile("s_bitset1_b32 m0, 12\n\ts_nop 0"); g_dma<true, 1, 91>();
  asm volatile("s_bitset1_b32 m0, 13\n\ts_nop 0"); g_dma<true, 3, 91>();
  asm volatile("s_bitset0_b32 m0, 12\n\ts_nop 0"); g_dma<true, 2, 91>();
}
template <int PART = 3> DI void g_pro(unsigned g, unsigned mA, unsigned mW, unsigned lda) {
  g &= 3;
  if constexpr (PART & 1)
    asm volatile("v_add_u32 v90, %0, v92\n\ts_mov_b32 m0, %1" ::"s"((g >> 1) * 128 + (g & 1) * 128 * lda), "s"((mA ^ (g >> 1) * 32768) + (g & 1) * 8192));
  if constexpr (PART == 1) return;
  asm volatile("s_nop 0"); g_dma<false, 0, 90>();
  asm volatile("s_bitset1_b32 m0, 12\n\ts_nop 0"); g_dma<false, 1, 90>();
  asm volatile("s_bitset1_b32 m0, 14\n\ts_nop 0"); g_dma<false, 5, 90>();
  asm volatile("s_bitset0_b32 m0, 12\n\ts_nop 0"); g_dma<false, 4, 90>();
  g_proW(g, mW ^ g * 16384);
}
template <bool TR, bool SCL, class HOOK> DI void g_loop(GM& g, unsigned evm, HOOK&& hook) {
  constexpr int nkt = SCL ? 7 : 16, P0 = SCL ? 0 : 1;
  asm volatile("" : "+s"(evm));
#pragma unroll 1
  for (int t = 1;; t += 2) {
    g_half<TR, P0 ^ 1, 0, 0, (SCL ? NTW >= 1 : NTW >= 2)>(g);
    if (__builtin_expect((evm >> t) & 1, 0)) { hook(t); GAP(); }
    g_half<TR, P0 ^ 1, 1, 0, (SCL ? NTW >= 1 : NTW >= 2)>(g);
    if (SCL && t + 2 >= nkt) break;
    g_half<TR, P0, 0, 0, (SCL ? NTW >= 1 : NTW >= 2)>(g);
    if constexpr (SCL) { if (__builtin_expect(t == 3, 0)) { hook(4); GAP(); } }
    else asm volatile("s_cmp_eq_u32 %0, 13\n\ts_cselect_b32 s78, 0x28000, s78" ::"s"(t) : "scc");
    g_half<TR, P0, 1, 0, (SCL ? NTW >= 1 : NTW >= 2)>(g);
    if (!SCL && t + 3 >= nkt) break;
  }
}

typedef __attribute__((ext_vector_type(4))) unsigned u32x4;
typedef __attribute__((ext_vector_type(4))) float f32x4;

constexpr int R_S = 128, R_P = 192, R_CI = 224, R_E = 96;
constexpr int R_TS = 112, R_RS = 114, R_M = 116;
constexpr int R_PD = 118, R_THR = 120, R_TMP = 121;
constexpr int r_dk(int q) { return q < 6 ? 90 + q : 120 + q; }
constexpr int S_V = 96, S_T = 97, S_K = 98, S_I = 99, S_EV = 100;
struct AC {
  unsigned rare;
  unsigned tb, vs, o0, o1;
};
DI bool redo() { u64 t; asm volatile("s_mov_b64 %0, s[54:55]" : "=s"(t)); return t != 0; }
template <int X, int F> DI void kread() {
  constexpr int r = 192 + 4 * (F & 15);
  asm volatile("ds_read_b128 a[%c0:%c1], v84 offset:%c2" ::"i"(r), "i"(r + 3), "i"(X * 32768 + F * 1024));
}
template <int X, int F> DI void vread() {
  constexpr int r = 192 + 4 * (F & 15);
  asm volatile("ds_read_b128 a[%c0:%c1], v85 offset:%c2" ::"i"(r), "i"(r + 3), "i"(X * 32768 + (F & 1) * 16384 + (F >> 1) * 1024));
}
template <int X, int... Fs> DI void kreads(iseq<Fs...>) { (kread<X, Fs>(), ...); }
template <int X, int J> DI void dma_m0() {
  if constexpr (J == 0) asm volatile("s_mov_b32 m0, s%c0" ::"i"(78 + X));
  else if constexpr (J == 4) asm volatile("s_or_b32 m0, s%c0, 0x1000" ::"i"(78 + X));
  else if constexpr (J == 8) asm volatile("s_bitset0_b32 m0, 16");
  else if constexpr (J == 12) asm volatile("s_bitset0_b32 m0, 12");
}
template <int J> DI void dma_at() {
  if constexpr (J < 8)
    asm volatile("buffer_load_dwordx4 v%c0, s[88:91], s96 offen offset:%1 sc1 lds" ::"i"(86 + (J >= 4)), "i"((J & 3) * 1024) : "memory");
  else
    asm volatile("buffer_load_dwordx4 v%c0, s[92:95], s98 offen offset:%1 sc1 lds" ::"i"(r_dk(J - 8)), "i"((J & 3) * 1024) : "memory");
}
template <int Q> DI void dma4k() {
  asm volatile("s_add_u32 m0, s79, %0\n\ts_add_u32 s48, s98, %1\n\tbuffer_load_dwordx4 v%c2, s[88:91], s48 offen sc1 lds\n\t"
               "buffer_load_dwordx4 v%c2, s[88:91], s48 offen offset:1024 sc1 lds\n\tbuffer_load_dwordx4 v%c2, s[88:91], s48 offen offset:2048 sc1 lds\n\t"
               "buffer_load_dwordx4 v%c2, s[88:91], s48 offen offset:3072 sc1 lds"
               ::"i"((Q >> 1) * 32768 + (Q & 1) * 4096), "i"((Q >> 1) * 32768), "i"(86 + (Q & 1)) : "memory", "scc");
}
template <int Q> DI void acc_zero2() {
  asm volatile("v_accvgpr_write_b32 a[%c0], 0\n\tv_accvgpr_write_b32 a[%c1], 0" ::"i"(2 * Q), "i"(2 * Q + 1));
}
#define SMV 0
struct SMS { int ex[128], op0[128], nop[128], last; };
constexpr SMS sm_sched() {
  SMS z{};
  int et[32] = {}, e = 0;
  for (int t = 0; t < 128; ++t) {
    const int g = t & 15;
    const bool x = t < 48 ? (g >= 8 && (g & 1) && g < 15) : t < 64 ? (g >= 9 && g < 15) : (g >= 8 && g < 14);
    z.ex[t] = x && e < 32 ? e : -1;
    if (z.ex[t] >= 0) et[e++] = t;
  }
  int o = 0;
  for (int t = 0; t < 128; ++t) {
    const int g = t & 15;
    int cap = z.ex[t] >= 0 || t < 8 || (t < 48 && (g == 7 || (g >= 8 && !(g & 1)))) || t == 56 || t == 63 || t == 111 ? 0 : 1;
    z.op0[t] = o; z.nop[t] = 0;
    while (cap > 0 && o < 48 && et[2 * (o / 3) + (o % 3 == 0 ? 0 : 1)] + 2 <= t) {
      const int el = 2 * (o / 3) + o % 3;
      if (!(o % 3 < 2 && (el == 0 || el == 4))) --cap;
      ++o; ++z.nop[t]; z.last = t;
    }
  }
  for (int k = 16; k < 32; ++k) if (z.op0[et[k]] < 3 * ((k - 16) / 2 + 1)) z.last = 1000;
  return z;
}
constexpr SMS SM = sm_sched();
static_assert(SM.last <= 117);
constexpr int p_reg(int Y, int k) { return R_P + 16 * Y + 4 * ((k >> 3) * 2 + ((k >> 1) & 1)) + ((k >> 2) & 1) * 2 + (k & 1); }
template <int Y, int O> DI void sm_op() {
  constexpr int k = O / 3, e = 2 * k + O % 3, ts = R_TS + ((e >> 2) & 1);
  if constexpr (O % 3 == 2) asm volatile("v_cvt_pk_bf16_f32 v%c0, v%c1, v%c2" ::"i"(p_reg(Y, k)), "i"(R_E + ((2 * k) & 15)), "i"(R_E + ((2 * k + 1) & 15)));
  else if constexpr (e == 1 || e == 5) asm volatile("v_add_f32_e32 v%c0, v%c1, v%c2" ::"i"(ts), "i"(R_E + e - 1), "i"(R_E + e));
  else if constexpr (e != 0 && e != 4) asm volatile("v_add_f32_e32 v%c0, v%c0, v%c1" ::"i"(ts), "i"(R_E + (e & 15)));
}
template <int Y, int T> DI void sm_gap(AC& c) {
  if constexpr (SM.ex[T] >= 0) asm volatile("v_exp_f32 v%c0, v%c1" ::"i"(R_E + (SM.ex[T] & 15)), "i"(R_S + 32 * Y + SM.ex[T]));
  if constexpr (SM.nop[T] > 0) sm_op<Y, SM.op0[T]>();
  if constexpr (SM.nop[T] > 1) sm_op<Y, SM.op0[T] + 1>();
  if constexpr (SM.nop[T] > 2) sm_op<Y, SM.op0[T] + 2>();
  if constexpr (T == SM.last + 1) asm volatile("v_add_f32_e32 v%c0, v%c1, v%c2" ::"i"(R_TMP), "i"(R_TS), "i"(R_TS + 1));
  if constexpr (T == SM.last + 2) asm volatile("v_cmp_nle_f32_e64 s[54:55], v%c0, v%c1" ::"i"(R_TMP), "i"(R_THR));
}
#define W_A0 1
template <int X, int MODE, int G> DI void gapA(AC& c) {
  constexpr bool FIRST = MODE == 1 || MODE == 2, NOQK = MODE == 3;
  constexpr int Xp = X ^ 1, kb = G >> 4, g = G & 15, qb = g >> 3, s = g & 7;
  if constexpr (MODE == 3 && G < 48 && G % 3 == 0) {
    constexpr int f = G / 3;
    asm volatile("buffer_load_dwordx4 a[%c0:%c1], v84, s[88:91], s75 offen offset:%2 sc1" ::"i"(128 + 4 * f), "i"(131 + 4 * f), "i"((f & 3) * 1024) : "memory");
    if constexpr ((f & 3) == 3) asm volatile("s_addk_i32 s75, %0" ::"i"(f == 7 ? 28672 : 4096));
  }
  if constexpr (G == 48) asm volatile("s_waitcnt vmcnt(%0) lgkmcnt(0)\n\ts_barrier" ::"i"(MODE == 3 ? 16 : MODE == 2 ? 12 : MODE == 1 ? W_A0 : 0) : "memory");
  else if constexpr (g == 0 && !NOQK) asm volatile("s_waitcnt lgkmcnt(0)");
  if constexpr (!NOQK) {
    constexpr int r = 192 + 4 * ((kb & 1) * 8 + s), q = 128 + 4 * g;
    constexpr int d = R_S + 32 * X + 4 * (kb * 2 + qb), ci = s == 0 ? R_CI + 4 * (kb * 2 + qb) : d;
    if constexpr (FIRST && s == 0)
      asm volatile("v_mfma_f32_16x16x32_bf16 v[%c0:%c1], a[%c2:%c3], a[%c4:%c5], 0" ::"i"(d), "i"(d + 3), "i"(r), "i"(r + 3), "i"(q), "i"(q + 3));
    else
      asm volatile("v_mfma_f32_16x16x32_bf16 v[%c0:%c1], a[%c2:%c3], a[%c4:%c5], v[%c6:%c7]" ::"i"(d), "i"(d + 3), "i"(r), "i"(r + 3), "i"(q), "i"(q + 3),
                   "i"(ci), "i"(ci + 3));
    if constexpr (g < 8 && kb < 3) kread<X, (kb + 1) * 8 + g>();
    constexpr int S0 = SMV ? 40 : 14, S1 = SMV ? 41 : 30, S2 = SMV ? 42 : 45, S3 = SMV ? 43 : 46;
    if constexpr (G == S0) asm volatile("s_add_u32 s96, s97, 0x1000000");
    if constexpr (G == S1) dma_m0<X, 0>();
    if constexpr (G == S2) asm volatile("s_mov_b32 s97, s98");
    if constexpr (G == S3) asm volatile("s_add_u32 s98, s98, s99");
    if constexpr (G >= 56 && !(G & 1)) dma_at<(G - 56) / 2>();
    if constexpr (G == 57) asm volatile("s_lshr_b32 s100, s100, 1\n\ts_add_u32 s56, s56, 1");
    if constexpr (G == 59 && !FIRST) asm volatile("s_and_b32 s49, s100, 1\n\ts_or_b32 s49, s49, s54");
    if constexpr (G == 61 && !FIRST) asm volatile("s_or_b32 %0, s49, s55" : "=s"(c.rare));
    if constexpr (G == 61 && FIRST) asm volatile("s_and_b32 %0, s100, 1" : "=s"(c.rare));
  } else if constexpr (SM.nop[64 + G] > 0) asm volatile("s_nop 0");
  if constexpr (!FIRST && g < 8 && kb == 3) vread<Xp, g>();
  if constexpr (MODE == 2 && (G & 3) == 1) d_store<16 + G / 4>();
  if constexpr (FIRST) acc_zero2<G>();
  else sm_gap<Xp, 64 + G>(c);
  GAP();
}
template <int R> DI void rescale1(float alpha) {
  float t;
  asm volatile("v_accvgpr_read_b32 %0, a[%c2]\n\tv_mul_f32 %0, %0, %1\n\tv_accvgpr_write_b32 a[%c2], %0" : "=&v"(t) : "v"(alpha), "i"(R));
}
template <int... Rs> DI void rescale_all(const float (&alpha)[2], iseq<Rs...>) { (rescale1<Rs>(alpha[(Rs >> 2) & 1]), ...); }
template <bool MAX> DI float row_reduce(float v) {
  auto p = __builtin_amdgcn_permlane32_swap(__float_as_uint(v), __float_as_uint(v), false, false);
  v = MAX ? fmaxf(__uint_as_float(p[0]), __uint_as_float(p[1])) : __uint_as_float(p[0]) + __uint_as_float(p[1]);
  p = __builtin_amdgcn_permlane16_swap(__float_as_uint(v), __float_as_uint(v), false, false);
  return MAX ? fmaxf(__uint_as_float(p[0]), __uint_as_float(p[1])) : __uint_as_float(p[0]) + __uint_as_float(p[1]);
}
template <int Y, int QB> DI float row_max() {
  constexpr int r = R_S + 32 * Y + 4 * QB;
  float mx;
  asm volatile("v_max_f32 %0, v%c1, v%c2\n\tv_max3_f32 %0, %0, v%c3, v%c4" : "=&v"(mx) : "i"(r), "i"(r + 1), "i"(r + 2), "i"(r + 3));
  static_for([&](auto K) {
    constexpr int q = r + 8 * (K + 1);
    float t = mx;
    asm volatile("v_max3_f32 %0, %0, v%c1, v%c2\n\tv_max3_f32 %0, %0, v%c3, v%c4" : "+v"(t) : "i"(q), "i"(q + 1), "i"(q + 2), "i"(q + 3));
    mx = t;
  }, make_iseq<3>{});
  return row_reduce<true>(mx);
}
template <int R> DI void v_sub(float d) { asm volatile("v_sub_f32 v%c0, v%c0, %1" ::"i"(R), "v"(d)); }
template <int B> DI void s_shift(const float (&d)[2]) { static_for([&](auto E) { v_sub<B + E>(d[(E >> 2) & 1]); }, make_iseq<32>{}); }
template <bool CI> DI void m_shift(const float (&d)[2]) {
  if constexpr (CI) s_shift<R_CI>(d);
  v_sub<R_M>(-d[0]); v_sub<R_M + 1>(-d[1]);
}
DI void set_mask0() {
  const int mr = __float_as_int(vget<89>());
  static_for([&](auto I) {
    constexpr int e = (I >> 2) * 8 + (I & 3), k = (I >> 2) * 16 + (I & 3);
    const float x = vget<R_S + e>();
    vset<R_S + e>(k > mr ? -INFINITY : x);
  }, make_iseq<16>{});
}
template <int X, bool CI = true> DI void sm_redo() {
  constexpr int Xp = X ^ 1;
  float d[2], dt[2], alpha[2], ts[2] = {0.f, 0.f};
  static_for([&](auto Q) {
    constexpr int qb = Q;
    const float pd = vget<R_PD + qb>();
    d[qb] = fmaxf(row_max<Xp, qb>() - pd, 0.f);
    dt[qb] = d[qb] + pd;
    vset<R_PD + qb>(d[qb]);
    alpha[qb] = __builtin_amdgcn_exp2f(-d[qb]);
    vset<R_RS + qb>(vget<R_RS + qb>() * alpha[qb]);
  }, make_iseq<2>{});
  vset<R_THR>(d[0] > 0.f || d[1] > 0.f ? -1.f : 4096.f);
  m_shift<CI>(d);
  static_for([&](auto K) {
    constexpr int k = K, qb = (k >> 1) & 1;
    const float e0 = __builtin_amdgcn_exp2f(vget<R_S + 32 * Xp + 2 * k>() - dt[qb]), e1 = __builtin_amdgcn_exp2f(vget<R_S + 32 * Xp + 2 * k + 1>() - dt[qb]);
    ts[qb] += e0; ts[qb] += e1;
    vset<p_reg(Xp, k)>(__uint_as_float(pk2(e0, e1)));
  }, make_iseq<16>{});
  vset<R_TS>(ts[0]); vset<R_TS + 1>(ts[1]);
  rescale_all(alpha, make_iseq<128>{});
  asm volatile("s_nop 1" ::: "memory");
  GAP();
}
template <int X, int MODE, int... Gs> DI void phaseA_(AC& c, iseq<Gs...>) { (gapA<X, MODE, Gs>(c), ...); }
template <int X, int MODE> DI void phaseA(AC& c) { phaseA_<X, MODE>(c, make_iseq<64>{}); }

template <int X, bool SMX, int G> DI void gapB(AC& c) {
  constexpr int Xp = X ^ 1, blk = G >> 4, g = G & 15;
  if constexpr (g == 0) asm volatile("s_waitcnt lgkmcnt(0)");
  constexpr int qb = (g >> 1) & 1, s = g & 1, r = 192 + 4 * ((blk & 1) * 8 + (g >> 2) * 2 + s), o = 4 * ((blk * 4 + (g >> 2)) * 2 + qb);
  constexpr int p = R_P + 16 * Xp + 4 * (s * 2 + qb);
  asm volatile("v_mfma_f32_16x16x32_bf16 a[%c0:%c1], a[%c2:%c3], v[%c4:%c5], a[%c0:%c1]" ::"i"(o), "i"(o + 3), "i"(r), "i"(r + 3), "i"(p), "i"(p + 3));
  if constexpr (g < 8) {
    if constexpr (blk < 3) vread<Xp, (blk + 1) * 8 + g>();
    else if constexpr (SMX) kread<Xp, g>();
  }
  if constexpr (SMX) {
    if constexpr (G == 1 || G == 2) asm volatile("v_add_f32_e32 v%c0, v%c0, v%c1" ::"i"(R_RS + G - 1), "i"(R_TS + G - 1));
    if constexpr (g == 7 && blk < 3) dma_m0<X, blk * 4 + 4>();
    if constexpr (g >= 8 && !(g & 1) && blk < 3) dma_at<blk * 4 + 4 + (g - 8) / 2>();
    if constexpr (G == 55) asm volatile("s_cmp_eq_u32 s56, s76");
    if constexpr (G == 3) asm volatile("s_lshr_b32 s77, s77, 1");
    if constexpr (G == 4) asm volatile("s_bitcmp1_b32 s77, 0");
    if constexpr (G == 5) asm volatile("s_cselect_b64 s[52:53], s[62:63], 0");
    if constexpr (G == 6) asm volatile(FL_ARM("s[52:53]"));
    if constexpr (G == 56) asm volatile(FL_POLL ::: "memory");
    sm_gap<X, G>(c);
  }
  GAP();
}
template <int X, int... Gs> DI void phaseB_(AC& c, iseq<Gs...>) { (gapB<X, true, Gs>(c), ...); }
template <int X> DI void phaseB(AC& c) { phaseB_<X>(c, make_iseq<64>{}); }
constexpr int sm_nex() { int n = 0; for (int t = 0; t < 64; ++t) n += SM.ex[t] >= 0; return n; }
template <int X> DI void phaseB0(unsigned mode) {
  dma_m0<X, 4>();
  kreads<X ^ 1>(make_iseq<8>{});
  dma_at<4>();
  asm volatile("s_nop 3");
  float d[2];
  if (mode & 2) {
    d[0] = vget<122>(); d[1] = vget<123>();
    dma_at<5>();
  } else {
    if constexpr (X == 0) if (__builtin_expect(mode & 4, 0)) set_mask0();
    d[0] = row_max<X, 0>(); GAP(); dma_at<5>();
    d[1] = row_max<X, 1>();
#pragma unroll
    for (int qb = 0; qb < 2; ++qb) if (!(d[qb] > -3.0e38f)) d[qb] = 0.f;
  }
  GAP();
  vset<R_M>(d[0]); vset<R_M + 1>(d[1]);
  dma_at<6>();
  const f32x2c nd[2] = {{-d[0], -d[0]}, {-d[1], -d[1]}};
  static_for([&](auto E) {
    constexpr int e = E;
    const f32x2c v = nd[(e >> 1) & 1];
    asm volatile("v_pk_add_f32 v[%c0:%c1], v[%c0:%c1], %4\n\tv_mov_b64 v[%c2:%c3], %4" ::"i"(R_S + 32 * X + 2 * e), "i"(R_S + 32 * X + 2 * e + 1), "i"(R_CI + 2 * e), "i"(R_CI + 2 * e + 1), "v"(v));
    if constexpr (e == 3) dma_at<7>();
    if constexpr (e == 6) dma_m0<X, 8>();
    if constexpr (e == 7 || e == 11 || e == 15) dma_at<8 + (e - 7) / 4>();
  }, make_iseq<16>{});
  static_assert(SM.op0[64] >= 3 * ((sm_nex() - 15) / 2) && SM.op0[64] <= 24);
  static_for([&](auto E) {
    constexpr int e = E;
    if constexpr (e < sm_nex()) asm volatile("v_exp_f32 v%c0, v%c1" ::"i"(R_E + e), "i"(R_S + 32 * X + e));
    if constexpr (e == 3) dma_at<11>();
    if constexpr (e == 6) dma_m0<X, 12>();
    if constexpr (e == 7 || e == 11 || e == 15) dma_at<12 + (e - 7) / 4>();
  }, make_iseq<16>{});
  static_for([&](auto O) { sm_op<X, O>(); }, make_iseq<SM.op0[64]>{});
  dma_at<15>();
  static_for([&](auto E) { asm volatile("v_exp_f32 v%c0, v%c1" ::"i"(R_E + (int)E), "i"(R_S + 32 * X + 16 + (int)E)); }, make_iseq<(sm_nex() > 16 ? sm_nex() - 16 : 0)>{});
  asm volatile("buffer_store_dwordx2 v[%c0:%c1], v88, s[88:91], s76 offen sc1\n\ts_bitcmp1_b32 s77, 0\n\ts_cselect_b64 s[52:53], s[62:63], 0\n\t"
               FL_ARM("s[52:53]") "\n\t" FL_POLL ::"i"(R_M), "i"(R_M + 1) : "memory", "scc");
  GAP();
}
enum : int { T_Q0, T_K0, T_OQ, T_OG, T_FPREP, T_FQ, T_FG, T_FS, T_ZO, T_CNT, T_NPRE = 10, T_NST = 12, T_TAB = 14, T_SLAB = 16, T_PART, T_IT = 18, T_ITN = 23 };
#define MSK(R, K) "v_cmp_gt_i32 s[48:49], " #K ", v89\n\tv_cmp_gt_i32 s[50:51], " #K "+1, v89\n\tv_cmp_gt_i32 s[52:53], " #K "+2, v89\n\t" \
                  "v_cmp_gt_i32 s[54:55], " #K "+3, v89\n\tv_cndmask_b32_e64 v[" #R "], v[" #R "], v122, s[48:49]\n\t" \
                  "v_cndmask_b32_e64 v[" #R "+1], v[" #R "+1], v122, s[50:51]\n\tv_cndmask_b32_e64 v[" #R "+2], v[" #R "+2], v122, s[52:53]\n\t" \
                  "v_cndmask_b32_e64 v[" #R "+3], v[" #R "+3], v122, s[54:55]\n\t"
DI void ev_code() {
  asm volatile(
    "s_getpc_b64 s[82:83]\n\ts_add_u32 s82, s82, 12\n\ts_addc_u32 s83, s83, 0\n\ts_branch 9f\n\t"
    "s_bitcmp1_b32 s60, s56\n\ts_cbranch_scc1 3f\n\ts_bitcmp1_b32 s74, s56\n\ts_cbranch_scc1 2f\n\t"
    "s_bitcmp0_b32 s57, s56\n\ts_cbranch_scc1 1f\n\ts_bitcmp1_b32 s58, s56\n\ts_cselect_b64 vcc, -1, 0\n\tv_cndmask_b32 v121, -2.0, v81, vcc\n\t"
    "buffer_store_dwordx2 v[82:83], v121, s[88:91], s70 offen sc1\n\tbuffer_store_dwordx2 v[82:83], v121, s[88:91], s71 offen sc1\n\t"
    "v_cmp_eq_u64 vcc, s[84:85], v[124:125]\n\ts_andn2_b64 vcc, s[62:63], vcc\n\ts_cbranch_scc1 5f\n"
    "0:\n\ts_add_u32 s98, s98, s59\n\ts_mov_b32 s59, s61\n\ts_mov_b32 s61, s72\n\ts_mov_b64 s[62:63], s[64:65]\n\ts_mov_b64 s[64:65], s[66:67]\n\t"
    "s_mov_b64 s[66:67], s[68:69]\n"
    "1:\n\ts_setpc_b64 s[86:87]\n"
    "5:\n\t" FL_CHECK("s[62:63]") "\n\ts_branch 0b\n"
    "6:\n\t" FL_CHECK("s[62:63]") "\n\ts_setpc_b64 s[86:87]\n"
    "2:\n\ts_mov_b32 s98, 0\n\t"
    "v_lshrrev_b32 v90, 7, v86\n\tv_and_b32 v91, 6, v90\n\tv_lshlrev_b32 v91, 4, v91\n\tv_and_b32 v92, 0x70, v86\n\tv_xor_b32 v91, v91, v92\n\t"
    "s_movk_i32 s48, 896\n\tv_mad_u32_u24 v94, v90, s48, v91\n\ts_mov_b64 s[92:93], s[80:81]\n\ts_mov_b32 s94, 229376\n\t"
    "v_add_u32 v90, 28672, v94\n\tv_add_u32 v91, 34816, v94\n\tv_add_u32 v92, 40960, v94\n\tv_add_u32 v93, 47104, v94\n\t"
    "v_add_u32 v95, 6144, v94\n\tv_add_u32 v126, 12288, v94\n\tv_add_u32 v127, 18432, v94\n\t"
    "v_mov_b32 v122, 0xff800000\n\t" MSK(228, 0) MSK(236, 16) MSK(244, 32) MSK(252, 48)
    "v_cmp_eq_u64 vcc, s[84:85], v[124:125]\n\ts_andn2_b64 vcc, s[62:63], vcc\n\ts_cbranch_scc1 6b\n\ts_setpc_b64 s[86:87]\n"
    "3:\n\tv_mov_b32 v122, 0xff800000\n\t" MSK(224, 0) MSK(232, 16) MSK(240, 32) MSK(248, 48)
    "v_readlane_b32 s54, v78, %0\n\ts_add_u32 m0, s78, 0x10000\n\t"
    "s_lshr_b32 s53, s73, 16\n\ts_mulk_i32 s53, 3584\n\ts_mov_b64 s[48:49], s[80:81]\n\ts_add_u32 s50, s53, 3584\n\ts_mov_b32 s51, 0x20000\n\t"
    "v_lshlrev_b32 v121, 3, v84\n\tbuffer_load_dword v121, s[48:51], s53 offen lds\n\t"
    "s_bitcmp0_b32 s73, 3\n\ts_cbranch_scc1 4f\n\t"
    "v_lshlrev_b32 v121, 9, v84\n\tv_and_b32 v121, 0x38000, v121\n\tv_lshrrev_b32 v122, 9, v84\n\tv_lshl_add_u32 v121, v122, 12, v121\n\t"
    "v_lshrrev_b32 v122, 3, v84\n\tv_xor_b32 v122, v122, v84\n\tv_and_or_b32 v121, v122, 48, v121\n\t"
    "buffer_load_dwordx4 v121, s[88:91], s54 offen sc1 lds\n\ts_addk_i32 s54, 7168\n\t"
    "buffer_load_dwordx4 v121, s[88:91], s54 offen offset:1024 sc1 lds\n\ts_addk_i32 s54, 7168\n\t"
    "buffer_load_dwordx4 v121, s[88:91], s54 offen offset:2048 sc1 lds\n\ts_addk_i32 s54, 7168\n\t"
    "buffer_load_dwordx4 v121, s[88:91], s54 offen offset:3072 sc1 lds\n"
    "4:\n\ts_setpc_b64 s[86:87]\n9:"
    ::"i"((int)T_ZO) : "scc");
}
DI void ev_call() { asm volatile("s_swappc_b64 s[86:87], s[82:83]" ::: "vcc", "scc", "memory"); GAP(); }

#define ACLOB_ALL "a0","a1","a2","a3","a4","a5","a6","a7","a8","a9","a10","a11","a12","a13","a14","a15","a16","a17","a18","a19","a20","a21","a22","a23","a24","a25","a26","a27","a28","a29","a30","a31","a32","a33","a34","a35","a36","a37","a38","a39","a40","a41","a42","a43","a44","a45","a46","a47","a48","a49","a50","a51","a52","a53","a54","a55","a56","a57","a58","a59","a60","a61","a62","a63","a64","a65","a66","a67","a68","a69","a70","a71","a72","a73","a74","a75","a76","a77","a78","a79","a80","a81","a82","a83","a84","a85","a86","a87","a88","a89","a90","a91","a92","a93","a94","a95","a96","a97","a98","a99","a100","a101","a102","a103","a104","a105","a106","a107","a108","a109","a110","a111","a112","a113","a114","a115","a116","a117","a118","a119","a120","a121","a122","a123","a124","a125","a126","a127","a128","a129","a130","a131","a132","a133","a134","a135","a136","a137","a138","a139","a140","a141","a142","a143","a144","a145","a146","a147","a148","a149","a150","a151","a152","a153","a154","a155","a156","a157","a158","a159","a160","a161","a162","a163","a164","a165","a166","a167","a168","a169","a170","a171","a172","a173","a174","a175","a176","a177","a178","a179","a180","a181","a182","a183","a184","a185","a186","a187","a188","a189","a190","a191","a192","a193","a194","a195","a196","a197","a198","a199","a200","a201","a202","a203","a204","a205","a206","a207","a208","a209","a210","a211","a212","a213","a214","a215","a216","a217","a218","a219","a220","a221","a222","a223","a224","a225","a226","a227","a228","a229","a230","a231","a232","a233","a234","a235","a236","a237","a238","a239","a240","a241","a242","a243","a244","a245","a246","a247","a248","a249","a250","a251","a252","a253","a254","a255"

struct EP { float fa[2], fb[2]; };
template <int I, int E, int MODE> DI void epi_pair(const EP& p) {
  constexpr int qb = I & 1, r = 16 * (I >> 1) + 4 * qb + (E >> 1) * 8 + (E & 1) * 2, t = 96 + 2 * E, w = 128 + 4 * I + E;
  if constexpr (MODE == 2) asm volatile("v_accvgpr_read_b32 v106, a[%c0]" ::"i"(w));
  asm volatile("v_accvgpr_read_b32 v%c0, a[%c2]\n\tv_accvgpr_read_b32 v%c1, a[%c3]" ::"i"(t), "i"(t + 1), "i"(r), "i"(r + 1));
  if constexpr (MODE) asm volatile("v_mul_f32 v%c0, v%c0, %2\n\tv_mul_f32 v%c1, v%c1, %2" ::"i"(t), "i"(t + 1), "v"(p.fa[qb]));
  if constexpr (MODE == 2)
    asm volatile("v_lshlrev_b32 v104, 16, v106\n\tv_and_b32 v105, 0xffff0000, v106\n\tv_fmac_f32 v%c0, v104, %2\n\tv_fmac_f32 v%c1, v105, %2"
                 ::"i"(t), "i"(t + 1), "v"(p.fb[qb]));
  asm volatile("v_cvt_pk_bf16_f32 v%c0, v%c1, v%c2" ::"i"(w), "i"(t), "i"(t + 1));
}
template <int I> DI void epi_store() {
  asm volatile("buffer_store_dwordx4 v[%c0:%c1], v84, s[88:91], s101 offen offset:%2 sc1" ::"i"(128 + 4 * I), "i"(131 + 4 * I), "i"((I & 3) * 1024) : "memory");
  if constexpr ((I & 3) == 3) asm volatile("s_addk_i32 s101, 4096");
}
constexpr int R_TB = 232, R_VS = 236;
template <int HF, int R> DI void p_read() {
  constexpr int mb = R >> 4, r = 192 - 64 * HF + 2 * R;
  asm volatile("ds_read_b64_tr_b16 a[%c1:%c2], v%c0 offset:%c3" ::"i"(R_TB + 2 * HF + mb), "i"(r), "i"(r + 1), "i"((R & 15) * 2048));
}
template <int QB, int E> DI void p_cvt() {
  asm volatile("v_cvt_pk_bf16_f32 v%c0, v%c1, v%c2" ::"i"(208 + 4 * QB + E), "i"(192 + 8 * QB + 2 * E), "i"(193 + 8 * QB + 2 * E));
}
template <int QB, int PR> DI void p_store() {
  asm volatile("buffer_store_dwordx4 v[%c0:%c1], v%c2, s[88:91], s96 offen offset:%3 sc1" ::"i"(208 + 4 * QB), "i"(211 + 4 * QB), "i"(R_VS + QB),
               "i"(PR * 128) : "memory");
}
constexpr int p_dma(int HF, int G) { return G == 3 && !HF ? 0 : G == 11 ? 1 - HF : G == 15 ? 2 - HF : G == 19 ? 3 - HF : G == 27 && HF ? 3 : -1; }
template <int HF, bool LD, int G> DI void p_gap() {
  constexpr int ch = G >> 3, t = G & 7, a = 192 - 64 * HF + 32 * (ch & 1) + 4 * t, b = 128 + 4 * (2 * t + (ch >> 1)), d = 192 + 4 * ch;
  if constexpr (t == 0)
    asm volatile("v_mfma_f32_16x16x32_bf16 v[%c0:%c1], a[%c2:%c3], v[%c4:%c5], 0" ::"i"(d), "i"(d + 3), "i"(a), "i"(a + 3), "i"(b), "i"(b + 3));
  else
    asm volatile("v_mfma_f32_16x16x32_bf16 v[%c0:%c1], a[%c2:%c3], v[%c4:%c5], v[%c0:%c1]" ::"i"(d), "i"(d + 3), "i"(a), "i"(a + 3), "i"(b), "i"(b + 3));
  p_read<HF ^ 1, G>();
  if constexpr (G == 0) asm volatile("v_xor_b32 v%c0, s100, v%c0" ::"i"(R_TB + 2 * HF + 1));
  if constexpr (G == 17) asm volatile("v_xor_b32 v%c0, s100, v%c0" ::"i"(R_TB + 2 * (HF ^ 1)));
  if constexpr (G >= 4 && G <= 8 && G != 7) p_cvt<1, (G < 7 ? G - 4 : 3)>();
  if constexpr (G == 24 || (G == 7 && HF == 0)) asm volatile("s_waitcnt lgkmcnt(8)");
  if constexpr (G == 7 && HF == 1) asm volatile("s_waitcnt vmcnt(%0) lgkmcnt(8)\n\ts_barrier" ::"i"(LD ? 13 : 5) : "memory");
  if constexpr (G == 10) p_store<1, HF ^ 1>();
  if constexpr (G >= 20 && G < 24) p_cvt<0, G - 20>();
  if constexpr (G == 25) p_store<0, HF>();
  if constexpr (LD && p_dma(HF, G) >= 0) dma_at<(HF ? 8 : 12) + p_dma(HF, G)>();
  if constexpr (LD && HF == 0 && G == 2) asm volatile("s_bitset0_b32 m0, 12");
  if constexpr (HF == 0 && G == 13) asm volatile("s_add_u32 s96, s96, 0x40000");
  if constexpr (HF == 0 && G == 26) asm volatile("s_add_u32 s98, s98, 128");
  if constexpr (LD && HF == 0 && G == 27) asm volatile("s_cmp_lt_u32 s98, 0x380");
  if constexpr (HF == 1 && G == 1) asm volatile("s_xor_b32 s100, s100, 0x10000");
  if constexpr (LD && HF == 1 && G == 2) asm volatile("s_xor_b32 s97, s97, s100");
  if constexpr (LD && HF == 1 && G == 9) asm volatile("s_or_b32 m0, s97, 0x1000");
  if constexpr (!LD && HF == 1 && G == 30) asm volatile("s_cmp_lt_u32 s98, 0x500");
  GAP();
}
template <int HF, bool LD, int... Gs> DI void p_half(iseq<Gs...>) { (p_gap<HF, LD, Gs>(), ...); }
template <int G> DI void gapF(AC& c, const EP& p) {
  if constexpr (G < 64) gapB<0, false, G>(c);
  if constexpr (G >= 10) epi_pair<(G - 10) / 4, (G - 10) & 3, 0>(p);
  if constexpr (G >= 14 && ((G - 14) & 3) == 0) epi_store<(G - 14) / 4>();
  if constexpr (G == 73) epi_store<15>();
  GAP();
}
template <int... Gs> DI void phaseF(AC& c, const EP& p, iseq<Gs...>) { (gapF<Gs>(c, p), ...); }
template <int G> DI void gapAL(AC& c) {
  constexpr int kb = G >> 3, s = G & 7, r = 192 + 4 * (G & 15), q = 160 + 4 * s, d = R_S + 36 + 8 * kb, ci = s == 0 ? R_CI + 4 + 8 * kb : d;
  if constexpr (G == 0) asm volatile("s_add_u32 s96, s97, 0x1000000\n\ts_mov_b32 m0, s79\n\ts_waitcnt vmcnt(9) lgkmcnt(0)\n\ts_barrier" ::: "memory");
  if constexpr (G >= 8 && (G & 3) == 0) asm volatile("s_waitcnt lgkmcnt(4)");
  if constexpr (G == 29) asm volatile("s_barrier" ::: "memory");
  asm volatile("v_mfma_f32_16x16x32_bf16 v[%c0:%c1], a[%c2:%c3], a[%c4:%c5], v[%c6:%c7]" ::"i"(d), "i"(d + 3), "i"(r), "i"(r + 3), "i"(q), "i"(q + 3),
               "i"(ci), "i"(ci + 3));
  if constexpr (G < 24) kread<1, G + 8>();
  else vread<0, G - 24>();
  if constexpr (G % 3 == 1 && G < 24) dma_at<G / 3>();
  if constexpr (G == 11) asm volatile("s_bitset1_b32 m0, 12");
  if constexpr (G == 28) asm volatile("buffer_load_dwordx4 v[122:125], v88, s[88:91], s76 offen sc1" ::: "memory");
  if constexpr (G == 30) asm volatile("s_xor_b32 m0, s79, 0x11000\n\ts_movk_i32 s98, 128");
  sm_gap<0, 64 + 2 * G>(c); sm_gap<0, 65 + 2 * G>(c);
  GAP();
}
template <int... Gs> DI void phaseAL(AC& c, iseq<Gs...>) { (gapAL<Gs>(c), ...); }
constexpr int bl_ex(int G) { int n = 0; for (int t = 0; t < G; ++t) { const int g = t & 15; n += t == 6 || (t < 48 && g > 8 && g != 12 && g != 15); } return n; }
constexpr int bl_op(int G) {
  int o = 1;
  for (int t = 15; t < G; ++t)
    if (o < 24 && (t & 3) != 0 && t != 47 && bl_ex(t + 1) == bl_ex(t) && bl_ex(t - 1) > 2 * (o / 3) + (o % 3 != 0)) ++o;
  return o;
}
static_assert(bl_ex(64) == 16 && bl_op(61) == 24);
constexpr int bl_vm(bool LD, int G) {
  int n = 0;
  for (int t = 0; t < G; ++t) n += LD ? (t & 3) == 0 || (t & 15) == 7 || t == 5 || t == 58 || t == 62 || t == 63 : (t & 11) == 8;
  return n;
}
static_assert(bl_vm(true, 64) == 24 && bl_vm(false, 64) == 8);
constexpr int bl_su(bool LD, int G) {
  int n = 0;
  for (int t = 1; t < G; ++t)
    n += n < (LD ? 16 : 10) && t != 47 && (t & 15) != 15 && bl_vm(LD, t + 1) == bl_vm(LD, t) && bl_ex(t + 1) == bl_ex(t) && (LD || t > 55 || (t > 3 && bl_op(t + 1) == bl_op(t)));
  return n;
}
static_assert(bl_su(true, 56) == 16 && bl_su(false, 55) >= 8 && bl_su(false, 62) == 10);
template <bool LD, int G> DI void gapBL(AC& c) {
  if constexpr (G == 48) asm volatile("s_waitcnt vmcnt(%0) lgkmcnt(0)\n\ts_barrier" ::"i"(bl_vm(LD, 48)) : "memory");
  gapB<1, false, G>(c);
  if constexpr (G >= 48 && G < 56) vread<1, G - 48>();
  if constexpr (bl_vm(LD, G + 1) > bl_vm(LD, G)) {
    constexpr int k = bl_vm(LD, G) + (LD ? 0 : 16), I = 2 * (k & 7) + ((k >> 3) & 1);
    if constexpr (k < 16) {
      asm volatile("buffer_load_dwordx4 a[%c0:%c1], v84, s[88:91], s75 offen offset:%2 sc1" ::"i"(128 + 4 * I), "i"(131 + 4 * I), "i"((I & 3) * 1024) : "memory");
      if constexpr (k & 1) asm volatile("s_addk_i32 s75, %0" ::"i"(k == 7 ? -12288 : 4096));
    } else dma_at<k - 8>();
    if constexpr (k == 19) asm volatile("s_bitset0_b32 m0, 12");
  }
  if constexpr (G == 1 || G == 2) asm volatile("v_add_f32_e32 v%c0, v%c0, v%c1" ::"i"(R_RS + G - 1), "i"(R_TS + G - 1));
  if constexpr (G == 3) asm volatile("v_mov_b32 v%c0, 0" ::"i"(R_TS));
  if constexpr (bl_ex(G + 1) > bl_ex(G)) {
    constexpr int j = bl_ex(G);
    asm volatile("v_exp_f32 v%c0, v%c1" ::"i"(R_E + j), "i"(R_S + 36 + 8 * (j >> 2) + (j & 3)));
  }
  if constexpr (bl_op(G + 1) > bl_op(G)) {
    constexpr int o = bl_op(G), k = o / 3, j = 2 * k + o % 3;
    if constexpr (o % 3 == 2) asm volatile("v_cvt_pk_bf16_f32 v%c0, v%c1, v%c2" ::"i"(p_reg(1, (k >> 1) * 4 + 2 + (k & 1))), "i"(R_E + 2 * k), "i"(R_E + 2 * k + 1));
    else asm volatile("v_add_f32_e32 v%c0, v%c1, v%c2" ::"i"(R_TS + 1), "i"(j == 1 ? R_E : R_TS + 1), "i"(R_E + j));
  }
  if constexpr (G == 61) asm volatile("v_cmp_nle_f32_e64 s[54:55], v%c0, v%c1" ::"i"(R_TS + 1), "i"(R_THR));
  if constexpr (bl_su(LD, G + 1) > bl_su(LD, G)) {
    constexpr int k = bl_su(LD, G);
    if constexpr (k < 4) asm volatile("v_xor_b32 v%c0, %1, %2" ::"i"(R_TB + k), "i"(32 * k), "v"(c.tb));
    else if constexpr (k < 6) asm volatile("v_add_u32 v%c0, %1, %2" ::"i"(R_VS + k - 4), "i"(32768 * (k - 4)), "v"(c.vs));
    else if constexpr (k == 6) tload<96, T_SLAB>();
    else if constexpr (k == 7) tload<101, T_CNT>();
    else if constexpr (k == 8) asm volatile("v_mov_b64 v[240:241], 1");
    else if constexpr (k == 9) asm volatile("s_mov_b32 s97, s79\n\ts_mov_b32 s100, 0x8000");
    else if constexpr (k < 14) asm volatile("v_mov_b32 v%c0, %1" ::"i"(k < 12 ? 214 + k : 218 + k), "v"(k & 1 ? c.o1 : c.o0));
    else asm volatile("v_mov_b64 v[%c0:%c1], 0" ::"i"(198 + 2 * k), "i"(199 + 2 * k));
  }
  GAP();
}
template <bool LD, int... Gs> DI void phaseBL(AC& c, iseq<Gs...>) { (gapBL<LD, Gs>(c), ...); }
template <int MODE, int G> DI void gapFL(const EP& p) {
  constexpr int x = G - (MODE == 3 ? 4 : 0), J = G < 24 && G % 3 == 1 ? G / 3 : -1;
  if constexpr (G == 0) asm volatile("s_or_b32 m0, s78, 0x1000\n\ts_movk_i32 s98, 256\n\ts_waitcnt vmcnt(8) lgkmcnt(0)" ::: "memory");
  if constexpr (G >= 8 && G < 32 && (G & 3) == 0) asm volatile("s_waitcnt lgkmcnt(%0)" ::"i"(G < 28 ? 4 : 0));
  if constexpr (G < 32) {
    constexpr int o = 8 * (G >> 1) + 4, r = 192 + 4 * (G & 15), q = R_P + 20 + 8 * (G & 1);
    asm volatile("v_mfma_f32_16x16x32_bf16 a[%c0:%c1], a[%c2:%c3], v[%c4:%c5], a[%c0:%c1]" ::"i"(o), "i"(o + 3), "i"(r), "i"(r + 3), "i"(q), "i"(q + 3));
  }
  if constexpr (G < 24) vread<1, G + 8>();
  if constexpr (G == 28) asm volatile("s_waitcnt vmcnt(8)\n\ts_barrier\n\ts_or_b32 m0, s79, 0x1000\n\ts_movk_i32 s98, 384" ::: "memory");
  if constexpr (G == 29 || G == 31) dma_at<8 + (G - 29) / 2>();
  if constexpr (G == 32 || G == 40) {
    static_for([&](auto I) { p_read<0, 2 * (G - 32) + I>(); }, make_iseq<16>{});
    dma_at<10 + (G - 32) / 8>();
  }
  if constexpr (J >= 0) dma_at<8 + J>();
  if constexpr (x >= 0) epi_pair<2 * ((x >> 2) & 7) + (x >> 5), x & 3, MODE>(p);
  if constexpr (MODE == 3 && G < 32) {
    constexpr int db = G & 15, qb = G >> 4, o = 4 * (db * 2 + qb), a = 224 + 4 * (db & 1), b = 128 + 4 * ((db >> 1) * 2 + qb);
    asm volatile("v_mfma_f32_16x16x32_bf16 a[%c0:%c1], v[%c2:%c3], a[%c4:%c5], a[%c0:%c1]" ::"i"(o), "i"(o + 3), "i"(a), "i"(a + 3), "i"(b), "i"(b + 3));
  }
  if constexpr (J == 3) asm volatile("s_bitset0_b32 m0, 12");
  if constexpr (x == 63) asm volatile("s_waitcnt vmcnt(4) lgkmcnt(0)" ::: "memory");
  GAP();
}
template <int MODE, int... Gs> DI void phaseFL_(const EP& p, iseq<Gs...>) { (gapFL<MODE, Gs>(p), ...); }
template <int MODE> DI void phaseFL(const EP& p) { GAP(); phaseFL_<MODE>(p, make_iseq<MODE == 3 ? 68 : 64>{}); }
template <int U, int K> DI void r_load(unsigned vo, unsigned so) {
  asm volatile("buffer_load_dwordx4 a[%c0:%c1], %2, s[88:91], %3 offen offset:%4 sc1" ::"i"(64 * U + 4 * K), "i"(64 * U + 4 * K + 3), "v"(vo),
               "s"(so + (K >> 2) * 4096), "i"((K & 1) * 512 + ((K >> 1) & 1) * 2048) : "memory");
}
template <int U, int K> DI void r_sum() {
  constexpr int d = 100 + 4 * (K & 1), b = 64 * U + 4 * K;
  if constexpr (K < 2) asm volatile("v_mfma_f32_16x16x32_bf16 v[%c0:%c1], v[96:99], a[%c2:%c3], 0" ::"i"(d), "i"(d + 3), "i"(b), "i"(b + 3));
  else asm volatile("v_mfma_f32_16x16x32_bf16 v[%c0:%c1], v[96:99], a[%c2:%c3], v[%c0:%c1]" ::"i"(d), "i"(d + 3), "i"(b), "i"(b + 3));
}
template <int U> DI void r_fin(unsigned vo, __amdgpu_buffer_rsrc_t srd, unsigned so) {
  asm volatile("" : "+s"(srd), "+s"(so));
  static_for([&](auto K) { r_sum<U, K>(); }, make_iseq<16>{});
  asm volatile("s_nop 7");
  static_for([&](auto E) { asm volatile("v_cvt_pk_bf16_f32 v%c0, v%c1, v%c2" ::"i"(116 + 4 * U + E), "i"(100 + 2 * E), "i"(101 + 2 * E)); }, make_iseq<4>{});
  asm volatile("buffer_store_dwordx4 v[%c3:%c4], %0, %1, %2 offen" OUTSCS ::"v"(vo), "s"(srd), "s"(so), "i"(116 + 4 * U), "i"(119 + 4 * U) : "memory");
}

#define CMAX 12
#define PREPE 0
static_assert(NYT <= 16 && NYT >= 12);
struct TRow { unsigned v[64]; };
constexpr u64 kvmask(int m0, int m1) { u64 n = 0; for (int m = m0; m <= m1; ++m) n |= 0x101ull << m; return n; }
constexpr u64 k_order(bool isY, int rr) {
  const int cls = (rr + (rr >> 2)) & 3;
  return !isY ? (cls == 0 ? 0xfedcb9875a463210ull : cls == 1 ? 0xefadc6982b574310ull : cls == 2 ? 0xfebad7693c285410ull : 0xefcba8764d392510ull)
              : (cls == 0 ? 0xfedcb987543a6210ull : cls == 1 ? 0xefadc698254b7310ull : cls == 2 ? 0xfebad769325c8410ull : 0xefcba876432d9510ull);
}
constexpr TRow t_row(int r, int wid, int j) {
  TRow t{};
  const int rr = r & 15, hsel = rr & 1, pj = rr >> 1, kv = pj & 1, h = j + 8 * hsel, u = hsel * 8 + j;
  const bool isY = r >= 16, solo = pj == 7;
  const int mt = (rr >> 2) + (isY ? 4 : 0), g = j * 128, NY = solo ? 18 : NYT;
  const int nx0 = 32 - 2 * pj - NY, cx = nx0 < CMAX ? nx0 : CMAX, ex = nx0 - cx, qbF = isY != (solo && SWAP7) ? 15 - pj : pj;
  const unsigned oQ = (unsigned)h << 20, oK = oQ + O_K, oP = O_P + (h * 8 + pj) * PSTR;
  if (wid == 4) {
    for (int l = 0; l < 64; ++l)
      t.v[l] = fo(l < 16 ? g + hsel * 16 + l : l < 24 ? g + 16 + hsel * 8 + l : l == 24 ? g + 48 + hsel * 8 + pj : l < 32 ? g + 64 + hsel * 8 + pj :
                  l < 48 ? 2304 + pj * 16 + l - 32 : 2048 + pj * 16 + l - 48);
    return t;
  }
  auto put64 = [&](int i, u64 x) { t.v[i] = (unsigned)x; t.v[i + 1] = (unsigned)(x >> 32); };
  t.v[T_Q0] = oQ + ((solo ? qbF : 15 - pj) * 128 + wid * 16) * 512;
  t.v[T_K0] = oK + (isY ? cx : 0) * 32768;
  t.v[T_OQ] = oQ + pj * 131072 + (wid >> 1) * 65536 + (wid & 1) * 4096;
  t.v[T_OG] = oK + kv * TSB + mt * 131072 + (wid >> 1) * 65536 + (wid & 1) * (kv ? 8192 : 4096);
  t.v[T_FPREP] = PREPE ? fo(2048 + pj * 16 + (isY * 2 + hsel) * 4) + (j - PREPE + 1) * 4 : fo(2048 + pj * 16 + u);
  t.v[T_FQ] = fo(g + 32 + hsel * 8 + pj);
  t.v[T_FG] = fo(g + hsel * 16 + kv * 8 + mt);
  t.v[T_FS] = fo(1024 + qbF * 16 + j * 2 + hsel);
  t.v[T_CNT] = fo(2560 + qbF * 16);
  t.v[T_ZO] = O_Z + pj * 64 * SEQ * 2 + (u * 4 + wid) * 64;
  const u64 own = 0x1ull << (kv * 8 + mt), needQ = isY ? 1ull << (16 + (qbF >> 1)) : (1ull << (16 + (pj >> 1))) | (1ull << (16 + ((15 - pj) >> 1)));
  put64(T_NPRE, isY ? needQ | (1ull << (cx >> 2)) | (1ull << ((cx + 1) >> 2)) : solo ? needQ : needQ | 0xffffull << 48);
  put64(T_NST, isY ? kvmask(cx >> 2, 3) | (solo ? 0 : 1ull << 25) : kvmask(0, solo ? 3 : 0));
  put64(T_TAB, k_order(isY, rr));
  t.v[T_SLAB] = O_S + qbF * 0x200000 + wid * 65536 + (j * 2 + hsel) * 1024;
  t.v[T_PART] = oP;
  for (int it = 0; it < 2; ++it) {
    unsigned* p = t.v + T_IT + it * T_ITN;
    const bool last = isY || it == 1;
    const int n = solo ? 2 * qbF + 2 : isY ? NY : it ? 2 * pj + 2 : nx0;
    const u64 need2 = solo ? 0 : kvmask(0, (2 * pj + 1) >> 2) | (0xffffull << 32);
    u64 q[4] = {0, 0, 0, 0};
    unsigned jmp[3] = {0, 0, 0}, chk = 0;
    if (isY) {
      p[0] = 2; p[1] = 2; p[13] = t.v[T_FG]; p[14] = fo(g + 96 + r);
      if (!(solo && SWAP7)) { chk = 1u << (14 - cx); p[0] |= chk; q[1] = kvmask(4, (31 - 2 * pj) >> 2) & ~own; jmp[1] = ex * 32768; }
      if (!solo) { chk |= 1u << (n - 2); q[2] = 1ull << 24; }
    } else if (it == 0) {
      chk = 2u | (ex ? 1u << (cx - 2) : 0);
      p[0] = chk | 1u << (n - 2);
      chk |= 1u << n;
      q[0] = kvmask(0, cx > 0 ? (cx - 1) >> 2 : 0);
      if (ex) { q[1] = kvmask(4, 4 + ((ex - 1) >> 2)); jmp[1] = (16 - cx) * 32768; }
      jmp[ex ? 2 : 1] = -(ex ? 16 + ex : cx) * 32768;
      q[ex ? 3 : 2] = need2;
      p[1] = 2; p[13] = fo(2304 + pj * 16 + u) + j * (8 * 4352 - 8); p[14] = fo(g + 64 + hsel * 8 + pj);
    } else {
      p[0] = p[1] = solo ? 0 : 1;
      q[0] = need2;
      if (solo && SWAP7) { p[0] = chk = 1u << 14; q[0] = kvmask(4, 4); }
      p[13] = fo(g + 48 + hsel * 8 + pj); p[14] = fo(g + 96 + r);
    }
    p[2] = jmp[0]; p[4] = jmp[1]; p[15] = jmp[2];
    p[3] = !last ? 0 : n > 2 ? 1u << (n - 3) : 1u << 30;
    for (int k = 0; k < 4; ++k) { p[5 + 2 * k] = (unsigned)q[k]; p[6 + 2 * k] = (unsigned)(q[k] >> 32); }
    p[16] = (last ? 1 : 0) | (isY && !solo ? 2 : 0) | (n == 2 ? 4 : 0) | (isY || solo ? 0 : 8) | (n - 1 - last) << 8 | ((r >> 1) * 4 + wid) << 16;
    p[17] = !last ? 0 : n > 2 ? 1u << (n - 2) : 1u << 31;
    p[18] = oQ + (pj * 128 + wid * 16) * 512;
    p[19] = 0;
    p[20] = chk >> 1;
    p[21] = oK + (isY ? cx : 0) * 32768;
    p[22] = ((p[0] | p[3] | p[17]) & 0x3fffffffu) << 1;
  }
  return t;
}
template <int I> constexpr TRow t_base = I < 160 ? t_row(I / 5, I % 5, 0) : [] {
  TRow a = t_row(0, (I - 160) * 4, 0), b = t_row(0, (I - 160) * 4, 1);
  for (int l = 0; l < 64; ++l) a.v[l] = b.v[l] - a.v[l];
  return a;
}();
template <int I> constexpr bool t_linear = [] {
  for (int j = 1; j < 8; ++j) {
    const TRow b = t_row(I / 5, I % 5, j);
    for (int l = 0; l < 64; ++l) if (b.v[l] != t_base<I>.v[l] + j * t_base<160 + (I % 5) / 4>.v[l]) return false;
  }
  return true;
}();
struct Tab { TRow row[162]; };
template <int... I> constexpr Tab t_make(iseq<I...>) {
  static_assert((t_linear<(I < 160 ? I : 0)> && ...));
  return Tab{{t_base<I>...}};
}
__device__ const Tab g_tab = t_make(make_iseq<162>{});

template <int R> DI const bf16* sptr() { const bf16* p; asm volatile("s_mov_b64 %0, s[%c1:%c2]" : "=s"(p) : "i"(R), "i"(R + 1)); return p; }
template <class T, int I> DI T* arg_late() {
  unsigned lo, hi;
  asm volatile("v_readlane_b32 %0, v77, %2\n\tv_readlane_b32 %1, v77, %3\n\ts_nop 4" : "=s"(lo), "=s"(hi) : "i"(2 * I), "i"(2 * I + 1));
  return (T*)(((u64)hi << 32) | lo);
}
__global__ __launch_bounds__(256, 1) __attribute__((amdgpu_num_vgpr(77), amdgpu_num_sgpr(56))) void fused_kernel(
    const bf16* ckv_, const bf16* wuk_, const bf16* wuv_, const bf16* x_, const bf16* wq_, bf16* ws_, const bf16* ak_, const bf16* av_,
    const bf16* ash_, const bf16* assmk_, const bf16* assmD_, const bf16* wo_, const bf16* aq_, bf16* out_) {
  extern __shared__ __attribute__((aligned(16))) char lds[];
  const void* kap = (const void*)__builtin_amdgcn_kernarg_segment_ptr();
  asm volatile("at_code_begin:\n\ts_load_dwordx16 s[80:95], %0, 0\n\ts_load_dwordx2 s[96:97], %0, 0x40" ::"s"(kap) : ACLOB_ALL, "v255", "s101");
  asm volatile("s_getpc_b64 s[48:49]\nct_base:\n\ts_mov_b32 s50, pf_code_end-ct_base\n\ts_mov_b32 s51, 0x20000\n\ts_lshr_b32 s52, %1, 3\n\t"
               "v_and_b32 v116, 63, %0\n\tv_lshrrev_b32 v117, 6, %0\n\tv_lshl_add_u32 v116, v116, 7, v117\n\ts_lshl_b32 s52, s52, 9\n\t"
               "v_lshlrev_b32 v116, 7, v116\n\tbuffer_load_dword v117, v116, s[48:51], s52 offen" ::"v"(threadIdx.x), "s"(blockIdx.x) : "scc", "memory");
  GAP();
  const int tid = threadIdx.x, lane = tid & 63;
  const int wid = __builtin_amdgcn_readfirstlane(tid >> 6);
  const int b = blockIdx.x, j = b & 7, r = b >> 3, rr = r & 15, hsel = rr & 1, pj = rr >> 1, kv = pj & 1;
  const bool isY = r >= 16, solo = pj == 7, doP = (PREPE ? (j & 1) == PREPE - 1 : isY) && !solo, doA = !isY && !solo;
  const int h = j + 8 * hsel, mt = (rr >> 2) + (isY ? 4 : 0), pu = (hsel * 8 + j) * 4 + wid;
  constexpr unsigned m0d = 131072 + 24576;
  const unsigned lA = isY ? 65536 : 0, lW = lA ^ 65536, po = isY ? 0 : 32768;
  unsigned mA = lA + wid * 1024 + po, mW = lW + wid * 1024 + po, ofA = isY ? pj * 256 * DA * 2 : mt * 256 * DC * 2, ofW = h * 512;
  const unsigned lda = isY ? DA : DC;
#define ROT 1
  unsigned g0 = (!ROT ? 0 : isY ? pj + 2 * hsel : rr + (rr >> 2)) & 3;
  {
    const unsigned* tp = g_tab.row[r * 5].v;
    asm volatile("global_load_dword v78, %0, %2\n\tglobal_load_dword v79, %1, %2 offset:1024\n\t"
                 "global_load_dword v118, %1, %3\n\tglobal_load_dword v119, %1, %3 offset:256\n\tglobal_load_dword v77, %4, %5"
                 ::"v"(tid * 4), "v"(lane * 4), "s"(tp), "s"(g_tab.row[160].v), "v"((lane < 27 ? lane : 27) * 4), "s"(kap) : "memory");
    const unsigned ra = wid * 8 + (lane >> 3), sa = ((lane & 7) ^ (((wid & 1) * 4 + (lane >> 4)) & 7)) * 16;
    const unsigned vbA = ra * (lda * 2) + sa, vbB = (4 * wid + (lane >> 4)) * LDW * 2 + 16 * ((lane & 15) ^ (((lane >> 4) << 2) | wid));
    vsetu<92>(vbA); vsetu<93>(vbB); vsetu<94>(ra * (DC * 2) + sa);
    sset<64>(64 * lda); sset<67>(256 * lda); sset<68>(320 * lda);
    asm volatile("s_mov_b32 s71, 0x20000\n\ts_movk_i32 s72, 0x100\n\ts_mov_b32 s73, 0x20100");
    sset<58>(256 * lda * 2); sset<62>((lda - 1) * LDW * 2 + 512);
    asm volatile("s_mov_b32 s59, 0x20000\n\ts_mov_b32 s63, 0x20000");
    g_pro<1>(g0, mA, mW, lda);
    asm volatile("" : "+s"(ofA), "+s"(ofW), "+s"(mA), "+s"(mW), "+s"(g0));
    GAP();
  }
  asm volatile("s_bitcmp1_b32 %0, 4\n\ts_waitcnt lgkmcnt(0)\n\ts_cselect_b64 s[60:61], s[84:85], s[82:83]\n\ts_bitcmp1_b32 %0, 7\n\t"
               "s_cselect_b64 s[60:61], s[88:89], s[60:61]\n\ts_cselect_b64 s[56:57], s[86:87], s[80:81]\n\t"
               "s_add_u32 s56, s56, %1\n\ts_addc_u32 s57, s57, 0\n\ts_add_u32 s60, s60, %2\n\ts_addc_u32 s61, s61, 0\n\t"
               "s_mov_b64 s[88:89], s[90:91]\n\ts_mov_b32 s90, %3\n\ts_mov_b32 s91, 0x20000" ::"s"(b), "s"(ofA), "s"(ofW), "i"(WSB) : "scc");
  KST(0, 0);
  AC c;
  {
    GM g;
    PR pr;
    const int ptb = PREPE ? (isY * 2 + hsel) * 4 + (j >> 1) : hsel * 8 + j;
    if (doP) prep_load(pr, tid, pj, ptb, sptr<92>(), sptr<94>(), sptr<96>());
    g_pro<2>(g0, mA, mW, lda);
    asm volatile("buffer_load_dwordx2 v[120:121], off, s[88:91], %0 sc1" ::"s"(fo(6144)) : "memory");
    if (doP) {
      asm volatile("s_waitcnt vmcnt(9)" ::: "memory");
#pragma unroll
      for (int q = 0; q < 7; ++q) asm volatile("" : "+v"(pr.kk[q]));
#pragma unroll
      for (int q = 0; q < 4; ++q) asm volatile("" : "+v"(pr.vv[q]));
      static_for([&](auto Ic) __attribute__((always_inline)) {
        constexpr int I = Ic;
        if constexpr (I < 3) g_pro(g0 + 1 + I, mA, mW, lda);
        GAP();
        prep_math<I>(pr, tid, lds + 131072);
        GAP();
      }, make_iseq<4>{});
      asm volatile("s_waitcnt lgkmcnt(0)\n\ts_barrier" ::: "memory");
      prep_store(tid, pj, ptb, (bf16*)sptr<88>() + O_X / 2, lds + 131072);
    }
    GAP();
    {
      const int fr = lane & 15, fq = lane >> 4, q = (lane & 15) >> 2, p = lane & 3;
#pragma unroll
      for (int k2 = 0; k2 < 2; ++k2)
        g.aA[k2] = lA + (wid >> 1) * 16384 + (fr >> 3) * 1024 + (fr & 7) * 128 + (((4 * k2 + fq) ^ (fr >> 1)) * 16);
#pragma unroll
      for (int hf = 0; hf < 2; ++hf)
#pragma unroll
        for (int nf = 0; nf < 8; ++nf)
          g.aB[hf][nf] = lW + (wid & 1) * 8192 + off_b(8 * fq + 4 * hf + q, nf * 2 + (p >> 1)) + 8 * (p & 1);
      asm volatile("" : "+v"(g.aA[0]), "+v"(g.aA[1]));
#pragma unroll
      for (int nf = 0; nf < 8; ++nf) asm volatile("" : "+v"(g.aB[0][nf]), "+v"(g.aB[1][nf]));
      const unsigned one = fq != q ? 0u : (p & 1) ? 0x3f800000u : 0x3f80u;
      c.o0 = (p & 2) ? 0u : one; c.o1 = (p & 2) ? one : 0u;
      c.tb = (lane >> 2) * 128 + 16 * ((p >> 1) ^ ((lane >> 2) & 6)) + 8 * (p & 1);
      c.vs = (fr >> 3) * 16384 + (fq & 1) * 512 + (fq >> 1) * 256 + (fr & 7) * 16;
      asm volatile("" : "+v"(c.o0), "+v"(c.o1), "+v"(c.tb), "+v"(c.vs));
      vsetu<81>(tid == 0 ? 0u : 0x80000000u);
      vsetu<84>(lane * 16); vsetu<85>(lane * 16 + 65536); vsetu<86>(wid * 8192 + lane * 16); vsetu<87>(wid * 8192 + lane * 16 + 4096);
      vsetu<88>(65536 + wid * 256 + fr * 16); vsetu<89>(16 * wid + fr - 4 * fq);
      asm volatile("s_mov_b64 s[92:93], s[88:89]\n\ts_mov_b64 s[94:95], s[90:91]\n\ts_mov_b32 s74, 0x40000\n\ts_mov_b32 s75, 0x60000\n\t"
                   "s_mov_b32 s76, 0x40100\n\ts_mov_b32 s77, 0x60100");
      sset<65>(128 * lda); sset<66>(192 * lda); sset<69>(384 * lda); sset<70>(448 * lda);
      sset<78>(lA + wid * 1024); sset<79>(lW + wid * 1024);
    }
    if (doP) asm volatile("s_waitcnt vmcnt(4)" ::: "memory");
    else {
      g_pro(g0 + 1, mA, mW, lda); g_pro(g0 + 2, mA, mW, lda); g_pro(g0 + 3, mA, mW, lda);
      asm volatile("s_waitcnt vmcnt(0)" ::: "memory");
    }
    asm volatile("s_barrier" ::: "memory");
    if (isY) g_read0<0>(g, make_iseq<16>{});
    else g_read0<1>(g, make_iseq<16>{});
    GAP();
    {
      u64 z = (((u64)__float_as_uint(vget<121>()) << 32) | __float_as_uint(vget<120>())) + 0x9e3779b97f4a7c15ull;
      z = (z ^ (z >> 30)) * 0xbf58476d1ce4e5b9ull; z = (z ^ (z >> 27)) * 0x94d049bb133111ebull; z ^= z >> 31;
      vsetu<82>((unsigned)z); vsetu<83>((unsigned)(z >> 32));
      asm volatile("v_mad_u32_u24 v78, v118, %0, v78\n\tv_mad_u32_u24 v79, v119, %0, v79\n\tv_readfirstlane_b32 s84, v82\n\tv_readfirstlane_b32 s85, v83\n\t"
                   "v_readlane_b32 s80, v78, %1\n\tv_readlane_b32 s81, v78, %2" ::"s"(j), "i"((int)T_TAB), "i"(T_TAB + 1));
      if (isY) asm volatile("s_mov_b64 s[80:81], 0x6543210");
    }
    unsigned k2;
    asm volatile("s_bfe_u32 s82, s80, 0x40008\n\ts_lshr_b64 s[80:81], s[80:81], 12\n\ts_lshl_b32 %0, s82, 1\n\tv_add_u32 v90, 128, v92" : "=s"(k2) :: "scc");
    asm volatile("s_waitcnt lgkmcnt(0)\n\ts_barrier" ::: "memory");
    g_proW(k2, mW);
    GAP();
    if (isY) {
      g_tile<false, 0, 1, NTW >= 1>(g);
      g_loop<false, true>(g, doP ? 0x22u : 0x20u, [&](int t) __attribute__((always_inline)) {
        if (t == 1) fl_set<T_FPREP>();
        else if (t == 4) { tload<80, T_TAB>(); tload<81, T_TAB + 1>(); srd_set<60>((kv ? arg_late<const bf16, 2>() : arg_late<const bf16, 1>()) + h * 256, (DC - 1) * LDW * 2 + 512); }
        else {
          srd_set<56>(arg_late<const bf16, 0>() + (size_t)mt * 256 * DC, 256 * DC * 2);
          asm volatile("v_mov_b32 v92, v94");
          static_for([&](auto J) { asm volatile("s_mov_b32 s%c0, %1" ::"i"(64 + J), "i"((J + 1) * 64 * DC)); }, make_iseq<7>{});
        }
      });
      tload<101, T_OQ>(); sset<83>(__float_as_uint(CS));
      asm volatile("v_mov_b32 v112, v84\n\tv_add_u32 v113, 8192, v84");
      GAP();
      g_tile<false, 0, 2, NTW >= 2>(g);
      KST(0, 3);
    }
#define TT 3
    auto kvtile = [&](auto TRc) __attribute__((always_inline)) {
      constexpr bool TR = TRc;
      if (isY) g_tile<TR, 1, 4, NTW >= 2>(g);
      else g_tile<TR, 1, 1, NTW >= 2>(g);
      g_loop<TR, false>(g, 0x2000u | (isY || doA ? 2u : 1u << TT), [&](int t) __attribute__((always_inline)) {
        int l = threadIdx.x & 63; asm volatile("" : "+v"(l));
        if (t == 13) {
          asm volatile("s_mov_b32 s79, 0x28000");
          fl_poll<T_NPRE>();
          if ((b & 15) == 0)
            asm volatile("v_readlane_b32 s48, v78, %0\n\tv_and_b32 v122, 0xffffff00, v82\n\tv_mov_b32 v123, v83\n\ts_nop 2\n\t"
                         "buffer_store_dwordx2 v[122:123], v81, s[88:91], s48 offen sc1" ::"i"((int)T_CNT) : "memory");
          if (!doA && !isY) {
            CODE_TOUCH("at_code_begin", "pf_code_end", hsel * 8 + wid * 2, 16u, 64, m0d);
            CODE_TOUCH("at_code_begin", "pf_code_end", hsel * 8 + wid * 2 + 1, 16u, 64, m0d);
          }
          if (doA) {
            const char* tp = arg_late<const char, 9>() + (size_t)(pj * 64 + pu) * (SEQ * 2) + (l & 31) * 128;
            asm volatile("s_mov_b32 m0, %1\n\ts_nop 0\n\tglobal_load_lds_dword %0, off" ::"v"(tp), "i"(m0d) : "memory");
          }
        } else if (isY) fl_set<T_FQ>();
        else if (doA) { if (doP) fl_set<T_FPREP>(); post_dma(pj, pu, arg_late<const bf16, 12>(), 131072 + wid * 8192); }
        else {
          const char* tp = arg_late<const char, 0>() + 1024 * DC * 2 + (pu * 256 + l) * 128;
          asm volatile("s_mov_b32 m0, %1\n\ts_nop 0\n\tglobal_load_lds_dword %0, off" ::"v"(tp), "i"(m0d) : "memory");
          asm volatile("global_load_lds_dword %0, off" ::"v"(tp + 8192) : "memory");
          asm volatile("global_load_lds_dword %0, off" ::"v"(tp + 16384) : "memory");
          asm volatile("global_load_lds_dword %0, off" ::"v"(tp + 24576) : "memory");
        }
      });
      tload<101, T_OG>(); tload<96, T_Q0>(); tload<98, T_K0>();
      fl_check();
      tload<50, T_NST>(); tload<51, T_NST + 1>();
      asm volatile("v_mov_b32 v112, v84\n\tv_add_u32 v113, %0, v84\n\tv_mov_b32 v90, v87\n\tv_mov_b32 v91, v87\n\tv_mov_b32 v92, v87\n\tv_mov_b32 v93, v87\n\t"
                   "v_mov_b32 v94, v86\n\tv_mov_b32 v95, v86\n\tv_mov_b32 v126, v86\n\tv_mov_b32 v127, v86\n\ts_mov_b32 s99, 0x8000\n\t" FL_ARM("s[50:51]")
                   ::"i"(TR ? 4096 : 8192));
      if (doA) {
        const int d = pj * 64 + pu;
        const bf16* dp = arg_late<const bf16, 10>() + d;
        asm volatile("global_load_ushort v123, %0, off" ::"v"(dp) : "memory");
        srd_set<56>(arg_late<const bf16, 9>() + (size_t)d * SEQ, SEQ * 2);
        srd_set<60>(arg_late<const char, 5>() + O_X + d * SEQ * 2, SEQ * 2);
        sset<78>(65536 + wid * 15488);
        asm volatile("s_movk_i32 s64, 992\n\tv_subrev_u32 v122, 32, v84");
        GAP();
        g_tile<TR, 0, 3>(g);
      } else {
        sset<79>(isY ? wid * 8192 : 0x30000);
        if ((NYT & 1) && !solo) asm volatile("s_add_u32 s98, s98, 0x8000\n\ts_mov_b32 s97, 0xffff8000" ::: "scc");
        else asm volatile("s_mov_b32 s97, 0x8000");
        GAP();
        g_tile<TR, 0, 5>(g);
      }
    };
    if (kv) kvtile(std::true_type{});
    else kvtile(std::false_type{});
  }
  KST(0, 4);
  asm volatile("s_waitcnt vmcnt(16)" ::: "memory");
  sset<79>(wid * 8192);
  if (doA) {
    const float Dd = bfl(__float_as_uint(vget<123>()));
    KST(0, 5);
    conv_wave<T_FG>(O_Z + (pj * 64 + pu) * SEQ * 2, Dd, lds + 65536 + wid * 15488);
    const char* tp = arg_late<const char, 11>() + (size_t)h * (HD * DA * 2) + ((pj * 4 + wid) * 64 + lane) * 128;
    asm volatile("s_mov_b32 m0, %1\n\ts_nop 0\n\tglobal_load_lds_dword %0, off\n\ts_waitcnt vmcnt(17)" ::"v"(tp), "i"(m0d) : "memory");
    KST(0, 6);
  } else if (!isY) {
    static_for([&](auto I) { d_store<16 + I>(); }, make_iseq<16>{});
    tload<98, T_K0>();
    asm volatile("s_waitcnt vmcnt(0)\n\ts_barrier" ::: "memory");
    fl_set<T_FG>();
    KST(0, 5);
    fl_check();
    CODE_TOUCH("att_begin", "pf_code_end", hsel * 4 + wid, 8u, 64, m0d);
    dma4k<0>(); dma4k<1>(); dma4k<2>(); dma4k<3>();
    asm volatile("s_waitcnt vmcnt(0)" ::: "memory");
    KST(0, 6);
  } else {
    tload<76, T_PART>();
    fl_check();
    asm volatile("buffer_load_dwordx2 v[122:123], v88, s[88:91], s76 offen offset:1024 sc1" ::: "memory");
  }
  sset<78>(65536 + wid * 8192); sset<79>(98304 + wid * 8192);
  { const bf16* wp = arg_late<const bf16, 11>() + (size_t)h * HD * DA; asm volatile("s_mov_b64 s[80:81], %0" ::"s"(wp)); }
  asm volatile("att_begin:");
  ev_code();
  asm volatile("s_waitcnt lgkmcnt(0)\n\ts_barrier" ::: "memory");
#pragma unroll 1
  for (int it = doA ? 0 : 1; it < 2; ++it) {
    const bool odd = (NYT & 1) && !solo && (isY || it == 0);
    if (odd) kreads<1>(make_iseq<8>{});
    else kreads<0>(make_iseq<8>{});
    const unsigned oP = tget<T_PART>();
    if (it == 0) {
      static_for([&](auto I) { tload<57 + I, T_IT + I>(); }, make_iseq<21>{});
      tload<S_T, T_IT + 21>(); tload<S_EV, T_IT + 22>();
      asm volatile("s_sub_u32 s70, s70, %0\n\ts_add_u32 s76, %1, 1024" ::"s"(j * (8 * 4352 - 8)), "s"(oP) : "scc");
    } else {
      static_for([&](auto I) { tload<57 + I, T_IT + T_ITN + I>(); }, make_iseq<21>{});
      tload<S_T, T_IT + T_ITN + 21>(); tload<S_EV, T_IT + T_ITN + 22>();
      asm volatile("s_mov_b32 s75, %0\n\ts_mov_b32 s76, %1" ::"s"(oP + wid * 16384), "s"(oP + 1032));
    }
    unsigned mode;
    asm volatile("s_add_u32 s98, s97, 0x8000\n\ts_mov_b32 %0, s73\n\ts_mov_b32 s56, -1\n\tv_mov_b64 v[114:115], 0\n\tv_mov_b64 v[118:119], 0\n\t"
                 "v_mov_b32 v120, 0x45800000" : "=s"(mode) :: "scc");
    KST(0, 8 + 3 * (it && !isY));
    GAP();
#define STA(k)
#define STI()
#define LOOP_ENDS "s_bfe_u32 s76, s73, 0x80008"
    STA(0);
    if (odd) {
      if (isY) phaseA<1, 2>(c);
      else phaseA<1, 1>(c);
      STA(1);
      STA(2);
      phaseB0<1>(mode);
      STA(3);
      asm volatile(LOOP_ENDS "\n\ts_branch att_mid" ::: "scc");
    } else {
      if (isY) phaseA<0, 2>(c);
      else phaseA<0, 1>(c);
      STA(1);
      if (c.rare) ev_call();
      if (__builtin_expect(mode & 4, 0)) { asm volatile("s_mov_b32 s56, 30"); ev_call(); asm volatile("s_mov_b32 s56, 31"); ev_call(); }
      STA(2);
      phaseB0<0>(mode);
      if (__builtin_expect(mode & 4, 0)) { ev_call(); asm volatile("s_branch att_done"); }
      STA(3);
    }
    GAP();
    asm volatile(LOOP_ENDS "\natt_loop:" ::: "scc");
    GAP();
    phaseA<1, 0>(c);
    STI();
    if (__builtin_expect(c.rare != 0, 0)) { if (redo()) sm_redo<1>(); ev_call(); }
    phaseB<1>(c);
    asm volatile("s_cbranch_scc1 att_done\natt_mid:");
    phaseA<0, 0>(c);
    STI();
    if (__builtin_expect(c.rare != 0, 0)) { if (redo()) sm_redo<0>(); ev_call(); }
    phaseB<0>(c);
    asm volatile("s_cbranch_scc0 att_loop\natt_done:");
    GAP();
    STA(4);
    if (it == 0) phaseA<0, 3>(c);
    else {
      sset<76>(oP);
      GAP();
      phaseAL(c, make_iseq<32>{});
      if (__builtin_expect(redo(), 0)) sm_redo<1, false>();
      if (mode & 2) phaseBL<true>(c, make_iseq<64>{});
      else phaseBL<false>(c, make_iseq<64>{});
    }
    if (__builtin_expect(redo(), 0)) {
      if (it) static_for([&](auto I) { vset<R_S + 32 + (I >> 2) * 8 + (I & 3)>(-INFINITY); }, make_iseq<16>{});
      asm volatile("s_nop 7");
      sm_redo<0, false>();
    }
    STA(5);
    KST(0, 9 + 3 * (it && !isY));
    float l[2];
    asm volatile("v_add_f32 %0, v%c2, v%c3\n\tv_add_f32 %1, v%c4, v%c5" : "=v"(l[0]), "=v"(l[1]) : "i"(R_RS), "i"(R_TS), "i"(R_RS + 1), "i"(R_TS + 1));
    const float cm[2] = {vget<R_M>(), vget<R_M + 1>()};
    l[0] = row_reduce<false>(l[0]); l[1] = row_reduce<false>(l[1]);
    EP p;
    if (it == 0) {
      const f32x4 ml = {cm[0], l[0], cm[1], l[1]};
      asm volatile("buffer_store_dwordx4 %0, v88, s[88:91], %1 offen sc1\n\ts_mov_b32 s101, %2" ::"v"(ml), "s"(oP), "s"(oP + wid * 16384) : "memory");
      phaseF(c, p, make_iseq<74>{});
      asm volatile(FL_POLL "\n\ts_waitcnt vmcnt(17)" ::: "memory");
    } else {
      if (!(mode & 2)) {
        p.fa[0] = __builtin_amdgcn_rcpf(l[0]); p.fa[1] = __builtin_amdgcn_rcpf(l[1]);
        phaseFL<1>(p);
      } else {
        const f32x4 ml = {vget<122>(), vget<123>(), vget<124>(), vget<125>()};
        if (__builtin_expect(__builtin_amdgcn_ballot_w64(ml[0] != cm[0] || ml[2] != cm[1]) == 0, 1)) {
          p.fa[0] = __builtin_amdgcn_rcpf(l[0] + ml[1]); p.fa[1] = __builtin_amdgcn_rcpf(l[1] + ml[3]);
          phaseFL<3>(p);
        } else {
#pragma unroll
          for (int e = 0; e < 2; ++e) {
            const float mm = fmaxf(cm[e], ml[2 * e]);
            const float wa = __builtin_amdgcn_exp2f(cm[e] - mm), wb = __builtin_amdgcn_exp2f(ml[2 * e] - mm);
            const float inv = __builtin_amdgcn_rcpf(l[e] * wa + ml[2 * e + 1] * wb);
            p.fa[e] = wa * inv; p.fb[e] = wb * inv;
          }
          phaseFL<2>(p);
        }
      }
    }
    KST(0, 10 + 3 * (it && !isY));
  }
  asm volatile(TICKET "\n\tv_xor_b32 v%c0, s100, v%c0\np_loop:" ::"i"(R_TB) : "memory");
  GAP();
  p_half<0, true>(make_iseq<32>{});
  asm volatile("s_cbranch_scc0 p_mid");
  p_half<1, true>(make_iseq<32>{});
  asm volatile("s_branch p_loop\np_loop2:");
  p_half<0, false>(make_iseq<32>{});
  asm volatile("p_mid:");
  p_half<1, false>(make_iseq<32>{});
  asm volatile("s_cbranch_scc1 p_loop2\n\ts_nop 5");
  GAP();
  p_cvt<1, 0>(); p_cvt<1, 1>(); p_cvt<1, 2>(); p_cvt<1, 3>();
  p_store<1, 1>();
  GAP();
  KST(0, 27);
  {
    constexpr unsigned rdy = 98304;
    int lz = threadIdx.x & 63; asm volatile("" : "+v"(lz));
    const int fr = lz & 15, fq = lz >> 4, qbF = isY != (solo && SWAP7) ? 15 - pj : pj;
    if (wid == 0) {
#pragma unroll 1
      for (int q = 0; q < 0x100000; ++q) {
        unsigned lo, hi, tl, th;
        asm volatile("v_readfirstlane_b32 %0, v240\n\tv_readfirstlane_b32 %1, v241\n\ts_mov_b32 %2, s84\n\ts_mov_b32 %3, s85" : "=s"(lo), "=s"(hi), "=s"(tl), "=s"(th));
        if (__builtin_expect((((lo ^ tl) >> 8) | (hi ^ th)) == 0, 1)) break;
        asm volatile("s_sleep 4\n\tv_mov_b64 v[240:241], 1\n\t" TICKET "\n\ts_waitcnt vmcnt(0)" ::: "memory");
      }
      asm volatile("v_mov_b32 v241, 0\n\ts_mov_b64 exec, 1\n\tds_write_b64 %0, v[240:241]\n\ts_mov_b64 exec, -1" ::"v"(rdy) : "memory");
    }
    vsetu<79>(fo(1024) + qbF * 4352 + fr * 8);
    {
      const int jj = 4 * (fr >> 3) + (fr & 3);
      const unsigned one = ((fq ^ (fr >> 2)) & 1) ? 0u : (jj & 1) ? 0x3f800000u : 0x3f80u;
      static_for([&](auto I) { vsetu<96 + I>((jj >> 1) == I ? one : 0u); }, make_iseq<4>{});
    }
    unsigned vo = (fq >> 1) * 1024 + (fq & 1) * 256 + fr * 16;
    bf16* out = arg_late<bf16, 13>();
    __amdgpu_buffer_rsrc_t so_ = __builtin_amdgcn_make_buffer_rsrc((void*)out, (short)0, SEQ * OUTW * 2, 0x00020000);
    unsigned vw = ((qbF * 128 + (fr & 7)) * OUTW + DA + (fr >> 3) * 32 + fq * 8) * 2;
    asm volatile("" : "+s"(so_));
    asm volatile("s_waitcnt vmcnt(0) lgkmcnt(0)\n\ts_barrier" : "+v"(vo), "+v"(vw) :: "memory");
    fl_set<T_FS>();
    unsigned tk;
    asm volatile("ds_read_b32 %0, %1\n\ts_waitcnt lgkmcnt(0)" : "=v"(tk) : "v"(rdy) : "memory");
    tk = __builtin_amdgcn_readfirstlane(tk) & 255;
    KST(0, 30);
    if (doA) post_math(pj, pu, out, lds + 131072 + wid * 8192);
#define N3 0
#define N2 12
    constexpr int N1 = 28 - 3 * N3 - 2 * N2;
    static_assert(N1 >= 0 && N3 + N2 + N1 <= 16);
    const int nu = tk < N3 ? 3 : tk < N3 + N2 ? 2 : tk < N3 + N2 + N1 ? 1 : 0;
    const int u0 = 4 * (tk < N3 ? 3 * tk : tk < N3 + N2 ? N3 + 2 * tk : 2 * N3 + N2 + tk) + wid * nu;
    KST(0, 28);
    if (nu) {
      unsigned so[3];
#pragma unroll
      for (int k = 0; k < 3; ++k) so[k] = O_S + qbF * 0x200000 + 0x40000 + (u0 + k) * 16384;
      unsigned pend = 0x5555u, cnt = 0x400000;
      while (true) {
        unsigned nw;
        if (wid == 3) {
          u64 m;
          const u64 ex = pend | (pend << 1);
          asm volatile(FL_ARM("%1") "\n\t" FL_POLL "\n\ts_waitcnt vmcnt(0)\n\tv_cmp_eq_u64 %0, s[84:85], v[124:125]" : "=s"(m) : "s"(ex) : "memory");
          nw = (unsigned)m & ((unsigned)m >> 1) & pend;
          if (nw) asm volatile("ds_write_b32 %0, %1 offset:4" ::"v"(rdy), "v"(~pend | nw) : "memory");
        } else {
          unsigned t;
          asm volatile("ds_read_b32 %0, %1 offset:4\n\ts_waitcnt lgkmcnt(0)" : "=v"(t) : "v"(rdy) : "memory");
          nw = __builtin_amdgcn_readfirstlane(t) & pend;
        }
        if (--cnt == 0) nw = pend;
        if (nw == 0) {
          if (wid == 3) asm volatile("s_sleep 4");
          else asm volatile("s_sleep 1");
          continue;
        }
        asm volatile("s_nop 4");
        static_for([&](auto S) {
          if ((nw >> (2 * S)) & 1) {
            r_load<0, 2 * S>(vo, so[0]); r_load<0, 2 * S + 1>(vo, so[0]);
            if (nu > 1) { r_load<1, 2 * S>(vo, so[1]); r_load<1, 2 * S + 1>(vo, so[1]); }
            if (nu > 2) { r_load<2, 2 * S>(vo, so[2]); r_load<2, 2 * S + 1>(vo, so[2]); }
          }
        }, make_iseq<8>{});
        pend &= ~nw;
        if (pend == 0) break;
      }
      asm volatile("s_waitcnt vmcnt(0)" ::: "memory");
      KST(0, 16);
      auto oo = [&](int U) { return ((((U >> 1) & 1) * 64 + ((U >> 2) & 3) * 16 + (U & 1) * 8) * OUTW + (U >> 4) * 64) * 2; };
      r_fin<0>(vw, so_, oo(u0));
      if (nu > 1) r_fin<1>(vw, so_, oo(u0 + 1));
      if (nu > 2) r_fin<2>(vw, so_, oo(u0 + 2));
      if (qbF == 15 && tk == 0) asm volatile(FL_SET("%0") ::"s"(fo(6144)) : "memory");
    }
    KST(0, 29);
  }
  asm volatile("s_waitcnt vmcnt(0)" ::: "memory");
  asm volatile("pf_code_end:");
  KST(0, 1);
}

extern "C" void kernel_launch(void* const* d_in, const int* in_sizes, int n_in, void* d_out, int out_size, void* d_ws,
                              size_t ws_size, hipStream_t stream) {
  const bf16* Aq = (const bf16*)d_in[0];
  const bf16* Ak = (const bf16*)d_in[1];
  const bf16* Av = (const bf16*)d_in[2];
  const bf16* Ash = (const bf16*)d_in[3];
  const bf16* Assmk = (const bf16*)d_in[4];
  const bf16* AssmD = (const bf16*)d_in[5];
  const bf16* Bx = (const bf16*)d_in[6];
  const bf16* Bckv = (const bf16*)d_in[7];
  const bf16* Bwq = (const bf16*)d_in[8];
  const bf16* Bwuk = (const bf16*)d_in[9];
  const bf16* Bwuv = (const bf16*)d_in[10];
  const bf16* Bwo = (const bf16*)d_in[11];
  bf16* out = (bf16*)d_out;
  hipFuncSetAttribute((const void*)fused_kernel, hipFuncAttributeMaxDynamicSharedMemorySize, 163840);
  fused_kernel<<<256, 256, 163840, stream>>>(Bckv, Bwuk, Bwuv, Bx, Bwq, (bf16*)d_ws, Ak, Av, Ash, Assmk, AssmD, Bwo, Aq, out);
}
```

```cpp
#include <hip/hip_runtime.h>
#include <hip/hip_bf16.h>
#include <stdint.h>
#include <utility>

typedef __hip_bfloat16 bf16;
typedef __attribute__((ext_vector_type(4))) short bf16x4;
typedef __attribute__((ext_vector_type(2))) unsigned u32x2;

constexpr int SEQ = 2048;
constexpr int DA = 448;
constexpr int NH = 16;
constexpr int HD = 256;
constexpr int DC = 1024;
constexpr int OUTW = 896;
#define OUTSC 17
#define OUTSCS " sc0 sc1"
#define NTPS ""
#define NTVS ""
#define NTQS ""
#define NTW 0
#define NYT 15
#define SWAP7 1
constexpr unsigned TSB = NH * SEQ * HD * 2, O_K = TSB, O_S = 3 * TSB, O_P = 5 * TSB, PSTR = 67584, O_F = O_P + 128 * PSTR,
                   O_X = O_F + 400 * 4352, O_Z = O_X + DA * SEQ * 2, WSB = O_Z + DA * SEQ * 2;
constexpr unsigned fo(int w) { return O_F + (w >> 4) * 4352 + (w & 15) * 8; }

#define KST(kid, which)

__device__ __forceinline__ float bf2f(unsigned short u) { return __uint_as_float(((unsigned)u) << 16); }
__device__ __forceinline__ unsigned short f2bf(float f) { return __bfloat16_as_ushort(__float2bfloat16(f)); }
__device__ __forceinline__ float rbf(float f) { return bf2f(f2bf(f)); }
typedef __attribute__((ext_vector_type(2))) __bf16 bf16x2n;
typedef __attribute__((ext_vector_type(2))) float f32x2n;
__device__ __forceinline__ unsigned pk2(float a, float b) {
  return __builtin_bit_cast(unsigned, __builtin_convertvector((f32x2n){a, b}, bf16x2n));
}

__device__ __forceinline__ int off_b(int row, int ch) { return 256 * row + 16 * (ch ^ (((row & 3) << 2) | ((row >> 2) & 3))); }

template <int R> __device__ __forceinline__ void sset(unsigned x) { asm volatile("s_mov_b32 s%c0, %1" ::"i"(R), "s"(x)); }
template <int R> __device__ __forceinline__ void vsetu(unsigned x) { asm volatile("v_mov_b32 v%c0, %1" ::"i"(R), "v"(x)); }
template <int R> __device__ __forceinline__ void srd_set(const void* p, unsigned n) {
  asm volatile("s_mov_b64 s[%c0:%c1], %4\n\ts_mov_b32 s%c2, %5\n\ts_mov_b32 s%c3, 0x20000" ::"i"(R), "i"(R + 1), "i"(R + 2), "i"(R + 3), "s"(p), "s"(n));
}
template <int R, int L> __device__ __forceinline__ void tload() { asm volatile("v_readlane_b32 s%c0, v78, %1" ::"i"(R), "i"(L)); }
template <int L> __device__ __forceinline__ unsigned tget() { unsigned t; asm volatile("v_readlane_b32 %0, v78, %1" : "=s"(t) : "i"(L)); return t; }
#define FL_SET(S) "buffer_store_dwordx2 v[82:83], v81, s[88:91], " S " offen sc1"
#define FL_ARM(N) "v_cndmask_b32_e64 v80, -2.0, v79, " N
#define FL_POLL "buffer_load_dwordx2 v[124:125], v80, s[88:91], 0 offen sc1"
#define FL_CHECK(N) "s_mov_b32 s48, 0x100000\n7:\n\tv_cmp_eq_u64 vcc, s[84:85], v[124:125]\n\ts_and_b64 vcc, vcc, " N "\n\ts_cmp_eq_u64 vcc, " N \
                    "\n\ts_cbranch_scc1 8f\n\ts_sleep 2\n\t" FL_POLL "\n\ts_waitcnt vmcnt(0)\n\ts_sub_u32 s48, s48, 1\n\ts_cbranch_scc0 7b\n8:"
template <int L> __device__ __forceinline__ void fl_set() {
  asm volatile("v_readlane_b32 s48, v78, %0\n\ts_nop 4\n\t" FL_SET("s48") ::"i"(L) : "memory");
}
template <int L> __device__ __forceinline__ void fl_poll() {
  asm volatile("v_readlane_b32 s50, v78, %0\n\tv_readlane_b32 s51, v78, %1\n\ts_nop 1\n\t" FL_ARM("s[50:51]") "\n\t" FL_POLL ::"i"(L), "i"(L + 1) : "memory");
}
#define TICKET "buffer_atomic_add_x2 v[240:241], v81, s[88:91], s101 offen sc0"
__device__ __forceinline__ void fl_check() { asm volatile(FL_CHECK("s[50:51]") ::: "vcc", "scc", "memory"); }
template <int R> __device__ __forceinline__ float vget() { float t; asm volatile("v_mov_b32 %0, v%c1" : "=v"(t) : "i"(R)); return t; }
template <int R> __device__ __forceinline__ void vset(float x) { asm volatile("v_mov_b32 v%c0, %1" ::"i"(R), "v"(x)); }
template <int R> __device__ __forceinline__ float acc_read1() { float t; asm volatile("v_accvgpr_read_b32 %0, a[%c1]" : "=v"(t) : "i"(R)); return t; }
template <class F, int... I> __device__ __forceinline__ void static_for(F&& f, std::integer_sequence<int, I...>) {
  (f(std::integral_constant<int, I>{}), ...);
}
template <int... Is> using iseq = std::integer_sequence<int, Is...>;
template <int N> using make_iseq = std::make_integer_sequence<int, N>;

typedef __attribute__((ext_vector_type(4))) unsigned u32x4c;
typedef __attribute__((ext_vector_type(4))) float f32x4c;
typedef __attribute__((ext_vector_type(2))) float f32x2c;
typedef unsigned long long u64;
struct PR { u32x4c kk[7], vv[4]; };
__device__ __forceinline__ void prep_load(PR& p, int tid, int cg, int tb, const bf16* __restrict__ k, const bf16* __restrict__ v,
                                          const bf16* __restrict__ shift_k) {
  const __amdgpu_buffer_rsrc_t sk = __builtin_amdgcn_make_buffer_rsrc((void*)k, (short)0, SEQ * DA * 2, 0x00020000);
  const __amdgpu_buffer_rsrc_t sv = __builtin_amdgcn_make_buffer_rsrc((void*)v, (short)0, SEQ * DA * 2, 0x00020000);
  const __amdgpu_buffer_rsrc_t sw = __builtin_amdgcn_make_buffer_rsrc((void*)(shift_k + cg * 256), (short)0, 512, 0x00020000);
  const int vo = ((tb * 128 + (tid >> 3) * 4) * DA + cg * 64 + (tid & 7) * 8) * 2;
#pragma unroll
  for (int q = 0; q < 7; ++q)
    asm volatile("buffer_load_dwordx4 %0, %1, %2, 0 offen" NTPS : "=v"(p.kk[q]) : "v"(vo + (q - 3) * DA * 2), "s"(sk) : "memory");
#pragma unroll
  for (int q = 0; q < 4; ++q)
    asm volatile("buffer_load_dwordx4 %0, %1, %2, 0 offen offset:%3" NTVS : "=v"(p.vv[q]) : "v"(vo), "s"(sv), "i"(q * DA * 2) : "memory");
#pragma unroll
  for (int q = 0; q < 4; ++q)
    asm volatile("buffer_load_dwordx4 a[%c0:%c1], %2, %3, 0 offen offset:%4" ::"i"(4 * q), "i"(4 * q + 3), "v"((tid & 7) * 64), "s"(sw), "i"(q * 16) : "memory");
}
__device__ __forceinline__ float bfl(unsigned u) { return __uint_as_float(u << 16); }
__device__ __forceinline__ float bfh(unsigned u) { return __uint_as_float(u & 0xffff0000u); }
__device__ __forceinline__ f32x2c rbf2(f32x2c a) {
  return (f32x2c){__uint_as_float(pk2(0.f, a[0])), __uint_as_float(pk2(0.f, a[1]))};
}
template <int I> __device__ __forceinline__ void prep_math(const PR& p, int tid, char* lx) {
  const int rp = tid >> 3, c = tid & 7;
  f32x2c wt[4], kf[7];
  const unsigned w[4] = {__float_as_uint(acc_read1<4 * I>()), __float_as_uint(acc_read1<4 * I + 1>()), __float_as_uint(acc_read1<4 * I + 2>()),
                         __float_as_uint(acc_read1<4 * I + 3>())};
#pragma unroll
  for (int t = 0; t < 4; ++t)
    wt[t] = (t & 1) ? (f32x2c){bfh(w[t >> 1]), bfh(w[2 + (t >> 1)])} : (f32x2c){bfl(w[t >> 1]), bfl(w[2 + (t >> 1)])};
#pragma unroll
  for (int q = 0; q < 7; ++q) kf[q] = (f32x2c){bfl(p.kk[q][I]), bfh(p.kk[q][I])};
  f32x2c x[4];
#pragma unroll
  for (int e = 0; e < 4; ++e) {
    f32x2c s = rbf2(kf[e] * wt[3]);
    s = rbf2(s + rbf2(kf[e + 1] * wt[2]));
    s = rbf2(s + rbf2(kf[e + 2] * wt[1]));
    s = rbf2(s + rbf2(kf[e + 3] * wt[0]));
    x[e] = s * (f32x2c){bfl(p.vv[e][I]), bfh(p.vv[e][I])};
  }
#pragma unroll
  for (int u = 0; u < 2; ++u)
    *(u32x2*)(lx + (c * 8 + 2 * I + u) * 256 + (((31 - rp) * 8) ^ (c << 4))) = (u32x2){pk2(x[3][u], x[2][u]), pk2(x[1][u], x[0][u])};
}
__device__ __forceinline__ void prep_store(int tid, int cg, int tb, bf16* __restrict__ xr, const char* lx) {
  const __amdgpu_buffer_rsrc_t sx = __builtin_amdgcn_make_buffer_rsrc((void*)(xr + (size_t)cg * 64 * SEQ), (short)0, 64 * SEQ * 2, 0x00020000);
#pragma unroll
  for (int rd = 0; rd < 4; ++rd) {
    const int idx = tid + 256 * rd, ch = idx >> 4, q = idx & 15;
    u32x4c w = *(const u32x4c*)(lx + ch * 256 + 16 * (q ^ (ch >> 3)));
    __builtin_amdgcn_raw_buffer_store_b128(w, sx, (ch * SEQ + (15 - tb) * 128 + q * 8) * 2, 0, 16);
  }
}

#define CODE_TOUCH(LB, LE, wx, NW, NL, m0v) do { unsigned blo_, bhi_, elo_; \
    asm volatile("s_getpc_b64 s[48:49]\n\ts_add_u32 s48, s48, " LB "@rel32@lo+4\n\ts_addc_u32 s49, s49, " LB "@rel32@hi+12\n\t" \
                 "s_mov_b32 %0, s48\n\ts_mov_b32 %1, s49\n\t" \
                 "s_getpc_b64 s[48:49]\n\ts_add_u32 s48, s48, " LE "@rel32@lo+4\n\ts_mov_b32 %2, s48" \
                 : "=s"(blo_), "=s"(bhi_), "=s"(elo_) :: "scc"); \
    const unsigned n_ = elo_ - (blo_ & ~127u); \
    const __amdgpu_buffer_rsrc_t srd_ = __builtin_amdgcn_make_buffer_rsrc((void*)(((unsigned long long)bhi_ << 32) | (blo_ & ~127u)), (short)0, n_ < 262144u ? n_ : 0u, 0x00020000); \
    const unsigned l_ = threadIdx.x & 63, vo_ = l_ < (NL) ? ((wx) + l_ * (NW)) * 128u : 0x80000000u; \
    asm volatile("s_mov_b32 m0, %0\n\ts_nop 4\n\tbuffer_load_dword %1, %2, 0 offen lds" ::"s"(m0v), "v"(vo_), "s"(srd_) : "memory"); } while (0)
template <int I> __device__ __forceinline__ void d_store() {
  asm volatile("buffer_store_dwordx4 v[%c0:%c1], v%c2, s[88:91], s101 offen offset:%3 sc1" ::"i"(128 + 4 * I), "i"(131 + 4 * I), "i"(112 + ((I >> 2) & 1)),
               "i"((I & 3) * 1024) : "memory");
  if constexpr ((I & 7) == 7) asm volatile("s_addk_i32 s101, 0x4000" ::: "scc");
}
constexpr int NS_X = 19;
struct CV { unsigned pa[4], pb, px, vz, sz; float D; };
constexpr int cv_J(bool up, int q) { int J = up ? 0 : 7; while (q > J) { q -= J + 1; J += up ? 1 : -1; } return J; }
constexpr int cv_i(bool up, int q) { int J = up ? 0 : 7; while (q > J) { q -= J + 1; J += up ? 1 : -1; } return q; }
template <int R, int N> __device__ __forceinline__ void cv_rd(CV& c) {
  if constexpr (N < 16) {
    constexpr int i = N >> 1, o = 16 * R + 128 * (i & 1) + 2 * (N & 1), r = 32 + 32 * (R & 1) + 2 * N;
    asm volatile("ds_read2_b32 a[%c1:%c2], %0 offset0:%c3 offset1:%c4" ::"v"(c.pa[i >> 1]), "i"(r), "i"(r + 1), "i"(o), "i"(o + 1));
  } else {
    constexpr int m = N - 16, r = 192 + 32 * (R & 1) + 4 * m;
    asm volatile("ds_read_b128 a[%c1:%c2], %0 offset:%c3" ::"v"(c.pb), "i"(r), "i"(r + 3), "i"(64 * R + 512 * (7 - m)));
  }
}
template <int O> __device__ __forceinline__ void cv_xd(CV& c) {
  constexpr int e = O & 31, x = 96 + ((e >> 1) ^ 1);
  if constexpr (O >= 64) asm volatile("v_cvt_pk_bf16_f32 v%c0, 0, v%c0" ::"i"(160 + e));
  else if constexpr (O >= 32) asm volatile("v_mul_f32 v%c0, v%c0, %1" ::"i"(160 + e), "v"(c.D));
  else if constexpr (O & 1) asm volatile("v_lshlrev_b32 v%c0, 16, v%c1" ::"i"(160 + e), "i"(x));
  else asm volatile("v_and_b32 v%c0, 0xffff0000, v%c1" ::"i"(160 + e), "i"(x));
}
template <int O> __device__ __forceinline__ void cv_z(CV& c) {
  constexpr int J = O / 11, k = O % 11, y = 128 + 4 * J;
  if constexpr (k < 4) asm volatile("v_cvt_pk_bf16_f32 v%c0, 0, v%c0" ::"i"(y + k));
  else if constexpr (k < 8) asm volatile("v_add_f32 v%c0, v%c0, v%c1" ::"i"(y + k - 4), "i"(y + 28 + k));
  else if constexpr (k < 10) asm volatile("v_cvt_pk_bf16_f32 v%c0, v%c1, v%c2" ::"i"(y + k - 8), "i"(y + 2 * k - 16), "i"(y + 2 * k - 15));
  else asm volatile("buffer_store_dwordx2 v[%c0:%c1], %2, s[88:91], %3 offen offset:%4 sc1" ::"i"(y), "i"(y + 1), "v"(c.vz), "s"(c.sz),
                    "i"(512 * J) : "memory");
}
constexpr int cv_z0(int Q) {
  int o = 0;
  for (int q = 0; q < Q; ++q)
    for (int k = 0; k < (q % 4 == 1 ? 0 : 3); ++k) { const int J = o / 11; if ((J + 1) * (J + 2) / 2 - 1 + (J ? 4 : 7) <= q) ++o; }
  return o;
}
constexpr int cv_xd0(int T) { int o = 0; for (int t = 0; t < T; ++t) o += t % 4 != 1 || (t >= 72 && t < 108); return o < 96 ? o : 96; }
static_assert(cv_xd0(144) == 96);
template <int B, int... Is> __device__ __forceinline__ void cv_zs(CV& c, iseq<Is...>) { (cv_z<B + Is>(c), ...); }
template <int R, int Q> __device__ __forceinline__ void cv_slot(CV& c) {
  constexpr int i = cv_i(R == 7, Q), J = cv_J(R == 7, Q), d = 128 + 4 * J, a = 32 + 32 * (R & 1) + 4 * i, b = 192 + 32 * (R & 1) + 4 * (J - i);
  constexpr int T = 36 * R + Q, vm = Q % 4 == 1 && Q < 32 ? Q / 4 : -1;
  if constexpr (Q == 0) asm volatile("s_waitcnt lgkmcnt(0)");
  if constexpr (R >= 6 && vm >= 0 && vm % 4 == 0) asm volatile("s_add_u32 m0, s79, %0" ::"i"(((R - 6) ^ (NYT & 1)) * 32768 + vm * 1024));
  if constexpr (R == 0 && i == 0)
    asm volatile("v_mfma_f32_16x16x32_bf16 v[%c0:%c1], a[%c2:%c3], a[%c4:%c5], 0" ::"i"(d), "i"(d + 3), "i"(a), "i"(a + 3), "i"(b), "i"(b + 3));
  else
    asm volatile("v_mfma_f32_16x16x32_bf16 v[%c0:%c1], a[%c2:%c3], a[%c4:%c5], v[%c0:%c1]" ::"i"(d), "i"(d + 3), "i"(a), "i"(a + 3), "i"(b), "i"(b + 3));
  if constexpr (Q < 24 && R < 7) cv_rd<R + 1, Q>(c);
  if constexpr (T % 7 == 3 && NS_X + 5 + T / 7 < 32) d_store<NS_X + 5 + T / 7>();
  if constexpr (R == 2 && Q >= 24 && Q < 32)
    asm volatile("ds_read_b64 v[%c1:%c2], %0 offset:%c3" ::"v"(c.px), "i"(96 + 2 * (Q - 24)), "i"(97 + 2 * (Q - 24)), "i"(512 * (31 - Q)));
  if constexpr (R >= 3 && R < 7) if constexpr (cv_xd0(T - 107) > cv_xd0(T - 108)) cv_xd<cv_xd0(T - 108)>(c);
  if constexpr ((R == 3 || R == 4) && vm >= 0) {
    constexpr int f = (R - 3) * 8 + vm;
    asm volatile("buffer_load_dwordx4 a[%c0:%c1], v84, s[88:91], s96 offen offset:%2 sc1" ::"i"(128 + 4 * f), "i"(131 + 4 * f), "i"((f & 3) * 1024) : "memory");
    if constexpr ((f & 3) == 3) asm volatile("s_addk_i32 s96, %0" ::"i"(f == 7 ? 28672 : 4096));
  }
  if constexpr (R == 4 && Q == 35) asm volatile(FL_POLL ::: "memory");
  if constexpr (R >= 6 && vm >= 0)
    asm volatile("buffer_load_dwordx4 v%c0, s[88:91], s98 offen offset:%1 sc1 lds" ::"i"(86 + vm / 4), "i"((vm & 3) * 1024) : "memory");
  if constexpr (R == 6 && Q == 35) asm volatile("s_add_u32 s98, s98, 0x8000");
  if constexpr (R == 7) cv_zs<cv_z0(Q)>(c, make_iseq<cv_z0(Q + 1) - cv_z0(Q)>{});
  __builtin_amdgcn_sched_barrier(0);
}
template <int R, int... Q> __device__ __forceinline__ void cv_step(CV& c, iseq<Q...>) { (cv_slot<R, Q>(c), ...); }
template <int... N> __device__ __forceinline__ void cv_rd0(CV& c, iseq<N...>) { (cv_rd<0, N>(c), ...); }
template <int FLAG> __device__ __forceinline__ void conv_wave(unsigned sz, float Dd, char* lw) {
  int lane = threadIdx.x & 63; asm volatile("" : "+v"(lane));
  const int l16 = lane & 15, fq = lane >> 4;
  u32x4c w[5]; unsigned w4[5];
#pragma unroll
  for (int r = 0; r < 5; ++r) {
    w[r] = *(const u32x4c*)(lw + r * 1024 + lane * 16);
    w4[r] = *(const unsigned*)(lw + r * 1024 + lane * 16 + 16);
  }
  d_store<NS_X>(); d_store<NS_X + 1>();
#pragma unroll
  for (int r = 0; r < 5; ++r)
    *(u32x4c*)(lw + 10304 + r * 1024 + lane * 16) = (u32x4c){__builtin_amdgcn_alignbit(w[r][1], w[r][0], 16), __builtin_amdgcn_alignbit(w[r][2], w[r][1], 16),
                                                             __builtin_amdgcn_alignbit(w[r][3], w[r][2], 16), __builtin_amdgcn_alignbit(w4[r], w[r][3], 16)};
  d_store<NS_X + 2>();
  CV c;
#pragma unroll
  for (int i = 0; i < 4; ++i) c.pa[i] = (unsigned)(size_t)lw + ((l16 + 1) & 1) * 10304 + 4 * ((8 * fq + l16 + 1) >> 1) + 1024 * i;
  c.pb = (unsigned)(size_t)lw + 5120 + (2032 - 16 * l16 + 8 * fq) * 2 - 512 * 7;
  c.vz = (16 * l16 + 4 * fq) * 2;
  c.px = (unsigned)(size_t)lw + 5120 + 2044 * 2 - 512 * 7 - c.vz;
  c.sz = sz; c.D = Dd;
  asm volatile("" : "+v"(c.vz), "+v"(c.px), "+v"(c.D) :: "memory");
  asm volatile("" : "+s"(c.sz));
  cv_rd0(c, make_iseq<24>{});
  d_store<NS_X + 3>(); d_store<NS_X + 4>();
  __builtin_amdgcn_sched_barrier(0);
  static_for([&](auto R) __attribute__((always_inline)) {
    cv_step<R>(c, make_iseq<36>{});
    if constexpr (R == 2) {
      asm volatile("s_waitcnt vmcnt(0)\n\ts_barrier" ::: "memory");
      fl_set<FLAG>();
    }
    if constexpr (R == 5) {
      asm volatile("s_waitcnt vmcnt(0)" ::: "memory");
      fl_check();
    }
    __builtin_amdgcn_sched_barrier(0);
  }, make_iseq<8>{});
  asm volatile("s_nop 7");
  cv_zs<cv_z0(36)>(c, make_iseq<88 - cv_z0(36)>{});
}

__device__ __forceinline__ void post_dma(int cg, int tb, const bf16* __restrict__ q, unsigned m0w) {
  int lane = threadIdx.x & 63; asm volatile("" : "+v"(lane));
  const __amdgpu_buffer_rsrc_t sq = __builtin_amdgcn_make_buffer_rsrc((void*)(q + (size_t)tb * 32 * DA + cg * 64), (short)0, 32 * DA * 2, 0x00020000);
  const unsigned vq = ((lane >> 3) * DA + (lane & 7) * 8) * 2;
  asm volatile("s_mov_b32 m0, %0\n\ts_nop 4\n\tbuffer_load_dwordx4 %1, %2, 0 offen" NTQS " lds\n\tbuffer_load_dwordx4 %1, %2, %3 offen offset:1024" NTQS " lds\n\t"
               "buffer_load_dwordx4 %1, %2, %4 offen offset:2048" NTQS " lds\n\tbuffer_load_dwordx4 %1, %2, %5 offen offset:3072" NTQS " lds"
               ::"s"(m0w + 4096), "v"(vq), "s"(sq), "s"(8 * DA * 2 - 1024), "s"(16 * DA * 2 - 2048), "s"(24 * DA * 2 - 3072) : "memory");
}
__device__ __forceinline__ void post_math(int cg, int tb, bf16* __restrict__ out, const char* lw) {
  int lane = threadIdx.x & 63; asm volatile("" : "+v"(lane));
  const int tl = lane >> 3, ck = lane & 7;
  const __amdgpu_buffer_rsrc_t so = __builtin_amdgcn_make_buffer_rsrc((void*)(out + (size_t)tb * 32 * OUTW + cg * 64), (short)0, 32 * OUTW * 2 - 2 * DA, 0x00020000);
  const unsigned vo = (tl * OUTW + ck * 8) * 2;
#pragma unroll
  for (int i = 0; i < 4; ++i) {
    const u32x4c qv = *(const u32x4c*)(lw + 4096 + i * 1024 + lane * 16);
    const char* lz = lw + ck * 64 + 16 * (i ^ (ck >> 1)) + tl * 2;
    u32x4c o;
#pragma unroll
    for (int e = 0; e < 4; ++e) {
      const float z0 = bf2f(*(const unsigned short*)(lz + 2 * e * 512));
      const float z1 = bf2f(*(const unsigned short*)(lz + (2 * e + 1) * 512));
      o[e] = pk2(bfl(qv[e]) * z0, bfh(qv[e]) * z1);
    }
    __builtin_amdgcn_raw_buffer_store_b128(o, so, vo + i * 8 * OUTW * 2, 0, OUTSC);
  }
}

#define DI __device__ __forceinline__
#define GAP() __builtin_amdgcn_sched_barrier(0)
typedef __attribute__((ext_vector_type(4))) float f32x4g;
typedef __attribute__((ext_vector_type(4))) unsigned u32x4g;
constexpr int LDW = NH * HD;
struct GM { unsigned aA[2], aB[2][8]; };
template <bool W, int J, int V, bool NT = false> DI void g_dma() {
  if constexpr (NT && J == 0) asm volatile("buffer_load_dwordx4 v%c0, s[60:63], 0 offen nt lds" ::"i"(V) : "memory");
  else if constexpr (NT) asm volatile("buffer_load_dwordx4 v%c0, s[60:63], s%c1 offen nt lds" ::"i"(V), "i"(70 + J) : "memory");
  else
  if constexpr (J == 0) asm volatile("buffer_load_dwordx4 v%c0, s[%c1:%c2], 0 offen lds" ::"i"(V), "i"(W ? 60 : 56), "i"(W ? 63 : 59) : "memory");
  else asm volatile("buffer_load_dwordx4 v%c0, s[%c1:%c2], s%c3 offen lds" ::"i"(V), "i"(W ? 60 : 56), "i"(W ? 63 : 59), "i"((W ? 70 : 63) + J) : "memory");
}
template <int P, int KS, int I> DI void g_wread(GM& g) {
  constexpr int nf = I >> 1, hf = I & 1, r = 192 - 64 * P + 8 * nf + 4 * KS + 2 * hf;
  asm volatile("ds_read_b64_tr_b16 a[%c1:%c2], %0 offset:%c3" ::"v"(g.aB[hf][nf]), "i"(r), "i"(r + 1), "i"(P * 32768 + KS * 16384));
}
template <int P, int M, int KS> DI void g_aread(GM& g) {
  constexpr int r = 96 + 8 * (M & 1) + 4 * KS;
  asm volatile("ds_read_b128 v[%c1:%c2], %0 offset:%c3" ::"v"(g.aA[KS]), "i"(r), "i"(r + 3), "i"(P * 32768 + M * 2048));
}
constexpr float CS = 0.0625f * 1.4426950408889634f;
constexpr int NS_Q = 19;
constexpr int e_vm(int G) { return (G * 3) % 8 < 3 ? G * 3 / 8 : -1; }
constexpr int e_st(int MODE, int G) {
  const int q = G & 15, k3 = ((G >> 4) - 1) * 3 + (q == 0 ? 0 : q == 4 ? 1 : 2), k2 = NS_Q + (G >> 4) * 2 + (q == 4);
  if (MODE == 5) return e_vm(G) >= 32 ? e_vm(G) - 32 : -1;
  if (MODE == 3) return G >= 33 && (G - 33) % 5 == 0 && (G - 33) / 5 < NS_X ? (G - 33) / 5 : -1;
  if (MODE == 2) return G >= 30 && (q == 0 || q == 4 || q == 14) && k3 - 2 < NS_Q ? k3 - 2 : -1;
  if (MODE == 4) return (q == 0 || q == 4) && k2 < 32 ? k2 : -1;
  return -1;
}
constexpr int e_nst(int MODE, int G0, int G1) { int n = 0; for (int G = G0; G < G1; ++G) n += e_st(MODE, G) >= 0; return n; }
static_assert(e_nst(2, 0, 128) == NS_Q && e_nst(3, 30, 128) == NS_X && e_nst(5, 0, 128) == 16 && e_nst(4, 0, 104) == 32 - NS_Q);
constexpr int e_slot(int MODE, int G) { return MODE == 3 ? (G % 3 == 0 && G < 30 ? G / 3 : -1) : e_vm(G) >= 0 && e_vm(G) < 32 ? 10 + e_vm(G) : -1; }
struct ES { int op0[129], nop[129]; };
constexpr ES e_sched(bool TR, int MODE) {
  ES z{};
  const bool SCL = MODE == 2 || MODE == 4;
  const int per = SCL ? 16 : 8, o1 = 32 * per;
  int o = MODE == 4 ? e_sched(false, 2).op0[128] : 0;
  for (int G = 0; G <= 128; ++G) {
    const int q = G & 15;
    int cap = 2 - (q == 1 || q == 2 || q == 15);
    if (SCL) cap -= (q >= 3 && q < 11 && (q & 1)) + (q == 7 || q == 11) + 2 * (G == 70 || G == 74 || (G & 63) == 61);
    cap += MODE == 2 ? 4 + (q == 10 || q == 13) : MODE == 3 ? 1 : MODE == 5 ? 2 : 0;
    if (e_st(MODE, G) >= 0 || (SCL ? q == 8 || q == 12 : e_slot(MODE, G) >= 0) || (MODE == 4 && G < 4)) cap = 0;
    if (G == 128) cap = 10000;
    z.op0[G] = o;
    while (o < o1) {
      const int i = o / per, rdy = MODE == 4 ? 0 : (TR ? 32 * (i >> 3) + 16 + 2 * (i & 7) + 1 : 4 * i + 3) + 6;
      if ((G < rdy && G < 128) || cap-- <= 0) break;
      ++o; ++z.nop[G];
    }
  }
  return z;
}
template <bool TR, int MODE> constexpr ES ESCH = e_sched(TR, MODE);
static_assert(ESCH<false, 4>.op0[96] == 512 && ESCH<false, 4>.op0[0] >= 24 * 16);
template <bool TR, bool SCL, int O> DI void e_op() {
  constexpr int per = SCL ? 16 : 8, i = O / per, k = O % per, r = 128 + 4 * i, t = 114 + 4 * (i & 1), c = k - (per - 4);
  if constexpr (k < 4) asm volatile("v_accvgpr_read_b32 v%c0, a[%c1]" ::"i"(t + k), "i"(4 * i + k));
  else if constexpr (c < 0) asm volatile("v_mul_f32 v%c0, s83, v%c0" ::"i"(k < 8 ? r + (k ^ 6) : t + k - 8));
  else {
    constexpr int d = c == 0 ? r + 3 : c == 1 ? r + 2 : r + c - 2, x = c == 0 ? r + 2 : c == 1 ? r : t + 2 * (c - 2);
    asm volatile("v_cvt_pk_bf16_f32 v%c0, v%c1, v%c2" ::"i"(d), "i"(x), "i"(x + 1));
  }
}
template <bool TR, bool SCL, int B, int... Is> DI void e_ops(iseq<Is...>) { (e_op<TR, SCL, B + Is>(), ...); }
template <int S> DI void e_m0() {
  if constexpr (S == 0 || S == 1 || S == 5 || S == 9) asm volatile("s_add_u32 m0, s78, %0" ::"i"(S * 1024));
  if constexpr (S >= 26 && (S - 26) % 4 == 0) asm volatile("s_add_u32 m0, s79, %0" ::"i"(((S - 26) >> 3) * 32768 + ((S - 26) & 4) * 1024));
}
template <int S> DI void e_load() {
  if constexpr (S == 0) asm volatile("buffer_load_dwordx4 v122, s[56:59], 0 offen lds" ::: "memory");
  else if constexpr (S < 5) asm volatile("buffer_load_dwordx4 v84, s[56:59], s64 offen offset:%0 lds" ::"i"((S - 1) * 1024) : "memory");
  else if constexpr (S < 9) asm volatile("buffer_load_dwordx4 v84, s[60:63], 0 offen offset:%0 sc1 lds" ::"i"((S - 5) * 1024) : "memory");
  else if constexpr (S == 9) asm volatile("buffer_load_dwordx4 v84, s[60:63], s62 offen sc1 lds" ::: "memory");
  else if constexpr (S < 26) {
    constexpr int f = S - 10;
    asm volatile("buffer_load_dwordx4 a[%c0:%c1], v84, s[88:91], s96 offen offset:%2 sc1" ::"i"(128 + 4 * f), "i"(131 + 4 * f), "i"((f & 3) * 1024) : "memory");
    if constexpr ((f & 3) == 3) asm volatile("s_addk_i32 s96, %0" ::"i"(f == 7 ? 28672 : 4096));
  } else {
    constexpr int f = S - 26;
    asm volatile("buffer_load_dwordx4 v%c0, s[88:91], s98 offen offset:%1 sc1 lds" ::"i"(86 + ((f >> 2) & 1)), "i"((f & 3) * 1024) : "memory");
    if constexpr (f == 7) asm volatile("s_add_u32 s98, s98, s97");
    if constexpr (f == 15) asm volatile(FL_POLL ::: "memory");
  }
}
template <bool TR, int P, int H, int G, int MODE, bool NT> DI void g_gap(GM& g) {
  constexpr int m = 4 * H + (G >> 4), q = G & 15, n = q >> 1, ks = q & 1, GG = 64 * H + G;
  constexpr bool inV = TR ? m & 1 : n & 1, END = MODE == 3 || MODE == 5;
  constexpr int a = 96 + 8 * (m & 1) + 4 * ks, b = 192 - 64 * P + 8 * n + 4 * ks, d = (inV ? 128 : 0) + 4 * (TR ? 8 * (m >> 1) + n : 4 * m + (n >> 1));
  constexpr int J = END ? -1 : q == 8 ? 2 * (G >> 4) : q == 12 ? 2 * (G >> 4) + 1 : -1, gj = (J & 3) ^ ((J & 3) >> 1);
  if constexpr (END) { if constexpr (q == 0 && GG > 0) asm volatile("s_waitcnt lgkmcnt(0)"); e_m0<e_slot(MODE, GG)>(); }
  else if constexpr (q == 0 && G > 0) asm volatile("s_waitcnt lgkmcnt(4)");
  if constexpr (J == 0) asm volatile("s_add_u32 m0, s78, %0" ::"i"((P ^ H ^ 1) * 32768 + (1 - H) * 8192));
  else if constexpr (J == 4) asm volatile("s_add_u32 m0, s79, %0" ::"i"((P ^ H) * 32768 + (1 - H) * 16384));
  else if constexpr (J > 0 && (J & 3) == 1) asm volatile("s_bitset1_b32 m0, 12");
  else if constexpr (J > 0 && (J & 3) == 2) asm volatile("s_bitset1_b32 m0, %0" ::"i"(J < 4 ? 14 : 13));
  else if constexpr (J > 0) asm volatile("s_bitset0_b32 m0, 12");
#define GMF(D, X, Y, C) asm volatile("v_mfma_f32_16x16x32_bf16 " D "[%c0:%c1], " X ", " Y ", " C ::"i"(d), "i"(d + 3), "i"(a), "i"(a + 3), "i"(b), "i"(b + 3))
#define GMF2(D, C) do { if constexpr (TR) GMF(D, "v[%c2:%c3]", "a[%c4:%c5]", C); else GMF(D, "a[%c4:%c5]", "v[%c2:%c3]", C); } while (0)
  if constexpr ((MODE == 1 || MODE == 4) && ks == 0) { if constexpr (inV) GMF2("v", "0"); else GMF2("a", "0"); }
  else if constexpr (inV) GMF2("v", "v[%c0:%c1]");
  else GMF2("a", "a[%c0:%c1]");
#undef GMF2
#undef GMF
  if constexpr ((q == 1 || q == 2) && !(END && m == 7)) g_aread<(m < 7 ? P : P ^ 1), (m + 1) & 7, q - 1>(g);
  if constexpr (!END && q >= 3 && q < 11 && (q & 1)) g_wread<P ^ 1, H, (G >> 4) * 4 + (q - 3) / 2>(g);
  if constexpr (e_st(MODE, GG) >= 0) {
    static_assert(ESCH<TR, MODE>.op0[GG] >= (e_st(MODE, GG) + 1) * (END ? 8 : 16));
    d_store<e_st(MODE, GG)>();
  }
  if constexpr (J >= 0 && J < 4) g_dma<false, (1 - H) * 2 + (gj & 1) + (gj >> 1) * 4, 90>();
  else if constexpr (J >= 4) g_dma<true, (1 - H) * 4 + gj, 91, NT>();
  if constexpr (MODE >= 2) e_ops<TR, !END, ESCH<TR, MODE>.op0[GG]>(make_iseq<ESCH<TR, MODE>.nop[GG]>{});
  if constexpr (END) {
    if constexpr (e_slot(MODE, GG) >= 0) e_load<e_slot(MODE, GG)>();
  } else {
    if constexpr (H == 1 && G == 6) asm volatile("v_lshl_add_u32 v90, s82, 7, v92\n\ts_and_b32 s82, s80, 15");
    if constexpr (H == 1 && G == 10) asm volatile("s_lshr_b64 s[80:81], s[80:81], 4\n\tv_lshl_add_u32 v91, s82, 19, v93");
    if constexpr (G == 61) {
      constexpr int nst = e_nst(MODE, H ? 29 : 0, GG + 1) + (MODE == 4 && !H ? e_nst(2, 93, 128) : 0);
      asm volatile("s_waitcnt vmcnt(%0) lgkmcnt(0)\n\ts_barrier" ::"i"(12 + nst) : "memory");
    }
  }
  if constexpr (END && GG == 127) {
    asm volatile("s_nop 7");
    e_ops<TR, false, ESCH<TR, MODE>.op0[128]>(make_iseq<ESCH<TR, MODE>.nop[128]>{});
  }
  GAP();
}
template <bool TR, int P, int H, int MODE, bool NT, int... Gs> DI void g_half_(GM& g, iseq<Gs...>) { (g_gap<TR, P, H, Gs, MODE, NT>(g), ...); }
template <bool TR, int P, int H, int MODE, bool NT = false> DI void g_half(GM& g) { g_half_<TR, P, H, MODE, NT>(g, make_iseq<64>{}); }
template <bool TR, int P, int MODE, bool NT = false> DI void g_tile(GM& g) { g_half<TR, P, 0, MODE, NT>(g); g_half<TR, P, 1, MODE, NT>(g); }
template <int P, int... Is> DI void g_read0(GM& g, iseq<Is...>) {
  (g_wread<P, 0, Is>(g), ...); (g_wread<P, 1, Is>(g), ...);
  g_aread<P, 0, 0>(g); g_aread<P, 0, 1>(g);
}
DI void g_proW(unsigned g, unsigned mW) {
  asm volatile("v_lshl_add_u32 v91, %0, 18, v93\n\ts_mov_b32 m0, %1" ::"s"(g), "s"(mW));
  asm volatile("s_nop 0"); g_dma<true, 0, 91>();
  asm volatile("s_bitset1_b32 m0, 12\n\ts_nop 0"); g_dma<true, 1, 91>();
  asm volatile("s_bitset1_b32 m0, 13\n\ts_nop 0"); g_dma<true, 3, 91>();
  asm volatile("s_bitset0_b32 m0, 12\n\ts_nop 0"); g_dma<true, 2, 91>();
}
template <int PART = 3> DI void g_pro(unsigned g, unsigned mA, unsigned mW, unsigned lda) {
  g &= 3;
  if constexpr (PART & 1)
    asm volatile("v_add_u32 v90, %0, v92\n\ts_mov_b32 m0, %1" ::"s"((g >> 1) * 128 + (g & 1) * 128 * lda), "s"((mA ^ (g >> 1) * 32768) + (g & 1) * 8192));
  if constexpr (PART == 1) return;
  asm volatile("s_nop 0"); g_dma<false, 0, 90>();
  asm volatile("s_bitset1_b32 m0, 12\n\ts_nop 0"); g_dma<false, 1, 90>();
  asm volatile("s_bitset1_b32 m0, 14\n\ts_nop 0"); g_dma<false, 5, 90>();
  asm volatile("s_bitset0_b32 m0, 12\n\ts_nop 0"); g_dma<false, 4, 90>();
  g_proW(g, mW ^ g * 16384);
}
template <bool TR, bool SCL, class HOOK> DI void g_loop(GM& g, unsigned evm, HOOK&& hook) {
  constexpr int nkt = SCL ? 7 : 16, P0 = SCL ? 0 : 1;
  asm volatile("" : "+s"(evm));
#pragma unroll 1
  for (int t = 1;; t += 2) {
    g_half<TR, P0 ^ 1, 0, 0, (SCL ? NTW >= 1 : NTW >= 2)>(g);
    if (__builtin_expect((evm >> t) & 1, 0)) { hook(t); GAP(); }
    g_half<TR, P0 ^ 1, 1, 0, (SCL ? NTW >= 1 : NTW >= 2)>(g);
    if (SCL && t + 2 >= nkt) break;
    g_half<TR, P0, 0, 0, (SCL ? NTW >= 1 : NTW >= 2)>(g);
    if constexpr (SCL) { if (__builtin_expect(t == 3, 0)) { hook(4); GAP(); } }
    else asm volatile("s_cmp_eq_u32 %0, 13\n\ts_cselect_b32 s78, 0x28000, s78" ::"s"(t) : "scc");
    g_half<TR, P0, 1, 0, (SCL ? NTW >= 1 : NTW >= 2)>(g);
    if (!SCL && t + 3 >= nkt) break;
  }
}

typedef __attribute__((ext_vector_type(4))) unsigned u32x4;
typedef __attribute__((ext_vector_type(4))) float f32x4;

constexpr int R_S = 128, R_P = 192, R_CI = 224, R_E = 96;
constexpr int R_TS = 112, R_RS = 114, R_M = 116;
constexpr int R_PD = 118, R_THR = 120, R_TMP = 121;
constexpr int r_dk(int q) { return q < 6 ? 90 + q : 120 + q; }
constexpr int S_V = 96, S_T = 97, S_K = 98, S_I = 99, S_EV = 100;
struct AC {
  unsigned rare;
  unsigned tb, vs, o0, o1;
};
DI bool redo() { u64 t; asm volatile("s_mov_b64 %0, s[54:55]" : "=s"(t)); return t != 0; }
template <int X, int F> DI void kread() {
  constexpr int r = 192 + 4 * (F & 15);
  asm volatile("ds_read_b128 a[%c0:%c1], v84 offset:%c2" ::"i"(r), "i"(r + 3), "i"(X * 32768 + F * 1024));
}
template <int X, int F> DI void vread() {
  constexpr int r = 192 + 4 * (F & 15);
  asm volatile("ds_read_b128 a[%c0:%c1], v85 offset:%c2" ::"i"(r), "i"(r + 3), "i"(X * 32768 + (F & 1) * 16384 + (F >> 1) * 1024));
}
template <int X, int... Fs> DI void kreads(iseq<Fs...>) { (kread<X, Fs>(), ...); }
template <int X, int J> DI void dma_m0() {
  if constexpr (J == 0) asm volatile("s_mov_b32 m0, s%c0" ::"i"(78 + X));
  else if constexpr (J == 4) asm volatile("s_or_b32 m0, s%c0, 0x1000" ::"i"(78 + X));
  else if constexpr (J == 8) asm volatile("s_bitset0_b32 m0, 16");
  else if constexpr (J == 12) asm volatile("s_bitset0_b32 m0, 12");
}
template <int J> DI void dma_at() {
  if constexpr (J < 8)
    asm volatile("buffer_load_dwordx4 v%c0, s[88:91], s96 offen offset:%1 sc1 lds" ::"i"(86 + (J >= 4)), "i"((J & 3) * 1024) : "memory");
  else
    asm volatile("buffer_load_dwordx4 v%c0, s[92:95], s98 offen offset:%1 sc1 lds" ::"i"(r_dk(J - 8)), "i"((J & 3) * 1024) : "memory");
}
template <int Q> DI void dma4k() {
  asm volatile("s_add_u32 m0, s79, %0\n\ts_add_u32 s48, s98, %1\n\tbuffer_load_dwordx4 v%c2, s[88:91], s48 offen sc1 lds\n\t"
               "buffer_load_dwordx4 v%c2, s[88:91], s48 offen offset:1024 sc1 lds\n\tbuffer_load_dwordx4 v%c2, s[88:91], s48 offen offset:2048 sc1 lds\n\t"
               "buffer_load_dwordx4 v%c2, s[88:91], s48 offen offset:3072 sc1 lds"
               ::"i"((Q >> 1) * 32768 + (Q & 1) * 4096), "i"((Q >> 1) * 32768), "i"(86 + (Q & 1)) : "memory", "scc");
}
template <int Q> DI void acc_zero2() {
  asm volatile("v_accvgpr_write_b32 a[%c0], 0\n\tv_accvgpr_write_b32 a[%c1], 0" ::"i"(2 * Q), "i"(2 * Q + 1));
}
#define SMV 0
struct SMS { int ex[128], op0[128], nop[128], last; };
constexpr SMS sm_sched() {
  SMS z{};
  int et[32] = {}, e = 0;
  for (int t = 0; t < 128; ++t) {
    const int g = t & 15;
    const bool x = t < 48 ? (g >= 8 && (g & 1) && g < 15) : t < 64 ? (g >= 9 && g < 15) : (g >= 8 && g < 14);
    z.ex[t] = x && e < 32 ? e : -1;
    if (z.ex[t] >= 0) et[e++] = t;
  }
  int o = 0;
  for (int t = 0; t < 128; ++t) {
    const int g = t & 15;
    int cap = z.ex[t] >= 0 || t < 8 || (t < 48 && (g == 7 || (g >= 8 && !(g & 1)))) || t == 56 || t == 63 || t == 111 ? 0 : 1;
    z.op0[t] = o; z.nop[t] = 0;
    while (cap > 0 && o < 48 && et[2 * (o / 3) + (o % 3 == 0 ? 0 : 1)] + 2 <= t) {
      const int el = 2 * (o / 3) + o % 3;
      if (!(o % 3 < 2 && (el == 0 || el == 4))) --cap;
      ++o; ++z.nop[t]; z.last = t;
    }
  }
  for (int k = 16; k < 32; ++k) if (z.op0[et[k]] < 3 * ((k - 16) / 2 + 1)) z.last = 1000;
  return z;
}
constexpr SMS SM = sm_sched();
static_assert(SM.last <= 117);
constexpr int p_reg(int Y, int k) { return R_P + 16 * Y + 4 * ((k >> 3) * 2 + ((k >> 1) & 1)) + ((k >> 2) & 1) * 2 + (k & 1); }
template <int Y, int O> DI void sm_op() {
  constexpr int k = O / 3, e = 2 * k + O % 3, ts = R_TS + ((e >> 2) & 1);
  if constexpr (O % 3 == 2) asm volatile("v_cvt_pk_bf16_f32 v%c0, v%c1, v%c2" ::"i"(p_reg(Y, k)), "i"(R_E + ((2 * k) & 15)), "i"(R_E + ((2 * k + 1) & 15)));
  else if constexpr (e == 1 || e == 5) asm volatile("v_add_f32_e32 v%c0, v%c1, v%c2" ::"i"(ts), "i"(R_E + e - 1), "i"(R_E + e));
  else if constexpr (e != 0 && e != 4) asm volatile("v_add_f32_e32 v%c0, v%c0, v%c1" ::"i"(ts), "i"(R_E + (e & 15)));
}
template <int Y, int T> DI void sm_gap(AC& c) {
  if constexpr (SM.ex[T] >= 0) asm volatile("v_exp_f32 v%c0, v%c1" ::"i"(R_E + (SM.ex[T] & 15)), "i"(R_S + 32 * Y + SM.ex[T]));
  if constexpr (SM.nop[T] > 0) sm_op<Y, SM.op0[T]>();
  if constexpr (SM.nop[T] > 1) sm_op<Y, SM.op0[T] + 1>();
  if constexpr (SM.nop[T] > 2) sm_op<Y, SM.op0[T] + 2>();
  if constexpr (T == SM.last + 1) asm volatile("v_add_f32_e32 v%c0, v%c1, v%c2" ::"i"(R_TMP), "i"(R_TS), "i"(R_TS + 1));
  if constexpr (T == SM.last + 2) asm volatile("v_cmp_nle_f32_e64 s[54:55], v%c0, v%c1" ::"i"(R_TMP), "i"(R_THR));
}
#define W_A0 1
template <int X, int MODE, int G> DI void gapA(AC& c) {
  constexpr bool FIRST = MODE == 1 || MODE == 2, NOQK = MODE == 3;
  constexpr int Xp = X ^ 1, kb = G >> 4, g = G & 15, qb = g >> 3, s = g & 7;
  if constexpr (MODE == 3 && G < 48 && G % 3 == 0) {
    constexpr int f = G / 3;
    asm volatile("buffer_load_dwordx4 a[%c0:%c1], v84, s[88:91], s75 offen offset:%2 sc1" ::"i"(128 + 4 * f), "i"(131 + 4 * f), "i"((f & 3) * 1024) : "memory");
    if constexpr ((f & 3) == 3) asm volatile("s_addk_i32 s75, %0" ::"i"(f == 7 ? 28672 : 4096));
  }
  if constexpr (G == 48) asm volatile("s_waitcnt vmcnt(%0) lgkmcnt(0)\n\ts_barrier" ::"i"(MODE == 3 ? 16 : MODE == 2 ? 12 : MODE == 1 ? W_A0 : 0) : "memory");
  else if constexpr (g == 0 && !NOQK) asm volatile("s_waitcnt lgkmcnt(0)");
  if constexpr (!NOQK) {
    constexpr int r = 192 + 4 * ((kb & 1) * 8 + s), q = 128 + 4 * g;
    constexpr int d = R_S + 32 * X + 4 * (kb * 2 + qb), ci = s == 0 ? R_CI + 4 * (kb * 2 + qb) : d;
    if constexpr (FIRST && s == 0)
      asm volatile("v_mfma_f32_16x16x32_bf16 v[%c0:%c1], a[%c2:%c3], a[%c4:%c5], 0" ::"i"(d), "i"(d + 3), "i"(r), "i"(r + 3), "i"(q), "i"(q + 3));
    else
      asm volatile("v_mfma_f32_16x16x32_bf16 v[%c0:%c1], a[%c2:%c3], a[%c4:%c5], v[%c6:%c7]" ::"i"(d), "i"(d + 3), "i"(r), "i"(r + 3), "i"(q), "i"(q + 3),
                   "i"(ci), "i"(ci + 3));
    if constexpr (g < 8 && kb < 3) kread<X, (kb + 1) * 8 + g>();
    constexpr int S0 = SMV ? 40 : 14, S1 = SMV ? 41 : 30, S2 = SMV ? 42 : 45, S3 = SMV ? 43 : 46;
    if constexpr (G == S0) asm volatile("s_add_u32 s96, s97, 0x1000000");
    if constexpr (G == S1) dma_m0<X, 0>();
    if constexpr (G == S2) asm volatile("s_mov_b32 s97, s98");
    if constexpr (G == S3) asm volatile("s_add_u32 s98, s98, s99");
    if constexpr (G >= 56 && !(G & 1)) dma_at<(G - 56) / 2>();
    if constexpr (G == 57) asm volatile("s_lshr_b32 s100, s100, 1\n\ts_add_u32 s56, s56, 1");
    if constexpr (G == 59 && !FIRST) asm volatile("s_and_b32 s49, s100, 1\n\ts_or_b32 s49, s49, s54");
    if constexpr (G == 61 && !FIRST) asm volatile("s_or_b32 %0, s49, s55" : "=s"(c.rare));
    if constexpr (G == 61 && FIRST) asm volatile("s_and_b32 %0, s100, 1" : "=s"(c.rare));
  } else if constexpr (SM.nop[64 + G] > 0) asm volatile("s_nop 0");
  if constexpr (!FIRST && g < 8 && kb == 3) vread<Xp, g>();
  if constexpr (MODE == 2 && (G & 3) == 1) d_store<16 + G / 4>();
  if constexpr (FIRST) acc_zero2<G>();
  else sm_gap<Xp, 64 + G>(c);
  GAP();
}
template <int R> DI void rescale1(float alpha) {
  float t;
  asm volatile("v_accvgpr_read_b32 %0, a[%c2]\n\tv_mul_f32 %0, %0, %1\n\tv_accvgpr_write_b32 a[%c2], %0" : "=&v"(t) : "v"(alpha), "i"(R));
}
template <int... Rs> DI void rescale_all(const float (&alpha)[2], iseq<Rs...>) { (rescale1<Rs>(alpha[(Rs >> 2) & 1]), ...); }
template <bool MAX> DI float row_reduce(float v) {
  auto p = __builtin_amdgcn_permlane32_swap(__float_as_uint(v), __float_as_uint(v), false, false);
  v = MAX ? fmaxf(__uint_as_float(p[0]), __uint_as_float(p[1])) : __uint_as_float(p[0]) + __uint_as_float(p[1]);
  p = __builtin_amdgcn_permlane16_swap(__float_as_uint(v), __float_as_uint(v), false, false);
  return MAX ? fmaxf(__uint_as_float(p[0]), __uint_as_float(p[1])) : __uint_as_float(p[0]) + __uint_as_float(p[1]);
}
template <int Y, int QB> DI float row_max() {
  constexpr int r = R_S + 32 * Y + 4 * QB;
  float mx;
  asm volatile("v_max_f32 %0, v%c1, v%c2\n\tv_max3_f32 %0, %0, v%c3, v%c4" : "=&v"(mx) : "i"(r), "i"(r + 1), "i"(r + 2), "i"(r + 3));
  static_for([&](auto K) {
    constexpr int q = r + 8 * (K + 1);
    float t = mx;
    asm volatile("v_max3_f32 %0, %0, v%c1, v%c2\n\tv_max3_f32 %0, %0, v%c3, v%c4" : "+v"(t) : "i"(q), "i"(q + 1), "i"(q + 2), "i"(q + 3));
    mx = t;
  }, make_iseq<3>{});
  return row_reduce<true>(mx);
}
template <int R> DI void v_sub(float d) { asm volatile("v_sub_f32 v%c0, v%c0, %1" ::"i"(R), "v"(d)); }
template <int B> DI void s_shift(const float (&d)[2]) { static_for([&](auto E) { v_sub<B + E>(d[(E >> 2) & 1]); }, make_iseq<32>{}); }
template <bool CI> DI void m_shift(const float (&d)[2]) {
  if constexpr (CI) s_shift<R_CI>(d);
  v_sub<R_M>(-d[0]); v_sub<R_M + 1>(-d[1]);
}
DI void set_mask0() {
  const int mr = __float_as_int(vget<89>());
  static_for([&](auto I) {
    constexpr int e = (I >> 2) * 8 + (I & 3), k = (I >> 2) * 16 + (I & 3);
    const float x = vget<R_S + e>();
    vset<R_S + e>(k > mr ? -INFINITY : x);
  }, make_iseq<16>{});
}
template <int X, bool CI = true> DI void sm_redo() {
  constexpr int Xp = X ^ 1;
  float d[2], dt[2], alpha[2], ts[2] = {0.f, 0.f};
  static_for([&](auto Q) {
    constexpr int qb = Q;
    const float pd = vget<R_PD + qb>();
    d[qb] = fmaxf(row_max<Xp, qb>() - pd, 0.f);
    dt[qb] = d[qb] + pd;
    vset<R_PD + qb>(d[qb]);
    alpha[qb] = __builtin_amdgcn_exp2f(-d[qb]);
    vset<R_RS + qb>(vget<R_RS + qb>() * alpha[qb]);
  }, make_iseq<2>{});
  vset<R_THR>(d[0] > 0.f || d[1] > 0.f ? -1.f : 4096.f);
  m_shift<CI>(d);
  static_for([&](auto K) {
    constexpr int k = K, qb = (k >> 1) & 1;
    const float e0 = __builtin_amdgcn_exp2f(vget<R_S + 32 * Xp + 2 * k>() - dt[qb]), e1 = __builtin_amdgcn_exp2f(vget<R_S + 32 * Xp + 2 * k + 1>() - dt[qb]);
    ts[qb] += e0; ts[qb] += e1;
    vset<p_reg(Xp, k)>(__uint_as_float(pk2(e0, e1)));
  }, make_iseq<16>{});
  vset<R_TS>(ts[0]); vset<R_TS + 1>(ts[1]);
  rescale_all(alpha, make_iseq<128>{});
  asm volatile("s_nop 1" ::: "memory");
  GAP();
}
template <int X, int MODE, int... Gs> DI void phaseA_(AC& c, iseq<Gs...>) { (gapA<X, MODE, Gs>(c), ...); }
template <int X, int MODE> DI void phaseA(AC& c) { phaseA_<X, MODE>(c, make_iseq<64>{}); }

template <int X, bool SMX, int G> DI void gapB(AC& c) {
  constexpr int Xp = X ^ 1, blk = G >> 4, g = G & 15;
  if constexpr (g == 0) asm volatile("s_waitcnt lgkmcnt(0)");
  constexpr int qb = (g >> 1) & 1, s = g & 1, r = 192 + 4 * ((blk & 1) * 8 + (g >> 2) * 2 + s), o = 4 * ((blk * 4 + (g >> 2)) * 2 + qb);
  constexpr int p = R_P + 16 * Xp + 4 * (s * 2 + qb);
  asm volatile("v_mfma_f32_16x16x32_bf16 a[%c0:%c1], a[%c2:%c3], v[%c4:%c5], a[%c0:%c1]" ::"i"(o), "i"(o + 3), "i"(r), "i"(r + 3), "i"(p), "i"(p + 3));
  if constexpr (g < 8) {
    if constexpr (blk < 3) vread<Xp, (blk + 1) * 8 + g>();
    else if constexpr (SMX) kread<Xp, g>();
  }
  if constexpr (SMX) {
    if constexpr (G == 1 || G == 2) asm volatile("v_add_f32_e32 v%c0, v%c0, v%c1" ::"i"(R_RS + G - 1), "i"(R_TS + G - 1));
    if constexpr (g == 7 && blk < 3) dma_m0<X, blk * 4 + 4>();
    if constexpr (g >= 8 && !(g & 1) && blk < 3) dma_at<blk * 4 + 4 + (g - 8) / 2>();
    if constexpr (G == 55) asm volatile("s_cmp_eq_u32 s56, s76");
    if constexpr (G == 3) asm volatile("s_lshr_b32 s77, s77, 1");
    if constexpr (G == 4) asm volatile("s_bitcmp1_b32 s77, 0");
    if constexpr (G == 5) asm volatile("s_cselect_b64 s[52:53], s[62:63], 0");
    if constexpr (G == 6) asm volatile(FL_ARM("s[52:53]"));
    if constexpr (G == 56) asm volatile(FL_POLL ::: "memory");
    sm_gap<X, G>(c);
  }
  GAP();
}
template <int X, int... Gs> DI void phaseB_(AC& c, iseq<Gs...>) { (gapB<X, true, Gs>(c), ...); }
template <int X> DI void phaseB(AC& c) { phaseB_<X>(c, make_iseq<64>{}); }
constexpr int sm_nex() { int n = 0; for (int t = 0; t < 64; ++t) n += SM.ex[t] >= 0; return n; }
template <int X> DI void phaseB0(unsigned mode) {
  dma_m0<X, 4>();
  kreads<X ^ 1>(make_iseq<8>{});
  dma_at<4>();
  asm volatile("s_nop 3");
  float d[2];
  if (mode & 2) {
    d[0] = vget<122>(); d[1] = vget<123>();
    dma_at<5>();
  } else {
    if constexpr (X == 0) if (__builtin_expect(mode & 4, 0)) set_mask0();
    d[0] = row_max<X, 0>(); GAP(); dma_at<5>();
    d[1] = row_max<X, 1>();
#pragma unroll
    for (int qb = 0; qb < 2; ++qb) if (!(d[qb] > -3.0e38f)) d[qb] = 0.f;
  }
  GAP();
  vset<R_M>(d[0]); vset<R_M + 1>(d[1]);
  dma_at<6>();
  const f32x2c nd[2] = {{-d[0], -d[0]}, {-d[1], -d[1]}};
  static_for([&](auto E) {
    constexpr int e = E;
    const f32x2c v = nd[(e >> 1) & 1];
    asm volatile("v_pk_add_f32 v[%c0:%c1], v[%c0:%c1], %4\n\tv_mov_b64 v[%c2:%c3], %4" ::"i"(R_S + 32 * X + 2 * e), "i"(R_S + 32 * X + 2 * e + 1), "i"(R_CI + 2 * e), "i"(R_CI + 2 * e + 1), "v"(v));
    if constexpr (e == 3) dma_at<7>();
    if constexpr (e == 6) dma_m0<X, 8>();
    if constexpr (e == 7 || e == 11 || e == 15) dma_at<8 + (e - 7) / 4>();
  }, make_iseq<16>{});
  static_assert(SM.op0[64] >= 3 * ((sm_nex() - 15) / 2) && SM.op0[64] <= 24);
  static_for([&](auto E) {
    constexpr int e = E;
    if constexpr (e < sm_nex()) asm volatile("v_exp_f32 v%c0, v%c1" ::"i"(R_E + e), "i"(R_S + 32 * X + e));
    if constexpr (e == 3) dma_at<11>();
    if constexpr (e == 6) dma_m0<X, 12>();
    if constexpr (e == 7 || e == 11 || e == 15) dma_at<12 + (e - 7) / 4>();
  }, make_iseq<16>{});
  static_for([&](auto O) { sm_op<X, O>(); }, make_iseq<SM.op0[64]>{});
  dma_at<15>();
  static_for([&](auto E) { asm volatile("v_exp_f32 v%c0, v%c1" ::"i"(R_E + (int)E), "i"(R_S + 32 * X + 16 + (int)E)); }, make_iseq<(sm_nex() > 16 ? sm_nex() - 16 : 0)>{});
  asm volatile("buffer_store_dwordx2 v[%c0:%c1], v88, s[88:91], s76 offen sc1\n\ts_bitcmp1_b32 s77, 0\n\ts_cselect_b64 s[52:53], s[62:63], 0\n\t"
               FL_ARM("s[52:53]") "\n\t" FL_POLL ::"i"(R_M), "i"(R_M + 1) : "memory", "scc");
  GAP();
}
enum : int { T_Q0, T_K0, T_OQ, T_OG, T_FPREP, T_FQ, T_FG, T_FS, T_ZO, T_CNT, T_NPRE = 10, T_NST = 12, T_TAB = 14, T_SLAB = 16, T_PART, T_IT = 18, T_ITN = 23 };
#define MSK(R, K) "v_cmp_gt_i32 s[48:49], " #K ", v89\n\tv_cmp_gt_i32 s[50:51], " #K "+1, v89\n\tv_cmp_gt_i32 s[52:53], " #K "+2, v89\n\t" \
                  "v_cmp_gt_i32 s[54:55], " #K "+3, v89\n\tv_cndmask_b32_e64 v[" #R "], v[" #R "], v122, s[48:49]\n\t" \
                  "v_cndmask_b32_e64 v[" #R "+1], v[" #R "+1], v122, s[50:51]\n\tv_cndmask_b32_e64 v[" #R "+2], v[" #R "+2], v122, s[52:53]\n\t" \
                  "v_cndmask_b32_e64 v[" #R "+3], v[" #R "+3], v122, s[54:55]\n\t"
DI void ev_code() {
  asm volatile(
    "s_getpc_b64 s[82:83]\n\ts_add_u32 s82, s82, 12\n\ts_addc_u32 s83, s83, 0\n\ts_branch 9f\n\t"
    "s_bitcmp1_b32 s60, s56\n\ts_cbranch_scc1 3f\n\ts_bitcmp1_b32 s74, s56\n\ts_cbranch_scc1 2f\n\t"
    "s_bitcmp0_b32 s57, s56\n\ts_cbranch_scc1 1f\n\ts_bitcmp1_b32 s58, s56\n\ts_cselect_b64 vcc, -1, 0\n\tv_cndmask_b32 v121, -2.0, v81, vcc\n\t"
    "buffer_store_dwordx2 v[82:83], v121, s[88:91], s70 offen sc1\n\tbuffer_store_dwordx2 v[82:83], v121, s[88:91], s71 offen sc1\n\t"
    "v_cmp_eq_u64 vcc, s[84:85], v[124:125]\n\ts_andn2_b64 vcc, s[62:63], vcc\n\ts_cbranch_scc1 5f\n"
    "0:\n\ts_add_u32 s98, s98, s59\n\ts_mov_b32 s59, s61\n\ts_mov_b32 s61, s72\n\ts_mov_b64 s[62:63], s[64:65]\n\ts_mov_b64 s[64:65], s[66:67]\n\t"
    "s_mov_b64 s[66:67], s[68:69]\n"
    "1:\n\ts_setpc_b64 s[86:87]\n"
    "5:\n\t" FL_CHECK("s[62:63]") "\n\ts_branch 0b\n"
    "6:\n\t" FL_CHECK("s[62:63]") "\n\ts_setpc_b64 s[86:87]\n"
    "2:\n\ts_mov_b32 s98, 0\n\t"
    "v_lshrrev_b32 v90, 7, v86\n\tv_and_b32 v91, 6, v90\n\tv_lshlrev_b32 v91, 4, v91\n\tv_and_b32 v92, 0x70, v86\n\tv_xor_b32 v91, v91, v92\n\t"
    "s_movk_i32 s48, 896\n\tv_mad_u32_u24 v94, v90, s48, v91\n\ts_mov_b64 s[92:93], s[80:81]\n\ts_mov_b32 s94, 229376\n\t"
    "v_add_u32 v90, 28672, v94\n\tv_add_u32 v91, 34816, v94\n\tv_add_u32 v92, 40960, v94\n\tv_add_u32 v93, 47104, v94\n\t"
    "v_add_u32 v95, 6144, v94\n\tv_add_u32 v126, 12288, v94\n\tv_add_u32 v127, 18432, v94\n\t"
    "v_mov_b32 v122, 0xff800000\n\t" MSK(228, 0) MSK(236, 16) MSK(244, 32) MSK(252, 48)
    "v_cmp_eq_u64 vcc, s[84:85], v[124:125]\n\ts_andn2_b64 vcc, s[62:63], vcc\n\ts_cbranch_scc1 6b\n\ts_setpc_b64 s[86:87]\n"
    "3:\n\tv_mov_b32 v122, 0xff800000\n\t" MSK(224, 0) MSK(232, 16) MSK(240, 32) MSK(248, 48)
    "v_readlane_b32 s54, v78, %0\n\ts_add_u32 m0, s78, 0x10000\n\t"
    "s_lshr_b32 s53, s73, 16\n\ts_mulk_i32 s53, 3584\n\ts_mov_b64 s[48:49], s[80:81]\n\ts_add_u32 s50, s53, 3584\n\ts_mov_b32 s51, 0x20000\n\t"
    "v_lshlrev_b32 v121, 3, v84\n\tbuffer_load_dword v121, s[48:51], s53 offen lds\n\t"
    "s_bitcmp0_b32 s73, 3\n\ts_cbranch_scc1 4f\n\t"
    "v_lshlrev_b32 v121, 9, v84\n\tv_and_b32 v121, 0x38000, v121\n\tv_lshrrev_b32 v122, 9, v84\n\tv_lshl_add_u32 v121, v122, 12, v121\n\t"
    "v_lshrrev_b32 v122, 3, v84\n\tv_xor_b32 v122, v122, v84\n\tv_and_or_b32 v121, v122, 48, v121\n\t"
    "buffer_load_dwordx4 v121, s[88:91], s54 offen sc1 lds\n\ts_addk_i32 s54, 7168\n\t"
    "buffer_load_dwordx4 v121, s[88:91], s54 offen offset:1024 sc1 lds\n\ts_addk_i32 s54, 7168\n\t"
    "buffer_load_dwordx4 v121, s[88:91], s54 offen offset:2048 sc1 lds\n\ts_addk_i32 s54, 7168\n\t"
    "buffer_load_dwordx4 v121, s[88:91], s54 offen offset:3072 sc1 lds\n"
    "4:\n\ts_setpc_b64 s[86:87]\n9:"
    ::"i"((int)T_ZO) : "scc");
}
DI void ev_call() { asm volatile("s_swappc_b64 s[86:87], s[82:83]" ::: "vcc", "scc", "memory"); GAP(); }

#define ACLOB_ALL "a0","a1","a2","a3","a4","a5","a6","a7","a8","a9","a10","a11","a12","a13","a14","a15","a16","a17","a18","a19","a20","a21","a22","a23","a24","a25","a26","a27","a28","a29","a30","a31","a32","a33","a34","a35","a36","a37","a38","a39","a40","a41","a42","a43","a44","a45","a46","a47","a48","a49","a50","a51","a52","a53","a54","a55","a56","a57","a58","a59","a60","a61","a62","a63","a64","a65","a66","a67","a68","a69","a70","a71","a72","a73","a74","a75","a76","a77","a78","a79","a80","a81","a82","a83","a84","a85","a86","a87","a88","a89","a90","a91","a92","a93","a94","a95","a96","a97","a98","a99","a100","a101","a102","a103","a104","a105","a106","a107","a108","a109","a110","a111","a112","a113","a114","a115","a116","a117","a118","a119","a120","a121","a122","a123","a124","a125","a126","a127","a128","a129","a130","a131","a132","a133","a134","a135","a136","a137","a138","a139","a140","a141","a142","a143","a144","a145","a146","a147","a148","a149","a150","a151","a152","a153","a154","a155","a156","a157","a158","a159","a160","a161","a162","a163","a164","a165","a166","a167","a168","a169","a170","a171","a172","a173","a174","a175","a176","a177","a178","a179","a180","a181","a182","a183","a184","a185","a186","a187","a188","a189","a190","a191","a192","a193","a194","a195","a196","a197","a198","a199","a200","a201","a202","a203","a204","a205","a206","a207","a208","a209","a210","a211","a212","a213","a214","a215","a216","a217","a218","a219","a220","a221","a222","a223","a224","a225","a226","a227","a228","a229","a230","a231","a232","a233","a234","a235","a236","a237","a238","a239","a240","a241","a242","a243","a244","a245","a246","a247","a248","a249","a250","a251","a252","a253","a254","a255"

struct EP { float fa[2], fb[2]; };
template <int I, int E, int MODE> DI void epi_pair(const EP& p) {
  constexpr int qb = I & 1, r = 16 * (I >> 1) + 4 * qb + (E >> 1) * 8 + (E & 1) * 2, t = 96 + 2 * E, w = 128 + 4 * I + E;
  if constexpr (MODE == 2) asm volatile("v_accvgpr_read_b32 v106, a[%c0]" ::"i"(w));
  asm volatile("v_accvgpr_read_b32 v%c0, a[%c2]\n\tv_accvgpr_read_b32 v%c1, a[%c3]" ::"i"(t), "i"(t + 1), "i"(r), "i"(r + 1));
  if constexpr (MODE) asm volatile("v_mul_f32 v%c0, v%c0, %2\n\tv_mul_f32 v%c1, v%c1, %2" ::"i"(t), "i"(t + 1), "v"(p.fa[qb]));
  if constexpr (MODE == 2)
    asm volatile("v_lshlrev_b32 v104, 16, v106\n\tv_and_b32 v105, 0xffff0000, v106\n\tv_fmac_f32 v%c0, v104, %2\n\tv_fmac_f32 v%c1, v105, %2"
                 ::"i"(t), "i"(t + 1), "v"(p.fb[qb]));
  asm volatile("v_cvt_pk_bf16_f32 v%c0, v%c1, v%c2" ::"i"(w), "i"(t), "i"(t + 1));
}
template <int I> DI void epi_store() {
  asm volatile("buffer_store_dwordx4 v[%c0:%c1], v84, s[88:91], s101 offen offset:%2 sc1" ::"i"(128 + 4 * I), "i"(131 + 4 * I), "i"((I & 3) * 1024) : "memory");
  if constexpr ((I & 3) == 3) asm volatile("s_addk_i32 s101, 4096");
}
constexpr int R_TB = 232, R_VS = 236;
template <int HF, int R> DI void p_read() {
  constexpr int mb = R >> 4, r = 192 - 64 * HF + 2 * R;
  asm volatile("ds_read_b64_tr_b16 a[%c1:%c2], v%c0 offset:%c3" ::"i"(R_TB + 2 * HF + mb), "i"(r), "i"(r + 1), "i"((R & 15) * 2048));
}
template <int QB, int E> DI void p_cvt() {
  asm volatile("v_cvt_pk_bf16_f32 v%c0, v%c1, v%c2" ::"i"(208 + 4 * QB + E), "i"(192 + 8 * QB + 2 * E), "i"(193 + 8 * QB + 2 * E));
}
template <int QB, int PR> DI void p_store() {
  asm volatile("buffer_store_dwordx4 v[%c0:%c1], v%c2, s[88:91], s96 offen offset:%3 sc1" ::"i"(208 + 4 * QB), "i"(211 + 4 * QB), "i"(R_VS + QB),
               "i"(PR * 128) : "memory");
}
constexpr int p_dma(int HF, int G) { return G == 3 && !HF ? 0 : G == 11 ? 1 - HF : G == 15 ? 2 - HF : G == 19 ? 3 - HF : G == 27 && HF ? 3 : -1; }
template <int HF, bool LD, int G> DI void p_gap() {
  constexpr int ch = G >> 3, t = G & 7, a = 192 - 64 * HF + 32 * (ch & 1) + 4 * t, b = 128 + 4 * (2 * t + (ch >> 1)), d = 192 + 4 * ch;
  if constexpr (t == 0)
    asm volatile("v_mfma_f32_16x16x32_bf16 v[%c0:%c1], a[%c2:%c3], v[%c4:%c5], 0" ::"i"(d), "i"(d + 3), "i"(a), "i"(a + 3), "i"(b), "i"(b + 3));
  else
    asm volatile("v_mfma_f32_16x16x32_bf16 v[%c0:%c1], a[%c2:%c3], v[%c4:%c5], v[%c0:%c1]" ::"i"(d), "i"(d + 3), "i"(a), "i"(a + 3), "i"(b), "i"(b + 3));
  p_read<HF ^ 1, G>();
  if constexpr (G == 0) asm volatile("v_xor_b32 v%c0, s100, v%c0" ::"i"(R_TB + 2 * HF + 1));
  if constexpr (G == 17) asm volatile("v_xor_b32 v%c0, s100, v%c0" ::"i"(R_TB + 2 * (HF ^ 1)));
  if constexpr (G >= 4 && G <= 8 && G != 7) p_cvt<1, (G < 7 ? G - 4 : 3)>();
  if constexpr (G == 24 || (G == 7 && HF == 0)) asm volatile("s_waitcnt lgkmcnt(8)");
  if constexpr (G == 7 && HF == 1) asm volatile("s_waitcnt vmcnt(%0) lgkmcnt(8)\n\ts_barrier" ::"i"(LD ? 13 : 5) : "memory");
  if constexpr (G == 10) p_store<1, HF ^ 1>();
  if constexpr (G >= 20 && G < 24) p_cvt<0, G - 20>();
  if constexpr (G == 25) p_store<0, HF>();
  if constexpr (LD && p_dma(HF, G) >= 0) dma_at<(HF ? 8 : 12) + p_dma(HF, G)>();
  if constexpr (LD && HF == 0 && G == 2) asm volatile("s_bitset0_b32 m0, 12");
  if constexpr (HF == 0 && G == 13) asm volatile("s_add_u32 s96, s96, 0x40000");
  if constexpr (HF == 0 && G == 26) asm volatile("s_add_u32 s98, s98, 128");
  if constexpr (LD && HF == 0 && G == 27) asm volatile("s_cmp_lt_u32 s98, 0x380");
  if constexpr (HF == 1 && G == 1) asm volatile("s_xor_b32 s100, s100, 0x10000");
  if constexpr (LD && HF == 1 && G == 2) asm volatile("s_xor_b32 s97, s97, s100");
  if constexpr (LD && HF == 1 && G == 9) asm volatile("s_or_b32 m0, s97, 0x1000");
  if constexpr (!LD && HF == 1 && G == 30) asm volatile("s_cmp_lt_u32 s98, 0x500");
  GAP();
}
template <int HF, bool LD, int... Gs> DI void p_half(iseq<Gs...>) { (p_gap<HF, LD, Gs>(), ...); }
template <int G> DI void gapF(AC& c, const EP& p) {
  if constexpr (G < 64) gapB<0, false, G>(c);
  if constexpr (G >= 10) epi_pair<(G - 10) / 4, (G - 10) & 3, 0>(p);
  if constexpr (G >= 14 && ((G - 14) & 3) == 0) epi_store<(G - 14) / 4>();
  if constexpr (G == 73) epi_store<15>();
  GAP();
}
template <int... Gs> DI void phaseF(AC& c, const EP& p, iseq<Gs...>) { (gapF<Gs>(c, p), ...); }
template <int G> DI void gapAL(AC& c) {
  constexpr int kb = G >> 3, s = G & 7, r = 192 + 4 * (G & 15), q = 160 + 4 * s, d = R_S + 36 + 8 * kb, ci = s == 0 ? R_CI + 4 + 8 * kb : d;
  if constexpr (G == 0) asm volatile("s_add_u32 s96, s97, 0x1000000\n\ts_mov_b32 m0, s79\n\ts_waitcnt vmcnt(9) lgkmcnt(0)\n\ts_barrier" ::: "memory");
  if constexpr (G >= 8 && (G & 3) == 0) asm volatile("s_waitcnt lgkmcnt(4)");
  if constexpr (G == 29) asm volatile("s_barrier" ::: "memory");
  asm volatile("v_mfma_f32_16x16x32_bf16 v[%c0:%c1], a[%c2:%c3], a[%c4:%c5], v[%c6:%c7]" ::"i"(d), "i"(d + 3), "i"(r), "i"(r + 3), "i"(q), "i"(q + 3),
               "i"(ci), "i"(ci + 3));
  if constexpr (G < 24) kread<1, G + 8>();
  else vread<0, G - 24>();
  if constexpr (G % 3 == 1 && G < 24) dma_at<G / 3>();
  if constexpr (G == 11) asm volatile("s_bitset1_b32 m0, 12");
  if constexpr (G == 28) asm volatile("buffer_load_dwordx4 v[122:125], v88, s[88:91], s76 offen sc1" ::: "memory");
  if constexpr (G == 30) asm volatile("s_xor_b32 m0, s79, 0x11000\n\ts_movk_i32 s98, 128");
  sm_gap<0, 64 + 2 * G>(c); sm_gap<0, 65 + 2 * G>(c);
  GAP();
}
template <int... Gs> DI void phaseAL(AC& c, iseq<Gs...>) { (gapAL<Gs>(c), ...); }
struct BLS { int ex[65], op[65], vm[65], su[65]; };
constexpr BLS bl_sched(bool LD) {
  BLS z{};
  z.op[0] = 1;
  for (int t = 0; t < 64; ++t) {
    const int g = t & 15, o = z.op[t];
    const bool x = t == 6 || (t < 48 && g > 8 && g != 12 && g != 15);
    const bool v = LD ? (t & 3) == 0 || g == 7 || t == 5 || t == 58 || t == 62 || t == 63 : (t & 11) == 8;
    const bool p = t >= 15 && o < 24 && (t & 3) != 0 && t != 47 && !x && z.ex[t - 1] > 2 * (o / 3) + (o % 3 != 0);
    z.ex[t + 1] = z.ex[t] + x; z.vm[t + 1] = z.vm[t] + v; z.op[t + 1] = o + p;
    z.su[t + 1] = z.su[t] + (t >= 1 && z.su[t] < (LD ? 16 : 10) && g != 15 && !v && !x && (LD || t > 55 || (t > 3 && !p)));
  }
  return z;
}
template <bool LD> constexpr BLS BL = bl_sched(LD);
static_assert(BL<true>.ex[64] == 16 && BL<true>.op[61] == 24 && BL<true>.vm[64] == 24 && BL<false>.vm[64] == 8);
static_assert(BL<true>.su[56] == 16 && BL<false>.su[62] == 10);
template <bool LD, int G> DI void gapBL(AC& c) {
  if constexpr (G == 48) asm volatile("s_waitcnt vmcnt(%0) lgkmcnt(0)\n\ts_barrier" ::"i"(BL<LD>.vm[48]) : "memory");
  gapB<1, false, G>(c);
  if constexpr (G >= 48 && G < 56) vread<1, G - 48>();
  if constexpr (BL<LD>.vm[G + 1] > BL<LD>.vm[G]) {
    constexpr int k = BL<LD>.vm[G] + (LD ? 0 : 16), I = 2 * (k & 7) + ((k >> 3) & 1);
    if constexpr (k < 16) {
      asm volatile("buffer_load_dwordx4 a[%c0:%c1], v84, s[88:91], s75 offen offset:%2 sc1" ::"i"(128 + 4 * I), "i"(131 + 4 * I), "i"((I & 3) * 1024) : "memory");
      if constexpr (k & 1) asm volatile("s_addk_i32 s75, %0" ::"i"(k == 7 ? -12288 : 4096));
    } else dma_at<k - 8>();
    if constexpr (k == 19) asm volatile("s_bitset0_b32 m0, 12");
  }
  if constexpr (G == 1 || G == 2) asm volatile("v_add_f32_e32 v%c0, v%c0, v%c1" ::"i"(R_RS + G - 1), "i"(R_TS + G - 1));
  if constexpr (G == 3) asm volatile("v_mov_b32 v%c0, 0" ::"i"(R_TS));
  if constexpr (BL<LD>.ex[G + 1] > BL<LD>.ex[G]) {
    constexpr int j = BL<LD>.ex[G];
    asm volatile("v_exp_f32 v%c0, v%c1" ::"i"(R_E + j), "i"(R_S + 36 + 8 * (j >> 2) + (j & 3)));
  }
  if constexpr (BL<LD>.op[G + 1] > BL<LD>.op[G]) {
    constexpr int o = BL<LD>.op[G], k = o / 3, j = 2 * k + o % 3;
    if constexpr (o % 3 == 2) asm volatile("v_cvt_pk_bf16_f32 v%c0, v%c1, v%c2" ::"i"(p_reg(1, (k >> 1) * 4 + 2 + (k & 1))), "i"(R_E + 2 * k), "i"(R_E + 2 * k + 1));
    else asm volatile("v_add_f32_e32 v%c0, v%c1, v%c2" ::"i"(R_TS + 1), "i"(j == 1 ? R_E : R_TS + 1), "i"(R_E + j));
  }
  if constexpr (G == 61) asm volatile("v_cmp_nle_f32_e64 s[54:55], v%c0, v%c1" ::"i"(R_TS + 1), "i"(R_THR));
  if constexpr (BL<LD>.su[G + 1] > BL<LD>.su[G]) {
    constexpr int k = BL<LD>.su[G];
    if constexpr (k < 4) asm volatile("v_xor_b32 v%c0, %1, %2" ::"i"(R_TB + k), "i"(32 * k), "v"(c.tb));
    else if constexpr (k < 6) asm volatile("v_add_u32 v%c0, %1, %2" ::"i"(R_VS + k - 4), "i"(32768 * (k - 4)), "v"(c.vs));
    else if constexpr (k == 6) tload<96, T_SLAB>();
    else if constexpr (k == 7) tload<101, T_CNT>();
    else if constexpr (k == 8) asm volatile("v_mov_b64 v[240:241], 1");
    else if constexpr (k == 9) asm volatile("s_mov_b32 s97, s79\n\ts_mov_b32 s100, 0x8000");
    else if constexpr (k < 14) asm volatile("v_mov_b32 v%c0, %1" ::"i"(k < 12 ? 214 + k : 218 + k), "v"(k & 1 ? c.o1 : c.o0));
    else asm volatile("v_mov_b64 v[%c0:%c1], 0" ::"i"(198 + 2 * k), "i"(199 + 2 * k));
  }
  GAP();
}
template <bool LD, int... Gs> DI void phaseBL(AC& c, iseq<Gs...>) { (gapBL<LD, Gs>(c), ...); }
template <int MODE, int G> DI void gapFL(const EP& p) {
  constexpr int x = G - (MODE == 3 ? 4 : 0), J = G < 24 && G % 3 == 1 ? G / 3 : -1;
  if constexpr (G == 0) asm volatile("s_or_b32 m0, s78, 0x1000\n\ts_movk_i32 s98, 256\n\ts_waitcnt vmcnt(8) lgkmcnt(0)" ::: "memory");
  if constexpr (G >= 8 && G < 32 && (G & 3) == 0) asm volatile("s_waitcnt lgkmcnt(%0)" ::"i"(G < 28 ? 4 : 0));
  if constexpr (G < 32) {
    constexpr int o = 8 * (G >> 1) + 4, r = 192 + 4 * (G & 15), q = R_P + 20 + 8 * (G & 1);
    asm volatile("v_mfma_f32_16x16x32_bf16 a[%c0:%c1], a[%c2:%c3], v[%c4:%c5], a[%c0:%c1]" ::"i"(o), "i"(o + 3), "i"(r), "i"(r + 3), "i"(q), "i"(q + 3));
  }
  if constexpr (G < 24) vread<1, G + 8>();
  if constexpr (G == 28) asm volatile("s_waitcnt vmcnt(8)\n\ts_barrier\n\ts_or_b32 m0, s79, 0x1000\n\ts_movk_i32 s98, 384" ::: "memory");
  if constexpr (G == 29 || G == 31) dma_at<8 + (G - 29) / 2>();
  if constexpr (G == 32 || G == 40) {
    static_for([&](auto I) { p_read<0, 2 * (G - 32) + I>(); }, make_iseq<16>{});
    dma_at<10 + (G - 32) / 8>();
  }
  if constexpr (J >= 0) dma_at<8 + J>();
  if constexpr (x >= 0) epi_pair<2 * ((x >> 2) & 7) + (x >> 5), x & 3, MODE>(p);
  if constexpr (MODE == 3 && G < 32) {
    constexpr int db = G & 15, qb = G >> 4, o = 4 * (db * 2 + qb), a = 224 + 4 * (db & 1), b = 128 + 4 * ((db >> 1) * 2 + qb);
    asm volatile("v_mfma_f32_16x16x32_bf16 a[%c0:%c1], v[%c2:%c3], a[%c4:%c5], a[%c0:%c1]" ::"i"(o), "i"(o + 3), "i"(a), "i"(a + 3), "i"(b), "i"(b + 3));
  }
  if constexpr (J == 3) asm volatile("s_bitset0_b32 m0, 12");
  if constexpr (x == 63) asm volatile("s_waitcnt vmcnt(4) lgkmcnt(0)" ::: "memory");
  GAP();
}
template <int MODE, int... Gs> DI void phaseFL_(const EP& p, iseq<Gs...>) { (gapFL<MODE, Gs>(p), ...); }
template <int MODE> DI void phaseFL(const EP& p) { GAP(); phaseFL_<MODE>(p, make_iseq<MODE == 3 ? 68 : 64>{}); }
template <int U, int K> DI void r_load(unsigned vo, unsigned so) {
  asm volatile("buffer_load_dwordx4 a[%c0:%c1], %2, s[88:91], %3 offen offset:%4 sc1" ::"i"(64 * U + 4 * K), "i"(64 * U + 4 * K + 3), "v"(vo),
               "s"(so + (K >> 2) * 4096), "i"((K & 1) * 512 + ((K >> 1) & 1) * 2048) : "memory");
}
template <int U, int K> DI void r_sum() {
  constexpr int d = 100 + 4 * (K & 1), b = 64 * U + 4 * K;
  if constexpr (K < 2) asm volatile("v_mfma_f32_16x16x32_bf16 v[%c0:%c1], v[96:99], a[%c2:%c3], 0" ::"i"(d), "i"(d + 3), "i"(b), "i"(b + 3));
  else asm volatile("v_mfma_f32_16x16x32_bf16 v[%c0:%c1], v[96:99], a[%c2:%c3], v[%c0:%c1]" ::"i"(d), "i"(d + 3), "i"(b), "i"(b + 3));
}
template <int U> DI void r_fin(unsigned vo, __amdgpu_buffer_rsrc_t srd, unsigned so) {
  asm volatile("" : "+s"(srd), "+s"(so));
  static_for([&](auto K) { r_sum<U, K>(); }, make_iseq<16>{});
  asm volatile("s_nop 7");
  static_for([&](auto E) { asm volatile("v_cvt_pk_bf16_f32 v%c0, v%c1, v%c2" ::"i"(116 + 4 * U + E), "i"(100 + 2 * E), "i"(101 + 2 * E)); }, make_iseq<4>{});
  asm volatile("buffer_store_dwordx4 v[%c3:%c4], %0, %1, %2 offen" OUTSCS ::"v"(vo), "s"(srd), "s"(so), "i"(116 + 4 * U), "i"(119 + 4 * U) : "memory");
}

#define CMAX 12
#define PREPE 0
static_assert(NYT <= 16 && NYT >= 12);
struct TRow { unsigned v[64]; };
constexpr u64 kvmask(int m0, int m1) { u64 n = 0; for (int m = m0; m <= m1; ++m) n |= 0x101ull << m; return n; }
constexpr u64 k_order(bool isY, int rr) {
  const int cls = (rr + (rr >> 2)) & 3;
  return !isY ? (cls == 0 ? 0xfedcb9875a463210ull : cls == 1 ? 0xefadc6982b574310ull : cls == 2 ? 0xfebad7693c285410ull : 0xefcba8764d392510ull)
              : (cls == 0 ? 0xfedcb987543a6210ull : cls == 1 ? 0xefadc698254b7310ull : cls == 2 ? 0xfebad769325c8410ull : 0xefcba876432d9510ull);
}
constexpr TRow t_row(int r, int wid, int j) {
  TRow t{};
  const int rr = r & 15, hsel = rr & 1, pj = rr >> 1, kv = pj & 1, h = j + 8 * hsel, u = hsel * 8 + j;
  const bool isY = r >= 16, solo = pj == 7;
  const int mt = (rr >> 2) + (isY ? 4 : 0), g = j * 128, NY = solo ? 18 : NYT;
  const int nx0 = 32 - 2 * pj - NY, cx = nx0 < CMAX ? nx0 : CMAX, ex = nx0 - cx, qbF = isY != (solo && SWAP7) ? 15 - pj : pj;
  const unsigned oQ = (unsigned)h << 20, oK = oQ + O_K, oP = O_P + (h * 8 + pj) * PSTR;
  if (wid == 4) {
    for (int l = 0; l < 64; ++l)
      t.v[l] = fo(l < 16 ? g + hsel * 16 + l : l < 24 ? g + 16 + hsel * 8 + l : l == 24 ? g + 48 + hsel * 8 + pj : l < 32 ? g + 64 + hsel * 8 + pj :
                  l < 48 ? 2304 + pj * 16 + l - 32 : 2048 + pj * 16 + l - 48);
    return t;
  }
  auto put64 = [&](int i, u64 x) { t.v[i] = (unsigned)x; t.v[i + 1] = (unsigned)(x >> 32); };
  t.v[T_Q0] = oQ + ((solo ? qbF : 15 - pj) * 128 + wid * 16) * 512;
  t.v[T_K0] = oK + (isY ? cx : 0) * 32768;
  t.v[T_OQ] = oQ + pj * 131072 + (wid >> 1) * 65536 + (wid & 1) * 4096;
  t.v[T_OG] = oK + kv * TSB + mt * 131072 + (wid >> 1) * 65536 + (wid & 1) * (kv ? 8192 : 4096);
  t.v[T_FPREP] = PREPE ? fo(2048 + pj * 16 + (isY * 2 + hsel) * 4) + (j - PREPE + 1) * 4 : fo(2048 + pj * 16 + u);
  t.v[T_FQ] = fo(g + 32 + hsel * 8 + pj);
  t.v[T_FG] = fo(g + hsel * 16 + kv * 8 + mt);
  t.v[T_FS] = fo(1024 + qbF * 16 + j * 2 + hsel);
  t.v[T_CNT] = fo(2560 + qbF * 16);
  t.v[T_ZO] = O_Z + pj * 64 * SEQ * 2 + (u * 4 + wid) * 64;
  const u64 own = 0x1ull << (kv * 8 + mt), needQ = isY ? 1ull << (16 + (qbF >> 1)) : (1ull << (16 + (pj >> 1))) | (1ull << (16 + ((15 - pj) >> 1)));
  put64(T_NPRE, isY ? needQ | (1ull << (cx >> 2)) | (1ull << ((cx + 1) >> 2)) : solo ? needQ : needQ | 0xffffull << 48);
  put64(T_NST, isY ? kvmask(cx >> 2, 3) | (solo ? 0 : 1ull << 25) : kvmask(0, solo ? 3 : 0));
  put64(T_TAB, k_order(isY, rr));
  t.v[T_SLAB] = O_S + qbF * 0x200000 + wid * 65536 + (j * 2 + hsel) * 1024;
  t.v[T_PART] = oP;
  for (int it = 0; it < 2; ++it) {
    unsigned* p = t.v + T_IT + it * T_ITN;
    const bool last = isY || it == 1;
    const int n = solo ? 2 * qbF + 2 : isY ? NY : it ? 2 * pj + 2 : nx0;
    const u64 need2 = solo ? 0 : kvmask(0, (2 * pj + 1) >> 2) | (0xffffull << 32);
    u64 q[4] = {0, 0, 0, 0};
    unsigned jmp[3] = {0, 0, 0}, chk = 0;
    if (isY) {
      p[0] = 2; p[1] = 2; p[13] = t.v[T_FG]; p[14] = fo(g + 96 + r);
      if (!(solo && SWAP7)) { chk = 1u << (14 - cx); p[0] |= chk; q[1] = kvmask(4, (31 - 2 * pj) >> 2) & ~own; jmp[1] = ex * 32768; }
      if (!solo) { chk |= 1u << (n - 2); q[2] = 1ull << 24; }
    } else if (it == 0) {
      chk = 2u | (ex ? 1u << (cx - 2) : 0);
      p[0] = chk | 1u << (n - 2);
      chk |= 1u << n;
      q[0] = kvmask(0, cx > 0 ? (cx - 1) >> 2 : 0);
      if (ex) { q[1] = kvmask(4, 4 + ((ex - 1) >> 2)); jmp[1] = (16 - cx) * 32768; }
      jmp[ex ? 2 : 1] = -(ex ? 16 + ex : cx) * 32768;
      q[ex ? 3 : 2] = need2;
      p[1] = 2; p[13] = fo(2304 + pj * 16 + u) + j * (8 * 4352 - 8); p[14] = fo(g + 64 + hsel * 8 + pj);
    } else {
      p[0] = p[1] = solo ? 0 : 1;
      q[0] = need2;
      if (solo && SWAP7) { p[0] = chk = 1u << 14; q[0] = kvmask(4, 4); }
      p[13] = fo(g + 48 + hsel * 8 + pj); p[14] = fo(g + 96 + r);
    }
    p[2] = jmp[0]; p[4] = jmp[1]; p[15] = jmp[2];
    p[3] = !last ? 0 : n > 2 ? 1u << (n - 3) : 1u << 30;
    for (int k = 0; k < 4; ++k) { p[5 + 2 * k] = (unsigned)q[k]; p[6 + 2 * k] = (unsigned)(q[k] >> 32); }
    p[16] = (last ? 1 : 0) | (isY && !solo ? 2 : 0) | (n == 2 ? 4 : 0) | (isY || solo ? 0 : 8) | (n - 1 - last) << 8 | ((r >> 1) * 4 + wid) << 16;
    p[17] = !last ? 0 : n > 2 ? 1u << (n - 2) : 1u << 31;
    p[18] = oQ + (pj * 128 + wid * 16) * 512;
    p[19] = 0;
    p[20] = chk >> 1;
    p[21] = oK + (isY ? cx : 0) * 32768;
    p[22] = ((p[0] | p[3] | p[17]) & 0x3fffffffu) << 1;
  }
  return t;
}
template <int I> constexpr TRow t_base = I < 160 ? t_row(I / 5, I % 5, 0) : [] {
  TRow a = t_row(0, (I - 160) * 4, 0), b = t_row(0, (I - 160) * 4, 1);
  for (int l = 0; l < 64; ++l) a.v[l] = b.v[l] - a.v[l];
  return a;
}();
template <int I> constexpr bool t_linear = [] {
  for (int j = 1; j < 8; ++j) {
    const TRow b = t_row(I / 5, I % 5, j);
    for (int l = 0; l < 64; ++l) if (b.v[l] != t_base<I>.v[l] + j * t_base<160 + (I % 5) / 4>.v[l]) return false;
  }
  return true;
}();
struct Tab { TRow row[162]; };
template <int... I> constexpr Tab t_make(iseq<I...>) {
  static_assert((t_linear<(I < 160 ? I : 0)> && ...));
  return Tab{{t_base<I>...}};
}
__device__ const Tab g_tab = t_make(make_iseq<162>{});

template <int R> DI const bf16* sptr() { const bf16* p; asm volatile("s_mov_b64 %0, s[%c1:%c2]" : "=s"(p) : "i"(R), "i"(R + 1)); return p; }
template <class T, int I> DI T* arg_late() {
  unsigned lo, hi;
  asm volatile("v_readlane_b32 %0, v77, %2\n\tv_readlane_b32 %1, v77, %3\n\ts_nop 4" : "=s"(lo), "=s"(hi) : "i"(2 * I), "i"(2 * I + 1));
  return (T*)(((u64)hi << 32) | lo);
}
__global__ __launch_bounds__(256, 1) __attribute__((amdgpu_num_vgpr(77), amdgpu_num_sgpr(56))) void fused_kernel(
    const bf16* ckv_, const bf16* wuk_, const bf16* wuv_, const bf16* x_, const bf16* wq_, bf16* ws_, const bf16* ak_, const bf16* av_,
    const bf16* ash_, const bf16* assmk_, const bf16* assmD_, const bf16* wo_, const bf16* aq_, bf16* out_) {
  extern __shared__ __attribute__((aligned(16))) char lds[];
  const void* kap = (const void*)__builtin_amdgcn_kernarg_segment_ptr();
  asm volatile("at_code_begin:\n\ts_load_dwordx16 s[80:95], %0, 0\n\ts_load_dwordx2 s[96:97], %0, 0x40" ::"s"(kap) : ACLOB_ALL, "v255", "s101");
  asm volatile("s_getpc_b64 s[48:49]\nct_base:\n\ts_mov_b32 s50, pf_code_end-ct_base\n\ts_mov_b32 s51, 0x20000\n\ts_lshr_b32 s52, %1, 3\n\t"
               "v_and_b32 v116, 63, %0\n\tv_lshrrev_b32 v117, 6, %0\n\tv_lshl_add_u32 v116, v116, 7, v117\n\ts_lshl_b32 s52, s52, 9\n\t"
               "v_lshlrev_b32 v116, 7, v116\n\tbuffer_load_dword v117, v116, s[48:51], s52 offen" ::"v"(threadIdx.x), "s"(blockIdx.x) : "scc", "memory");
  GAP();
  const int tid = threadIdx.x, lane = tid & 63;
  const int wid = __builtin_amdgcn_readfirstlane(tid >> 6);
  const int b = blockIdx.x, j = b & 7, r = b >> 3, rr = r & 15, hsel = rr & 1, pj = rr >> 1, kv = pj & 1;
  const bool isY = r >= 16, solo = pj == 7, doP = (PREPE ? (j & 1) == PREPE - 1 : isY) && !solo, doA = !isY && !solo;
  const int h = j + 8 * hsel, mt = (rr >> 2) + (isY ? 4 : 0), pu = (hsel * 8 + j) * 4 + wid;
  constexpr unsigned m0d = 131072 + 24576;
  const unsigned lA = isY ? 65536 : 0, lW = lA ^ 65536, po = isY ? 0 : 32768;
  unsigned mA = lA + wid * 1024 + po, mW = lW + wid * 1024 + po, ofA = isY ? pj * 256 * DA * 2 : mt * 256 * DC * 2, ofW = h * 512;
  const unsigned lda = isY ? DA : DC;
#define ROT 1
  unsigned g0 = (!ROT ? 0 : isY ? pj + 2 * hsel : rr + (rr >> 2)) & 3;
  {
    const unsigned* tp = g_tab.row[r * 5].v;
    asm volatile("global_load_dword v78, %0, %2\n\tglobal_load_dword v79, %1, %2 offset:1024\n\t"
                 "global_load_dword v118, %1, %3\n\tglobal_load_dword v119, %1, %3 offset:256\n\tglobal_load_dword v77, %4, %5"
                 ::"v"(tid * 4), "v"(lane * 4), "s"(tp), "s"(g_tab.row[160].v), "v"((lane < 27 ? lane : 27) * 4), "s"(kap) : "memory");
    const unsigned ra = wid * 8 + (lane >> 3), sa = ((lane & 7) ^ (((wid & 1) * 4 + (lane >> 4)) & 7)) * 16;
    const unsigned vbA = ra * (lda * 2) + sa, vbB = (4 * wid + (lane >> 4)) * LDW * 2 + 16 * ((lane & 15) ^ (((lane >> 4) << 2) | wid));
    vsetu<92>(vbA); vsetu<93>(vbB); vsetu<94>(ra * (DC * 2) + sa);
    sset<64>(64 * lda); sset<67>(256 * lda); sset<68>(320 * lda);
    asm volatile("s_mov_b32 s71, 0x20000\n\ts_movk_i32 s72, 0x100\n\ts_mov_b32 s73, 0x20100");
    sset<58>(256 * lda * 2); sset<62>((lda - 1) * LDW * 2 + 512);
    asm volatile("s_mov_b32 s59, 0x20000\n\ts_mov_b32 s63, 0x20000");
    g_pro<1>(g0, mA, mW, lda);
    asm volatile("" : "+s"(ofA), "+s"(ofW), "+s"(mA), "+s"(mW), "+s"(g0));
    GAP();
  }
  asm volatile("s_bitcmp1_b32 %0, 4\n\ts_waitcnt lgkmcnt(0)\n\ts_cselect_b64 s[60:61], s[84:85], s[82:83]\n\ts_bitcmp1_b32 %0, 7\n\t"
               "s_cselect_b64 s[60:61], s[88:89], s[60:61]\n\ts_cselect_b64 s[56:57], s[86:87], s[80:81]\n\t"
               "s_add_u32 s56, s56, %1\n\ts_addc_u32 s57, s57, 0\n\ts_add_u32 s60, s60, %2\n\ts_addc_u32 s61, s61, 0\n\t"
               "s_mov_b64 s[88:89], s[90:91]\n\ts_mov_b32 s90, %3\n\ts_mov_b32 s91, 0x20000" ::"s"(b), "s"(ofA), "s"(ofW), "i"(WSB) : "scc");
  KST(0, 0);
  AC c;
  {
    GM g;
    PR pr;
    const int ptb = PREPE ? (isY * 2 + hsel) * 4 + (j >> 1) : hsel * 8 + j;
    if (doP) prep_load(pr, tid, pj, ptb, sptr<92>(), sptr<94>(), sptr<96>());
    g_pro<2>(g0, mA, mW, lda);
    asm volatile("buffer_load_dwordx2 v[120:121], off, s[88:91], %0 sc1" ::"s"(fo(6144)) : "memory");
    if (doP) {
      asm volatile("s_waitcnt vmcnt(9)" ::: "memory");
#pragma unroll
      for (int q = 0; q < 7; ++q) asm volatile("" : "+v"(pr.kk[q]));
#pragma unroll
      for (int q = 0; q < 4; ++q) asm volatile("" : "+v"(pr.vv[q]));
      static_for([&](auto Ic) __attribute__((always_inline)) {
        constexpr int I = Ic;
        if constexpr (I < 3) g_pro(g0 + 1 + I, mA, mW, lda);
        GAP();
        prep_math<I>(pr, tid, lds + 131072);
        GAP();
      }, make_iseq<4>{});
      asm volatile("s_waitcnt lgkmcnt(0)\n\ts_barrier" ::: "memory");
      prep_store(tid, pj, ptb, (bf16*)sptr<88>() + O_X / 2, lds + 131072);
    }
    GAP();
    {
      const int fr = lane & 15, fq = lane >> 4, q = (lane & 15) >> 2, p = lane & 3;
#pragma unroll
      for (int k2 = 0; k2 < 2; ++k2)
        g.aA[k2] = lA + (wid >> 1) * 16384 + (fr >> 3) * 1024 + (fr & 7) * 128 + (((4 * k2 + fq) ^ (fr >> 1)) * 16);
#pragma unroll
      for (int hf = 0; hf < 2; ++hf)
#pragma unroll
        for (int nf = 0; nf < 8; ++nf)
          g.aB[hf][nf] = lW + (wid & 1) * 8192 + off_b(8 * fq + 4 * hf + q, nf * 2 + (p >> 1)) + 8 * (p & 1);
      asm volatile("" : "+v"(g.aA[0]), "+v"(g.aA[1]));
#pragma unroll
      for (int nf = 0; nf < 8; ++nf) asm volatile("" : "+v"(g.aB[0][nf]), "+v"(g.aB[1][nf]));
      const unsigned one = fq != q ? 0u : (p & 1) ? 0x3f800000u : 0x3f80u;
      c.o0 = (p & 2) ? 0u : one; c.o1 = (p & 2) ? one : 0u;
      c.tb = (lane >> 2) * 128 + 16 * ((p >> 1) ^ ((lane >> 2) & 6)) + 8 * (p & 1);
      c.vs = (fr >> 3) * 16384 + (fq & 1) * 512 + (fq >> 1) * 256 + (fr & 7) * 16;
      asm volatile("" : "+v"(c.o0), "+v"(c.o1), "+v"(c.tb), "+v"(c.vs));
      vsetu<81>(tid == 0 ? 0u : 0x80000000u);
      vsetu<84>(lane * 16); vsetu<85>(lane * 16 + 65536); vsetu<86>(wid * 8192 + lane * 16); vsetu<87>(wid * 8192 + lane * 16 + 4096);
      vsetu<88>(65536 + wid * 256 + fr * 16); vsetu<89>(16 * wid + fr - 4 * fq);
      asm volatile("s_mov_b64 s[92:93], s[88:89]\n\ts_mov_b64 s[94:95], s[90:91]\n\ts_mov_b32 s74, 0x40000\n\ts_mov_b32 s75, 0x60000\n\t"
                   "s_mov_b32 s76, 0x40100\n\ts_mov_b32 s77, 0x60100");
      sset<65>(128 * lda); sset<66>(192 * lda); sset<69>(384 * lda); sset<70>(448 * lda);
      sset<78>(lA + wid * 1024); sset<79>(lW + wid * 1024);
    }
    if (doP) asm volatile("s_waitcnt vmcnt(4)" ::: "memory");
    else {
      g_pro(g0 + 1, mA, mW, lda); g_pro(g0 + 2, mA, mW, lda); g_pro(g0 + 3, mA, mW, lda);
      asm volatile("s_waitcnt vmcnt(0)" ::: "memory");
    }
    asm volatile("s_barrier" ::: "memory");
    if (isY) g_read0<0>(g, make_iseq<16>{});
    else g_read0<1>(g, make_iseq<16>{});
    GAP();
    {
      u64 z = (((u64)__float_as_uint(vget<121>()) << 32) | __float_as_uint(vget<120>())) + 0x9e3779b97f4a7c15ull;
      z = (z ^ (z >> 30)) * 0xbf58476d1ce4e5b9ull; z = (z ^ (z >> 27)) * 0x94d049bb133111ebull; z ^= z >> 31;
      vsetu<82>((unsigned)z); vsetu<83>((unsigned)(z >> 32));
      asm volatile("v_mad_u32_u24 v78, v118, %0, v78\n\tv_mad_u32_u24 v79, v119, %0, v79\n\tv_readfirstlane_b32 s84, v82\n\tv_readfirstlane_b32 s85, v83\n\t"
                   "v_readlane_b32 s80, v78, %1\n\tv_readlane_b32 s81, v78, %2" ::"s"(j), "i"((int)T_TAB), "i"(T_TAB + 1));
      if (isY) asm volatile("s_mov_b64 s[80:81], 0x6543210");
    }
    unsigned k2;
    asm volatile("s_bfe_u32 s82, s80, 0x40008\n\ts_lshr_b64 s[80:81], s[80:81], 12\n\ts_lshl_b32 %0, s82, 1\n\tv_add_u32 v90, 128, v92" : "=s"(k2) :: "scc");
    asm volatile("s_waitcnt lgkmcnt(0)\n\ts_barrier" ::: "memory");
    g_proW(k2, mW);
    GAP();
    if (isY) {
      g_tile<false, 0, 1, NTW >= 1>(g);
      g_loop<false, true>(g, doP ? 0x22u : 0x20u, [&](int t) __attribute__((always_inline)) {
        if (t == 1) fl_set<T_FPREP>();
        else if (t == 4) { tload<80, T_TAB>(); tload<81, T_TAB + 1>(); srd_set<60>((kv ? arg_late<const bf16, 2>() : arg_late<const bf16, 1>()) + h * 256, (DC - 1) * LDW * 2 + 512); }
        else {
          srd_set<56>(arg_late<const bf16, 0>() + (size_t)mt * 256 * DC, 256 * DC * 2);
          asm volatile("v_mov_b32 v92, v94");
          static_for([&](auto J) { asm volatile("s_mov_b32 s%c0, %1" ::"i"(64 + J), "i"((J + 1) * 64 * DC)); }, make_iseq<7>{});
        }
      });
      tload<101, T_OQ>(); sset<83>(__float_as_uint(CS));
      asm volatile("v_mov_b32 v112, v84\n\tv_add_u32 v113, 8192, v84");
      GAP();
      g_tile<false, 0, 2, NTW >= 2>(g);
      KST(0, 3);
    }
#define TT 3
    auto kvtile = [&](auto TRc) __attribute__((always_inline)) {
      constexpr bool TR = TRc;
      if (isY) g_tile<TR, 1, 4, NTW >= 2>(g);
      else g_tile<TR, 1, 1, NTW >= 2>(g);
      g_loop<TR, false>(g, 0x2000u | (isY || doA ? 2u : 1u << TT), [&](int t) __attribute__((always_inline)) {
        int l = threadIdx.x & 63; asm volatile("" : "+v"(l));
        if (t == 13) {
          asm volatile("s_mov_b32 s79, 0x28000");
          fl_poll<T_NPRE>();
          if ((b & 15) == 0)
            asm volatile("v_readlane_b32 s48, v78, %0\n\tv_and_b32 v122, 0xffffff00, v82\n\tv_mov_b32 v123, v83\n\ts_nop 2\n\t"
                         "buffer_store_dwordx2 v[122:123], v81, s[88:91], s48 offen sc1" ::"i"((int)T_CNT) : "memory");
          if (!doA && !isY) {
            CODE_TOUCH("at_code_begin", "pf_code_end", hsel * 8 + wid * 2, 16u, 64, m0d);
            CODE_TOUCH("at_code_begin", "pf_code_end", hsel * 8 + wid * 2 + 1, 16u, 64, m0d);
          }
          if (doA) {
            const char* tp = arg_late<const char, 9>() + (size_t)(pj * 64 + pu) * (SEQ * 2) + (l & 31) * 128;
            asm volatile("s_mov_b32 m0, %1\n\ts_nop 0\n\tglobal_load_lds_dword %0, off" ::"v"(tp), "i"(m0d) : "memory");
          }
        } else if (isY) fl_set<T_FQ>();
        else if (doA) { if (doP) fl_set<T_FPREP>(); post_dma(pj, pu, arg_late<const bf16, 12>(), 131072 + wid * 8192); }
        else {
          const char* tp = arg_late<const char, 0>() + 1024 * DC * 2 + (pu * 256 + l) * 128;
          asm volatile("s_mov_b32 m0, %1\n\ts_nop 0\n\tglobal_load_lds_dword %0, off" ::"v"(tp), "i"(m0d) : "memory");
          asm volatile("global_load_lds_dword %0, off" ::"v"(tp + 8192) : "memory");
          asm volatile("global_load_lds_dword %0, off" ::"v"(tp + 16384) : "memory");
          asm volatile("global_load_lds_dword %0, off" ::"v"(tp + 24576) : "memory");
        }
      });
      tload<101, T_OG>(); tload<96, T_Q0>(); tload<98, T_K0>();
      fl_check();
      tload<50, T_NST>(); tload<51, T_NST + 1>();
      asm volatile("v_mov_b32 v112, v84\n\tv_add_u32 v113, %0, v84\n\tv_mov_b32 v90, v87\n\tv_mov_b32 v91, v87\n\tv_mov_b32 v92, v87\n\tv_mov_b32 v93, v87\n\t"
                   "v_mov_b32 v94, v86\n\tv_mov_b32 v95, v86\n\tv_mov_b32 v126, v86\n\tv_mov_b32 v127, v86\n\ts_mov_b32 s99, 0x8000\n\t" FL_ARM("s[50:51]")
                   ::"i"(TR ? 4096 : 8192));
      if (doA) {
        const int d = pj * 64 + pu;
        const bf16* dp = arg_late<const bf16, 10>() + d;
        asm volatile("global_load_ushort v123, %0, off" ::"v"(dp) : "memory");
        srd_set<56>(arg_late<const bf16, 9>() + (size_t)d * SEQ, SEQ * 2);
        srd_set<60>(arg_late<const char, 5>() + O_X + d * SEQ * 2, SEQ * 2);
        sset<78>(65536 + wid * 15488);
        asm volatile("s_movk_i32 s64, 992\n\tv_subrev_u32 v122, 32, v84");
        GAP();
        g_tile<TR, 0, 3>(g);
      } else {
        sset<79>(isY ? wid * 8192 : 0x30000);
        if ((NYT & 1) && !solo) asm volatile("s_add_u32 s98, s98, 0x8000\n\ts_mov_b32 s97, 0xffff8000" ::: "scc");
        else asm volatile("s_mov_b32 s97, 0x8000");
        GAP();
        g_tile<TR, 0, 5>(g);
      }
    };
    if (kv) kvtile(std::true_type{});
    else kvtile(std::false_type{});
  }
  KST(0, 4);
  asm volatile("s_waitcnt vmcnt(16)" ::: "memory");
  sset<79>(wid * 8192);
  if (doA) {
    const float Dd = bfl(__float_as_uint(vget<123>()));
    KST(0, 5);
    conv_wave<T_FG>(O_Z + (pj * 64 + pu) * SEQ * 2, Dd, lds + 65536 + wid * 15488);
    const char* tp = arg_late<const char, 11>() + (size_t)h * (HD * DA * 2) + ((pj * 4 + wid) * 64 + lane) * 128;
    asm volatile("s_mov_b32 m0, %1\n\ts_nop 0\n\tglobal_load_lds_dword %0, off\n\ts_waitcnt vmcnt(17)" ::"v"(tp), "i"(m0d) : "memory");
    KST(0, 6);
  } else if (!isY) {
    static_for([&](auto I) { d_store<16 + I>(); }, make_iseq<16>{});
    tload<98, T_K0>();
    asm volatile("s_waitcnt vmcnt(0)\n\ts_barrier" ::: "memory");
    fl_set<T_FG>();
    KST(0, 5);
    fl_check();
    CODE_TOUCH("att_begin", "pf_code_end", hsel * 4 + wid, 8u, 64, m0d);
    dma4k<0>(); dma4k<1>(); dma4k<2>(); dma4k<3>();
    asm volatile("s_waitcnt vmcnt(0)" ::: "memory");
    KST(0, 6);
  } else {
    tload<76, T_PART>();
    fl_check();
    asm volatile("buffer_load_dwordx2 v[122:123], v88, s[88:91], s76 offen offset:1024 sc1" ::: "memory");
  }
  sset<78>(65536 + wid * 8192); sset<79>(98304 + wid * 8192);
  { const bf16* wp = arg_late<const bf16, 11>() + (size_t)h * HD * DA; asm volatile("s_mov_b64 s[80:81], %0" ::"s"(wp)); }
  asm volatile("att_begin:");
  ev_code();
  asm volatile("s_waitcnt lgkmcnt(0)\n\ts_barrier" ::: "memory");
#pragma unroll 1
  for (int it = doA ? 0 : 1; it < 2; ++it) {
    const bool odd = (NYT & 1) && !solo && (isY || it == 0);
    if (odd) kreads<1>(make_iseq<8>{});
    else kreads<0>(make_iseq<8>{});
    const unsigned oP = tget<T_PART>();
    if (it == 0) {
      static_for([&](auto I) { tload<57 + I, T_IT + I>(); }, make_iseq<21>{});
      tload<S_T, T_IT + 21>(); tload<S_EV, T_IT + 22>();
      asm volatile("s_sub_u32 s70, s70, %0\n\ts_add_u32 s76, %1, 1024" ::"s"(j * (8 * 4352 - 8)), "s"(oP) : "scc");
    } else {
      static_for([&](auto I) { tload<57 + I, T_IT + T_ITN + I>(); }, make_iseq<21>{});
      tload<S_T, T_IT + T_ITN + 21>(); tload<S_EV, T_IT + T_ITN + 22>();
      asm volatile("s_mov_b32 s75, %0\n\ts_mov_b32 s76, %1" ::"s"(oP + wid * 16384), "s"(oP + 1032));
    }
    unsigned mode;
    asm volatile("s_add_u32 s98, s97, 0x8000\n\ts_mov_b32 %0, s73\n\ts_mov_b32 s56, -1\n\tv_mov_b64 v[114:115], 0\n\tv_mov_b64 v[118:119], 0\n\t"
                 "v_mov_b32 v120, 0x45800000" : "=s"(mode) :: "scc");
    KST(0, 8 + 3 * (it && !isY));
    GAP();
#define STA(k)
#define STI()
#define LOOP_ENDS "s_bfe_u32 s76, s73, 0x80008"
    STA(0);
    if (odd) {
      if (isY) phaseA<1, 2>(c);
      else phaseA<1, 1>(c);
      STA(1);
      STA(2);
      phaseB0<1>(mode);
      STA(3);
      asm volatile(LOOP_ENDS "\n\ts_branch att_mid" ::: "scc");
    } else {
      if (isY) phaseA<0, 2>(c);
      else phaseA<0, 1>(c);
      STA(1);
      if (c.rare) ev_call();
      if (__builtin_expect(mode & 4, 0)) { asm volatile("s_mov_b32 s56, 30"); ev_call(); asm volatile("s_mov_b32 s56, 31"); ev_call(); }
      STA(2);
      phaseB0<0>(mode);
      if (__builtin_expect(mode & 4, 0)) { ev_call(); asm volatile("s_branch att_done"); }
      STA(3);
    }
    GAP();
    asm volatile(LOOP_ENDS "\natt_loop:" ::: "scc");
    GAP();
    phaseA<1, 0>(c);
    STI();
    if (__builtin_expect(c.rare != 0, 0)) { if (redo()) sm_redo<1>(); ev_call(); }
    phaseB<1>(c);
    asm volatile("s_cbranch_scc1 att_done\natt_mid:");
    phaseA<0, 0>(c);
    STI();
    if (__builtin_expect(c.rare != 0, 0)) { if (redo()) sm_redo<0>(); ev_call(); }
    phaseB<0>(c);
    asm volatile("s_cbranch_scc0 att_loop\natt_done:");
    GAP();
    STA(4);
    if (it == 0) phaseA<0, 3>(c);
    else {
      sset<76>(oP);
      GAP();
      phaseAL(c, make_iseq<32>{});
      if (__builtin_expect(redo(), 0)) sm_redo<1, false>();
      if (mode & 2) phaseBL<true>(c, make_iseq<64>{});
      else phaseBL<false>(c, make_iseq<64>{});
    }
    if (__builtin_expect(redo(), 0)) {
      if (it) static_for([&](auto I) { vset<R_S + 32 + (I >> 2) * 8 + (I & 3)>(-INFINITY); }, make_iseq<16>{});
      asm volatile("s_nop 7");
      sm_redo<0, false>();
    }
    STA(5);
    KST(0, 9 + 3 * (it && !isY));
    float l[2];
    asm volatile("v_add_f32 %0, v%c2, v%c3\n\tv_add_f32 %1, v%c4, v%c5" : "=v"(l[0]), "=v"(l[1]) : "i"(R_RS), "i"(R_TS), "i"(R_RS + 1), "i"(R_TS + 1));
    const float cm[2] = {vget<R_M>(), vget<R_M + 1>()};
    l[0] = row_reduce<false>(l[0]); l[1] = row_reduce<false>(l[1]);
    EP p;
    if (it == 0) {
      const f32x4 ml = {cm[0], l[0], cm[1], l[1]};
      asm volatile("buffer_store_dwordx4 %0, v88, s[88:91], %1 offen sc1\n\ts_mov_b32 s101, %2" ::"v"(ml), "s"(oP), "s"(oP + wid * 16384) : "memory");
      phaseF(c, p, make_iseq<74>{});
      asm volatile(FL_POLL "\n\ts_waitcnt vmcnt(17)" ::: "memory");
    } else {
      if (!(mode & 2)) {
        p.fa[0] = __builtin_amdgcn_rcpf(l[0]); p.fa[1] = __builtin_amdgcn_rcpf(l[1]);
        phaseFL<1>(p);
      } else {
        const f32x4 ml = {vget<122>(), vget<123>(), vget<124>(), vget<125>()};
        if (__builtin_expect(__builtin_amdgcn_ballot_w64(ml[0] != cm[0] || ml[2] != cm[1]) == 0, 1)) {
          p.fa[0] = __builtin_amdgcn_rcpf(l[0] + ml[1]); p.fa[1] = __builtin_amdgcn_rcpf(l[1] + ml[3]);
          phaseFL<3>(p);
        } else {
#pragma unroll
          for (int e = 0; e < 2; ++e) {
            const float mm = fmaxf(cm[e], ml[2 * e]);
            const float wa = __builtin_amdgcn_exp2f(cm[e] - mm), wb = __builtin_amdgcn_exp2f(ml[2 * e] - mm);
            const float inv = __builtin_amdgcn_rcpf(l[e] * wa + ml[2 * e + 1] * wb);
            p.fa[e] = wa * inv; p.fb[e] = wb * inv;
          }
          phaseFL<2>(p);
        }
      }
    }
    KST(0, 10 + 3 * (it && !isY));
  }
  asm volatile(TICKET "\n\tv_xor_b32 v%c0, s100, v%c0\np_loop:" ::"i"(R_TB) : "memory");
  GAP();
  p_half<0, true>(make_iseq<32>{});
  asm volatile("s_cbranch_scc0 p_mid");
  p_half<1, true>(make_iseq<32>{});
  asm volatile("s_branch p_loop\np_loop2:");
  p_half<0, false>(make_iseq<32>{});
  asm volatile("p_mid:");
  p_half<1, false>(make_iseq<32>{});
  asm volatile("s_cbranch_scc1 p_loop2\n\ts_nop 5");
  GAP();
  p_cvt<1, 0>(); p_cvt<1, 1>(); p_cvt<1, 2>(); p_cvt<1, 3>();
  p_store<1, 1>();
  GAP();
  KST(0, 27);
  {
    constexpr unsigned rdy = 98304;
    int lz = threadIdx.x & 63; asm volatile("" : "+v"(lz));
    const int fr = lz & 15, fq = lz >> 4, qbF = isY != (solo && SWAP7) ? 15 - pj : pj;
    if (wid == 0) {
#pragma unroll 1
      for (int q = 0; q < 0x100000; ++q) {
        unsigned lo, hi, tl, th;
        asm volatile("v_readfirstlane_b32 %0, v240\n\tv_readfirstlane_b32 %1, v241\n\ts_mov_b32 %2, s84\n\ts_mov_b32 %3, s85" : "=s"(lo), "=s"(hi), "=s"(tl), "=s"(th));
        if (__builtin_expect((((lo ^ tl) >> 8) | (hi ^ th)) == 0, 1)) break;
        asm volatile("s_sleep 4\n\tv_mov_b64 v[240:241], 1\n\t" TICKET "\n\ts_waitcnt vmcnt(0)" ::: "memory");
      }
      asm volatile("v_mov_b32 v241, 0\n\ts_mov_b64 exec, 1\n\tds_write_b64 %0, v[240:241]\n\ts_mov_b64 exec, -1" ::"v"(rdy) : "memory");
    }
    vsetu<79>(fo(1024) + qbF * 4352 + fr * 8);
    {
      const int jj = 4 * (fr >> 3) + (fr & 3);
      const unsigned one = ((fq ^ (fr >> 2)) & 1) ? 0u : (jj & 1) ? 0x3f800000u : 0x3f80u;
      static_for([&](auto I) { vsetu<96 + I>((jj >> 1) == I ? one : 0u); }, make_iseq<4>{});
    }
    unsigned vo = (fq >> 1) * 1024 + (fq & 1) * 256 + fr * 16;
    bf16* out = arg_late<bf16, 13>();
    __amdgpu_buffer_rsrc_t so_ = __builtin_amdgcn_make_buffer_rsrc((void*)out, (short)0, SEQ * OUTW * 2, 0x00020000);
    unsigned vw = ((qbF * 128 + (fr & 7)) * OUTW + DA + (fr >> 3) * 32 + fq * 8) * 2;
    asm volatile("" : "+s"(so_));
    asm volatile("s_waitcnt vmcnt(0) lgkmcnt(0)\n\ts_barrier" : "+v"(vo), "+v"(vw) :: "memory");
    fl_set<T_FS>();
    unsigned tk;
    asm volatile("ds_read_b32 %0, %1\n\ts_waitcnt lgkmcnt(0)" : "=v"(tk) : "v"(rdy) : "memory");
    tk = __builtin_amdgcn_readfirstlane(tk) & 255;
    KST(0, 30);
    if (doA) post_math(pj, pu, out, lds + 131072 + wid * 8192);
#define N3 0
#define N2 12
    constexpr int N1 = 28 - 3 * N3 - 2 * N2;
    static_assert(N1 >= 0 && N3 + N2 + N1 <= 16);
    const int nu = tk < N3 ? 3 : tk < N3 + N2 ? 2 : tk < N3 + N2 + N1 ? 1 : 0;
    const int u0 = 4 * (tk < N3 ? 3 * tk : tk < N3 + N2 ? N3 + 2 * tk : 2 * N3 + N2 + tk) + wid * nu;
    KST(0, 28);
    if (nu) {
      unsigned so[3];
#pragma unroll
      for (int k = 0; k < 3; ++k) so[k] = O_S + qbF * 0x200000 + 0x40000 + (u0 + k) * 16384;
      unsigned pend = 0x5555u, cnt = 0x400000;
      while (true) {
        unsigned nw;
        if (wid == 3) {
          u64 m;
          const u64 ex = pend | (pend << 1);
          asm volatile(FL_ARM("%1") "\n\t" FL_POLL "\n\ts_waitcnt vmcnt(0)\n\tv_cmp_eq_u64 %0, s[84:85], v[124:125]" : "=s"(m) : "s"(ex) : "memory");
          nw = (unsigned)m & ((unsigned)m >> 1) & pend;
          if (nw) asm volatile("ds_write_b32 %0, %1 offset:4" ::"v"(rdy), "v"(~pend | nw) : "memory");
        } else {
          unsigned t;
          asm volatile("ds_read_b32 %0, %1 offset:4\n\ts_waitcnt lgkmcnt(0)" : "=v"(t) : "v"(rdy) : "memory");
          nw = __builtin_amdgcn_readfirstlane(t) & pend;
        }
        if (--cnt == 0) nw = pend;
        if (nw == 0) {
          if (wid == 3) asm volatile("s_sleep 4");
          else asm volatile("s_sleep 1");
          continue;
        }
        asm volatile("s_nop 4");
        static_for([&](auto S) {
          if ((nw >> (2 * S)) & 1) {
            r_load<0, 2 * S>(vo, so[0]); r_load<0, 2 * S + 1>(vo, so[0]);
            if (nu > 1) { r_load<1, 2 * S>(vo, so[1]); r_load<1, 2 * S + 1>(vo, so[1]); }
            if (nu > 2) { r_load<2, 2 * S>(vo, so[2]); r_load<2, 2 * S + 1>(vo, so[2]); }
          }
        }, make_iseq<8>{});
        pend &= ~nw;
        if (pend == 0) break;
      }
      asm volatile("s_waitcnt vmcnt(0)" ::: "memory");
      KST(0, 16);
      auto oo = [&](int U) { return ((((U >> 1) & 1) * 64 + ((U >> 2) & 3) * 16 + (U & 1) * 8) * OUTW + (U >> 4) * 64) * 2; };
      r_fin<0>(vw, so_, oo(u0));
      if (nu > 1) r_fin<1>(vw, so_, oo(u0 + 1));
      if (nu > 2) r_fin<2>(vw, so_, oo(u0 + 2));
      if (qbF == 15 && tk == 0) asm volatile(FL_SET("%0") ::"s"(fo(6144)) : "memory");
    }
    KST(0, 29);
  }
  asm volatile("s_waitcnt vmcnt(0)" ::: "memory");
  asm volatile("pf_code_end:");
  KST(0, 1);
}

extern "C" void kernel_launch(void* const* d_in, const int* in_sizes, int n_in, void* d_out, int out_size, void* d_ws,
                              size_t ws_size, hipStream_t stream) {
  const bf16* Aq = (const bf16*)d_in[0];
  const bf16* Ak = (const bf16*)d_in[1];
  const bf16* Av = (const bf16*)d_in[2];
  const bf16* Ash = (const bf16*)d_in[3];
  const bf16* Assmk = (const bf16*)d_in[4];
  const bf16* AssmD = (const bf16*)d_in[5];
  const bf16* Bx = (const bf16*)d_in[6];
  const bf16* Bckv = (const bf16*)d_in[7];
  const bf16* Bwq = (const bf16*)d_in[8];
  const bf16* Bwuk = (const bf16*)d_in[9];
  const bf16* Bwuv = (const bf16*)d_in[10];
  const bf16* Bwo = (const bf16*)d_in[11];
  bf16* out = (bf16*)d_out;
  hipFuncSetAttribute((const void*)fused_kernel, hipFuncAttributeMaxDynamicSharedMemorySize, 163840);
  fused_kernel<<<256, 256, 163840, stream>>>(Bckv, Bwuk, Bwuv, Bx, Bwq, (bf16*)d_ws, Ak, Av, Ash, Assmk, AssmD, Bwo, Aq, out);
}
```

```cpp
#include <hip/hip_runtime.h>
#include <hip/hip_bf16.h>
#include <stdint.h>
#include <utility>

typedef __hip_bfloat16 bf16;
typedef __attribute__((ext_vector_type(4))) short bf16x4;
typedef __attribute__((ext_vector_type(2))) unsigned u32x2;

constexpr int SEQ = 2048;
constexpr int DA = 448;
constexpr int NH = 16;
constexpr int HD = 256;
constexpr int DC = 1024;
constexpr int OUTW = 896;
constexpr unsigned TSB = NH * SEQ * HD * 2, O_K = TSB, O_S = 3 * TSB, O_P = 5 * TSB, PSTR = 67584, O_F = O_P + 128 * PSTR,
                   O_X = O_F + 400 * 4352, O_Z = O_X + DA * SEQ * 2, WSB = O_Z + DA * SEQ * 2;
constexpr unsigned fo(int w) { return O_F + (w >> 4) * 4352 + (w & 15) * 8; }

#define KST(kid, which)

__device__ __forceinline__ float bf2f(unsigned short u) { return __uint_as_float(((unsigned)u) << 16); }
__device__ __forceinline__ unsigned short f2bf(float f) { return __bfloat16_as_ushort(__float2bfloat16(f)); }
__device__ __forceinline__ float rbf(float f) { return bf2f(f2bf(f)); }
typedef __attribute__((ext_vector_type(2))) __bf16 bf16x2n;
typedef __attribute__((ext_vector_type(2))) float f32x2n;
__device__ __forceinline__ unsigned pk2(float a, float b) {
  return __builtin_bit_cast(unsigned, __builtin_convertvector((f32x2n){a, b}, bf16x2n));
}

__device__ __forceinline__ int off_b(int row, int ch) { return 256 * row + 16 * (ch ^ (((row & 3) << 2) | ((row >> 2) & 3))); }

template <int R> __device__ __forceinline__ void sset(unsigned x) { asm volatile("s_mov_b32 s%c0, %1" ::"i"(R), "s"(x)); }
template <int R> __device__ __forceinline__ void vsetu(unsigned x) { asm volatile("v_mov_b32 v%c0, %1" ::"i"(R), "v"(x)); }
template <int R> __device__ __forceinline__ void srd_set(const void* p, unsigned n) {
  asm volatile("s_mov_b64 s[%c0:%c1], %4\n\ts_mov_b32 s%c2, %5\n\ts_mov_b32 s%c3, 0x20000" ::"i"(R), "i"(R + 1), "i"(R + 2), "i"(R + 3), "s"(p), "s"(n));
}
template <int R, int L> __device__ __forceinline__ void tload() { asm volatile("v_readlane_b32 s%c0, v78, %1" ::"i"(R), "i"(L)); }
template <int L> __device__ __forceinline__ unsigned tget() { unsigned t; asm volatile("v_readlane_b32 %0, v78, %1" : "=s"(t) : "i"(L)); return t; }
#define FL_SET(S) "buffer_store_dwordx2 v[82:83], v81, s[88:91], " S " offen sc1"
#define FL_ARM(N) "v_cndmask_b32_e64 v80, -2.0, v79, " N
#define FL_POLL "buffer_load_dwordx2 v[124:125], v80, s[88:91], 0 offen sc1"
#define FL_CHECK(N) "s_mov_b32 s48, 0x100000\n7:\n\tv_cmp_eq_u64 vcc, s[84:85], v[124:125]\n\ts_and_b64 vcc, vcc, " N "\n\ts_cmp_eq_u64 vcc, " N \
                    "\n\ts_cbranch_scc1 8f\n\ts_sleep 2\n\t" FL_POLL "\n\ts_waitcnt vmcnt(0)\n\ts_sub_u32 s48, s48, 1\n\ts_cbranch_scc0 7b\n8:"
template <int L> __device__ __forceinline__ void fl_set() {
  asm volatile("v_readlane_b32 s48, v78, %0\n\ts_nop 4\n\t" FL_SET("s48") ::"i"(L) : "memory");
}
template <int L> __device__ __forceinline__ void fl_poll() {
  asm volatile("v_readlane_b32 s50, v78, %0\n\tv_readlane_b32 s51, v78, %1\n\ts_nop 1\n\t" FL_ARM("s[50:51]") "\n\t" FL_POLL ::"i"(L), "i"(L + 1) : "memory");
}
#define TICKET "buffer_atomic_add_x2 v[240:241], v81, s[88:91], s101 offen sc0"
__device__ __forceinline__ void fl_check() { asm volatile(FL_CHECK("s[50:51]") ::: "vcc", "scc", "memory"); }
template <int R> __device__ __forceinline__ float vget() { float t; asm volatile("v_mov_b32 %0, v%c1" : "=v"(t) : "i"(R)); return t; }
template <int R> __device__ __forceinline__ void vset(float x) { asm volatile("v_mov_b32 v%c0, %1" ::"i"(R), "v"(x)); }
template <int R> __device__ __forceinline__ float acc_read1() { float t; asm volatile("v_accvgpr_read_b32 %0, a[%c1]" : "=v"(t) : "i"(R)); return t; }
template <class F, int... I> __device__ __forceinline__ void static_for(F&& f, std::integer_sequence<int, I...>) {
  (f(std::integral_constant<int, I>{}), ...);
}
template <int... Is> using iseq = std::integer_sequence<int, Is...>;
template <int N> using make_iseq = std::make_integer_sequence<int, N>;

typedef __attribute__((ext_vector_type(4))) unsigned u32x4c;
typedef __attribute__((ext_vector_type(4))) float f32x4c;
typedef __attribute__((ext_vector_type(2))) float f32x2c;
typedef unsigned long long u64;
struct PR { u32x4c kk[7], vv[4]; };
__device__ __forceinline__ void prep_load(PR& p, int tid, int cg, int tb, const bf16* __restrict__ k, const bf16* __restrict__ v,
                                          const bf16* __restrict__ shift_k) {
  const __amdgpu_buffer_rsrc_t sk = __builtin_amdgcn_make_buffer_rsrc((void*)k, (short)0, SEQ * DA * 2, 0x00020000);
  const __amdgpu_buffer_rsrc_t sv = __builtin_amdgcn_make_buffer_rsrc((void*)v, (short)0, SEQ * DA * 2, 0x00020000);
  const __amdgpu_buffer_rsrc_t sw = __builtin_amdgcn_make_buffer_rsrc((void*)(shift_k + cg * 256), (short)0, 512, 0x00020000);
  const int vo = ((tb * 128 + (tid >> 3) * 4) * DA + cg * 64 + (tid & 7) * 8) * 2;
#pragma unroll
  for (int q = 0; q < 7; ++q)
    asm volatile("buffer_load_dwordx4 %0, %1, %2, 0 offen" : "=v"(p.kk[q]) : "v"(vo + (q - 3) * DA * 2), "s"(sk) : "memory");
#pragma unroll
  for (int q = 0; q < 4; ++q)
    asm volatile("buffer_load_dwordx4 %0, %1, %2, 0 offen offset:%3" : "=v"(p.vv[q]) : "v"(vo), "s"(sv), "i"(q * DA * 2) : "memory");
#pragma unroll
  for (int q = 0; q < 4; ++q)
    asm volatile("buffer_load_dwordx4 a[%c0:%c1], %2, %3, 0 offen offset:%4" ::"i"(4 * q), "i"(4 * q + 3), "v"((tid & 7) * 64), "s"(sw), "i"(q * 16) : "memory");
}
__device__ __forceinline__ float bfl(unsigned u) { return __uint_as_float(u << 16); }
__device__ __forceinline__ float bfh(unsigned u) { return __uint_as_float(u & 0xffff0000u); }
__device__ __forceinline__ f32x2c rbf2(f32x2c a) {
  return (f32x2c){__uint_as_float(pk2(0.f, a[0])), __uint_as_float(pk2(0.f, a[1]))};
}
template <int I> __device__ __forceinline__ void prep_math(const PR& p, int tid, char* lx) {
  const int rp = tid >> 3, c = tid & 7;
  f32x2c wt[4], kf[7];
  const unsigned w[4] = {__float_as_uint(acc_read1<4 * I>()), __float_as_uint(acc_read1<4 * I + 1>()), __float_as_uint(acc_read1<4 * I + 2>()),
                         __float_as_uint(acc_read1<4 * I + 3>())};
#pragma unroll
  for (int t = 0; t < 4; ++t)
    wt[t] = (t & 1) ? (f32x2c){bfh(w[t >> 1]), bfh(w[2 + (t >> 1)])} : (f32x2c){bfl(w[t >> 1]), bfl(w[2 + (t >> 1)])};
#pragma unroll
  for (int q = 0; q < 7; ++q) kf[q] = (f32x2c){bfl(p.kk[q][I]), bfh(p.kk[q][I])};
  f32x2c x[4];
#pragma unroll
  for (int e = 0; e < 4; ++e) {
    f32x2c s = rbf2(kf[e] * wt[3]);
    s = rbf2(s + rbf2(kf[e + 1] * wt[2]));
    s = rbf2(s + rbf2(kf[e + 2] * wt[1]));
    s = rbf2(s + rbf2(kf[e + 3] * wt[0]));
    x[e] = s * (f32x2c){bfl(p.vv[e][I]), bfh(p.vv[e][I])};
  }
#pragma unroll
  for (int u = 0; u < 2; ++u)
    *(u32x2*)(lx + (c * 8 + 2 * I + u) * 256 + (((31 - rp) * 8) ^ (c << 4))) = (u32x2){pk2(x[3][u], x[2][u]), pk2(x[1][u], x[0][u])};
}
__device__ __forceinline__ void prep_store(int tid, int cg, int tb, bf16* __restrict__ xr, const char* lx) {
  const __amdgpu_buffer_rsrc_t sx = __builtin_amdgcn_make_buffer_rsrc((void*)(xr + (size_t)cg * 64 * SEQ), (short)0, 64 * SEQ * 2, 0x00020000);
#pragma unroll
  for (int rd = 0; rd < 4; ++rd) {
    const int idx = tid + 256 * rd, ch = idx >> 4, q = idx & 15;
    u32x4c w = *(const u32x4c*)(lx + ch * 256 + 16 * (q ^ (ch >> 3)));
    __builtin_amdgcn_raw_buffer_store_b128(w, sx, (ch * SEQ + (15 - tb) * 128 + q * 8) * 2, 0, 16);
  }
}

#define CODE_TOUCH(LB, LE, wx, NW, NL, m0v) do { unsigned blo_, bhi_, elo_; \
    asm volatile("s_getpc_b64 s[48:49]\n\ts_add_u32 s48, s48, " LB "@rel32@lo+4\n\ts_addc_u32 s49, s49, " LB "@rel32@hi+12\n\t" \
                 "s_mov_b32 %0, s48\n\ts_mov_b32 %1, s49\n\t" \
                 "s_getpc_b64 s[48:49]\n\ts_add_u32 s48, s48, " LE "@rel32@lo+4\n\ts_mov_b32 %2, s48" \
                 : "=s"(blo_), "=s"(bhi_), "=s"(elo_) :: "scc"); \
    const unsigned n_ = elo_ - (blo_ & ~127u); \
    const __amdgpu_buffer_rsrc_t srd_ = __builtin_amdgcn_make_buffer_rsrc((void*)(((unsigned long long)bhi_ << 32) | (blo_ & ~127u)), (short)0, n_ < 262144u ? n_ : 0u, 0x00020000); \
    const unsigned l_ = threadIdx.x & 63, vo_ = l_ < (NL) ? ((wx) + l_ * (NW)) * 128u : 0x80000000u; \
    asm volatile("s_mov_b32 m0, %0\n\ts_nop 4\n\tbuffer_load_dword %1, %2, 0 offen lds" ::"s"(m0v), "v"(vo_), "s"(srd_) : "memory"); } while (0)
template <int I> __device__ __forceinline__ void d_store() {
  asm volatile("buffer_store_dwordx4 v[%c0:%c1], v%c2, s[88:91], s101 offen offset:%3 sc1" ::"i"(128 + 4 * I), "i"(131 + 4 * I), "i"(112 + ((I >> 2) & 1)),
               "i"((I & 3) * 1024) : "memory");
  if constexpr ((I & 7) == 7) asm volatile("s_addk_i32 s101, 0x4000" ::: "scc");
}
constexpr int NS_X = 19;
struct CV { unsigned pa[4], pb, px, vz, sz; float D; };
constexpr int cv_J(bool up, int q) { int J = up ? 0 : 7; while (q > J) { q -= J + 1; J += up ? 1 : -1; } return J; }
constexpr int cv_i(bool up, int q) { int J = up ? 0 : 7; while (q > J) { q -= J + 1; J += up ? 1 : -1; } return q; }
template <int R, int N> __device__ __forceinline__ void cv_rd(CV& c) {
  if constexpr (N < 16) {
    constexpr int i = N >> 1, o = 16 * R + 128 * (i & 1) + 2 * (N & 1), r = 32 + 32 * (R & 1) + 2 * N;
    asm volatile("ds_read2_b32 a[%c1:%c2], %0 offset0:%c3 offset1:%c4" ::"v"(c.pa[i >> 1]), "i"(r), "i"(r + 1), "i"(o), "i"(o + 1));
  } else {
    constexpr int m = N - 16, r = 192 + 32 * (R & 1) + 4 * m;
    asm volatile("ds_read_b128 a[%c1:%c2], %0 offset:%c3" ::"v"(c.pb), "i"(r), "i"(r + 3), "i"(64 * R + 512 * (7 - m)));
  }
}
template <int O> __device__ __forceinline__ void cv_xd(CV& c) {
  constexpr int e = O & 31, x = 96 + ((e >> 1) ^ 1);
  if constexpr (O >= 64) asm volatile("v_cvt_pk_bf16_f32 v%c0, 0, v%c0" ::"i"(160 + e));
  else if constexpr (O >= 32) asm volatile("v_mul_f32 v%c0, v%c0, %1" ::"i"(160 + e), "v"(c.D));
  else if constexpr (O & 1) asm volatile("v_lshlrev_b32 v%c0, 16, v%c1" ::"i"(160 + e), "i"(x));
  else asm volatile("v_and_b32 v%c0, 0xffff0000, v%c1" ::"i"(160 + e), "i"(x));
}
template <int O> __device__ __forceinline__ void cv_z(CV& c) {
  constexpr int J = O / 11, k = O % 11, y = 128 + 4 * J;
  if constexpr (k < 4) asm volatile("v_cvt_pk_bf16_f32 v%c0, 0, v%c0" ::"i"(y + k));
  else if constexpr (k < 8) asm volatile("v_add_f32 v%c0, v%c0, v%c1" ::"i"(y + k - 4), "i"(y + 28 + k));
  else if constexpr (k < 10) asm volatile("v_cvt_pk_bf16_f32 v%c0, v%c1, v%c2" ::"i"(y + k - 8), "i"(y + 2 * k - 16), "i"(y + 2 * k - 15));
  else asm volatile("buffer_store_dwordx2 v[%c0:%c1], %2, s[88:91], %3 offen offset:%4 sc1" ::"i"(y), "i"(y + 1), "v"(c.vz), "s"(c.sz),
                    "i"(512 * J) : "memory");
}
constexpr int cv_z0(int Q) {
  int o = 0;
  for (int q = 0; q < Q; ++q)
    for (int k = 0; k < (q % 4 == 1 ? 0 : 3); ++k) { const int J = o / 11; if ((J + 1) * (J + 2) / 2 - 1 + (J ? 4 : 7) <= q) ++o; }
  return o;
}
constexpr int cv_xd0(int T) { int o = 0; for (int t = 0; t < T; ++t) o += t % 4 != 1 || (t >= 72 && t < 108); return o < 96 ? o : 96; }
static_assert(cv_xd0(144) == 96);
template <int B, int... Is> __device__ __forceinline__ void cv_zs(CV& c, iseq<Is...>) { (cv_z<B + Is>(c), ...); }
template <int R, int Q> __device__ __forceinline__ void cv_slot(CV& c) {
  constexpr int i = cv_i(R == 7, Q), J = cv_J(R == 7, Q), d = 128 + 4 * J, a = 32 + 32 * (R & 1) + 4 * i, b = 192 + 32 * (R & 1) + 4 * (J - i);
  constexpr int T = 36 * R + Q, vm = Q % 4 == 1 && Q < 32 ? Q / 4 : -1;
  if constexpr (Q == 0) asm volatile("s_waitcnt lgkmcnt(0)");
  if constexpr (R >= 6 && vm >= 0 && vm % 4 == 0) asm volatile("s_add_u32 m0, s79, %0" ::"i"(vm * 1024));
  if constexpr (R == 0 && i == 0)
    asm volatile("v_mfma_f32_16x16x32_bf16 v[%c0:%c1], a[%c2:%c3], a[%c4:%c5], 0" ::"i"(d), "i"(d + 3), "i"(a), "i"(a + 3), "i"(b), "i"(b + 3));
  else
    asm volatile("v_mfma_f32_16x16x32_bf16 v[%c0:%c1], a[%c2:%c3], a[%c4:%c5], v[%c0:%c1]" ::"i"(d), "i"(d + 3), "i"(a), "i"(a + 3), "i"(b), "i"(b + 3));
  if constexpr (Q < 24 && R < 7) cv_rd<R + 1, Q>(c);
  if constexpr (T % 7 == 3 && NS_X + 5 + T / 7 < 32) d_store<NS_X + 5 + T / 7>();
  if constexpr (R == 2 && Q >= 24 && Q < 32)
    asm volatile("ds_read_b64 v[%c1:%c2], %0 offset:%c3" ::"v"(c.px), "i"(96 + 2 * (Q - 24)), "i"(97 + 2 * (Q - 24)), "i"(512 * (31 - Q)));
  if constexpr (R >= 3 && R < 7) if constexpr (cv_xd0(T - 107) > cv_xd0(T - 108)) cv_xd<cv_xd0(T - 108)>(c);
  if constexpr ((R == 3 || R == 4) && vm >= 0) {
    constexpr int f = (R - 3) * 8 + vm;
    asm volatile("buffer_load_dwordx4 a[%c0:%c1], v84, s[88:91], s96 offen offset:%2 sc1" ::"i"(128 + 4 * f), "i"(131 + 4 * f), "i"((f & 3) * 1024) : "memory");
    if constexpr ((f & 3) == 3) asm volatile("s_addk_i32 s96, %0" ::"i"(f == 7 ? 28672 : 4096));
  }
  if constexpr (R == 4 && Q == 35) asm volatile(FL_POLL ::: "memory");
  if constexpr (R >= 6 && vm >= 0)
    asm volatile("buffer_load_dwordx4 v%c0, s[88:91], s98 offen offset:%1 sc1 lds" ::"i"(86 + vm / 4), "i"((vm & 3) * 1024) : "memory");
  if constexpr (R == 6 && Q == 35) asm volatile("s_add_u32 s98, s98, 0x8000\n\ts_xor_b32 s79, s79, 0x8000");
  if constexpr (R == 7) cv_zs<cv_z0(Q)>(c, make_iseq<cv_z0(Q + 1) - cv_z0(Q)>{});
  __builtin_amdgcn_sched_barrier(0);
}
template <int R, int... Q> __device__ __forceinline__ void cv_step(CV& c, iseq<Q...>) { (cv_slot<R, Q>(c), ...); }
template <int... N> __device__ __forceinline__ void cv_rd0(CV& c, iseq<N...>) { (cv_rd<0, N>(c), ...); }
template <int FLAG> __device__ __forceinline__ void conv_wave(unsigned sz, float Dd, char* lw) {
  int lane = threadIdx.x & 63; asm volatile("" : "+v"(lane));
  const int l16 = lane & 15, fq = lane >> 4;
  u32x4c w[5]; unsigned w4[5];
#pragma unroll
  for (int r = 0; r < 5; ++r) {
    w[r] = *(const u32x4c*)(lw + r * 1024 + lane * 16);
    w4[r] = *(const unsigned*)(lw + r * 1024 + lane * 16 + 16);
  }
  d_store<NS_X>(); d_store<NS_X + 1>();
#pragma unroll
  for (int r = 0; r < 5; ++r)
    *(u32x4c*)(lw + 10304 + r * 1024 + lane * 16) = (u32x4c){__builtin_amdgcn_alignbit(w[r][1], w[r][0], 16), __builtin_amdgcn_alignbit(w[r][2], w[r][1], 16),
                                                             __builtin_amdgcn_alignbit(w[r][3], w[r][2], 16), __builtin_amdgcn_alignbit(w4[r], w[r][3], 16)};
  d_store<NS_X + 2>();
  CV c;
#pragma unroll
  for (int i = 0; i < 4; ++i) c.pa[i] = (unsigned)(size_t)lw + ((l16 + 1) & 1) * 10304 + 4 * ((8 * fq + l16 + 1) >> 1) + 1024 * i;
  c.pb = (unsigned)(size_t)lw + 5120 + (2032 - 16 * l16 + 8 * fq) * 2 - 512 * 7;
  c.vz = (16 * l16 + 4 * fq) * 2;
  c.px = (unsigned)(size_t)lw + 5120 + 2044 * 2 - 512 * 7 - c.vz;
  c.sz = sz; c.D = Dd;
  asm volatile("" : "+v"(c.vz), "+v"(c.px), "+v"(c.D) :: "memory");
  asm volatile("" : "+s"(c.sz));
  cv_rd0(c, make_iseq<24>{});
  d_store<NS_X + 3>(); d_store<NS_X + 4>();
  __builtin_amdgcn_sched_barrier(0);
  static_for([&](auto R) __attribute__((always_inline)) {
    cv_step<R>(c, make_iseq<36>{});
    if constexpr (R == 2) {
      asm volatile("s_waitcnt vmcnt(0)\n\ts_barrier" ::: "memory");
      fl_set<FLAG>();
    }
    if constexpr (R == 5) {
      asm volatile("s_waitcnt vmcnt(0)" ::: "memory");
      fl_check();
    }
    __builtin_amdgcn_sched_barrier(0);
  }, make_iseq<8>{});
  asm volatile("s_nop 7");
  cv_zs<cv_z0(36)>(c, make_iseq<88 - cv_z0(36)>{});
}

__device__ __forceinline__ void post_dma(int cg, int tb, const bf16* __restrict__ q, unsigned m0w) {
  int lane = threadIdx.x & 63; asm volatile("" : "+v"(lane));
  const __amdgpu_buffer_rsrc_t sq = __builtin_amdgcn_make_buffer_rsrc((void*)(q + (size_t)tb * 32 * DA + cg * 64), (short)0, 32 * DA * 2, 0x00020000);
  const unsigned vq = ((lane >> 3) * DA + (lane & 7) * 8) * 2;
  asm volatile("s_mov_b32 m0, %0\n\ts_nop 4\n\tbuffer_load_dwordx4 %1, %2, 0 offen lds\n\tbuffer_load_dwordx4 %1, %2, %3 offen offset:1024 lds\n\t"
               "buffer_load_dwordx4 %1, %2, %4 offen offset:2048 lds\n\tbuffer_load_dwordx4 %1, %2, %5 offen offset:3072 lds"
               ::"s"(m0w + 4096), "v"(vq), "s"(sq), "s"(8 * DA * 2 - 1024), "s"(16 * DA * 2 - 2048), "s"(24 * DA * 2 - 3072) : "memory");
}
__device__ __forceinline__ void post_math(int cg, int tb, bf16* __restrict__ out, const char* lw) {
  int lane = threadIdx.x & 63; asm volatile("" : "+v"(lane));
  const int tl = lane >> 3, ck = lane & 7;
  const __amdgpu_buffer_rsrc_t so = __builtin_amdgcn_make_buffer_rsrc((void*)(out + (size_t)tb * 32 * OUTW + cg * 64), (short)0, 32 * OUTW * 2 - 2 * DA, 0x00020000);
  const unsigned vo = (tl * OUTW + ck * 8) * 2;
#pragma unroll
  for (int i = 0; i < 4; ++i) {
    const u32x4c qv = *(const u32x4c*)(lw + 4096 + i * 1024 + lane * 16);
    const char* lz = lw + ck * 64 + 16 * (i ^ (ck >> 1)) + tl * 2;
    u32x4c o;
#pragma unroll
    for (int e = 0; e < 4; ++e) {
      const float z0 = bf2f(*(const unsigned short*)(lz + 2 * e * 512));
      const float z1 = bf2f(*(const unsigned short*)(lz + (2 * e + 1) * 512));
      o[e] = pk2(bfl(qv[e]) * z0, bfh(qv[e]) * z1);
    }
    __builtin_amdgcn_raw_buffer_store_b128(o, so, vo + i * 8 * OUTW * 2, 0, 17);
  }
}

#define DI __device__ __forceinline__
#define GAP() __builtin_amdgcn_sched_barrier(0)
typedef __attribute__((ext_vector_type(4))) float f32x4g;
typedef __attribute__((ext_vector_type(4))) unsigned u32x4g;
constexpr int LDW = NH * HD;
struct GM { unsigned aA[2], aB[2][8]; };
template <bool W, int J, int V> DI void g_dma() {
  if constexpr (J == 0) asm volatile("buffer_load_dwordx4 v%c0, s[%c1:%c2], 0 offen lds" ::"i"(V), "i"(W ? 60 : 56), "i"(W ? 63 : 59) : "memory");
  else asm volatile("buffer_load_dwordx4 v%c0, s[%c1:%c2], s%c3 offen lds" ::"i"(V), "i"(W ? 60 : 56), "i"(W ? 63 : 59), "i"((W ? 70 : 63) + J) : "memory");
}
template <int P, int KS, int I> DI void g_wread(GM& g) {
  constexpr int nf = I >> 1, hf = I & 1, r = 192 - 64 * P + 8 * nf + 4 * KS + 2 * hf;
  asm volatile("ds_read_b64_tr_b16 a[%c1:%c2], %0 offset:%c3" ::"v"(g.aB[hf][nf]), "i"(r), "i"(r + 1), "i"(P * 32768 + KS * 16384));
}
template <int P, int M, int KS> DI void g_aread(GM& g) {
  constexpr int r = 96 + 8 * (M & 1) + 4 * KS;
  asm volatile("ds_read_b128 v[%c1:%c2], %0 offset:%c3" ::"v"(g.aA[KS]), "i"(r), "i"(r + 3), "i"(P * 32768 + M * 2048));
}
constexpr float CS = 0.0625f * 1.4426950408889634f;
constexpr int NS_Q = 19;
constexpr int e_vm(int G) { return (G * 3) % 8 < 3 ? G * 3 / 8 : -1; }
constexpr int e_st(int MODE, int G) {
  const int q = G & 15, k3 = ((G >> 4) - 1) * 3 + (q == 0 ? 0 : q == 4 ? 1 : 2), k2 = NS_Q + (G >> 4) * 2 + (q == 4);
  if (MODE == 5) return e_vm(G) >= 32 ? e_vm(G) - 32 : -1;
  if (MODE == 3) return G >= 33 && (G - 33) % 5 == 0 && (G - 33) / 5 < NS_X ? (G - 33) / 5 : -1;
  if (MODE == 2) return G >= 30 && (q == 0 || q == 4 || q == 14) && k3 - 2 < NS_Q ? k3 - 2 : -1;
  if (MODE == 4) return (q == 0 || q == 4) && k2 < 32 ? k2 : -1;
  return -1;
}
constexpr int e_nst(int MODE, int G0, int G1) { int n = 0; for (int G = G0; G < G1; ++G) n += e_st(MODE, G) >= 0; return n; }
static_assert(e_nst(2, 0, 128) == NS_Q && e_nst(3, 30, 128) == NS_X && e_nst(5, 0, 128) == 16 && e_nst(4, 0, 104) == 32 - NS_Q);
constexpr int e_slot(int MODE, int G) { return MODE == 3 ? (G % 3 == 0 && G < 30 ? G / 3 : -1) : e_vm(G) >= 0 && e_vm(G) < 32 ? 10 + e_vm(G) : -1; }
struct ES { int op0[129], nop[129]; };
constexpr ES e_sched(bool TR, int MODE) {
  ES z{};
  const bool SCL = MODE == 2 || MODE == 4;
  const int per = SCL ? 16 : 8, o1 = 32 * per;
  int o = MODE == 4 ? e_sched(false, 2).op0[128] : 0;
  for (int G = 0; G <= 128; ++G) {
    const int q = G & 15;
    int cap = 2 - (q == 1 || q == 2 || q == 15);
    if (SCL) cap -= (q >= 3 && q < 11 && (q & 1)) + (q == 7 || q == 11) + 2 * (G == 70 || G == 74 || (G & 63) == 61);
    cap += MODE == 2 ? 4 + (q == 10 || q == 13) : MODE == 3 ? 1 : MODE == 5 ? 2 : 0;
    if (e_st(MODE, G) >= 0 || (SCL ? q == 8 || q == 12 : e_slot(MODE, G) >= 0) || (MODE == 4 && G < 4)) cap = 0;
    if (G == 128) cap = 10000;
    z.op0[G] = o;
    while (o < o1) {
      const int i = o / per, rdy = MODE == 4 ? 0 : (TR ? 32 * (i >> 3) + 16 + 2 * (i & 7) + 1 : 4 * i + 3) + 6;
      if ((G < rdy && G < 128) || cap-- <= 0) break;
      ++o; ++z.nop[G];
    }
  }
  return z;
}
template <bool TR, int MODE> constexpr ES ESCH = e_sched(TR, MODE);
static_assert(ESCH<false, 4>.op0[96] == 512 && ESCH<false, 4>.op0[0] >= 24 * 16);
template <bool TR, bool SCL, int O> DI void e_op() {
  constexpr int per = SCL ? 16 : 8, i = O / per, k = O % per, r = 128 + 4 * i, t = 114 + 4 * (i & 1), c = k - (per - 4);
  if constexpr (k < 4) asm volatile("v_accvgpr_read_b32 v%c0, a[%c1]" ::"i"(t + k), "i"(4 * i + k));
  else if constexpr (c < 0) asm volatile("v_mul_f32 v%c0, s83, v%c0" ::"i"(k < 8 ? r + (k ^ 6) : t + k - 8));
  else {
    constexpr int d = c == 0 ? r + 3 : c == 1 ? r + 2 : r + c - 2, x = c == 0 ? r + 2 : c == 1 ? r : t + 2 * (c - 2);
    asm volatile("v_cvt_pk_bf16_f32 v%c0, v%c1, v%c2" ::"i"(d), "i"(x), "i"(x + 1));
  }
}
template <bool TR, bool SCL, int B, int... Is> DI void e_ops(iseq<Is...>) { (e_op<TR, SCL, B + Is>(), ...); }
template <int S> DI void e_m0() {
  if constexpr (S == 0 || S == 1 || S == 5 || S == 9) asm volatile("s_add_u32 m0, s78, %0" ::"i"(S * 1024));
  if constexpr (S >= 26 && (S - 26) % 4 == 0) asm volatile("s_add_u32 m0, s79, %0" ::"i"(((S - 26) >> 3) * 32768 + ((S - 26) & 4) * 1024));
}
template <int S> DI void e_load() {
  if constexpr (S == 0) asm volatile("buffer_load_dwordx4 v122, s[56:59], 0 offen lds" ::: "memory");
  else if constexpr (S < 5) asm volatile("buffer_load_dwordx4 v84, s[56:59], s64 offen offset:%0 lds" ::"i"((S - 1) * 1024) : "memory");
  else if constexpr (S < 9) asm volatile("buffer_load_dwordx4 v84, s[60:63], 0 offen offset:%0 sc1 lds" ::"i"((S - 5) * 1024) : "memory");
  else if constexpr (S == 9) asm volatile("buffer_load_dwordx4 v84, s[60:63], s62 offen sc1 lds" ::: "memory");
  else if constexpr (S < 26) {
    constexpr int f = S - 10;
    asm volatile("buffer_load_dwordx4 a[%c0:%c1], v84, s[88:91], s96 offen offset:%2 sc1" ::"i"(128 + 4 * f), "i"(131 + 4 * f), "i"((f & 3) * 1024) : "memory");
    if constexpr ((f & 3) == 3) asm volatile("s_addk_i32 s96, %0" ::"i"(f == 7 ? 28672 : 4096));
  } else {
    constexpr int f = S - 26;
    asm volatile("buffer_load_dwordx4 v%c0, s[88:91], s98 offen offset:%1 sc1 lds" ::"i"(86 + ((f >> 2) & 1)), "i"((f & 3) * 1024) : "memory");
    if constexpr (f == 7) asm volatile("s_add_u32 s98, s98, s97");
    if constexpr (f == 15) asm volatile(FL_POLL ::: "memory");
  }
}
template <bool TR, int P, int H, int G, int MODE> DI void g_gap(GM& g) {
  constexpr int m = 4 * H + (G >> 4), q = G & 15, n = q >> 1, ks = q & 1, GG = 64 * H + G;
  constexpr bool inV = TR ? m & 1 : n & 1, END = MODE == 3 || MODE == 5;
  constexpr int a = 96 + 8 * (m & 1) + 4 * ks, b = 192 - 64 * P + 8 * n + 4 * ks, d = (inV ? 128 : 0) + 4 * (TR ? 8 * (m >> 1) + n : 4 * m + (n >> 1));
  constexpr int J = END ? -1 : q == 8 ? 2 * (G >> 4) : q == 12 ? 2 * (G >> 4) + 1 : -1, gj = (J & 3) ^ ((J & 3) >> 1);
  if constexpr (END) { if constexpr (q == 0 && GG > 0) asm volatile("s_waitcnt lgkmcnt(0)"); e_m0<e_slot(MODE, GG)>(); }
  else if constexpr (q == 0 && G > 0) asm volatile("s_waitcnt lgkmcnt(4)");
  if constexpr (J == 0) asm volatile("s_add_u32 m0, s78, %0" ::"i"((P ^ H ^ 1) * 32768 + (1 - H) * 8192));
  else if constexpr (J == 4) asm volatile("s_add_u32 m0, s79, %0" ::"i"((P ^ H) * 32768 + (1 - H) * 16384));
  else if constexpr (J > 0 && (J & 3) == 1) asm volatile("s_bitset1_b32 m0, 12");
  else if constexpr (J > 0 && (J & 3) == 2) asm volatile("s_bitset1_b32 m0, %0" ::"i"(J < 4 ? 14 : 13));
  else if constexpr (J > 0) asm volatile("s_bitset0_b32 m0, 12");
#define GMF(D, X, Y, C) asm volatile("v_mfma_f32_16x16x32_bf16 " D "[%c0:%c1], " X ", " Y ", " C ::"i"(d), "i"(d + 3), "i"(a), "i"(a + 3), "i"(b), "i"(b + 3))
#define GMF2(D, C) do { if constexpr (TR) GMF(D, "v[%c2:%c3]", "a[%c4:%c5]", C); else GMF(D, "a[%c4:%c5]", "v[%c2:%c3]", C); } while (0)
  if constexpr ((MODE == 1 || MODE == 4) && ks == 0) { if constexpr (inV) GMF2("v", "0"); else GMF2("a", "0"); }
  else if constexpr (inV) GMF2("v", "v[%c0:%c1]");
  else GMF2("a", "a[%c0:%c1]");
#undef GMF2
#undef GMF
  if constexpr ((q == 1 || q == 2) && !(END && m == 7)) g_aread<(m < 7 ? P : P ^ 1), (m + 1) & 7, q - 1>(g);
  if constexpr (!END && q >= 3 && q < 11 && (q & 1)) g_wread<P ^ 1, H, (G >> 4) * 4 + (q - 3) / 2>(g);
  if constexpr (e_st(MODE, GG) >= 0) {
    static_assert(ESCH<TR, MODE>.op0[GG] >= (e_st(MODE, GG) + 1) * (END ? 8 : 16));
    d_store<e_st(MODE, GG)>();
  }
  if constexpr (J >= 0 && J < 4) g_dma<false, (1 - H) * 2 + (gj & 1) + (gj >> 1) * 4, 90>();
  else if constexpr (J >= 4) g_dma<true, (1 - H) * 4 + gj, 91>();
  if constexpr (MODE >= 2) e_ops<TR, !END, ESCH<TR, MODE>.op0[GG]>(make_iseq<ESCH<TR, MODE>.nop[GG]>{});
  if constexpr (END) {
    if constexpr (e_slot(MODE, GG) >= 0) e_load<e_slot(MODE, GG)>();
  } else {
    if constexpr (H == 1 && G == 6) asm volatile("v_lshl_add_u32 v90, s82, 7, v92\n\ts_and_b32 s82, s80, 15");
    if constexpr (H == 1 && G == 10) asm volatile("s_lshr_b64 s[80:81], s[80:81], 4\n\tv_lshl_add_u32 v91, s82, 19, v93");
    if constexpr (G == 61) {
      constexpr int nst = e_nst(MODE, H ? 29 : 0, GG + 1) + (MODE == 4 && !H ? e_nst(2, 93, 128) : 0);
      asm volatile("s_waitcnt vmcnt(%0) lgkmcnt(0)\n\ts_barrier" ::"i"(12 + nst) : "memory");
    }
  }
  if constexpr (END && GG == 127) {
    asm volatile("s_nop 7");
    e_ops<TR, false, ESCH<TR, MODE>.op0[128]>(make_iseq<ESCH<TR, MODE>.nop[128]>{});
  }
  GAP();
}
template <bool TR, int P, int H, int MODE, int... Gs> DI void g_half_(GM& g, iseq<Gs...>) { (g_gap<TR, P, H, Gs, MODE>(g), ...); }
template <bool TR, int P, int H, int MODE> DI void g_half(GM& g) { g_half_<TR, P, H, MODE>(g, make_iseq<64>{}); }
template <bool TR, int P, int MODE> DI void g_tile(GM& g) { g_half<TR, P, 0, MODE>(g); g_half<TR, P, 1, MODE>(g); }
template <int P, int... Is> DI void g_read0(GM& g, iseq<Is...>) {
  (g_wread<P, 0, Is>(g), ...); (g_wread<P, 1, Is>(g), ...);
  g_aread<P, 0, 0>(g); g_aread<P, 0, 1>(g);
}
DI void g_proW(unsigned g, unsigned mW) {
  asm volatile("v_lshl_add_u32 v91, %0, 18, v93\n\ts_mov_b32 m0, %1" ::"s"(g), "s"(mW));
  asm volatile("s_nop 0"); g_dma<true, 0, 91>();
  asm volatile("s_bitset1_b32 m0, 12\n\ts_nop 0"); g_dma<true, 1, 91>();
  asm volatile("s_bitset1_b32 m0, 13\n\ts_nop 0"); g_dma<true, 3, 91>();
  asm volatile("s_bitset0_b32 m0, 12\n\ts_nop 0"); g_dma<true, 2, 91>();
}
template <int PART = 3> DI void g_pro(unsigned g, unsigned mA, unsigned mW, unsigned lda) {
  g &= 3;
  if constexpr (PART & 1)
    asm volatile("v_add_u32 v90, %0, v92\n\ts_mov_b32 m0, %1" ::"s"((g >> 1) * 128 + (g & 1) * 128 * lda), "s"((mA ^ (g >> 1) * 32768) + (g & 1) * 8192));
  if constexpr (PART == 1) return;
  asm volatile("s_nop 0"); g_dma<false, 0, 90>();
  asm volatile("s_bitset1_b32 m0, 12\n\ts_nop 0"); g_dma<false, 1, 90>();
  asm volatile("s_bitset1_b32 m0, 14\n\ts_nop 0"); g_dma<false, 5, 90>();
  asm volatile("s_bitset0_b32 m0, 12\n\ts_nop 0"); g_dma<false, 4, 90>();
  g_proW(g, mW ^ g * 16384);
}
template <bool TR, bool SCL, class HOOK> DI void g_loop(GM& g, unsigned evm, HOOK&& hook) {
  constexpr int nkt = SCL ? 7 : 16, P0 = SCL ? 0 : 1;
  asm volatile("" : "+s"(evm));
#pragma unroll 1
  for (int t = 1;; t += 2) {
    g_half<TR, P0 ^ 1, 0, 0>(g);
    if (__builtin_expect((evm >> t) & 1, 0)) { hook(t); GAP(); }
    g_half<TR, P0 ^ 1, 1, 0>(g);
    if (SCL && t + 2 >= nkt) break;
    g_half<TR, P0, 0, 0>(g);
    if constexpr (SCL) { if (__builtin_expect(t == 3, 0)) { hook(4); GAP(); } }
    else asm volatile("s_cmp_eq_u32 %0, 13\n\ts_cselect_b32 s78, 0x28000, s78" ::"s"(t) : "scc");
    g_half<TR, P0, 1, 0>(g);
    if (!SCL && t + 3 >= nkt) break;
  }
}

typedef __attribute__((ext_vector_type(4))) unsigned u32x4;
typedef __attribute__((ext_vector_type(4))) float f32x4;

constexpr int R_S = 128, R_P = 192, R_CI = 224, R_E = 96;
constexpr int R_TS = 112, R_RS = 114, R_M = 116;
constexpr int R_PD = 118, R_THR = 120, R_TMP = 121;
constexpr int r_dk(int q) { return q < 6 ? 90 + q : 120 + q; }
constexpr int S_V = 96, S_T = 97, S_K = 98, S_I = 99, S_EV = 100;
struct AC {
  unsigned rare;
  unsigned tb, vs, o0, o1;
};
DI bool redo() { u64 t; asm volatile("s_mov_b64 %0, s[54:55]" : "=s"(t)); return t != 0; }
template <int X, int F> DI void kread() {
  constexpr int r = 192 + 4 * (F & 15);
  asm volatile("ds_read_b128 a[%c0:%c1], v84 offset:%c2" ::"i"(r), "i"(r + 3), "i"(X * 32768 + F * 1024));
}
template <int X, int F> DI void vread() {
  constexpr int r = 192 + 4 * (F & 15);
  asm volatile("ds_read_b128 a[%c0:%c1], v85 offset:%c2" ::"i"(r), "i"(r + 3), "i"(X * 32768 + (F & 1) * 16384 + (F >> 1) * 1024));
}
template <int X, int... Fs> DI void kreads(iseq<Fs...>) { (kread<X, Fs>(), ...); }
template <int X, int J> DI void dma_m0() {
  if constexpr (J == 0) asm volatile("s_mov_b32 m0, s%c0" ::"i"(78 + X));
  else if constexpr (J == 4) asm volatile("s_or_b32 m0, s%c0, 0x1000" ::"i"(78 + X));
  else if constexpr (J == 8) asm volatile("s_bitset0_b32 m0, 16");
  else if constexpr (J == 12) asm volatile("s_bitset0_b32 m0, 12");
}
template <int J> DI void dma_at() {
  if constexpr (J < 8)
    asm volatile("buffer_load_dwordx4 v%c0, s[88:91], s96 offen offset:%1 sc1 lds" ::"i"(86 + (J >= 4)), "i"((J & 3) * 1024) : "memory");
  else
    asm volatile("buffer_load_dwordx4 v%c0, s[92:95], s98 offen offset:%1 sc1 lds" ::"i"(r_dk(J - 8)), "i"((J & 3) * 1024) : "memory");
}
template <int Q> DI void dma4k() {
  asm volatile("s_xor_b32 m0, s79, %0\n\ts_add_u32 s48, s98, %1\n\tbuffer_load_dwordx4 v%c2, s[88:91], s48 offen sc1 lds\n\t"
               "buffer_load_dwordx4 v%c2, s[88:91], s48 offen offset:1024 sc1 lds\n\tbuffer_load_dwordx4 v%c2, s[88:91], s48 offen offset:2048 sc1 lds\n\t"
               "buffer_load_dwordx4 v%c2, s[88:91], s48 offen offset:3072 sc1 lds"
               ::"i"((Q >> 1) * 32768 + (Q & 1) * 4096), "i"((Q >> 1) * 32768), "i"(86 + (Q & 1)) : "memory", "scc");
}
template <int Q> DI void acc_zero2() {
  asm volatile("v_accvgpr_write_b32 a[%c0], 0\n\tv_accvgpr_write_b32 a[%c1], 0" ::"i"(2 * Q), "i"(2 * Q + 1));
}
#define SMV 0
struct SMS { int ex[128], op0[128], nop[128], last; };
constexpr SMS sm_sched() {
  SMS z{};
  int et[32] = {}, e = 0;
  for (int t = 0; t < 128; ++t) {
    const int g = t & 15;
    const bool x = t < 48 ? (g >= 8 && (g & 1) && g < 15) : t < 64 ? (g >= 9 && g < 15) : (g >= 8 && g < 14);
    z.ex[t] = x && e < 32 ? e : -1;
    if (z.ex[t] >= 0) et[e++] = t;
  }
  int o = 0;
  for (int t = 0; t < 128; ++t) {
    const int g = t & 15;
    int cap = z.ex[t] >= 0 || t < 8 || (t < 48 && (g == 7 || (g >= 8 && !(g & 1)))) || t == 56 || t == 63 || t == 111 ? 0 : 1;
    z.op0[t] = o; z.nop[t] = 0;
    while (cap > 0 && o < 48 && et[2 * (o / 3) + (o % 3 == 0 ? 0 : 1)] + 2 <= t) {
      const int el = 2 * (o / 3) + o % 3;
      if (!(o % 3 < 2 && (el == 0 || el == 4))) --cap;
      ++o; ++z.nop[t]; z.last = t;
    }
  }
  for (int k = 16; k < 32; ++k) if (z.op0[et[k]] < 3 * ((k - 16) / 2 + 1)) z.last = 1000;
  return z;
}
constexpr SMS SM = sm_sched();
static_assert(SM.last <= 117);
constexpr int p_reg(int Y, int k) { return R_P + 16 * Y + 4 * ((k >> 3) * 2 + ((k >> 1) & 1)) + ((k >> 2) & 1) * 2 + (k & 1); }
template <int Y, int O> DI void sm_op() {
  constexpr int k = O / 3, e = 2 * k + O % 3, ts = R_TS + ((e >> 2) & 1);
  if constexpr (O % 3 == 2) asm volatile("v_cvt_pk_bf16_f32 v%c0, v%c1, v%c2" ::"i"(p_reg(Y, k)), "i"(R_E + ((2 * k) & 15)), "i"(R_E + ((2 * k + 1) & 15)));
  else if constexpr (e == 1 || e == 5) asm volatile("v_add_f32_e32 v%c0, v%c1, v%c2" ::"i"(ts), "i"(R_E + e - 1), "i"(R_E + e));
  else if constexpr (e != 0 && e != 4) asm volatile("v_add_f32_e32 v%c0, v%c0, v%c1" ::"i"(ts), "i"(R_E + (e & 15)));
}
template <int Y, int T> DI void sm_gap(AC& c) {
  if constexpr (SM.ex[T] >= 0) asm volatile("v_exp_f32 v%c0, v%c1" ::"i"(R_E + (SM.ex[T] & 15)), "i"(R_S + 32 * Y + SM.ex[T]));
  if constexpr (SM.nop[T] > 0) sm_op<Y, SM.op0[T]>();
  if constexpr (SM.nop[T] > 1) sm_op<Y, SM.op0[T] + 1>();
  if constexpr (SM.nop[T] > 2) sm_op<Y, SM.op0[T] + 2>();
  if constexpr (T == SM.last + 1) asm volatile("v_add_f32_e32 v%c0, v%c1, v%c2" ::"i"(R_TMP), "i"(R_TS), "i"(R_TS + 1));
  if constexpr (T == SM.last + 2) asm volatile("v_cmp_nle_f32_e64 s[54:55], v%c0, v%c1" ::"i"(R_TMP), "i"(R_THR));
}
#define W_A0 1
template <int X, int MODE, int G> DI void gapA(AC& c) {
  constexpr bool FIRST = MODE == 1 || MODE == 2, NOQK = MODE == 3;
  constexpr int Xp = X ^ 1, kb = G >> 4, g = G & 15, qb = g >> 3, s = g & 7;
  if constexpr (MODE == 3 && G < 48 && G % 3 == 0) {
    constexpr int f = G / 3;
    asm volatile("buffer_load_dwordx4 a[%c0:%c1], v84, s[88:91], s75 offen offset:%2 sc1" ::"i"(128 + 4 * f), "i"(131 + 4 * f), "i"((f & 3) * 1024) : "memory");
    if constexpr ((f & 3) == 3) asm volatile("s_addk_i32 s75, %0" ::"i"(f == 7 ? 28672 : 4096));
  }
  if constexpr (G == 48) asm volatile("s_waitcnt vmcnt(%0) lgkmcnt(0)\n\ts_barrier" ::"i"(MODE == 3 ? 16 : MODE == 2 ? 12 : MODE == 1 ? W_A0 : 0) : "memory");
  else if constexpr (g == 0 && !NOQK) asm volatile("s_waitcnt lgkmcnt(0)");
  if constexpr (!NOQK) {
    constexpr int r = 192 + 4 * ((kb & 1) * 8 + s), q = 128 + 4 * g;
    constexpr int d = R_S + 32 * X + 4 * (kb * 2 + qb), ci = s == 0 ? R_CI + 4 * (kb * 2 + qb) : d;
    if constexpr (FIRST && s == 0)
      asm volatile("v_mfma_f32_16x16x32_bf16 v[%c0:%c1], a[%c2:%c3], a[%c4:%c5], 0" ::"i"(d), "i"(d + 3), "i"(r), "i"(r + 3), "i"(q), "i"(q + 3));
    else
      asm volatile("v_mfma_f32_16x16x32_bf16 v[%c0:%c1], a[%c2:%c3], a[%c4:%c5], v[%c6:%c7]" ::"i"(d), "i"(d + 3), "i"(r), "i"(r + 3), "i"(q), "i"(q + 3),
                   "i"(ci), "i"(ci + 3));
    if constexpr (g < 8 && kb < 3) kread<X, (kb + 1) * 8 + g>();
    constexpr int S0 = SMV ? 40 : 14, S1 = SMV ? 41 : 30, S2 = SMV ? 42 : 45, S3 = SMV ? 43 : 46;
    if constexpr (G == S0) asm volatile("s_add_u32 s96, s97, 0x1000000");
    if constexpr (G == S1) dma_m0<X, 0>();
    if constexpr (G == S2) asm volatile("s_mov_b32 s97, s98");
    if constexpr (G == S3) asm volatile("s_add_u32 s98, s98, s99");
    if constexpr (G >= 56 && !(G & 1)) dma_at<(G - 56) / 2>();
    if constexpr (G == 57) asm volatile("s_lshr_b32 s100, s100, 1\n\ts_add_u32 s56, s56, 1");
    if constexpr (G == 59 && !FIRST) asm volatile("s_and_b32 s49, s100, 1\n\ts_or_b32 s49, s49, s54");
    if constexpr (G == 61 && !FIRST) asm volatile("s_or_b32 %0, s49, s55" : "=s"(c.rare));
    if constexpr (G == 61 && FIRST) asm volatile("s_and_b32 %0, s100, 1" : "=s"(c.rare));
  } else if constexpr (SM.nop[64 + G] > 0) asm volatile("s_nop 0");
  if constexpr (!FIRST && g < 8 && kb == 3) vread<Xp, g>();
  if constexpr (MODE == 2 && (G & 3) == 1) d_store<16 + G / 4>();
  if constexpr (FIRST) acc_zero2<G>();
  else sm_gap<Xp, 64 + G>(c);
  GAP();
}
template <int R> DI void rescale1(float alpha) {
  float t;
  asm volatile("v_accvgpr_read_b32 %0, a[%c2]\n\tv_mul_f32 %0, %0, %1\n\tv_accvgpr_write_b32 a[%c2], %0" : "=&v"(t) : "v"(alpha), "i"(R));
}
template <int... Rs> DI void rescale_all(const float (&alpha)[2], iseq<Rs...>) { (rescale1<Rs>(alpha[(Rs >> 2) & 1]), ...); }
template <bool MAX> DI float row_reduce(float v) {
  auto p = __builtin_amdgcn_permlane32_swap(__float_as_uint(v), __float_as_uint(v), false, false);
  v = MAX ? fmaxf(__uint_as_float(p[0]), __uint_as_float(p[1])) : __uint_as_float(p[0]) + __uint_as_float(p[1]);
  p = __builtin_amdgcn_permlane16_swap(__float_as_uint(v), __float_as_uint(v), false, false);
  return MAX ? fmaxf(__uint_as_float(p[0]), __uint_as_float(p[1])) : __uint_as_float(p[0]) + __uint_as_float(p[1]);
}
template <int Y, int QB> DI float row_max() {
  constexpr int r = R_S + 32 * Y + 4 * QB;
  float mx;
  asm volatile("v_max_f32 %0, v%c1, v%c2\n\tv_max3_f32 %0, %0, v%c3, v%c4" : "=&v"(mx) : "i"(r), "i"(r + 1), "i"(r + 2), "i"(r + 3));
  static_for([&](auto K) {
    constexpr int q = r + 8 * (K + 1);
    float t = mx;
    asm volatile("v_max3_f32 %0, %0, v%c1, v%c2\n\tv_max3_f32 %0, %0, v%c3, v%c4" : "+v"(t) : "i"(q), "i"(q + 1), "i"(q + 2), "i"(q + 3));
    mx = t;
  }, make_iseq<3>{});
  return row_reduce<true>(mx);
}
template <int R> DI void v_sub(float d) { asm volatile("v_sub_f32 v%c0, v%c0, %1" ::"i"(R), "v"(d)); }
template <int B> DI void s_shift(const float (&d)[2]) { static_for([&](auto E) { v_sub<B + E>(d[(E >> 2) & 1]); }, make_iseq<32>{}); }
template <bool CI> DI void m_shift(const float (&d)[2]) {
  if constexpr (CI) s_shift<R_CI>(d);
  v_sub<R_M>(-d[0]); v_sub<R_M + 1>(-d[1]);
}
DI void set_mask0() {
  const int mr = __float_as_int(vget<89>());
  static_for([&](auto I) {
    constexpr int e = (I >> 2) * 8 + (I & 3), k = (I >> 2) * 16 + (I & 3);
    const float x = vget<R_S + e>();
    vset<R_S + e>(k > mr ? -INFINITY : x);
  }, make_iseq<16>{});
}
template <int X, bool CI = true> DI void sm_redo() {
  constexpr int Xp = X ^ 1;
  float d[2], dt[2], alpha[2], ts[2] = {0.f, 0.f};
  static_for([&](auto Q) {
    constexpr int qb = Q;
    const float pd = vget<R_PD + qb>();
    d[qb] = fmaxf(row_max<Xp, qb>() - pd, 0.f);
    dt[qb] = d[qb] + pd;
    vset<R_PD + qb>(d[qb]);
    alpha[qb] = __builtin_amdgcn_exp2f(-d[qb]);
    vset<R_RS + qb>(vget<R_RS + qb>() * alpha[qb]);
  }, make_iseq<2>{});
  vset<R_THR>(d[0] > 0.f || d[1] > 0.f ? -1.f : 4096.f);
  m_shift<CI>(d);
  static_for([&](auto K) {
    constexpr int k = K, qb = (k >> 1) & 1;
    const float e0 = __builtin_amdgcn_exp2f(vget<R_S + 32 * Xp + 2 * k>() - dt[qb]), e1 = __builtin_amdgcn_exp2f(vget<R_S + 32 * Xp + 2 * k + 1>() - dt[qb]);
    ts[qb] += e0; ts[qb] += e1;
    vset<p_reg(Xp, k)>(__uint_as_float(pk2(e0, e1)));
  }, make_iseq<16>{});
  vset<R_TS>(ts[0]); vset<R_TS + 1>(ts[1]);
  rescale_all(alpha, make_iseq<128>{});
  asm volatile("s_nop 1" ::: "memory");
  GAP();
}
template <int X, int MODE, int... Gs> DI void phaseA_(AC& c, iseq<Gs...>) { (gapA<X, MODE, Gs>(c), ...); }
template <int X, int MODE> DI void phaseA(AC& c) { phaseA_<X, MODE>(c, make_iseq<64>{}); }

template <int X, bool SMX, int G> DI void gapB(AC& c) {
  constexpr int Xp = X ^ 1, blk = G >> 4, g = G & 15;
  if constexpr (g == 0) asm volatile("s_waitcnt lgkmcnt(0)");
  constexpr int qb = (g >> 1) & 1, s = g & 1, r = 192 + 4 * ((blk & 1) * 8 + (g >> 2) * 2 + s), o = 4 * ((blk * 4 + (g >> 2)) * 2 + qb);
  constexpr int p = R_P + 16 * Xp + 4 * (s * 2 + qb);
  asm volatile("v_mfma_f32_16x16x32_bf16 a[%c0:%c1], a[%c2:%c3], v[%c4:%c5], a[%c0:%c1]" ::"i"(o), "i"(o + 3), "i"(r), "i"(r + 3), "i"(p), "i"(p + 3));
  if constexpr (g < 8) {
    if constexpr (blk < 3) vread<Xp, (blk + 1) * 8 + g>();
    else if constexpr (SMX) kread<Xp, g>();
  }
  if constexpr (SMX) {
    if constexpr (G == 1 || G == 2) asm volatile("v_add_f32_e32 v%c0, v%c0, v%c1" ::"i"(R_RS + G - 1), "i"(R_TS + G - 1));
    if constexpr (g == 7 && blk < 3) dma_m0<X, blk * 4 + 4>();
    if constexpr (g >= 8 && !(g & 1) && blk < 3) dma_at<blk * 4 + 4 + (g - 8) / 2>();
    if constexpr (G == 55) asm volatile("s_cmp_eq_u32 s56, s76");
    if constexpr (G == 3) asm volatile("s_lshr_b32 s77, s77, 1");
    if constexpr (G == 4) asm volatile("s_bitcmp1_b32 s77, 0");
    if constexpr (G == 5) asm volatile("s_cselect_b64 s[52:53], s[62:63], 0");
    if constexpr (G == 6) asm volatile(FL_ARM("s[52:53]"));
    if constexpr (G == 56) asm volatile(FL_POLL ::: "memory");
    sm_gap<X, G>(c);
  }
  GAP();
}
template <int X, int... Gs> DI void phaseB_(AC& c, iseq<Gs...>) { (gapB<X, true, Gs>(c), ...); }
template <int X> DI void phaseB(AC& c) { phaseB_<X>(c, make_iseq<64>{}); }
constexpr int sm_nex() { int n = 0; for (int t = 0; t < 64; ++t) n += SM.ex[t] >= 0; return n; }
template <int X> DI void phaseB0(unsigned mode) {
  dma_m0<X, 4>();
  kreads<X ^ 1>(make_iseq<8>{});
  dma_at<4>();
  asm volatile("s_nop 3");
  float d[2];
  if (mode & 2) {
    d[0] = vget<122>(); d[1] = vget<123>();
    dma_at<5>();
  } else {
    if constexpr (X == 0) if (__builtin_expect(mode & 4, 0)) set_mask0();
    d[0] = row_max<X, 0>(); GAP(); dma_at<5>();
    d[1] = row_max<X, 1>();
#pragma unroll
    for (int qb = 0; qb < 2; ++qb) if (!(d[qb] > -3.0e38f)) d[qb] = 0.f;
  }
  GAP();
  vset<R_M>(d[0]); vset<R_M + 1>(d[1]);
  dma_at<6>();
  const f32x2c nd[2] = {{-d[0], -d[0]}, {-d[1], -d[1]}};
  static_for([&](auto E) {
    constexpr int e = E;
    const f32x2c v = nd[(e >> 1) & 1];
    asm volatile("v_pk_add_f32 v[%c0:%c1], v[%c0:%c1], %4\n\tv_mov_b64 v[%c2:%c3], %4" ::"i"(R_S + 32 * X + 2 * e), "i"(R_S + 32 * X + 2 * e + 1), "i"(R_CI + 2 * e), "i"(R_CI + 2 * e + 1), "v"(v));
    if constexpr (e == 3) dma_at<7>();
    if constexpr (e == 6) dma_m0<X, 8>();
    if constexpr (e == 7 || e == 11 || e == 15) dma_at<8 + (e - 7) / 4>();
  }, make_iseq<16>{});
  static_assert(SM.op0[64] >= 3 * ((sm_nex() - 15) / 2) && SM.op0[64] <= 24);
  static_for([&](auto E) {
    constexpr int e = E;
    if constexpr (e < sm_nex()) asm volatile("v_exp_f32 v%c0, v%c1" ::"i"(R_E + e), "i"(R_S + 32 * X + e));
    if constexpr (e == 3) dma_at<11>();
    if constexpr (e == 6) dma_m0<X, 12>();
    if constexpr (e == 7 || e == 11 || e == 15) dma_at<12 + (e - 7) / 4>();
  }, make_iseq<16>{});
  static_for([&](auto O) { sm_op<X, O>(); }, make_iseq<SM.op0[64]>{});
  dma_at<15>();
  static_for([&](auto E) { asm volatile("v_exp_f32 v%c0, v%c1" ::"i"(R_E + (int)E), "i"(R_S + 32 * X + 16 + (int)E)); }, make_iseq<(sm_nex() > 16 ? sm_nex() - 16 : 0)>{});
  asm volatile("buffer_store_dwordx2 v[%c0:%c1], v88, s[88:91], s76 offen sc1\n\ts_bitcmp1_b32 s77, 0\n\ts_cselect_b64 s[52:53], s[62:63], 0\n\t"
               FL_ARM("s[52:53]") "\n\t" FL_POLL ::"i"(R_M), "i"(R_M + 1) : "memory", "scc");
  GAP();
}
enum : int { T_Q0, T_K0, T_OQ, T_OG, T_FPREP, T_FQ, T_FG, T_FS, T_ZO, T_CNT, T_NPRE = 10, T_NST = 12, T_TAB = 14, T_SLAB = 16, T_PART, T_IT = 18, T_ITN = 23 };
#define MSK(R, K) "v_cmp_gt_i32 s[48:49], " #K ", v89\n\tv_cmp_gt_i32 s[50:51], " #K "+1, v89\n\tv_cmp_gt_i32 s[52:53], " #K "+2, v89\n\t" \
                  "v_cmp_gt_i32 s[54:55], " #K "+3, v89\n\tv_cndmask_b32_e64 v[" #R "], v[" #R "], v122, s[48:49]\n\t" \
                  "v_cndmask_b32_e64 v[" #R "+1], v[" #R "+1], v122, s[50:51]\n\tv_cndmask_b32_e64 v[" #R "+2], v[" #R "+2], v122, s[52:53]\n\t" \
                  "v_cndmask_b32_e64 v[" #R "+3], v[" #R "+3], v122, s[54:55]\n\t"
DI void ev_code() {
  asm volatile(
    "s_getpc_b64 s[82:83]\n\ts_add_u32 s82, s82, 12\n\ts_addc_u32 s83, s83, 0\n\ts_branch 9f\n\t"
    "s_bitcmp1_b32 s60, s56\n\ts_cbranch_scc1 3f\n\ts_bitcmp1_b32 s74, s56\n\ts_cbranch_scc1 2f\n\t"
    "s_bitcmp0_b32 s57, s56\n\ts_cbranch_scc1 1f\n\ts_bitcmp1_b32 s58, s56\n\ts_cselect_b64 vcc, -1, 0\n\tv_cndmask_b32 v121, -2.0, v81, vcc\n\t"
    "buffer_store_dwordx2 v[82:83], v121, s[88:91], s70 offen sc1\n\tbuffer_store_dwordx2 v[82:83], v121, s[88:91], s71 offen sc1\n\t"
    "v_cmp_eq_u64 vcc, s[84:85], v[124:125]\n\ts_andn2_b64 vcc, s[62:63], vcc\n\ts_cbranch_scc1 5f\n"
    "0:\n\ts_add_u32 s98, s98, s59\n\ts_mov_b32 s59, s61\n\ts_mov_b32 s61, s72\n\ts_mov_b64 s[62:63], s[64:65]\n\ts_mov_b64 s[64:65], s[66:67]\n\t"
    "s_mov_b64 s[66:67], s[68:69]\n"
    "1:\n\ts_setpc_b64 s[86:87]\n"
    "5:\n\t" FL_CHECK("s[62:63]") "\n\ts_branch 0b\n"
    "6:\n\t" FL_CHECK("s[62:63]") "\n\ts_setpc_b64 s[86:87]\n"
    "2:\n\ts_mov_b32 s98, 0\n\t"
    "v_lshrrev_b32 v90, 7, v86\n\tv_and_b32 v91, 6, v90\n\tv_lshlrev_b32 v91, 4, v91\n\tv_and_b32 v92, 0x70, v86\n\tv_xor_b32 v91, v91, v92\n\t"
    "s_movk_i32 s48, 896\n\tv_mad_u32_u24 v94, v90, s48, v91\n\ts_mov_b64 s[92:93], s[80:81]\n\ts_mov_b32 s94, 229376\n\t"
    "v_add_u32 v90, 28672, v94\n\tv_add_u32 v91, 34816, v94\n\tv_add_u32 v92, 40960, v94\n\tv_add_u32 v93, 47104, v94\n\t"
    "v_add_u32 v95, 6144, v94\n\tv_add_u32 v126, 12288, v94\n\tv_add_u32 v127, 18432, v94\n\t"
    "v_mov_b32 v122, 0xff800000\n\t" MSK(228, 0) MSK(236, 16) MSK(244, 32) MSK(252, 48)
    "v_cmp_eq_u64 vcc, s[84:85], v[124:125]\n\ts_andn2_b64 vcc, s[62:63], vcc\n\ts_cbranch_scc1 6b\n\ts_setpc_b64 s[86:87]\n"
    "3:\n\tv_mov_b32 v122, 0xff800000\n\t" MSK(224, 0) MSK(232, 16) MSK(240, 32) MSK(248, 48)
    "v_readlane_b32 s54, v78, %0\n\ts_add_u32 m0, s78, 0x10000\n\t"
    "s_bfe_u32 s53, s73, 0x80010\n\ts_mulk_i32 s53, 3840\n\ts_mov_b64 s[48:49], s[80:81]\n\ts_add_u32 s50, s53, 3840\n\ts_min_u32 s50, s50, 229376\n\t"
    "s_mov_b32 s51, 0x20000\n\t"
    "v_lshlrev_b32 v121, 3, v84\n\tbuffer_load_dword v121, s[48:51], s53 offen lds\n\t"
    "s_bitcmp0_b32 s73, 3\n\ts_cbranch_scc1 4f\n\t"
    "v_lshlrev_b32 v121, 9, v84\n\tv_and_b32 v121, 0x38000, v121\n\tv_lshrrev_b32 v122, 9, v84\n\tv_lshl_add_u32 v121, v122, 12, v121\n\t"
    "v_lshrrev_b32 v122, 3, v84\n\tv_xor_b32 v122, v122, v84\n\tv_and_or_b32 v121, v122, 48, v121\n\t"
    "buffer_load_dwordx4 v121, s[88:91], s54 offen sc1 lds\n\ts_addk_i32 s54, 7168\n\t"
    "buffer_load_dwordx4 v121, s[88:91], s54 offen offset:1024 sc1 lds\n\ts_addk_i32 s54, 7168\n\t"
    "buffer_load_dwordx4 v121, s[88:91], s54 offen offset:2048 sc1 lds\n\ts_addk_i32 s54, 7168\n\t"
    "buffer_load_dwordx4 v121, s[88:91], s54 offen offset:3072 sc1 lds\n"
    "4:\n\ts_setpc_b64 s[86:87]\n9:"
    ::"i"((int)T_ZO) : "scc");
}
DI void ev_call() { asm volatile("s_swappc_b64 s[86:87], s[82:83]" ::: "vcc", "scc", "memory"); GAP(); }

#define ACLOB_ALL "a0","a1","a2","a3","a4","a5","a6","a7","a8","a9","a10","a11","a12","a13","a14","a15","a16","a17","a18","a19","a20","a21","a22","a23","a24","a25","a26","a27","a28","a29","a30","a31","a32","a33","a34","a35","a36","a37","a38","a39","a40","a41","a42","a43","a44","a45","a46","a47","a48","a49","a50","a51","a52","a53","a54","a55","a56","a57","a58","a59","a60","a61","a62","a63","a64","a65","a66","a67","a68","a69","a70","a71","a72","a73","a74","a75","a76","a77","a78","a79","a80","a81","a82","a83","a84","a85","a86","a87","a88","a89","a90","a91","a92","a93","a94","a95","a96","a97","a98","a99","a100","a101","a102","a103","a104","a105","a106","a107","a108","a109","a110","a111","a112","a113","a114","a115","a116","a117","a118","a119","a120","a121","a122","a123","a124","a125","a126","a127","a128","a129","a130","a131","a132","a133","a134","a135","a136","a137","a138","a139","a140","a141","a142","a143","a144","a145","a146","a147","a148","a149","a150","a151","a152","a153","a154","a155","a156","a157","a158","a159","a160","a161","a162","a163","a164","a165","a166","a167","a168","a169","a170","a171","a172","a173","a174","a175","a176","a177","a178","a179","a180","a181","a182","a183","a184","a185","a186","a187","a188","a189","a190","a191","a192","a193","a194","a195","a196","a197","a198","a199","a200","a201","a202","a203","a204","a205","a206","a207","a208","a209","a210","a211","a212","a213","a214","a215","a216","a217","a218","a219","a220","a221","a222","a223","a224","a225","a226","a227","a228","a229","a230","a231","a232","a233","a234","a235","a236","a237","a238","a239","a240","a241","a242","a243","a244","a245","a246","a247","a248","a249","a250","a251","a252","a253","a254","a255"

struct EP { float fa[2], fb[2]; };
template <int I, int E, int MODE> DI void epi_pair(const EP& p) {
  constexpr int qb = I & 1, r = 16 * (I >> 1) + 4 * qb + (E >> 1) * 8 + (E & 1) * 2, t = 96 + 2 * E, w = 128 + 4 * I + E;
  if constexpr (MODE == 2) asm volatile("v_accvgpr_read_b32 v106, a[%c0]" ::"i"(w));
  asm volatile("v_accvgpr_read_b32 v%c0, a[%c2]\n\tv_accvgpr_read_b32 v%c1, a[%c3]" ::"i"(t), "i"(t + 1), "i"(r), "i"(r + 1));
  if constexpr (MODE) asm volatile("v_mul_f32 v%c0, v%c0, %2\n\tv_mul_f32 v%c1, v%c1, %2" ::"i"(t), "i"(t + 1), "v"(p.fa[qb]));
  if constexpr (MODE == 2)
    asm volatile("v_lshlrev_b32 v104, 16, v106\n\tv_and_b32 v105, 0xffff0000, v106\n\tv_fmac_f32 v%c0, v104, %2\n\tv_fmac_f32 v%c1, v105, %2"
                 ::"i"(t), "i"(t + 1), "v"(p.fb[qb]));
  asm volatile("v_cvt_pk_bf16_f32 v%c0, v%c1, v%c2" ::"i"(w), "i"(t), "i"(t + 1));
}
template <int I> DI void epi_store() {
  asm volatile("buffer_store_dwordx4 v[%c0:%c1], v84, s[88:91], s101 offen offset:%2 sc1" ::"i"(128 + 4 * I), "i"(131 + 4 * I), "i"((I & 3) * 1024) : "memory");
  if constexpr ((I & 3) == 3) asm volatile("s_addk_i32 s101, 4096");
}
constexpr int R_TB = 232, R_VS = 236;
template <int HF, int R> DI void p_read() {
  constexpr int mb = R >> 4, r = 192 - 64 * HF + 2 * R;
  asm volatile("ds_read_b64_tr_b16 a[%c1:%c2], v%c0 offset:%c3" ::"i"(R_TB + 2 * HF + mb), "i"(r), "i"(r + 1), "i"((R & 15) * 2048));
}
template <int QB, int E> DI void p_cvt() {
  asm volatile("v_cvt_pk_bf16_f32 v%c0, v%c1, v%c2" ::"i"(208 + 4 * QB + E), "i"(192 + 8 * QB + 2 * E), "i"(193 + 8 * QB + 2 * E));
}
template <int QB, int PR> DI void p_store() {
  asm volatile("buffer_store_dwordx4 v[%c0:%c1], v%c2, s[88:91], s96 offen offset:%3 sc1" ::"i"(208 + 4 * QB), "i"(211 + 4 * QB), "i"(R_VS + QB),
               "i"(PR * 128) : "memory");
}
constexpr int p_dma(int HF, int G) { return G == 3 && !HF ? 0 : G == 11 ? 1 - HF : G == 15 ? 2 - HF : G == 19 ? 3 - HF : G == 27 && HF ? 3 : -1; }
template <int HF, bool LD, int G> DI void p_gap() {
  constexpr int ch = G >> 3, t = G & 7, a = 192 - 64 * HF + 32 * (ch & 1) + 4 * t, b = 128 + 4 * (2 * t + (ch >> 1)), d = 192 + 4 * ch;
  if constexpr (t == 0)
    asm volatile("v_mfma_f32_16x16x32_bf16 v[%c0:%c1], a[%c2:%c3], v[%c4:%c5], 0" ::"i"(d), "i"(d + 3), "i"(a), "i"(a + 3), "i"(b), "i"(b + 3));
  else
    asm volatile("v_mfma_f32_16x16x32_bf16 v[%c0:%c1], a[%c2:%c3], v[%c4:%c5], v[%c0:%c1]" ::"i"(d), "i"(d + 3), "i"(a), "i"(a + 3), "i"(b), "i"(b + 3));
  p_read<HF ^ 1, G>();
  if constexpr (G == 0) asm volatile("v_xor_b32 v%c0, s100, v%c0" ::"i"(R_TB + 2 * HF + 1));
  if constexpr (G == 17) asm volatile("v_xor_b32 v%c0, s100, v%c0" ::"i"(R_TB + 2 * (HF ^ 1)));
  if constexpr (G >= 4 && G <= 8 && G != 7) p_cvt<1, (G < 7 ? G - 4 : 3)>();
  if constexpr (G == 24 || (G == 7 && HF == 0)) asm volatile("s_waitcnt lgkmcnt(8)");
  if constexpr (G == 7 && HF == 1) asm volatile("s_waitcnt vmcnt(%0) lgkmcnt(8)\n\ts_barrier" ::"i"(LD ? 13 : 5) : "memory");
  if constexpr (G == 10) p_store<1, HF ^ 1>();
  if constexpr (G >= 20 && G < 24) p_cvt<0, G - 20>();
  if constexpr (G == 25) p_store<0, HF>();
  if constexpr (LD && p_dma(HF, G) >= 0) dma_at<(HF ? 8 : 12) + p_dma(HF, G)>();
  if constexpr (LD && HF == 0 && G == 2) asm volatile("s_bitset0_b32 m0, 12");
  if constexpr (HF == 0 && G == 13) asm volatile("s_add_u32 s96, s96, 0x8000");
  if constexpr (HF == 0 && G == 26) asm volatile("s_add_u32 s98, s98, 128");
  if constexpr (LD && HF == 0 && G == 27) asm volatile("s_cmp_lt_u32 s98, 0x380");
  if constexpr (HF == 1 && G == 1) asm volatile("s_xor_b32 s100, s100, 0x10000");
  if constexpr (LD && HF == 1 && G == 2) asm volatile("s_xor_b32 s97, s97, s100");
  if constexpr (LD && HF == 1 && G == 9) asm volatile("s_or_b32 m0, s97, 0x1000");
  if constexpr (!LD && HF == 1 && G == 30) asm volatile("s_cmp_lt_u32 s98, 0x500");
  GAP();
}
template <int HF, bool LD, int... Gs> DI void p_half(iseq<Gs...>) { (p_gap<HF, LD, Gs>(), ...); }
template <int G> DI void gapF(AC& c, const EP& p) {
  if constexpr (G < 64) gapB<0, false, G>(c);
  if constexpr (G >= 10) epi_pair<(G - 10) / 4, (G - 10) & 3, 0>(p);
  if constexpr (G >= 14 && ((G - 14) & 3) == 0) epi_store<(G - 14) / 4>();
  if constexpr (G == 73) epi_store<15>();
  GAP();
}
template <int... Gs> DI void phaseF(AC& c, const EP& p, iseq<Gs...>) { (gapF<Gs>(c, p), ...); }
template <int G> DI void gapAL(AC& c) {
  constexpr int kb = G >> 3, s = G & 7, r = 192 + 4 * (G & 15), q = 160 + 4 * s, d = R_S + 36 + 8 * kb, ci = s == 0 ? R_CI + 4 + 8 * kb : d;
  if constexpr (G == 0) asm volatile("s_add_u32 s96, s97, 0x1000000\n\ts_mov_b32 m0, s79\n\ts_waitcnt vmcnt(9) lgkmcnt(0)\n\ts_barrier" ::: "memory");
  if constexpr (G >= 8 && (G & 3) == 0) asm volatile("s_waitcnt lgkmcnt(4)");
  if constexpr (G == 29) asm volatile("s_barrier" ::: "memory");
  asm volatile("v_mfma_f32_16x16x32_bf16 v[%c0:%c1], a[%c2:%c3], a[%c4:%c5], v[%c6:%c7]" ::"i"(d), "i"(d + 3), "i"(r), "i"(r + 3), "i"(q), "i"(q + 3),
               "i"(ci), "i"(ci + 3));
  if constexpr (G < 24) kread<1, G + 8>();
  else vread<0, G - 24>();
  if constexpr (G % 3 == 1 && G < 24) dma_at<G / 3>();
  if constexpr (G == 11) asm volatile("s_bitset1_b32 m0, 12");
  if constexpr (G == 28) asm volatile("buffer_load_dwordx4 v[122:125], v88, s[88:91], s76 offen sc1" ::: "memory");
  if constexpr (G == 30) asm volatile("s_xor_b32 m0, s79, 0x11000\n\ts_movk_i32 s98, 128");
  sm_gap<0, 64 + 2 * G>(c); sm_gap<0, 65 + 2 * G>(c);
  GAP();
}
template <int... Gs> DI void phaseAL(AC& c, iseq<Gs...>) { (gapAL<Gs>(c), ...); }
struct BLS { int ex[65], op[65], vm[65], su[65]; };
constexpr BLS bl_sched(bool LD) {
  BLS z{};
  z.op[0] = 1;
  for (int t = 0; t < 64; ++t) {
    const int g = t & 15, o = z.op[t];
    const bool x = t == 6 || (t < 48 && g > 8 && g != 12 && g != 15);
    const bool v = LD ? (t & 3) == 0 || g == 7 || t == 5 || t == 58 || t == 62 || t == 63 : (t & 11) == 8;
    const bool p = t >= 15 && o < 24 && (t & 3) != 0 && t != 47 && !x && z.ex[t - 1] > 2 * (o / 3) + (o % 3 != 0);
    z.ex[t + 1] = z.ex[t] + x; z.vm[t + 1] = z.vm[t] + v; z.op[t + 1] = o + p;
    z.su[t + 1] = z.su[t] + (t >= 1 && z.su[t] < (LD ? 16 : 10) && g != 15 && !v && !x && (LD || t > 55 || (t > 3 && !p)));
  }
  return z;
}
template <bool LD> constexpr BLS BL = bl_sched(LD);
static_assert(BL<true>.ex[64] == 16 && BL<true>.op[61] == 24 && BL<true>.vm[64] == 24 && BL<false>.vm[64] == 8);
static_assert(BL<true>.su[56] == 16 && BL<false>.su[62] == 10);
template <bool LD, int G> DI void gapBL(AC& c) {
  if constexpr (G == 48) asm volatile("s_waitcnt vmcnt(%0) lgkmcnt(0)\n\ts_barrier" ::"i"(BL<LD>.vm[48]) : "memory");
  gapB<1, false, G>(c);
  if constexpr (G >= 48 && G < 56) vread<1, G - 48>();
  if constexpr (BL<LD>.vm[G + 1] > BL<LD>.vm[G]) {
    constexpr int k = BL<LD>.vm[G] + (LD ? 0 : 16), I = 2 * (k & 7) + ((k >> 3) & 1);
    if constexpr (k < 16) {
      asm volatile("buffer_load_dwordx4 a[%c0:%c1], v84, s[88:91], s75 offen offset:%2 sc1" ::"i"(128 + 4 * I), "i"(131 + 4 * I), "i"((I & 3) * 1024) : "memory");
      if constexpr (k & 1) asm volatile("s_addk_i32 s75, %0" ::"i"(k == 7 ? -12288 : 4096));
    } else dma_at<k - 8>();
    if constexpr (k == 19) asm volatile("s_bitset0_b32 m0, 12");
  }
  if constexpr (G == 1 || G == 2) asm volatile("v_add_f32_e32 v%c0, v%c0, v%c1" ::"i"(R_RS + G - 1), "i"(R_TS + G - 1));
  if constexpr (G == 3) asm volatile("v_mov_b32 v%c0, 0" ::"i"(R_TS));
  if constexpr (BL<LD>.ex[G + 1] > BL<LD>.ex[G]) {
    constexpr int j = BL<LD>.ex[G];
    asm volatile("v_exp_f32 v%c0, v%c1" ::"i"(R_E + j), "i"(R_S + 36 + 8 * (j >> 2) + (j & 3)));
  }
  if constexpr (BL<LD>.op[G + 1] > BL<LD>.op[G]) {
    constexpr int o = BL<LD>.op[G], k = o / 3, j = 2 * k + o % 3;
    if constexpr (o % 3 == 2) asm volatile("v_cvt_pk_bf16_f32 v%c0, v%c1, v%c2" ::"i"(p_reg(1, (k >> 1) * 4 + 2 + (k & 1))), "i"(R_E + 2 * k), "i"(R_E + 2 * k + 1));
    else asm volatile("v_add_f32_e32 v%c0, v%c1, v%c2" ::"i"(R_TS + 1), "i"(j == 1 ? R_E : R_TS + 1), "i"(R_E + j));
  }
  if constexpr (G == 61) asm volatile("v_cmp_nle_f32_e64 s[54:55], v%c0, v%c1" ::"i"(R_TS + 1), "i"(R_THR));
  if constexpr (BL<LD>.su[G + 1] > BL<LD>.su[G]) {
    constexpr int k = BL<LD>.su[G];
    if constexpr (k < 4) asm volatile("v_xor_b32 v%c0, %1, %2" ::"i"(R_TB + k), "i"(32 * k), "v"(c.tb));
    else if constexpr (k < 6) asm volatile("v_add_u32 v%c0, %1, %2" ::"i"(R_VS + k - 4), "i"(4096 * (k - 4)), "v"(c.vs));
    else if constexpr (k == 6) tload<96, T_SLAB>();
    else if constexpr (k == 7) tload<101, T_CNT>();
    else if constexpr (k == 8) asm volatile("v_mov_b64 v[240:241], 1");
    else if constexpr (k == 9) asm volatile("s_mov_b32 s97, s79\n\ts_mov_b32 s100, 0x8000");
    else if constexpr (k < 14) asm volatile("v_mov_b32 v%c0, %1" ::"i"(k < 12 ? 214 + k : 218 + k), "v"(k & 1 ? c.o1 : c.o0));
    else asm volatile("v_mov_b64 v[%c0:%c1], 0" ::"i"(198 + 2 * k), "i"(199 + 2 * k));
  }
  GAP();
}
template <bool LD, int... Gs> DI void phaseBL(AC& c, iseq<Gs...>) { (gapBL<LD, Gs>(c), ...); }
template <int MODE, int G> DI void gapFL(const EP& p) {
  constexpr int x = G - (MODE == 3 ? 4 : 0), J = G < 24 && G % 3 == 1 ? G / 3 : -1;
  if constexpr (G == 0) asm volatile("s_or_b32 m0, s78, 0x1000\n\ts_movk_i32 s98, 256\n\ts_waitcnt vmcnt(8) lgkmcnt(0)" ::: "memory");
  if constexpr (G >= 8 && G < 32 && (G & 3) == 0) asm volatile("s_waitcnt lgkmcnt(%0)" ::"i"(G < 28 ? 4 : 0));
  if constexpr (G < 32) {
    constexpr int o = 8 * (G >> 1) + 4, r = 192 + 4 * (G & 15), q = R_P + 20 + 8 * (G & 1);
    asm volatile("v_mfma_f32_16x16x32_bf16 a[%c0:%c1], a[%c2:%c3], v[%c4:%c5], a[%c0:%c1]" ::"i"(o), "i"(o + 3), "i"(r), "i"(r + 3), "i"(q), "i"(q + 3));
  }
  if constexpr (G < 24) vread<1, G + 8>();
  if constexpr (G == 28) asm volatile("s_waitcnt vmcnt(8)\n\ts_barrier\n\ts_or_b32 m0, s79, 0x1000\n\ts_movk_i32 s98, 384" ::: "memory");
  if constexpr (G == 29 || G == 31) dma_at<8 + (G - 29) / 2>();
  if constexpr (G == 32 || G == 40) {
    static_for([&](auto I) { p_read<0, 2 * (G - 32) + I>(); }, make_iseq<16>{});
    dma_at<10 + (G - 32) / 8>();
  }
  if constexpr (J >= 0) dma_at<8 + J>();
  if constexpr (x >= 0) epi_pair<2 * ((x >> 2) & 7) + (x >> 5), x & 3, MODE>(p);
  if constexpr (MODE == 3 && G < 32) {
    constexpr int db = G & 15, qb = G >> 4, o = 4 * (db * 2 + qb), a = 224 + 4 * (db & 1), b = 128 + 4 * ((db >> 1) * 2 + qb);
    asm volatile("v_mfma_f32_16x16x32_bf16 a[%c0:%c1], v[%c2:%c3], a[%c4:%c5], a[%c0:%c1]" ::"i"(o), "i"(o + 3), "i"(a), "i"(a + 3), "i"(b), "i"(b + 3));
  }
  if constexpr (J == 3) asm volatile("s_bitset0_b32 m0, 12");
  if constexpr (x == 63) asm volatile("s_waitcnt vmcnt(4) lgkmcnt(0)" ::: "memory");
  GAP();
}
template <int MODE, int... Gs> DI void phaseFL_(const EP& p, iseq<Gs...>) { (gapFL<MODE, Gs>(p), ...); }
template <int MODE> DI void phaseFL(const EP& p) { GAP(); phaseFL_<MODE>(p, make_iseq<MODE == 3 ? 68 : 64>{}); }
template <int U, int K> DI void r_load(unsigned vo, unsigned so) {
  asm volatile("buffer_load_dwordx4 a[%c0:%c1], %2, s[88:91], %3 offen offset:%4 sc1" ::"i"(64 * U + 4 * K), "i"(64 * U + 4 * K + 3), "v"(vo),
               "s"(so + (K >> 1) * 0x40000 + (U >> 1) * 4096), "i"((K & 1) * 512 + (U & 1) * 2048) : "memory");
}
template <int U, int K> DI void r_sum() {
  constexpr int d = 100 + 4 * (K & 1), b = 64 * U + 4 * K;
  if constexpr (K < 2) asm volatile("v_mfma_f32_16x16x32_bf16 v[%c0:%c1], v[96:99], a[%c2:%c3], 0" ::"i"(d), "i"(d + 3), "i"(b), "i"(b + 3));
  else asm volatile("v_mfma_f32_16x16x32_bf16 v[%c0:%c1], v[96:99], a[%c2:%c3], v[%c0:%c1]" ::"i"(d), "i"(d + 3), "i"(b), "i"(b + 3));
}
template <int U> DI void r_fin(unsigned vo, __amdgpu_buffer_rsrc_t srd, unsigned so) {
  asm volatile("" : "+s"(srd), "+s"(so));
  static_for([&](auto K) { r_sum<U, K>(); }, make_iseq<16>{});
  asm volatile("s_nop 7");
  static_for([&](auto E) { asm volatile("v_cvt_pk_bf16_f32 v%c0, v%c1, v%c2" ::"i"(116 + 4 * U + E), "i"(100 + 2 * E), "i"(101 + 2 * E)); }, make_iseq<4>{});
  asm volatile("buffer_store_dwordx4 v[%c3:%c4], %0, %1, %2 offen sc0 sc1" ::"v"(vo), "s"(srd), "s"(so), "i"(116 + 4 * U), "i"(119 + 4 * U) : "memory");
}

#define CMAX 12
#define BAL 0
#define NYE (BAL ? 16 : 15)
#define NYO (BAL ? 14 : 15)
#define NY0 15
struct TRow { unsigned v[64]; };
constexpr u64 kvmask(int m0, int m1) { u64 n = 0; for (int m = m0; m <= m1; ++m) n |= 0x101ull << m; return n; }
constexpr u64 k_order(bool isY, int rr) {
  const int cls = (rr + (rr >> 2)) & 3;
  return !isY ? (cls == 0 ? 0xfedcb9875a463210ull : cls == 1 ? 0xefadc6982b574310ull : cls == 2 ? 0xfebad7693c285410ull : 0xefcba8764d392510ull)
              : (cls == 0 ? 0xfedcb987543a6210ull : cls == 1 ? 0xefadc698254b7310ull : cls == 2 ? 0xfebad769325c8410ull : 0xefcba876432d9510ull);
}
constexpr TRow t_row(int b, int wid) {
  TRow t{};
  const int j = b & 7, r = b >> 3, rr = r & 15, hsel = rr & 1, pj = rr >> 1, kv = pj & 1, h = j + 8 * hsel, u = hsel * 8 + j;
  const bool isY = r >= 16, noA = pj == 7, slow = BAL && (j & 1);
  const int pa = slow && (pj == 0 || pj == 7) ? 7 - pj : pj;
  const bool solo = pa == 7;
  const int jF = isY || !BAL ? j : slow && pa == 0 ? j - 1 : !slow && pa == 6 ? j + 1 : j, hF = jF + 8 * hsel;
  const int qbF = isY ? (solo ? (slow ? 6 : 7) : 15 - pa) : solo ? 8 : !BAL ? pa : slow ? (pa ? pa - 1 : 0) : pa + 1;
  const int mt = (rr >> 2) + (isY ? 4 : 0), g = j * 128, gF = jF * 128, NY = !(j & 1) ? NYE : pa ? NYO : NY0;
  const int nx0 = solo ? 0 : 32 - 2 * pa - NY, cx = nx0 < CMAX ? nx0 : CMAX, ex = nx0 - cx, qb0 = solo ? qbF : 15 - pa;
  const unsigned oQ = (unsigned)h << 20, oK = oQ + O_K, oP = O_P + (h * 8 + pa) * PSTR, oQF = (unsigned)hF << 20;
  if (wid == 4) {
    for (int l = 0; l < 64; ++l)
      t.v[l] = fo(l < 16 ? g + hsel * 16 + l : l == 16 ? g + 32 + hsel * 8 + (qb0 >> 1) : l == 17 ? gF + 32 + hsel * 8 + (qbF >> 1) :
                  l < 26 ? gF + hsel * 16 + ((l - 18) >> 2) * 8 + ((l - 18) & 3) : l == 26 ? g + 48 + hsel * 8 + pa : l < 32 ? g + 64 + hsel * 8 + pa :
                  l < 48 ? 2304 + pj * 16 + l - 32 : 2048 + pj * 16 + l - 48);
    return t;
  }
  auto put64 = [&](int i, u64 x) { t.v[i] = (unsigned)x; t.v[i + 1] = (unsigned)(x >> 32); };
  t.v[T_Q0] = oQ + (qb0 * 128 + wid * 16) * 512;
  t.v[T_K0] = oK + (isY ? cx : 0) * 32768;
  t.v[T_OQ] = oQ + pj * 131072 + (wid >> 1) * 65536 + (wid & 1) * 4096;
  t.v[T_OG] = oK + kv * TSB + mt * 131072 + (wid >> 1) * 65536 + (wid & 1) * (kv ? 8192 : 4096);
  t.v[T_FPREP] = fo(2048 + pj * 16 + u);
  t.v[T_FQ] = fo(g + 32 + hsel * 8 + pj);
  t.v[T_FG] = fo(g + hsel * 16 + kv * 8 + mt);
  t.v[T_FS] = fo(1024 + qbF * 16 + jF * 2 + hsel);
  t.v[T_CNT] = fo(2560 + qbF * 16);
  t.v[T_ZO] = O_Z + pj * 64 * SEQ * 2 + (u * 4 + wid) * 64;
  const u64 own = 0x1ull << (kv * 8 + mt), conv = noA ? 0 : 0xffffull << 32;
  put64(T_NPRE, 0x10000ull | (isY ? (1ull << (cx >> 2)) | (1ull << ((cx + 1) >> 2)) : noA ? 0 : 0xffffull << 48));
  put64(T_NST, isY ? kvmask(cx >> 2, 3) | (solo ? 0 : 1ull << 27) : kvmask(0, 0));
  put64(T_TAB, k_order(isY, rr));
  t.v[T_SLAB] = O_S + qbF * 0x200000 + jF * 0x40000 + wid * 8192 + hsel * 1024;
  t.v[T_PART] = oP;
  const unsigned junk = fo(g + 96 + r), fconv = noA ? junk : fo(2304 + pj * 16 + u);
  for (int it = isY || solo; it < 2; ++it) {
    unsigned* p = t.v + T_IT + it * T_ITN;
    const int n = !it ? nx0 : isY && !solo ? NY : 2 * qbF + 2;
    u64 q[4] = {0, 0, 0, 0};
    unsigned jmp[3] = {0, 0, 0}, chk = 0;
    p[14] = junk;
    if (isY) {
      p[0] = 2; p[1] = 2; p[13] = t.v[T_FG];
      if (!solo) {
        chk = 1u << (14 - cx); p[0] |= chk; q[1] = kvmask(4, (31 - 2 * pa) >> 2) & ~own; jmp[1] = ex * 32768;
        chk |= 1u << (n - 2); q[2] = 1ull << 26;
      }
    } else if (it == 0) {
      chk = p[0] = 2u | (ex ? 1u << (cx - 2) : 0) | 1u << (n - 2);
      chk |= noA ? 0 : 1u << n;
      q[0] = kvmask(0, (cx - 1) >> 2);
      if (ex) { q[1] = kvmask(4, 4 + ((ex - 1) >> 2)); jmp[1] = (16 - cx) * 32768; }
      jmp[ex ? 2 : 1] = oQF - oQ - (ex ? 16 + ex : cx) * 32768;
      q[ex ? 2 : 1] = 0x20000ull | ((0x22ull << ((2 * qbF + 1) >> 2)) - 0x11ull) << 18;
      q[ex ? 3 : 2] = conv;
      p[1] = 2; p[13] = fconv; p[14] = fo(g + 64 + hsel * 8 + pa);
    } else if (solo) {
      chk = p[0] = 0x402; p[1] = 2; p[13] = fconv;
      q[0] = kvmask(0, 2); q[1] = kvmask(3, 4) | conv;
    } else {
      p[0] = p[1] = 1; p[13] = fo(g + 48 + hsel * 8 + pa);
      q[0] = conv;
    }
    p[2] = jmp[0]; p[4] = jmp[1]; p[15] = jmp[2];
    p[3] = !it ? 0 : n > 2 ? 1u << (n - 3) : 1u << 30;
    for (int k = 0; k < 4; ++k) { p[5 + 2 * k] = (unsigned)q[k]; p[6 + 2 * k] = (unsigned)(q[k] >> 32); }
    const int rk = (r >> 1) - (BAL && (r >> 1) > (slow ? 7 : 6));
    p[16] = 32 | (isY && !solo ? 2 : 0) | (n == 2 ? 4 : 0) | (isY || noA ? 0 : 8) | (n & 1) << 4 | (n - 1 - it) << 8 | (rk * 4 + wid) << 16 | hF << 24 | qbF << 28;
    p[17] = !it ? 0 : n > 2 ? 1u << (n - 2) : 1u << 31;
    p[18] = it ? oP + wid * 16384 : oQF + (qbF * 128 + wid * 16) * 512;
    p[19] = oP + (it ? 1032 : 1024);
    p[20] = chk >> 1;
    p[21] = (it ? oQF + O_K : oK) + (isY ? cx : 0) * 32768;
    p[22] = ((p[0] | p[3] | p[17]) & 0x3fffffffu) << 1;
  }
  return t;
}
struct TBlk { TRow row[5]; };
template <int B> constexpr TBlk t_blk = {{t_row(B, 0), t_row(B, 1), t_row(B, 2), t_row(B, 3), t_row(B, 4)}};
struct Tab { TBlk blk[256]; };
template <int... I> constexpr Tab t_make(iseq<I...>) { return Tab{{t_blk<I>...}}; }
__device__ const Tab g_tab = t_make(make_iseq<256>{});

template <int R> DI const bf16* sptr() { const bf16* p; asm volatile("s_mov_b64 %0, s[%c1:%c2]" : "=s"(p) : "i"(R), "i"(R + 1)); return p; }
template <class T, int I> DI T* arg_late() {
  unsigned lo, hi;
  asm volatile("v_readlane_b32 %0, v77, %2\n\tv_readlane_b32 %1, v77, %3\n\ts_nop 4" : "=s"(lo), "=s"(hi) : "i"(2 * I), "i"(2 * I + 1));
  return (T*)(((u64)hi << 32) | lo);
}
__global__ __launch_bounds__(256, 1) __attribute__((amdgpu_num_vgpr(77), amdgpu_num_sgpr(56))) void fused_kernel(
    const bf16* ckv_, const bf16* wuk_, const bf16* wuv_, const bf16* x_, const bf16* wq_, bf16* ws_, const bf16* ak_, const bf16* av_,
    const bf16* ash_, const bf16* assmk_, const bf16* assmD_, const bf16* wo_, const bf16* aq_, bf16* out_) {
  extern __shared__ __attribute__((aligned(16))) char lds[];
  const void* kap = (const void*)__builtin_amdgcn_kernarg_segment_ptr();
  asm volatile("at_code_begin:\n\ts_load_dwordx16 s[80:95], %0, 0\n\ts_load_dwordx2 s[96:97], %0, 0x40" ::"s"(kap) : ACLOB_ALL, "v255", "s101");
  asm volatile("s_getpc_b64 s[48:49]\nct_base:\n\ts_mov_b32 s50, pf_code_end-ct_base\n\ts_mov_b32 s51, 0x20000\n\ts_lshr_b32 s52, %1, 3\n\t"
               "v_and_b32 v116, 63, %0\n\tv_lshrrev_b32 v117, 6, %0\n\tv_lshl_add_u32 v116, v116, 7, v117\n\ts_lshl_b32 s52, s52, 9\n\t"
               "v_lshlrev_b32 v116, 7, v116\n\tbuffer_load_dword v117, v116, s[48:51], s52 offen" ::"v"(threadIdx.x), "s"(blockIdx.x) : "scc", "memory");
  GAP();
  const int tid = threadIdx.x, lane = tid & 63;
  const int wid = __builtin_amdgcn_readfirstlane(tid >> 6);
  const int b = blockIdx.x, j = b & 7, r = b >> 3, rr = r & 15, hsel = rr & 1, pj = rr >> 1, kv = pj & 1;
  const bool isY = r >= 16, doP = isY && pj != 7, doA = !isY && pj != 7;
  const int h = j + 8 * hsel, mt = (rr >> 2) + (isY ? 4 : 0), pu = (hsel * 8 + j) * 4 + wid;
  constexpr unsigned m0d = 131072 + 24576;
  const unsigned lA = isY ? 65536 : 0, lW = lA ^ 65536, po = isY ? 0 : 32768;
  unsigned mA = lA + wid * 1024 + po, mW = lW + wid * 1024 + po, ofA = isY ? pj * 256 * DA * 2 : mt * 256 * DC * 2, ofW = h * 512;
  const unsigned lda = isY ? DA : DC;
#define ROT 1
  unsigned g0 = (!ROT ? 0 : isY ? pj + 2 * hsel : rr + (rr >> 2)) & 3;
  {
    asm volatile("global_load_dword v78, %0, %2\n\tglobal_load_dword v79, %1, %2 offset:1024\n\tglobal_load_dword v77, %3, %4"
                 ::"v"(tid * 4), "v"(lane * 4), "s"(&g_tab.blk[b]), "v"((lane < 27 ? lane : 27) * 4), "s"(kap) : "memory");
    const unsigned ra = wid * 8 + (lane >> 3), sa = ((lane & 7) ^ (((wid & 1) * 4 + (lane >> 4)) & 7)) * 16;
    const unsigned vbA = ra * (lda * 2) + sa, vbB = (4 * wid + (lane >> 4)) * LDW * 2 + 16 * ((lane & 15) ^ (((lane >> 4) << 2) | wid));
    vsetu<92>(vbA); vsetu<93>(vbB); vsetu<94>(ra * (DC * 2) + sa);
    sset<64>(64 * lda); sset<67>(256 * lda); sset<68>(320 * lda);
    asm volatile("s_mov_b32 s71, 0x20000\n\ts_movk_i32 s72, 0x100\n\ts_mov_b32 s73, 0x20100");
    sset<58>(256 * lda * 2); sset<62>((lda - 1) * LDW * 2 + 512);
    asm volatile("s_mov_b32 s59, 0x20000\n\ts_mov_b32 s63, 0x20000");
    g_pro<1>(g0, mA, mW, lda);
    asm volatile("" : "+s"(ofA), "+s"(ofW), "+s"(mA), "+s"(mW), "+s"(g0));
    GAP();
  }
  asm volatile("s_bitcmp1_b32 %0, 4\n\ts_waitcnt lgkmcnt(0)\n\ts_cselect_b64 s[60:61], s[84:85], s[82:83]\n\ts_bitcmp1_b32 %0, 7\n\t"
               "s_cselect_b64 s[60:61], s[88:89], s[60:61]\n\ts_cselect_b64 s[56:57], s[86:87], s[80:81]\n\t"
               "s_add_u32 s56, s56, %1\n\ts_addc_u32 s57, s57, 0\n\ts_add_u32 s60, s60, %2\n\ts_addc_u32 s61, s61, 0\n\t"
               "s_mov_b64 s[88:89], s[90:91]\n\ts_mov_b32 s90, %3\n\ts_mov_b32 s91, 0x20000" ::"s"(b), "s"(ofA), "s"(ofW), "i"(WSB) : "scc");
  KST(0, 0);
  AC c;
  {
    GM g;
    PR pr;
    const int ptb = hsel * 8 + j;
    if (doP) prep_load(pr, tid, pj, ptb, sptr<92>(), sptr<94>(), sptr<96>());
    g_pro<2>(g0, mA, mW, lda);
    asm volatile("buffer_load_dwordx2 v[120:121], off, s[88:91], %0 sc1" ::"s"(fo(6144)) : "memory");
    if (doP) {
      asm volatile("s_waitcnt vmcnt(9)" ::: "memory");
#pragma unroll
      for (int q = 0; q < 7; ++q) asm volatile("" : "+v"(pr.kk[q]));
#pragma unroll
      for (int q = 0; q < 4; ++q) asm volatile("" : "+v"(pr.vv[q]));
      static_for([&](auto Ic) __attribute__((always_inline)) {
        constexpr int I = Ic;
        if constexpr (I < 3) g_pro(g0 + 1 + I, mA, mW, lda);
        GAP();
        prep_math<I>(pr, tid, lds + 131072);
        GAP();
      }, make_iseq<4>{});
      asm volatile("s_waitcnt lgkmcnt(0)\n\ts_barrier" ::: "memory");
      prep_store(tid, pj, ptb, (bf16*)sptr<88>() + O_X / 2, lds + 131072);
    }
    GAP();
    {
      const int fr = lane & 15, fq = lane >> 4, q = (lane & 15) >> 2, p = lane & 3;
#pragma unroll
      for (int k2 = 0; k2 < 2; ++k2)
        g.aA[k2] = lA + (wid >> 1) * 16384 + (fr >> 3) * 1024 + (fr & 7) * 128 + (((4 * k2 + fq) ^ (fr >> 1)) * 16);
#pragma unroll
      for (int hf = 0; hf < 2; ++hf)
#pragma unroll
        for (int nf = 0; nf < 8; ++nf)
          g.aB[hf][nf] = lW + (wid & 1) * 8192 + off_b(8 * fq + 4 * hf + q, nf * 2 + (p >> 1)) + 8 * (p & 1);
      asm volatile("" : "+v"(g.aA[0]), "+v"(g.aA[1]));
#pragma unroll
      for (int nf = 0; nf < 8; ++nf) asm volatile("" : "+v"(g.aB[0][nf]), "+v"(g.aB[1][nf]));
      const unsigned one = fq != q ? 0u : (p & 1) ? 0x3f800000u : 0x3f80u;
      c.o0 = (p & 2) ? 0u : one; c.o1 = (p & 2) ? one : 0u;
      c.tb = (lane >> 2) * 128 + 16 * ((p >> 1) ^ ((lane >> 2) & 6)) + 8 * (p & 1);
      c.vs = (fr >> 3) * 2048 + (fq & 1) * 512 + (fq >> 1) * 256 + (fr & 7) * 16;
      asm volatile("" : "+v"(c.o0), "+v"(c.o1), "+v"(c.tb), "+v"(c.vs));
      vsetu<81>(tid == 0 ? 0u : 0x80000000u);
      vsetu<84>(lane * 16); vsetu<85>(lane * 16 + 65536); vsetu<86>(wid * 8192 + lane * 16); vsetu<87>(wid * 8192 + lane * 16 + 4096);
      vsetu<88>(65536 + wid * 256 + fr * 16); vsetu<89>(16 * wid + fr - 4 * fq);
      asm volatile("s_mov_b64 s[92:93], s[88:89]\n\ts_mov_b64 s[94:95], s[90:91]\n\ts_mov_b32 s74, 0x40000\n\ts_mov_b32 s75, 0x60000\n\t"
                   "s_mov_b32 s76, 0x40100\n\ts_mov_b32 s77, 0x60100");
      sset<65>(128 * lda); sset<66>(192 * lda); sset<69>(384 * lda); sset<70>(448 * lda);
      sset<78>(lA + wid * 1024); sset<79>(lW + wid * 1024);
    }
    if (doP) asm volatile("s_waitcnt vmcnt(4)" ::: "memory");
    else {
      g_pro(g0 + 1, mA, mW, lda); g_pro(g0 + 2, mA, mW, lda); g_pro(g0 + 3, mA, mW, lda);
      asm volatile("s_waitcnt vmcnt(0)" ::: "memory");
    }
    asm volatile("s_barrier" ::: "memory");
    if (isY) g_read0<0>(g, make_iseq<16>{});
    else g_read0<1>(g, make_iseq<16>{});
    GAP();
    {
      u64 z = (((u64)__float_as_uint(vget<121>()) << 32) | __float_as_uint(vget<120>())) + 0x9e3779b97f4a7c15ull;
      z = (z ^ (z >> 30)) * 0xbf58476d1ce4e5b9ull; z = (z ^ (z >> 27)) * 0x94d049bb133111ebull; z ^= z >> 31;
      vsetu<82>((unsigned)z); vsetu<83>((unsigned)(z >> 32));
      asm volatile("v_readlane_b32 s80, v78, %0\n\tv_readlane_b32 s81, v78, %1\n\tv_readfirstlane_b32 s84, v82\n\tv_readfirstlane_b32 s85, v83"
                   ::"i"((int)T_TAB), "i"(T_TAB + 1));
      if (isY) asm volatile("s_mov_b64 s[80:81], 0x6543210");
    }
    unsigned k2;
    asm volatile("s_bfe_u32 s82, s80, 0x40008\n\ts_lshr_b64 s[80:81], s[80:81], 12\n\ts_lshl_b32 %0, s82, 1\n\tv_add_u32 v90, 128, v92" : "=s"(k2) :: "scc");
    asm volatile("s_waitcnt lgkmcnt(0)\n\ts_barrier" ::: "memory");
    g_proW(k2, mW);
    GAP();
    if (isY) {
      g_tile<false, 0, 1>(g);
      g_loop<false, true>(g, doP ? 0x22u : 0x20u, [&](int t) __attribute__((always_inline)) {
        if (t == 1) fl_set<T_FPREP>();
        else if (t == 4) { tload<80, T_TAB>(); tload<81, T_TAB + 1>(); srd_set<60>((kv ? arg_late<const bf16, 2>() : arg_late<const bf16, 1>()) + h * 256, (DC - 1) * LDW * 2 + 512); }
        else {
          srd_set<56>(arg_late<const bf16, 0>() + (size_t)mt * 256 * DC, 256 * DC * 2);
          asm volatile("v_mov_b32 v92, v94");
          static_for([&](auto J) { asm volatile("s_mov_b32 s%c0, %1" ::"i"(64 + J), "i"((J + 1) * 64 * DC)); }, make_iseq<7>{});
        }
      });
      tload<101, T_OQ>(); sset<83>(__float_as_uint(CS));
      asm volatile("v_mov_b32 v112, v84\n\tv_add_u32 v113, 8192, v84");
      GAP();
      g_tile<false, 0, 2>(g);
      KST(0, 3);
    }
#define TT 3
    auto kvtile = [&](auto TRc) __attribute__((always_inline)) {
      constexpr bool TR = TRc;
      if (isY) g_tile<TR, 1, 4>(g);
      else g_tile<TR, 1, 1>(g);
      g_loop<TR, false>(g, 0x2000u | (isY || doA ? 2u : BAL && (j & 1) ? 0u : 1u << TT), [&](int t) __attribute__((always_inline)) {
        int l = threadIdx.x & 63; asm volatile("" : "+v"(l));
        if (t == 13) {
          asm volatile("s_mov_b32 s79, 0x28000");
          fl_poll<T_NPRE>();
          if ((tget<T_IT + T_ITN + 16>() & 0xf000000) == 0)
            asm volatile("v_readlane_b32 s48, v78, %0\n\tv_and_b32 v122, 0xffffff00, v82\n\tv_mov_b32 v123, v83\n\ts_nop 2\n\t"
                         "buffer_store_dwordx2 v[122:123], v81, s[88:91], s48 offen sc1" ::"i"((int)T_CNT) : "memory");
          if (!doA && !isY) {
            CODE_TOUCH("at_code_begin", "pf_code_end", hsel * 8 + wid * 2, 16u, 64, m0d);
            CODE_TOUCH("at_code_begin", "pf_code_end", hsel * 8 + wid * 2 + 1, 16u, 64, m0d);
          }
          if (doA) {
            const char* tp = arg_late<const char, 9>() + (size_t)(pj * 64 + pu) * (SEQ * 2) + (l & 31) * 128;
            asm volatile("s_mov_b32 m0, %1\n\ts_nop 0\n\tglobal_load_lds_dword %0, off" ::"v"(tp), "i"(m0d) : "memory");
          }
        } else if (isY) fl_set<T_FQ>();
        else if (doA) post_dma(pj, pu, arg_late<const bf16, 12>(), 131072 + wid * 8192);
        else {
          const char* tp = arg_late<const char, 0>() + 1024 * DC * 2 + (pu * 256 + l) * 128;
          asm volatile("s_mov_b32 m0, %0\n\ts_nop 0" ::"i"(m0d));
#pragma unroll
          for (int i = 0; i < (BAL ? 8 : 4); ++i) asm volatile("global_load_lds_dword %0, off" ::"v"(tp + (i & 3) * 8192 + (i >> 2) * 131072) : "memory");
        }
      });
      tload<101, T_OG>(); tload<96, T_Q0>(); tload<98, T_K0>();
      fl_check();
      tload<50, T_NST>(); tload<51, T_NST + 1>();
      asm volatile("v_mov_b32 v112, v84\n\tv_add_u32 v113, %0, v84\n\tv_mov_b32 v90, v87\n\tv_mov_b32 v91, v87\n\tv_mov_b32 v92, v87\n\tv_mov_b32 v93, v87\n\t"
                   "v_mov_b32 v94, v86\n\tv_mov_b32 v95, v86\n\tv_mov_b32 v126, v86\n\tv_mov_b32 v127, v86\n\ts_mov_b32 s99, 0x8000\n\t" FL_ARM("s[50:51]")
                   ::"i"(TR ? 4096 : 8192));
      if (doA) {
        const int d = pj * 64 + pu;
        const bf16* dp = arg_late<const bf16, 10>() + d;
        asm volatile("global_load_ushort v123, %0, off" ::"v"(dp) : "memory");
        srd_set<56>(arg_late<const bf16, 9>() + (size_t)d * SEQ, SEQ * 2);
        srd_set<60>(arg_late<const char, 5>() + O_X + d * SEQ * 2, SEQ * 2);
        sset<78>(65536 + wid * 15488);
        asm volatile("s_movk_i32 s64, 992\n\tv_subrev_u32 v122, 32, v84");
        GAP();
        g_tile<TR, 0, 3>(g);
      } else {
        sset<79>(isY ? wid * 8192 : 0x30000);
        if (tget<T_IT + T_ITN + 16>() & 16) asm volatile("s_add_u32 s98, s98, 0x8000\n\ts_mov_b32 s97, 0xffff8000" ::: "scc");
        else asm volatile("s_mov_b32 s97, 0x8000");
        GAP();
        g_tile<TR, 0, 5>(g);
      }
    };
    if (kv) kvtile(std::true_type{});
    else kvtile(std::false_type{});
  }
  KST(0, 4);
  asm volatile("s_waitcnt vmcnt(16)" ::: "memory");
  const unsigned md0 = tget<T_IT + 16>(), md1 = tget<T_IT + T_ITN + 16>();
  sset<79>(wid * 8192 + (md0 & 16) * 2048);
  if (doA) {
    const float Dd = bfl(__float_as_uint(vget<123>()));
    KST(0, 5);
    conv_wave<T_FG>(O_Z + (pj * 64 + pu) * SEQ * 2, Dd, lds + 65536 + wid * 15488);
    const char* tp = arg_late<const char, 11>() + (size_t)h * (HD * DA * 2) + ((pj * 4 + wid) * 64 + lane) * 128;
    asm volatile("s_mov_b32 m0, %1\n\ts_nop 0\n\tglobal_load_lds_dword %0, off\n\ts_waitcnt vmcnt(17)" ::"v"(tp), "i"(m0d) : "memory");
    KST(0, 6);
  } else if (!isY) {
    static_for([&](auto I) { d_store<16 + I>(); }, make_iseq<16>{});
    tload<98, T_K0>();
    asm volatile("s_waitcnt vmcnt(0)\n\ts_barrier" ::: "memory");
    fl_set<T_FG>();
    KST(0, 5);
    fl_check();
    CODE_TOUCH("att_begin", "pf_code_end", hsel * 4 + wid, 8u, 64, m0d);
    dma4k<0>(); dma4k<1>(); dma4k<2>(); dma4k<3>();
    asm volatile("s_waitcnt vmcnt(0)" ::: "memory");
    KST(0, 6);
  } else {
    tload<76, T_PART>();
    fl_check();
    asm volatile("buffer_load_dwordx2 v[122:123], v88, s[88:91], s76 offen offset:1024 sc1" ::: "memory");
  }
  sset<78>(65536 + wid * 8192); sset<79>(98304 + wid * 8192);
  { const bf16* wp = arg_late<const bf16, 11>() + ((md1 >> 24) & 15) * HD * DA; asm volatile("s_mov_b64 s[80:81], %0" ::"s"(wp)); }
  asm volatile("att_begin:");
  ev_code();
  asm volatile("s_waitcnt lgkmcnt(0)\n\ts_barrier" ::: "memory");
#pragma unroll 1
  for (int it = md0 ? 0 : 1; it < 2; ++it) {
    const unsigned mode = it ? md1 : md0;
    const bool odd = mode & 16;
    if (odd) kreads<1>(make_iseq<8>{});
    else kreads<0>(make_iseq<8>{});
    const unsigned oP = tget<T_PART>();
    if (it == 0) {
      static_for([&](auto I) { tload<57 + I, T_IT + I>(); }, make_iseq<21>{});
      tload<S_T, T_IT + 21>(); tload<S_EV, T_IT + 22>();
    } else {
      static_for([&](auto I) { tload<57 + I, T_IT + T_ITN + I>(); }, make_iseq<21>{});
      tload<S_T, T_IT + T_ITN + 21>(); tload<S_EV, T_IT + T_ITN + 22>();
    }
    asm volatile("s_add_u32 s98, s97, 0x8000\n\ts_mov_b32 s56, -1\n\tv_mov_b64 v[114:115], 0\n\tv_mov_b64 v[118:119], 0\n\t"
                 "v_mov_b32 v120, 0x45800000" ::: "scc");
    KST(0, 8 + 3 * (it && !isY));
    GAP();
#define STA(k)
#define STI()
#define LOOP_ENDS "s_bfe_u32 s76, s73, 0x80008"
    STA(0);
    if (odd) {
      if (isY) phaseA<1, 2>(c);
      else phaseA<1, 1>(c);
      STA(1);
      STA(2);
      phaseB0<1>(mode);
      STA(3);
      asm volatile(LOOP_ENDS "\n\ts_branch att_mid" ::: "scc");
    } else {
      if (isY) phaseA<0, 2>(c);
      else phaseA<0, 1>(c);
      STA(1);
      if (c.rare) ev_call();
      if (__builtin_expect(mode & 4, 0)) { asm volatile("s_mov_b32 s56, 30"); ev_call(); asm volatile("s_mov_b32 s56, 31"); ev_call(); }
      STA(2);
      phaseB0<0>(mode);
      if (__builtin_expect(mode & 4, 0)) { ev_call(); asm volatile("s_branch att_done"); }
      STA(3);
    }
    GAP();
    asm volatile(LOOP_ENDS "\natt_loop:" ::: "scc");
    GAP();
    phaseA<1, 0>(c);
    STI();
    if (__builtin_expect(c.rare != 0, 0)) { if (redo()) sm_redo<1>(); ev_call(); }
    phaseB<1>(c);
    asm volatile("s_cbranch_scc1 att_done\natt_mid:");
    phaseA<0, 0>(c);
    STI();
    if (__builtin_expect(c.rare != 0, 0)) { if (redo()) sm_redo<0>(); ev_call(); }
    phaseB<0>(c);
    asm volatile("s_cbranch_scc0 att_loop\natt_done:");
    GAP();
    STA(4);
    if (it == 0) phaseA<0, 3>(c);
    else {
      sset<76>(oP);
      GAP();
      phaseAL(c, make_iseq<32>{});
      if (__builtin_expect(redo(), 0)) sm_redo<1, false>();
      if (mode & 2) phaseBL<true>(c, make_iseq<64>{});
      else phaseBL<false>(c, make_iseq<64>{});
    }
    if (__builtin_expect(redo(), 0)) {
      if (it) static_for([&](auto I) { vset<R_S + 32 + (I >> 2) * 8 + (I & 3)>(-INFINITY); }, make_iseq<16>{});
      asm volatile("s_nop 7");
      sm_redo<0, false>();
    }
    STA(5);
    KST(0, 9 + 3 * (it && !isY));
    float l[2];
    asm volatile("v_add_f32 %0, v%c2, v%c3\n\tv_add_f32 %1, v%c4, v%c5" : "=v"(l[0]), "=v"(l[1]) : "i"(R_RS), "i"(R_TS), "i"(R_RS + 1), "i"(R_TS + 1));
    const float cm[2] = {vget<R_M>(), vget<R_M + 1>()};
    l[0] = row_reduce<false>(l[0]); l[1] = row_reduce<false>(l[1]);
    EP p;
    if (it == 0) {
      const f32x4 ml = {cm[0], l[0], cm[1], l[1]};
      asm volatile("buffer_store_dwordx4 %0, v88, s[88:91], %1 offen sc1\n\ts_mov_b32 s101, %2" ::"v"(ml), "s"(oP), "s"(oP + wid * 16384) : "memory");
      phaseF(c, p, make_iseq<74>{});
      asm volatile(FL_POLL "\n\ts_waitcnt vmcnt(17)" ::: "memory");
    } else {
      if (!(mode & 2)) {
        p.fa[0] = __builtin_amdgcn_rcpf(l[0]); p.fa[1] = __builtin_amdgcn_rcpf(l[1]);
        phaseFL<1>(p);
      } else {
        const f32x4 ml = {vget<122>(), vget<123>(), vget<124>(), vget<125>()};
        if (__builtin_expect(__builtin_amdgcn_ballot_w64(ml[0] != cm[0] || ml[2] != cm[1]) == 0, 1)) {
          p.fa[0] = __builtin_amdgcn_rcpf(l[0] + ml[1]); p.fa[1] = __builtin_amdgcn_rcpf(l[1] + ml[3]);
          phaseFL<3>(p);
        } else {
#pragma unroll
          for (int e = 0; e < 2; ++e) {
            const float mm = fmaxf(cm[e], ml[2 * e]);
            const float wa = __builtin_amdgcn_exp2f(cm[e] - mm), wb = __builtin_amdgcn_exp2f(ml[2 * e] - mm);
            const float inv = __builtin_amdgcn_rcpf(l[e] * wa + ml[2 * e + 1] * wb);
            p.fa[e] = wa * inv; p.fb[e] = wb * inv;
          }
          phaseFL<2>(p);
        }
      }
    }
    KST(0, 10 + 3 * (it && !isY));
  }
  asm volatile(TICKET "\n\tv_xor_b32 v%c0, s100, v%c0\np_loop:" ::"i"(R_TB) : "memory");
  GAP();
  p_half<0, true>(make_iseq<32>{});
  asm volatile("s_cbranch_scc0 p_mid");
  p_half<1, true>(make_iseq<32>{});
  asm volatile("s_branch p_loop\np_loop2:");
  p_half<0, false>(make_iseq<32>{});
  asm volatile("p_mid:");
  p_half<1, false>(make_iseq<32>{});
  asm volatile("s_cbranch_scc1 p_loop2\n\ts_nop 5");
  GAP();
  p_cvt<1, 0>(); p_cvt<1, 1>(); p_cvt<1, 2>(); p_cvt<1, 3>();
  p_store<1, 1>();
  GAP();
  KST(0, 27);
  {
    constexpr unsigned rdy = 98304;
    int lz = threadIdx.x & 63; asm volatile("" : "+v"(lz));
    const int fr = lz & 15, fq = lz >> 4, qbF = md1 >> 28;
    if (wid == 0) {
#pragma unroll 1
      for (int q = 0; q < 0x100000; ++q) {
        unsigned lo, hi, tl, th;
        asm volatile("v_readfirstlane_b32 %0, v240\n\tv_readfirstlane_b32 %1, v241\n\ts_mov_b32 %2, s84\n\ts_mov_b32 %3, s85" : "=s"(lo), "=s"(hi), "=s"(tl), "=s"(th));
        if (__builtin_expect((((lo ^ tl) >> 8) | (hi ^ th)) == 0, 1)) break;
        asm volatile("s_sleep 4\n\tv_mov_b64 v[240:241], 1\n\t" TICKET "\n\ts_waitcnt vmcnt(0)" ::: "memory");
      }
      asm volatile("v_mov_b32 v241, 0\n\ts_mov_b64 exec, 1\n\tds_write_b64 %0, v[240:241]\n\ts_mov_b64 exec, -1" ::"v"(rdy) : "memory");
    }
    vsetu<79>(fo(1024) + qbF * 4352 + fr * 8);
    {
      const int jj = 4 * (fr >> 3) + (fr & 3);
      const unsigned one = ((fq ^ (fr >> 2)) & 1) ? 0u : (jj & 1) ? 0x3f800000u : 0x3f80u;
      static_for([&](auto I) { vsetu<96 + I>((jj >> 1) == I ? one : 0u); }, make_iseq<4>{});
    }
    unsigned vo = (fq >> 1) * 1024 + (fq & 1) * 256 + fr * 16;
    bf16* out = arg_late<bf16, 13>();
    __amdgpu_buffer_rsrc_t so_ = __builtin_amdgcn_make_buffer_rsrc((void*)out, (short)0, SEQ * OUTW * 2, 0x00020000);
    unsigned vw = ((qbF * 128 + (fr & 7)) * OUTW + DA + (fr >> 3) * 32 + fq * 8) * 2;
    asm volatile("" : "+s"(so_));
    asm volatile("s_waitcnt vmcnt(0) lgkmcnt(0)\n\ts_barrier" : "+v"(vo), "+v"(vw) :: "memory");
    fl_set<T_FS>();
    unsigned tk;
    asm volatile("ds_read_b32 %0, %1\n\ts_waitcnt lgkmcnt(0)" : "=v"(tk) : "v"(rdy) : "memory");
    tk = __builtin_amdgcn_readfirstlane(tk) & 255;
    KST(0, 30);
    if (doA) post_math(pj, pu, out, lds + 131072 + wid * 8192);
#define N3 0
#define N2 12
    constexpr int N1 = 28 - 3 * N3 - 2 * N2;
    static_assert(N1 >= 0 && N3 + N2 + N1 <= 16);
    const int nu = tk < N3 ? 3 : tk < N3 + N2 ? 2 : tk < N3 + N2 + N1 ? 1 : 0;
    const int u0 = 4 * (tk < N3 ? 3 * tk : tk < N3 + N2 ? N3 + 2 * tk : 2 * N3 + N2 + tk) + wid * nu;
    KST(0, 28);
    if (nu) {
      const unsigned so0 = O_S + qbF * 0x200000 + (16 + u0) * 2048, so[3] = {so0, so0, so0};
      unsigned pend = 0x5555u, cnt = 0x400000;
      while (true) {
        unsigned nw;
        if (wid == 3) {
          u64 m;
          const u64 ex = pend | (pend << 1);
          asm volatile(FL_ARM("%1") "\n\t" FL_POLL "\n\ts_waitcnt vmcnt(0)\n\tv_cmp_eq_u64 %0, s[84:85], v[124:125]" : "=s"(m) : "s"(ex) : "memory");
          nw = (unsigned)m & ((unsigned)m >> 1) & pend;
          if (nw) asm volatile("ds_write_b32 %0, %1 offset:4" ::"v"(rdy), "v"(~pend | nw) : "memory");
        } else {
          unsigned t;
          asm volatile("ds_read_b32 %0, %1 offset:4\n\ts_waitcnt lgkmcnt(0)" : "=v"(t) : "v"(rdy) : "memory");
          nw = __builtin_amdgcn_readfirstlane(t) & pend;
        }
        if (--cnt == 0) nw = pend;
        if (nw == 0) {
          if (wid == 3) asm volatile("s_sleep 4");
          else asm volatile("s_sleep 1");
          continue;
        }
        asm volatile("s_nop 4");
        static_for([&](auto S) {
          if ((nw >> (2 * S)) & 1) {

            r_load<0, 2 * S>(vo, so[0]); r_load<0, 2 * S + 1>(vo, so[0]);
            if (nu > 1) { r_load<1, 2 * S>(vo, so[1]); r_load<1, 2 * S + 1>(vo, so[1]); }
            if (nu > 2) { r_load<2, 2 * S>(vo, so[2]); r_load<2, 2 * S + 1>(vo, so[2]); }
          }
        }, make_iseq<8>{});
        pend &= ~nw;
        if (pend == 0) break;
      }
      asm volatile("s_waitcnt vmcnt(0)" ::: "memory");
      KST(0, 16);
      auto oo = [&](int U) { return ((((U >> 1) & 1) * 64 + ((U >> 2) & 3) * 16 + (U & 1) * 8) * OUTW + (U >> 4) * 64) * 2; };
      r_fin<0>(vw, so_, oo(u0));
      if (nu > 1) r_fin<1>(vw, so_, oo(u0 + 1));
      if (nu > 2) r_fin<2>(vw, so_, oo(u0 + 2));
      if (qbF == 15 && tk == 0) asm volatile(FL_SET("%0") ::"s"(fo(6144)) : "memory");
    }
    KST(0, 29);
  }
  asm volatile("s_waitcnt vmcnt(0)" ::: "memory");
  asm volatile("pf_code_end:");
  KST(0, 1);
}

extern "C" void kernel_launch(void* const* d_in, const int* in_sizes, int n_in, void* d_out, int out_size, void* d_ws,
                              size_t ws_size, hipStream_t stream) {
  const bf16* Aq = (const bf16*)d_in[0];
  const bf16* Ak = (const bf16*)d_in[1];
  const bf16* Av = (const bf16*)d_in[2];
  const bf16* Ash = (const bf16*)d_in[3];
  const bf16* Assmk = (const bf16*)d_in[4];
  const bf16* AssmD = (const bf16*)d_in[5];
  const bf16* Bx = (const bf16*)d_in[6];
  const bf16* Bckv = (const bf16*)d_in[7];
  const bf16* Bwq = (const bf16*)d_in[8];
  const bf16* Bwuk = (const bf16*)d_in[9];
  const bf16* Bwuv = (const bf16*)d_in[10];
  const bf16* Bwo = (const bf16*)d_in[11];
  bf16* out = (bf16*)d_out;
  hipFuncSetAttribute((const void*)fused_kernel, hipFuncAttributeMaxDynamicSharedMemorySize, 163840);
  fused_kernel<<<256, 256, 163840, stream>>>(Bckv, Bwuk, Bwuv, Bx, Bwq, (bf16*)d_ws, Ak, Av, Ash, Assmk, AssmD, Bwo, Aq, out);
}
```

```cpp
#include <hip/hip_runtime.h>
#include <hip/hip_bf16.h>
#include <stdint.h>
#include <utility>

typedef __hip_bfloat16 bf16;
typedef __attribute__((ext_vector_type(4))) short bf16x4;
typedef __attribute__((ext_vector_type(2))) unsigned u32x2;

constexpr int SEQ = 2048;
constexpr int DA = 448;
constexpr int NH = 16;
constexpr int HD = 256;
constexpr int DC = 1024;
constexpr int OUTW = 896;
constexpr unsigned TSB = NH * SEQ * HD * 2, O_K = TSB, O_S = 3 * TSB, O_P = 5 * TSB, PSTR = 67584, O_F = O_P + 128 * PSTR,
                   O_X = O_F + 400 * 4352, O_Z = O_X + DA * SEQ * 2, WSB = O_Z + DA * SEQ * 2;
constexpr unsigned fo(int w) { return O_F + (w >> 4) * 4352 + (w & 15) * 8; }

#define KST(kid, which)

__device__ __forceinline__ float bf2f(unsigned short u) { return __uint_as_float(((unsigned)u) << 16); }
__device__ __forceinline__ unsigned short f2bf(float f) { return __bfloat16_as_ushort(__float2bfloat16(f)); }
__device__ __forceinline__ float rbf(float f) { return bf2f(f2bf(f)); }
typedef __attribute__((ext_vector_type(2))) __bf16 bf16x2n;
typedef __attribute__((ext_vector_type(2))) float f32x2n;
__device__ __forceinline__ unsigned pk2(float a, float b) {
  return __builtin_bit_cast(unsigned, __builtin_convertvector((f32x2n){a, b}, bf16x2n));
}

__device__ __forceinline__ int off_b(int row, int ch) { return 256 * row + 16 * (ch ^ (((row & 3) << 2) | ((row >> 2) & 3))); }

template <int R> __device__ __forceinline__ void sset(unsigned x) { asm volatile("s_mov_b32 s%c0, %1" ::"i"(R), "s"(x)); }
template <int R> __device__ __forceinline__ void vsetu(unsigned x) { asm volatile("v_mov_b32 v%c0, %1" ::"i"(R), "v"(x)); }
template <int R> __device__ __forceinline__ void srd_set(const void* p, unsigned n) {
  asm volatile("s_mov_b64 s[%c0:%c1], %4\n\ts_mov_b32 s%c2, %5\n\ts_mov_b32 s%c3, 0x20000" ::"i"(R), "i"(R + 1), "i"(R + 2), "i"(R + 3), "s"(p), "s"(n));
}
template <int R, int L> __device__ __forceinline__ void tload() { asm volatile("v_readlane_b32 s%c0, v78, %1" ::"i"(R), "i"(L)); }
template <int L> __device__ __forceinline__ unsigned tget() { unsigned t; asm volatile("v_readlane_b32 %0, v78, %1" : "=s"(t) : "i"(L)); return t; }
#define FL_SET(S) "buffer_store_dwordx2 v[82:83], v81, s[88:91], " S " offen sc1"
#define FL_ARM(N) "v_cndmask_b32_e64 v80, -2.0, v79, " N
#define FL_POLL "buffer_load_dwordx2 v[124:125], v80, s[88:91], 0 offen sc1"
#define FL_CHECK(N) "s_mov_b32 s48, 0x100000\n7:\n\tv_cmp_eq_u64 vcc, s[84:85], v[124:125]\n\ts_and_b64 vcc, vcc, " N "\n\ts_cmp_eq_u64 vcc, " N \
                    "\n\ts_cbranch_scc1 8f\n\ts_sleep 2\n\t" FL_POLL "\n\ts_waitcnt vmcnt(0)\n\ts_sub_u32 s48, s48, 1\n\ts_cbranch_scc0 7b\n8:"
template <int L> __device__ __forceinline__ void fl_set() {
  asm volatile("v_readlane_b32 s48, v78, %0\n\ts_nop 4\n\t" FL_SET("s48") ::"i"(L) : "memory");
}
template <int L> __device__ __forceinline__ void fl_poll() {
  asm volatile("v_readlane_b32 s50, v78, %0\n\tv_readlane_b32 s51, v78, %1\n\ts_nop 1\n\t" FL_ARM("s[50:51]") "\n\t" FL_POLL ::"i"(L), "i"(L + 1) : "memory");
}
#define TICKET "buffer_atomic_add_x2 v[240:241], v81, s[88:91], s101 offen sc0"
__device__ __forceinline__ void fl_check() { asm volatile(FL_CHECK("s[50:51]") ::: "vcc", "scc", "memory"); }
template <int R> __device__ __forceinline__ float vget() { float t; asm volatile("v_mov_b32 %0, v%c1" : "=v"(t) : "i"(R)); return t; }
template <int R> __device__ __forceinline__ void vset(float x) { asm volatile("v_mov_b32 v%c0, %1" ::"i"(R), "v"(x)); }
template <int R> __device__ __forceinline__ float acc_read1() { float t; asm volatile("v_accvgpr_read_b32 %0, a[%c1]" : "=v"(t) : "i"(R)); return t; }
template <class F, int... I> __device__ __forceinline__ void static_for(F&& f, std::integer_sequence<int, I...>) {
  (f(std::integral_constant<int, I>{}), ...);
}
template <int... Is> using iseq = std::integer_sequence<int, Is...>;
template <int N> using make_iseq = std::make_integer_sequence<int, N>;

typedef __attribute__((ext_vector_type(4))) unsigned u32x4c;
typedef __attribute__((ext_vector_type(4))) float f32x4c;
typedef __attribute__((ext_vector_type(2))) float f32x2c;
typedef unsigned long long u64;
struct PR { u32x4c kk[7], vv[4]; };
__device__ __forceinline__ void prep_load(PR& p, int tid, int cg, int tb, const bf16* __restrict__ k, const bf16* __restrict__ v,
                                          const bf16* __restrict__ shift_k) {
  const __amdgpu_buffer_rsrc_t sk = __builtin_amdgcn_make_buffer_rsrc((void*)k, (short)0, SEQ * DA * 2, 0x00020000);
  const __amdgpu_buffer_rsrc_t sv = __builtin_amdgcn_make_buffer_rsrc((void*)v, (short)0, SEQ * DA * 2, 0x00020000);
  const __amdgpu_buffer_rsrc_t sw = __builtin_amdgcn_make_buffer_rsrc((void*)(shift_k + cg * 256), (short)0, 512, 0x00020000);
  const int vo = ((tb * 128 + (tid >> 3) * 4) * DA + cg * 64 + (tid & 7) * 8) * 2;
#pragma unroll
  for (int q = 0; q < 7; ++q)
    asm volatile("buffer_load_dwordx4 %0, %1, %2, 0 offen" : "=v"(p.kk[q]) : "v"(vo + (q - 3) * DA * 2), "s"(sk) : "memory");
#pragma unroll
  for (int q = 0; q < 4; ++q)
    asm volatile("buffer_load_dwordx4 %0, %1, %2, 0 offen offset:%3" : "=v"(p.vv[q]) : "v"(vo), "s"(sv), "i"(q * DA * 2) : "memory");
#pragma unroll
  for (int q = 0; q < 4; ++q)
    asm volatile("buffer_load_dwordx4 a[%c0:%c1], %2, %3, 0 offen offset:%4" ::"i"(4 * q), "i"(4 * q + 3), "v"((tid & 7) * 64), "s"(sw), "i"(q * 16) : "memory");
}
__device__ __forceinline__ float bfl(unsigned u) { return __uint_as_float(u << 16); }
__device__ __forceinline__ float bfh(unsigned u) { return __uint_as_float(u & 0xffff0000u); }
__device__ __forceinline__ f32x2c rbf2(f32x2c a) {
  return (f32x2c){__uint_as_float(pk2(0.f, a[0])), __uint_as_float(pk2(0.f, a[1]))};
}
template <int I> __device__ __forceinline__ void prep_math(const PR& p, int tid, char* lx) {
  const int rp = tid >> 3, c = tid & 7;
  f32x2c wt[4], kf[7];
  const unsigned w[4] = {__float_as_uint(acc_read1<4 * I>()), __float_as_uint(acc_read1<4 * I + 1>()), __float_as_uint(acc_read1<4 * I + 2>()),
                         __float_as_uint(acc_read1<4 * I + 3>())};
#pragma unroll
  for (int t = 0; t < 4; ++t)
    wt[t] = (t & 1) ? (f32x2c){bfh(w[t >> 1]), bfh(w[2 + (t >> 1)])} : (f32x2c){bfl(w[t >> 1]), bfl(w[2 + (t >> 1)])};
#pragma unroll
  for (int q = 0; q < 7; ++q) kf[q] = (f32x2c){bfl(p.kk[q][I]), bfh(p.kk[q][I])};
  f32x2c x[4];
#pragma unroll
  for (int e = 0; e < 4; ++e) {
    f32x2c s = rbf2(kf[e] * wt[3]);
    s = rbf2(s + rbf2(kf[e + 1] * wt[2]));
    s = rbf2(s + rbf2(kf[e + 2] * wt[1]));
    s = rbf2(s + rbf2(kf[e + 3] * wt[0]));
    x[e] = s * (f32x2c){bfl(p.vv[e][I]), bfh(p.vv[e][I])};
  }
#pragma unroll
  for (int u = 0; u < 2; ++u)
    *(u32x2*)(lx + (c * 8 + 2 * I + u) * 256 + (((31 - rp) * 8) ^ (c << 4))) = (u32x2){pk2(x[3][u], x[2][u]), pk2(x[1][u], x[0][u])};
}
__device__ __forceinline__ void prep_store(int tid, int cg, int tb, bf16* __restrict__ xr, const char* lx) {
  const __amdgpu_buffer_rsrc_t sx = __builtin_amdgcn_make_buffer_rsrc((void*)(xr + (size_t)cg * 64 * SEQ), (short)0, 64 * SEQ * 2, 0x00020000);
#pragma unroll
  for (int rd = 0; rd < 4; ++rd) {
    const int idx = tid + 256 * rd, ch = idx >> 4, q = idx & 15;
    u32x4c w = *(const u32x4c*)(lx + ch * 256 + 16 * (q ^ (ch >> 3)));
    __builtin_amdgcn_raw_buffer_store_b128(w, sx, (ch * SEQ + (15 - tb) * 128 + q * 8) * 2, 0, 16);
  }
}

#define CODE_TOUCH(LB, LE, wx, NW, NL, m0v) do { unsigned blo_, bhi_, elo_; \
    asm volatile("s_getpc_b64 s[48:49]\n\ts_add_u32 s48, s48, " LB "@rel32@lo+4\n\ts_addc_u32 s49, s49, " LB "@rel32@hi+12\n\t" \
                 "s_mov_b32 %0, s48\n\ts_mov_b32 %1, s49\n\t" \
                 "s_getpc_b64 s[48:49]\n\ts_add_u32 s48, s48, " LE "@rel32@lo+4\n\ts_mov_b32 %2, s48" \
                 : "=s"(blo_), "=s"(bhi_), "=s"(elo_) :: "scc"); \
    const unsigned n_ = elo_ - (blo_ & ~127u); \
    const __amdgpu_buffer_rsrc_t srd_ = __builtin_amdgcn_make_buffer_rsrc((void*)(((unsigned long long)bhi_ << 32) | (blo_ & ~127u)), (short)0, n_ < 262144u ? n_ : 0u, 0x00020000); \
    const unsigned l_ = threadIdx.x & 63, vo_ = l_ < (NL) ? ((wx) + l_ * (NW)) * 128u : 0x80000000u; \
    asm volatile("s_mov_b32 m0, %0\n\ts_nop 4\n\tbuffer_load_dword %1, %2, 0 offen lds" ::"s"(m0v), "v"(vo_), "s"(srd_) : "memory"); } while (0)
template <int I> __device__ __forceinline__ void d_store() {
  asm volatile("buffer_store_dwordx4 v[%c0:%c1], v%c2, s[88:91], s101 offen offset:%3 sc1" ::"i"(128 + 4 * I), "i"(131 + 4 * I), "i"(112 + ((I >> 2) & 1)),
               "i"((I & 3) * 1024) : "memory");
  if constexpr ((I & 7) == 7) asm volatile("s_addk_i32 s101, 0x4000" ::: "scc");
}
constexpr int NS_X = 19;
struct CV { unsigned pa[4], pb, px, vz, sz; float D; };
constexpr int cv_J(bool up, int q) { int J = up ? 0 : 7; while (q > J) { q -= J + 1; J += up ? 1 : -1; } return J; }
constexpr int cv_i(bool up, int q) { int J = up ? 0 : 7; while (q > J) { q -= J + 1; J += up ? 1 : -1; } return q; }
template <int R, int N> __device__ __forceinline__ void cv_rd(CV& c) {
  if constexpr (N < 16) {
    constexpr int i = N >> 1, o = 16 * R + 128 * (i & 1) + 2 * (N & 1), r = 32 + 32 * (R & 1) + 2 * N;
    asm volatile("ds_read2_b32 a[%c1:%c2], %0 offset0:%c3 offset1:%c4" ::"v"(c.pa[i >> 1]), "i"(r), "i"(r + 1), "i"(o), "i"(o + 1));
  } else {
    constexpr int m = N - 16, r = 192 + 32 * (R & 1) + 4 * m;
    asm volatile("ds_read_b128 a[%c1:%c2], %0 offset:%c3" ::"v"(c.pb), "i"(r), "i"(r + 3), "i"(64 * R + 512 * (7 - m)));
  }
}
template <int O> __device__ __forceinline__ void cv_xd(CV& c) {
  constexpr int e = O & 31, x = 96 + ((e >> 1) ^ 1);
  if constexpr (O >= 64) asm volatile("v_cvt_pk_bf16_f32 v%c0, 0, v%c0" ::"i"(160 + e));
  else if constexpr (O >= 32) asm volatile("v_mul_f32 v%c0, v%c0, %1" ::"i"(160 + e), "v"(c.D));
  else if constexpr (O & 1) asm volatile("v_lshlrev_b32 v%c0, 16, v%c1" ::"i"(160 + e), "i"(x));
  else asm volatile("v_and_b32 v%c0, 0xffff0000, v%c1" ::"i"(160 + e), "i"(x));
}
template <int O> __device__ __forceinline__ void cv_z(CV& c) {
  constexpr int J = O / 11, k = O % 11, y = 128 + 4 * J;
  if constexpr (k < 4) asm volatile("v_cvt_pk_bf16_f32 v%c0, 0, v%c0" ::"i"(y + k));
  else if constexpr (k < 8) asm volatile("v_add_f32 v%c0, v%c0, v%c1" ::"i"(y + k - 4), "i"(y + 28 + k));
  else if constexpr (k < 10) asm volatile("v_cvt_pk_bf16_f32 v%c0, v%c1, v%c2" ::"i"(y + k - 8), "i"(y + 2 * k - 16), "i"(y + 2 * k - 15));
  else asm volatile("buffer_store_dwordx2 v[%c0:%c1], %2, s[88:91], %3 offen offset:%4 sc1" ::"i"(y), "i"(y + 1), "v"(c.vz), "s"(c.sz),
                    "i"(512 * J) : "memory");
}
constexpr int cv_z0(int Q) {
  int o = 0;
  for (int q = 0; q < Q; ++q)
    for (int k = 0; k < (q % 4 == 1 ? 0 : 3); ++k) { const int J = o / 11; if ((J + 1) * (J + 2) / 2 - 1 + (J ? 4 : 7) <= q) ++o; }
  return o;
}
constexpr int cv_xd0(int T) { int o = 0; for (int t = 0; t < T; ++t) o += t % 4 != 1 || (t >= 72 && t < 108); return o < 96 ? o : 96; }
static_assert(cv_xd0(144) == 96);
template <int B, int... Is> __device__ __forceinline__ void cv_zs(CV& c, iseq<Is...>) { (cv_z<B + Is>(c), ...); }
template <int R, int Q> __device__ __forceinline__ void cv_slot(CV& c) {
  constexpr int i = cv_i(R == 7, Q), J = cv_J(R == 7, Q), d = 128 + 4 * J, a = 32 + 32 * (R & 1) + 4 * i, b = 192 + 32 * (R & 1) + 4 * (J - i);
  constexpr int T = 36 * R + Q, vm = Q % 4 == 1 && Q < 32 ? Q / 4 : -1;
  if constexpr (Q == 0) asm volatile("s_waitcnt lgkmcnt(0)");
  if constexpr (R >= 6 && vm >= 0 && vm % 4 == 0) asm volatile("s_add_u32 m0, s79, %0" ::"i"(vm * 1024));
  if constexpr (R == 0 && i == 0)
    asm volatile("v_mfma_f32_16x16x32_bf16 v[%c0:%c1], a[%c2:%c3], a[%c4:%c5], 0" ::"i"(d), "i"(d + 3), "i"(a), "i"(a + 3), "i"(b), "i"(b + 3));
  else
    asm volatile("v_mfma_f32_16x16x32_bf16 v[%c0:%c1], a[%c2:%c3], a[%c4:%c5], v[%c0:%c1]" ::"i"(d), "i"(d + 3), "i"(a), "i"(a + 3), "i"(b), "i"(b + 3));
  if constexpr (Q < 24 && R < 7) cv_rd<R + 1, Q>(c);
  if constexpr (T % 7 == 3 && NS_X + 5 + T / 7 < 32) d_store<NS_X + 5 + T / 7>();
  if constexpr (R == 2 && Q >= 24 && Q < 32)
    asm volatile("ds_read_b64 v[%c1:%c2], %0 offset:%c3" ::"v"(c.px), "i"(96 + 2 * (Q - 24)), "i"(97 + 2 * (Q - 24)), "i"(512 * (31 - Q)));
  if constexpr (R >= 3 && R < 7) if constexpr (cv_xd0(T - 107) > cv_xd0(T - 108)) cv_xd<cv_xd0(T - 108)>(c);
  if constexpr ((R == 3 || R == 4) && vm >= 0) {
    constexpr int f = (R - 3) * 8 + vm;
    asm volatile("buffer_load_dwordx4 a[%c0:%c1], v84, s[88:91], s96 offen offset:%2 sc1" ::"i"(128 + 4 * f), "i"(131 + 4 * f), "i"((f & 3) * 1024) : "memory");
    if constexpr ((f & 3) == 3) asm volatile("s_addk_i32 s96, %0" ::"i"(f == 7 ? 28672 : 4096));
  }
  if constexpr (R == 4 && Q == 35) asm volatile(FL_POLL ::: "memory");
  if constexpr (R >= 6 && vm >= 0)
    asm volatile("buffer_load_dwordx4 v%c0, s[88:91], s98 offen offset:%1 sc1 lds" ::"i"(86 + vm / 4), "i"((vm & 3) * 1024) : "memory");
  if constexpr (R == 6 && Q == 35) asm volatile("s_add_u32 s98, s98, 0x8000\n\ts_xor_b32 s79, s79, 0x8000");
  if constexpr (R == 7) cv_zs<cv_z0(Q)>(c, make_iseq<cv_z0(Q + 1) - cv_z0(Q)>{});
  __builtin_amdgcn_sched_barrier(0);
}
template <int R, int... Q> __device__ __forceinline__ void cv_step(CV& c, iseq<Q...>) { (cv_slot<R, Q>(c), ...); }
template <int... N> __device__ __forceinline__ void cv_rd0(CV& c, iseq<N...>) { (cv_rd<0, N>(c), ...); }
template <int FLAG> __device__ __forceinline__ void conv_wave(unsigned sz, float Dd, char* lw) {
  int lane = threadIdx.x & 63; asm volatile("" : "+v"(lane));
  const int l16 = lane & 15, fq = lane >> 4;
  u32x4c w[5]; unsigned w4[5];
#pragma unroll
  for (int r = 0; r < 5; ++r) {
    w[r] = *(const u32x4c*)(lw + r * 1024 + lane * 16);
    w4[r] = *(const unsigned*)(lw + r * 1024 + lane * 16 + 16);
  }
  d_store<NS_X>(); d_store<NS_X + 1>();
#pragma unroll
  for (int r = 0; r < 5; ++r)
    *(u32x4c*)(lw + 10304 + r * 1024 + lane * 16) = (u32x4c){__builtin_amdgcn_alignbit(w[r][1], w[r][0], 16), __builtin_amdgcn_alignbit(w[r][2], w[r][1], 16),
                                                             __builtin_amdgcn_alignbit(w[r][3], w[r][2], 16), __builtin_amdgcn_alignbit(w4[r], w[r][3], 16)};
  d_store<NS_X + 2>();
  CV c;
#pragma unroll
  for (int i = 0; i < 4; ++i) c.pa[i] = (unsigned)(size_t)lw + ((l16 + 1) & 1) * 10304 + 4 * ((8 * fq + l16 + 1) >> 1) + 1024 * i;
  c.pb = (unsigned)(size_t)lw + 5120 + (2032 - 16 * l16 + 8 * fq) * 2 - 512 * 7;
  c.vz = (16 * l16 + 4 * fq) * 2;
  c.px = (unsigned)(size_t)lw + 5120 + 2044 * 2 - 512 * 7 - c.vz;
  c.sz = sz; c.D = Dd;
  asm volatile("" : "+v"(c.vz), "+v"(c.px), "+v"(c.D) :: "memory");
  asm volatile("" : "+s"(c.sz));
  cv_rd0(c, make_iseq<24>{});
  d_store<NS_X + 3>(); d_store<NS_X + 4>();
  __builtin_amdgcn_sched_barrier(0);
  static_for([&](auto R) __attribute__((always_inline)) {
    cv_step<R>(c, make_iseq<36>{});
    if constexpr (R == 2) {
      asm volatile("s_waitcnt vmcnt(0)\n\ts_barrier" ::: "memory");
      fl_set<FLAG>();
    }
    if constexpr (R == 5) {
      asm volatile("s_waitcnt vmcnt(0)" ::: "memory");
      fl_check();
    }
    __builtin_amdgcn_sched_barrier(0);
  }, make_iseq<8>{});
  asm volatile("s_nop 7");
  cv_zs<cv_z0(36)>(c, make_iseq<88 - cv_z0(36)>{});
}

__device__ __forceinline__ void post_dma(int cg, int tb, const bf16* __restrict__ q, unsigned m0w) {
  int lane = threadIdx.x & 63; asm volatile("" : "+v"(lane));
  const __amdgpu_buffer_rsrc_t sq = __builtin_amdgcn_make_buffer_rsrc((void*)(q + (size_t)tb * 32 * DA + cg * 64), (short)0, 32 * DA * 2, 0x00020000);
  const unsigned vq = ((lane >> 3) * DA + (lane & 7) * 8) * 2;
  asm volatile("s_mov_b32 m0, %0\n\ts_nop 4\n\tbuffer_load_dwordx4 %1, %2, 0 offen lds\n\tbuffer_load_dwordx4 %1, %2, %3 offen offset:1024 lds\n\t"
               "buffer_load_dwordx4 %1, %2, %4 offen offset:2048 lds\n\tbuffer_load_dwordx4 %1, %2, %5 offen offset:3072 lds"
               ::"s"(m0w + 4096), "v"(vq), "s"(sq), "s"(8 * DA * 2 - 1024), "s"(16 * DA * 2 - 2048), "s"(24 * DA * 2 - 3072) : "memory");
}
__device__ __forceinline__ void post_math(int cg, int tb, bf16* __restrict__ out, const char* lw) {
  int lane = threadIdx.x & 63; asm volatile("" : "+v"(lane));
  const int tl = lane >> 3, ck = lane & 7;
  const __amdgpu_buffer_rsrc_t so = __builtin_amdgcn_make_buffer_rsrc((void*)(out + (size_t)tb * 32 * OUTW + cg * 64), (short)0, 32 * OUTW * 2 - 2 * DA, 0x00020000);
  const unsigned vo = (tl * OUTW + ck * 8) * 2;
#pragma unroll
  for (int i = 0; i < 4; ++i) {
    const u32x4c qv = *(const u32x4c*)(lw + 4096 + i * 1024 + lane * 16);
    const char* lz = lw + ck * 64 + 16 * (i ^ (ck >> 1)) + tl * 2;
    u32x4c o;
#pragma unroll
    for (int e = 0; e < 4; ++e) {
      const float z0 = bf2f(*(const unsigned short*)(lz + 2 * e * 512));
      const float z1 = bf2f(*(const unsigned short*)(lz + (2 * e + 1) * 512));
      o[e] = pk2(bfl(qv[e]) * z0, bfh(qv[e]) * z1);
    }
    __builtin_amdgcn_raw_buffer_store_b128(o, so, vo + i * 8 * OUTW * 2, 0, 17);
  }
}

#define DI __device__ __forceinline__
#define GAP() __builtin_amdgcn_sched_barrier(0)
typedef __attribute__((ext_vector_type(4))) float f32x4g;
typedef __attribute__((ext_vector_type(4))) unsigned u32x4g;
constexpr int LDW = NH * HD;
struct GM { unsigned aA[2], aB[2][8]; };
template <bool W, int J, int V> DI void g_dma() {
  if constexpr (J == 0) asm volatile("buffer_load_dwordx4 v%c0, s[%c1:%c2], 0 offen lds" ::"i"(V), "i"(W ? 60 : 56), "i"(W ? 63 : 59) : "memory");
  else asm volatile("buffer_load_dwordx4 v%c0, s[%c1:%c2], s%c3 offen lds" ::"i"(V), "i"(W ? 60 : 56), "i"(W ? 63 : 59), "i"((W ? 70 : 63) + J) : "memory");
}
template <int P, int KS, int I> DI void g_wread(GM& g) {
  constexpr int nf = I >> 1, hf = I & 1, r = 192 - 64 * P + 8 * nf + 4 * KS + 2 * hf;
  asm volatile("ds_read_b64_tr_b16 a[%c1:%c2], %0 offset:%c3" ::"v"(g.aB[hf][nf]), "i"(r), "i"(r + 1), "i"(P * 32768 + KS * 16384));
}
template <int P, int M, int KS> DI void g_aread(GM& g) {
  constexpr int r = 96 + 8 * (M & 1) + 4 * KS;
  asm volatile("ds_read_b128 v[%c1:%c2], %0 offset:%c3" ::"v"(g.aA[KS]), "i"(r), "i"(r + 3), "i"(P * 32768 + M * 2048));
}
constexpr float CS = 0.0625f * 1.4426950408889634f;
constexpr int NS_Q = 19;
constexpr int e_vm(int G) { return (G * 3) % 8 < 3 ? G * 3 / 8 : -1; }
constexpr int e_st(int MODE, int G) {
  const int q = G & 15, k3 = ((G >> 4) - 1) * 3 + (q == 0 ? 0 : q == 4 ? 1 : 2), k2 = NS_Q + (G >> 4) * 2 + (q == 4);
  if (MODE == 5) return e_vm(G) >= 32 ? e_vm(G) - 32 : -1;
  if (MODE == 3) return G >= 33 && (G - 33) % 5 == 0 && (G - 33) / 5 < NS_X ? (G - 33) / 5 : -1;
  if (MODE == 2) return G >= 30 && (q == 0 || q == 4 || q == 14) && k3 - 2 < NS_Q ? k3 - 2 : -1;
  if (MODE == 4) return (q == 0 || q == 4) && k2 < 32 ? k2 : -1;
  return -1;
}
constexpr int e_nst(int MODE, int G0, int G1) { int n = 0; for (int G = G0; G < G1; ++G) n += e_st(MODE, G) >= 0; return n; }
static_assert(e_nst(2, 0, 128) == NS_Q && e_nst(3, 30, 128) == NS_X && e_nst(5, 0, 128) == 16 && e_nst(4, 0, 104) == 32 - NS_Q);
constexpr int e_slot(int MODE, int G) { return MODE == 3 ? (G % 3 == 0 && G < 30 ? G / 3 : -1) : e_vm(G) >= 0 && e_vm(G) < 32 ? 10 + e_vm(G) : -1; }
struct ES { int op0[129], nop[129]; };
constexpr ES e_sched(bool TR, int MODE) {
  ES z{};
  const bool SCL = MODE == 2 || MODE == 4;
  const int per = SCL ? 16 : 8, o1 = 32 * per;
  int o = MODE == 4 ? e_sched(false, 2).op0[128] : 0;
  for (int G = 0; G <= 128; ++G) {
    const int q = G & 15;
    int cap = 2 - (q == 1 || q == 2 || q == 15);
    if (SCL) cap -= (q >= 3 && q < 11 && (q & 1)) + (q == 7 || q == 11) + 2 * (G == 70 || G == 74 || (G & 63) == 61);
    cap += MODE == 2 ? 4 + (q == 10 || q == 13) : MODE == 3 ? 1 : MODE == 5 ? 2 : 0;
    if (e_st(MODE, G) >= 0 || (SCL ? q == 8 || q == 12 : e_slot(MODE, G) >= 0) || (MODE == 4 && G < 4)) cap = 0;
    if (G == 128) cap = 10000;
    z.op0[G] = o;
    while (o < o1) {
      const int i = o / per, rdy = MODE == 4 ? 0 : (TR ? 32 * (i >> 3) + 16 + 2 * (i & 7) + 1 : 4 * i + 3) + 6;
      if ((G < rdy && G < 128) || cap-- <= 0) break;
      ++o; ++z.nop[G];
    }
  }
  return z;
}
template <bool TR, int MODE> constexpr ES ESCH = e_sched(TR, MODE);
static_assert(ESCH<false, 4>.op0[96] == 512 && ESCH<false, 4>.op0[0] >= 24 * 16);
template <bool TR, bool SCL, int O> DI void e_op() {
  constexpr int per = SCL ? 16 : 8, i = O / per, k = O % per, r = 128 + 4 * i, t = 114 + 4 * (i & 1), c = k - (per - 4);
  if constexpr (k < 4) asm volatile("v_accvgpr_read_b32 v%c0, a[%c1]" ::"i"(t + k), "i"(4 * i + k));
  else if constexpr (c < 0) asm volatile("v_mul_f32 v%c0, s83, v%c0" ::"i"(k < 8 ? r + (k ^ 6) : t + k - 8));
  else {
    constexpr int d = c == 0 ? r + 3 : c == 1 ? r + 2 : r + c - 2, x = c == 0 ? r + 2 : c == 1 ? r : t + 2 * (c - 2);
    asm volatile("v_cvt_pk_bf16_f32 v%c0, v%c1, v%c2" ::"i"(d), "i"(x), "i"(x + 1));
  }
}
template <bool TR, bool SCL, int B, int... Is> DI void e_ops(iseq<Is...>) { (e_op<TR, SCL, B + Is>(), ...); }
template <int S> DI void e_m0() {
  if constexpr (S == 0 || S == 1 || S == 5 || S == 9) asm volatile("s_add_u32 m0, s78, %0" ::"i"(S * 1024));
  if constexpr (S >= 26 && (S - 26) % 4 == 0) asm volatile("s_add_u32 m0, s79, %0" ::"i"(((S - 26) >> 3) * 32768 + ((S - 26) & 4) * 1024));
}
template <int S> DI void e_load() {
  if constexpr (S == 0) asm volatile("buffer_load_dwordx4 v122, s[56:59], 0 offen lds" ::: "memory");
  else if constexpr (S < 5) asm volatile("buffer_load_dwordx4 v84, s[56:59], s64 offen offset:%0 lds" ::"i"((S - 1) * 1024) : "memory");
  else if constexpr (S < 9) asm volatile("buffer_load_dwordx4 v84, s[60:63], 0 offen offset:%0 sc1 lds" ::"i"((S - 5) * 1024) : "memory");
  else if constexpr (S == 9) asm volatile("buffer_load_dwordx4 v84, s[60:63], s62 offen sc1 lds" ::: "memory");
  else if constexpr (S < 26) {
    constexpr int f = S - 10;
    asm volatile("buffer_load_dwordx4 a[%c0:%c1], v84, s[88:91], s96 offen offset:%2 sc1" ::"i"(128 + 4 * f), "i"(131 + 4 * f), "i"((f & 3) * 1024) : "memory");
    if constexpr ((f & 3) == 3) asm volatile("s_addk_i32 s96, %0" ::"i"(f == 7 ? 28672 : 4096));
  } else {
    constexpr int f = S - 26;
    asm volatile("buffer_load_dwordx4 v%c0, s[88:91], s98 offen offset:%1 sc1 lds" ::"i"(86 + ((f >> 2) & 1)), "i"((f & 3) * 1024) : "memory");
    if constexpr (f == 7) asm volatile("s_add_u32 s98, s98, s97");
    if constexpr (f == 15) asm volatile(FL_POLL ::: "memory");
  }
}
template <bool TR, int P, int H, int G, int MODE> DI void g_gap(GM& g) {
  constexpr int m = 4 * H + (G >> 4), q = G & 15, n = q >> 1, ks = q & 1, GG = 64 * H + G;
  constexpr bool inV = TR ? m & 1 : n & 1, END = MODE == 3 || MODE == 5;
  constexpr int a = 96 + 8 * (m & 1) + 4 * ks, b = 192 - 64 * P + 8 * n + 4 * ks, d = (inV ? 128 : 0) + 4 * (TR ? 8 * (m >> 1) + n : 4 * m + (n >> 1));
  constexpr int J = END ? -1 : q == 8 ? 2 * (G >> 4) : q == 12 ? 2 * (G >> 4) + 1 : -1, gj = (J & 3) ^ ((J & 3) >> 1);
  if constexpr (END) { if constexpr (q == 0 && GG > 0) asm volatile("s_waitcnt lgkmcnt(0)"); e_m0<e_slot(MODE, GG)>(); }
  else if constexpr (q == 0 && G > 0) asm volatile("s_waitcnt lgkmcnt(4)");
  if constexpr (J == 0) asm volatile("s_add_u32 m0, s78, %0" ::"i"((P ^ H ^ 1) * 32768 + (1 - H) * 8192));
  else if constexpr (J == 4) asm volatile("s_add_u32 m0, s79, %0" ::"i"((P ^ H) * 32768 + (1 - H) * 16384));
  else if constexpr (J > 0 && (J & 3) == 1) asm volatile("s_bitset1_b32 m0, 12");
  else if constexpr (J > 0 && (J & 3) == 2) asm volatile("s_bitset1_b32 m0, %0" ::"i"(J < 4 ? 14 : 13));
  else if constexpr (J > 0) asm volatile("s_bitset0_b32 m0, 12");
#define GMF(D, X, Y, C) asm volatile("v_mfma_f32_16x16x32_bf16 " D "[%c0:%c1], " X ", " Y ", " C ::"i"(d), "i"(d + 3), "i"(a), "i"(a + 3), "i"(b), "i"(b + 3))
#define GMF2(D, C) do { if constexpr (TR) GMF(D, "v[%c2:%c3]", "a[%c4:%c5]", C); else GMF(D, "a[%c4:%c5]", "v[%c2:%c3]", C); } while (0)
  if constexpr ((MODE == 1 || MODE == 4) && ks == 0) { if constexpr (inV) GMF2("v", "0"); else GMF2("a", "0"); }
  else if constexpr (inV) GMF2("v", "v[%c0:%c1]");
  else GMF2("a", "a[%c0:%c1]");
#undef GMF2
#undef GMF
  if constexpr ((q == 1 || q == 2) && !(END && m == 7)) g_aread<(m < 7 ? P : P ^ 1), (m + 1) & 7, q - 1>(g);
  if constexpr (!END && q >= 3 && q < 11 && (q & 1)) g_wread<P ^ 1, H, (G >> 4) * 4 + (q - 3) / 2>(g);
  if constexpr (e_st(MODE, GG) >= 0) {
    static_assert(ESCH<TR, MODE>.op0[GG] >= (e_st(MODE, GG) + 1) * (END ? 8 : 16));
    d_store<e_st(MODE, GG)>();
  }
  if constexpr (J >= 0 && J < 4) g_dma<false, (1 - H) * 2 + (gj & 1) + (gj >> 1) * 4, 90>();
  else if constexpr (J >= 4) g_dma<true, (1 - H) * 4 + gj, 91>();
  if constexpr (MODE >= 2) e_ops<TR, !END, ESCH<TR, MODE>.op0[GG]>(make_iseq<ESCH<TR, MODE>.nop[GG]>{});
  if constexpr (END) {
    if constexpr (e_slot(MODE, GG) >= 0) e_load<e_slot(MODE, GG)>();
  } else {
    if constexpr (H == 1 && G == 6) asm volatile("v_lshl_add_u32 v90, s82, 7, v92\n\ts_and_b32 s82, s80, 15");
    if constexpr (H == 1 && G == 10) asm volatile("s_lshr_b64 s[80:81], s[80:81], 4\n\tv_lshl_add_u32 v91, s82, 19, v93");
    if constexpr (G == 61) {
      constexpr int nst = e_nst(MODE, H ? 29 : 0, GG + 1) + (MODE == 4 && !H ? e_nst(2, 93, 128) : 0);
      asm volatile("s_waitcnt vmcnt(%0) lgkmcnt(0)\n\ts_barrier" ::"i"(12 + nst) : "memory");
    }
  }
  if constexpr (END && GG == 127) {
    asm volatile("s_nop 7");
    e_ops<TR, false, ESCH<TR, MODE>.op0[128]>(make_iseq<ESCH<TR, MODE>.nop[128]>{});
  }
  GAP();
}
template <bool TR, int P, int H, int MODE, int... Gs> DI void g_half_(GM& g, iseq<Gs...>) { (g_gap<TR, P, H, Gs, MODE>(g), ...); }
template <bool TR, int P, int H, int MODE> DI void g_half(GM& g) { g_half_<TR, P, H, MODE>(g, make_iseq<64>{}); }
template <bool TR, int P, int MODE> DI void g_tile(GM& g) { g_half<TR, P, 0, MODE>(g); g_half<TR, P, 1, MODE>(g); }
template <int P, int... Is> DI void g_read0(GM& g, iseq<Is...>) {
  (g_wread<P, 0, Is>(g), ...); (g_wread<P, 1, Is>(g), ...);
  g_aread<P, 0, 0>(g); g_aread<P, 0, 1>(g);
}
DI void g_proW(unsigned g, unsigned mW) {
  asm volatile("v_lshl_add_u32 v91, %0, 18, v93\n\ts_mov_b32 m0, %1" ::"s"(g), "s"(mW));
  asm volatile("s_nop 0"); g_dma<true, 0, 91>();
  asm volatile("s_bitset1_b32 m0, 12\n\ts_nop 0"); g_dma<true, 1, 91>();
  asm volatile("s_bitset1_b32 m0, 13\n\ts_nop 0"); g_dma<true, 3, 91>();
  asm volatile("s_bitset0_b32 m0, 12\n\ts_nop 0"); g_dma<true, 2, 91>();
}
template <int PART = 3> DI void g_pro(unsigned g, unsigned mA, unsigned mW, unsigned lda) {
  g &= 3;
  if constexpr (PART & 1)
    asm volatile("v_add_u32 v90, %0, v92\n\ts_mov_b32 m0, %1" ::"s"((g >> 1) * 128 + (g & 1) * 128 * lda), "s"((mA ^ (g >> 1) * 32768) + (g & 1) * 8192));
  if constexpr (PART == 1) return;
  asm volatile("s_nop 0"); g_dma<false, 0, 90>();
  asm volatile("s_bitset1_b32 m0, 12\n\ts_nop 0"); g_dma<false, 1, 90>();
  asm volatile("s_bitset1_b32 m0, 14\n\ts_nop 0"); g_dma<false, 5, 90>();
  asm volatile("s_bitset0_b32 m0, 12\n\ts_nop 0"); g_dma<false, 4, 90>();
  g_proW(g, mW ^ g * 16384);
}
template <bool TR, bool SCL, class HOOK> DI void g_loop(GM& g, unsigned evm, HOOK&& hook) {
  constexpr int nkt = SCL ? 7 : 16, P0 = SCL ? 0 : 1;
  asm volatile("" : "+s"(evm));
#pragma unroll 1
  for (int t = 1;; t += 2) {
    g_half<TR, P0 ^ 1, 0, 0>(g);
    if (__builtin_expect((evm >> t) & 1, 0)) { hook(t); GAP(); }
    g_half<TR, P0 ^ 1, 1, 0>(g);
    if (SCL && t + 2 >= nkt) break;
    g_half<TR, P0, 0, 0>(g);
    if constexpr (SCL) { if (__builtin_expect(t == 3, 0)) { hook(4); GAP(); } }
    else asm volatile("s_cmp_eq_u32 %0, 13\n\ts_cselect_b32 s78, 0x28000, s78" ::"s"(t) : "scc");
    g_half<TR, P0, 1, 0>(g);
    if (!SCL && t + 3 >= nkt) break;
  }
}

typedef __attribute__((ext_vector_type(4))) unsigned u32x4;
typedef __attribute__((ext_vector_type(4))) float f32x4;

constexpr int R_S = 128, R_P = 192, R_CI = 224, R_E = 96;
constexpr int R_TS = 112, R_RS = 114, R_M = 116;
constexpr int R_PD = 118, R_THR = 120, R_TMP = 121;
constexpr int r_dk(int q) { return q < 6 ? 90 + q : 120 + q; }
constexpr int S_V = 96, S_T = 97, S_K = 98, S_I = 99, S_EV = 100;
struct AC {
  unsigned rare;
  unsigned tb, vs, o0, o1;
};
DI bool redo() { u64 t; asm volatile("s_mov_b64 %0, s[54:55]" : "=s"(t)); return t != 0; }
template <int X, int F> DI void kread() {
  constexpr int r = 192 + 4 * (F & 15);
  asm volatile("ds_read_b128 a[%c0:%c1], v84 offset:%c2" ::"i"(r), "i"(r + 3), "i"(X * 32768 + F * 1024));
}
template <int X, int F> DI void vread() {
  constexpr int r = 192 + 4 * (F & 15);
  asm volatile("ds_read_b128 a[%c0:%c1], v85 offset:%c2" ::"i"(r), "i"(r + 3), "i"(X * 32768 + (F & 1) * 16384 + (F >> 1) * 1024));
}
template <int X, int... Fs> DI void kreads(iseq<Fs...>) { (kread<X, Fs>(), ...); }
template <int X, int J> DI void dma_m0() {
  if constexpr (J == 0) asm volatile("s_mov_b32 m0, s%c0" ::"i"(78 + X));
  else if constexpr (J == 4) asm volatile("s_or_b32 m0, s%c0, 0x1000" ::"i"(78 + X));
  else if constexpr (J == 8) asm volatile("s_bitset0_b32 m0, 16");
  else if constexpr (J == 12) asm volatile("s_bitset0_b32 m0, 12");
}
template <int J> DI void dma_at() {
  if constexpr (J < 8)
    asm volatile("buffer_load_dwordx4 v%c0, s[88:91], s96 offen offset:%1 sc1 lds" ::"i"(86 + (J >= 4)), "i"((J & 3) * 1024) : "memory");
  else
    asm volatile("buffer_load_dwordx4 v%c0, s[92:95], s98 offen offset:%1 sc1 lds" ::"i"(r_dk(J - 8)), "i"((J & 3) * 1024) : "memory");
}
template <int Q> DI void dma4k() {
  asm volatile("s_xor_b32 m0, s79, %0\n\ts_add_u32 s48, s98, %1\n\tbuffer_load_dwordx4 v%c2, s[88:91], s48 offen sc1 lds\n\t"
               "buffer_load_dwordx4 v%c2, s[88:91], s48 offen offset:1024 sc1 lds\n\tbuffer_load_dwordx4 v%c2, s[88:91], s48 offen offset:2048 sc1 lds\n\t"
               "buffer_load_dwordx4 v%c2, s[88:91], s48 offen offset:3072 sc1 lds"
               ::"i"((Q >> 1) * 32768 + (Q & 1) * 4096), "i"((Q >> 1) * 32768), "i"(86 + (Q & 1)) : "memory", "scc");
}
template <int Q> DI void acc_zero2() {
  asm volatile("v_accvgpr_write_b32 a[%c0], 0\n\tv_accvgpr_write_b32 a[%c1], 0" ::"i"(2 * Q), "i"(2 * Q + 1));
}
#define SMV 0
struct SMS { int ex[128], op0[128], nop[128], last; };
constexpr SMS sm_sched() {
  SMS z{};
  int et[32] = {}, e = 0;
  for (int t = 0; t < 128; ++t) {
    const int g = t & 15;
    const bool x = t < 48 ? (g >= 8 && (g & 1) && g < 15) : t < 64 ? (g >= 9 && g < 15) : (g >= 8 && g < 14);
    z.ex[t] = x && e < 32 ? e : -1;
    if (z.ex[t] >= 0) et[e++] = t;
  }
  int o = 0;
  for (int t = 0; t < 128; ++t) {
    const int g = t & 15;
    int cap = z.ex[t] >= 0 || t < 8 || (t < 48 && (g == 7 || (g >= 8 && !(g & 1)))) || t == 56 || t == 63 || t == 111 ? 0 : 1;
    z.op0[t] = o; z.nop[t] = 0;
    while (cap > 0 && o < 48 && et[2 * (o / 3) + (o % 3 == 0 ? 0 : 1)] + 2 <= t) {
      const int el = 2 * (o / 3) + o % 3;
      if (!(o % 3 < 2 && (el == 0 || el == 4))) --cap;
      ++o; ++z.nop[t]; z.last = t;
    }
  }
  for (int k = 16; k < 32; ++k) if (z.op0[et[k]] < 3 * ((k - 16) / 2 + 1)) z.last = 1000;
  return z;
}
constexpr SMS SM = sm_sched();
static_assert(SM.last <= 117);
constexpr int p_reg(int Y, int k) { return R_P + 16 * Y + 4 * ((k >> 3) * 2 + ((k >> 1) & 1)) + ((k >> 2) & 1) * 2 + (k & 1); }
template <int Y, int O> DI void sm_op() {
  constexpr int k = O / 3, e = 2 * k + O % 3, ts = R_TS + ((e >> 2) & 1);
  if constexpr (O % 3 == 2) asm volatile("v_cvt_pk_bf16_f32 v%c0, v%c1, v%c2" ::"i"(p_reg(Y, k)), "i"(R_E + ((2 * k) & 15)), "i"(R_E + ((2 * k + 1) & 15)));
  else if constexpr (e == 1 || e == 5) asm volatile("v_add_f32_e32 v%c0, v%c1, v%c2" ::"i"(ts), "i"(R_E + e - 1), "i"(R_E + e));
  else if constexpr (e != 0 && e != 4) asm volatile("v_add_f32_e32 v%c0, v%c0, v%c1" ::"i"(ts), "i"(R_E + (e & 15)));
}
template <int Y, int T> DI void sm_gap(AC& c) {
  if constexpr (SM.ex[T] >= 0) asm volatile("v_exp_f32 v%c0, v%c1" ::"i"(R_E + (SM.ex[T] & 15)), "i"(R_S + 32 * Y + SM.ex[T]));
  if constexpr (SM.nop[T] > 0) sm_op<Y, SM.op0[T]>();
  if constexpr (SM.nop[T] > 1) sm_op<Y, SM.op0[T] + 1>();
  if constexpr (SM.nop[T] > 2) sm_op<Y, SM.op0[T] + 2>();
  if constexpr (T == SM.last + 1) asm volatile("v_add_f32_e32 v%c0, v%c1, v%c2" ::"i"(R_TMP), "i"(R_TS), "i"(R_TS + 1));
  if constexpr (T == SM.last + 2) asm volatile("v_cmp_nle_f32_e64 s[54:55], v%c0, v%c1" ::"i"(R_TMP), "i"(R_THR));
}
template <int X, int MODE, int G> DI void gapA(AC& c) {
  constexpr bool FIRST = MODE == 1 || MODE == 2, NOQK = MODE == 3;
  constexpr int Xp = X ^ 1, kb = G >> 4, g = G & 15, qb = g >> 3, s = g & 7;
  if constexpr (MODE == 3 && G < 48 && G % 3 == 0) {
    constexpr int f = G / 3;
    asm volatile("buffer_load_dwordx4 a[%c0:%c1], v84, s[88:91], s75 offen offset:%2 sc1" ::"i"(128 + 4 * f), "i"(131 + 4 * f), "i"((f & 3) * 1024) : "memory");
    if constexpr ((f & 3) == 3) asm volatile("s_addk_i32 s75, %0" ::"i"(f == 7 ? 28672 : 4096));
  }
  if constexpr (G == 48) asm volatile("s_waitcnt vmcnt(%0) lgkmcnt(0)\n\ts_barrier" ::"i"(MODE == 3 ? 16 : MODE == 2 ? 12 : MODE == 1 ? 7 : 0) : "memory");
  else if constexpr (g == 0 && !NOQK) asm volatile("s_waitcnt lgkmcnt(0)");
  if constexpr (!NOQK) {
    constexpr int r = 192 + 4 * ((kb & 1) * 8 + s), q = 128 + 4 * g;
    constexpr int d = R_S + 32 * X + 4 * (kb * 2 + qb), ci = s == 0 ? R_CI + 4 * (kb * 2 + qb) : d;
    if constexpr (FIRST && s == 0)
      asm volatile("v_mfma_f32_16x16x32_bf16 v[%c0:%c1], a[%c2:%c3], a[%c4:%c5], 0" ::"i"(d), "i"(d + 3), "i"(r), "i"(r + 3), "i"(q), "i"(q + 3));
    else
      asm volatile("v_mfma_f32_16x16x32_bf16 v[%c0:%c1], a[%c2:%c3], a[%c4:%c5], v[%c6:%c7]" ::"i"(d), "i"(d + 3), "i"(r), "i"(r + 3), "i"(q), "i"(q + 3),
                   "i"(ci), "i"(ci + 3));
    if constexpr (g < 8 && kb < 3) kread<X, (kb + 1) * 8 + g>();
    constexpr int S0 = MODE == 1 ? 0 : SMV ? 40 : 14, S1 = MODE == 1 ? 1 : SMV ? 41 : 30, S2 = SMV ? 42 : 45, S3 = SMV ? 43 : 46;
    if constexpr (G == S0) asm volatile("s_add_u32 s96, s97, 0x1000000");
    if constexpr (G == S1) dma_m0<X, 0>();
    if constexpr (G == S2) asm volatile("s_mov_b32 s97, s98");
    if constexpr (G == S3) asm volatile("s_add_u32 s98, s98, s99");
    if constexpr (MODE != 1 && G >= 56 && !(G & 1)) dma_at<(G - 56) / 2>();
    if constexpr (MODE == 1 && (G & 11) == 9) dma_at<(G >> 4) * 2 + ((G >> 2) & 1)>();
    if constexpr (MODE == 1 && G == 31) dma_m0<X, 4>();
    if constexpr (G == 57) asm volatile("s_lshr_b32 s100, s100, 1\n\ts_add_u32 s56, s56, 1");
    if constexpr (G == 59) asm volatile("s_getpc_b64 s[86:87]\nev_pc%c0:\n\ts_add_u32 s86, s86, ev_ret%c0-ev_pc%c0\n\ts_addc_u32 s87, s87, 0" ::"i"(X * 4 + MODE));
    if constexpr (G == 61 && !FIRST) asm volatile("s_or_b32 %0, s54, s55\n\ts_bitcmp1_b32 s100, 0" : "=s"(c.rare));
    if constexpr (G == 61 && FIRST) asm volatile("s_bitcmp1_b32 s100, 0");
  } else if constexpr (SM.nop[64 + G] > 0) asm volatile("s_nop 0");
  if constexpr (!FIRST && g < 8 && kb == 3) vread<Xp, g>();
  if constexpr (MODE == 2 && (G & 3) == 0) d_store<16 + G / 4>();
  if constexpr (FIRST) acc_zero2<G>();
  else sm_gap<Xp, 64 + G>(c);
  if constexpr (G == 63 && !NOQK) asm volatile("s_cbranch_scc1 ev_entry\nev_ret%c0:" ::"i"(X * 4 + MODE) : "vcc", "memory");
  GAP();
}
template <int R> DI void rescale1(float alpha) {
  float t;
  asm volatile("v_accvgpr_read_b32 %0, a[%c2]\n\tv_mul_f32 %0, %0, %1\n\tv_accvgpr_write_b32 a[%c2], %0" : "=&v"(t) : "v"(alpha), "i"(R));
}
template <int... Rs> DI void rescale_all(const float (&alpha)[2], iseq<Rs...>) { (rescale1<Rs>(alpha[(Rs >> 2) & 1]), ...); }
template <bool MAX> DI float row_reduce(float v) {
  auto p = __builtin_amdgcn_permlane32_swap(__float_as_uint(v), __float_as_uint(v), false, false);
  v = MAX ? fmaxf(__uint_as_float(p[0]), __uint_as_float(p[1])) : __uint_as_float(p[0]) + __uint_as_float(p[1]);
  p = __builtin_amdgcn_permlane16_swap(__float_as_uint(v), __float_as_uint(v), false, false);
  return MAX ? fmaxf(__uint_as_float(p[0]), __uint_as_float(p[1])) : __uint_as_float(p[0]) + __uint_as_float(p[1]);
}
template <int Y, int QB> DI float row_max() {
  constexpr int r = R_S + 32 * Y + 4 * QB;
  float mx;
  asm volatile("v_max_f32 %0, v%c1, v%c2\n\tv_max3_f32 %0, %0, v%c3, v%c4" : "=&v"(mx) : "i"(r), "i"(r + 1), "i"(r + 2), "i"(r + 3));
  static_for([&](auto K) {
    constexpr int q = r + 8 * (K + 1);
    float t = mx;
    asm volatile("v_max3_f32 %0, %0, v%c1, v%c2\n\tv_max3_f32 %0, %0, v%c3, v%c4" : "+v"(t) : "i"(q), "i"(q + 1), "i"(q + 2), "i"(q + 3));
    mx = t;
  }, make_iseq<3>{});
  return row_reduce<true>(mx);
}
template <int R> DI void v_sub(float d) { asm volatile("v_sub_f32 v%c0, v%c0, %1" ::"i"(R), "v"(d)); }
template <int B> DI void s_shift(const float (&d)[2]) { static_for([&](auto E) { v_sub<B + E>(d[(E >> 2) & 1]); }, make_iseq<32>{}); }
template <bool CI> DI void m_shift(const float (&d)[2]) {
  if constexpr (CI) s_shift<R_CI>(d);
  v_sub<R_M>(-d[0]); v_sub<R_M + 1>(-d[1]);
}
DI void set_mask0() {
  const int mr = __float_as_int(vget<89>());
  static_for([&](auto I) {
    constexpr int e = (I >> 2) * 8 + (I & 3), k = (I >> 2) * 16 + (I & 3);
    const float x = vget<R_S + e>();
    vset<R_S + e>(k > mr ? -INFINITY : x);
  }, make_iseq<16>{});
}
template <int X, bool CI = true> DI void sm_redo() {
  constexpr int Xp = X ^ 1;
  float d[2], dt[2], alpha[2], ts[2] = {0.f, 0.f};
  static_for([&](auto Q) {
    constexpr int qb = Q;
    const float pd = vget<R_PD + qb>();
    d[qb] = fmaxf(row_max<Xp, qb>() - pd, 0.f);
    dt[qb] = d[qb] + pd;
    vset<R_PD + qb>(d[qb]);
    alpha[qb] = __builtin_amdgcn_exp2f(-d[qb]);
    vset<R_RS + qb>(vget<R_RS + qb>() * alpha[qb]);
  }, make_iseq<2>{});
  vset<R_THR>(d[0] > 0.f || d[1] > 0.f ? -1.f : 4096.f);
  m_shift<CI>(d);
  static_for([&](auto K) {
    constexpr int k = K, qb = (k >> 1) & 1;
    const float e0 = __builtin_amdgcn_exp2f(vget<R_S + 32 * Xp + 2 * k>() - dt[qb]), e1 = __builtin_amdgcn_exp2f(vget<R_S + 32 * Xp + 2 * k + 1>() - dt[qb]);
    ts[qb] += e0; ts[qb] += e1;
    vset<p_reg(Xp, k)>(__uint_as_float(pk2(e0, e1)));
  }, make_iseq<16>{});
  vset<R_TS>(ts[0]); vset<R_TS + 1>(ts[1]);
  rescale_all(alpha, make_iseq<128>{});
  asm volatile("s_nop 1" ::: "memory");
  GAP();
}
template <int X, int MODE, int... Gs> DI void phaseA_(AC& c, iseq<Gs...>) { (gapA<X, MODE, Gs>(c), ...); }
template <int X, int MODE> DI void phaseA(AC& c) { phaseA_<X, MODE>(c, make_iseq<64>{}); }

template <int X, bool SMX, int G> DI void gapB(AC& c) {
  constexpr int Xp = X ^ 1, blk = G >> 4, g = G & 15;
  if constexpr (g == 0) asm volatile("s_waitcnt lgkmcnt(0)");
  constexpr int qb = (g >> 1) & 1, s = g & 1, r = 192 + 4 * ((blk & 1) * 8 + (g >> 2) * 2 + s), o = 4 * ((blk * 4 + (g >> 2)) * 2 + qb);
  constexpr int p = R_P + 16 * Xp + 4 * (s * 2 + qb);
  asm volatile("v_mfma_f32_16x16x32_bf16 a[%c0:%c1], a[%c2:%c3], v[%c4:%c5], a[%c0:%c1]" ::"i"(o), "i"(o + 3), "i"(r), "i"(r + 3), "i"(p), "i"(p + 3));
  if constexpr (g < 8) {
    if constexpr (blk < 3) vread<Xp, (blk + 1) * 8 + g>();
    else if constexpr (SMX) kread<Xp, g>();
  }
  if constexpr (SMX) {
    if constexpr (G == 1 || G == 2) asm volatile("v_add_f32_e32 v%c0, v%c0, v%c1" ::"i"(R_RS + G - 1), "i"(R_TS + G - 1));
    if constexpr (g == 7 && blk < 3) dma_m0<X, blk * 4 + 4>();
    if constexpr (g >= 8 && !(g & 1) && blk < 3) dma_at<blk * 4 + 4 + (g - 8) / 2>();
    if constexpr (G == 55) asm volatile("s_cmp_eq_u32 s56, s76");
    if constexpr (G == 3) asm volatile("s_lshr_b32 s77, s77, 1");
    if constexpr (G == 4) asm volatile("s_bitcmp1_b32 s77, 0");
    if constexpr (G == 5) asm volatile("s_cselect_b64 s[52:53], s[62:63], 0");
    if constexpr (G == 6) asm volatile(FL_ARM("s[52:53]"));
    if constexpr (G == 56) asm volatile(FL_POLL ::: "memory");
    sm_gap<X, G>(c);
  }
  GAP();
}
template <int X, int... Gs> DI void phaseB_(AC& c, iseq<Gs...>) { (gapB<X, true, Gs>(c), ...); }
template <int X> DI void phaseB(AC& c) { phaseB_<X>(c, make_iseq<64>{}); }
constexpr int sm_nex() { int n = 0; for (int t = 0; t < 64; ++t) n += SM.ex[t] >= 0; return n; }
template <int X> DI void phaseB0(unsigned mode, bool vh) {
  dma_m0<X, 4>();
  kreads<X ^ 1>(make_iseq<8>{});
  if (vh) { dma_at<4>(); dma_at<5>(); dma_at<6>(); dma_at<7>(); }
  dma_m0<X, 8>();
  asm volatile("s_nop 3");
  float d[2];
  if (mode & 2) {
    d[0] = vget<122>(); d[1] = vget<123>();
    dma_at<8>();
  } else {
    if constexpr (X == 0) if (__builtin_expect(mode & 4, 0)) set_mask0();
    d[0] = row_max<X, 0>(); GAP(); dma_at<8>();
    d[1] = row_max<X, 1>();
#pragma unroll
    for (int qb = 0; qb < 2; ++qb) if (!(d[qb] > -3.0e38f)) d[qb] = 0.f;
  }
  GAP();
  vset<R_M>(d[0]); vset<R_M + 1>(d[1]);
  dma_at<9>();
  const f32x2c nd[2] = {{-d[0], -d[0]}, {-d[1], -d[1]}};
  static_for([&](auto E) {
    constexpr int e = E;
    const f32x2c v = nd[(e >> 1) & 1];
    asm volatile("v_pk_add_f32 v[%c0:%c1], v[%c0:%c1], %4\n\tv_mov_b64 v[%c2:%c3], %4" ::"i"(R_S + 32 * X + 2 * e), "i"(R_S + 32 * X + 2 * e + 1), "i"(R_CI + 2 * e), "i"(R_CI + 2 * e + 1), "v"(v));
    if constexpr (e == 7 || e == 15) dma_at<10 + e / 8>();
  }, make_iseq<16>{});
  static_assert(SM.op0[64] >= 3 * ((sm_nex() - 15) / 2) && SM.op0[64] <= 24);
  static_for([&](auto E) {
    constexpr int e = E;
    if constexpr (e < sm_nex()) asm volatile("v_exp_f32 v%c0, v%c1" ::"i"(R_E + e), "i"(R_S + 32 * X + e));
    if constexpr (e == 2) dma_m0<X, 12>();
    if constexpr (e == 5 || e == 10 || e == 15) dma_at<11 + e / 5>();
  }, make_iseq<16>{});
  static_for([&](auto O) { sm_op<X, O>(); }, make_iseq<SM.op0[64]>{});
  dma_at<15>();
  static_for([&](auto E) { asm volatile("v_exp_f32 v%c0, v%c1" ::"i"(R_E + (int)E), "i"(R_S + 32 * X + 16 + (int)E)); }, make_iseq<(sm_nex() > 16 ? sm_nex() - 16 : 0)>{});
  asm volatile("buffer_store_dwordx2 v[%c0:%c1], v88, s[88:91], s76 offen sc1\n\ts_bitcmp1_b32 s77, 0\n\ts_cselect_b64 s[52:53], s[62:63], 0\n\t"
               FL_ARM("s[52:53]") "\n\t" FL_POLL ::"i"(R_M), "i"(R_M + 1) : "memory", "scc");
  GAP();
}
enum : int { T_Q0, T_K0, T_OQ, T_OG, T_FPREP, T_FQ, T_FG, T_FS, T_ZO, T_CNT, T_NPRE = 10, T_NST = 12, T_TAB = 14, T_SLAB = 16, T_PART, T_IT = 18, T_ITN = 23 };
#define MSK(R, K) "v_cmp_gt_i32 s[48:49], " #K ", v89\n\tv_cmp_gt_i32 s[50:51], " #K "+1, v89\n\tv_cmp_gt_i32 s[52:53], " #K "+2, v89\n\t" \
                  "v_cmp_gt_i32 s[54:55], " #K "+3, v89\n\tv_cndmask_b32_e64 v[" #R "], v[" #R "], v122, s[48:49]\n\t" \
                  "v_cndmask_b32_e64 v[" #R "+1], v[" #R "+1], v122, s[50:51]\n\tv_cndmask_b32_e64 v[" #R "+2], v[" #R "+2], v122, s[52:53]\n\t" \
                  "v_cndmask_b32_e64 v[" #R "+3], v[" #R "+3], v122, s[54:55]\n\t"
DI void ev_code() {
  asm volatile(
    "s_getpc_b64 s[82:83]\n\ts_add_u32 s82, s82, 12\n\ts_addc_u32 s83, s83, 0\n\ts_branch 9f\nev_entry:\n\t"
    "s_bitcmp1_b32 s60, s56\n\ts_cbranch_scc1 3f\n\ts_bitcmp1_b32 s74, s56\n\ts_cbranch_scc1 2f\n\t"
    "s_bitcmp1_b32 s58, s56\n\ts_cselect_b64 vcc, -1, 0\n\tv_cndmask_b32 v121, -2.0, v81, vcc\n\t"
    "buffer_store_dwordx2 v[82:83], v121, s[88:91], s70 offen sc1\n\tbuffer_store_dwordx2 v[82:83], v121, s[88:91], s71 offen sc1\n\t"
    "v_cmp_eq_u64 vcc, s[84:85], v[124:125]\n\ts_andn2_b64 vcc, s[62:63], vcc\n\ts_cbranch_scc1 5f\n"
    "0:\n\ts_add_u32 s98, s98, s59\n\ts_mov_b32 s59, s61\n\ts_mov_b32 s61, s72\n\ts_mov_b64 s[62:63], s[64:65]\n\ts_mov_b64 s[64:65], s[66:67]\n\t"
    "s_mov_b64 s[66:67], s[68:69]\n"
    "1:\n\ts_setpc_b64 s[86:87]\n"
    "5:\n\t" FL_CHECK("s[62:63]") "\n\ts_branch 0b\n"
    "6:\n\t" FL_CHECK("s[62:63]") "\n\ts_setpc_b64 s[86:87]\n"
    "2:\n\ts_mov_b32 s98, 0\n\t"
    "v_lshrrev_b32 v90, 7, v86\n\tv_and_b32 v91, 6, v90\n\tv_lshlrev_b32 v91, 4, v91\n\tv_and_b32 v92, 0x70, v86\n\tv_xor_b32 v91, v91, v92\n\t"
    "s_movk_i32 s48, 896\n\tv_mad_u32_u24 v94, v90, s48, v91\n\ts_mov_b64 s[92:93], s[80:81]\n\ts_mov_b32 s94, 229376\n\t"
    "v_add_u32 v90, 28672, v94\n\tv_add_u32 v91, 34816, v94\n\tv_add_u32 v92, 40960, v94\n\tv_add_u32 v93, 47104, v94\n\t"
    "v_add_u32 v95, 6144, v94\n\tv_add_u32 v126, 12288, v94\n\tv_add_u32 v127, 18432, v94\n\t"
    "v_mov_b32 v122, 0xff800000\n\t" MSK(228, 0) MSK(236, 16) MSK(244, 32) MSK(252, 48)
    "v_cmp_eq_u64 vcc, s[84:85], v[124:125]\n\ts_andn2_b64 vcc, s[62:63], vcc\n\ts_cbranch_scc1 6b\n\ts_setpc_b64 s[86:87]\n"
    "3:\n\tv_mov_b32 v122, 0xff800000\n\t" MSK(224, 0) MSK(232, 16) MSK(240, 32) MSK(248, 48)
    "v_readlane_b32 s54, v78, %0\n\ts_add_u32 m0, s78, 0x10000\n\t"
    "s_bfe_u32 s53, s73, 0x80010\n\ts_mulk_i32 s53, 3840\n\ts_mov_b64 s[48:49], s[80:81]\n\ts_add_u32 s50, s53, 3840\n\ts_min_u32 s50, s50, 229376\n\t"
    "s_mov_b32 s51, 0x20000\n\t"
    "v_lshlrev_b32 v121, 3, v84\n\tbuffer_load_dword v121, s[48:51], s53 offen lds\n\t"
    "s_bitcmp0_b32 s73, 3\n\ts_cbranch_scc1 4f\n\t"
    "v_lshlrev_b32 v121, 9, v84\n\tv_and_b32 v121, 0x38000, v121\n\tv_lshrrev_b32 v122, 9, v84\n\tv_lshl_add_u32 v121, v122, 12, v121\n\t"
    "v_lshrrev_b32 v122, 3, v84\n\tv_xor_b32 v122, v122, v84\n\tv_and_or_b32 v121, v122, 48, v121\n\t"
    "buffer_load_dwordx4 v121, s[88:91], s54 offen sc1 lds\n\ts_addk_i32 s54, 7168\n\t"
    "buffer_load_dwordx4 v121, s[88:91], s54 offen offset:1024 sc1 lds\n\ts_addk_i32 s54, 7168\n\t"
    "buffer_load_dwordx4 v121, s[88:91], s54 offen offset:2048 sc1 lds\n\ts_addk_i32 s54, 7168\n\t"
    "buffer_load_dwordx4 v121, s[88:91], s54 offen offset:3072 sc1 lds\n"
    "4:\n\ts_setpc_b64 s[86:87]\n9:"
    ::"i"((int)T_ZO) : "scc");
}
DI void ev_call() { asm volatile("s_swappc_b64 s[86:87], s[82:83]" ::: "vcc", "scc", "memory"); GAP(); }

#define ACLOB_ALL "a0","a1","a2","a3","a4","a5","a6","a7","a8","a9","a10","a11","a12","a13","a14","a15","a16","a17","a18","a19","a20","a21","a22","a23","a24","a25","a26","a27","a28","a29","a30","a31","a32","a33","a34","a35","a36","a37","a38","a39","a40","a41","a42","a43","a44","a45","a46","a47","a48","a49","a50","a51","a52","a53","a54","a55","a56","a57","a58","a59","a60","a61","a62","a63","a64","a65","a66","a67","a68","a69","a70","a71","a72","a73","a74","a75","a76","a77","a78","a79","a80","a81","a82","a83","a84","a85","a86","a87","a88","a89","a90","a91","a92","a93","a94","a95","a96","a97","a98","a99","a100","a101","a102","a103","a104","a105","a106","a107","a108","a109","a110","a111","a112","a113","a114","a115","a116","a117","a118","a119","a120","a121","a122","a123","a124","a125","a126","a127","a128","a129","a130","a131","a132","a133","a134","a135","a136","a137","a138","a139","a140","a141","a142","a143","a144","a145","a146","a147","a148","a149","a150","a151","a152","a153","a154","a155","a156","a157","a158","a159","a160","a161","a162","a163","a164","a165","a166","a167","a168","a169","a170","a171","a172","a173","a174","a175","a176","a177","a178","a179","a180","a181","a182","a183","a184","a185","a186","a187","a188","a189","a190","a191","a192","a193","a194","a195","a196","a197","a198","a199","a200","a201","a202","a203","a204","a205","a206","a207","a208","a209","a210","a211","a212","a213","a214","a215","a216","a217","a218","a219","a220","a221","a222","a223","a224","a225","a226","a227","a228","a229","a230","a231","a232","a233","a234","a235","a236","a237","a238","a239","a240","a241","a242","a243","a244","a245","a246","a247","a248","a249","a250","a251","a252","a253","a254","a255"

struct EP { float fa[2], fb[2]; };
template <int I, int E, int MODE> DI void epi_pair(const EP& p) {
  constexpr int qb = I & 1, r = 16 * (I >> 1) + 4 * qb + (E >> 1) * 8 + (E & 1) * 2, t = 96 + 2 * E, w = 128 + 4 * I + E;
  if constexpr (MODE == 2) asm volatile("v_accvgpr_read_b32 v106, a[%c0]" ::"i"(w));
  asm volatile("v_accvgpr_read_b32 v%c0, a[%c2]\n\tv_accvgpr_read_b32 v%c1, a[%c3]" ::"i"(t), "i"(t + 1), "i"(r), "i"(r + 1));
  if constexpr (MODE) asm volatile("v_mul_f32 v%c0, v%c0, %2\n\tv_mul_f32 v%c1, v%c1, %2" ::"i"(t), "i"(t + 1), "v"(p.fa[qb]));
  if constexpr (MODE == 2)
    asm volatile("v_lshlrev_b32 v104, 16, v106\n\tv_and_b32 v105, 0xffff0000, v106\n\tv_fmac_f32 v%c0, v104, %2\n\tv_fmac_f32 v%c1, v105, %2"
                 ::"i"(t), "i"(t + 1), "v"(p.fb[qb]));
  asm volatile("v_cvt_pk_bf16_f32 v%c0, v%c1, v%c2" ::"i"(w), "i"(t), "i"(t + 1));
}
template <int I> DI void epi_store() {
  asm volatile("buffer_store_dwordx4 v[%c0:%c1], v84, s[88:91], s101 offen offset:%2 sc1" ::"i"(128 + 4 * I), "i"(131 + 4 * I), "i"((I & 3) * 1024) : "memory");
  if constexpr ((I & 3) == 3) asm volatile("s_addk_i32 s101, 4096");
}
constexpr int R_TB = 232, R_VS = 236;
template <int HF, int R> DI void p_read() {
  constexpr int mb = R >> 4, r = 192 - 64 * HF + 2 * R;
  asm volatile("ds_read_b64_tr_b16 a[%c1:%c2], v%c0 offset:%c3" ::"i"(R_TB + 2 * HF + mb), "i"(r), "i"(r + 1), "i"((R & 15) * 2048));
}
template <int QB, int E> DI void p_cvt() {
  asm volatile("v_cvt_pk_bf16_f32 v%c0, v%c1, v%c2" ::"i"(208 + 4 * QB + E), "i"(192 + 8 * QB + 2 * E), "i"(193 + 8 * QB + 2 * E));
}
template <int QB, int PR> DI void p_store() {
  asm volatile("buffer_store_dwordx4 v[%c0:%c1], v%c2, s[88:91], s96 offen offset:%3 sc1" ::"i"(208 + 4 * QB), "i"(211 + 4 * QB), "i"(R_VS + QB),
               "i"(PR * 128) : "memory");
}
constexpr int p_dma(int HF, int G) { return G == 3 && !HF ? 0 : G == 11 ? 1 - HF : G == 15 ? 2 - HF : G == 19 ? 3 - HF : G == 27 && HF ? 3 : -1; }
template <int HF, bool LD, int G> DI void p_gap() {
  constexpr int ch = G >> 3, t = G & 7, a = 192 - 64 * HF + 32 * (ch & 1) + 4 * t, b = 128 + 4 * (2 * t + (ch >> 1)), d = 192 + 4 * ch;
  if constexpr (t == 0)
    asm volatile("v_mfma_f32_16x16x32_bf16 v[%c0:%c1], a[%c2:%c3], v[%c4:%c5], 0" ::"i"(d), "i"(d + 3), "i"(a), "i"(a + 3), "i"(b), "i"(b + 3));
  else
    asm volatile("v_mfma_f32_16x16x32_bf16 v[%c0:%c1], a[%c2:%c3], v[%c4:%c5], v[%c0:%c1]" ::"i"(d), "i"(d + 3), "i"(a), "i"(a + 3), "i"(b), "i"(b + 3));
  p_read<HF ^ 1, G>();
  if constexpr (G == 0) asm volatile("v_xor_b32 v%c0, s100, v%c0" ::"i"(R_TB + 2 * HF + 1));
  if constexpr (G == 17) asm volatile("v_xor_b32 v%c0, s100, v%c0" ::"i"(R_TB + 2 * (HF ^ 1)));
  if constexpr (G >= 4 && G <= 8 && G != 7) p_cvt<1, (G < 7 ? G - 4 : 3)>();
  if constexpr (G == 24 || (G == 7 && HF == 0)) asm volatile("s_waitcnt lgkmcnt(8)");
  if constexpr (G == 7 && HF == 1) asm volatile("s_waitcnt vmcnt(%0) lgkmcnt(8)\n\ts_barrier" ::"i"(LD ? 13 : 5) : "memory");
  if constexpr (G == 10) p_store<1, HF ^ 1>();
  if constexpr (G >= 20 && G < 24) p_cvt<0, G - 20>();
  if constexpr (G == 25) p_store<0, HF>();
  if constexpr (LD && p_dma(HF, G) >= 0) dma_at<(HF ? 8 : 12) + p_dma(HF, G)>();
  if constexpr (LD && HF == 0 && G == 2) asm volatile("s_bitset0_b32 m0, 12");
  if constexpr (HF == 0 && G == 13) asm volatile("s_add_u32 s96, s96, 0x8000");
  if constexpr (HF == 0 && G == 26) asm volatile("s_add_u32 s98, s98, 128");
  if constexpr (LD && HF == 0 && G == 27) asm volatile("s_cmp_lt_u32 s98, 0x380");
  if constexpr (HF == 1 && G == 1) asm volatile("s_xor_b32 s100, s100, 0x10000");
  if constexpr (LD && HF == 1 && G == 2) asm volatile("s_xor_b32 s97, s97, s100");
  if constexpr (LD && HF == 1 && G == 9) asm volatile("s_or_b32 m0, s97, 0x1000");
  if constexpr (!LD && HF == 1 && G == 30) asm volatile("s_cmp_lt_u32 s98, 0x500");
  GAP();
}
template <int HF, bool LD, int... Gs> DI void p_half(iseq<Gs...>) { (p_gap<HF, LD, Gs>(), ...); }
template <int G> DI void gapF(AC& c, const EP& p) {
  if constexpr (G < 64) gapB<0, false, G>(c);
  if constexpr (G >= 10) epi_pair<(G - 10) / 4, (G - 10) & 3, 0>(p);
  if constexpr (G >= 14 && ((G - 14) & 3) == 0) epi_store<(G - 14) / 4>();
  if constexpr (G == 73) epi_store<15>();
  GAP();
}
template <int... Gs> DI void phaseF(AC& c, const EP& p, iseq<Gs...>) { (gapF<Gs>(c, p), ...); }
template <int G> DI void gapAL(AC& c) {
  constexpr int kb = G >> 3, s = G & 7, r = 192 + 4 * (G & 15), q = 160 + 4 * s, d = R_S + 36 + 8 * kb, ci = s == 0 ? R_CI + 4 + 8 * kb : d;
  if constexpr (G == 0) asm volatile("s_add_u32 s96, s97, 0x1000000\n\ts_mov_b32 m0, s79\n\ts_waitcnt vmcnt(9) lgkmcnt(0)\n\ts_barrier" ::: "memory");
  if constexpr (G >= 8 && (G & 3) == 0) asm volatile("s_waitcnt lgkmcnt(4)");
  if constexpr (G == 29) asm volatile("s_barrier" ::: "memory");
  asm volatile("v_mfma_f32_16x16x32_bf16 v[%c0:%c1], a[%c2:%c3], a[%c4:%c5], v[%c6:%c7]" ::"i"(d), "i"(d + 3), "i"(r), "i"(r + 3), "i"(q), "i"(q + 3),
               "i"(ci), "i"(ci + 3));
  if constexpr (G < 24) kread<1, G + 8>();
  else vread<0, G - 24>();
  if constexpr (G % 3 == 1 && G < 24) dma_at<G / 3>();
  if constexpr (G == 11) asm volatile("s_bitset1_b32 m0, 12");
  if constexpr (G == 28) asm volatile("buffer_load_dwordx4 v[122:125], v88, s[88:91], s76 offen sc1" ::: "memory");
  if constexpr (G == 30) asm volatile("s_xor_b32 m0, s79, 0x11000\n\ts_movk_i32 s98, 128");
  sm_gap<0, 64 + 2 * G>(c); sm_gap<0, 65 + 2 * G>(c);
  GAP();
}
template <int... Gs> DI void phaseAL(AC& c, iseq<Gs...>) { (gapAL<Gs>(c), ...); }
struct BLS { int ex[65], op[65], vm[65], su[65]; };
constexpr BLS bl_sched(bool LD) {
  BLS z{};
  z.op[0] = 1;
  for (int t = 0; t < 64; ++t) {
    const int g = t & 15, o = z.op[t];
    const bool x = t == 6 || (t < 48 && g > 8 && g != 12 && g != 15);
    const bool v = LD ? (t & 3) == 0 : (t & 11) == 8;
    const bool p = t >= 15 && o < 24 && (t & 3) != 0 && t != 47 && !x && z.ex[t - 1] > 2 * (o / 3) + (o % 3 != 0);
    z.ex[t + 1] = z.ex[t] + x; z.vm[t + 1] = z.vm[t] + v; z.op[t + 1] = o + p;
    z.su[t + 1] = z.su[t] + (t >= 1 && z.su[t] < (LD ? 16 : 10) && g != 15 && !v && !x && (LD || t > 55 || (t > 3 && !p)));
  }
  return z;
}
template <bool LD> constexpr BLS BL = bl_sched(LD);
static_assert(BL<true>.ex[64] == 16 && BL<true>.op[61] == 24 && BL<true>.vm[64] == 16 && BL<false>.vm[64] == 8);
static_assert(BL<true>.su[56] == 16 && BL<false>.su[62] == 10);
template <bool LD, int G> DI void gapBL(AC& c) {
  if constexpr (G == 48) asm volatile("s_waitcnt vmcnt(%0) lgkmcnt(0)\n\ts_barrier" ::"i"(BL<LD>.vm[48]) : "memory");
  gapB<1, false, G>(c);
  if constexpr (G >= 48 && G < 56) vread<1, G - 48>();
  if constexpr (BL<LD>.vm[G + 1] > BL<LD>.vm[G]) {
    constexpr int k = BL<LD>.vm[G] + (LD ? 0 : 16), I = 2 * (k & 7) + ((k >> 3) & 1);
    if constexpr (k < 16) {
      asm volatile("buffer_load_dwordx4 a[%c0:%c1], v84, s[88:91], s75 offen offset:%2 sc1" ::"i"(128 + 4 * I), "i"(131 + 4 * I), "i"((I & 3) * 1024) : "memory");
      if constexpr (k & 1) asm volatile("s_addk_i32 s75, %0" ::"i"(k == 7 ? -12288 : 4096));
    } else dma_at<k - 8>();
    if constexpr (k == 19) asm volatile("s_bitset0_b32 m0, 12");
  }
  if constexpr (G == 1 || G == 2) asm volatile("v_add_f32_e32 v%c0, v%c0, v%c1" ::"i"(R_RS + G - 1), "i"(R_TS + G - 1));
  if constexpr (G == 3) asm volatile("v_mov_b32 v%c0, 0" ::"i"(R_TS));
  if constexpr (BL<LD>.ex[G + 1] > BL<LD>.ex[G]) {
    constexpr int j = BL<LD>.ex[G];
    asm volatile("v_exp_f32 v%c0, v%c1" ::"i"(R_E + j), "i"(R_S + 36 + 8 * (j >> 2) + (j & 3)));
  }
  if constexpr (BL<LD>.op[G + 1] > BL<LD>.op[G]) {
    constexpr int o = BL<LD>.op[G], k = o / 3, j = 2 * k + o % 3;
    if constexpr (o % 3 == 2) asm volatile("v_cvt_pk_bf16_f32 v%c0, v%c1, v%c2" ::"i"(p_reg(1, (k >> 1) * 4 + 2 + (k & 1))), "i"(R_E + 2 * k), "i"(R_E + 2 * k + 1));
    else asm volatile("v_add_f32_e32 v%c0, v%c1, v%c2" ::"i"(R_TS + 1), "i"(j == 1 ? R_E : R_TS + 1), "i"(R_E + j));
  }
  if constexpr (G == 61) asm volatile("v_cmp_nle_f32_e64 s[54:55], v%c0, v%c1" ::"i"(R_TS + 1), "i"(R_THR));
  if constexpr (BL<LD>.su[G + 1] > BL<LD>.su[G]) {
    constexpr int k = BL<LD>.su[G];
    if constexpr (k < 4) asm volatile("v_xor_b32 v%c0, %1, %2" ::"i"(R_TB + k), "i"(32 * k), "v"(c.tb));
    else if constexpr (k < 6) asm volatile("v_add_u32 v%c0, %1, %2" ::"i"(R_VS + k - 4), "i"(4096 * (k - 4)), "v"(c.vs));
    else if constexpr (k == 6) tload<96, T_SLAB>();
    else if constexpr (k == 7) tload<101, T_CNT>();
    else if constexpr (k == 8) asm volatile("v_mov_b64 v[240:241], 1");
    else if constexpr (k == 9) asm volatile("s_mov_b32 s97, s79\n\ts_mov_b32 s100, 0x8000");
    else if constexpr (k < 14) asm volatile("v_mov_b32 v%c0, %1" ::"i"(k < 12 ? 214 + k : 218 + k), "v"(k & 1 ? c.o1 : c.o0));
    else asm volatile("v_mov_b64 v[%c0:%c1], 0" ::"i"(198 + 2 * k), "i"(199 + 2 * k));
  }
  GAP();
}
template <bool LD, int... Gs> DI void phaseBL(AC& c, iseq<Gs...>) { (gapBL<LD, Gs>(c), ...); }
template <int MODE, int G> DI void gapFL(const EP& p) {
  constexpr int x = G - (MODE == 3 ? 4 : 0), J = MODE == 1 ? (G < 24 && G % 3 == 1 ? G / 3 : -1) : G < 27 && G % 5 != 0 && G % 5 != 3 ? G / 5 * 3 + (G % 5) / 2 : -1;
  if constexpr (G == 0 && MODE == 1) asm volatile("s_or_b32 m0, s78, 0x1000\n\ts_movk_i32 s98, 256\n\ts_waitcnt lgkmcnt(0)");
  if constexpr (G == 0 && MODE > 1) asm volatile("s_waitcnt vmcnt(8) lgkmcnt(0)" ::: "memory");
  if constexpr (G == 16 && MODE > 1) asm volatile("s_waitcnt vmcnt(9)" ::: "memory");
  if constexpr (G >= 8 && G < 32 && (G & 3) == 0) asm volatile("s_waitcnt lgkmcnt(%0)" ::"i"(G < 28 ? 4 : 0));
  if constexpr (G < 32) {
    constexpr int o = 8 * (G >> 1) + 4, r = 192 + 4 * (G & 15), q = R_P + 20 + 8 * (G & 1);
    asm volatile("v_mfma_f32_16x16x32_bf16 a[%c0:%c1], a[%c2:%c3], v[%c4:%c5], a[%c0:%c1]" ::"i"(o), "i"(o + 3), "i"(r), "i"(r + 3), "i"(q), "i"(q + 3));
  }
  if constexpr (G < 24) vread<1, G + 8>();
  if constexpr (G == 28) asm volatile("s_waitcnt vmcnt(8)\n\ts_barrier\n\ts_or_b32 m0, s79, 0x1000\n\ts_movk_i32 s98, 384" ::: "memory");
  if constexpr (G == 29 || G == 31) dma_at<8 + (G - 29) / 2>();
  if constexpr (G == 32 || G == 40) {
    static_for([&](auto I) { p_read<0, 2 * (G - 32) + I>(); }, make_iseq<16>{});
    dma_at<10 + (G - 32) / 8>();
  }
  if constexpr (J >= 0) dma_at<8 + (J & 7)>();
  if constexpr (x >= 0) epi_pair<2 * ((x >> 2) & 7) + (x >> 5), x & 3, MODE>(p);
  if constexpr (MODE == 3 && G < 32) {
    constexpr int db = G & 15, qb = G >> 4, o = 4 * (db * 2 + qb), a = 224 + 4 * (db & 1), b = 128 + 4 * ((db >> 1) * 2 + qb);
    asm volatile("v_mfma_f32_16x16x32_bf16 a[%c0:%c1], v[%c2:%c3], a[%c4:%c5], a[%c0:%c1]" ::"i"(o), "i"(o + 3), "i"(a), "i"(a + 3), "i"(b), "i"(b + 3));
  }
  if constexpr (J == 3 || J == 11) asm volatile("s_bitset0_b32 m0, 12");
  if constexpr (J == 7 && MODE > 1) asm volatile("s_or_b32 m0, s78, 0x1000\n\ts_movk_i32 s98, 256");
  if constexpr (x == 63) asm volatile("s_waitcnt vmcnt(4) lgkmcnt(0)" ::: "memory");
  GAP();
}
template <int MODE, int... Gs> DI void phaseFL_(const EP& p, iseq<Gs...>) { (gapFL<MODE, Gs>(p), ...); }
template <int MODE> DI void phaseFL(const EP& p) { GAP(); phaseFL_<MODE>(p, make_iseq<MODE == 3 ? 68 : 64>{}); }
template <int U, int K> DI void r_load(unsigned vo, unsigned so) {
  asm volatile("buffer_load_dwordx4 a[%c0:%c1], %2, s[88:91], %3 offen offset:%4 sc1" ::"i"(64 * U + 4 * K), "i"(64 * U + 4 * K + 3), "v"(vo),
               "s"(so + (K >> 1) * 0x40000 + (U >> 1) * 4096), "i"((K & 1) * 512 + (U & 1) * 2048) : "memory");
}
template <int U, int K> DI void r_sum() {
  constexpr int d = 100 + 4 * (K & 1), b = 64 * U + 4 * K;
  if constexpr (K < 2) asm volatile("v_mfma_f32_16x16x32_bf16 v[%c0:%c1], v[96:99], a[%c2:%c3], 0" ::"i"(d), "i"(d + 3), "i"(b), "i"(b + 3));
  else asm volatile("v_mfma_f32_16x16x32_bf16 v[%c0:%c1], v[96:99], a[%c2:%c3], v[%c0:%c1]" ::"i"(d), "i"(d + 3), "i"(b), "i"(b + 3));
}
template <int U> DI void r_fin(unsigned vo, __amdgpu_buffer_rsrc_t srd, unsigned so) {
  asm volatile("" : "+s"(srd), "+s"(so));
  static_for([&](auto K) { r_sum<U, K>(); }, make_iseq<16>{});
  asm volatile("s_nop 7");
  static_for([&](auto E) { asm volatile("v_cvt_pk_bf16_f32 v%c0, v%c1, v%c2" ::"i"(116 + 4 * U + E), "i"(100 + 2 * E), "i"(101 + 2 * E)); }, make_iseq<4>{});
  asm volatile("buffer_store_dwordx4 v[%c3:%c4], %0, %1, %2 offen sc0 sc1" ::"v"(vo), "s"(srd), "s"(so), "i"(116 + 4 * U), "i"(119 + 4 * U) : "memory");
}

#define CMAX 12
#define BAL 0
#define NYE (BAL ? 16 : 15)
#define NYO (BAL ? 14 : 15)
#define NY0 15
struct TRow { unsigned v[64]; };
constexpr u64 kvmask(int m0, int m1) { u64 n = 0; for (int m = m0; m <= m1; ++m) n |= 0x101ull << m; return n; }
constexpr u64 k_order(bool isY, int rr) {
  const int cls = (rr + (rr >> 2)) & 3;
  return !isY ? (cls == 0 ? 0xfedcb9875a463210ull : cls == 1 ? 0xefadc6982b574310ull : cls == 2 ? 0xfebad7693c285410ull : 0xefcba8764d392510ull)
              : (cls == 0 ? 0xfedcb987543a6210ull : cls == 1 ? 0xefadc698254b7310ull : cls == 2 ? 0xfebad769325c8410ull : 0xefcba876432d9510ull);
}
constexpr TRow t_row(int b, int wid) {
  TRow t{};
  const int j = b & 7, r = b >> 3, rr = r & 15, hsel = rr & 1, pj = rr >> 1, kv = pj & 1, h = j + 8 * hsel, u = hsel * 8 + j;
  const bool isY = r >= 16, noA = pj == 7, slow = BAL && (j & 1);
  const int pa = slow && (pj == 0 || pj == 7) ? 7 - pj : pj;
  const bool solo = pa == 7;
  const int jF = isY || !BAL ? j : slow && pa == 0 ? j - 1 : !slow && pa == 6 ? j + 1 : j, hF = jF + 8 * hsel;
  const int qbF = isY ? (solo ? (slow ? 6 : 7) : 15 - pa) : solo ? 8 : !BAL ? pa : slow ? (pa ? pa - 1 : 0) : pa + 1;
  const int mt = (rr >> 2) + (isY ? 4 : 0), g = j * 128, gF = jF * 128, NY = !(j & 1) ? NYE : pa ? NYO : NY0;
  const int nx0 = solo ? 0 : 32 - 2 * pa - NY, cx = nx0 < CMAX ? nx0 : CMAX, ex = nx0 - cx, qb0 = solo ? qbF : 15 - pa;
  const unsigned oQ = (unsigned)h << 20, oK = oQ + O_K, oP = O_P + (h * 8 + pa) * PSTR, oQF = (unsigned)hF << 20;
  if (wid == 4) {
    for (int l = 0; l < 64; ++l)
      t.v[l] = fo(l < 16 ? g + hsel * 16 + l : l == 16 ? g + 32 + hsel * 8 + (qb0 >> 1) : l == 17 ? gF + 32 + hsel * 8 + (qbF >> 1) :
                  l < 26 ? gF + hsel * 16 + ((l - 18) >> 2) * 8 + ((l - 18) & 3) : l == 26 ? g + 48 + hsel * 8 + pa : l < 32 ? g + 64 + hsel * 8 + pa :
                  l < 48 ? 2304 + pj * 16 + l - 32 : 2048 + pj * 16 + l - 48);
    return t;
  }
  auto put64 = [&](int i, u64 x) { t.v[i] = (unsigned)x; t.v[i + 1] = (unsigned)(x >> 32); };
  t.v[T_Q0] = oQ + (qb0 * 128 + wid * 16) * 512;
  t.v[T_K0] = oK + (isY ? cx : 0) * 32768;
  t.v[T_OQ] = oQ + pj * 131072 + (wid >> 1) * 65536 + (wid & 1) * 4096;
  t.v[T_OG] = oK + kv * TSB + mt * 131072 + (wid >> 1) * 65536 + (wid & 1) * (kv ? 8192 : 4096);
  t.v[T_FPREP] = fo(2048 + pj * 16 + u);
  t.v[T_FQ] = fo(g + 32 + hsel * 8 + pj);
  t.v[T_FG] = fo(g + hsel * 16 + kv * 8 + mt);
  t.v[T_FS] = fo(1024 + qbF * 16 + jF * 2 + hsel);
  t.v[T_CNT] = fo(2560 + qbF * 16);
  t.v[T_ZO] = O_Z + pj * 64 * SEQ * 2 + (u * 4 + wid) * 64;
  const u64 own = 0x1ull << (kv * 8 + mt), conv = noA ? 0 : 0xffffull << 32;
  put64(T_NPRE, 0x10000ull | (isY ? (1ull << (cx >> 2)) | (1ull << ((cx + 1) >> 2)) : noA ? 0 : 0xffffull << 48));
  put64(T_NST, isY ? kvmask(cx >> 2, 3) | (solo ? 0 : 1ull << 27) : kvmask(0, 0));
  put64(T_TAB, k_order(isY, rr));
  t.v[T_SLAB] = O_S + qbF * 0x200000 + jF * 0x40000 + wid * 8192 + hsel * 1024;
  t.v[T_PART] = oP;
  const unsigned junk = fo(g + 96 + r), fconv = noA ? junk : fo(2304 + pj * 16 + u);
  for (int it = isY || solo; it < 2; ++it) {
    unsigned* p = t.v + T_IT + it * T_ITN;
    const int n = !it ? nx0 : isY && !solo ? NY : 2 * qbF + 2;
    u64 q[4] = {0, 0, 0, 0};
    unsigned jmp[3] = {0, 0, 0}, chk = 0;
    p[14] = junk;
    if (isY) {
      p[0] = 2; p[1] = 2; p[13] = t.v[T_FG];
      if (!solo) {
        chk = 1u << (14 - cx); p[0] |= chk; q[1] = kvmask(4, (31 - 2 * pa) >> 2) & ~own; jmp[1] = ex * 32768;
        chk |= 1u << (n - 2); q[2] = 1ull << 26;
      }
    } else if (it == 0) {
      chk = p[0] = 2u | (ex ? 1u << (cx - 2) : 0) | 1u << (n - 2);
      chk |= noA ? 0 : 1u << n;
      q[0] = kvmask(0, (cx - 1) >> 2);
      if (ex) { q[1] = kvmask(4, 4 + ((ex - 1) >> 2)); jmp[1] = (16 - cx) * 32768; }
      jmp[ex ? 2 : 1] = oQF - oQ - (ex ? 16 + ex : cx) * 32768;
      q[ex ? 2 : 1] = 0x20000ull | ((0x22ull << ((2 * qbF + 1) >> 2)) - 0x11ull) << 18;
      q[ex ? 3 : 2] = conv;
      p[1] = 2; p[13] = fconv; p[14] = fo(g + 64 + hsel * 8 + pa);
    } else if (solo) {
      chk = p[0] = 0x402; p[1] = 2; p[13] = fconv;
      q[0] = kvmask(0, 2); q[1] = kvmask(3, 4) | conv;
    } else {
      p[0] = p[1] = 1; p[13] = fo(g + 48 + hsel * 8 + pa);
      q[0] = conv;
    }
    p[2] = jmp[0]; p[4] = jmp[1]; p[15] = jmp[2];
    p[3] = !it ? 0 : n > 2 ? 1u << (n - 3) : 1u << 30;
    for (int k = 0; k < 4; ++k) { p[5 + 2 * k] = (unsigned)q[k]; p[6 + 2 * k] = (unsigned)(q[k] >> 32); }
    const int rk = (r >> 1) - (BAL && (r >> 1) > (slow ? 7 : 6));
    p[16] = 32 | (isY && !solo ? 2 : 0) | (n == 2 ? 4 : 0) | (isY || noA ? 0 : 8) | (n & 1) << 4 | (n - 1 - it) << 8 | (rk * 4 + wid) << 16 | hF << 24 | qbF << 28;
    p[17] = !it ? 0 : n > 2 ? 1u << (n - 2) : 1u << 31;
    p[18] = it ? oP + wid * 16384 : oQF + (qbF * 128 + wid * 16) * 512;
    p[19] = oP + (it ? 1032 : 1024);
    p[20] = chk >> 1;
    p[21] = (it ? oQF + O_K : oK) + (isY ? cx : 0) * 32768;
    p[22] = ((p[0] | p[3] | p[17]) & 0x3fffffffu) << 1;
  }
  return t;
}
struct TBlk { TRow row[5]; };
template <int B> constexpr TBlk t_blk = {{t_row(B, 0), t_row(B, 1), t_row(B, 2), t_row(B, 3), t_row(B, 4)}};
struct Tab { TBlk blk[256]; };
template <int... I> constexpr Tab t_make(iseq<I...>) { return Tab{{t_blk<I>...}}; }
__device__ const Tab g_tab = t_make(make_iseq<256>{});

template <int R> DI const bf16* sptr() { const bf16* p; asm volatile("s_mov_b64 %0, s[%c1:%c2]" : "=s"(p) : "i"(R), "i"(R + 1)); return p; }
template <class T, int I> DI T* arg_late() {
  unsigned lo, hi;
  asm volatile("v_readlane_b32 %0, v77, %2\n\tv_readlane_b32 %1, v77, %3\n\ts_nop 4" : "=s"(lo), "=s"(hi) : "i"(2 * I), "i"(2 * I + 1));
  return (T*)(((u64)hi << 32) | lo);
}
__global__ __launch_bounds__(256, 1) __attribute__((amdgpu_num_vgpr(77), amdgpu_num_sgpr(56))) void fused_kernel(
    const bf16* ckv_, const bf16* wuk_, const bf16* wuv_, const bf16* x_, const bf16* wq_, bf16* ws_, const bf16* ak_, const bf16* av_,
    const bf16* ash_, const bf16* assmk_, const bf16* assmD_, const bf16* wo_, const bf16* aq_, bf16* out_) {
  extern __shared__ __attribute__((aligned(16))) char lds[];
  const void* kap = (const void*)__builtin_amdgcn_kernarg_segment_ptr();
  asm volatile("at_code_begin:\n\ts_load_dwordx16 s[80:95], %0, 0\n\ts_load_dwordx2 s[96:97], %0, 0x40" ::"s"(kap) : ACLOB_ALL, "v255", "s101");
  asm volatile("s_getpc_b64 s[48:49]\nct_base:\n\ts_mov_b32 s50, pf_code_end-ct_base\n\ts_mov_b32 s51, 0x20000\n\ts_lshr_b32 s52, %1, 3\n\t"
               "v_and_b32 v116, 63, %0\n\tv_lshrrev_b32 v117, 6, %0\n\tv_lshl_add_u32 v116, v116, 7, v117\n\ts_lshl_b32 s52, s52, 9\n\t"
               "v_lshlrev_b32 v116, 7, v116\n\tbuffer_load_dword v117, v116, s[48:51], s52 offen" ::"v"(threadIdx.x), "s"(blockIdx.x) : "scc", "memory");
  GAP();
  const int tid = threadIdx.x, lane = tid & 63;
  const int wid = __builtin_amdgcn_readfirstlane(tid >> 6);
  const int b = blockIdx.x, j = b & 7, r = b >> 3, rr = r & 15, hsel = rr & 1, pj = rr >> 1, kv = pj & 1;
  const bool isY = r >= 16, doP = isY && pj != 7, doA = !isY && pj != 7;
  const int h = j + 8 * hsel, mt = (rr >> 2) + (isY ? 4 : 0), pu = (hsel * 8 + j) * 4 + wid;
  constexpr unsigned m0d = 131072 + 24576;
  const unsigned lA = isY ? 65536 : 0, lW = lA ^ 65536, po = isY ? 0 : 32768;
  unsigned mA = lA + wid * 1024 + po, mW = lW + wid * 1024 + po, ofA = isY ? pj * 256 * DA * 2 : mt * 256 * DC * 2, ofW = h * 512;
  const unsigned lda = isY ? DA : DC;
#define ROT 1
  unsigned g0 = (!ROT ? 0 : isY ? pj + 2 * hsel : rr + (rr >> 2)) & 3;
  {
    asm volatile("global_load_dword v78, %0, %2\n\tglobal_load_dword v79, %1, %2 offset:1024\n\tglobal_load_dword v77, %3, %4"
                 ::"v"(tid * 4), "v"(lane * 4), "s"(&g_tab.blk[b]), "v"((lane < 27 ? lane : 27) * 4), "s"(kap) : "memory");
    const unsigned ra = wid * 8 + (lane >> 3), sa = ((lane & 7) ^ (((wid & 1) * 4 + (lane >> 4)) & 7)) * 16;
    const unsigned vbA = ra * (lda * 2) + sa, vbB = (4 * wid + (lane >> 4)) * LDW * 2 + 16 * ((lane & 15) ^ (((lane >> 4) << 2) | wid));
    vsetu<92>(vbA); vsetu<93>(vbB); vsetu<94>(ra * (DC * 2) + sa);
    sset<64>(64 * lda); sset<67>(256 * lda); sset<68>(320 * lda);
    asm volatile("s_mov_b32 s71, 0x20000\n\ts_movk_i32 s72, 0x100\n\ts_mov_b32 s73, 0x20100");
    sset<58>(256 * lda * 2); sset<62>((lda - 1) * LDW * 2 + 512);
    asm volatile("s_mov_b32 s59, 0x20000\n\ts_mov_b32 s63, 0x20000");
    g_pro<1>(g0, mA, mW, lda);
    asm volatile("" : "+s"(ofA), "+s"(ofW), "+s"(mA), "+s"(mW), "+s"(g0));
    GAP();
  }
  asm volatile("s_bitcmp1_b32 %0, 4\n\ts_waitcnt lgkmcnt(0)\n\ts_cselect_b64 s[60:61], s[84:85], s[82:83]\n\ts_bitcmp1_b32 %0, 7\n\t"
               "s_cselect_b64 s[60:61], s[88:89], s[60:61]\n\ts_cselect_b64 s[56:57], s[86:87], s[80:81]\n\t"
               "s_add_u32 s56, s56, %1\n\ts_addc_u32 s57, s57, 0\n\ts_add_u32 s60, s60, %2\n\ts_addc_u32 s61, s61, 0\n\t"
               "s_mov_b64 s[88:89], s[90:91]\n\ts_mov_b32 s90, %3\n\ts_mov_b32 s91, 0x20000" ::"s"(b), "s"(ofA), "s"(ofW), "i"(WSB) : "scc");
  KST(0, 0);
  AC c;
  {
    GM g;
    PR pr;
    const int ptb = hsel * 8 + j;
    if (doP) prep_load(pr, tid, pj, ptb, sptr<92>(), sptr<94>(), sptr<96>());
    g_pro<2>(g0, mA, mW, lda);
    asm volatile("buffer_load_dwordx2 v[120:121], off, s[88:91], %0 sc1" ::"s"(fo(6144)) : "memory");
    if (doP) {
      asm volatile("s_waitcnt vmcnt(9)" ::: "memory");
#pragma unroll
      for (int q = 0; q < 7; ++q) asm volatile("" : "+v"(pr.kk[q]));
#pragma unroll
      for (int q = 0; q < 4; ++q) asm volatile("" : "+v"(pr.vv[q]));
      static_for([&](auto Ic) __attribute__((always_inline)) {
        constexpr int I = Ic;
        if constexpr (I < 3) g_pro(g0 + 1 + I, mA, mW, lda);
        GAP();
        prep_math<I>(pr, tid, lds + 131072);
        GAP();
      }, make_iseq<4>{});
      asm volatile("s_waitcnt lgkmcnt(0)\n\ts_barrier" ::: "memory");
      prep_store(tid, pj, ptb, (bf16*)sptr<88>() + O_X / 2, lds + 131072);
    }
    GAP();
    {
      const int fr = lane & 15, fq = lane >> 4, q = (lane & 15) >> 2, p = lane & 3;
#pragma unroll
      for (int k2 = 0; k2 < 2; ++k2)
        g.aA[k2] = lA + (wid >> 1) * 16384 + (fr >> 3) * 1024 + (fr & 7) * 128 + (((4 * k2 + fq) ^ (fr >> 1)) * 16);
#pragma unroll
      for (int hf = 0; hf < 2; ++hf)
#pragma unroll
        for (int nf = 0; nf < 8; ++nf)
          g.aB[hf][nf] = lW + (wid & 1) * 8192 + off_b(8 * fq + 4 * hf + q, nf * 2 + (p >> 1)) + 8 * (p & 1);
      asm volatile("" : "+v"(g.aA[0]), "+v"(g.aA[1]));
#pragma unroll
      for (int nf = 0; nf < 8; ++nf) asm volatile("" : "+v"(g.aB[0][nf]), "+v"(g.aB[1][nf]));
      const unsigned one = fq != q ? 0u : (p & 1) ? 0x3f800000u : 0x3f80u;
      c.o0 = (p & 2) ? 0u : one; c.o1 = (p & 2) ? one : 0u;
      c.tb = (lane >> 2) * 128 + 16 * ((p >> 1) ^ ((lane >> 2) & 6)) + 8 * (p & 1);
      c.vs = (fr >> 3) * 2048 + (fq & 1) * 512 + (fq >> 1) * 256 + (fr & 7) * 16;
      asm volatile("" : "+v"(c.o0), "+v"(c.o1), "+v"(c.tb), "+v"(c.vs));
      vsetu<81>(tid == 0 ? 0u : 0x80000000u);
      vsetu<84>(lane * 16); vsetu<85>(lane * 16 + 65536); vsetu<86>(wid * 8192 + lane * 16); vsetu<87>(wid * 8192 + lane * 16 + 4096);
      vsetu<88>(65536 + wid * 256 + fr * 16); vsetu<89>(16 * wid + fr - 4 * fq);
      asm volatile("s_mov_b64 s[92:93], s[88:89]\n\ts_mov_b64 s[94:95], s[90:91]\n\ts_mov_b32 s74, 0x40000\n\ts_mov_b32 s75, 0x60000\n\t"
                   "s_mov_b32 s76, 0x40100\n\ts_mov_b32 s77, 0x60100");
      sset<65>(128 * lda); sset<66>(192 * lda); sset<69>(384 * lda); sset<70>(448 * lda);
      sset<78>(lA + wid * 1024); sset<79>(lW + wid * 1024);
    }
    if (doP) asm volatile("s_waitcnt vmcnt(4)" ::: "memory");
    else {
      g_pro(g0 + 1, mA, mW, lda); g_pro(g0 + 2, mA, mW, lda); g_pro(g0 + 3, mA, mW, lda);
      asm volatile("s_waitcnt vmcnt(0)" ::: "memory");
    }
    asm volatile("s_barrier" ::: "memory");
    if (isY) g_read0<0>(g, make_iseq<16>{});
    else g_read0<1>(g, make_iseq<16>{});
    GAP();
    {
      u64 z = (((u64)__float_as_uint(vget<121>()) << 32) | __float_as_uint(vget<120>())) + 0x9e3779b97f4a7c15ull;
      z = (z ^ (z >> 30)) * 0xbf58476d1ce4e5b9ull; z = (z ^ (z >> 27)) * 0x94d049bb133111ebull; z ^= z >> 31;
      vsetu<82>((unsigned)z); vsetu<83>((unsigned)(z >> 32));
      asm volatile("v_readlane_b32 s80, v78, %0\n\tv_readlane_b32 s81, v78, %1\n\tv_readfirstlane_b32 s84, v82\n\tv_readfirstlane_b32 s85, v83"
                   ::"i"((int)T_TAB), "i"(T_TAB + 1));
      if (isY) asm volatile("s_mov_b64 s[80:81], 0x6543210");
    }
    unsigned k2;
    asm volatile("s_bfe_u32 s82, s80, 0x40008\n\ts_lshr_b64 s[80:81], s[80:81], 12\n\ts_lshl_b32 %0, s82, 1\n\tv_add_u32 v90, 128, v92" : "=s"(k2) :: "scc");
    asm volatile("s_waitcnt lgkmcnt(0)\n\ts_barrier" ::: "memory");
    g_proW(k2, mW);
    GAP();
    if (isY) {
      g_tile<false, 0, 1>(g);
      g_loop<false, true>(g, doP ? 0x22u : 0x20u, [&](int t) __attribute__((always_inline)) {
        if (t == 1) fl_set<T_FPREP>();
        else if (t == 4) { tload<80, T_TAB>(); tload<81, T_TAB + 1>(); srd_set<60>((kv ? arg_late<const bf16, 2>() : arg_late<const bf16, 1>()) + h * 256, (DC - 1) * LDW * 2 + 512); }
        else {
          srd_set<56>(arg_late<const bf16, 0>() + (size_t)mt * 256 * DC, 256 * DC * 2);
          asm volatile("v_mov_b32 v92, v94");
          static_for([&](auto J) { asm volatile("s_mov_b32 s%c0, %1" ::"i"(64 + J), "i"((J + 1) * 64 * DC)); }, make_iseq<7>{});
        }
      });
      tload<101, T_OQ>(); sset<83>(__float_as_uint(CS));
      asm volatile("v_mov_b32 v112, v84\n\tv_add_u32 v113, 8192, v84");
      GAP();
      g_tile<false, 0, 2>(g);
      KST(0, 3);
    }
#define TT 3
    auto kvtile = [&](auto TRc) __attribute__((always_inline)) {
      constexpr bool TR = TRc;
      if (isY) g_tile<TR, 1, 4>(g);
      else g_tile<TR, 1, 1>(g);
      g_loop<TR, false>(g, 0x2000u | (isY || doA ? 2u : BAL && (j & 1) ? 0u : 1u << TT), [&](int t) __attribute__((always_inline)) {
        int l = threadIdx.x & 63; asm volatile("" : "+v"(l));
        if (t == 13) {
          asm volatile("s_mov_b32 s79, 0x28000");
          fl_poll<T_NPRE>();
          if ((tget<T_IT + T_ITN + 16>() & 0xf000000) == 0)
            asm volatile("v_readlane_b32 s48, v78, %0\n\tv_and_b32 v122, 0xffffff00, v82\n\tv_mov_b32 v123, v83\n\ts_nop 2\n\t"
                         "buffer_store_dwordx2 v[122:123], v81, s[88:91], s48 offen sc1" ::"i"((int)T_CNT) : "memory");
          if (!doA && !isY) {
            CODE_TOUCH("at_code_begin", "pf_code_end", hsel * 8 + wid * 2, 16u, 64, m0d);
            CODE_TOUCH("at_code_begin", "pf_code_end", hsel * 8 + wid * 2 + 1, 16u, 64, m0d);
          }
          if (doA) {
            const char* tp = arg_late<const char, 9>() + (size_t)(pj * 64 + pu) * (SEQ * 2) + (l & 31) * 128;
            asm volatile("s_mov_b32 m0, %1\n\ts_nop 0\n\tglobal_load_lds_dword %0, off" ::"v"(tp), "i"(m0d) : "memory");
          }
        } else if (isY) fl_set<T_FQ>();
        else if (doA) post_dma(pj, pu, arg_late<const bf16, 12>(), 131072 + wid * 8192);
        else {
          const char* tp = arg_late<const char, 0>() + 1024 * DC * 2 + (pu * 256 + l) * 128;
          asm volatile("s_mov_b32 m0, %0\n\ts_nop 0" ::"i"(m0d));
#pragma unroll
          for (int i = 0; i < (BAL ? 8 : 4); ++i) asm volatile("global_load_lds_dword %0, off" ::"v"(tp + (i & 3) * 8192 + (i >> 2) * 131072) : "memory");
        }
      });
      tload<101, T_OG>(); tload<96, T_Q0>(); tload<98, T_K0>();
      fl_check();
      tload<50, T_NST>(); tload<51, T_NST + 1>();
      asm volatile("v_mov_b32 v112, v84\n\tv_add_u32 v113, %0, v84\n\tv_mov_b32 v90, v87\n\tv_mov_b32 v91, v87\n\tv_mov_b32 v92, v87\n\tv_mov_b32 v93, v87\n\t"
                   "v_mov_b32 v94, v86\n\tv_mov_b32 v95, v86\n\tv_mov_b32 v126, v86\n\tv_mov_b32 v127, v86\n\ts_mov_b32 s99, 0x8000\n\t" FL_ARM("s[50:51]")
                   ::"i"(TR ? 4096 : 8192));
      if (doA) {
        const int d = pj * 64 + pu;
        const bf16* dp = arg_late<const bf16, 10>() + d;
        asm volatile("global_load_ushort v123, %0, off" ::"v"(dp) : "memory");
        srd_set<56>(arg_late<const bf16, 9>() + (size_t)d * SEQ, SEQ * 2);
        srd_set<60>(arg_late<const char, 5>() + O_X + d * SEQ * 2, SEQ * 2);
        sset<78>(65536 + wid * 15488);
        asm volatile("s_movk_i32 s64, 992\n\tv_subrev_u32 v122, 32, v84");
        GAP();
        g_tile<TR, 0, 3>(g);
      } else {
        sset<79>(isY ? wid * 8192 : 0x30000);
        if (tget<T_IT + T_ITN + 16>() & 16) asm volatile("s_add_u32 s98, s98, 0x8000\n\ts_mov_b32 s97, 0xffff8000" ::: "scc");
        else asm volatile("s_mov_b32 s97, 0x8000");
        GAP();
        g_tile<TR, 0, 5>(g);
      }
    };
    if (kv) kvtile(std::true_type{});
    else kvtile(std::false_type{});
  }
  KST(0, 4);
  asm volatile("s_waitcnt vmcnt(16)" ::: "memory");
  const unsigned md0 = tget<T_IT + 16>(), md1 = tget<T_IT + T_ITN + 16>();
  sset<79>(wid * 8192 + (md0 & 16) * 2048);
  if (doA) {
    const float Dd = bfl(__float_as_uint(vget<123>()));
    KST(0, 5);
    conv_wave<T_FG>(O_Z + (pj * 64 + pu) * SEQ * 2, Dd, lds + 65536 + wid * 15488);
    const char* tp = arg_late<const char, 11>() + (size_t)h * (HD * DA * 2) + ((pj * 4 + wid) * 64 + lane) * 128;
    asm volatile("s_mov_b32 m0, %1\n\ts_nop 0\n\tglobal_load_lds_dword %0, off\n\ts_waitcnt vmcnt(17)" ::"v"(tp), "i"(m0d) : "memory");
    KST(0, 6);
  } else if (!isY) {
    static_for([&](auto I) { d_store<16 + I>(); }, make_iseq<16>{});
    tload<98, T_K0>();
    asm volatile("s_waitcnt vmcnt(0)\n\ts_barrier" ::: "memory");
    fl_set<T_FG>();
    KST(0, 5);
    fl_check();
    CODE_TOUCH("att_begin", "pf_code_end", hsel * 4 + wid, 8u, 64, m0d);
    dma4k<0>(); dma4k<1>(); dma4k<2>(); dma4k<3>();
    asm volatile("s_waitcnt vmcnt(0)" ::: "memory");
    KST(0, 6);
  } else {
    tload<76, T_PART>();
    fl_check();
    asm volatile("buffer_load_dwordx2 v[122:123], v88, s[88:91], s76 offen offset:1024 sc1" ::: "memory");
  }
  sset<78>(65536 + wid * 8192); sset<79>(98304 + wid * 8192);
  { const bf16* wp = arg_late<const bf16, 11>() + ((md1 >> 24) & 15) * HD * DA; asm volatile("s_mov_b64 s[80:81], %0" ::"s"(wp)); }
  asm volatile("att_begin:");
  ev_code();
  asm volatile("s_waitcnt lgkmcnt(0)\n\ts_barrier" ::: "memory");
#pragma unroll 1
  for (int it = md0 ? 0 : 1; it < 2; ++it) {
    const unsigned mode = it ? md1 : md0;
    const bool odd = mode & 16;
    if (odd) kreads<1>(make_iseq<8>{});
    else kreads<0>(make_iseq<8>{});
    const unsigned oP = tget<T_PART>();
    if (it == 0) {
      static_for([&](auto I) { tload<57 + I, T_IT + I>(); }, make_iseq<21>{});
      tload<S_T, T_IT + 21>(); tload<S_EV, T_IT + 22>();
    } else {
      static_for([&](auto I) { tload<57 + I, T_IT + T_ITN + I>(); }, make_iseq<21>{});
      tload<S_T, T_IT + T_ITN + 21>(); tload<S_EV, T_IT + T_ITN + 22>();
    }
    asm volatile("s_add_u32 s98, s97, 0x8000\n\ts_mov_b32 s56, -1\n\tv_mov_b64 v[114:115], 0\n\tv_mov_b64 v[118:119], 0\n\t"
                 "v_mov_b32 v120, 0x45800000" ::: "scc");
    KST(0, 8 + 3 * (it && !isY));
    GAP();
#define STA(k)
#define STI()
#define LOOP_ENDS "s_bfe_u32 s76, s73, 0x80008"
    STA(0);
    if (odd) {
      if (isY) phaseA<1, 2>(c);
      else phaseA<1, 1>(c);
      STA(1);
      STA(2);
      phaseB0<1>(mode, isY);
      STA(3);
      asm volatile(LOOP_ENDS "\n\ts_branch att_mid" ::: "scc");
    } else {
      if (isY) phaseA<0, 2>(c);
      else phaseA<0, 1>(c);
      STA(1);
      if (__builtin_expect(mode & 4, 0)) { asm volatile("s_mov_b32 s56, 30"); ev_call(); asm volatile("s_mov_b32 s56, 31"); ev_call(); }
      STA(2);
      phaseB0<0>(mode, isY);
      if (__builtin_expect(mode & 4, 0)) { ev_call(); asm volatile("s_branch att_done"); }
      STA(3);
    }
    GAP();
    asm volatile(LOOP_ENDS "\natt_loop:" ::: "scc");
    GAP();
    phaseA<1, 0>(c);
    STI();
    if (__builtin_expect(c.rare != 0, 0)) sm_redo<1>();
    phaseB<1>(c);
    asm volatile("s_cbranch_scc1 att_done\natt_mid:");
    phaseA<0, 0>(c);
    STI();
    if (__builtin_expect(c.rare != 0, 0)) sm_redo<0>();
    phaseB<0>(c);
    asm volatile("s_cbranch_scc0 att_loop\natt_done:");
    GAP();
    STA(4);
    if (it == 0) phaseA<0, 3>(c);
    else {
      sset<76>(oP);
      GAP();
      phaseAL(c, make_iseq<32>{});
      if (__builtin_expect(redo(), 0)) sm_redo<1, false>();
      if (mode & 2) phaseBL<true>(c, make_iseq<64>{});
      else phaseBL<false>(c, make_iseq<64>{});
    }
    if (__builtin_expect(redo(), 0)) {
      if (it) static_for([&](auto I) { vset<R_S + 32 + (I >> 2) * 8 + (I & 3)>(-INFINITY); }, make_iseq<16>{});
      asm volatile("s_nop 7");
      sm_redo<0, false>();
    }
    STA(5);
    KST(0, 9 + 3 * (it && !isY));
    float l[2];
    asm volatile("v_add_f32 %0, v%c2, v%c3\n\tv_add_f32 %1, v%c4, v%c5" : "=v"(l[0]), "=v"(l[1]) : "i"(R_RS), "i"(R_TS), "i"(R_RS + 1), "i"(R_TS + 1));
    const float cm[2] = {vget<R_M>(), vget<R_M + 1>()};
    l[0] = row_reduce<false>(l[0]); l[1] = row_reduce<false>(l[1]);
    EP p;
    if (it == 0) {
      const f32x4 ml = {cm[0], l[0], cm[1], l[1]};
      asm volatile("buffer_store_dwordx4 %0, v88, s[88:91], %1 offen sc1\n\ts_mov_b32 s101, %2" ::"v"(ml), "s"(oP), "s"(oP + wid * 16384) : "memory");
      phaseF(c, p, make_iseq<74>{});
      asm volatile(FL_POLL "\n\ts_waitcnt vmcnt(17)" ::: "memory");
    } else {
      if (!(mode & 2)) {
        p.fa[0] = __builtin_amdgcn_rcpf(l[0]); p.fa[1] = __builtin_amdgcn_rcpf(l[1]);
        phaseFL<1>(p);
      } else {
        const f32x4 ml = {vget<122>(), vget<123>(), vget<124>(), vget<125>()};
        if (__builtin_expect(__builtin_amdgcn_ballot_w64(ml[0] != cm[0] || ml[2] != cm[1]) == 0, 1)) {
          p.fa[0] = __builtin_amdgcn_rcpf(l[0] + ml[1]); p.fa[1] = __builtin_amdgcn_rcpf(l[1] + ml[3]);
          phaseFL<3>(p);
        } else {
#pragma unroll
          for (int e = 0; e < 2; ++e) {
            const float mm = fmaxf(cm[e], ml[2 * e]);
            const float wa = __builtin_amdgcn_exp2f(cm[e] - mm), wb = __builtin_amdgcn_exp2f(ml[2 * e] - mm);
            const float inv = __builtin_amdgcn_rcpf(l[e] * wa + ml[2 * e + 1] * wb);
            p.fa[e] = wa * inv; p.fb[e] = wb * inv;
          }
          phaseFL<2>(p);
        }
      }
    }
    KST(0, 10 + 3 * (it && !isY));
  }
  asm volatile(TICKET "\n\tv_xor_b32 v%c0, s100, v%c0\np_loop:" ::"i"(R_TB) : "memory");
  GAP();
  p_half<0, true>(make_iseq<32>{});
  asm volatile("s_cbranch_scc0 p_mid");
  p_half<1, true>(make_iseq<32>{});
  asm volatile("s_branch p_loop\np_loop2:");
  p_half<0, false>(make_iseq<32>{});
  asm volatile("p_mid:");
  p_half<1, false>(make_iseq<32>{});
  asm volatile("s_cbranch_scc1 p_loop2\n\ts_nop 5");
  GAP();
  p_cvt<1, 0>(); p_cvt<1, 1>(); p_cvt<1, 2>(); p_cvt<1, 3>();
  p_store<1, 1>();
  GAP();
  KST(0, 27);
  {
    constexpr unsigned rdy = 98304;
    int lz = threadIdx.x & 63; asm volatile("" : "+v"(lz));
    const int fr = lz & 15, fq = lz >> 4, qbF = md1 >> 28;
    if (wid == 0) {
#pragma unroll 1
      for (int q = 0; q < 0x100000; ++q) {
        unsigned lo, hi, tl, th;
        asm volatile("v_readfirstlane_b32 %0, v240\n\tv_readfirstlane_b32 %1, v241\n\ts_mov_b32 %2, s84\n\ts_mov_b32 %3, s85" : "=s"(lo), "=s"(hi), "=s"(tl), "=s"(th));
        if (__builtin_expect((((lo ^ tl) >> 8) | (hi ^ th)) == 0, 1)) break;
        asm volatile("s_sleep 4\n\tv_mov_b64 v[240:241], 1\n\t" TICKET "\n\ts_waitcnt vmcnt(0)" ::: "memory");
      }
      asm volatile("v_mov_b32 v241, 0\n\ts_mov_b64 exec, 1\n\tds_write_b64 %0, v[240:241]\n\ts_mov_b64 exec, -1" ::"v"(rdy) : "memory");
    }
    vsetu<79>(fo(1024) + qbF * 4352 + fr * 8);
    {
      const int jj = 4 * (fr >> 3) + (fr & 3);
      const unsigned one = ((fq ^ (fr >> 2)) & 1) ? 0u : (jj & 1) ? 0x3f800000u : 0x3f80u;
      static_for([&](auto I) { vsetu<96 + I>((jj >> 1) == I ? one : 0u); }, make_iseq<4>{});
    }
    unsigned vo = (fq >> 1) * 1024 + (fq & 1) * 256 + fr * 16;
    bf16* out = arg_late<bf16, 13>();
    __amdgpu_buffer_rsrc_t so_ = __builtin_amdgcn_make_buffer_rsrc((void*)out, (short)0, SEQ * OUTW * 2, 0x00020000);
    unsigned vw = ((qbF * 128 + (fr & 7)) * OUTW + DA + (fr >> 3) * 32 + fq * 8) * 2;
    asm volatile("" : "+s"(so_));
    asm volatile("s_waitcnt vmcnt(0) lgkmcnt(0)\n\ts_barrier" : "+v"(vo), "+v"(vw) :: "memory");
    fl_set<T_FS>();
    unsigned tk;
    asm volatile("ds_read_b32 %0, %1\n\ts_waitcnt lgkmcnt(0)" : "=v"(tk) : "v"(rdy) : "memory");
    tk = __builtin_amdgcn_readfirstlane(tk) & 255;
    KST(0, 30);
    if (doA) post_math(pj, pu, out, lds + 131072 + wid * 8192);
#define N3 0
#define N2 12
    constexpr int N1 = 28 - 3 * N3 - 2 * N2;
    static_assert(N1 >= 0 && N3 + N2 + N1 <= 16);
    const int nu = tk < N3 ? 3 : tk < N3 + N2 ? 2 : tk < N3 + N2 + N1 ? 1 : 0;
    const int u0 = 4 * (tk < N3 ? 3 * tk : tk < N3 + N2 ? N3 + 2 * tk : 2 * N3 + N2 + tk) + wid * nu;
    KST(0, 28);
    if (nu) {
      const unsigned so0 = O_S + qbF * 0x200000 + (16 + u0) * 2048, so[3] = {so0, so0, so0};
      unsigned pend = 0x5555u, cnt = 0x400000;
      while (true) {
        unsigned nw;
        if (wid == 3) {
          u64 m;
          const u64 ex = pend | (pend << 1);
          asm volatile(FL_ARM("%1") "\n\t" FL_POLL "\n\ts_waitcnt vmcnt(0)\n\tv_cmp_eq_u64 %0, s[84:85], v[124:125]" : "=s"(m) : "s"(ex) : "memory");
          nw = (unsigned)m & ((unsigned)m >> 1) & pend;
          if (nw) asm volatile("ds_write_b32 %0, %1 offset:4" ::"v"(rdy), "v"(~pend | nw) : "memory");
        } else {
          unsigned t;
          asm volatile("ds_read_b32 %0, %1 offset:4\n\ts_waitcnt lgkmcnt(0)" : "=v"(t) : "v"(rdy) : "memory");
          nw = __builtin_amdgcn_readfirstlane(t) & pend;
        }
        if (--cnt == 0) nw = pend;
        if (nw == 0) {
          if (wid == 3) asm volatile("s_sleep 4");
          else asm volatile("s_sleep 1");
          continue;
        }
        asm volatile("s_nop 4");
        static_for([&](auto S) {
          if ((nw >> (2 * S)) & 1) {

            r_load<0, 2 * S>(vo, so[0]); r_load<0, 2 * S + 1>(vo, so[0]);
            if (nu > 1) { r_load<1, 2 * S>(vo, so[1]); r_load<1, 2 * S + 1>(vo, so[1]); }
            if (nu > 2) { r_load<2, 2 * S>(vo, so[2]); r_load<2, 2 * S + 1>(vo, so[2]); }
          }
        }, make_iseq<8>{});
        pend &= ~nw;
        if (pend == 0) break;
      }
      asm volatile("s_waitcnt vmcnt(0)" ::: "memory");
      KST(0, 16);
      auto oo = [&](int U) { return ((((U >> 1) & 1) * 64 + ((U >> 2) & 3) * 16 + (U & 1) * 8) * OUTW + (U >> 4) * 64) * 2; };
      r_fin<0>(vw, so_, oo(u0));
      if (nu > 1) r_fin<1>(vw, so_, oo(u0 + 1));
      if (nu > 2) r_fin<2>(vw, so_, oo(u0 + 2));
      if (qbF == 15 && tk == 0) asm volatile(FL_SET("%0") ::"s"(fo(6144)) : "memory");
    }
    KST(0, 29);
  }
  asm volatile("s_waitcnt vmcnt(0)" ::: "memory");
  asm volatile("pf_code_end:");
  KST(0, 1);
}

extern "C" void kernel_launch(void* const* d_in, const int* in_sizes, int n_in, void* d_out, int out_size, void* d_ws,
                              size_t ws_size, hipStream_t stream) {
  const bf16* Aq = (const bf16*)d_in[0];
  const bf16* Ak = (const bf16*)d_in[1];
  const bf16* Av = (const bf16*)d_in[2];
  const bf16* Ash = (const bf16*)d_in[3];
  const bf16* Assmk = (const bf16*)d_in[4];
  const bf16* AssmD = (const bf16*)d_in[5];
  const bf16* Bx = (const bf16*)d_in[6];
  const bf16* Bckv = (const bf16*)d_in[7];
  const bf16* Bwq = (const bf16*)d_in[8];
  const bf16* Bwuk = (const bf16*)d_in[9];
  const bf16* Bwuv = (const bf16*)d_in[10];
  const bf16* Bwo = (const bf16*)d_in[11];
  bf16* out = (bf16*)d_out;
  hipFuncSetAttribute((const void*)fused_kernel, hipFuncAttributeMaxDynamicSharedMemorySize, 163840);
  fused_kernel<<<256, 256, 163840, stream>>>(Bckv, Bwuk, Bwuv, Bx, Bwq, (bf16*)d_ws, Ak, Av, Ash, Assmk, AssmD, Bwo, Aq, out);
}
```

```cpp
#include <hip/hip_runtime.h>
#include <hip/hip_bf16.h>
#include <stdint.h>
#include <utility>

typedef __hip_bfloat16 bf16;
typedef __attribute__((ext_vector_type(4))) short bf16x4;
typedef __attribute__((ext_vector_type(2))) unsigned u32x2;

constexpr int SEQ = 2048;
constexpr int DA = 448;
constexpr int NH = 16;
constexpr int HD = 256;
constexpr int DC = 1024;
constexpr int OUTW = 896;
constexpr unsigned TSB = NH * SEQ * HD * 2, O_K = TSB, O_S = 3 * TSB, O_P = 5 * TSB, PSTR = 67584, O_F = O_P + 128 * PSTR,
                   O_X = O_F + 400 * 4352, O_Z = O_X + DA * SEQ * 2, WSB = O_Z + DA * SEQ * 2;
constexpr unsigned fo(int w) { return O_F + (w >> 4) * 4352 + (w & 15) * 8; }

#define KST(kid, which)

__device__ __forceinline__ float bf2f(unsigned short u) { return __uint_as_float(((unsigned)u) << 16); }
__device__ __forceinline__ unsigned short f2bf(float f) { return __bfloat16_as_ushort(__float2bfloat16(f)); }
__device__ __forceinline__ float rbf(float f) { return bf2f(f2bf(f)); }
typedef __attribute__((ext_vector_type(2))) __bf16 bf16x2n;
typedef __attribute__((ext_vector_type(2))) float f32x2n;
__device__ __forceinline__ unsigned pk2(float a, float b) {
  return __builtin_bit_cast(unsigned, __builtin_convertvector((f32x2n){a, b}, bf16x2n));
}

__device__ __forceinline__ int off_b(int row, int ch) { return 256 * row + 16 * (ch ^ (((row & 3) << 2) | ((row >> 2) & 3))); }

template <int R> __device__ __forceinline__ void sset(unsigned x) { asm volatile("s_mov_b32 s%c0, %1" ::"i"(R), "s"(x)); }
template <int R> __device__ __forceinline__ void vsetu(unsigned x) { asm volatile("v_mov_b32 v%c0, %1" ::"i"(R), "v"(x)); }
template <int R> __device__ __forceinline__ void srd_set(const void* p, unsigned n) {
  asm volatile("s_mov_b64 s[%c0:%c1], %4\n\ts_mov_b32 s%c2, %5\n\ts_mov_b32 s%c3, 0x20000" ::"i"(R), "i"(R + 1), "i"(R + 2), "i"(R + 3), "s"(p), "s"(n));
}
template <int R, int L> __device__ __forceinline__ void tload() { asm volatile("v_readlane_b32 s%c0, v78, %1" ::"i"(R), "i"(L)); }
template <int L> __device__ __forceinline__ unsigned tget() { unsigned t; asm volatile("v_readlane_b32 %0, v78, %1" : "=s"(t) : "i"(L)); return t; }
#define FL_SET(S) "buffer_store_dwordx2 v[82:83], v81, s[88:91], " S " offen sc1"
#define FL_ARM(N) "v_cndmask_b32_e64 v80, -2.0, v79, " N
#define FL_POLL "buffer_load_dwordx2 v[124:125], v80, s[88:91], 0 offen sc1"
#define FL_CHECK(N) "s_mov_b32 s48, 0x100000\n7:\n\tv_cmp_eq_u64 vcc, s[84:85], v[124:125]\n\ts_and_b64 vcc, vcc, " N "\n\ts_cmp_eq_u64 vcc, " N \
                    "\n\ts_cbranch_scc1 8f\n\ts_sleep 2\n\t" FL_POLL "\n\ts_waitcnt vmcnt(0)\n\ts_sub_u32 s48, s48, 1\n\ts_cbranch_scc0 7b\n8:"
template <int L> __device__ __forceinline__ void fl_set() {
  asm volatile("v_readlane_b32 s48, v78, %0\n\ts_nop 4\n\t" FL_SET("s48") ::"i"(L) : "memory");
}
template <int L> __device__ __forceinline__ void fl_poll() {
  asm volatile("v_readlane_b32 s50, v78, %0\n\tv_readlane_b32 s51, v78, %1\n\ts_nop 1\n\t" FL_ARM("s[50:51]") "\n\t" FL_POLL ::"i"(L), "i"(L + 1) : "memory");
}
#define TICKET "buffer_atomic_add_x2 v[240:241], v81, s[88:91], s101 offen sc0"
__device__ __forceinline__ void fl_check() { asm volatile(FL_CHECK("s[50:51]") ::: "vcc", "scc", "memory"); }
template <int R> __device__ __forceinline__ float vget() { float t; asm volatile("v_mov_b32 %0, v%c1" : "=v"(t) : "i"(R)); return t; }
template <int R> __device__ __forceinline__ void vset(float x) { asm volatile("v_mov_b32 v%c0, %1" ::"i"(R), "v"(x)); }
template <int R> __device__ __forceinline__ float acc_read1() { float t; asm volatile("v_accvgpr_read_b32 %0, a[%c1]" : "=v"(t) : "i"(R)); return t; }
template <class F, int... I> __device__ __forceinline__ void static_for(F&& f, std::integer_sequence<int, I...>) {
  (f(std::integral_constant<int, I>{}), ...);
}
template <int... Is> using iseq = std::integer_sequence<int, Is...>;
template <int N> using make_iseq = std::make_integer_sequence<int, N>;

typedef __attribute__((ext_vector_type(4))) unsigned u32x4c;
typedef __attribute__((ext_vector_type(4))) float f32x4c;
typedef __attribute__((ext_vector_type(2))) float f32x2c;
typedef unsigned long long u64;
struct PR { u32x4c kk[7], vv[4]; };
__device__ __forceinline__ void prep_load(PR& p, int tid, int cg, int tb, const bf16* __restrict__ k, const bf16* __restrict__ v,
                                          const bf16* __restrict__ shift_k) {
  const __amdgpu_buffer_rsrc_t sk = __builtin_amdgcn_make_buffer_rsrc((void*)k, (short)0, SEQ * DA * 2, 0x00020000);
  const __amdgpu_buffer_rsrc_t sv = __builtin_amdgcn_make_buffer_rsrc((void*)v, (short)0, SEQ * DA * 2, 0x00020000);
  const __amdgpu_buffer_rsrc_t sw = __builtin_amdgcn_make_buffer_rsrc((void*)(shift_k + cg * 256), (short)0, 512, 0x00020000);
  const int vo = ((tb * 128 + (tid >> 3) * 4) * DA + cg * 64 + (tid & 7) * 8) * 2;
#pragma unroll
  for (int q = 0; q < 7; ++q)
    asm volatile("buffer_load_dwordx4 %0, %1, %2, 0 offen" : "=v"(p.kk[q]) : "v"(vo + (q - 3) * DA * 2), "s"(sk) : "memory");
#pragma unroll
  for (int q = 0; q < 4; ++q)
    asm volatile("buffer_load_dwordx4 %0, %1, %2, 0 offen offset:%3" : "=v"(p.vv[q]) : "v"(vo), "s"(sv), "i"(q * DA * 2) : "memory");
#pragma unroll
  for (int q = 0; q < 4; ++q)
    asm volatile("buffer_load_dwordx4 a[%c0:%c1], %2, %3, 0 offen offset:%4" ::"i"(4 * q), "i"(4 * q + 3), "v"((tid & 7) * 64), "s"(sw), "i"(q * 16) : "memory");
}
__device__ __forceinline__ float bfl(unsigned u) { return __uint_as_float(u << 16); }
__device__ __forceinline__ float bfh(unsigned u) { return __uint_as_float(u & 0xffff0000u); }
__device__ __forceinline__ f32x2c rbf2(f32x2c a) {
  return (f32x2c){__uint_as_float(pk2(0.f, a[0])), __uint_as_float(pk2(0.f, a[1]))};
}
template <int I> __device__ __forceinline__ void prep_math(const PR& p, int tid, char* lx) {
  const int rp = tid >> 3, c = tid & 7;
  f32x2c wt[4], kf[7];
  const unsigned w[4] = {__float_as_uint(acc_read1<4 * I>()), __float_as_uint(acc_read1<4 * I + 1>()), __float_as_uint(acc_read1<4 * I + 2>()),
                         __float_as_uint(acc_read1<4 * I + 3>())};
#pragma unroll
  for (int t = 0; t < 4; ++t)
    wt[t] = (t & 1) ? (f32x2c){bfh(w[t >> 1]), bfh(w[2 + (t >> 1)])} : (f32x2c){bfl(w[t >> 1]), bfl(w[2 + (t >> 1)])};
#pragma unroll
  for (int q = 0; q < 7; ++q) kf[q] = (f32x2c){bfl(p.kk[q][I]), bfh(p.kk[q][I])};
  f32x2c x[4];
#pragma unroll
  for (int e = 0; e < 4; ++e) {
    f32x2c s = rbf2(kf[e] * wt[3]);
    s = rbf2(s + rbf2(kf[e + 1] * wt[2]));
    s = rbf2(s + rbf2(kf[e + 2] * wt[1]));
    s = rbf2(s + rbf2(kf[e + 3] * wt[0]));
    x[e] = s * (f32x2c){bfl(p.vv[e][I]), bfh(p.vv[e][I])};
  }
#pragma unroll
  for (int u = 0; u < 2; ++u)
    *(u32x2*)(lx + (c * 8 + 2 * I + u) * 256 + (((31 - rp) * 8) ^ (c << 4))) = (u32x2){pk2(x[3][u], x[2][u]), pk2(x[1][u], x[0][u])};
}
__device__ __forceinline__ void prep_store(int tid, int cg, int tb, bf16* __restrict__ xr, const char* lx) {
  const __amdgpu_buffer_rsrc_t sx = __builtin_amdgcn_make_buffer_rsrc((void*)(xr + (size_t)cg * 64 * SEQ), (short)0, 64 * SEQ * 2, 0x00020000);
#pragma unroll
  for (int rd = 0; rd < 4; ++rd) {
    const int idx = tid + 256 * rd, ch = idx >> 4, q = idx & 15;
    u32x4c w = *(const u32x4c*)(lx + ch * 256 + 16 * (q ^ (ch >> 3)));
    __builtin_amdgcn_raw_buffer_store_b128(w, sx, (ch * SEQ + (15 - tb) * 128 + q * 8) * 2, 0, 16);
  }
}

#define CODE_TOUCH(LB, LE, wx, NW, NL, m0v) do { unsigned blo_, bhi_, elo_; \
    asm volatile("s_getpc_b64 s[48:49]\n\ts_add_u32 s48, s48, " LB "@rel32@lo+4\n\ts_addc_u32 s49, s49, " LB "@rel32@hi+12\n\t" \
                 "s_mov_b32 %0, s48\n\ts_mov_b32 %1, s49\n\t" \
                 "s_getpc_b64 s[48:49]\n\ts_add_u32 s48, s48, " LE "@rel32@lo+4\n\ts_mov_b32 %2, s48" \
                 : "=s"(blo_), "=s"(bhi_), "=s"(elo_) :: "scc"); \
    const unsigned n_ = elo_ - (blo_ & ~127u); \
    const __amdgpu_buffer_rsrc_t srd_ = __builtin_amdgcn_make_buffer_rsrc((void*)(((unsigned long long)bhi_ << 32) | (blo_ & ~127u)), (short)0, n_ < 262144u ? n_ : 0u, 0x00020000); \
    const unsigned l_ = threadIdx.x & 63, vo_ = l_ < (NL) ? ((wx) + l_ * (NW)) * 128u : 0x80000000u; \
    asm volatile("s_mov_b32 m0, %0\n\ts_nop 4\n\tbuffer_load_dword %1, %2, 0 offen lds" ::"s"(m0v), "v"(vo_), "s"(srd_) : "memory"); } while (0)
template <int I> __device__ __forceinline__ void d_store() {
  asm volatile("buffer_store_dwordx4 v[%c0:%c1], v%c2, s[88:91], s101 offen offset:%3 sc1" ::"i"(128 + 4 * I), "i"(131 + 4 * I), "i"(112 + ((I >> 2) & 1)),
               "i"((I & 3) * 1024) : "memory");
  if constexpr ((I & 7) == 7) asm volatile("s_addk_i32 s101, 0x4000" ::: "scc");
}
constexpr int NS_X = 19;
struct CV { unsigned pa[4], pb, px, vz, sz; float D; };
constexpr int cv_J(bool up, int q) { int J = up ? 0 : 7; while (q > J) { q -= J + 1; J += up ? 1 : -1; } return J; }
constexpr int cv_i(bool up, int q) { int J = up ? 0 : 7; while (q > J) { q -= J + 1; J += up ? 1 : -1; } return q; }
template <int R, int N> __device__ __forceinline__ void cv_rd(CV& c) {
  if constexpr (N < 16) {
    constexpr int i = N >> 1, o = 16 * R + 128 * (i & 1) + 2 * (N & 1), r = 32 + 32 * (R & 1) + 2 * N;
    asm volatile("ds_read2_b32 a[%c1:%c2], %0 offset0:%c3 offset1:%c4" ::"v"(c.pa[i >> 1]), "i"(r), "i"(r + 1), "i"(o), "i"(o + 1));
  } else {
    constexpr int m = N - 16, r = 192 + 32 * (R & 1) + 4 * m;
    asm volatile("ds_read_b128 a[%c1:%c2], %0 offset:%c3" ::"v"(c.pb), "i"(r), "i"(r + 3), "i"(64 * R + 512 * (7 - m)));
  }
}
template <int O> __device__ __forceinline__ void cv_xd(CV& c) {
  constexpr int e = O & 31, x = 96 + ((e >> 1) ^ 1);
  if constexpr (O >= 64) asm volatile("v_cvt_pk_bf16_f32 v%c0, 0, v%c0" ::"i"(160 + e));
  else if constexpr (O >= 32) asm volatile("v_mul_f32 v%c0, v%c0, %1" ::"i"(160 + e), "v"(c.D));
  else if constexpr (O & 1) asm volatile("v_lshlrev_b32 v%c0, 16, v%c1" ::"i"(160 + e), "i"(x));
  else asm volatile("v_and_b32 v%c0, 0xffff0000, v%c1" ::"i"(160 + e), "i"(x));
}
template <int O> __device__ __forceinline__ void cv_z(CV& c) {
  constexpr int J = O / 11, k = O % 11, y = 128 + 4 * J;
  if constexpr (k < 4) asm volatile("v_cvt_pk_bf16_f32 v%c0, 0, v%c0" ::"i"(y + k));
  else if constexpr (k < 8) asm volatile("v_add_f32 v%c0, v%c0, v%c1" ::"i"(y + k - 4), "i"(y + 28 + k));
  else if constexpr (k < 10) asm volatile("v_cvt_pk_bf16_f32 v%c0, v%c1, v%c2" ::"i"(y + k - 8), "i"(y + 2 * k - 16), "i"(y + 2 * k - 15));
  else asm volatile("buffer_store_dwordx2 v[%c0:%c1], %2, s[88:91], s101 offen sc1\n\ts_add_u32 s101, s101, 0x8000" ::"i"(y), "i"(y + 1), "v"(c.vz) : "memory");
}
constexpr int cv_z0(int Q) {
  int o = 0;
  for (int q = 0; q < Q; ++q)
    for (int k = 0; k < (q % 4 == 1 ? 0 : 3); ++k) { const int J = o / 11; if ((J + 1) * (J + 2) / 2 - 1 + (J ? 4 : 7) <= q) ++o; }
  return o;
}
constexpr int cv_xd0(int T) { int o = 0; for (int t = 0; t < T; ++t) o += t % 4 != 1 || (t >= 72 && t < 108); return o < 96 ? o : 96; }
static_assert(cv_xd0(144) == 96);
template <int B, int... Is> __device__ __forceinline__ void cv_zs(CV& c, iseq<Is...>) { (cv_z<B + Is>(c), ...); }
template <int R, int Q> __device__ __forceinline__ void cv_slot(CV& c) {
  constexpr int i = cv_i(R == 7, Q), J = cv_J(R == 7, Q), d = 128 + 4 * J, a = 32 + 32 * (R & 1) + 4 * i, b = 192 + 32 * (R & 1) + 4 * (J - i);
  constexpr int T = 36 * R + Q, vm = Q % 4 == 1 && Q < 32 ? Q / 4 : -1;
  if constexpr (Q == 0) asm volatile("s_waitcnt lgkmcnt(0)");
  if constexpr (R >= 6 && vm >= 0 && vm % 4 == 0) asm volatile("s_add_u32 m0, s79, %0" ::"i"(vm * 1024));
  if constexpr (R == 0 && i == 0)
    asm volatile("v_mfma_f32_16x16x32_bf16 v[%c0:%c1], a[%c2:%c3], a[%c4:%c5], 0" ::"i"(d), "i"(d + 3), "i"(a), "i"(a + 3), "i"(b), "i"(b + 3));
  else
    asm volatile("v_mfma_f32_16x16x32_bf16 v[%c0:%c1], a[%c2:%c3], a[%c4:%c5], v[%c0:%c1]" ::"i"(d), "i"(d + 3), "i"(a), "i"(a + 3), "i"(b), "i"(b + 3));
  if constexpr (Q < 24 && R < 7) cv_rd<R + 1, Q>(c);
  if constexpr (T % 7 == 3 && NS_X + 5 + T / 7 < 32) d_store<NS_X + 5 + T / 7>();
  if constexpr (R == 2 && Q >= 24 && Q < 32)
    asm volatile("ds_read_b64 v[%c1:%c2], %0 offset:%c3" ::"v"(c.px), "i"(96 + 2 * (Q - 24)), "i"(97 + 2 * (Q - 24)), "i"(512 * (31 - Q)));
  if constexpr (R >= 3 && R < 7) if constexpr (cv_xd0(T - 107) > cv_xd0(T - 108)) cv_xd<cv_xd0(T - 108)>(c);
  if constexpr ((R == 3 || R == 4) && vm >= 0) {
    constexpr int f = (R - 3) * 8 + vm;
    asm volatile("buffer_load_dwordx4 a[%c0:%c1], v84, s[88:91], s96 offen offset:%2 sc1" ::"i"(128 + 4 * f), "i"(131 + 4 * f), "i"((f & 3) * 1024) : "memory");
    if constexpr ((f & 3) == 3) asm volatile("s_addk_i32 s96, %0" ::"i"(f == 7 ? 28672 : 4096));
  }
  if constexpr (R == 4 && Q == 35) asm volatile(FL_POLL ::: "memory");
  if constexpr (R >= 6 && vm >= 0)
    asm volatile("buffer_load_dwordx4 v%c0, s[88:91], s98 offen offset:%1 sc1 lds" ::"i"(86 + vm / 4), "i"((vm & 3) * 1024) : "memory");
  if constexpr (R == 6 && Q == 35) asm volatile("s_add_u32 s98, s98, 0x8000\n\ts_xor_b32 s79, s79, 0x8000");
  if constexpr (R == 7) cv_zs<cv_z0(Q)>(c, make_iseq<cv_z0(Q + 1) - cv_z0(Q)>{});
  __builtin_amdgcn_sched_barrier(0);
}
template <int R, int... Q> __device__ __forceinline__ void cv_step(CV& c, iseq<Q...>) { (cv_slot<R, Q>(c), ...); }
template <int... N> __device__ __forceinline__ void cv_rd0(CV& c, iseq<N...>) { (cv_rd<0, N>(c), ...); }
template <int FLAG> __device__ __forceinline__ void conv_wave(unsigned sz, int ck, float Dd, char* lw) {
  int lane = threadIdx.x & 63; asm volatile("" : "+v"(lane));
  const int l16 = lane & 15, fq = lane >> 4;
  u32x4c w[5]; unsigned w4[5];
#pragma unroll
  for (int r = 0; r < 5; ++r) {
    w[r] = *(const u32x4c*)(lw + r * 1024 + lane * 16);
    w4[r] = *(const unsigned*)(lw + r * 1024 + lane * 16 + 16);
  }
  d_store<NS_X>(); d_store<NS_X + 1>();
#pragma unroll
  for (int r = 0; r < 5; ++r)
    *(u32x4c*)(lw + 10304 + r * 1024 + lane * 16) = (u32x4c){__builtin_amdgcn_alignbit(w[r][1], w[r][0], 16), __builtin_amdgcn_alignbit(w[r][2], w[r][1], 16),
                                                             __builtin_amdgcn_alignbit(w[r][3], w[r][2], 16), __builtin_amdgcn_alignbit(w4[r], w[r][3], 16)};
  d_store<NS_X + 2>();
  CV c;
#pragma unroll
  for (int i = 0; i < 4; ++i) c.pa[i] = (unsigned)(size_t)lw + ((l16 + 1) & 1) * 10304 + 4 * ((8 * fq + l16 + 1) >> 1) + 1024 * i;
  c.pb = (unsigned)(size_t)lw + 5120 + (2032 - 16 * l16 + 8 * fq) * 2 - 512 * 7;
  c.vz = (l16 >> 1) * 4096 + (((l16 & 1) * 2 + (fq >> 1)) ^ (ck >> 1)) * 16 + (fq & 1) * 8;
  c.px = (unsigned)(size_t)lw + 5120 + 2044 * 2 - 512 * 7 - (16 * l16 + 4 * fq) * 2;
  c.sz = sz; c.D = Dd;
  asm volatile("" : "+v"(c.vz), "+v"(c.px), "+v"(c.D) :: "memory");
  asm volatile("" : "+s"(c.sz));
  cv_rd0(c, make_iseq<24>{});
  d_store<NS_X + 3>(); d_store<NS_X + 4>();
  __builtin_amdgcn_sched_barrier(0);
  static_for([&](auto R) __attribute__((always_inline)) {
    cv_step<R>(c, make_iseq<36>{});
    if constexpr (R == 2) {
      asm volatile("s_waitcnt vmcnt(0)\n\ts_barrier" ::: "memory");
      fl_set<FLAG>();
      sset<101>(c.sz);
    }
    if constexpr (R == 5) {
      asm volatile("s_waitcnt vmcnt(0)" ::: "memory");
      fl_check();
    }
    __builtin_amdgcn_sched_barrier(0);
  }, make_iseq<8>{});
  asm volatile("s_nop 7");
  cv_zs<cv_z0(36)>(c, make_iseq<88 - cv_z0(36)>{});
  __builtin_amdgcn_sched_barrier(0);
}

__device__ __forceinline__ void post_dma(int cg, int tb, const bf16* __restrict__ q, unsigned m0w) {
  int lane = threadIdx.x & 63; asm volatile("" : "+v"(lane));
  const __amdgpu_buffer_rsrc_t sq = __builtin_amdgcn_make_buffer_rsrc((void*)(q + (size_t)tb * 32 * DA + cg * 64), (short)0, 32 * DA * 2, 0x00020000);
  const unsigned vq = ((lane >> 3) * DA + (lane & 7) * 8) * 2;
  asm volatile("s_mov_b32 m0, %0\n\ts_nop 4\n\tbuffer_load_dwordx4 %1, %2, 0 offen lds\n\tbuffer_load_dwordx4 %1, %2, %3 offen offset:1024 lds\n\t"
               "buffer_load_dwordx4 %1, %2, %4 offen offset:2048 lds\n\tbuffer_load_dwordx4 %1, %2, %5 offen offset:3072 lds"
               ::"s"(m0w + 4096), "v"(vq), "s"(sq), "s"(8 * DA * 2 - 1024), "s"(16 * DA * 2 - 2048), "s"(24 * DA * 2 - 3072) : "memory");
}
__device__ __forceinline__ void post_math(int cg, int tb, bf16* __restrict__ out, const char* lw) {
  int lane = threadIdx.x & 63; asm volatile("" : "+v"(lane));
  const int tl = lane >> 3, ck = lane & 7;
  const __amdgpu_buffer_rsrc_t so = __builtin_amdgcn_make_buffer_rsrc((void*)(out + (size_t)tb * 32 * OUTW + cg * 64), (short)0, 32 * OUTW * 2 - 2 * DA, 0x00020000);
  const unsigned vo = (tl * OUTW + ck * 8) * 2;
#pragma unroll
  for (int i = 0; i < 4; ++i) {
    const u32x4c qv = *(const u32x4c*)(lw + 4096 + i * 1024 + lane * 16);
    const char* lz = lw + ck * 64 + 16 * (i ^ (ck >> 1)) + tl * 2;
    u32x4c o;
#pragma unroll
    for (int e = 0; e < 4; ++e) {
      const float z0 = bf2f(*(const unsigned short*)(lz + 2 * e * 512));
      const float z1 = bf2f(*(const unsigned short*)(lz + (2 * e + 1) * 512));
      o[e] = pk2(bfl(qv[e]) * z0, bfh(qv[e]) * z1);
    }
    __builtin_amdgcn_raw_buffer_store_b128(o, so, vo + i * 8 * OUTW * 2, 0, 17);
  }
}

#define DI __device__ __forceinline__
#define GAP() __builtin_amdgcn_sched_barrier(0)
typedef __attribute__((ext_vector_type(4))) float f32x4g;
typedef __attribute__((ext_vector_type(4))) unsigned u32x4g;
constexpr int LDW = NH * HD;
struct GM { unsigned aA[2], aB[2][8]; };
template <bool W, int J, int V> DI void g_dma() {
  if constexpr (J == 0) asm volatile("buffer_load_dwordx4 v%c0, s[%c1:%c2], 0 offen lds" ::"i"(V), "i"(W ? 60 : 56), "i"(W ? 63 : 59) : "memory");
  else asm volatile("buffer_load_dwordx4 v%c0, s[%c1:%c2], s%c3 offen lds" ::"i"(V), "i"(W ? 60 : 56), "i"(W ? 63 : 59), "i"((W ? 70 : 63) + J) : "memory");
}
template <int P, int KS, int I> DI void g_wread(GM& g) {
  constexpr int nf = I >> 1, hf = I & 1, r = 192 - 64 * P + 8 * nf + 4 * KS + 2 * hf;
  asm volatile("ds_read_b64_tr_b16 a[%c1:%c2], %0 offset:%c3" ::"v"(g.aB[hf][nf]), "i"(r), "i"(r + 1), "i"(P * 32768 + KS * 16384));
}
template <int P, int M, int KS> DI void g_aread(GM& g) {
  constexpr int r = 96 + 8 * (M & 1) + 4 * KS;
  asm volatile("ds_read_b128 v[%c1:%c2], %0 offset:%c3" ::"v"(g.aA[KS]), "i"(r), "i"(r + 3), "i"(P * 32768 + M * 2048));
}
constexpr float CS = 0.0625f * 1.4426950408889634f;
constexpr int NS_Q = 19;
constexpr int e_vm(int G) { return (G * 3) % 8 < 3 ? G * 3 / 8 : -1; }
constexpr int e_st(int MODE, int G) {
  const int q = G & 15, k3 = ((G >> 4) - 1) * 3 + (q == 0 ? 0 : q == 4 ? 1 : 2), k2 = NS_Q + (G >> 4) * 2 + (q == 4);
  if (MODE == 5) return e_vm(G) >= 32 ? e_vm(G) - 32 : -1;
  if (MODE == 3) return G >= 33 && (G - 33) % 5 == 0 && (G - 33) / 5 < NS_X ? (G - 33) / 5 : -1;
  if (MODE == 2) return G >= 30 && (q == 0 || q == 4 || q == 14) && k3 - 2 < NS_Q ? k3 - 2 : -1;
  if (MODE == 4) return (q == 0 || q == 4) && k2 < 32 ? k2 : -1;
  return -1;
}
constexpr int e_nst(int MODE, int G0, int G1) { int n = 0; for (int G = G0; G < G1; ++G) n += e_st(MODE, G) >= 0; return n; }
static_assert(e_nst(2, 0, 128) == NS_Q && e_nst(3, 30, 128) == NS_X && e_nst(5, 0, 128) == 16 && e_nst(4, 0, 104) == 32 - NS_Q);
constexpr int e_slot(int MODE, int G) { return MODE == 3 ? (G % 3 == 0 && G < 30 ? G / 3 : -1) : e_vm(G) >= 0 && e_vm(G) < 32 ? 10 + e_vm(G) : -1; }
struct ES { int op0[129], nop[129]; };
constexpr ES e_sched(bool TR, int MODE) {
  ES z{};
  const bool SCL = MODE == 2 || MODE == 4;
  const int per = SCL ? 16 : 8, o1 = 32 * per;
  int o = MODE == 4 ? e_sched(false, 2).op0[128] : 0;
  for (int G = 0; G <= 128; ++G) {
    const int q = G & 15;
    int cap = 2 - (q == 1 || q == 2 || q == 15);
    if (SCL) cap -= (q >= 3 && q < 11 && (q & 1)) + (q == 7 || q == 11) + 2 * (G == 70 || G == 74 || (G & 63) == 61);
    cap += MODE == 2 ? 4 + (q == 10 || q == 13) : MODE == 3 ? 1 : MODE == 5 ? 2 : 0;
    if (e_st(MODE, G) >= 0 || (SCL ? q == 8 || q == 12 : e_slot(MODE, G) >= 0) || (MODE == 4 && G < 4)) cap = 0;
    if (G == 128) cap = 10000;
    z.op0[G] = o;
    while (o < o1) {
      const int i = o / per, rdy = MODE == 4 ? 0 : (TR ? 32 * (i >> 3) + 16 + 2 * (i & 7) + 1 : 4 * i + 3) + 6;
      if ((G < rdy && G < 128) || cap-- <= 0) break;
      ++o; ++z.nop[G];
    }
  }
  return z;
}
template <bool TR, int MODE> constexpr ES ESCH = e_sched(TR, MODE);
static_assert(ESCH<false, 4>.op0[96] == 512 && ESCH<false, 4>.op0[0] >= 24 * 16);
template <bool TR, bool SCL, int O> DI void e_op() {
  constexpr int per = SCL ? 16 : 8, i = O / per, k = O % per, r = 128 + 4 * i, t = 114 + 4 * (i & 1), c = k - (per - 4);
  if constexpr (k < 4) asm volatile("v_accvgpr_read_b32 v%c0, a[%c1]" ::"i"(t + k), "i"(4 * i + k));
  else if constexpr (c < 0) asm volatile("v_mul_f32 v%c0, s83, v%c0" ::"i"(k < 8 ? r + (k ^ 6) : t + k - 8));
  else {
    constexpr int d = c == 0 ? r + 3 : c == 1 ? r + 2 : r + c - 2, x = c == 0 ? r + 2 : c == 1 ? r : t + 2 * (c - 2);
    asm volatile("v_cvt_pk_bf16_f32 v%c0, v%c1, v%c2" ::"i"(d), "i"(x), "i"(x + 1));
  }
}
template <bool TR, bool SCL, int B, int... Is> DI void e_ops(iseq<Is...>) { (e_op<TR, SCL, B + Is>(), ...); }
template <int S> DI void e_m0() {
  if constexpr (S == 0 || S == 1 || S == 5 || S == 9) asm volatile("s_add_u32 m0, s78, %0" ::"i"(S * 1024));
  if constexpr (S >= 26 && (S - 26) % 4 == 0) asm volatile("s_add_u32 m0, s79, %0" ::"i"(((S - 26) >> 3) * 32768 + ((S - 26) & 4) * 1024));
}
template <int S> DI void e_load() {
  if constexpr (S == 0) asm volatile("buffer_load_dwordx4 v122, s[56:59], 0 offen lds" ::: "memory");
  else if constexpr (S < 5) asm volatile("buffer_load_dwordx4 v84, s[56:59], s64 offen offset:%0 lds" ::"i"((S - 1) * 1024) : "memory");
  else if constexpr (S < 9) asm volatile("buffer_load_dwordx4 v84, s[60:63], 0 offen offset:%0 sc1 lds" ::"i"((S - 5) * 1024) : "memory");
  else if constexpr (S == 9) asm volatile("buffer_load_dwordx4 v84, s[60:63], s62 offen sc1 lds" ::: "memory");
  else if constexpr (S < 26) {
    constexpr int f = S - 10;
    asm volatile("buffer_load_dwordx4 a[%c0:%c1], v84, s[88:91], s96 offen offset:%2 sc1" ::"i"(128 + 4 * f), "i"(131 + 4 * f), "i"((f & 3) * 1024) : "memory");
    if constexpr ((f & 3) == 3) asm volatile("s_addk_i32 s96, %0" ::"i"(f == 7 ? 28672 : 4096));
  } else {
    constexpr int f = S - 26;
    asm volatile("buffer_load_dwordx4 v%c0, s[88:91], s98 offen offset:%1 sc1 lds" ::"i"(86 + ((f >> 2) & 1)), "i"((f & 3) * 1024) : "memory");
    if constexpr (f == 7) asm volatile("s_add_u32 s98, s98, s97");
    if constexpr (f == 15) asm volatile(FL_POLL ::: "memory");
  }
}
template <bool TR, int P, int H, int G, int MODE> DI void g_gap(GM& g) {
  constexpr int m = 4 * H + (G >> 4), q = G & 15, n = q >> 1, ks = q & 1, GG = 64 * H + G;
  constexpr bool inV = TR ? m & 1 : n & 1, END = MODE == 3 || MODE == 5;
  constexpr int a = 96 + 8 * (m & 1) + 4 * ks, b = 192 - 64 * P + 8 * n + 4 * ks, d = (inV ? 128 : 0) + 4 * (TR ? 8 * (m >> 1) + n : 4 * m + (n >> 1));
  constexpr int J = END ? -1 : q == 8 ? 2 * (G >> 4) : q == 12 ? 2 * (G >> 4) + 1 : -1, gj = (J & 3) ^ ((J & 3) >> 1);
  if constexpr (END) { if constexpr (q == 0 && GG > 0) asm volatile("s_waitcnt lgkmcnt(0)"); e_m0<e_slot(MODE, GG)>(); }
  else if constexpr (q == 0 && G > 0) asm volatile("s_waitcnt lgkmcnt(4)");
  if constexpr (J == 0) asm volatile("s_add_u32 m0, s78, %0" ::"i"((P ^ H ^ 1) * 32768 + (1 - H) * 8192));
  else if constexpr (J == 4) asm volatile("s_add_u32 m0, s79, %0" ::"i"((P ^ H) * 32768 + (1 - H) * 16384));
  else if constexpr (J > 0 && (J & 3) == 1) asm volatile("s_bitset1_b32 m0, 12");
  else if constexpr (J > 0 && (J & 3) == 2) asm volatile("s_bitset1_b32 m0, %0" ::"i"(J < 4 ? 14 : 13));
  else if constexpr (J > 0) asm volatile("s_bitset0_b32 m0, 12");
#define GMF(D, X, Y, C) asm volatile("v_mfma_f32_16x16x32_bf16 " D "[%c0:%c1], " X ", " Y ", " C ::"i"(d), "i"(d + 3), "i"(a), "i"(a + 3), "i"(b), "i"(b + 3))
#define GMF2(D, C) do { if constexpr (TR) GMF(D, "v[%c2:%c3]", "a[%c4:%c5]", C); else GMF(D, "a[%c4:%c5]", "v[%c2:%c3]", C); } while (0)
  if constexpr ((MODE == 1 || MODE == 4) && ks == 0) { if constexpr (inV) GMF2("v", "0"); else GMF2("a", "0"); }
  else if constexpr (inV) GMF2("v", "v[%c0:%c1]");
  else GMF2("a", "a[%c0:%c1]");
#undef GMF2
#undef GMF
  if constexpr ((q == 1 || q == 2) && !(END && m == 7)) g_aread<(m < 7 ? P : P ^ 1), (m + 1) & 7, q - 1>(g);
  if constexpr (!END && q >= 3 && q < 11 && (q & 1)) g_wread<P ^ 1, H, (G >> 4) * 4 + (q - 3) / 2>(g);
  if constexpr (e_st(MODE, GG) >= 0) {
    static_assert(ESCH<TR, MODE>.op0[GG] >= (e_st(MODE, GG) + 1) * (END ? 8 : 16));
    d_store<e_st(MODE, GG)>();
  }
  if constexpr (J >= 0 && J < 4) g_dma<false, (1 - H) * 2 + (gj & 1) + (gj >> 1) * 4, 90>();
  else if constexpr (J >= 4) g_dma<true, (1 - H) * 4 + gj, 91>();
  if constexpr (MODE >= 2) e_ops<TR, !END, ESCH<TR, MODE>.op0[GG]>(make_iseq<ESCH<TR, MODE>.nop[GG]>{});
  if constexpr (END) {
    if constexpr (e_slot(MODE, GG) >= 0) e_load<e_slot(MODE, GG)>();
  } else {
    if constexpr (H == 1 && G == 6) asm volatile("v_lshl_add_u32 v90, s82, 7, v92\n\ts_and_b32 s82, s80, 15");
    if constexpr (H == 1 && G == 10) asm volatile("s_lshr_b64 s[80:81], s[80:81], 4\n\tv_lshl_add_u32 v91, s82, 19, v93");
    if constexpr (G == 61) {
      constexpr int nst = e_nst(MODE, H ? 29 : 0, GG + 1) + (MODE == 4 && !H ? e_nst(2, 93, 128) : 0);
      asm volatile("s_waitcnt vmcnt(%0) lgkmcnt(0)\n\ts_barrier" ::"i"(12 + nst) : "memory");
    }
  }
  if constexpr (END && GG == 127) {
    asm volatile("s_nop 7");
    e_ops<TR, false, ESCH<TR, MODE>.op0[128]>(make_iseq<ESCH<TR, MODE>.nop[128]>{});
  }
  GAP();
}
template <bool TR, int P, int H, int MODE, int... Gs> DI void g_half_(GM& g, iseq<Gs...>) { (g_gap<TR, P, H, Gs, MODE>(g), ...); }
template <bool TR, int P, int H, int MODE> DI void g_half(GM& g) { g_half_<TR, P, H, MODE>(g, make_iseq<64>{}); }
template <bool TR, int P, int MODE> DI void g_tile(GM& g) { g_half<TR, P, 0, MODE>(g); g_half<TR, P, 1, MODE>(g); }
template <int P, int... Is> DI void g_read0(GM& g, iseq<Is...>) {
  (g_wread<P, 0, Is>(g), ...); (g_wread<P, 1, Is>(g), ...);
  g_aread<P, 0, 0>(g); g_aread<P, 0, 1>(g);
}
DI void g_proW(unsigned g, unsigned mW) {
  asm volatile("v_lshl_add_u32 v91, %0, 18, v93\n\ts_mov_b32 m0, %1" ::"s"(g), "s"(mW));
  asm volatile("s_nop 0"); g_dma<true, 0, 91>();
  asm volatile("s_bitset1_b32 m0, 12\n\ts_nop 0"); g_dma<true, 1, 91>();
  asm volatile("s_bitset1_b32 m0, 13\n\ts_nop 0"); g_dma<true, 3, 91>();
  asm volatile("s_bitset0_b32 m0, 12\n\ts_nop 0"); g_dma<true, 2, 91>();
}
template <int PART = 3> DI void g_pro(unsigned g, unsigned mA, unsigned mW, unsigned lda) {
  g &= 3;
  if constexpr (PART & 1)
    asm volatile("v_add_u32 v90, %0, v92\n\ts_mov_b32 m0, %1" ::"s"((g >> 1) * 128 + (g & 1) * 128 * lda), "s"((mA ^ (g >> 1) * 32768) + (g & 1) * 8192));
  if constexpr (PART == 1) return;
  asm volatile("s_nop 0"); g_dma<false, 0, 90>();
  asm volatile("s_bitset1_b32 m0, 12\n\ts_nop 0"); g_dma<false, 1, 90>();
  asm volatile("s_bitset1_b32 m0, 14\n\ts_nop 0"); g_dma<false, 5, 90>();
  asm volatile("s_bitset0_b32 m0, 12\n\ts_nop 0"); g_dma<false, 4, 90>();
  g_proW(g, mW ^ g * 16384);
}
template <bool TR, bool SCL, class HOOK> DI void g_loop(GM& g, unsigned evm, HOOK&& hook) {
  constexpr int nkt = SCL ? 7 : 16, P0 = SCL ? 0 : 1;
  asm volatile("" : "+s"(evm));
#pragma unroll 1
  for (int t = 1;; t += 2) {
    g_half<TR, P0 ^ 1, 0, 0>(g);
    if (__builtin_expect((evm >> t) & 1, 0)) { hook(t); GAP(); }
    g_half<TR, P0 ^ 1, 1, 0>(g);
    if (SCL && t + 2 >= nkt) break;
    g_half<TR, P0, 0, 0>(g);
    if constexpr (SCL) { if (__builtin_expect(t == 3, 0)) { hook(4); GAP(); } }
    else asm volatile("s_cmp_eq_u32 %0, 13\n\ts_cselect_b32 s78, 0x28000, s78" ::"s"(t) : "scc");
    g_half<TR, P0, 1, 0>(g);
    if (!SCL && t + 3 >= nkt) break;
  }
}

typedef __attribute__((ext_vector_type(4))) unsigned u32x4;
typedef __attribute__((ext_vector_type(4))) float f32x4;

constexpr int R_S = 128, R_P = 192, R_CI = 224, R_E = 96;
constexpr int R_TS = 112, R_RS = 114, R_M = 116;
constexpr int R_PD = 118, R_THR = 120, R_TMP = 121;
constexpr int r_dk(int q) { return q < 6 ? 90 + q : 120 + q; }
constexpr int S_V = 96, S_T = 97, S_K = 98, S_I = 99, S_EV = 100;
struct AC {
  unsigned rare;
  unsigned tb, vs, o0, o1;
};
DI bool redo() { u64 t; asm volatile("s_mov_b64 %0, s[54:55]" : "=s"(t)); return t != 0; }
template <int X, int F> DI void kread() {
  constexpr int r = 192 + 4 * (F & 15);
  asm volatile("ds_read_b128 a[%c0:%c1], v84 offset:%c2" ::"i"(r), "i"(r + 3), "i"(X * 32768 + F * 1024));
}
template <int X, int F> DI void vread() {
  constexpr int r = 192 + 4 * (F & 15);
  asm volatile("ds_read_b128 a[%c0:%c1], v85 offset:%c2" ::"i"(r), "i"(r + 3), "i"(X * 32768 + (F & 1) * 16384 + (F >> 1) * 1024));
}
template <int X, int... Fs> DI void kreads(iseq<Fs...>) { (kread<X, Fs>(), ...); }
template <int X, int J> DI void dma_m0() {
  if constexpr (J == 0) asm volatile("s_mov_b32 m0, s%c0" ::"i"(78 + X));
  else if constexpr (J == 4) asm volatile("s_or_b32 m0, s%c0, 0x1000" ::"i"(78 + X));
  else if constexpr (J == 8) asm volatile("s_bitset0_b32 m0, 16");
  else if constexpr (J == 12) asm volatile("s_bitset0_b32 m0, 12");
}
template <int J> DI void dma_at() {
  if constexpr (J < 8)
    asm volatile("buffer_load_dwordx4 v%c0, s[88:91], s96 offen offset:%1 sc1 lds" ::"i"(86 + (J >= 4)), "i"((J & 3) * 1024) : "memory");
  else
    asm volatile("buffer_load_dwordx4 v%c0, s[92:95], s98 offen offset:%1 sc1 lds" ::"i"(r_dk(J - 8)), "i"((J & 3) * 1024) : "memory");
}
template <int Q> DI void dma4k() {
  asm volatile("s_xor_b32 m0, s79, %0\n\ts_add_u32 s48, s98, %1\n\tbuffer_load_dwordx4 v%c2, s[88:91], s48 offen sc1 lds\n\t"
               "buffer_load_dwordx4 v%c2, s[88:91], s48 offen offset:1024 sc1 lds\n\tbuffer_load_dwordx4 v%c2, s[88:91], s48 offen offset:2048 sc1 lds\n\t"
               "buffer_load_dwordx4 v%c2, s[88:91], s48 offen offset:3072 sc1 lds"
               ::"i"((Q >> 1) * 32768 + (Q & 1) * 4096), "i"((Q >> 1) * 32768), "i"(86 + (Q & 1)) : "memory", "scc");
}
template <int Q> DI void acc_zero2() {
  asm volatile("v_accvgpr_write_b32 a[%c0], 0\n\tv_accvgpr_write_b32 a[%c1], 0" ::"i"(2 * Q), "i"(2 * Q + 1));
}
#define SMV 0
struct SMS { int ex[128], op0[128], nop[128], last; };
constexpr SMS sm_sched() {
  SMS z{};
  int et[32] = {}, e = 0;
  for (int t = 0; t < 128; ++t) {
    const int g = t & 15;
    const bool x = t < 48 ? (g >= 8 && (g & 1) && g < 15) : t < 64 ? (g >= 9 && g < 15) : (g >= 8 && g < 14);
    z.ex[t] = x && e < 32 ? e : -1;
    if (z.ex[t] >= 0) et[e++] = t;
  }
  int o = 0;
  for (int t = 0; t < 128; ++t) {
    const int g = t & 15;
    int cap = z.ex[t] >= 0 || t < 8 || (t < 48 && (g == 7 || (g >= 8 && !(g & 1)))) || t == 56 || t == 63 || t == 111 ? 0 : 1;
    z.op0[t] = o; z.nop[t] = 0;
    while (cap > 0 && o < 48 && et[2 * (o / 3) + (o % 3 == 0 ? 0 : 1)] + 2 <= t) {
      const int el = 2 * (o / 3) + o % 3;
      if (!(o % 3 < 2 && (el == 0 || el == 4))) --cap;
      ++o; ++z.nop[t]; z.last = t;
    }
  }
  for (int k = 16; k < 32; ++k) if (z.op0[et[k]] < 3 * ((k - 16) / 2 + 1)) z.last = 1000;
  return z;
}
constexpr SMS SM = sm_sched();
static_assert(SM.last <= 117);
constexpr int p_reg(int Y, int k) { return R_P + 16 * Y + 4 * ((k >> 3) * 2 + ((k >> 1) & 1)) + ((k >> 2) & 1) * 2 + (k & 1); }
template <int Y, int O> DI void sm_op() {
  constexpr int k = O / 3, e = 2 * k + O % 3, ts = R_TS + ((e >> 2) & 1);
  if constexpr (O % 3 == 2) asm volatile("v_cvt_pk_bf16_f32 v%c0, v%c1, v%c2" ::"i"(p_reg(Y, k)), "i"(R_E + ((2 * k) & 15)), "i"(R_E + ((2 * k + 1) & 15)));
  else if constexpr (e == 1 || e == 5) asm volatile("v_add_f32_e32 v%c0, v%c1, v%c2" ::"i"(ts), "i"(R_E + e - 1), "i"(R_E + e));
  else if constexpr (e != 0 && e != 4) asm volatile("v_add_f32_e32 v%c0, v%c0, v%c1" ::"i"(ts), "i"(R_E + (e & 15)));
}
template <int Y, int T> DI void sm_gap(AC& c) {
  if constexpr (SM.ex[T] >= 0) asm volatile("v_exp_f32 v%c0, v%c1" ::"i"(R_E + (SM.ex[T] & 15)), "i"(R_S + 32 * Y + SM.ex[T]));
  if constexpr (SM.nop[T] > 0) sm_op<Y, SM.op0[T]>();
  if constexpr (SM.nop[T] > 1) sm_op<Y, SM.op0[T] + 1>();
  if constexpr (SM.nop[T] > 2) sm_op<Y, SM.op0[T] + 2>();
  if constexpr (T == SM.last + 1) asm volatile("v_add_f32_e32 v%c0, v%c1, v%c2" ::"i"(R_TMP), "i"(R_TS), "i"(R_TS + 1));
  if constexpr (T == SM.last + 2) asm volatile("v_cmp_nle_f32_e64 s[54:55], v%c0, v%c1" ::"i"(R_TMP), "i"(R_THR));
}
template <int X, int MODE, int G> DI void gapA(AC& c) {
  constexpr bool FIRST = MODE == 1 || MODE == 2, NOQK = MODE == 3;
  constexpr int Xp = X ^ 1, kb = G >> 4, g = G & 15, qb = g >> 3, s = g & 7;
  if constexpr (MODE == 3 && G < 48 && G % 3 == 0) {
    constexpr int f = G / 3;
    asm volatile("buffer_load_dwordx4 a[%c0:%c1], v84, s[88:91], s75 offen offset:%2 sc1" ::"i"(128 + 4 * f), "i"(131 + 4 * f), "i"((f & 3) * 1024) : "memory");
    if constexpr ((f & 3) == 3) asm volatile("s_addk_i32 s75, %0" ::"i"(f == 7 ? 28672 : 4096));
  }
  if constexpr (G == 48) asm volatile("s_waitcnt vmcnt(%0) lgkmcnt(0)\n\ts_barrier" ::"i"(MODE == 3 ? 16 : MODE == 2 ? 12 : MODE == 1 ? 7 : 0) : "memory");
  else if constexpr (g == 0 && !NOQK) asm volatile("s_waitcnt lgkmcnt(0)");
  if constexpr (!NOQK) {
    constexpr int r = 192 + 4 * ((kb & 1) * 8 + s), q = 128 + 4 * g;
    constexpr int d = R_S + 32 * X + 4 * (kb * 2 + qb), ci = s == 0 ? R_CI + 4 * (kb * 2 + qb) : d;
    if constexpr (FIRST && s == 0)
      asm volatile("v_mfma_f32_16x16x32_bf16 v[%c0:%c1], a[%c2:%c3], a[%c4:%c5], 0" ::"i"(d), "i"(d + 3), "i"(r), "i"(r + 3), "i"(q), "i"(q + 3));
    else
      asm volatile("v_mfma_f32_16x16x32_bf16 v[%c0:%c1], a[%c2:%c3], a[%c4:%c5], v[%c6:%c7]" ::"i"(d), "i"(d + 3), "i"(r), "i"(r + 3), "i"(q), "i"(q + 3),
                   "i"(ci), "i"(ci + 3));
    if constexpr (g < 8 && kb < 3) kread<X, (kb + 1) * 8 + g>();
    constexpr int S0 = MODE == 1 ? 0 : SMV ? 40 : 14, S1 = MODE == 1 ? 1 : SMV ? 41 : 30, S2 = SMV ? 42 : 45, S3 = SMV ? 43 : 46;
    if constexpr (G == S0) asm volatile("s_add_u32 s96, s97, 0x1000000");
    if constexpr (G == S1) dma_m0<X, 0>();
    if constexpr (G == S2) asm volatile("s_mov_b32 s97, s98");
    if constexpr (G == S3) asm volatile("s_add_u32 s98, s98, s99");
    if constexpr (MODE != 1 && G >= 56 && !(G & 1)) dma_at<(G - 56) / 2>();
    if constexpr (MODE == 1 && (G & 11) == 9) dma_at<(G >> 4) * 2 + ((G >> 2) & 1)>();
    if constexpr (MODE == 1 && G == 31) dma_m0<X, 4>();
    if constexpr (G == 57) asm volatile("s_lshr_b32 s100, s100, 1\n\ts_add_u32 s56, s56, 1");
    if constexpr (G == 59) asm volatile("s_getpc_b64 s[86:87]\nev_pc%c0:\n\ts_add_u32 s86, s86, ev_ret%c0-ev_pc%c0\n\ts_addc_u32 s87, s87, 0" ::"i"(X * 4 + MODE));
    if constexpr (G == 61 && !FIRST) asm volatile("s_or_b32 %0, s54, s55\n\ts_bitcmp1_b32 s100, 0" : "=s"(c.rare));
    if constexpr (G == 61 && FIRST) asm volatile("s_bitcmp1_b32 s100, 0");
  } else if constexpr (SM.nop[64 + G] > 0) asm volatile("s_nop 0");
  if constexpr (!FIRST && g < 8 && kb == 3) vread<Xp, g>();
  if constexpr (MODE == 2 && (G & 3) == 0) d_store<16 + G / 4>();
  if constexpr (FIRST) acc_zero2<G>();
  else sm_gap<Xp, 64 + G>(c);
  if constexpr (G == 63 && !NOQK) asm volatile("s_cbranch_scc1 ev_entry\nev_ret%c0:" ::"i"(X * 4 + MODE) : "vcc", "memory");
  GAP();
}
template <int R> DI void rescale1(float alpha) {
  float t;
  asm volatile("v_accvgpr_read_b32 %0, a[%c2]\n\tv_mul_f32 %0, %0, %1\n\tv_accvgpr_write_b32 a[%c2], %0" : "=&v"(t) : "v"(alpha), "i"(R));
}
template <int... Rs> DI void rescale_all(const float (&alpha)[2], iseq<Rs...>) { (rescale1<Rs>(alpha[(Rs >> 2) & 1]), ...); }
template <bool MAX> DI float row_reduce(float v) {
  auto p = __builtin_amdgcn_permlane32_swap(__float_as_uint(v), __float_as_uint(v), false, false);
  v = MAX ? fmaxf(__uint_as_float(p[0]), __uint_as_float(p[1])) : __uint_as_float(p[0]) + __uint_as_float(p[1]);
  p = __builtin_amdgcn_permlane16_swap(__float_as_uint(v), __float_as_uint(v), false, false);
  return MAX ? fmaxf(__uint_as_float(p[0]), __uint_as_float(p[1])) : __uint_as_float(p[0]) + __uint_as_float(p[1]);
}
template <int Y, int QB> DI float row_max() {
  constexpr int r = R_S + 32 * Y + 4 * QB;
  float mx;
  asm volatile("v_max_f32 %0, v%c1, v%c2\n\tv_max3_f32 %0, %0, v%c3, v%c4" : "=&v"(mx) : "i"(r), "i"(r + 1), "i"(r + 2), "i"(r + 3));
  static_for([&](auto K) {
    constexpr int q = r + 8 * (K + 1);
    float t = mx;
    asm volatile("v_max3_f32 %0, %0, v%c1, v%c2\n\tv_max3_f32 %0, %0, v%c3, v%c4" : "+v"(t) : "i"(q), "i"(q + 1), "i"(q + 2), "i"(q + 3));
    mx = t;
  }, make_iseq<3>{});
  return row_reduce<true>(mx);
}
template <int R> DI void v_sub(float d) { asm volatile("v_sub_f32 v%c0, v%c0, %1" ::"i"(R), "v"(d)); }
template <int B> DI void s_shift(const float (&d)[2]) { static_for([&](auto E) { v_sub<B + E>(d[(E >> 2) & 1]); }, make_iseq<32>{}); }
template <bool CI> DI void m_shift(const float (&d)[2]) {
  if constexpr (CI) s_shift<R_CI>(d);
  v_sub<R_M>(-d[0]); v_sub<R_M + 1>(-d[1]);
}
DI void set_mask0() {
  const int mr = __float_as_int(vget<89>());
  static_for([&](auto I) {
    constexpr int e = (I >> 2) * 8 + (I & 3), k = (I >> 2) * 16 + (I & 3);
    const float x = vget<R_S + e>();
    vset<R_S + e>(k > mr ? -INFINITY : x);
  }, make_iseq<16>{});
}
template <int X, bool CI = true> DI void sm_redo() {
  constexpr int Xp = X ^ 1;
  float d[2], dt[2], alpha[2], ts[2] = {0.f, 0.f};
  static_for([&](auto Q) {
    constexpr int qb = Q;
    const float pd = vget<R_PD + qb>();
    d[qb] = fmaxf(row_max<Xp, qb>() - pd, 0.f);
    dt[qb] = d[qb] + pd;
    vset<R_PD + qb>(d[qb]);
    alpha[qb] = __builtin_amdgcn_exp2f(-d[qb]);
    vset<R_RS + qb>(vget<R_RS + qb>() * alpha[qb]);
  }, make_iseq<2>{});
  vset<R_THR>(d[0] > 0.f || d[1] > 0.f ? -1.f : 4096.f);
  m_shift<CI>(d);
  static_for([&](auto K) {
    constexpr int k = K, qb = (k >> 1) & 1;
    const float e0 = __builtin_amdgcn_exp2f(vget<R_S + 32 * Xp + 2 * k>() - dt[qb]), e1 = __builtin_amdgcn_exp2f(vget<R_S + 32 * Xp + 2 * k + 1>() - dt[qb]);
    ts[qb] += e0; ts[qb] += e1;
    vset<p_reg(Xp, k)>(__uint_as_float(pk2(e0, e1)));
  }, make_iseq<16>{});
  vset<R_TS>(ts[0]); vset<R_TS + 1>(ts[1]);
  rescale_all(alpha, make_iseq<128>{});
  asm volatile("s_nop 1" ::: "memory");
  GAP();
}
template <int X, int MODE, int... Gs> DI void phaseA_(AC& c, iseq<Gs...>) { (gapA<X, MODE, Gs>(c), ...); }
template <int X, int MODE> DI void phaseA(AC& c) { phaseA_<X, MODE>(c, make_iseq<64>{}); }

template <int X, bool SMX, int G> DI void gapB(AC& c) {
  constexpr int Xp = X ^ 1, blk = G >> 4, g = G & 15;
  if constexpr (g == 0) asm volatile("s_waitcnt lgkmcnt(0)");
  constexpr int qb = (g >> 1) & 1, s = g & 1, r = 192 + 4 * ((blk & 1) * 8 + (g >> 2) * 2 + s), o = 4 * ((blk * 4 + (g >> 2)) * 2 + qb);
  constexpr int p = R_P + 16 * Xp + 4 * (s * 2 + qb);
  asm volatile("v_mfma_f32_16x16x32_bf16 a[%c0:%c1], a[%c2:%c3], v[%c4:%c5], a[%c0:%c1]" ::"i"(o), "i"(o + 3), "i"(r), "i"(r + 3), "i"(p), "i"(p + 3));
  if constexpr (g < 8) {
    if constexpr (blk < 3) vread<Xp, (blk + 1) * 8 + g>();
    else if constexpr (SMX) kread<Xp, g>();
  }
  if constexpr (SMX) {
    if constexpr (G == 1 || G == 2) asm volatile("v_add_f32_e32 v%c0, v%c0, v%c1" ::"i"(R_RS + G - 1), "i"(R_TS + G - 1));
    if constexpr (g == 7 && blk < 3) dma_m0<X, blk * 4 + 4>();
    if constexpr (g >= 8 && !(g & 1) && blk < 3) dma_at<blk * 4 + 4 + (g - 8) / 2>();
    if constexpr (G == 55) asm volatile("s_cmp_eq_u32 s56, s76");
    if constexpr (G == 3) asm volatile("s_lshr_b32 s77, s77, 1");
    if constexpr (G == 4) asm volatile("s_bitcmp1_b32 s77, 0");
    if constexpr (G == 5) asm volatile("s_cselect_b64 s[52:53], s[62:63], 0");
    if constexpr (G == 6) asm volatile(FL_ARM("s[52:53]"));
    if constexpr (G == 56) asm volatile(FL_POLL ::: "memory");
    sm_gap<X, G>(c);
  }
  GAP();
}
template <int X, int... Gs> DI void phaseB_(AC& c, iseq<Gs...>) { (gapB<X, true, Gs>(c), ...); }
template <int X> DI void phaseB(AC& c) { phaseB_<X>(c, make_iseq<64>{}); }
constexpr int sm_nex() { int n = 0; for (int t = 0; t < 64; ++t) n += SM.ex[t] >= 0; return n; }
template <int X> DI void phaseB0(unsigned mode, bool vh) {
  dma_m0<X, 4>();
  kreads<X ^ 1>(make_iseq<8>{});
  if (vh) { dma_at<4>(); dma_at<5>(); dma_at<6>(); dma_at<7>(); }
  dma_m0<X, 8>();
  asm volatile("s_nop 3");
  float d[2];
  if (mode & 2) {
    d[0] = vget<122>(); d[1] = vget<123>();
    dma_at<8>();
  } else {
    if constexpr (X == 0) if (__builtin_expect(mode & 4, 0)) set_mask0();
    d[0] = row_max<X, 0>(); GAP(); dma_at<8>();
    d[1] = row_max<X, 1>();
#pragma unroll
    for (int qb = 0; qb < 2; ++qb) if (!(d[qb] > -3.0e38f)) d[qb] = 0.f;
  }
  GAP();
  vset<R_M>(d[0]); vset<R_M + 1>(d[1]);
  dma_at<9>();
  const f32x2c nd[2] = {{-d[0], -d[0]}, {-d[1], -d[1]}};
  static_for([&](auto E) {
    constexpr int e = E;
    const f32x2c v = nd[(e >> 1) & 1];
    asm volatile("v_pk_add_f32 v[%c0:%c1], v[%c0:%c1], %4\n\tv_mov_b64 v[%c2:%c3], %4" ::"i"(R_S + 32 * X + 2 * e), "i"(R_S + 32 * X + 2 * e + 1), "i"(R_CI + 2 * e), "i"(R_CI + 2 * e + 1), "v"(v));
    if constexpr (e == 7 || e == 15) dma_at<10 + e / 8>();
  }, make_iseq<16>{});
  static_assert(SM.op0[64] >= 3 * ((sm_nex() - 15) / 2) && SM.op0[64] <= 24);
  static_for([&](auto E) {
    constexpr int e = E;
    if constexpr (e < sm_nex()) asm volatile("v_exp_f32 v%c0, v%c1" ::"i"(R_E + e), "i"(R_S + 32 * X + e));
    if constexpr (e == 2) dma_m0<X, 12>();
    if constexpr (e == 5 || e == 10 || e == 15) dma_at<11 + e / 5>();
  }, make_iseq<16>{});
  static_for([&](auto O) { sm_op<X, O>(); }, make_iseq<SM.op0[64]>{});
  dma_at<15>();
  static_for([&](auto E) { asm volatile("v_exp_f32 v%c0, v%c1" ::"i"(R_E + (int)E), "i"(R_S + 32 * X + 16 + (int)E)); }, make_iseq<(sm_nex() > 16 ? sm_nex() - 16 : 0)>{});
  asm volatile("buffer_store_dwordx2 v[%c0:%c1], v88, s[88:91], s76 offen sc1\n\ts_bitcmp1_b32 s77, 0\n\ts_cselect_b64 s[52:53], s[62:63], 0\n\t"
               FL_ARM("s[52:53]") "\n\t" FL_POLL ::"i"(R_M), "i"(R_M + 1) : "memory", "scc");
  GAP();
}
enum : int { T_Q0, T_K0, T_OQ, T_OG, T_FPREP, T_FQ, T_FG, T_FS, T_ZO, T_CNT, T_NPRE = 10, T_NST = 12, T_TAB = 14, T_SLAB = 16, T_PART, T_IT = 18, T_ITN = 23 };
#define MSK(R, K) "v_cmp_gt_i32 s[48:49], " #K ", v89\n\tv_cmp_gt_i32 s[50:51], " #K "+1, v89\n\tv_cmp_gt_i32 s[52:53], " #K "+2, v89\n\t" \
                  "v_cmp_gt_i32 s[54:55], " #K "+3, v89\n\tv_cndmask_b32_e64 v[" #R "], v[" #R "], v122, s[48:49]\n\t" \
                  "v_cndmask_b32_e64 v[" #R "+1], v[" #R "+1], v122, s[50:51]\n\tv_cndmask_b32_e64 v[" #R "+2], v[" #R "+2], v122, s[52:53]\n\t" \
                  "v_cndmask_b32_e64 v[" #R "+3], v[" #R "+3], v122, s[54:55]\n\t"
DI void ev_code() {
  asm volatile(
    "s_getpc_b64 s[82:83]\n\ts_add_u32 s82, s82, 12\n\ts_addc_u32 s83, s83, 0\n\ts_branch 9f\nev_entry:\n\t"
    "s_bitcmp1_b32 s60, s56\n\ts_cbranch_scc1 3f\n\ts_bitcmp1_b32 s74, s56\n\ts_cbranch_scc1 2f\n\t"
    "s_bitcmp1_b32 s58, s56\n\ts_cselect_b64 vcc, -1, 0\n\tv_cndmask_b32 v121, -2.0, v81, vcc\n\t"
    "buffer_store_dwordx2 v[82:83], v121, s[88:91], s70 offen sc1\n\tbuffer_store_dwordx2 v[82:83], v121, s[88:91], s71 offen sc1\n\t"
    "v_cmp_eq_u64 vcc, s[84:85], v[124:125]\n\ts_andn2_b64 vcc, s[62:63], vcc\n\ts_cbranch_scc1 5f\n"
    "0:\n\ts_add_u32 s98, s98, s59\n\ts_mov_b32 s59, s61\n\ts_mov_b32 s61, s72\n\ts_mov_b64 s[62:63], s[64:65]\n\ts_mov_b64 s[64:65], s[66:67]\n\t"
    "s_mov_b64 s[66:67], s[68:69]\n"
    "1:\n\ts_setpc_b64 s[86:87]\n"
    "5:\n\t" FL_CHECK("s[62:63]") "\n\ts_branch 0b\n"
    "6:\n\t" FL_CHECK("s[62:63]") "\n\ts_setpc_b64 s[86:87]\n"
    "2:\n\ts_mov_b32 s98, 0\n\t"
    "v_lshrrev_b32 v90, 7, v86\n\tv_and_b32 v91, 6, v90\n\tv_lshlrev_b32 v91, 4, v91\n\tv_and_b32 v92, 0x70, v86\n\tv_xor_b32 v91, v91, v92\n\t"
    "s_movk_i32 s48, 896\n\tv_mad_u32_u24 v94, v90, s48, v91\n\ts_mov_b64 s[92:93], s[80:81]\n\ts_mov_b32 s94, 229376\n\t"
    "v_add_u32 v90, 28672, v94\n\tv_add_u32 v91, 34816, v94\n\tv_add_u32 v92, 40960, v94\n\tv_add_u32 v93, 47104, v94\n\t"
    "v_add_u32 v95, 6144, v94\n\tv_add_u32 v126, 12288, v94\n\tv_add_u32 v127, 18432, v94\n\t"
    "v_mov_b32 v122, 0xff800000\n\t" MSK(228, 0) MSK(236, 16) MSK(244, 32) MSK(252, 48)
    "v_cmp_eq_u64 vcc, s[84:85], v[124:125]\n\ts_andn2_b64 vcc, s[62:63], vcc\n\ts_cbranch_scc1 6b\n\ts_setpc_b64 s[86:87]\n"
    "3:\n\tv_mov_b32 v122, 0xff800000\n\t" MSK(224, 0) MSK(232, 16) MSK(240, 32) MSK(248, 48)
    "v_readlane_b32 s54, v78, %0\n\ts_add_u32 m0, s78, 0x10000\n\t"
    "s_bfe_u32 s53, s73, 0x80010\n\ts_mulk_i32 s53, 3840\n\ts_mov_b64 s[48:49], s[80:81]\n\ts_add_u32 s50, s53, 3840\n\ts_min_u32 s50, s50, 229376\n\t"
    "s_mov_b32 s51, 0x20000\n\t"
    "v_lshlrev_b32 v121, 3, v84\n\tbuffer_load_dword v121, s[48:51], s53 offen lds\n\t"
    "s_bitcmp0_b32 s73, 3\n\ts_cbranch_scc1 4f\n\t"
    "buffer_load_dwordx4 v84, s[88:91], s54 offen sc1 lds\n\tbuffer_load_dwordx4 v84, s[88:91], s54 offen offset:1024 sc1 lds\n\t"
    "buffer_load_dwordx4 v84, s[88:91], s54 offen offset:2048 sc1 lds\n\tbuffer_load_dwordx4 v84, s[88:91], s54 offen offset:3072 sc1 lds\n"
    "4:\n\ts_setpc_b64 s[86:87]\n9:"
    ::"i"((int)T_ZO) : "scc");
}
DI void ev_call() { asm volatile("s_swappc_b64 s[86:87], s[82:83]" ::: "vcc", "scc", "memory"); GAP(); }

#define ACLOB_ALL "a0","a1","a2","a3","a4","a5","a6","a7","a8","a9","a10","a11","a12","a13","a14","a15","a16","a17","a18","a19","a20","a21","a22","a23","a24","a25","a26","a27","a28","a29","a30","a31","a32","a33","a34","a35","a36","a37","a38","a39","a40","a41","a42","a43","a44","a45","a46","a47","a48","a49","a50","a51","a52","a53","a54","a55","a56","a57","a58","a59","a60","a61","a62","a63","a64","a65","a66","a67","a68","a69","a70","a71","a72","a73","a74","a75","a76","a77","a78","a79","a80","a81","a82","a83","a84","a85","a86","a87","a88","a89","a90","a91","a92","a93","a94","a95","a96","a97","a98","a99","a100","a101","a102","a103","a104","a105","a106","a107","a108","a109","a110","a111","a112","a113","a114","a115","a116","a117","a118","a119","a120","a121","a122","a123","a124","a125","a126","a127","a128","a129","a130","a131","a132","a133","a134","a135","a136","a137","a138","a139","a140","a141","a142","a143","a144","a145","a146","a147","a148","a149","a150","a151","a152","a153","a154","a155","a156","a157","a158","a159","a160","a161","a162","a163","a164","a165","a166","a167","a168","a169","a170","a171","a172","a173","a174","a175","a176","a177","a178","a179","a180","a181","a182","a183","a184","a185","a186","a187","a188","a189","a190","a191","a192","a193","a194","a195","a196","a197","a198","a199","a200","a201","a202","a203","a204","a205","a206","a207","a208","a209","a210","a211","a212","a213","a214","a215","a216","a217","a218","a219","a220","a221","a222","a223","a224","a225","a226","a227","a228","a229","a230","a231","a232","a233","a234","a235","a236","a237","a238","a239","a240","a241","a242","a243","a244","a245","a246","a247","a248","a249","a250","a251","a252","a253","a254","a255"

struct EP { float fa[2], fb[2]; };
template <int I, int E, int MODE> DI void epi_pair(const EP& p) {
  constexpr int qb = I & 1, r = 16 * (I >> 1) + 4 * qb + (E >> 1) * 8 + (E & 1) * 2, t = 96 + 2 * E, w = 128 + 4 * I + E;
  if constexpr (MODE == 2) asm volatile("v_accvgpr_read_b32 v106, a[%c0]" ::"i"(w));
  asm volatile("v_accvgpr_read_b32 v%c0, a[%c2]\n\tv_accvgpr_read_b32 v%c1, a[%c3]" ::"i"(t), "i"(t + 1), "i"(r), "i"(r + 1));
  if constexpr (MODE) asm volatile("v_mul_f32 v%c0, v%c0, %2\n\tv_mul_f32 v%c1, v%c1, %2" ::"i"(t), "i"(t + 1), "v"(p.fa[qb]));
  if constexpr (MODE == 2)
    asm volatile("v_lshlrev_b32 v104, 16, v106\n\tv_and_b32 v105, 0xffff0000, v106\n\tv_fmac_f32 v%c0, v104, %2\n\tv_fmac_f32 v%c1, v105, %2"
                 ::"i"(t), "i"(t + 1), "v"(p.fb[qb]));
  asm volatile("v_cvt_pk_bf16_f32 v%c0, v%c1, v%c2" ::"i"(w), "i"(t), "i"(t + 1));
}
template <int I> DI void epi_store() {
  asm volatile("buffer_store_dwordx4 v[%c0:%c1], v84, s[88:91], s101 offen offset:%2 sc1" ::"i"(128 + 4 * I), "i"(131 + 4 * I), "i"((I & 3) * 1024) : "memory");
  if constexpr ((I & 3) == 3) asm volatile("s_addk_i32 s101, 4096");
}
constexpr int R_TB = 232, R_VS = 236;
template <int HF, int R> DI void p_read() {
  constexpr int mb = R >> 4, r = 192 - 64 * HF + 2 * R;
  asm volatile("ds_read_b64_tr_b16 a[%c1:%c2], v%c0 offset:%c3" ::"i"(R_TB + 2 * HF + mb), "i"(r), "i"(r + 1), "i"((R & 15) * 2048));
}
template <int QB, int E> DI void p_cvt() {
  asm volatile("v_cvt_pk_bf16_f32 v%c0, v%c1, v%c2" ::"i"(208 + 4 * QB + E), "i"(192 + 8 * QB + 2 * E), "i"(193 + 8 * QB + 2 * E));
}
template <int QB, int PR> DI void p_store() {
  asm volatile("buffer_store_dwordx4 v[%c0:%c1], v%c2, s[88:91], s96 offen offset:%3 sc1" ::"i"(208 + 4 * QB), "i"(211 + 4 * QB), "i"(R_VS + QB),
               "i"(PR * 128) : "memory");
}
constexpr int p_dma(int HF, int G) { return G == 3 && !HF ? 0 : G == 11 ? 1 - HF : G == 15 ? 2 - HF : G == 19 ? 3 - HF : G == 27 && HF ? 3 : -1; }
template <int HF, bool LD, int G> DI void p_gap() {
  constexpr int ch = G >> 3, t = G & 7, a = 192 - 64 * HF + 32 * (ch & 1) + 4 * t, b = 128 + 4 * (2 * t + (ch >> 1)), d = 192 + 4 * ch;
  if constexpr (t == 0)
    asm volatile("v_mfma_f32_16x16x32_bf16 v[%c0:%c1], a[%c2:%c3], v[%c4:%c5], 0" ::"i"(d), "i"(d + 3), "i"(a), "i"(a + 3), "i"(b), "i"(b + 3));
  else
    asm volatile("v_mfma_f32_16x16x32_bf16 v[%c0:%c1], a[%c2:%c3], v[%c4:%c5], v[%c0:%c1]" ::"i"(d), "i"(d + 3), "i"(a), "i"(a + 3), "i"(b), "i"(b + 3));
  p_read<HF ^ 1, G>();
  if constexpr (G == 0) asm volatile("v_xor_b32 v%c0, s100, v%c0" ::"i"(R_TB + 2 * HF + 1));
  if constexpr (G == 17) asm volatile("v_xor_b32 v%c0, s100, v%c0" ::"i"(R_TB + 2 * (HF ^ 1)));
  if constexpr (G >= 4 && G <= 8 && G != 7) p_cvt<1, (G < 7 ? G - 4 : 3)>();
  if constexpr (G == 24 || (G == 7 && HF == 0)) asm volatile("s_waitcnt lgkmcnt(8)");
  if constexpr (G == 7 && HF == 1) asm volatile("s_waitcnt vmcnt(%0) lgkmcnt(8)\n\ts_barrier" ::"i"(LD ? 13 : 5) : "memory");
  if constexpr (G == 10) p_store<1, HF ^ 1>();
  if constexpr (G >= 20 && G < 24) p_cvt<0, G - 20>();
  if constexpr (G == 25) p_store<0, HF>();
  if constexpr (LD && p_dma(HF, G) >= 0) dma_at<(HF ? 8 : 12) + p_dma(HF, G)>();
  if constexpr (LD && HF == 0 && G == 2) asm volatile("s_bitset0_b32 m0, 12");
  if constexpr (HF == 0 && G == 13) asm volatile("s_add_u32 s96, s96, 0x8000");
  if constexpr (HF == 0 && G == 26) asm volatile("s_add_u32 s98, s98, 128");
  if constexpr (LD && HF == 0 && G == 27) asm volatile("s_cmp_lt_u32 s98, 0x380");
  if constexpr (HF == 1 && G == 1) asm volatile("s_xor_b32 s100, s100, 0x10000");
  if constexpr (LD && HF == 1 && G == 2) asm volatile("s_xor_b32 s97, s97, s100");
  if constexpr (LD && HF == 1 && G == 9) asm volatile("s_or_b32 m0, s97, 0x1000");
  if constexpr (!LD && HF == 1 && G == 30) asm volatile("s_cmp_lt_u32 s98, 0x500");
  GAP();
}
template <int HF, bool LD, int... Gs> DI void p_half(iseq<Gs...>) { (p_gap<HF, LD, Gs>(), ...); }
template <int G> DI void gapF(AC& c, const EP& p) {
  if constexpr (G < 64) gapB<0, false, G>(c);
  if constexpr (G >= 10) epi_pair<(G - 10) / 4, (G - 10) & 3, 0>(p);
  if constexpr (G >= 14 && ((G - 14) & 3) == 0) epi_store<(G - 14) / 4>();
  if constexpr (G == 73) epi_store<15>();
  GAP();
}
template <int... Gs> DI void phaseF(AC& c, const EP& p, iseq<Gs...>) { (gapF<Gs>(c, p), ...); }
template <int G> DI void gapAL(AC& c) {
  constexpr int kb = G >> 3, s = G & 7, r = 192 + 4 * (G & 15), q = 160 + 4 * s, d = R_S + 36 + 8 * kb, ci = s == 0 ? R_CI + 4 + 8 * kb : d;
  if constexpr (G == 0) asm volatile("s_add_u32 s96, s97, 0x1000000\n\ts_mov_b32 m0, s79\n\ts_waitcnt vmcnt(9) lgkmcnt(0)\n\ts_barrier" ::: "memory");
  if constexpr (G >= 8 && (G & 3) == 0) asm volatile("s_waitcnt lgkmcnt(4)");
  if constexpr (G == 29) asm volatile("s_barrier" ::: "memory");
  asm volatile("v_mfma_f32_16x16x32_bf16 v[%c0:%c1], a[%c2:%c3], a[%c4:%c5], v[%c6:%c7]" ::"i"(d), "i"(d + 3), "i"(r), "i"(r + 3), "i"(q), "i"(q + 3),
               "i"(ci), "i"(ci + 3));
  if constexpr (G < 24) kread<1, G + 8>();
  else vread<0, G - 24>();
  if constexpr (G % 3 == 1 && G < 24) dma_at<G / 3>();
  if constexpr (G == 11) asm volatile("s_bitset1_b32 m0, 12");
  if constexpr (G == 28) asm volatile("buffer_load_dwordx4 v[122:125], v88, s[88:91], s76 offen sc1" ::: "memory");
  if constexpr (G == 30) asm volatile("s_xor_b32 m0, s79, 0x11000\n\ts_movk_i32 s98, 128");
  sm_gap<0, 64 + 2 * G>(c); sm_gap<0, 65 + 2 * G>(c);
  GAP();
}
template <int... Gs> DI void phaseAL(AC& c, iseq<Gs...>) { (gapAL<Gs>(c), ...); }
struct BLS { int ex[65], op[65], vm[65], su[65]; };
constexpr BLS bl_sched(bool LD) {
  BLS z{};
  z.op[0] = 1;
  for (int t = 0; t < 64; ++t) {
    const int g = t & 15, o = z.op[t];
    const bool x = t == 6 || (t < 48 && g > 8 && g != 12 && g != 15);
    const bool v = LD ? (t & 3) == 0 : (t & 11) == 8;
    const bool p = t >= 15 && o < 24 && (t & 3) != 0 && t != 47 && !x && z.ex[t - 1] > 2 * (o / 3) + (o % 3 != 0);
    z.ex[t + 1] = z.ex[t] + x; z.vm[t + 1] = z.vm[t] + v; z.op[t + 1] = o + p;
    z.su[t + 1] = z.su[t] + (t >= 1 && z.su[t] < (LD ? 16 : 10) && g != 15 && !v && !x && (LD || t > 55 || (t > 3 && !p)));
  }
  return z;
}
template <bool LD> constexpr BLS BL = bl_sched(LD);
static_assert(BL<true>.ex[64] == 16 && BL<true>.op[61] == 24 && BL<true>.vm[64] == 16 && BL<false>.vm[64] == 8);
static_assert(BL<true>.su[56] == 16 && BL<false>.su[62] == 10);
template <bool LD, int G> DI void gapBL(AC& c) {
  if constexpr (G == 48) asm volatile("s_waitcnt vmcnt(%0) lgkmcnt(0)\n\ts_barrier" ::"i"(BL<LD>.vm[48]) : "memory");
  gapB<1, false, G>(c);
  if constexpr (G >= 48 && G < 56) vread<1, G - 48>();
  if constexpr (BL<LD>.vm[G + 1] > BL<LD>.vm[G]) {
    constexpr int k = BL<LD>.vm[G] + (LD ? 0 : 16), I = 2 * (k & 7) + ((k >> 3) & 1);
    if constexpr (k < 16) {
      asm volatile("buffer_load_dwordx4 a[%c0:%c1], v84, s[88:91], s75 offen offset:%2 sc1" ::"i"(128 + 4 * I), "i"(131 + 4 * I), "i"((I & 3) * 1024) : "memory");
      if constexpr (k & 1) asm volatile("s_addk_i32 s75, %0" ::"i"(k == 7 ? -12288 : 4096));
    } else dma_at<k - 8>();
    if constexpr (k == 19) asm volatile("s_bitset0_b32 m0, 12");
  }
  if constexpr (G == 1 || G == 2) asm volatile("v_add_f32_e32 v%c0, v%c0, v%c1" ::"i"(R_RS + G - 1), "i"(R_TS + G - 1));
  if constexpr (G == 3) asm volatile("v_mov_b32 v%c0, 0" ::"i"(R_TS));
  if constexpr (BL<LD>.ex[G + 1] > BL<LD>.ex[G]) {
    constexpr int j = BL<LD>.ex[G];
    asm volatile("v_exp_f32 v%c0, v%c1" ::"i"(R_E + j), "i"(R_S + 36 + 8 * (j >> 2) + (j & 3)));
  }
  if constexpr (BL<LD>.op[G + 1] > BL<LD>.op[G]) {
    constexpr int o = BL<LD>.op[G], k = o / 3, j = 2 * k + o % 3;
    if constexpr (o % 3 == 2) asm volatile("v_cvt_pk_bf16_f32 v%c0, v%c1, v%c2" ::"i"(p_reg(1, (k >> 1) * 4 + 2 + (k & 1))), "i"(R_E + 2 * k), "i"(R_E + 2 * k + 1));
    else asm volatile("v_add_f32_e32 v%c0, v%c1, v%c2" ::"i"(R_TS + 1), "i"(j == 1 ? R_E : R_TS + 1), "i"(R_E + j));
  }
  if constexpr (G == 61) asm volatile("v_cmp_nle_f32_e64 s[54:55], v%c0, v%c1" ::"i"(R_TS + 1), "i"(R_THR));
  if constexpr (BL<LD>.su[G + 1] > BL<LD>.su[G]) {
    constexpr int k = BL<LD>.su[G];
    if constexpr (k < 4) asm volatile("v_xor_b32 v%c0, %1, %2" ::"i"(R_TB + k), "i"(32 * k), "v"(c.tb));
    else if constexpr (k < 6) asm volatile("v_add_u32 v%c0, %1, %2" ::"i"(R_VS + k - 4), "i"(4096 * (k - 4)), "v"(c.vs));
    else if constexpr (k == 6) tload<96, T_SLAB>();
    else if constexpr (k == 7) tload<101, T_CNT>();
    else if constexpr (k == 8) asm volatile("v_mov_b64 v[240:241], 1");
    else if constexpr (k == 9) asm volatile("s_mov_b32 s97, s79\n\ts_mov_b32 s100, 0x8000");
    else if constexpr (k < 14) asm volatile("v_mov_b32 v%c0, %1" ::"i"(k < 12 ? 214 + k : 218 + k), "v"(k & 1 ? c.o1 : c.o0));
    else asm volatile("v_mov_b64 v[%c0:%c1], 0" ::"i"(198 + 2 * k), "i"(199 + 2 * k));
  }
  GAP();
}
template <bool LD, int... Gs> DI void phaseBL(AC& c, iseq<Gs...>) { (gapBL<LD, Gs>(c), ...); }
template <int MODE, int G> DI void gapFL(const EP& p) {
  constexpr int x = G - (MODE == 3 ? 4 : 0), J = MODE == 1 ? (G < 24 && G % 3 == 1 ? G / 3 : -1) : G < 27 && G % 5 != 0 && G % 5 != 3 ? G / 5 * 3 + (G % 5) / 2 : -1;
  if constexpr (G == 0 && MODE == 1) asm volatile("s_or_b32 m0, s78, 0x1000\n\ts_movk_i32 s98, 256\n\ts_waitcnt lgkmcnt(0)");
  if constexpr (G == 0 && MODE > 1) asm volatile("s_waitcnt vmcnt(8) lgkmcnt(0)" ::: "memory");
  if constexpr (G == 16 && MODE > 1) asm volatile("s_waitcnt vmcnt(9)" ::: "memory");
  if constexpr (G >= 8 && G < 32 && (G & 3) == 0) asm volatile("s_waitcnt lgkmcnt(%0)" ::"i"(G < 28 ? 4 : 0));
  if constexpr (G < 32) {
    constexpr int o = 8 * (G >> 1) + 4, r = 192 + 4 * (G & 15), q = R_P + 20 + 8 * (G & 1);
    asm volatile("v_mfma_f32_16x16x32_bf16 a[%c0:%c1], a[%c2:%c3], v[%c4:%c5], a[%c0:%c1]" ::"i"(o), "i"(o + 3), "i"(r), "i"(r + 3), "i"(q), "i"(q + 3));
  }
  if constexpr (G < 24) vread<1, G + 8>();
  if constexpr (G == 28) asm volatile("s_waitcnt vmcnt(8)\n\ts_barrier\n\ts_or_b32 m0, s79, 0x1000\n\ts_movk_i32 s98, 384" ::: "memory");
  if constexpr (G == 29 || G == 31) dma_at<8 + (G - 29) / 2>();
  if constexpr (G == 32 || G == 40) {
    static_for([&](auto I) { p_read<0, 2 * (G - 32) + I>(); }, make_iseq<16>{});
    dma_at<10 + (G - 32) / 8>();
  }
  if constexpr (J >= 0) dma_at<8 + (J & 7)>();
  if constexpr (x >= 0) epi_pair<2 * ((x >> 2) & 7) + (x >> 5), x & 3, MODE>(p);
  if constexpr (MODE == 3 && G < 32) {
    constexpr int db = G & 15, qb = G >> 4, o = 4 * (db * 2 + qb), a = 224 + 4 * (db & 1), b = 128 + 4 * ((db >> 1) * 2 + qb);
    asm volatile("v_mfma_f32_16x16x32_bf16 a[%c0:%c1], v[%c2:%c3], a[%c4:%c5], a[%c0:%c1]" ::"i"(o), "i"(o + 3), "i"(a), "i"(a + 3), "i"(b), "i"(b + 3));
  }
  if constexpr (J == 3 || J == 11) asm volatile("s_bitset0_b32 m0, 12");
  if constexpr (J == 7 && MODE > 1) asm volatile("s_or_b32 m0, s78, 0x1000\n\ts_movk_i32 s98, 256");
  if constexpr (x == 63) asm volatile("s_waitcnt vmcnt(4) lgkmcnt(0)" ::: "memory");
  GAP();
}
template <int MODE, int... Gs> DI void phaseFL_(const EP& p, iseq<Gs...>) { (gapFL<MODE, Gs>(p), ...); }
template <int MODE> DI void phaseFL(const EP& p) { GAP(); phaseFL_<MODE>(p, make_iseq<MODE == 3 ? 68 : 64>{}); }
template <int U, int K> DI void r_load(unsigned vo, unsigned so) {
  asm volatile("buffer_load_dwordx4 a[%c0:%c1], %2, s[88:91], %3 offen offset:%4 sc1" ::"i"(64 * U + 4 * K), "i"(64 * U + 4 * K + 3), "v"(vo),
               "s"(so + (K >> 1) * 0x40000 + (U >> 1) * 4096), "i"((K & 1) * 512 + (U & 1) * 2048) : "memory");
}
template <int U, int K> DI void r_sum() {
  constexpr int d = 100 + 4 * (K & 1), b = 64 * U + 4 * K;
  if constexpr (K < 2) asm volatile("v_mfma_f32_16x16x32_bf16 v[%c0:%c1], v[96:99], a[%c2:%c3], 0" ::"i"(d), "i"(d + 3), "i"(b), "i"(b + 3));
  else asm volatile("v_mfma_f32_16x16x32_bf16 v[%c0:%c1], v[96:99], a[%c2:%c3], v[%c0:%c1]" ::"i"(d), "i"(d + 3), "i"(b), "i"(b + 3));
}
template <int U> DI void r_fin(unsigned vo, __amdgpu_buffer_rsrc_t srd, unsigned so) {
  asm volatile("" : "+s"(srd), "+s"(so));
  static_for([&](auto K) { r_sum<U, K>(); }, make_iseq<16>{});
  asm volatile("s_nop 7");
  static_for([&](auto E) { asm volatile("v_cvt_pk_bf16_f32 v%c0, v%c1, v%c2" ::"i"(116 + 4 * U + E), "i"(100 + 2 * E), "i"(101 + 2 * E)); }, make_iseq<4>{});
  asm volatile("buffer_store_dwordx4 v[%c3:%c4], %0, %1, %2 offen sc0 sc1" ::"v"(vo), "s"(srd), "s"(so), "i"(116 + 4 * U), "i"(119 + 4 * U) : "memory");
}

#define CMAX 12
#define BAL 0
#define NYE (BAL ? 16 : 15)
#define NYO (BAL ? 14 : 15)
#define NY0 15
struct TRow { unsigned v[64]; };
constexpr u64 kvmask(int m0, int m1) { u64 n = 0; for (int m = m0; m <= m1; ++m) n |= 0x101ull << m; return n; }
constexpr u64 k_order(bool isY, int rr) {
  const int cls = (rr + (rr >> 2)) & 3;
  return !isY ? (cls == 0 ? 0xfedcb9875a463210ull : cls == 1 ? 0xefadc6982b574310ull : cls == 2 ? 0xfebad7693c285410ull : 0xefcba8764d392510ull)
              : (cls == 0 ? 0xfedcb987543a6210ull : cls == 1 ? 0xefadc698254b7310ull : cls == 2 ? 0xfebad769325c8410ull : 0xefcba876432d9510ull);
}
constexpr TRow t_row(int b, int wid) {
  TRow t{};
  const int j = b & 7, r = b >> 3, rr = r & 15, hsel = rr & 1, pj = rr >> 1, kv = pj & 1, h = j + 8 * hsel, u = hsel * 8 + j;
  const bool isY = r >= 16, noA = pj == 7, slow = BAL && (j & 1);
  const int pa = slow && (pj == 0 || pj == 7) ? 7 - pj : pj;
  const bool solo = pa == 7;
  const int jF = isY || !BAL ? j : slow && pa == 0 ? j - 1 : !slow && pa == 6 ? j + 1 : j, hF = jF + 8 * hsel;
  const int qbF = isY ? (solo ? (slow ? 6 : 7) : 15 - pa) : solo ? 8 : !BAL ? pa : slow ? (pa ? pa - 1 : 0) : pa + 1;
  const int mt = (rr >> 2) + (isY ? 4 : 0), g = j * 128, gF = jF * 128, NY = !(j & 1) ? NYE : pa ? NYO : NY0;
  const int nx0 = solo ? 0 : 32 - 2 * pa - NY, cx = nx0 < CMAX ? nx0 : CMAX, ex = nx0 - cx, qb0 = solo ? qbF : 15 - pa;
  const unsigned oQ = (unsigned)h << 20, oK = oQ + O_K, oP = O_P + (h * 8 + pa) * PSTR, oQF = (unsigned)hF << 20;
  if (wid == 4) {
    for (int l = 0; l < 64; ++l)
      t.v[l] = fo(l < 16 ? g + hsel * 16 + l : l == 16 ? g + 32 + hsel * 8 + (qb0 >> 1) : l == 17 ? gF + 32 + hsel * 8 + (qbF >> 1) :
                  l < 26 ? gF + hsel * 16 + ((l - 18) >> 2) * 8 + ((l - 18) & 3) : l == 26 ? g + 48 + hsel * 8 + pa : l < 32 ? g + 64 + hsel * 8 + pa :
                  l < 48 ? 2304 + pj * 16 + l - 32 : 2048 + pj * 16 + l - 48);
    return t;
  }
  auto put64 = [&](int i, u64 x) { t.v[i] = (unsigned)x; t.v[i + 1] = (unsigned)(x >> 32); };
  t.v[T_Q0] = oQ + (qb0 * 128 + wid * 16) * 512;
  t.v[T_K0] = oK + (isY ? cx : 0) * 32768;
  t.v[T_OQ] = oQ + pj * 131072 + (wid >> 1) * 65536 + (wid & 1) * 4096;
  t.v[T_OG] = oK + kv * TSB + mt * 131072 + (wid >> 1) * 65536 + (wid & 1) * (kv ? 8192 : 4096);
  t.v[T_FPREP] = fo(2048 + pj * 16 + u);
  t.v[T_FQ] = fo(g + 32 + hsel * 8 + pj);
  t.v[T_FG] = fo(g + hsel * 16 + kv * 8 + mt);
  t.v[T_FS] = fo(1024 + qbF * 16 + jF * 2 + hsel);
  t.v[T_CNT] = fo(2560 + qbF * 16);
  t.v[T_ZO] = O_Z + pj * 64 * SEQ * 2 + (u * 4 + wid) * 4096;
  const u64 own = 0x1ull << (kv * 8 + mt), conv = noA ? 0 : 0xffffull << 32;
  put64(T_NPRE, 0x10000ull | (isY ? (1ull << (cx >> 2)) | (1ull << ((cx + 1) >> 2)) : noA ? 0 : 0xffffull << 48));
  put64(T_NST, isY ? kvmask(cx >> 2, 3) | (solo ? 0 : 1ull << 27) : kvmask(0, 0));
  put64(T_TAB, k_order(isY, rr));
  t.v[T_SLAB] = O_S + qbF * 0x200000 + jF * 0x40000 + wid * 8192 + hsel * 1024;
  t.v[T_PART] = oP;
  const unsigned junk = fo(g + 96 + r), fconv = noA ? junk : fo(2304 + pj * 16 + u);
  for (int it = isY || solo; it < 2; ++it) {
    unsigned* p = t.v + T_IT + it * T_ITN;
    const int n = !it ? nx0 : isY && !solo ? NY : 2 * qbF + 2;
    u64 q[4] = {0, 0, 0, 0};
    unsigned jmp[3] = {0, 0, 0}, chk = 0;
    p[14] = junk;
    if (isY) {
      p[0] = 2; p[1] = 2; p[13] = t.v[T_FG];
      if (!solo) {
        chk = 1u << (14 - cx); p[0] |= chk; q[1] = kvmask(4, (31 - 2 * pa) >> 2) & ~own; jmp[1] = ex * 32768;
        chk |= 1u << (n - 2); q[2] = 1ull << 26;
      }
    } else if (it == 0) {
      chk = p[0] = 2u | (ex ? 1u << (cx - 2) : 0) | 1u << (n - 2);
      chk |= noA ? 0 : 1u << n;
      q[0] = kvmask(0, (cx - 1) >> 2);
      if (ex) { q[1] = kvmask(4, 4 + ((ex - 1) >> 2)); jmp[1] = (16 - cx) * 32768; }
      jmp[ex ? 2 : 1] = oQF - oQ - (ex ? 16 + ex : cx) * 32768;
      q[ex ? 2 : 1] = 0x20000ull | ((0x22ull << ((2 * qbF + 1) >> 2)) - 0x11ull) << 18;
      q[ex ? 3 : 2] = conv;
      p[1] = 2; p[13] = fconv; p[14] = fo(g + 64 + hsel * 8 + pa);
    } else if (solo) {
      chk = p[0] = 0x402; p[1] = 2; p[13] = fconv;
      q[0] = kvmask(0, 2); q[1] = kvmask(3, 4) | conv;
    } else {
      p[0] = p[1] = 1; p[13] = fo(g + 48 + hsel * 8 + pa);
      q[0] = conv;
    }
    p[2] = jmp[0]; p[4] = jmp[1]; p[15] = jmp[2];
    p[3] = !it ? 0 : n > 2 ? 1u << (n - 3) : 1u << 30;
    for (int k = 0; k < 4; ++k) { p[5 + 2 * k] = (unsigned)q[k]; p[6 + 2 * k] = (unsigned)(q[k] >> 32); }
    const int rk = (r >> 1) - (BAL && (r >> 1) > (slow ? 7 : 6));
    p[16] = 32 | (isY && !solo ? 2 : 0) | (n == 2 ? 4 : 0) | (isY || noA ? 0 : 8) | (n & 1) << 4 | (n - 1 - it) << 8 | (rk * 4 + wid) << 16 | hF << 24 | qbF << 28;
    p[17] = !it ? 0 : n > 2 ? 1u << (n - 2) : 1u << 31;
    p[18] = it ? oP + wid * 16384 : oQF + (qbF * 128 + wid * 16) * 512;
    p[19] = oP + (it ? 1032 : 1024);
    p[20] = chk >> 1;
    p[21] = (it ? oQF + O_K : oK) + (isY ? cx : 0) * 32768;
    p[22] = ((p[0] | p[3] | p[17]) & 0x3fffffffu) << 1;
  }
  return t;
}
struct TBlk { TRow row[5]; };
template <int B> constexpr TBlk t_blk = {{t_row(B, 0), t_row(B, 1), t_row(B, 2), t_row(B, 3), t_row(B, 4)}};
struct Tab { TBlk blk[256]; };
template <int... I> constexpr Tab t_make(iseq<I...>) { return Tab{{t_blk<I>...}}; }
__device__ const Tab g_tab = t_make(make_iseq<256>{});

template <int R> DI const bf16* sptr() { const bf16* p; asm volatile("s_mov_b64 %0, s[%c1:%c2]" : "=s"(p) : "i"(R), "i"(R + 1)); return p; }
template <class T, int I> DI T* arg_late() {
  unsigned lo, hi;
  asm volatile("v_readlane_b32 %0, v77, %2\n\tv_readlane_b32 %1, v77, %3\n\ts_nop 4" : "=s"(lo), "=s"(hi) : "i"(2 * I), "i"(2 * I + 1));
  return (T*)(((u64)hi << 32) | lo);
}
__global__ __launch_bounds__(256, 1) __attribute__((amdgpu_num_vgpr(77), amdgpu_num_sgpr(56))) void fused_kernel(
    const bf16* ckv_, const bf16* wuk_, const bf16* wuv_, const bf16* x_, const bf16* wq_, bf16* ws_, const bf16* ak_, const bf16* av_,
    const bf16* ash_, const bf16* assmk_, const bf16* assmD_, const bf16* wo_, const bf16* aq_, bf16* out_) {
  extern __shared__ __attribute__((aligned(16))) char lds[];
  const void* kap = (const void*)__builtin_amdgcn_kernarg_segment_ptr();
  asm volatile("at_code_begin:\n\ts_load_dwordx16 s[80:95], %0, 0\n\ts_load_dwordx2 s[96:97], %0, 0x40" ::"s"(kap) : ACLOB_ALL, "v255", "s101");
  asm volatile("s_getpc_b64 s[48:49]\nct_base:\n\ts_mov_b32 s50, pf_code_end-ct_base\n\ts_mov_b32 s51, 0x20000\n\ts_lshr_b32 s52, %1, 3\n\t"
               "v_and_b32 v116, 63, %0\n\tv_lshrrev_b32 v117, 6, %0\n\tv_lshl_add_u32 v116, v116, 7, v117\n\ts_lshl_b32 s52, s52, 9\n\t"
               "v_lshlrev_b32 v116, 7, v116\n\tbuffer_load_dword v117, v116, s[48:51], s52 offen" ::"v"(threadIdx.x), "s"(blockIdx.x) : "scc", "memory");
  GAP();
  const int tid = threadIdx.x, lane = tid & 63;
  const int wid = __builtin_amdgcn_readfirstlane(tid >> 6);
  const int b = blockIdx.x, j = b & 7, r = b >> 3, rr = r & 15, hsel = rr & 1, pj = rr >> 1, kv = pj & 1;
  const bool isY = r >= 16, doP = isY && pj != 7, doA = !isY && pj != 7;
  const int h = j + 8 * hsel, mt = (rr >> 2) + (isY ? 4 : 0), pu = (hsel * 8 + j) * 4 + wid;
  constexpr unsigned m0d = 131072 + 24576;
  const unsigned lA = isY ? 65536 : 0, lW = lA ^ 65536, po = isY ? 0 : 32768;
  unsigned mA = lA + wid * 1024 + po, mW = lW + wid * 1024 + po, ofA = isY ? pj * 256 * DA * 2 : mt * 256 * DC * 2, ofW = h * 512;
  const unsigned lda = isY ? DA : DC;
#define ROT 1
  unsigned g0 = (!ROT ? 0 : isY ? pj + 2 * hsel : rr + (rr >> 2)) & 3;
  {
    asm volatile("global_load_dword v78, %0, %2\n\tglobal_load_dword v79, %1, %2 offset:1024\n\tglobal_load_dword v77, %3, %4"
                 ::"v"(tid * 4), "v"(lane * 4), "s"(&g_tab.blk[b]), "v"((lane < 27 ? lane : 27) * 4), "s"(kap) : "memory");
    const unsigned ra = wid * 8 + (lane >> 3), sa = ((lane & 7) ^ (((wid & 1) * 4 + (lane >> 4)) & 7)) * 16;
    const unsigned vbA = ra * (lda * 2) + sa, vbB = (4 * wid + (lane >> 4)) * LDW * 2 + 16 * ((lane & 15) ^ (((lane >> 4) << 2) | wid));
    vsetu<92>(vbA); vsetu<93>(vbB); vsetu<94>(ra * (DC * 2) + sa);
    sset<64>(64 * lda); sset<67>(256 * lda); sset<68>(320 * lda);
    asm volatile("s_mov_b32 s71, 0x20000\n\ts_movk_i32 s72, 0x100\n\ts_mov_b32 s73, 0x20100");
    sset<58>(256 * lda * 2); sset<62>((lda - 1) * LDW * 2 + 512);
    asm volatile("s_mov_b32 s59, 0x20000\n\ts_mov_b32 s63, 0x20000");
    g_pro<1>(g0, mA, mW, lda);
    asm volatile("" : "+s"(ofA), "+s"(ofW), "+s"(mA), "+s"(mW), "+s"(g0));
    GAP();
  }
  asm volatile("s_bitcmp1_b32 %0, 4\n\ts_waitcnt lgkmcnt(0)\n\ts_cselect_b64 s[60:61], s[84:85], s[82:83]\n\ts_bitcmp1_b32 %0, 7\n\t"
               "s_cselect_b64 s[60:61], s[88:89], s[60:61]\n\ts_cselect_b64 s[56:57], s[86:87], s[80:81]\n\t"
               "s_add_u32 s56, s56, %1\n\ts_addc_u32 s57, s57, 0\n\ts_add_u32 s60, s60, %2\n\ts_addc_u32 s61, s61, 0\n\t"
               "s_mov_b64 s[88:89], s[90:91]\n\ts_mov_b32 s90, %3\n\ts_mov_b32 s91, 0x20000" ::"s"(b), "s"(ofA), "s"(ofW), "i"(WSB) : "scc");
  KST(0, 0);
  AC c;
  {
    GM g;
    PR pr;
    const int ptb = hsel * 8 + j;
    if (doP) prep_load(pr, tid, pj, ptb, sptr<92>(), sptr<94>(), sptr<96>());
    g_pro<2>(g0, mA, mW, lda);
    asm volatile("buffer_load_dwordx2 v[120:121], off, s[88:91], %0 sc1" ::"s"(fo(6144)) : "memory");
    if (doP) {
      asm volatile("s_waitcnt vmcnt(9)" ::: "memory");
#pragma unroll
      for (int q = 0; q < 7; ++q) asm volatile("" : "+v"(pr.kk[q]));
#pragma unroll
      for (int q = 0; q < 4; ++q) asm volatile("" : "+v"(pr.vv[q]));
      static_for([&](auto Ic) __attribute__((always_inline)) {
        constexpr int I = Ic;
        if constexpr (I < 3) g_pro(g0 + 1 + I, mA, mW, lda);
        GAP();
        prep_math<I>(pr, tid, lds + 131072);
        GAP();
      }, make_iseq<4>{});
      asm volatile("s_waitcnt lgkmcnt(0)\n\ts_barrier" ::: "memory");
      prep_store(tid, pj, ptb, (bf16*)sptr<88>() + O_X / 2, lds + 131072);
    }
    GAP();
    {
      const int fr = lane & 15, fq = lane >> 4, q = (lane & 15) >> 2, p = lane & 3;
#pragma unroll
      for (int k2 = 0; k2 < 2; ++k2)
        g.aA[k2] = lA + (wid >> 1) * 16384 + (fr >> 3) * 1024 + (fr & 7) * 128 + (((4 * k2 + fq) ^ (fr >> 1)) * 16);
#pragma unroll
      for (int hf = 0; hf < 2; ++hf)
#pragma unroll
        for (int nf = 0; nf < 8; ++nf)
          g.aB[hf][nf] = lW + (wid & 1) * 8192 + off_b(8 * fq + 4 * hf + q, nf * 2 + (p >> 1)) + 8 * (p & 1);
      asm volatile("" : "+v"(g.aA[0]), "+v"(g.aA[1]));
#pragma unroll
      for (int nf = 0; nf < 8; ++nf) asm volatile("" : "+v"(g.aB[0][nf]), "+v"(g.aB[1][nf]));
      const unsigned one = fq != q ? 0u : (p & 1) ? 0x3f800000u : 0x3f80u;
      c.o0 = (p & 2) ? 0u : one; c.o1 = (p & 2) ? one : 0u;
      c.tb = (lane >> 2) * 128 + 16 * ((p >> 1) ^ ((lane >> 2) & 6)) + 8 * (p & 1);
      c.vs = (fr >> 3) * 2048 + (fq & 1) * 512 + (fq >> 1) * 256 + (fr & 7) * 16;
      asm volatile("" : "+v"(c.o0), "+v"(c.o1), "+v"(c.tb), "+v"(c.vs));
      vsetu<81>(tid == 0 ? 0u : 0x80000000u);
      vsetu<84>(lane * 16); vsetu<85>(lane * 16 + 65536); vsetu<86>(wid * 8192 + lane * 16); vsetu<87>(wid * 8192 + lane * 16 + 4096);
      vsetu<88>(65536 + wid * 256 + fr * 16); vsetu<89>(16 * wid + fr - 4 * fq);
      asm volatile("s_mov_b64 s[92:93], s[88:89]\n\ts_mov_b64 s[94:95], s[90:91]\n\ts_mov_b32 s74, 0x40000\n\ts_mov_b32 s75, 0x60000\n\t"
                   "s_mov_b32 s76, 0x40100\n\ts_mov_b32 s77, 0x60100");
      sset<65>(128 * lda); sset<66>(192 * lda); sset<69>(384 * lda); sset<70>(448 * lda);
      sset<78>(lA + wid * 1024); sset<79>(lW + wid * 1024);
    }
    if (doP) asm volatile("s_waitcnt vmcnt(4)" ::: "memory");
    else {
      g_pro(g0 + 1, mA, mW, lda); g_pro(g0 + 2, mA, mW, lda); g_pro(g0 + 3, mA, mW, lda);
      asm volatile("s_waitcnt vmcnt(0)" ::: "memory");
    }
    asm volatile("s_barrier" ::: "memory");
    if (isY) g_read0<0>(g, make_iseq<16>{});
    else g_read0<1>(g, make_iseq<16>{});
    GAP();
    {
      u64 z = (((u64)__float_as_uint(vget<121>()) << 32) | __float_as_uint(vget<120>())) + 0x9e3779b97f4a7c15ull;
      z = (z ^ (z >> 30)) * 0xbf58476d1ce4e5b9ull; z = (z ^ (z >> 27)) * 0x94d049bb133111ebull; z ^= z >> 31;
      vsetu<82>((unsigned)z); vsetu<83>((unsigned)(z >> 32));
      asm volatile("v_readlane_b32 s80, v78, %0\n\tv_readlane_b32 s81, v78, %1\n\tv_readfirstlane_b32 s84, v82\n\tv_readfirstlane_b32 s85, v83"
                   ::"i"((int)T_TAB), "i"(T_TAB + 1));
      if (isY) asm volatile("s_mov_b64 s[80:81], 0x6543210");
    }
    unsigned k2;
    asm volatile("s_bfe_u32 s82, s80, 0x40008\n\ts_lshr_b64 s[80:81], s[80:81], 12\n\ts_lshl_b32 %0, s82, 1\n\tv_add_u32 v90, 128, v92" : "=s"(k2) :: "scc");
    asm volatile("s_waitcnt lgkmcnt(0)\n\ts_barrier" ::: "memory");
    g_proW(k2, mW);
    GAP();
    if (isY) {
      g_tile<false, 0, 1>(g);
      g_loop<false, true>(g, doP ? 0x22u : 0x20u, [&](int t) __attribute__((always_inline)) {
        if (t == 1) fl_set<T_FPREP>();
        else if (t == 4) { tload<80, T_TAB>(); tload<81, T_TAB + 1>(); srd_set<60>((kv ? arg_late<const bf16, 2>() : arg_late<const bf16, 1>()) + h * 256, (DC - 1) * LDW * 2 + 512); }
        else {
          srd_set<56>(arg_late<const bf16, 0>() + (size_t)mt * 256 * DC, 256 * DC * 2);
          asm volatile("v_mov_b32 v92, v94");
          static_for([&](auto J) { asm volatile("s_mov_b32 s%c0, %1" ::"i"(64 + J), "i"((J + 1) * 64 * DC)); }, make_iseq<7>{});
        }
      });
      tload<101, T_OQ>(); sset<83>(__float_as_uint(CS));
      asm volatile("v_mov_b32 v112, v84\n\tv_add_u32 v113, 8192, v84");
      GAP();
      g_tile<false, 0, 2>(g);
      KST(0, 3);
    }
#define TT 3
    auto kvtile = [&](auto TRc) __attribute__((always_inline)) {
      constexpr bool TR = TRc;
      if (isY) g_tile<TR, 1, 4>(g);
      else g_tile<TR, 1, 1>(g);
      g_loop<TR, false>(g, 0x2000u | (isY || doA ? 2u : BAL && (j & 1) ? 0u : 1u << TT), [&](int t) __attribute__((always_inline)) {
        int l = threadIdx.x & 63; asm volatile("" : "+v"(l));
        if (t == 13) {
          asm volatile("s_mov_b32 s79, 0x28000");
          fl_poll<T_NPRE>();
          if ((tget<T_IT + T_ITN + 16>() & 0xf000000) == 0)
            asm volatile("v_readlane_b32 s48, v78, %0\n\tv_and_b32 v122, 0xffffff00, v82\n\tv_mov_b32 v123, v83\n\ts_nop 2\n\t"
                         "buffer_store_dwordx2 v[122:123], v81, s[88:91], s48 offen sc1" ::"i"((int)T_CNT) : "memory");
          if (!doA && !isY) {
            CODE_TOUCH("at_code_begin", "pf_code_end", hsel * 8 + wid * 2, 16u, 64, m0d);
            CODE_TOUCH("at_code_begin", "pf_code_end", hsel * 8 + wid * 2 + 1, 16u, 64, m0d);
          }
          if (doA) {
            const char* tp = arg_late<const char, 9>() + (size_t)(pj * 64 + pu) * (SEQ * 2) + (l & 31) * 128;
            asm volatile("s_mov_b32 m0, %1\n\ts_nop 0\n\tglobal_load_lds_dword %0, off" ::"v"(tp), "i"(m0d) : "memory");
          }
        } else if (isY) fl_set<T_FQ>();
        else if (doA) post_dma(pj, pu, arg_late<const bf16, 12>(), 131072 + wid * 8192);
        else {
          const char* tp = arg_late<const char, 0>() + 1024 * DC * 2 + (pu * 256 + l) * 128;
          asm volatile("s_mov_b32 m0, %0\n\ts_nop 0" ::"i"(m0d));
#pragma unroll
          for (int i = 0; i < (BAL ? 8 : 4); ++i) asm volatile("global_load_lds_dword %0, off" ::"v"(tp + (i & 3) * 8192 + (i >> 2) * 131072) : "memory");
        }
      });
      tload<101, T_OG>(); tload<96, T_Q0>(); tload<98, T_K0>();
      fl_check();
      tload<50, T_NST>(); tload<51, T_NST + 1>();
      asm volatile("v_mov_b32 v112, v84\n\tv_add_u32 v113, %0, v84\n\tv_mov_b32 v90, v87\n\tv_mov_b32 v91, v87\n\tv_mov_b32 v92, v87\n\tv_mov_b32 v93, v87\n\t"
                   "v_mov_b32 v94, v86\n\tv_mov_b32 v95, v86\n\tv_mov_b32 v126, v86\n\tv_mov_b32 v127, v86\n\ts_mov_b32 s99, 0x8000\n\t" FL_ARM("s[50:51]")
                   ::"i"(TR ? 4096 : 8192));
      if (doA) {
        const int d = pj * 64 + pu;
        const bf16* dp = arg_late<const bf16, 10>() + d;
        asm volatile("global_load_ushort v123, %0, off" ::"v"(dp) : "memory");
        srd_set<56>(arg_late<const bf16, 9>() + (size_t)d * SEQ, SEQ * 2);
        srd_set<60>(arg_late<const char, 5>() + O_X + d * SEQ * 2, SEQ * 2);
        sset<78>(65536 + wid * 15488);
        asm volatile("s_movk_i32 s64, 992\n\tv_subrev_u32 v122, 32, v84");
        GAP();
        g_tile<TR, 0, 3>(g);
      } else {
        sset<79>(isY ? wid * 8192 : 0x30000);
        if (tget<T_IT + T_ITN + 16>() & 16) asm volatile("s_add_u32 s98, s98, 0x8000\n\ts_mov_b32 s97, 0xffff8000" ::: "scc");
        else asm volatile("s_mov_b32 s97, 0x8000");
        GAP();
        g_tile<TR, 0, 5>(g);
      }
    };
    if (kv) kvtile(std::true_type{});
    else kvtile(std::false_type{});
  }
  KST(0, 4);
  asm volatile("s_waitcnt vmcnt(16)" ::: "memory");
  const unsigned md0 = tget<T_IT + 16>(), md1 = tget<T_IT + T_ITN + 16>();
  sset<79>(wid * 8192 + (md0 & 16) * 2048);
  if (doA) {
    const float Dd = bfl(__float_as_uint(vget<123>()));
    KST(0, 5);
    conv_wave<T_FG>(O_Z + pj * 64 * SEQ * 2 + ((pu & 7) * 8 + (pu >> 3)) * 64, pu >> 3, Dd, lds + 65536 + wid * 15488);
    const char* tp = arg_late<const char, 11>() + (size_t)h * (HD * DA * 2) + ((pj * 4 + wid) * 64 + lane) * 128;
    asm volatile("s_mov_b32 m0, %1\n\ts_nop 0\n\tglobal_load_lds_dword %0, off\n\ts_waitcnt vmcnt(17)" ::"v"(tp), "i"(m0d) : "memory");
    KST(0, 6);
  } else if (!isY) {
    static_for([&](auto I) { d_store<16 + I>(); }, make_iseq<16>{});
    tload<98, T_K0>();
    asm volatile("s_waitcnt vmcnt(0)\n\ts_barrier" ::: "memory");
    fl_set<T_FG>();
    KST(0, 5);
    fl_check();
    CODE_TOUCH("att_begin", "pf_code_end", hsel * 4 + wid, 8u, 64, m0d);
    dma4k<0>(); dma4k<1>(); dma4k<2>(); dma4k<3>();
    asm volatile("s_waitcnt vmcnt(0)" ::: "memory");
    KST(0, 6);
  } else {
    tload<76, T_PART>();
    fl_check();
    asm volatile("buffer_load_dwordx2 v[122:123], v88, s[88:91], s76 offen offset:1024 sc1" ::: "memory");
  }
  sset<78>(65536 + wid * 8192); sset<79>(98304 + wid * 8192);
  { const bf16* wp = arg_late<const bf16, 11>() + ((md1 >> 24) & 15) * HD * DA; asm volatile("s_mov_b64 s[80:81], %0" ::"s"(wp)); }
  asm volatile("att_begin:");
  ev_code();
  asm volatile("s_waitcnt lgkmcnt(0)\n\ts_barrier" ::: "memory");
#pragma unroll 1
  for (int it = md0 ? 0 : 1; it < 2; ++it) {
    const unsigned mode = it ? md1 : md0;
    const bool odd = mode & 16;
    if (odd) kreads<1>(make_iseq<8>{});
    else kreads<0>(make_iseq<8>{});
    const unsigned oP = tget<T_PART>();
    if (it == 0) {
      static_for([&](auto I) { tload<57 + I, T_IT + I>(); }, make_iseq<21>{});
      tload<S_T, T_IT + 21>(); tload<S_EV, T_IT + 22>();
    } else {
      static_for([&](auto I) { tload<57 + I, T_IT + T_ITN + I>(); }, make_iseq<21>{});
      tload<S_T, T_IT + T_ITN + 21>(); tload<S_EV, T_IT + T_ITN + 22>();
    }
    asm volatile("s_add_u32 s98, s97, 0x8000\n\ts_mov_b32 s56, -1\n\tv_mov_b64 v[114:115], 0\n\tv_mov_b64 v[118:119], 0\n\t"
                 "v_mov_b32 v120, 0x45800000" ::: "scc");
    KST(0, 8 + 3 * (it && !isY));
    GAP();
#define STA(k)
#define STI()
#define LOOP_ENDS "s_bfe_u32 s76, s73, 0x80008"
    STA(0);
    if (odd) {
      if (isY) phaseA<1, 2>(c);
      else phaseA<1, 1>(c);
      STA(1);
      STA(2);
      phaseB0<1>(mode, isY);
      STA(3);
      asm volatile(LOOP_ENDS "\n\ts_branch att_mid" ::: "scc");
    } else {
      if (isY) phaseA<0, 2>(c);
      else phaseA<0, 1>(c);
      STA(1);
      if (__builtin_expect(mode & 4, 0)) { asm volatile("s_mov_b32 s56, 30"); ev_call(); asm volatile("s_mov_b32 s56, 31"); ev_call(); }
      STA(2);
      phaseB0<0>(mode, isY);
      if (__builtin_expect(mode & 4, 0)) { ev_call(); asm volatile("s_branch att_done"); }
      STA(3);
    }
    GAP();
    asm volatile(LOOP_ENDS "\natt_loop:" ::: "scc");
    GAP();
    phaseA<1, 0>(c);
    STI();
    if (__builtin_expect(c.rare != 0, 0)) sm_redo<1>();
    phaseB<1>(c);
    asm volatile("s_cbranch_scc1 att_done\natt_mid:");
    phaseA<0, 0>(c);
    STI();
    if (__builtin_expect(c.rare != 0, 0)) sm_redo<0>();
    phaseB<0>(c);
    asm volatile("s_cbranch_scc0 att_loop\natt_done:");
    GAP();
    STA(4);
    if (it == 0) phaseA<0, 3>(c);
    else {
      sset<76>(oP);
      GAP();
      phaseAL(c, make_iseq<32>{});
      if (__builtin_expect(redo(), 0)) sm_redo<1, false>();
      if (mode & 2) phaseBL<true>(c, make_iseq<64>{});
      else phaseBL<false>(c, make_iseq<64>{});
    }
    if (__builtin_expect(redo(), 0)) {
      if (it) static_for([&](auto I) { vset<R_S + 32 + (I >> 2) * 8 + (I & 3)>(-INFINITY); }, make_iseq<16>{});
      asm volatile("s_nop 7");
      sm_redo<0, false>();
    }
    STA(5);
    KST(0, 9 + 3 * (it && !isY));
    float l[2];
    asm volatile("v_add_f32 %0, v%c2, v%c3\n\tv_add_f32 %1, v%c4, v%c5" : "=v"(l[0]), "=v"(l[1]) : "i"(R_RS), "i"(R_TS), "i"(R_RS + 1), "i"(R_TS + 1));
    const float cm[2] = {vget<R_M>(), vget<R_M + 1>()};
    l[0] = row_reduce<false>(l[0]); l[1] = row_reduce<false>(l[1]);
    EP p;
    if (it == 0) {
      const f32x4 ml = {cm[0], l[0], cm[1], l[1]};
      asm volatile("buffer_store_dwordx4 %0, v88, s[88:91], %1 offen sc1\n\ts_mov_b32 s101, %2" ::"v"(ml), "s"(oP), "s"(oP + wid * 16384) : "memory");
      phaseF(c, p, make_iseq<74>{});
      asm volatile(FL_POLL "\n\ts_waitcnt vmcnt(17)" ::: "memory");
    } else {
      if (!(mode & 2)) {
        p.fa[0] = __builtin_amdgcn_rcpf(l[0]); p.fa[1] = __builtin_amdgcn_rcpf(l[1]);
        phaseFL<1>(p);
      } else {
        const f32x4 ml = {vget<122>(), vget<123>(), vget<124>(), vget<125>()};
        if (__builtin_expect(__builtin_amdgcn_ballot_w64(ml[0] != cm[0] || ml[2] != cm[1]) == 0, 1)) {
          p.fa[0] = __builtin_amdgcn_rcpf(l[0] + ml[1]); p.fa[1] = __builtin_amdgcn_rcpf(l[1] + ml[3]);
          phaseFL<3>(p);
        } else {
#pragma unroll
          for (int e = 0; e < 2; ++e) {
            const float mm = fmaxf(cm[e], ml[2 * e]);
            const float wa = __builtin_amdgcn_exp2f(cm[e] - mm), wb = __builtin_amdgcn_exp2f(ml[2 * e] - mm);
            const float inv = __builtin_amdgcn_rcpf(l[e] * wa + ml[2 * e + 1] * wb);
            p.fa[e] = wa * inv; p.fb[e] = wb * inv;
          }
          phaseFL<2>(p);
        }
      }
    }
    KST(0, 10 + 3 * (it && !isY));
  }
  asm volatile(TICKET "\n\tv_xor_b32 v%c0, s100, v%c0\np_loop:" ::"i"(R_TB) : "memory");
  GAP();
  p_half<0, true>(make_iseq<32>{});
  asm volatile("s_cbranch_scc0 p_mid");
  p_half<1, true>(make_iseq<32>{});
  asm volatile("s_branch p_loop\np_loop2:");
  p_half<0, false>(make_iseq<32>{});
  asm volatile("p_mid:");
  p_half<1, false>(make_iseq<32>{});
  asm volatile("s_cbranch_scc1 p_loop2\n\ts_nop 5");
  GAP();
  p_cvt<1, 0>(); p_cvt<1, 1>(); p_cvt<1, 2>(); p_cvt<1, 3>();
  p_store<1, 1>();
  GAP();
  KST(0, 27);
  {
    constexpr unsigned rdy = 98304;
    int lz = threadIdx.x & 63; asm volatile("" : "+v"(lz));
    const int fr = lz & 15, fq = lz >> 4, qbF = md1 >> 28;
    if (wid == 0) {
#pragma unroll 1
      for (int q = 0; q < 0x100000; ++q) {
        unsigned lo, hi, tl, th;
        asm volatile("v_readfirstlane_b32 %0, v240\n\tv_readfirstlane_b32 %1, v241\n\ts_mov_b32 %2, s84\n\ts_mov_b32 %3, s85" : "=s"(lo), "=s"(hi), "=s"(tl), "=s"(th));
        if (__builtin_expect((((lo ^ tl) >> 8) | (hi ^ th)) == 0, 1)) break;
        asm volatile("s_sleep 4\n\tv_mov_b64 v[240:241], 1\n\t" TICKET "\n\ts_waitcnt vmcnt(0)" ::: "memory");
      }
      asm volatile("v_mov_b32 v241, 0\n\ts_mov_b64 exec, 1\n\tds_write_b64 %0, v[240:241]\n\ts_mov_b64 exec, -1" ::"v"(rdy) : "memory");
    }
    vsetu<79>(fo(1024) + qbF * 4352 + fr * 8);
    {
      const int jj = 4 * (fr >> 3) + (fr & 3);
      const unsigned one = ((fq ^ (fr >> 2)) & 1) ? 0u : (jj & 1) ? 0x3f800000u : 0x3f80u;
      static_for([&](auto I) { vsetu<96 + I>((jj >> 1) == I ? one : 0u); }, make_iseq<4>{});
    }
    unsigned vo = (fq >> 1) * 1024 + (fq & 1) * 256 + fr * 16;
    bf16* out = arg_late<bf16, 13>();
    __amdgpu_buffer_rsrc_t so_ = __builtin_amdgcn_make_buffer_rsrc((void*)out, (short)0, SEQ * OUTW * 2, 0x00020000);
    unsigned vw = ((qbF * 128 + (fr & 7)) * OUTW + DA + (fr >> 3) * 32 + fq * 8) * 2;
    asm volatile("" : "+s"(so_));
    asm volatile("s_waitcnt vmcnt(0) lgkmcnt(0)\n\ts_barrier" : "+v"(vo), "+v"(vw) :: "memory");
    fl_set<T_FS>();
    unsigned tk;
    asm volatile("ds_read_b32 %0, %1\n\ts_waitcnt lgkmcnt(0)" : "=v"(tk) : "v"(rdy) : "memory");
    tk = __builtin_amdgcn_readfirstlane(tk) & 255;
    KST(0, 30);
    if (doA) post_math(pj, pu, out, lds + 131072 + wid * 8192);
#define N3 0
#define N2 12
    constexpr int N1 = 28 - 3 * N3 - 2 * N2;
    static_assert(N1 >= 0 && N3 + N2 + N1 <= 16);
    const int nu = tk < N3 ? 3 : tk < N3 + N2 ? 2 : tk < N3 + N2 + N1 ? 1 : 0;
    const int u0 = 4 * (tk < N3 ? 3 * tk : tk < N3 + N2 ? N3 + 2 * tk : 2 * N3 + N2 + tk) + wid * nu;
    KST(0, 28);
    if (nu) {
      const unsigned so0 = O_S + qbF * 0x200000 + (16 + u0) * 2048, so[3] = {so0, so0, so0};
      unsigned pend = 0x5555u, cnt = 0x400000;
      while (true) {
        unsigned nw;
        if (wid == 3) {
          u64 m;
          const u64 ex = pend | (pend << 1);
          asm volatile(FL_ARM("%1") "\n\t" FL_POLL "\n\ts_waitcnt vmcnt(0)\n\tv_cmp_eq_u64 %0, s[84:85], v[124:125]" : "=s"(m) : "s"(ex) : "memory");
          nw = (unsigned)m & ((unsigned)m >> 1) & pend;
          if (nw) asm volatile("ds_write_b32 %0, %1 offset:4" ::"v"(rdy), "v"(~pend | nw) : "memory");
        } else {
          unsigned t;
          asm volatile("ds_read_b32 %0, %1 offset:4\n\ts_waitcnt lgkmcnt(0)" : "=v"(t) : "v"(rdy) : "memory");
          nw = __builtin_amdgcn_readfirstlane(t) & pend;
        }
        if (--cnt == 0) nw = pend;
        if (nw == 0) {
          if (wid == 3) asm volatile("s_sleep 4");
          else asm volatile("s_sleep 1");
          continue;
        }
        asm volatile("s_nop 4");
        static_for([&](auto S) {
          if ((nw >> (2 * S)) & 1) {

            r_load<0, 2 * S>(vo, so[0]); r_load<0, 2 * S + 1>(vo, so[0]);
            if (nu > 1) { r_load<1, 2 * S>(vo, so[1]); r_load<1, 2 * S + 1>(vo, so[1]); }
            if (nu > 2) { r_load<2, 2 * S>(vo, so[2]); r_load<2, 2 * S + 1>(vo, so[2]); }
          }
        }, make_iseq<8>{});
        pend &= ~nw;
        if (pend == 0) break;
      }
      asm volatile("s_waitcnt vmcnt(0)" ::: "memory");
      KST(0, 16);
      auto oo = [&](int U) { return ((((U >> 1) & 1) * 64 + ((U >> 2) & 3) * 16 + (U & 1) * 8) * OUTW + (U >> 4) * 64) * 2; };
      r_fin<0>(vw, so_, oo(u0));
      if (nu > 1) r_fin<1>(vw, so_, oo(u0 + 1));
      if (nu > 2) r_fin<2>(vw, so_, oo(u0 + 2));
      if (qbF == 15 && tk == 0) asm volatile(FL_SET("%0") ::"s"(fo(6144)) : "memory");
    }
    KST(0, 29);
  }
  asm volatile("s_waitcnt vmcnt(0)" ::: "memory");
  asm volatile("pf_code_end:");
  KST(0, 1);
}

extern "C" void kernel_launch(void* const* d_in, const int* in_sizes, int n_in, void* d_out, int out_size, void* d_ws,
                              size_t ws_size, hipStream_t stream) {
  const bf16* Aq = (const bf16*)d_in[0];
  const bf16* Ak = (const bf16*)d_in[1];
  const bf16* Av = (const bf16*)d_in[2];
  const bf16* Ash = (const bf16*)d_in[3];
  const bf16* Assmk = (const bf16*)d_in[4];
  const bf16* AssmD = (const bf16*)d_in[5];
  const bf16* Bx = (const bf16*)d_in[6];
  const bf16* Bckv = (const bf16*)d_in[7];
  const bf16* Bwq = (const bf16*)d_in[8];
  const bf16* Bwuk = (const bf16*)d_in[9];
  const bf16* Bwuv = (const bf16*)d_in[10];
  const bf16* Bwo = (const bf16*)d_in[11];
  bf16* out = (bf16*)d_out;
  hipFuncSetAttribute((const void*)fused_kernel, hipFuncAttributeMaxDynamicSharedMemorySize, 163840);
  fused_kernel<<<256, 256, 163840, stream>>>(Bckv, Bwuk, Bwuv, Bx, Bwq, (bf16*)d_ws, Ak, Av, Ash, Assmk, AssmD, Bwo, Aq, out);
}
```

```cpp
#include <hip/hip_runtime.h>
#include <hip/hip_bf16.h>
#include <stdint.h>
#include <utility>

typedef __hip_bfloat16 bf16;
typedef __attribute__((ext_vector_type(4))) short bf16x4;
typedef __attribute__((ext_vector_type(2))) unsigned u32x2;

constexpr int SEQ = 2048;
constexpr int DA = 448;
constexpr int NH = 16;
constexpr int HD = 256;
constexpr int DC = 1024;
constexpr int OUTW = 896;
constexpr unsigned TSB = NH * SEQ * HD * 2, O_K = TSB, O_S = 3 * TSB, O_P = 5 * TSB, PSTR = 67584, O_F = O_P + 128 * PSTR,
                   O_X = O_F + 400 * 4352, O_Z = O_X + DA * SEQ * 2, WSB = O_Z + DA * SEQ * 2;
constexpr unsigned fo(int w) { return O_F + (w >> 4) * 4352 + (w & 15) * 8; }

#define KST(kid, which)

__device__ __forceinline__ float bf2f(unsigned short u) { return __uint_as_float(((unsigned)u) << 16); }
__device__ __forceinline__ unsigned short f2bf(float f) { return __bfloat16_as_ushort(__float2bfloat16(f)); }
__device__ __forceinline__ float rbf(float f) { return bf2f(f2bf(f)); }
typedef __attribute__((ext_vector_type(2))) __bf16 bf16x2n;
typedef __attribute__((ext_vector_type(2))) float f32x2n;
__device__ __forceinline__ unsigned pk2(float a, float b) {
  return __builtin_bit_cast(unsigned, __builtin_convertvector((f32x2n){a, b}, bf16x2n));
}

__device__ __forceinline__ int off_b(int row, int ch) { return 256 * row + 16 * (ch ^ (((row & 3) << 2) | ((row >> 2) & 3))); }

template <int R> __device__ __forceinline__ void sset(unsigned x) { asm volatile("s_mov_b32 s%c0, %1" ::"i"(R), "s"(x)); }
template <int R> __device__ __forceinline__ void vsetu(unsigned x) { asm volatile("v_mov_b32 v%c0, %1" ::"i"(R), "v"(x)); }
template <int R> __device__ __forceinline__ void srd_set(const void* p, unsigned n) {
  asm volatile("s_mov_b64 s[%c0:%c1], %4\n\ts_mov_b32 s%c2, %5\n\ts_mov_b32 s%c3, 0x20000" ::"i"(R), "i"(R + 1), "i"(R + 2), "i"(R + 3), "s"(p), "s"(n));
}
template <int R, int L> __device__ __forceinline__ void tload() { asm volatile("v_readlane_b32 s%c0, v78, %1" ::"i"(R), "i"(L)); }
template <int L> __device__ __forceinline__ unsigned tget() { unsigned t; asm volatile("v_readlane_b32 %0, v78, %1" : "=s"(t) : "i"(L)); return t; }
#define FL_SET(S) "buffer_store_dwordx2 v[82:83], v81, s[88:91], " S " offen sc1"
#define FL_ARM(N) "v_cndmask_b32_e64 v80, -2.0, v79, " N
#define FL_POLL "buffer_load_dwordx2 v[124:125], v80, s[88:91], 0 offen sc1"
#define FL_CHECK(N) "s_mov_b32 s48, 0x100000\n7:\n\tv_cmp_eq_u64 vcc, s[84:85], v[124:125]\n\ts_and_b64 vcc, vcc, " N "\n\ts_cmp_eq_u64 vcc, " N \
                    "\n\ts_cbranch_scc1 8f\n\ts_sleep 2\n\t" FL_POLL "\n\ts_waitcnt vmcnt(0)\n\ts_sub_u32 s48, s48, 1\n\ts_cbranch_scc0 7b\n8:"
template <int L> __device__ __forceinline__ void fl_set() {
  asm volatile("v_readlane_b32 s48, v78, %0\n\ts_nop 4\n\t" FL_SET("s48") ::"i"(L) : "memory");
}
template <int L> __device__ __forceinline__ void fl_poll() {
  asm volatile("v_readlane_b32 s50, v78, %0\n\tv_readlane_b32 s51, v78, %1\n\ts_nop 1\n\t" FL_ARM("s[50:51]") "\n\t" FL_POLL ::"i"(L), "i"(L + 1) : "memory");
}
#define TICKET "buffer_atomic_add_x2 v[240:241], v81, s[88:91], s101 offen sc0"
__device__ __forceinline__ void fl_check() { asm volatile(FL_CHECK("s[50:51]") ::: "vcc", "scc", "memory"); }
template <int R> __device__ __forceinline__ float vget() { float t; asm volatile("v_mov_b32 %0, v%c1" : "=v"(t) : "i"(R)); return t; }
template <int R> __device__ __forceinline__ void vset(float x) { asm volatile("v_mov_b32 v%c0, %1" ::"i"(R), "v"(x)); }
template <int R> __device__ __forceinline__ float acc_read1() { float t; asm volatile("v_accvgpr_read_b32 %0, a[%c1]" : "=v"(t) : "i"(R)); return t; }
template <class F, int... I> __device__ __forceinline__ void static_for(F&& f, std::integer_sequence<int, I...>) {
  (f(std::integral_constant<int, I>{}), ...);
}
template <int... Is> using iseq = std::integer_sequence<int, Is...>;
template <int N> using make_iseq = std::make_integer_sequence<int, N>;

typedef __attribute__((ext_vector_type(4))) unsigned u32x4c;
typedef __attribute__((ext_vector_type(4))) float f32x4c;
typedef __attribute__((ext_vector_type(2))) float f32x2c;
typedef unsigned long long u64;
struct PR { u32x4c kk[7], vv[4]; };
__device__ __forceinline__ void prep_load(PR& p, int tid, int cg, int tb, const bf16* __restrict__ k, const bf16* __restrict__ v,
                                          const bf16* __restrict__ shift_k) {
  const __amdgpu_buffer_rsrc_t sk = __builtin_amdgcn_make_buffer_rsrc((void*)k, (short)0, SEQ * DA * 2, 0x00020000);
  const __amdgpu_buffer_rsrc_t sv = __builtin_amdgcn_make_buffer_rsrc((void*)v, (short)0, SEQ * DA * 2, 0x00020000);
  const __amdgpu_buffer_rsrc_t sw = __builtin_amdgcn_make_buffer_rsrc((void*)(shift_k + cg * 256), (short)0, 512, 0x00020000);
  const int vo = ((tb * 128 + (tid >> 3) * 4) * DA + cg * 64 + (tid & 7) * 8) * 2;
#pragma unroll
  for (int q = 0; q < 7; ++q)
    asm volatile("buffer_load_dwordx4 %0, %1, %2, 0 offen" : "=v"(p.kk[q]) : "v"(vo + (q - 3) * DA * 2), "s"(sk) : "memory");
#pragma unroll
  for (int q = 0; q < 4; ++q)
    asm volatile("buffer_load_dwordx4 %0, %1, %2, 0 offen offset:%3" : "=v"(p.vv[q]) : "v"(vo), "s"(sv), "i"(q * DA * 2) : "memory");
#pragma unroll
  for (int q = 0; q < 4; ++q)
    asm volatile("buffer_load_dwordx4 a[%c0:%c1], %2, %3, 0 offen offset:%4" ::"i"(4 * q), "i"(4 * q + 3), "v"((tid & 7) * 64), "s"(sw), "i"(q * 16) : "memory");
}
__device__ __forceinline__ float bfl(unsigned u) { return __uint_as_float(u << 16); }
__device__ __forceinline__ float bfh(unsigned u) { return __uint_as_float(u & 0xffff0000u); }
__device__ __forceinline__ f32x2c rbf2(f32x2c a) {
  return (f32x2c){__uint_as_float(pk2(0.f, a[0])), __uint_as_float(pk2(0.f, a[1]))};
}
template <int I> __device__ __forceinline__ void prep_math(const PR& p, int tid, char* lx) {
  const int rp = tid >> 3, c = tid & 7;
  f32x2c wt[4], kf[7];
  const unsigned w[4] = {__float_as_uint(acc_read1<4 * I>()), __float_as_uint(acc_read1<4 * I + 1>()), __float_as_uint(acc_read1<4 * I + 2>()),
                         __float_as_uint(acc_read1<4 * I + 3>())};
#pragma unroll
  for (int t = 0; t < 4; ++t)
    wt[t] = (t & 1) ? (f32x2c){bfh(w[t >> 1]), bfh(w[2 + (t >> 1)])} : (f32x2c){bfl(w[t >> 1]), bfl(w[2 + (t >> 1)])};
#pragma unroll
  for (int q = 0; q < 7; ++q) kf[q] = (f32x2c){bfl(p.kk[q][I]), bfh(p.kk[q][I])};
  f32x2c x[4];
#pragma unroll
  for (int e = 0; e < 4; ++e) {
    f32x2c s = rbf2(kf[e] * wt[3]);
    s = rbf2(s + rbf2(kf[e + 1] * wt[2]));
    s = rbf2(s + rbf2(kf[e + 2] * wt[1]));
    s = rbf2(s + rbf2(kf[e + 3] * wt[0]));
    x[e] = s * (f32x2c){bfl(p.vv[e][I]), bfh(p.vv[e][I])};
  }
#pragma unroll
  for (int u = 0; u < 2; ++u)
    *(u32x2*)(lx + (c * 8 + 2 * I + u) * 256 + (((31 - rp) * 8) ^ (c << 4))) = (u32x2){pk2(x[3][u], x[2][u]), pk2(x[1][u], x[0][u])};
}
__device__ __forceinline__ void prep_store(int tid, int cg, int tb, bf16* __restrict__ xr, const char* lx) {
  const __amdgpu_buffer_rsrc_t sx = __builtin_amdgcn_make_buffer_rsrc((void*)(xr + (size_t)cg * 64 * SEQ), (short)0, 64 * SEQ * 2, 0x00020000);
#pragma unroll
  for (int rd = 0; rd < 4; ++rd) {
    const int idx = tid + 256 * rd, ch = idx >> 4, q = idx & 15;
    u32x4c w = *(const u32x4c*)(lx + ch * 256 + 16 * (q ^ (ch >> 3)));
    __builtin_amdgcn_raw_buffer_store_b128(w, sx, (ch * SEQ + (15 - tb) * 128 + q * 8) * 2, 0, 16);
  }
}

#define CODE_TOUCH(LB, LE, wx, NW, NL, m0v) do { unsigned blo_, bhi_, elo_; \
    asm volatile("s_getpc_b64 s[48:49]\n\ts_add_u32 s48, s48, " LB "@rel32@lo+4\n\ts_addc_u32 s49, s49, " LB "@rel32@hi+12\n\t" \
                 "s_mov_b32 %0, s48\n\ts_mov_b32 %1, s49\n\t" \
                 "s_getpc_b64 s[48:49]\n\ts_add_u32 s48, s48, " LE "@rel32@lo+4\n\ts_mov_b32 %2, s48" \
                 : "=s"(blo_), "=s"(bhi_), "=s"(elo_) :: "scc"); \
    const unsigned n_ = elo_ - (blo_ & ~127u); \
    const __amdgpu_buffer_rsrc_t srd_ = __builtin_amdgcn_make_buffer_rsrc((void*)(((unsigned long long)bhi_ << 32) | (blo_ & ~127u)), (short)0, n_ < 262144u ? n_ : 0u, 0x00020000); \
    const unsigned l_ = threadIdx.x & 63, vo_ = l_ < (NL) ? ((wx) + l_ * (NW)) * 128u : 0x80000000u; \
    asm volatile("s_mov_b32 m0, %0\n\ts_nop 4\n\tbuffer_load_dword %1, %2, 0 offen lds" ::"s"(m0v), "v"(vo_), "s"(srd_) : "memory"); } while (0)
template <int I> __device__ __forceinline__ void d_store() {
  asm volatile("buffer_store_dwordx4 v[%c0:%c1], v%c2, s[88:91], s101 offen offset:%3 sc1" ::"i"(128 + 4 * I), "i"(131 + 4 * I), "i"(112 + ((I >> 2) & 1)),
               "i"((I & 3) * 1024) : "memory");
  if constexpr ((I & 7) == 7) asm volatile("s_addk_i32 s101, 0x4000" ::: "scc");
}
constexpr int NS_X = 19;
struct CV { unsigned pa[4], pb, px, vz, sz; float D; };
constexpr int cv_J(bool up, int q) { int J = up ? 0 : 7; while (q > J) { q -= J + 1; J += up ? 1 : -1; } return J; }
constexpr int cv_i(bool up, int q) { int J = up ? 0 : 7; while (q > J) { q -= J + 1; J += up ? 1 : -1; } return q; }
template <int R, int N> __device__ __forceinline__ void cv_rd(CV& c) {
  if constexpr (N < 16) {
    constexpr int i = N >> 1, o = 16 * R + 128 * (i & 1) + 2 * (N & 1), r = 32 + 32 * (R & 1) + 2 * N;
    asm volatile("ds_read2_b32 a[%c1:%c2], %0 offset0:%c3 offset1:%c4" ::"v"(c.pa[i >> 1]), "i"(r), "i"(r + 1), "i"(o), "i"(o + 1));
  } else {
    constexpr int m = N - 16, r = 192 + 32 * (R & 1) + 4 * m;
    asm volatile("ds_read_b128 a[%c1:%c2], %0 offset:%c3" ::"v"(c.pb), "i"(r), "i"(r + 3), "i"(64 * R + 512 * (7 - m)));
  }
}
template <int O> __device__ __forceinline__ void cv_xd(CV& c) {
  constexpr int e = O & 31, x = 96 + ((e >> 1) ^ 1);
  if constexpr (O >= 64) asm volatile("v_cvt_pk_bf16_f32 v%c0, 0, v%c0" ::"i"(160 + e));
  else if constexpr (O >= 32) asm volatile("v_mul_f32 v%c0, v%c0, %1" ::"i"(160 + e), "v"(c.D));
  else if constexpr (O & 1) asm volatile("v_lshlrev_b32 v%c0, 16, v%c1" ::"i"(160 + e), "i"(x));
  else asm volatile("v_and_b32 v%c0, 0xffff0000, v%c1" ::"i"(160 + e), "i"(x));
}
template <int O> __device__ __forceinline__ void cv_z(CV& c) {
  constexpr int J = O / 11, k = O % 11, y = 128 + 4 * J;
  if constexpr (k < 4) asm volatile("v_cvt_pk_bf16_f32 v%c0, 0, v%c0" ::"i"(y + k));
  else if constexpr (k < 8) asm volatile("v_add_f32 v%c0, v%c0, v%c1" ::"i"(y + k - 4), "i"(y + 28 + k));
  else if constexpr (k < 10) asm volatile("v_cvt_pk_bf16_f32 v%c0, v%c1, v%c2" ::"i"(y + k - 8), "i"(y + 2 * k - 16), "i"(y + 2 * k - 15));
  else asm volatile("buffer_store_dwordx2 v[%c0:%c1], %2, s[88:91], s101 offen sc1\n\ts_add_u32 s101, s101, 0x8000" ::"i"(y), "i"(y + 1), "v"(c.vz) : "memory");
}
constexpr int cv_z0(int Q) {
  int o = 0;
  for (int q = 0; q < Q; ++q)
    for (int k = 0; k < (q % 4 == 1 ? 0 : 3); ++k) { const int J = o / 11; if ((J + 1) * (J + 2) / 2 - 1 + (J ? 4 : 7) <= q) ++o; }
  return o;
}
constexpr int cv_xd0(int T) { int o = 0; for (int t = 0; t < T; ++t) o += t % 4 != 1 || (t >= 72 && t < 108); return o < 96 ? o : 96; }
static_assert(cv_xd0(144) == 96);
template <int B, int... Is> __device__ __forceinline__ void cv_zs(CV& c, iseq<Is...>) { (cv_z<B + Is>(c), ...); }
template <int R, int Q> __device__ __forceinline__ void cv_slot(CV& c) {
  constexpr int i = cv_i(R == 7, Q), J = cv_J(R == 7, Q), d = 128 + 4 * J, a = 32 + 32 * (R & 1) + 4 * i, b = 192 + 32 * (R & 1) + 4 * (J - i);
  constexpr int T = 36 * R + Q, vm = Q % 4 == 1 && Q < 32 ? Q / 4 : -1;
  if constexpr (Q == 0) asm volatile("s_waitcnt lgkmcnt(0)");
  if constexpr (R >= 6 && vm >= 0 && vm % 4 == 0) asm volatile("s_add_u32 m0, s79, %0" ::"i"(vm * 1024));
  if constexpr (R == 0 && i == 0)
    asm volatile("v_mfma_f32_16x16x32_bf16 v[%c0:%c1], a[%c2:%c3], a[%c4:%c5], 0" ::"i"(d), "i"(d + 3), "i"(a), "i"(a + 3), "i"(b), "i"(b + 3));
  else
    asm volatile("v_mfma_f32_16x16x32_bf16 v[%c0:%c1], a[%c2:%c3], a[%c4:%c5], v[%c0:%c1]" ::"i"(d), "i"(d + 3), "i"(a), "i"(a + 3), "i"(b), "i"(b + 3));
  if constexpr (Q < 24 && R < 7) cv_rd<R + 1, Q>(c);
  if constexpr (T % 7 == 3 && NS_X + 5 + T / 7 < 32) d_store<NS_X + 5 + T / 7>();
  if constexpr (R == 2 && Q >= 24 && Q < 32)
    asm volatile("ds_read_b64 v[%c1:%c2], %0 offset:%c3" ::"v"(c.px), "i"(96 + 2 * (Q - 24)), "i"(97 + 2 * (Q - 24)), "i"(512 * (31 - Q)));
  if constexpr (R >= 3 && R < 7) if constexpr (cv_xd0(T - 107) > cv_xd0(T - 108)) cv_xd<cv_xd0(T - 108)>(c);
  if constexpr ((R == 3 || R == 4) && vm >= 0) {
    constexpr int f = (R - 3) * 8 + vm;
    asm volatile("buffer_load_dwordx4 a[%c0:%c1], v84, s[88:91], s96 offen offset:%2 sc1" ::"i"(128 + 4 * f), "i"(131 + 4 * f), "i"((f & 3) * 1024) : "memory");
    if constexpr ((f & 3) == 3) asm volatile("s_addk_i32 s96, %0" ::"i"(f == 7 ? 28672 : 4096));
  }
  if constexpr (R == 4 && Q == 35) asm volatile(FL_POLL ::: "memory");
  if constexpr (R >= 6 && vm >= 0)
    asm volatile("buffer_load_dwordx4 v%c0, s[88:91], s98 offen offset:%1 sc1 lds" ::"i"(86 + vm / 4), "i"((vm & 3) * 1024) : "memory");
  if constexpr (R == 6 && Q == 35) asm volatile("s_add_u32 s98, s98, 0x8000\n\ts_xor_b32 s79, s79, 0x8000");
  if constexpr (R == 7) cv_zs<cv_z0(Q)>(c, make_iseq<cv_z0(Q + 1) - cv_z0(Q)>{});
  __builtin_amdgcn_sched_barrier(0);
}
template <int R, int... Q> __device__ __forceinline__ void cv_step(CV& c, iseq<Q...>) { (cv_slot<R, Q>(c), ...); }
template <int... N> __device__ __forceinline__ void cv_rd0(CV& c, iseq<N...>) { (cv_rd<0, N>(c), ...); }
template <int FLAG> __device__ __forceinline__ void conv_wave(unsigned sz, int ck, float Dd, char* lw) {
  int lane = threadIdx.x & 63; asm volatile("" : "+v"(lane));
  const int l16 = lane & 15, fq = lane >> 4;
  u32x4c w[5]; unsigned w4[5];
#pragma unroll
  for (int r = 0; r < 5; ++r) {
    w[r] = *(const u32x4c*)(lw + r * 1024 + lane * 16);
    w4[r] = *(const unsigned*)(lw + r * 1024 + lane * 16 + 16);
  }
  d_store<NS_X>(); d_store<NS_X + 1>();
#pragma unroll
  for (int r = 0; r < 5; ++r)
    *(u32x4c*)(lw + 10304 + r * 1024 + lane * 16) = (u32x4c){__builtin_amdgcn_alignbit(w[r][1], w[r][0], 16), __builtin_amdgcn_alignbit(w[r][2], w[r][1], 16),
                                                             __builtin_amdgcn_alignbit(w[r][3], w[r][2], 16), __builtin_amdgcn_alignbit(w4[r], w[r][3], 16)};
  d_store<NS_X + 2>();
  CV c;
#pragma unroll
  for (int i = 0; i < 4; ++i) c.pa[i] = (unsigned)(size_t)lw + ((l16 + 1) & 1) * 10304 + 4 * ((8 * fq + l16 + 1) >> 1) + 1024 * i;
  c.pb = (unsigned)(size_t)lw + 5120 + (2032 - 16 * l16 + 8 * fq) * 2 - 512 * 7;
  c.vz = (l16 >> 1) * 4096 + (((l16 & 1) * 2 + (fq >> 1)) ^ (ck >> 1)) * 16 + (fq & 1) * 8;
  c.px = (unsigned)(size_t)lw + 5120 + 2044 * 2 - 512 * 7 - (16 * l16 + 4 * fq) * 2;
  c.sz = sz; c.D = Dd;
  asm volatile("" : "+v"(c.vz), "+v"(c.px), "+v"(c.D) :: "memory");
  asm volatile("" : "+s"(c.sz));
  cv_rd0(c, make_iseq<24>{});
  d_store<NS_X + 3>(); d_store<NS_X + 4>();
  __builtin_amdgcn_sched_barrier(0);
  static_for([&](auto R) __attribute__((always_inline)) {
    cv_step<R>(c, make_iseq<36>{});
    if constexpr (R == 2) {
      asm volatile("s_waitcnt vmcnt(0)\n\ts_barrier" ::: "memory");
      fl_set<FLAG>();
      sset<101>(c.sz);
    }
    if constexpr (R == 5) {
      asm volatile("s_waitcnt vmcnt(0)" ::: "memory");
      fl_check();
    }
    __builtin_amdgcn_sched_barrier(0);
  }, make_iseq<8>{});
  asm volatile("s_nop 7");
  cv_zs<cv_z0(36)>(c, make_iseq<88 - cv_z0(36)>{});
  __builtin_amdgcn_sched_barrier(0);
}

__device__ __forceinline__ void post_dma(int cg, int tb, const bf16* __restrict__ q, unsigned m0w) {
  int lane = threadIdx.x & 63; asm volatile("" : "+v"(lane));
  const __amdgpu_buffer_rsrc_t sq = __builtin_amdgcn_make_buffer_rsrc((void*)(q + (size_t)tb * 32 * DA + cg * 64), (short)0, 32 * DA * 2, 0x00020000);
  const unsigned vq = ((lane >> 3) * DA + (lane & 7) * 8) * 2;
  asm volatile("s_mov_b32 m0, %0\n\ts_nop 4\n\tbuffer_load_dwordx4 %1, %2, 0 offen lds\n\tbuffer_load_dwordx4 %1, %2, %3 offen offset:1024 lds\n\t"
               "buffer_load_dwordx4 %1, %2, %4 offen offset:2048 lds\n\tbuffer_load_dwordx4 %1, %2, %5 offen offset:3072 lds"
               ::"s"(m0w + 4096), "v"(vq), "s"(sq), "s"(8 * DA * 2 - 1024), "s"(16 * DA * 2 - 2048), "s"(24 * DA * 2 - 3072) : "memory");
}
__device__ __forceinline__ void post_math(int cg, int tb, bf16* __restrict__ out, const char* lw) {
  int lane = threadIdx.x & 63; asm volatile("" : "+v"(lane));
  const int tl = lane >> 3, ck = lane & 7;
  const __amdgpu_buffer_rsrc_t so = __builtin_amdgcn_make_buffer_rsrc((void*)(out + (size_t)tb * 32 * OUTW + cg * 64), (short)0, 32 * OUTW * 2 - 2 * DA, 0x00020000);
  const unsigned vo = (tl * OUTW + ck * 8) * 2;
#pragma unroll
  for (int i = 0; i < 4; ++i) {
    const u32x4c qv = *(const u32x4c*)(lw + 4096 + i * 1024 + lane * 16);
    const char* lz = lw + ck * 64 + 16 * (i ^ (ck >> 1)) + tl * 2;
    u32x4c o;
#pragma unroll
    for (int e = 0; e < 4; ++e) {
      const float z0 = bf2f(*(const unsigned short*)(lz + 2 * e * 512));
      const float z1 = bf2f(*(const unsigned short*)(lz + (2 * e + 1) * 512));
      o[e] = pk2(bfl(qv[e]) * z0, bfh(qv[e]) * z1);
    }
    __builtin_amdgcn_raw_buffer_store_b128(o, so, vo + i * 8 * OUTW * 2, 0, 17);
  }
}

#define DI __device__ __forceinline__
#define GAP() __builtin_amdgcn_sched_barrier(0)
typedef __attribute__((ext_vector_type(4))) float f32x4g;
typedef __attribute__((ext_vector_type(4))) unsigned u32x4g;
constexpr int LDW = NH * HD;
struct GM { unsigned aA[2], aB[2][8]; };
template <bool W, int J, int V> DI void g_dma() {
  if constexpr (J == 0) asm volatile("buffer_load_dwordx4 v%c0, s[%c1:%c2], 0 offen lds" ::"i"(V), "i"(W ? 60 : 56), "i"(W ? 63 : 59) : "memory");
  else asm volatile("buffer_load_dwordx4 v%c0, s[%c1:%c2], s%c3 offen lds" ::"i"(V), "i"(W ? 60 : 56), "i"(W ? 63 : 59), "i"((W ? 70 : 63) + J) : "memory");
}
template <int P, int KS, int I> DI void g_wread(GM& g) {
  constexpr int nf = I >> 1, hf = I & 1, r = 192 - 64 * P + 8 * nf + 4 * KS + 2 * hf;
  asm volatile("ds_read_b64_tr_b16 a[%c1:%c2], %0 offset:%c3" ::"v"(g.aB[hf][nf]), "i"(r), "i"(r + 1), "i"(P * 32768 + KS * 16384));
}
template <int P, int M, int KS> DI void g_aread(GM& g) {
  constexpr int r = 96 + 8 * (M & 1) + 4 * KS;
  asm volatile("ds_read_b128 v[%c1:%c2], %0 offset:%c3" ::"v"(g.aA[KS]), "i"(r), "i"(r + 3), "i"(P * 32768 + M * 2048));
}
constexpr float CS = 0.0625f * 1.4426950408889634f;
constexpr int NS_Q = 19;
constexpr int e_vm(int G) { return (G * 3) % 8 < 3 ? G * 3 / 8 : -1; }
constexpr int e_st(int MODE, int G) {
  const int q = G & 15, k3 = ((G >> 4) - 1) * 3 + (q == 0 ? 0 : q == 4 ? 1 : 2), k2 = NS_Q + (G >> 4) * 2 + (q == 4);
  if (MODE == 5) return e_vm(G) >= 32 ? e_vm(G) - 32 : -1;
  if (MODE == 3) return G >= 33 && (G - 33) % 5 == 0 && (G - 33) / 5 < NS_X ? (G - 33) / 5 : -1;
  if (MODE == 2) return G >= 30 && (q == 0 || q == 4 || q == 14) && k3 - 2 < NS_Q ? k3 - 2 : -1;
  if (MODE == 4) return (q == 0 || q == 4) && k2 < 32 ? k2 : -1;
  return -1;
}
constexpr int e_nst(int MODE, int G0, int G1) { int n = 0; for (int G = G0; G < G1; ++G) n += e_st(MODE, G) >= 0; return n; }
static_assert(e_nst(2, 0, 128) == NS_Q && e_nst(3, 30, 128) == NS_X && e_nst(5, 0, 128) == 16 && e_nst(4, 0, 104) == 32 - NS_Q);
constexpr int e_slot(int MODE, int G) { return MODE == 3 ? (G % 3 == 0 && G < 30 ? G / 3 : -1) : e_vm(G) >= 0 && e_vm(G) < 32 ? 10 + e_vm(G) : -1; }
struct ES { int op0[129], nop[129]; };
constexpr ES e_sched(bool TR, int MODE) {
  ES z{};
  const bool SCL = MODE == 2 || MODE == 4;
  const int per = SCL ? 16 : 8, o1 = 32 * per;
  int o = MODE == 4 ? e_sched(false, 2).op0[128] : 0;
  for (int G = 0; G <= 128; ++G) {
    const int q = G & 15;
    int cap = 2 - (q == 1 || q == 2 || q == 15);
    if (SCL) cap -= (q >= 3 && q < 11 && (q & 1)) + (q == 7 || q == 11) + 2 * (G == 70 || G == 74 || (G & 63) == 61);
    cap += MODE == 2 ? 4 + (q == 10 || q == 13) : MODE == 3 ? 1 : MODE == 5 ? 2 : 0;
    if (e_st(MODE, G) >= 0 || (SCL ? q == 8 || q == 12 : e_slot(MODE, G) >= 0) || (MODE == 4 && G < 4)) cap = 0;
    if (G == 128) cap = 10000;
    z.op0[G] = o;
    while (o < o1) {
      const int i = o / per, rdy = MODE == 4 ? 0 : (TR ? 32 * (i >> 3) + 16 + 2 * (i & 7) + 1 : 4 * i + 3) + 6;
      if ((G < rdy && G < 128) || cap-- <= 0) break;
      ++o; ++z.nop[G];
    }
  }
  return z;
}
template <bool TR, int MODE> constexpr ES ESCH = e_sched(TR, MODE);
static_assert(ESCH<false, 4>.op0[96] == 512 && ESCH<false, 4>.op0[0] >= 24 * 16);
template <bool TR, bool SCL, int O> DI void e_op() {
  constexpr int per = SCL ? 16 : 8, i = O / per, k = O % per, r = 128 + 4 * i, t = 114 + 4 * (i & 1), c = k - (per - 4);
  if constexpr (k < 4) asm volatile("v_accvgpr_read_b32 v%c0, a[%c1]" ::"i"(t + k), "i"(4 * i + k));
  else if constexpr (c < 0) asm volatile("v_mul_f32 v%c0, s83, v%c0" ::"i"(k < 8 ? r + (k ^ 6) : t + k - 8));
  else {
    constexpr int d = c == 0 ? r + 3 : c == 1 ? r + 2 : r + c - 2, x = c == 0 ? r + 2 : c == 1 ? r : t + 2 * (c - 2);
    asm volatile("v_cvt_pk_bf16_f32 v%c0, v%c1, v%c2" ::"i"(d), "i"(x), "i"(x + 1));
  }
}
template <bool TR, bool SCL, int B, int... Is> DI void e_ops(iseq<Is...>) { (e_op<TR, SCL, B + Is>(), ...); }
template <int S> DI void e_m0() {
  if constexpr (S == 0 || S == 1 || S == 5 || S == 9) asm volatile("s_add_u32 m0, s78, %0" ::"i"(S * 1024));
  if constexpr (S >= 26 && (S - 26) % 4 == 0) asm volatile("s_add_u32 m0, s79, %0" ::"i"(((S - 26) >> 3) * 32768 + ((S - 26) & 4) * 1024));
}
template <int S> DI void e_load() {
  if constexpr (S == 0) asm volatile("buffer_load_dwordx4 v122, s[56:59], 0 offen lds" ::: "memory");
  else if constexpr (S < 5) asm volatile("buffer_load_dwordx4 v84, s[56:59], s64 offen offset:%0 lds" ::"i"((S - 1) * 1024) : "memory");
  else if constexpr (S < 9) asm volatile("buffer_load_dwordx4 v84, s[60:63], 0 offen offset:%0 sc1 lds" ::"i"((S - 5) * 1024) : "memory");
  else if constexpr (S == 9) asm volatile("buffer_load_dwordx4 v84, s[60:63], s62 offen sc1 lds" ::: "memory");
  else if constexpr (S < 26) {
    constexpr int f = S - 10;
    asm volatile("buffer_load_dwordx4 a[%c0:%c1], v84, s[88:91], s96 offen offset:%2 sc1" ::"i"(128 + 4 * f), "i"(131 + 4 * f), "i"((f & 3) * 1024) : "memory");
    if constexpr ((f & 3) == 3) asm volatile("s_addk_i32 s96, %0" ::"i"(f == 7 ? 28672 : 4096));
  } else {
    constexpr int f = S - 26;
    asm volatile("buffer_load_dwordx4 v%c0, s[88:91], s98 offen offset:%1 sc1 lds" ::"i"(86 + ((f >> 2) & 1)), "i"((f & 3) * 1024) : "memory");
    if constexpr (f == 7) asm volatile("s_add_u32 s98, s98, s97");
    if constexpr (f == 15) asm volatile(FL_POLL ::: "memory");
  }
}
template <bool TR, int P, int H, int G, int MODE> DI void g_gap(GM& g) {
  constexpr int m = 4 * H + (G >> 4), q = G & 15, n = q >> 1, ks = q & 1, GG = 64 * H + G;
  constexpr bool inV = TR ? m & 1 : n & 1, END = MODE == 3 || MODE == 5;
  constexpr int a = 96 + 8 * (m & 1) + 4 * ks, b = 192 - 64 * P + 8 * n + 4 * ks, d = (inV ? 128 : 0) + 4 * (TR ? 8 * (m >> 1) + n : 4 * m + (n >> 1));
  constexpr int J = END ? -1 : q == 8 ? 2 * (G >> 4) : q == 12 ? 2 * (G >> 4) + 1 : -1, gj = (J & 3) ^ ((J & 3) >> 1);
  if constexpr (END) { if constexpr (q == 0 && GG > 0) asm volatile("s_waitcnt lgkmcnt(0)"); e_m0<e_slot(MODE, GG)>(); }
  else if constexpr (q == 0 && G > 0) asm volatile("s_waitcnt lgkmcnt(4)");
  if constexpr (J == 0) asm volatile("s_add_u32 m0, s78, %0" ::"i"((P ^ H ^ 1) * 32768 + (1 - H) * 8192));
  else if constexpr (J == 4) asm volatile("s_add_u32 m0, s79, %0" ::"i"((P ^ H) * 32768 + (1 - H) * 16384));
  else if constexpr (J > 0 && (J & 3) == 1) asm volatile("s_bitset1_b32 m0, 12");
  else if constexpr (J > 0 && (J & 3) == 2) asm volatile("s_bitset1_b32 m0, %0" ::"i"(J < 4 ? 14 : 13));
  else if constexpr (J > 0) asm volatile("s_bitset0_b32 m0, 12");
#define GMF(D, X, Y, C) asm volatile("v_mfma_f32_16x16x32_bf16 " D "[%c0:%c1], " X ", " Y ", " C ::"i"(d), "i"(d + 3), "i"(a), "i"(a + 3), "i"(b), "i"(b + 3))
#define GMF2(D, C) do { if constexpr (TR) GMF(D, "v[%c2:%c3]", "a[%c4:%c5]", C); else GMF(D, "a[%c4:%c5]", "v[%c2:%c3]", C); } while (0)
  if constexpr ((MODE == 1 || MODE == 4) && ks == 0) { if constexpr (inV) GMF2("v", "0"); else GMF2("a", "0"); }
  else if constexpr (inV) GMF2("v", "v[%c0:%c1]");
  else GMF2("a", "a[%c0:%c1]");
#undef GMF2
#undef GMF
  if constexpr ((q == 1 || q == 2) && !(END && m == 7)) g_aread<(m < 7 ? P : P ^ 1), (m + 1) & 7, q - 1>(g);
  if constexpr (!END && q >= 3 && q < 11 && (q & 1)) g_wread<P ^ 1, H, (G >> 4) * 4 + (q - 3) / 2>(g);
  if constexpr (e_st(MODE, GG) >= 0) {
    static_assert(ESCH<TR, MODE>.op0[GG] >= (e_st(MODE, GG) + 1) * (END ? 8 : 16));
    d_store<e_st(MODE, GG)>();
  }
  if constexpr (J >= 0 && J < 4) g_dma<false, (1 - H) * 2 + (gj & 1) + (gj >> 1) * 4, 90>();
  else if constexpr (J >= 4) g_dma<true, (1 - H) * 4 + gj, 91>();
  if constexpr (MODE >= 2) e_ops<TR, !END, ESCH<TR, MODE>.op0[GG]>(make_iseq<ESCH<TR, MODE>.nop[GG]>{});
  if constexpr (END) {
    if constexpr (e_slot(MODE, GG) >= 0) e_load<e_slot(MODE, GG)>();
  } else {
    if constexpr (H == 1 && G == 6) asm volatile("v_lshl_add_u32 v90, s82, 7, v92\n\ts_and_b32 s82, s80, 15");
    if constexpr (H == 1 && G == 10) asm volatile("s_lshr_b64 s[80:81], s[80:81], 4\n\tv_lshl_add_u32 v91, s82, 19, v93");
    if constexpr (G == 61) {
      constexpr int nst = e_nst(MODE, H ? 29 : 0, GG + 1) + (MODE == 4 && !H ? e_nst(2, 93, 128) : 0);
      asm volatile("s_waitcnt vmcnt(%0) lgkmcnt(0)\n\ts_barrier" ::"i"(12 + nst) : "memory");
    }
  }
  if constexpr (END && GG == 127) {
    asm volatile("s_nop 7");
    e_ops<TR, false, ESCH<TR, MODE>.op0[128]>(make_iseq<ESCH<TR, MODE>.nop[128]>{});
  }
  GAP();
}
template <bool TR, int P, int H, int MODE, int... Gs> DI void g_half_(GM& g, iseq<Gs...>) { (g_gap<TR, P, H, Gs, MODE>(g), ...); }
template <bool TR, int P, int H, int MODE> DI void g_half(GM& g) { g_half_<TR, P, H, MODE>(g, make_iseq<64>{}); }
template <bool TR, int P, int MODE> DI void g_tile(GM& g) { g_half<TR, P, 0, MODE>(g); g_half<TR, P, 1, MODE>(g); }
template <int P, int... Is> DI void g_read0(GM& g, iseq<Is...>) {
  (g_wread<P, 0, Is>(g), ...); (g_wread<P, 1, Is>(g), ...);
  g_aread<P, 0, 0>(g); g_aread<P, 0, 1>(g);
}
DI void g_proW(unsigned g, unsigned mW) {
  asm volatile("v_lshl_add_u32 v91, %0, 18, v93\n\ts_mov_b32 m0, %1" ::"s"(g), "s"(mW));
  asm volatile("s_nop 0"); g_dma<true, 0, 91>();
  asm volatile("s_bitset1_b32 m0, 12\n\ts_nop 0"); g_dma<true, 1, 91>();
  asm volatile("s_bitset1_b32 m0, 13\n\ts_nop 0"); g_dma<true, 3, 91>();
  asm volatile("s_bitset0_b32 m0, 12\n\ts_nop 0"); g_dma<true, 2, 91>();
}
template <int PART = 3> DI void g_pro(unsigned g, unsigned mA, unsigned mW, unsigned lda) {
  g &= 3;
  if constexpr (PART & 1)
    asm volatile("v_add_u32 v90, %0, v92\n\ts_mov_b32 m0, %1" ::"s"((g >> 1) * 128 + (g & 1) * 128 * lda), "s"((mA ^ (g >> 1) * 32768) + (g & 1) * 8192));
  if constexpr (PART == 1) return;
  asm volatile("s_nop 0"); g_dma<false, 0, 90>();
  asm volatile("s_bitset1_b32 m0, 12\n\ts_nop 0"); g_dma<false, 1, 90>();
  asm volatile("s_bitset1_b32 m0, 14\n\ts_nop 0"); g_dma<false, 5, 90>();
  asm volatile("s_bitset0_b32 m0, 12\n\ts_nop 0"); g_dma<false, 4, 90>();
  g_proW(g, mW ^ g * 16384);
}
template <bool TR, bool SCL, class HOOK> DI void g_loop(GM& g, unsigned evm, HOOK&& hook) {
  constexpr int nkt = SCL ? 7 : 16, P0 = SCL ? 0 : 1;
  asm volatile("" : "+s"(evm));
#pragma unroll 1
  for (int t = 1;; t += 2) {
    g_half<TR, P0 ^ 1, 0, 0>(g);
    if (__builtin_expect((evm >> t) & 1, 0)) { hook(t); GAP(); }
    g_half<TR, P0 ^ 1, 1, 0>(g);
    if (SCL && t + 2 >= nkt) break;
    g_half<TR, P0, 0, 0>(g);
    if constexpr (SCL) { if (__builtin_expect(t == 3, 0)) { hook(4); GAP(); } }
    else asm volatile("s_cmp_eq_u32 %0, 13\n\ts_cselect_b32 s78, 0x28000, s78" ::"s"(t) : "scc");
    g_half<TR, P0, 1, 0>(g);
    if (!SCL && t + 3 >= nkt) break;
  }
}

typedef __attribute__((ext_vector_type(4))) unsigned u32x4;
typedef __attribute__((ext_vector_type(4))) float f32x4;

constexpr int R_S = 128, R_P = 192, R_CI = 224, R_E = 96;
constexpr int R_TS = 112, R_RS = 114, R_M = 116;
constexpr int R_PD = 118, R_THR = 120, R_TMP = 121;
constexpr int r_dk(int q) { return q < 6 ? 90 + q : 120 + q; }
constexpr int S_V = 96, S_T = 97, S_K = 98, S_I = 99, S_EV = 100;
struct AC {
  unsigned rare;
  unsigned tb, vs, o0, o1;
};
DI bool redo() { u64 t; asm volatile("s_mov_b64 %0, s[54:55]" : "=s"(t)); return t != 0; }
template <int X, int F> DI void kread() {
  constexpr int r = 192 + 4 * (F & 15);
  asm volatile("ds_read_b128 a[%c0:%c1], v84 offset:%c2" ::"i"(r), "i"(r + 3), "i"(X * 32768 + F * 1024));
}
template <int X, int F> DI void vread() {
  constexpr int r = 192 + 4 * (F & 15);
  asm volatile("ds_read_b128 a[%c0:%c1], v85 offset:%c2" ::"i"(r), "i"(r + 3), "i"(X * 32768 + (F & 1) * 16384 + (F >> 1) * 1024));
}
template <int X, int... Fs> DI void kreads(iseq<Fs...>) { (kread<X, Fs>(), ...); }
template <int X, int J> DI void dma_m0() {
  if constexpr (J == 0) asm volatile("s_mov_b32 m0, s%c0" ::"i"(78 + X));
  else if constexpr (J == 4) asm volatile("s_or_b32 m0, s%c0, 0x1000" ::"i"(78 + X));
  else if constexpr (J == 8) asm volatile("s_bitset0_b32 m0, 16");
  else if constexpr (J == 12) asm volatile("s_bitset0_b32 m0, 12");
}
template <int J> DI void dma_at() {
  if constexpr (J < 8)
    asm volatile("buffer_load_dwordx4 v%c0, s[88:91], s96 offen offset:%1 sc1 lds" ::"i"(86 + (J >= 4)), "i"((J & 3) * 1024) : "memory");
  else
    asm volatile("buffer_load_dwordx4 v%c0, s[92:95], s98 offen offset:%1 sc1 lds" ::"i"(r_dk(J - 8)), "i"((J & 3) * 1024) : "memory");
}
template <int Q> DI void dma4k() {
  asm volatile("s_xor_b32 m0, s79, %0\n\ts_add_u32 s48, s98, %1\n\tbuffer_load_dwordx4 v%c2, s[88:91], s48 offen sc1 lds\n\t"
               "buffer_load_dwordx4 v%c2, s[88:91], s48 offen offset:1024 sc1 lds\n\tbuffer_load_dwordx4 v%c2, s[88:91], s48 offen offset:2048 sc1 lds\n\t"
               "buffer_load_dwordx4 v%c2, s[88:91], s48 offen offset:3072 sc1 lds"
               ::"i"((Q >> 1) * 32768 + (Q & 1) * 4096), "i"((Q >> 1) * 32768), "i"(86 + (Q & 1)) : "memory", "scc");
}
DI void acc_zero() { static_for([](auto I) { asm volatile("v_accvgpr_write_b32 a[%c0], 0" ::"i"((int)I)); }, make_iseq<128>{}); }
#define SMV 0
struct SMS { int ex[128], op0[128], nop[128], last; };
constexpr SMS sm_sched() {
  SMS z{};
  int et[32] = {}, e = 0;
  for (int t = 0; t < 128; ++t) {
    const int g = t & 15;
    const bool x = t < 48 ? (g >= 8 && (g & 1) && g < 15) : t < 64 ? (g >= 9 && g < 15) : (g >= 8 && g < 14);
    z.ex[t] = x && e < 32 ? e : -1;
    if (z.ex[t] >= 0) et[e++] = t;
  }
  int o = 0;
  for (int t = 0; t < 128; ++t) {
    const int g = t & 15;
    int cap = z.ex[t] >= 0 || t < 8 || (t < 48 && (g == 7 || (g >= 8 && !(g & 1)))) || t == 56 || t == 63 || t == 111 ? 0 : 1;
    z.op0[t] = o; z.nop[t] = 0;
    while (cap > 0 && o < 48 && et[2 * (o / 3) + (o % 3 == 0 ? 0 : 1)] + 2 <= t) {
      const int el = 2 * (o / 3) + o % 3;
      if (!(o % 3 < 2 && (el == 0 || el == 4))) --cap;
      ++o; ++z.nop[t]; z.last = t;
    }
  }
  for (int k = 16; k < 32; ++k) if (z.op0[et[k]] < 3 * ((k - 16) / 2 + 1)) z.last = 1000;
  return z;
}
constexpr SMS SM = sm_sched();
static_assert(SM.last <= 117);
constexpr int p_reg(int Y, int k) { return R_P + 16 * Y + 4 * ((k >> 3) * 2 + ((k >> 1) & 1)) + ((k >> 2) & 1) * 2 + (k & 1); }
template <int Y, int O> DI void sm_op() {
  constexpr int k = O / 3, e = 2 * k + O % 3, ts = R_TS + ((e >> 2) & 1);
  if constexpr (O % 3 == 2) asm volatile("v_cvt_pk_bf16_f32 v%c0, v%c1, v%c2" ::"i"(p_reg(Y, k)), "i"(R_E + ((2 * k) & 15)), "i"(R_E + ((2 * k + 1) & 15)));
  else if constexpr (e == 1 || e == 5) asm volatile("v_add_f32_e32 v%c0, v%c1, v%c2" ::"i"(ts), "i"(R_E + e - 1), "i"(R_E + e));
  else if constexpr (e != 0 && e != 4) asm volatile("v_add_f32_e32 v%c0, v%c0, v%c1" ::"i"(ts), "i"(R_E + (e & 15)));
}
template <int Y, int T> DI void sm_gap(AC& c) {
  if constexpr (SM.ex[T] >= 0) asm volatile("v_exp_f32 v%c0, v%c1" ::"i"(R_E + (SM.ex[T] & 15)), "i"(R_S + 32 * Y + SM.ex[T]));
  if constexpr (SM.nop[T] > 0) sm_op<Y, SM.op0[T]>();
  if constexpr (SM.nop[T] > 1) sm_op<Y, SM.op0[T] + 1>();
  if constexpr (SM.nop[T] > 2) sm_op<Y, SM.op0[T] + 2>();
  if constexpr (T == SM.last + 1) asm volatile("v_add_f32_e32 v%c0, v%c1, v%c2" ::"i"(R_TMP), "i"(R_TS), "i"(R_TS + 1));
  if constexpr (T == SM.last + 2) asm volatile("v_cmp_nle_f32_e64 s[54:55], v%c0, v%c1" ::"i"(R_TMP), "i"(R_THR));
}
template <int X, int MODE, int G> DI void gapA(AC& c) {
  constexpr bool FIRST = MODE == 1 || MODE == 2, NOQK = MODE == 3;
  constexpr int Xp = X ^ 1, kb = G >> 4, g = G & 15, qb = g >> 3, s = g & 7;
  if constexpr (MODE == 3 && G < 48 && G % 3 == 0) {
    constexpr int f = G / 3;
    asm volatile("buffer_load_dwordx4 a[%c0:%c1], v84, s[88:91], s75 offen offset:%2 sc1" ::"i"(128 + 4 * f), "i"(131 + 4 * f), "i"((f & 3) * 1024) : "memory");
    if constexpr ((f & 3) == 3) asm volatile("s_addk_i32 s75, %0" ::"i"(f == 7 ? 28672 : 4096));
  }
  if constexpr (G == 48) asm volatile("s_waitcnt vmcnt(%0) lgkmcnt(0)\n\ts_barrier" ::"i"(MODE == 3 ? 16 : MODE == 2 ? 12 : MODE == 1 ? 7 : 0) : "memory");
  else if constexpr (g == 0 && !NOQK) asm volatile("s_waitcnt lgkmcnt(0)");
  if constexpr (!NOQK) {
    constexpr int r = 192 + 4 * ((kb & 1) * 8 + s), q = 128 + 4 * g;
    constexpr int d = R_S + 32 * X + 4 * (kb * 2 + qb), ci = s == 0 ? R_CI + 4 * (kb * 2 + qb) : d;
    if constexpr (FIRST && s == 0)
      asm volatile("v_mfma_f32_16x16x32_bf16 v[%c0:%c1], a[%c2:%c3], a[%c4:%c5], 0" ::"i"(d), "i"(d + 3), "i"(r), "i"(r + 3), "i"(q), "i"(q + 3));
    else
      asm volatile("v_mfma_f32_16x16x32_bf16 v[%c0:%c1], a[%c2:%c3], a[%c4:%c5], v[%c6:%c7]" ::"i"(d), "i"(d + 3), "i"(r), "i"(r + 3), "i"(q), "i"(q + 3),
                   "i"(ci), "i"(ci + 3));
    if constexpr (g < 8 && kb < 3) kread<X, (kb + 1) * 8 + g>();
    constexpr int S0 = MODE == 1 ? 0 : SMV ? 40 : 14, S1 = MODE == 1 ? 1 : SMV ? 41 : 30, S2 = SMV ? 42 : 45, S3 = SMV ? 43 : 46;
    if constexpr (G == S0) asm volatile("s_add_u32 s96, s97, 0x1000000");
    if constexpr (G == S1) dma_m0<X, 0>();
    if constexpr (G == S2) asm volatile("s_mov_b32 s97, s98");
    if constexpr (G == S3) asm volatile("s_add_u32 s98, s98, s99");
    if constexpr (MODE != 1 && G >= 56 && !(G & 1)) dma_at<(G - 56) / 2>();
    if constexpr (MODE == 1 && (G & 11) == 9) dma_at<(G >> 4) * 2 + ((G >> 2) & 1)>();
    if constexpr (MODE == 1 && G == 31) dma_m0<X, 4>();
    if constexpr (G == 57) asm volatile("s_lshr_b32 s100, s100, 1\n\ts_add_u32 s56, s56, 1");
    if constexpr (G == 59) asm volatile("s_getpc_b64 s[86:87]\nev_pc%c0:\n\ts_add_u32 s86, s86, ev_ret%c0-ev_pc%c0\n\ts_addc_u32 s87, s87, 0" ::"i"(X * 4 + MODE));
    if constexpr (G == 61 && !FIRST) asm volatile("s_or_b32 s57, s54, s55\n\ts_mov_b32 %0, s57\n\ts_bitcmp1_b32 s100, 0" : "=s"(c.rare));
    if constexpr (G == 61 && FIRST) asm volatile("s_bitcmp1_b32 s100, 0");
  } else if constexpr (SM.nop[64 + G] > 0) asm volatile("s_nop 0");
  if constexpr (!FIRST && g < 8 && kb == 3) vread<Xp, g>();
  if constexpr (MODE == 2 && (G & 3) == 0) d_store<16 + G / 4>();
  if constexpr (!FIRST) sm_gap<Xp, 64 + G>(c);
  if constexpr (G == 63 && !NOQK) asm volatile("s_cbranch_scc1 ev_entry\nev_ret%c0:" ::"i"(X * 4 + MODE) : "vcc", "memory");
  GAP();
}
template <int R> DI void rescale1(float alpha) {
  float t;
  asm volatile("v_accvgpr_read_b32 %0, a[%c2]\n\tv_mul_f32 %0, %0, %1\n\tv_accvgpr_write_b32 a[%c2], %0" : "=&v"(t) : "v"(alpha), "i"(R));
}
template <int... Rs> DI void rescale_all(const float (&alpha)[2], iseq<Rs...>) { (rescale1<Rs>(alpha[(Rs >> 2) & 1]), ...); }
template <bool MAX> DI float row_reduce(float v) {
  auto p = __builtin_amdgcn_permlane32_swap(__float_as_uint(v), __float_as_uint(v), false, false);
  v = MAX ? fmaxf(__uint_as_float(p[0]), __uint_as_float(p[1])) : __uint_as_float(p[0]) + __uint_as_float(p[1]);
  p = __builtin_amdgcn_permlane16_swap(__float_as_uint(v), __float_as_uint(v), false, false);
  return MAX ? fmaxf(__uint_as_float(p[0]), __uint_as_float(p[1])) : __uint_as_float(p[0]) + __uint_as_float(p[1]);
}
template <int Y, int QB> DI float row_max() {
  constexpr int r = R_S + 32 * Y + 4 * QB;
  float mx;
  asm volatile("v_max_f32 %0, v%c1, v%c2\n\tv_max3_f32 %0, %0, v%c3, v%c4" : "=&v"(mx) : "i"(r), "i"(r + 1), "i"(r + 2), "i"(r + 3));
  static_for([&](auto K) {
    constexpr int q = r + 8 * (K + 1);
    float t = mx;
    asm volatile("v_max3_f32 %0, %0, v%c1, v%c2\n\tv_max3_f32 %0, %0, v%c3, v%c4" : "+v"(t) : "i"(q), "i"(q + 1), "i"(q + 2), "i"(q + 3));
    mx = t;
  }, make_iseq<3>{});
  return row_reduce<true>(mx);
}
template <int R> DI void v_sub(float d) { asm volatile("v_sub_f32 v%c0, v%c0, %1" ::"i"(R), "v"(d)); }
template <int B> DI void s_shift(const float (&d)[2]) { static_for([&](auto E) { v_sub<B + E>(d[(E >> 2) & 1]); }, make_iseq<32>{}); }
template <bool CI> DI void m_shift(const float (&d)[2]) {
  if constexpr (CI) s_shift<R_CI>(d);
  v_sub<R_M>(-d[0]); v_sub<R_M + 1>(-d[1]);
}
DI void set_mask0() {
  const int mr = __float_as_int(vget<89>());
  static_for([&](auto I) {
    constexpr int e = (I >> 2) * 8 + (I & 3), k = (I >> 2) * 16 + (I & 3);
    const float x = vget<R_S + e>();
    vset<R_S + e>(k > mr ? -INFINITY : x);
  }, make_iseq<16>{});
}
template <int X, bool CI = true> DI void sm_redo() {
  constexpr int Xp = X ^ 1;
  float d[2], dt[2], alpha[2], ts[2] = {0.f, 0.f};
  static_for([&](auto Q) {
    constexpr int qb = Q;
    const float pd = vget<R_PD + qb>();
    d[qb] = fmaxf(row_max<Xp, qb>() - pd, 0.f);
    dt[qb] = d[qb] + pd;
    vset<R_PD + qb>(d[qb]);
    alpha[qb] = __builtin_amdgcn_exp2f(-d[qb]);
    vset<R_RS + qb>(vget<R_RS + qb>() * alpha[qb]);
  }, make_iseq<2>{});
  vset<R_THR>(d[0] > 0.f || d[1] > 0.f ? -1.f : 4096.f);
  m_shift<CI>(d);
  static_for([&](auto K) {
    constexpr int k = K, qb = (k >> 1) & 1;
    const float e0 = __builtin_amdgcn_exp2f(vget<R_S + 32 * Xp + 2 * k>() - dt[qb]), e1 = __builtin_amdgcn_exp2f(vget<R_S + 32 * Xp + 2 * k + 1>() - dt[qb]);
    ts[qb] += e0; ts[qb] += e1;
    vset<p_reg(Xp, k)>(__uint_as_float(pk2(e0, e1)));
  }, make_iseq<16>{});
  vset<R_TS>(ts[0]); vset<R_TS + 1>(ts[1]);
  rescale_all(alpha, make_iseq<128>{});
  asm volatile("s_nop 1" ::: "memory");
  GAP();
}
template <int X, int MODE, int... Gs> DI void phaseA_(AC& c, iseq<Gs...>) { (gapA<X, MODE, Gs>(c), ...); }
template <int X, int MODE> DI void phaseA(AC& c) { phaseA_<X, MODE>(c, make_iseq<64>{}); }

template <int X, bool SMX, int G, bool FIRST = false> DI void gapB(AC& c) {
  constexpr int Xp = X ^ 1, blk = G >> 4, g = G & 15;
  if constexpr (g == 0) asm volatile("s_waitcnt lgkmcnt(0)");
  constexpr int qb = (g >> 1) & 1, s = g & 1, r = 192 + 4 * ((blk & 1) * 8 + (g >> 2) * 2 + s), o = 4 * ((blk * 4 + (g >> 2)) * 2 + qb);
  constexpr int p = R_P + 16 * Xp + 4 * (s * 2 + qb);
  if constexpr (FIRST && s == 0)
    asm volatile("v_mfma_f32_16x16x32_bf16 a[%c0:%c1], a[%c2:%c3], v[%c4:%c5], 0" ::"i"(o), "i"(o + 3), "i"(r), "i"(r + 3), "i"(p), "i"(p + 3));
  else
  asm volatile("v_mfma_f32_16x16x32_bf16 a[%c0:%c1], a[%c2:%c3], v[%c4:%c5], a[%c0:%c1]" ::"i"(o), "i"(o + 3), "i"(r), "i"(r + 3), "i"(p), "i"(p + 3));
  if constexpr (g < 8) {
    if constexpr (blk < 3) vread<Xp, (blk + 1) * 8 + g>();
    else if constexpr (SMX) kread<Xp, g>();
  }
  if constexpr (SMX) {
    if constexpr (G == 1 || G == 2) asm volatile("v_add_f32_e32 v%c0, v%c0, v%c1" ::"i"(R_RS + G - 1), "i"(R_TS + G - 1));
    if constexpr (g == 7 && blk < 3) dma_m0<X, blk * 4 + 4>();
    if constexpr (g >= 8 && !(g & 1) && blk < 3) dma_at<blk * 4 + 4 + (g - 8) / 2>();
    if constexpr (G == 55) asm volatile("s_cmp_eq_u32 s56, s76");
    if constexpr (G == 3) asm volatile("s_lshr_b32 s77, s77, 1");
    if constexpr (G == 4) asm volatile("s_bitcmp1_b32 s77, 0");
    if constexpr (G == 5) asm volatile("s_cselect_b64 s[52:53], s[62:63], 0");
    if constexpr (G == 6) asm volatile(FL_ARM("s[52:53]"));
    if constexpr (G == 56) asm volatile(FL_POLL ::: "memory");
    sm_gap<X, G>(c);
  }
  GAP();
}
template <int X, bool FIRST, int... Gs> DI void phaseB_(AC& c, iseq<Gs...>) { (gapB<X, true, Gs, FIRST>(c), ...); }
template <int X, bool FIRST = false> DI void phaseB(AC& c) { phaseB_<X, FIRST>(c, make_iseq<64>{}); }
constexpr int sm_nex() { int n = 0; for (int t = 0; t < 64; ++t) n += SM.ex[t] >= 0; return n; }
template <int X> DI void phaseB0(unsigned mode, bool vh) {
  dma_m0<X, 4>();
  kreads<X ^ 1>(make_iseq<8>{});
  if (vh) { dma_at<4>(); dma_at<5>(); dma_at<6>(); dma_at<7>(); }
  dma_m0<X, 8>();
  asm volatile("s_nop 3");
  float d[2];
  if (mode & 2) {
    d[0] = vget<122>(); d[1] = vget<123>();
    dma_at<8>();
  } else {
    if constexpr (X == 0) if (__builtin_expect(mode & 4, 0)) set_mask0();
    d[0] = row_max<X, 0>(); GAP(); dma_at<8>();
    d[1] = row_max<X, 1>();
#pragma unroll
    for (int qb = 0; qb < 2; ++qb) if (!(d[qb] > -3.0e38f)) d[qb] = 0.f;
  }
  GAP();
  vset<R_M>(d[0]); vset<R_M + 1>(d[1]);
  dma_at<9>();
  const f32x2c nd[2] = {{-d[0], -d[0]}, {-d[1], -d[1]}};
  static_for([&](auto E) {
    constexpr int e = E;
    const f32x2c v = nd[(e >> 1) & 1];
    asm volatile("v_pk_add_f32 v[%c0:%c1], v[%c0:%c1], %4\n\tv_mov_b64 v[%c2:%c3], %4" ::"i"(R_S + 32 * X + 2 * e), "i"(R_S + 32 * X + 2 * e + 1), "i"(R_CI + 2 * e), "i"(R_CI + 2 * e + 1), "v"(v));
    if constexpr (e == 7 || e == 15) dma_at<10 + e / 8>();
  }, make_iseq<16>{});
  static_assert(SM.op0[64] >= 3 * ((sm_nex() - 15) / 2) && SM.op0[64] <= 24);
  static_for([&](auto E) {
    constexpr int e = E;
    if constexpr (e < sm_nex()) asm volatile("v_exp_f32 v%c0, v%c1" ::"i"(R_E + e), "i"(R_S + 32 * X + e));
    if constexpr (e == 2) dma_m0<X, 12>();
    if constexpr (e == 5 || e == 10 || e == 15) dma_at<11 + e / 5>();
  }, make_iseq<16>{});
  static_for([&](auto O) { sm_op<X, O>(); }, make_iseq<SM.op0[64]>{});
  dma_at<15>();
  static_for([&](auto E) { asm volatile("v_exp_f32 v%c0, v%c1" ::"i"(R_E + (int)E), "i"(R_S + 32 * X + 16 + (int)E)); }, make_iseq<(sm_nex() > 16 ? sm_nex() - 16 : 0)>{});
  asm volatile("buffer_store_dwordx2 v[%c0:%c1], v88, s[88:91], s76 offen sc1\n\ts_bitcmp1_b32 s77, 0\n\ts_cselect_b64 s[52:53], s[62:63], 0\n\t"
               FL_ARM("s[52:53]") "\n\t" FL_POLL ::"i"(R_M), "i"(R_M + 1) : "memory", "scc");
  GAP();
}
enum : int { T_Q0, T_K0, T_OQ, T_OG, T_FPREP, T_FQ, T_FG, T_FS, T_ZO, T_CNT, T_NPRE = 10, T_NST = 12, T_TAB = 14, T_SLAB = 16, T_PART, T_IT = 18, T_ITN = 23 };
#define MSK(R, K) "v_cmp_gt_i32 s[48:49], " #K ", v89\n\tv_cmp_gt_i32 s[50:51], " #K "+1, v89\n\tv_cmp_gt_i32 s[52:53], " #K "+2, v89\n\t" \
                  "v_cmp_gt_i32 s[54:55], " #K "+3, v89\n\tv_cndmask_b32_e64 v[" #R "], v[" #R "], v122, s[48:49]\n\t" \
                  "v_cndmask_b32_e64 v[" #R "+1], v[" #R "+1], v122, s[50:51]\n\tv_cndmask_b32_e64 v[" #R "+2], v[" #R "+2], v122, s[52:53]\n\t" \
                  "v_cndmask_b32_e64 v[" #R "+3], v[" #R "+3], v122, s[54:55]\n\t"
DI void ev_code(AC& c) {
  GAP();
  asm volatile(
    "s_getpc_b64 s[82:83]\n\ts_add_u32 s82, s82, 12\n\ts_addc_u32 s83, s83, 0\n\ts_branch 9f\nev_entry:\n\t"
    "s_bitcmp1_b32 s60, s56\n\ts_cbranch_scc1 3f\n\ts_bitcmp1_b32 s74, s56\n\ts_cbranch_scc1 2f\n\t"
    "s_bitcmp1_b32 s58, s56\n\ts_cselect_b64 vcc, -1, 0\n\tv_cndmask_b32 v121, -2.0, v81, vcc\n\t"
    "buffer_store_dwordx2 v[82:83], v121, s[88:91], s70 offen sc1\n\tbuffer_store_dwordx2 v[82:83], v121, s[88:91], s71 offen sc1\n\t"
    "v_cmp_eq_u64 vcc, s[84:85], v[124:125]\n\ts_andn2_b64 vcc, s[62:63], vcc\n\ts_cbranch_scc1 5f\n"
    "0:\n\ts_add_u32 s98, s98, s59\n\ts_mov_b32 s59, s61\n\ts_mov_b32 s61, s72\n\ts_mov_b64 s[62:63], s[64:65]\n\ts_mov_b64 s[64:65], s[66:67]\n\t"
    "s_mov_b64 s[66:67], s[68:69]\n\ts_cmp_eq_u32 s56, 1\n\ts_cbranch_scc1 ev_first\n\ts_setpc_b64 s[86:87]\n"
    "5:\n\t" FL_CHECK("s[62:63]") "\n\ts_branch 0b\n"
    "6:\n\t" FL_CHECK("s[62:63]") "\n\ts_setpc_b64 s[86:87]\n"
    "2:\n\ts_mov_b32 s98, 0\n\t"
    "v_lshrrev_b32 v90, 7, v86\n\tv_and_b32 v91, 6, v90\n\tv_lshlrev_b32 v91, 4, v91\n\tv_and_b32 v92, 0x70, v86\n\tv_xor_b32 v91, v91, v92\n\t"
    "s_movk_i32 s48, 896\n\tv_mad_u32_u24 v94, v90, s48, v91\n\ts_mov_b64 s[92:93], s[80:81]\n\ts_mov_b32 s94, 229376\n\t"
    "v_add_u32 v90, 28672, v94\n\tv_add_u32 v91, 34816, v94\n\tv_add_u32 v92, 40960, v94\n\tv_add_u32 v93, 47104, v94\n\t"
    "v_add_u32 v95, 6144, v94\n\tv_add_u32 v126, 12288, v94\n\tv_add_u32 v127, 18432, v94\n\t"
    "v_mov_b32 v122, 0xff800000\n\t" MSK(228, 0) MSK(236, 16) MSK(244, 32) MSK(252, 48)
    "v_cmp_eq_u64 vcc, s[84:85], v[124:125]\n\ts_andn2_b64 vcc, s[62:63], vcc\n\ts_cbranch_scc1 6b\n\ts_setpc_b64 s[86:87]\n"
    "3:\n\tv_mov_b32 v122, 0xff800000\n\t" MSK(224, 0) MSK(232, 16) MSK(240, 32) MSK(248, 48)
    "v_readlane_b32 s54, v78, %0\n\ts_add_u32 m0, s78, 0x10000\n\t"
    "s_bfe_u32 s53, s73, 0x80010\n\ts_mulk_i32 s53, 3840\n\ts_mov_b64 s[48:49], s[80:81]\n\ts_add_u32 s50, s53, 3840\n\ts_min_u32 s50, s50, 229376\n\t"
    "s_mov_b32 s51, 0x20000\n\t"
    "v_lshlrev_b32 v121, 3, v84\n\tbuffer_load_dword v121, s[48:51], s53 offen lds\n\t"
    "s_bitcmp0_b32 s73, 3\n\ts_cbranch_scc1 4f\n\t"
    "buffer_load_dwordx4 v84, s[88:91], s54 offen sc1 lds\n\tbuffer_load_dwordx4 v84, s[88:91], s54 offen offset:1024 sc1 lds\n\t"
    "buffer_load_dwordx4 v84, s[88:91], s54 offen offset:2048 sc1 lds\n\tbuffer_load_dwordx4 v84, s[88:91], s54 offen offset:3072 sc1 lds\n"
    "4:\n\ts_cmp_eq_u32 s56, 1\n\ts_cbranch_scc1 ev_first\n\ts_setpc_b64 s[86:87]\n"
    ::"i"((int)T_ZO) : "scc");
  asm volatile("ev_zero:");
  acc_zero();
  asm volatile("s_setpc_b64 s[86:87]\nev_first:\n\ts_cmp_lg_u32 s57, 0\n\ts_cbranch_scc1 ev_zero\n\ts_bitcmp0_b32 s73, 4\n\ts_cbranch_scc1 ev_first1");
  GAP();
  phaseB<0, true>(c);
  asm volatile("s_branch att_loop\nev_first1:");
  phaseB<1, true>(c);
  asm volatile("s_branch att_mid\n9:");
  GAP();
}
DI void ev_call() { asm volatile("s_swappc_b64 s[86:87], s[82:83]" ::: "vcc", "scc", "memory"); GAP(); }

#define ACLOB_ALL "a0","a1","a2","a3","a4","a5","a6","a7","a8","a9","a10","a11","a12","a13","a14","a15","a16","a17","a18","a19","a20","a21","a22","a23","a24","a25","a26","a27","a28","a29","a30","a31","a32","a33","a34","a35","a36","a37","a38","a39","a40","a41","a42","a43","a44","a45","a46","a47","a48","a49","a50","a51","a52","a53","a54","a55","a56","a57","a58","a59","a60","a61","a62","a63","a64","a65","a66","a67","a68","a69","a70","a71","a72","a73","a74","a75","a76","a77","a78","a79","a80","a81","a82","a83","a84","a85","a86","a87","a88","a89","a90","a91","a92","a93","a94","a95","a96","a97","a98","a99","a100","a101","a102","a103","a104","a105","a106","a107","a108","a109","a110","a111","a112","a113","a114","a115","a116","a117","a118","a119","a120","a121","a122","a123","a124","a125","a126","a127","a128","a129","a130","a131","a132","a133","a134","a135","a136","a137","a138","a139","a140","a141","a142","a143","a144","a145","a146","a147","a148","a149","a150","a151","a152","a153","a154","a155","a156","a157","a158","a159","a160","a161","a162","a163","a164","a165","a166","a167","a168","a169","a170","a171","a172","a173","a174","a175","a176","a177","a178","a179","a180","a181","a182","a183","a184","a185","a186","a187","a188","a189","a190","a191","a192","a193","a194","a195","a196","a197","a198","a199","a200","a201","a202","a203","a204","a205","a206","a207","a208","a209","a210","a211","a212","a213","a214","a215","a216","a217","a218","a219","a220","a221","a222","a223","a224","a225","a226","a227","a228","a229","a230","a231","a232","a233","a234","a235","a236","a237","a238","a239","a240","a241","a242","a243","a244","a245","a246","a247","a248","a249","a250","a251","a252","a253","a254","a255"

struct EP { float fa[2], fb[2]; };
template <int I, int E, int MODE> DI void epi_pair(const EP& p) {
  constexpr int qb = I & 1, r = 16 * (I >> 1) + 4 * qb + (E >> 1) * 8 + (E & 1) * 2, t = 96 + 2 * E, w = 128 + 4 * I + E;
  if constexpr (MODE == 2) asm volatile("v_accvgpr_read_b32 v106, a[%c0]" ::"i"(w));
  asm volatile("v_accvgpr_read_b32 v%c0, a[%c2]\n\tv_accvgpr_read_b32 v%c1, a[%c3]" ::"i"(t), "i"(t + 1), "i"(r), "i"(r + 1));
  if constexpr (MODE) asm volatile("v_mul_f32 v%c0, v%c0, %2\n\tv_mul_f32 v%c1, v%c1, %2" ::"i"(t), "i"(t + 1), "v"(p.fa[qb]));
  if constexpr (MODE == 2)
    asm volatile("v_lshlrev_b32 v104, 16, v106\n\tv_and_b32 v105, 0xffff0000, v106\n\tv_fmac_f32 v%c0, v104, %2\n\tv_fmac_f32 v%c1, v105, %2"
                 ::"i"(t), "i"(t + 1), "v"(p.fb[qb]));
  asm volatile("v_cvt_pk_bf16_f32 v%c0, v%c1, v%c2" ::"i"(w), "i"(t), "i"(t + 1));
}
template <int I> DI void epi_store() {
  asm volatile("buffer_store_dwordx4 v[%c0:%c1], v84, s[88:91], s101 offen offset:%2 sc1" ::"i"(128 + 4 * I), "i"(131 + 4 * I), "i"((I & 3) * 1024) : "memory");
  if constexpr ((I & 3) == 3) asm volatile("s_addk_i32 s101, 4096");
}
constexpr int R_TB = 232, R_VS = 236;
template <int HF, int R> DI void p_read() {
  constexpr int mb = R >> 4, r = 192 - 64 * HF + 2 * R;
  asm volatile("ds_read_b64_tr_b16 a[%c1:%c2], v%c0 offset:%c3" ::"i"(R_TB + 2 * HF + mb), "i"(r), "i"(r + 1), "i"((R & 15) * 2048));
}
template <int QB, int E> DI void p_cvt() {
  asm volatile("v_cvt_pk_bf16_f32 v%c0, v%c1, v%c2" ::"i"(208 + 4 * QB + E), "i"(192 + 8 * QB + 2 * E), "i"(193 + 8 * QB + 2 * E));
}
template <int QB, int PR> DI void p_store() {
  asm volatile("buffer_store_dwordx4 v[%c0:%c1], v%c2, s[88:91], s96 offen offset:%3 sc1" ::"i"(208 + 4 * QB), "i"(211 + 4 * QB), "i"(R_VS + QB),
               "i"(PR * 128) : "memory");
}
constexpr int p_dma(int HF, int G) { return G == 3 && !HF ? 0 : G == 11 ? 1 - HF : G == 15 ? 2 - HF : G == 19 ? 3 - HF : G == 27 && HF ? 3 : -1; }
template <int HF, bool LD, int G> DI void p_gap() {
  constexpr int ch = G >> 3, t = G & 7, a = 192 - 64 * HF + 32 * (ch & 1) + 4 * t, b = 128 + 4 * (2 * t + (ch >> 1)), d = 192 + 4 * ch;
  if constexpr (t == 0)
    asm volatile("v_mfma_f32_16x16x32_bf16 v[%c0:%c1], a[%c2:%c3], v[%c4:%c5], 0" ::"i"(d), "i"(d + 3), "i"(a), "i"(a + 3), "i"(b), "i"(b + 3));
  else
    asm volatile("v_mfma_f32_16x16x32_bf16 v[%c0:%c1], a[%c2:%c3], v[%c4:%c5], v[%c0:%c1]" ::"i"(d), "i"(d + 3), "i"(a), "i"(a + 3), "i"(b), "i"(b + 3));
  p_read<HF ^ 1, G>();
  if constexpr (G == 0) asm volatile("v_xor_b32 v%c0, s100, v%c0" ::"i"(R_TB + 2 * HF + 1));
  if constexpr (G == 17) asm volatile("v_xor_b32 v%c0, s100, v%c0" ::"i"(R_TB + 2 * (HF ^ 1)));
  if constexpr (G >= 4 && G <= 8 && G != 7) p_cvt<1, (G < 7 ? G - 4 : 3)>();
  if constexpr (G == 24 || (G == 7 && HF == 0)) asm volatile("s_waitcnt lgkmcnt(8)");
  if constexpr (G == 7 && HF == 1) asm volatile("s_waitcnt vmcnt(%0) lgkmcnt(8)\n\ts_barrier" ::"i"(LD ? 13 : 5) : "memory");
  if constexpr (G == 10) p_store<1, HF ^ 1>();
  if constexpr (G >= 20 && G < 24) p_cvt<0, G - 20>();
  if constexpr (G == 25) p_store<0, HF>();
  if constexpr (LD && p_dma(HF, G) >= 0) dma_at<(HF ? 8 : 12) + p_dma(HF, G)>();
  if constexpr (LD && HF == 0 && G == 2) asm volatile("s_bitset0_b32 m0, 12");
  if constexpr (HF == 0 && G == 13) asm volatile("s_add_u32 s96, s96, 0x8000");
  if constexpr (HF == 0 && G == 26) asm volatile("s_add_u32 s98, s98, 128");
  if constexpr (LD && HF == 0 && G == 27) asm volatile("s_cmp_lt_u32 s98, 0x380");
  if constexpr (HF == 1 && G == 1) asm volatile("s_xor_b32 s100, s100, 0x10000");
  if constexpr (LD && HF == 1 && G == 2) asm volatile("s_xor_b32 s97, s97, s100");
  if constexpr (LD && HF == 1 && G == 9) asm volatile("s_or_b32 m0, s97, 0x1000");
  if constexpr (!LD && HF == 1 && G == 30) asm volatile("s_cmp_lt_u32 s98, 0x500");
  GAP();
}
template <int HF, bool LD, int... Gs> DI void p_half(iseq<Gs...>) { (p_gap<HF, LD, Gs>(), ...); }
template <int G> DI void gapF(AC& c, const EP& p) {
  if constexpr (G < 64) gapB<0, false, G>(c);
  if constexpr (G >= 10) epi_pair<(G - 10) / 4, (G - 10) & 3, 0>(p);
  if constexpr (G >= 14 && ((G - 14) & 3) == 0) epi_store<(G - 14) / 4>();
  if constexpr (G == 73) epi_store<15>();
  GAP();
}
template <int... Gs> DI void phaseF(AC& c, const EP& p, iseq<Gs...>) { (gapF<Gs>(c, p), ...); }
template <int G> DI void gapAL(AC& c) {
  constexpr int kb = G >> 3, s = G & 7, r = 192 + 4 * (G & 15), q = 160 + 4 * s, d = R_S + 36 + 8 * kb, ci = s == 0 ? R_CI + 4 + 8 * kb : d;
  if constexpr (G == 0) asm volatile("s_add_u32 s96, s97, 0x1000000\n\ts_mov_b32 m0, s79\n\ts_waitcnt vmcnt(9) lgkmcnt(0)\n\ts_barrier" ::: "memory");
  if constexpr (G >= 8 && (G & 3) == 0) asm volatile("s_waitcnt lgkmcnt(4)");
  if constexpr (G == 29) asm volatile("s_barrier" ::: "memory");
  asm volatile("v_mfma_f32_16x16x32_bf16 v[%c0:%c1], a[%c2:%c3], a[%c4:%c5], v[%c6:%c7]" ::"i"(d), "i"(d + 3), "i"(r), "i"(r + 3), "i"(q), "i"(q + 3),
               "i"(ci), "i"(ci + 3));
  if constexpr (G < 24) kread<1, G + 8>();
  else vread<0, G - 24>();
  if constexpr (G % 3 == 1 && G < 24) dma_at<G / 3>();
  if constexpr (G == 11) asm volatile("s_bitset1_b32 m0, 12");
  if constexpr (G == 28) asm volatile("buffer_load_dwordx4 v[122:125], v88, s[88:91], s76 offen sc1" ::: "memory");
  if constexpr (G == 30) asm volatile("s_xor_b32 m0, s79, 0x11000\n\ts_movk_i32 s98, 128");
  sm_gap<0, 64 + 2 * G>(c); sm_gap<0, 65 + 2 * G>(c);
  GAP();
}
template <int... Gs> DI void phaseAL(AC& c, iseq<Gs...>) { (gapAL<Gs>(c), ...); }
struct BLS { int ex[65], op[65], vm[65], su[65]; };
constexpr BLS bl_sched(bool LD) {
  BLS z{};
  z.op[0] = 1;
  for (int t = 0; t < 64; ++t) {
    const int g = t & 15, o = z.op[t];
    const bool x = t == 6 || (t < 48 && g > 8 && g != 12 && g != 15);
    const bool v = LD ? (t & 3) == 0 : (t & 11) == 8;
    const bool p = t >= 15 && o < 24 && (t & 3) != 0 && t != 47 && !x && z.ex[t - 1] > 2 * (o / 3) + (o % 3 != 0);
    z.ex[t + 1] = z.ex[t] + x; z.vm[t + 1] = z.vm[t] + v; z.op[t + 1] = o + p;
    z.su[t + 1] = z.su[t] + (t >= 1 && z.su[t] < (LD ? 16 : 10) && g != 15 && !v && !x && (LD || t > 55 || (t > 3 && !p)));
  }
  return z;
}
template <bool LD> constexpr BLS BL = bl_sched(LD);
static_assert(BL<true>.ex[64] == 16 && BL<true>.op[61] == 24 && BL<true>.vm[64] == 16 && BL<false>.vm[64] == 8);
static_assert(BL<true>.su[56] == 16 && BL<false>.su[62] == 10);
template <bool LD, int G> DI void gapBL(AC& c) {
  if constexpr (G == 48) asm volatile("s_waitcnt vmcnt(%0) lgkmcnt(0)\n\ts_barrier" ::"i"(BL<LD>.vm[48]) : "memory");
  gapB<1, false, G>(c);
  if constexpr (G >= 48 && G < 56) vread<1, G - 48>();
  if constexpr (BL<LD>.vm[G + 1] > BL<LD>.vm[G]) {
    constexpr int k = BL<LD>.vm[G] + (LD ? 0 : 16), I = 2 * (k & 7) + ((k >> 3) & 1);
    if constexpr (k < 16) {
      asm volatile("buffer_load_dwordx4 a[%c0:%c1], v84, s[88:91], s75 offen offset:%2 sc1" ::"i"(128 + 4 * I), "i"(131 + 4 * I), "i"((I & 3) * 1024) : "memory");
      if constexpr (k & 1) asm volatile("s_addk_i32 s75, %0" ::"i"(k == 7 ? -12288 : 4096));
    } else dma_at<k - 8>();
    if constexpr (k == 19) asm volatile("s_bitset0_b32 m0, 12");
  }
  if constexpr (G == 1 || G == 2) asm volatile("v_add_f32_e32 v%c0, v%c0, v%c1" ::"i"(R_RS + G - 1), "i"(R_TS + G - 1));
  if constexpr (G == 3) asm volatile("v_mov_b32 v%c0, 0" ::"i"(R_TS));
  if constexpr (BL<LD>.ex[G + 1] > BL<LD>.ex[G]) {
    constexpr int j = BL<LD>.ex[G];
    asm volatile("v_exp_f32 v%c0, v%c1" ::"i"(R_E + j), "i"(R_S + 36 + 8 * (j >> 2) + (j & 3)));
  }
  if constexpr (BL<LD>.op[G + 1] > BL<LD>.op[G]) {
    constexpr int o = BL<LD>.op[G], k = o / 3, j = 2 * k + o % 3;
    if constexpr (o % 3 == 2) asm volatile("v_cvt_pk_bf16_f32 v%c0, v%c1, v%c2" ::"i"(p_reg(1, (k >> 1) * 4 + 2 + (k & 1))), "i"(R_E + 2 * k), "i"(R_E + 2 * k + 1));
    else asm volatile("v_add_f32_e32 v%c0, v%c1, v%c2" ::"i"(R_TS + 1), "i"(j == 1 ? R_E : R_TS + 1), "i"(R_E + j));
  }
  if constexpr (G == 61) asm volatile("v_cmp_nle_f32_e64 s[54:55], v%c0, v%c1" ::"i"(R_TS + 1), "i"(R_THR));
  if constexpr (BL<LD>.su[G + 1] > BL<LD>.su[G]) {
    constexpr int k = BL<LD>.su[G];
    if constexpr (k < 4) asm volatile("v_xor_b32 v%c0, %1, %2" ::"i"(R_TB + k), "i"(32 * k), "v"(c.tb));
    else if constexpr (k < 6) asm volatile("v_add_u32 v%c0, %1, %2" ::"i"(R_VS + k - 4), "i"(4096 * (k - 4)), "v"(c.vs));
    else if constexpr (k == 6) tload<96, T_SLAB>();
    else if constexpr (k == 7) tload<101, T_CNT>();
    else if constexpr (k == 8) asm volatile("v_mov_b64 v[240:241], 1");
    else if constexpr (k == 9) asm volatile("s_mov_b32 s97, s79\n\ts_mov_b32 s100, 0x8000");
    else if constexpr (k < 14) asm volatile("v_mov_b32 v%c0, %1" ::"i"(k < 12 ? 214 + k : 218 + k), "v"(k & 1 ? c.o1 : c.o0));
    else asm volatile("v_mov_b64 v[%c0:%c1], 0" ::"i"(198 + 2 * k), "i"(199 + 2 * k));
  }
  GAP();
}
template <bool LD, int... Gs> DI void phaseBL(AC& c, iseq<Gs...>) { (gapBL<LD, Gs>(c), ...); }
template <int MODE, int G> DI void gapFL(const EP& p) {
  constexpr int x = G - (MODE == 3 ? 4 : 0), J = MODE == 1 ? (G < 24 && G % 3 == 1 ? G / 3 : -1) : G < 27 && G % 5 != 0 && G % 5 != 3 ? G / 5 * 3 + (G % 5) / 2 : -1;
  if constexpr (G == 0 && MODE == 1) asm volatile("s_or_b32 m0, s78, 0x1000\n\ts_movk_i32 s98, 256\n\ts_waitcnt lgkmcnt(0)");
  if constexpr (G == 0 && MODE > 1) asm volatile("s_waitcnt vmcnt(8) lgkmcnt(0)" ::: "memory");
  if constexpr (G == 16 && MODE > 1) asm volatile("s_waitcnt vmcnt(9)" ::: "memory");
  if constexpr (G >= 8 && G < 32 && (G & 3) == 0) asm volatile("s_waitcnt lgkmcnt(%0)" ::"i"(G < 28 ? 4 : 0));
  if constexpr (G < 32) {
    constexpr int o = 8 * (G >> 1) + 4, r = 192 + 4 * (G & 15), q = R_P + 20 + 8 * (G & 1);
    asm volatile("v_mfma_f32_16x16x32_bf16 a[%c0:%c1], a[%c2:%c3], v[%c4:%c5], a[%c0:%c1]" ::"i"(o), "i"(o + 3), "i"(r), "i"(r + 3), "i"(q), "i"(q + 3));
  }
  if constexpr (G < 24) vread<1, G + 8>();
  if constexpr (G == 28) asm volatile("s_waitcnt vmcnt(8)\n\ts_barrier\n\ts_or_b32 m0, s79, 0x1000\n\ts_movk_i32 s98, 384" ::: "memory");
  if constexpr (G == 29 || G == 31) dma_at<8 + (G - 29) / 2>();
  if constexpr (G == 32 || G == 40) {
    static_for([&](auto I) { p_read<0, 2 * (G - 32) + I>(); }, make_iseq<16>{});
    dma_at<10 + (G - 32) / 8>();
  }
  if constexpr (J >= 0) dma_at<8 + (J & 7)>();
  if constexpr (x >= 0) epi_pair<2 * ((x >> 2) & 7) + (x >> 5), x & 3, MODE>(p);
  if constexpr (MODE == 3 && G < 32) {
    constexpr int db = G & 15, qb = G >> 4, o = 4 * (db * 2 + qb), a = 224 + 4 * (db & 1), b = 128 + 4 * ((db >> 1) * 2 + qb);
    asm volatile("v_mfma_f32_16x16x32_bf16 a[%c0:%c1], v[%c2:%c3], a[%c4:%c5], a[%c0:%c1]" ::"i"(o), "i"(o + 3), "i"(a), "i"(a + 3), "i"(b), "i"(b + 3));
  }
  if constexpr (J == 3 || J == 11) asm volatile("s_bitset0_b32 m0, 12");
  if constexpr (J == 7 && MODE > 1) asm volatile("s_or_b32 m0, s78, 0x1000\n\ts_movk_i32 s98, 256");
  if constexpr (x == 63) asm volatile("s_waitcnt vmcnt(4) lgkmcnt(0)" ::: "memory");
  GAP();
}
template <int MODE, int... Gs> DI void phaseFL_(const EP& p, iseq<Gs...>) { (gapFL<MODE, Gs>(p), ...); }
template <int MODE> DI void phaseFL(const EP& p) { GAP(); phaseFL_<MODE>(p, make_iseq<MODE == 3 ? 68 : 64>{}); }
template <int U, int K> DI void r_load(unsigned vo, unsigned so) {
  asm volatile("buffer_load_dwordx4 a[%c0:%c1], %2, s[88:91], %3 offen offset:%4 sc1" ::"i"(64 * U + 4 * K), "i"(64 * U + 4 * K + 3), "v"(vo),
               "s"(so + (K >> 1) * 0x40000 + (U >> 1) * 4096), "i"((K & 1) * 512 + (U & 1) * 2048) : "memory");
}
template <int U, int K> DI void r_sum() {
  constexpr int d = 100 + 4 * (K & 1), b = 64 * U + 4 * K;
  if constexpr (K < 2) asm volatile("v_mfma_f32_16x16x32_bf16 v[%c0:%c1], v[96:99], a[%c2:%c3], 0" ::"i"(d), "i"(d + 3), "i"(b), "i"(b + 3));
  else asm volatile("v_mfma_f32_16x16x32_bf16 v[%c0:%c1], v[96:99], a[%c2:%c3], v[%c0:%c1]" ::"i"(d), "i"(d + 3), "i"(b), "i"(b + 3));
}
template <int U> DI void r_fin(unsigned vo, __amdgpu_buffer_rsrc_t srd, unsigned so) {
  asm volatile("" : "+s"(srd), "+s"(so));
  static_for([&](auto K) { r_sum<U, K>(); }, make_iseq<16>{});
  asm volatile("s_nop 7");
  static_for([&](auto E) { asm volatile("v_cvt_pk_bf16_f32 v%c0, v%c1, v%c2" ::"i"(116 + 4 * U + E), "i"(100 + 2 * E), "i"(101 + 2 * E)); }, make_iseq<4>{});
  asm volatile("buffer_store_dwordx4 v[%c3:%c4], %0, %1, %2 offen sc0 sc1" ::"v"(vo), "s"(srd), "s"(so), "i"(116 + 4 * U), "i"(119 + 4 * U) : "memory");
}

#define CMAX 12
#define BAL 0
#define NYE (BAL ? 16 : 15)
#define NYO (BAL ? 14 : 15)
#define NY0 15
struct TRow { unsigned v[64]; };
constexpr u64 kvmask(int m0, int m1) { u64 n = 0; for (int m = m0; m <= m1; ++m) n |= 0x101ull << m; return n; }
constexpr u64 k_order(bool isY, int rr) {
  const int cls = (rr + (rr >> 2)) & 3;
  return isY ? 0x012345feadcb6987ull : cls == 0 ? 0xfedcb9875a463210ull : cls == 1 ? 0xefadc6982b574310ull : cls == 2 ? 0xfebad7693c285410ull : 0xefcba8764d392510ull;
}
constexpr TRow t_row(int b, int wid) {
  TRow t{};
  const int j = b & 7, r = b >> 3, rr = r & 15, hsel = rr & 1, pj = rr >> 1, kv = pj & 1, h = j + 8 * hsel, u = hsel * 8 + j;
  const bool isY = r >= 16, noA = pj == 7, slow = BAL && (j & 1);
  const int pa = slow && (pj == 0 || pj == 7) ? 7 - pj : pj;
  const bool solo = pa == 7;
  const int jF = isY || !BAL ? j : slow && pa == 0 ? j - 1 : !slow && pa == 6 ? j + 1 : j, hF = jF + 8 * hsel;
  const int qbF = isY ? (solo ? (slow ? 6 : 7) : 15 - pa) : solo ? 8 : !BAL ? pa : slow ? (pa ? pa - 1 : 0) : pa + 1;
  const int mt = (rr >> 2) + (isY ? 4 : 0), g = j * 128, gF = jF * 128, NY = !(j & 1) ? NYE : pa ? NYO : NY0;
  const int nx0 = solo ? 0 : 32 - 2 * pa - NY, cx = nx0 < CMAX ? nx0 : CMAX, ex = nx0 - cx, qb0 = solo ? qbF : 15 - pa;
  const unsigned oQ = (unsigned)h << 20, oK = oQ + O_K, oP = O_P + (h * 8 + pa) * PSTR, oQF = (unsigned)hF << 20;
  if (wid == 4) {
    for (int l = 0; l < 64; ++l)
      t.v[l] = fo(l < 16 ? g + hsel * 16 + l : l == 16 ? g + 32 + hsel * 8 + (qb0 >> 1) : l == 17 ? gF + 32 + hsel * 8 + (qbF >> 1) :
                  l < 26 ? gF + hsel * 16 + ((l - 18) >> 2) * 8 + ((l - 18) & 3) : l == 26 ? g + 48 + hsel * 8 + pa : l < 32 ? g + 64 + hsel * 8 + pa :
                  l < 48 ? 2304 + pj * 16 + l - 32 : 2048 + pj * 16 + l - 48);
    return t;
  }
  auto put64 = [&](int i, u64 x) { t.v[i] = (unsigned)x; t.v[i + 1] = (unsigned)(x >> 32); };
  t.v[T_Q0] = oQ + (qb0 * 128 + wid * 16) * 512;
  t.v[T_K0] = oK + (isY ? cx : 0) * 32768;
  t.v[T_OQ] = oQ + pj * 131072 + (wid >> 1) * 65536 + (wid & 1) * 4096;
  t.v[T_OG] = oK + kv * TSB + mt * 131072 + (wid >> 1) * 65536 + (wid & 1) * (kv ? 8192 : 4096);
  t.v[T_FPREP] = fo(2048 + pj * 16 + u);
  t.v[T_FQ] = fo(g + 32 + hsel * 8 + pj);
  t.v[T_FG] = fo(g + hsel * 16 + kv * 8 + mt);
  t.v[T_FS] = fo(1024 + qbF * 16 + jF * 2 + hsel);
  t.v[T_CNT] = fo(2560 + qbF * 16);
  t.v[T_ZO] = O_Z + pj * 64 * SEQ * 2 + (u * 4 + wid) * 4096;
  const u64 own = 0x1ull << (kv * 8 + mt), conv = noA ? 0 : 0xffffull << 32;
  put64(T_NPRE, 0x10000ull | (isY ? (1ull << (cx >> 2)) | (1ull << ((cx + 1) >> 2)) : noA ? 0 : 0xffffull << 48));
  put64(T_NST, isY ? kvmask(cx >> 2, 3) | (solo ? 0 : 1ull << 27) : kvmask(0, 0));
  put64(T_TAB, k_order(isY, rr));
  t.v[T_SLAB] = O_S + qbF * 0x200000 + jF * 0x40000 + wid * 8192 + hsel * 1024;
  t.v[T_PART] = oP;
  const unsigned junk = fo(g + 96 + r), fconv = noA ? junk : fo(2304 + pj * 16 + u);
  for (int it = isY || solo; it < 2; ++it) {
    unsigned* p = t.v + T_IT + it * T_ITN;
    const int n = !it ? nx0 : isY && !solo ? NY : 2 * qbF + 2;
    u64 q[4] = {0, 0, 0, 0};
    unsigned jmp[3] = {0, 0, 0}, chk = 0;
    p[14] = junk;
    if (isY) {
      p[0] = 2; p[1] = 2; p[13] = t.v[T_FG];
      if (!solo) {
        chk = 1u << (14 - cx); p[0] |= chk; q[1] = kvmask(4, (31 - 2 * pa) >> 2) & ~own; jmp[1] = ex * 32768;
        chk |= 1u << (n - 2); q[2] = 1ull << 26;
      }
    } else if (it == 0) {
      chk = p[0] = 2u | (ex ? 1u << (cx - 2) : 0) | 1u << (n - 2);
      chk |= noA || qbF > 1 ? 0 : 1u << n;
      q[0] = kvmask(0, (cx - 1) >> 2);
      if (ex) { q[1] = kvmask(4, 4 + ((ex - 1) >> 2)); jmp[1] = (16 - cx) * 32768; }
      jmp[ex ? 2 : 1] = oQF - oQ - (ex ? 16 + ex : cx) * 32768;
      q[ex ? 2 : 1] = 0x20000ull | ((0x22ull << ((2 * qbF + 1) >> 2)) - 0x11ull) << 18;
      q[ex ? 3 : 2] = conv;
      p[1] = 2; p[13] = fconv; p[14] = fo(g + 64 + hsel * 8 + pa);
    } else if (solo) {
      chk = p[0] = 0x402; p[1] = 2; p[13] = fconv;
      q[0] = kvmask(0, 2); q[1] = kvmask(3, 4) | conv;
    } else {
      p[0] = p[1] = n > 4 ? 2 : 1; p[13] = fo(g + 48 + hsel * 8 + pa);
      chk = noA ? 0 : p[0];
      q[0] = conv;
    }
    p[2] = jmp[0]; p[4] = jmp[1]; p[15] = jmp[2];
    p[3] = !it ? 0 : n > 2 ? 1u << (n - 3) : 1u << 30;
    for (int k = 0; k < 4; ++k) { p[5 + 2 * k] = (unsigned)q[k]; p[6 + 2 * k] = (unsigned)(q[k] >> 32); }
    const int rk = (r >> 1) - (BAL && (r >> 1) > (slow ? 7 : 6));
    p[16] = 32 | (isY && !solo ? 2 : 0) | (n == 2 ? 4 : 0) | (isY || noA ? 0 : 8) | (n & 1) << 4 | (n - 1 - it) << 8 | (rk * 4 + wid) << 16 | hF << 24 | qbF << 28;
    p[17] = !it ? 0 : n > 2 ? 1u << (n - 2) : 1u << 31;
    p[18] = it ? oP + wid * 16384 : oQF + (qbF * 128 + wid * 16) * 512;
    p[19] = oP + (it ? 1032 : 1024);
    p[20] = chk >> 1;
    p[21] = (it ? oQF + O_K : oK) + (isY ? cx : 0) * 32768;
    p[22] = ((p[0] | p[3] | p[17]) & 0x3fffffffu) << 1;
  }
  return t;
}
struct TBlk { TRow row[5]; };
template <int B> constexpr TBlk t_blk = {{t_row(B, 0), t_row(B, 1), t_row(B, 2), t_row(B, 3), t_row(B, 4)}};
struct Tab { TBlk blk[256]; };
template <int... I> constexpr Tab t_make(iseq<I...>) { return Tab{{t_blk<I>...}}; }
__device__ const Tab g_tab = t_make(make_iseq<256>{});

template <int R> DI const bf16* sptr() { const bf16* p; asm volatile("s_mov_b64 %0, s[%c1:%c2]" : "=s"(p) : "i"(R), "i"(R + 1)); return p; }
template <class T, int I> DI T* arg_late() {
  unsigned lo, hi;
  asm volatile("v_readlane_b32 %0, v77, %2\n\tv_readlane_b32 %1, v77, %3\n\ts_nop 4" : "=s"(lo), "=s"(hi) : "i"(2 * I), "i"(2 * I + 1));
  return (T*)(((u64)hi << 32) | lo);
}
__global__ __launch_bounds__(256, 1) __attribute__((amdgpu_num_vgpr(77), amdgpu_num_sgpr(56))) void fused_kernel(
    const bf16* ckv_, const bf16* wuk_, const bf16* wuv_, const bf16* x_, const bf16* wq_, bf16* ws_, const bf16* ak_, const bf16* av_,
    const bf16* ash_, const bf16* assmk_, const bf16* assmD_, const bf16* wo_, const bf16* aq_, bf16* out_) {
  extern __shared__ __attribute__((aligned(16))) char lds[];
  const void* kap = (const void*)__builtin_amdgcn_kernarg_segment_ptr();
  asm volatile("at_code_begin:\n\ts_load_dwordx16 s[80:95], %0, 0\n\ts_load_dwordx2 s[96:97], %0, 0x40" ::"s"(kap) : ACLOB_ALL, "v255", "s101");
  asm volatile("s_getpc_b64 s[48:49]\nct_base:\n\ts_mov_b32 s50, pf_code_end-ct_base\n\ts_mov_b32 s51, 0x20000\n\ts_lshr_b32 s52, %1, 3\n\t"
               "v_and_b32 v116, 63, %0\n\tv_lshrrev_b32 v117, 6, %0\n\tv_lshl_add_u32 v116, v116, 7, v117\n\ts_lshl_b32 s52, s52, 9\n\t"
               "v_lshlrev_b32 v116, 7, v116\n\tbuffer_load_dword v117, v116, s[48:51], s52 offen" ::"v"(threadIdx.x), "s"(blockIdx.x) : "scc", "memory");
  GAP();
  const int tid = threadIdx.x, lane = tid & 63;
  const int wid = __builtin_amdgcn_readfirstlane(tid >> 6);
  const int b = blockIdx.x, j = b & 7, r = b >> 3, rr = r & 15, hsel = rr & 1, pj = rr >> 1, kv = pj & 1;
  const bool isY = r >= 16, doP = isY && pj != 7, doA = !isY && pj != 7;
  const int h = j + 8 * hsel, mt = (rr >> 2) + (isY ? 4 : 0), pu = (hsel * 8 + j) * 4 + wid;
  constexpr unsigned m0d = 131072 + 24576;
  const unsigned lA = isY ? 65536 : 0, lW = lA ^ 65536, po = isY ? 0 : 32768;
  unsigned mA = lA + wid * 1024 + po, mW = lW + wid * 1024 + po, ofA = isY ? pj * 256 * DA * 2 : mt * 256 * DC * 2, ofW = h * 512;
  const unsigned lda = isY ? DA : DC;
#define ROT 1
  unsigned g0 = (!ROT ? 0 : isY ? pj + 2 * hsel : rr + (rr >> 2)) & 3;
  {
    asm volatile("global_load_dword v78, %0, %2\n\tglobal_load_dword v79, %1, %2 offset:1024\n\tglobal_load_dword v77, %3, %4"
                 ::"v"(tid * 4), "v"(lane * 4), "s"(&g_tab.blk[b]), "v"((lane < 27 ? lane : 27) * 4), "s"(kap) : "memory");
    const unsigned ra = wid * 8 + (lane >> 3), sa = ((lane & 7) ^ (((wid & 1) * 4 + (lane >> 4)) & 7)) * 16;
    const unsigned vbA = ra * (lda * 2) + sa, vbB = (4 * wid + (lane >> 4)) * LDW * 2 + 16 * ((lane & 15) ^ (((lane >> 4) << 2) | wid));
    vsetu<92>(vbA); vsetu<93>(vbB); vsetu<94>(ra * (DC * 2) + sa);
    sset<64>(64 * lda); sset<67>(256 * lda); sset<68>(320 * lda);
    asm volatile("s_mov_b32 s71, 0x20000\n\ts_movk_i32 s72, 0x100\n\ts_mov_b32 s73, 0x20100");
    sset<58>(256 * lda * 2); sset<62>((lda - 1) * LDW * 2 + 512);
    asm volatile("s_mov_b32 s59, 0x20000\n\ts_mov_b32 s63, 0x20000");
    g_pro<1>(g0, mA, mW, lda);
    asm volatile("" : "+s"(ofA), "+s"(ofW), "+s"(mA), "+s"(mW), "+s"(g0));
    GAP();
  }
  asm volatile("s_bitcmp1_b32 %0, 4\n\ts_waitcnt lgkmcnt(0)\n\ts_cselect_b64 s[60:61], s[84:85], s[82:83]\n\ts_bitcmp1_b32 %0, 7\n\t"
               "s_cselect_b64 s[60:61], s[88:89], s[60:61]\n\ts_cselect_b64 s[56:57], s[86:87], s[80:81]\n\t"
               "s_add_u32 s56, s56, %1\n\ts_addc_u32 s57, s57, 0\n\ts_add_u32 s60, s60, %2\n\ts_addc_u32 s61, s61, 0\n\t"
               "s_mov_b64 s[88:89], s[90:91]\n\ts_mov_b32 s90, %3\n\ts_mov_b32 s91, 0x20000" ::"s"(b), "s"(ofA), "s"(ofW), "i"(WSB) : "scc");
  KST(0, 0);
  AC c;
  {
    GM g;
    PR pr;
    const int ptb = hsel * 8 + j;
    if (doP) prep_load(pr, tid, pj, ptb, sptr<92>(), sptr<94>(), sptr<96>());
    g_pro<2>(g0, mA, mW, lda);
    asm volatile("buffer_load_dwordx2 v[120:121], off, s[88:91], %0 sc1" ::"s"(fo(6144)) : "memory");
    if (doP) {
      asm volatile("s_waitcnt vmcnt(9)" ::: "memory");
#pragma unroll
      for (int q = 0; q < 7; ++q) asm volatile("" : "+v"(pr.kk[q]));
#pragma unroll
      for (int q = 0; q < 4; ++q) asm volatile("" : "+v"(pr.vv[q]));
      static_for([&](auto Ic) __attribute__((always_inline)) {
        constexpr int I = Ic;
        if constexpr (I < 3) g_pro(g0 + 1 + I, mA, mW, lda);
        GAP();
        prep_math<I>(pr, tid, lds + 131072);
        GAP();
      }, make_iseq<4>{});
      asm volatile("s_waitcnt lgkmcnt(0)\n\ts_barrier" ::: "memory");
      prep_store(tid, pj, ptb, (bf16*)sptr<88>() + O_X / 2, lds + 131072);
    }
    GAP();
    {
      const int fr = lane & 15, fq = lane >> 4, q = (lane & 15) >> 2, p = lane & 3;
#pragma unroll
      for (int k2 = 0; k2 < 2; ++k2)
        g.aA[k2] = lA + (wid >> 1) * 16384 + (fr >> 3) * 1024 + (fr & 7) * 128 + (((4 * k2 + fq) ^ (fr >> 1)) * 16);
#pragma unroll
      for (int hf = 0; hf < 2; ++hf)
#pragma unroll
        for (int nf = 0; nf < 8; ++nf)
          g.aB[hf][nf] = lW + (wid & 1) * 8192 + off_b(8 * fq + 4 * hf + q, nf * 2 + (p >> 1)) + 8 * (p & 1);
      asm volatile("" : "+v"(g.aA[0]), "+v"(g.aA[1]));
#pragma unroll
      for (int nf = 0; nf < 8; ++nf) asm volatile("" : "+v"(g.aB[0][nf]), "+v"(g.aB[1][nf]));
      const unsigned one = fq != q ? 0u : (p & 1) ? 0x3f800000u : 0x3f80u;
      c.o0 = (p & 2) ? 0u : one; c.o1 = (p & 2) ? one : 0u;
      c.tb = (lane >> 2) * 128 + 16 * ((p >> 1) ^ ((lane >> 2) & 6)) + 8 * (p & 1);
      c.vs = (fr >> 3) * 2048 + (fq & 1) * 512 + (fq >> 1) * 256 + (fr & 7) * 16;
      asm volatile("" : "+v"(c.o0), "+v"(c.o1), "+v"(c.tb), "+v"(c.vs));
      vsetu<81>(tid == 0 ? 0u : 0x80000000u);
      vsetu<84>(lane * 16); vsetu<85>(lane * 16 + 65536); vsetu<86>(wid * 8192 + lane * 16); vsetu<87>(wid * 8192 + lane * 16 + 4096);
      vsetu<88>(65536 + wid * 256 + fr * 16); vsetu<89>(16 * wid + fr - 4 * fq);
      asm volatile("s_mov_b64 s[92:93], s[88:89]\n\ts_mov_b64 s[94:95], s[90:91]\n\ts_mov_b32 s74, 0x40000\n\ts_mov_b32 s75, 0x60000\n\t"
                   "s_mov_b32 s76, 0x40100\n\ts_mov_b32 s77, 0x60100");
      sset<65>(128 * lda); sset<66>(192 * lda); sset<69>(384 * lda); sset<70>(448 * lda);
      sset<78>(lA + wid * 1024); sset<79>(lW + wid * 1024);
    }
    if (doP) asm volatile("s_waitcnt vmcnt(4)" ::: "memory");
    else {
      g_pro(g0 + 1, mA, mW, lda); g_pro(g0 + 2, mA, mW, lda); g_pro(g0 + 3, mA, mW, lda);
      asm volatile("s_waitcnt vmcnt(0)" ::: "memory");
    }
    asm volatile("s_barrier" ::: "memory");
    if (isY) g_read0<0>(g, make_iseq<16>{});
    else g_read0<1>(g, make_iseq<16>{});
    GAP();
    {
      u64 z = (((u64)__float_as_uint(vget<121>()) << 32) | __float_as_uint(vget<120>())) + 0x9e3779b97f4a7c15ull;
      z = (z ^ (z >> 30)) * 0xbf58476d1ce4e5b9ull; z = (z ^ (z >> 27)) * 0x94d049bb133111ebull; z ^= z >> 31;
      vsetu<82>((unsigned)z); vsetu<83>((unsigned)(z >> 32));
      asm volatile("v_readlane_b32 s80, v78, %0\n\tv_readlane_b32 s81, v78, %1\n\tv_readfirstlane_b32 s84, v82\n\tv_readfirstlane_b32 s85, v83"
                   ::"i"((int)T_TAB), "i"(T_TAB + 1));
      if (isY) asm volatile("s_mov_b64 s[80:81], 0x6543210");
    }
    unsigned k2;
    asm volatile("s_bfe_u32 s82, s80, 0x40008\n\ts_lshr_b64 s[80:81], s[80:81], 12\n\ts_lshl_b32 %0, s82, 1\n\tv_add_u32 v90, 128, v92" : "=s"(k2) :: "scc");
    asm volatile("s_waitcnt lgkmcnt(0)\n\ts_barrier" ::: "memory");
    g_proW(k2, mW);
    GAP();
    if (isY) {
      g_tile<false, 0, 1>(g);
      g_loop<false, true>(g, doP ? 0x22u : 0x20u, [&](int t) __attribute__((always_inline)) {
        if (t == 1) fl_set<T_FPREP>();
        else if (t == 4) { tload<80, T_TAB>(); tload<81, T_TAB + 1>(); srd_set<60>((kv ? arg_late<const bf16, 2>() : arg_late<const bf16, 1>()) + h * 256, (DC - 1) * LDW * 2 + 512); }
        else {
          srd_set<56>(arg_late<const bf16, 0>() + (size_t)mt * 256 * DC, 256 * DC * 2);
          asm volatile("v_mov_b32 v92, v94");
          static_for([&](auto J) { asm volatile("s_mov_b32 s%c0, %1" ::"i"(64 + J), "i"((J + 1) * 64 * DC)); }, make_iseq<7>{});
        }
      });
      tload<101, T_OQ>(); sset<83>(__float_as_uint(CS));
      asm volatile("v_mov_b32 v112, v84\n\tv_add_u32 v113, 8192, v84");
      GAP();
      g_tile<false, 0, 2>(g);
      KST(0, 3);
    }
#define TT 3
    auto kvtile = [&](auto TRc) __attribute__((always_inline)) {
      constexpr bool TR = TRc;
      if (isY) g_tile<TR, 1, 4>(g);
      else g_tile<TR, 1, 1>(g);
      g_loop<TR, false>(g, 0x2000u | (isY || doA ? 2u : BAL && (j & 1) ? 0u : 1u << TT), [&](int t) __attribute__((always_inline)) {
        int l = threadIdx.x & 63; asm volatile("" : "+v"(l));
        if (t == 13) {
          asm volatile("s_mov_b32 s79, 0x28000");
          fl_poll<T_NPRE>();
          if ((tget<T_IT + T_ITN + 16>() & 0xf000000) == 0)
            asm volatile("v_readlane_b32 s48, v78, %0\n\tv_and_b32 v122, 0xffffff00, v82\n\tv_mov_b32 v123, v83\n\ts_nop 2\n\t"
                         "buffer_store_dwordx2 v[122:123], v81, s[88:91], s48 offen sc1" ::"i"((int)T_CNT) : "memory");
          if (!doA && !isY) {
            CODE_TOUCH("at_code_begin", "pf_code_end", hsel * 8 + wid * 2, 16u, 64, m0d);
            CODE_TOUCH("at_code_begin", "pf_code_end", hsel * 8 + wid * 2 + 1, 16u, 64, m0d);
          }
          if (doA) {
            const char* tp = arg_late<const char, 9>() + (size_t)(pj * 64 + pu) * (SEQ * 2) + (l & 31) * 128;
            asm volatile("s_mov_b32 m0, %1\n\ts_nop 0\n\tglobal_load_lds_dword %0, off" ::"v"(tp), "i"(m0d) : "memory");
          }
        } else if (isY) fl_set<T_FQ>();
        else if (doA) post_dma(pj, pu, arg_late<const bf16, 12>(), 131072 + wid * 8192);
        else {
          const char* tp = arg_late<const char, 0>() + 1024 * DC * 2 + (pu * 256 + l) * 128;
          asm volatile("s_mov_b32 m0, %0\n\ts_nop 0" ::"i"(m0d));
#pragma unroll
          for (int i = 0; i < (BAL ? 8 : 4); ++i) asm volatile("global_load_lds_dword %0, off" ::"v"(tp + (i & 3) * 8192 + (i >> 2) * 131072) : "memory");
        }
      });
      tload<101, T_OG>(); tload<96, T_Q0>(); tload<98, T_K0>();
      fl_check();
      tload<50, T_NST>(); tload<51, T_NST + 1>();
      asm volatile("v_mov_b32 v112, v84\n\tv_add_u32 v113, %0, v84\n\tv_mov_b32 v90, v87\n\tv_mov_b32 v91, v87\n\tv_mov_b32 v92, v87\n\tv_mov_b32 v93, v87\n\t"
                   "v_mov_b32 v94, v86\n\tv_mov_b32 v95, v86\n\tv_mov_b32 v126, v86\n\tv_mov_b32 v127, v86\n\ts_mov_b32 s99, 0x8000\n\t" FL_ARM("s[50:51]")
                   ::"i"(TR ? 4096 : 8192));
      if (doA) {
        const int d = pj * 64 + pu;
        const bf16* dp = arg_late<const bf16, 10>() + d;
        asm volatile("global_load_ushort v123, %0, off" ::"v"(dp) : "memory");
        srd_set<56>(arg_late<const bf16, 9>() + (size_t)d * SEQ, SEQ * 2);
        srd_set<60>(arg_late<const char, 5>() + O_X + d * SEQ * 2, SEQ * 2);
        sset<78>(65536 + wid * 15488);
        asm volatile("s_movk_i32 s64, 992\n\tv_subrev_u32 v122, 32, v84");
        GAP();
        g_tile<TR, 0, 3>(g);
      } else {
        sset<79>(isY ? wid * 8192 : 0x30000);
        if (tget<T_IT + T_ITN + 16>() & 16) asm volatile("s_add_u32 s98, s98, 0x8000\n\ts_mov_b32 s97, 0xffff8000" ::: "scc");
        else asm volatile("s_mov_b32 s97, 0x8000");
        GAP();
        g_tile<TR, 0, 5>(g);
      }
    };
    if (kv) kvtile(std::true_type{});
    else kvtile(std::false_type{});
  }
  KST(0, 4);
  asm volatile("s_waitcnt vmcnt(16)" ::: "memory");
  const unsigned md0 = tget<T_IT + 16>(), md1 = tget<T_IT + T_ITN + 16>();
  sset<79>(wid * 8192 + (md0 & 16) * 2048);
  if (doA) {
    const float Dd = bfl(__float_as_uint(vget<123>()));
    KST(0, 5);
    conv_wave<T_FG>(O_Z + pj * 64 * SEQ * 2 + ((pu & 7) * 8 + (pu >> 3)) * 64, pu >> 3, Dd, lds + 65536 + wid * 15488);
    const char* tp = arg_late<const char, 11>() + (size_t)h * (HD * DA * 2) + ((pj * 4 + wid) * 64 + lane) * 128;
    asm volatile("s_mov_b32 m0, %1\n\ts_nop 0\n\tglobal_load_lds_dword %0, off\n\ts_waitcnt vmcnt(17)" ::"v"(tp), "i"(m0d) : "memory");
    KST(0, 6);
  } else if (!isY) {
    static_for([&](auto I) { d_store<16 + I>(); }, make_iseq<16>{});
    tload<98, T_K0>();
    asm volatile("s_waitcnt vmcnt(0)\n\ts_barrier" ::: "memory");
    fl_set<T_FG>();
    KST(0, 5);
    fl_check();
    CODE_TOUCH("att_begin", "pf_code_end", hsel * 4 + wid, 8u, 64, m0d);
    dma4k<0>(); dma4k<1>(); dma4k<2>(); dma4k<3>();
    asm volatile("s_waitcnt vmcnt(0)" ::: "memory");
    KST(0, 6);
  } else {
    tload<76, T_PART>();
    fl_check();
    asm volatile("buffer_load_dwordx2 v[122:123], v88, s[88:91], s76 offen offset:1024 sc1" ::: "memory");
  }
  sset<78>(65536 + wid * 8192); sset<79>(98304 + wid * 8192);
  { const bf16* wp = arg_late<const bf16, 11>() + ((md1 >> 24) & 15) * HD * DA; asm volatile("s_mov_b64 s[80:81], %0" ::"s"(wp)); }
  asm volatile("att_begin:");
  ev_code(c);
  asm volatile("s_waitcnt lgkmcnt(0)\n\ts_barrier" ::: "memory");
#pragma unroll 1
  for (int it = md0 ? 0 : 1; it < 2; ++it) {
    const unsigned mode = it ? md1 : md0;
    const bool odd = mode & 16;
    if (odd) kreads<1>(make_iseq<8>{});
    else kreads<0>(make_iseq<8>{});
    const unsigned oP = tget<T_PART>();
    if (it == 0) {
      static_for([&](auto I) { tload<57 + I, T_IT + I>(); }, make_iseq<21>{});
      tload<S_T, T_IT + 21>(); tload<S_EV, T_IT + 22>();
    } else {
      static_for([&](auto I) { tload<57 + I, T_IT + T_ITN + I>(); }, make_iseq<21>{});
      tload<S_T, T_IT + T_ITN + 21>(); tload<S_EV, T_IT + T_ITN + 22>();
    }
    asm volatile("s_add_u32 s98, s97, 0x8000\n\ts_mov_b32 s56, -1\n\tv_mov_b64 v[114:115], 0\n\tv_mov_b64 v[118:119], 0\n\t"
                 "v_mov_b32 v120, 0x45800000" ::: "scc");
    KST(0, 8 + 3 * (it && !isY));
    GAP();
#define STA(k)
#define STI()
#define LOOP_ENDS "s_bfe_u32 s76, s73, 0x80008"
    STA(0);
    if (odd) {
      if (isY) phaseA<1, 2>(c);
      else phaseA<1, 1>(c);
      STA(1);
      STA(2);
      phaseB0<1>(mode, isY);
      STA(3);
      asm volatile(LOOP_ENDS "\n\ts_branch att_mid" ::: "scc");
    } else {
      if (isY) phaseA<0, 2>(c);
      else phaseA<0, 1>(c);
      STA(1);
      if (__builtin_expect(mode & 4, 0)) { asm volatile("s_mov_b32 s56, 30"); ev_call(); asm volatile("s_mov_b32 s56, 31"); ev_call(); acc_zero(); }
      STA(2);
      phaseB0<0>(mode, isY);
      if (__builtin_expect(mode & 4, 0)) { ev_call(); asm volatile("s_branch att_done"); }
      STA(3);
    }
    GAP();
    asm volatile(LOOP_ENDS "\natt_loop:" ::: "scc");
    GAP();
    phaseA<1, 0>(c);
    STI();
    if (__builtin_expect(c.rare != 0, 0)) sm_redo<1>();
    phaseB<1>(c);
    asm volatile("s_cbranch_scc1 att_done\natt_mid:");
    phaseA<0, 0>(c);
    STI();
    if (__builtin_expect(c.rare != 0, 0)) sm_redo<0>();
    phaseB<0>(c);
    asm volatile("s_cbranch_scc0 att_loop\natt_done:");
    GAP();
    STA(4);
    if (it == 0) phaseA<0, 3>(c);
    else {
      sset<76>(oP);
      GAP();
      phaseAL(c, make_iseq<32>{});
      if (__builtin_expect(redo(), 0)) sm_redo<1, false>();
      if (mode & 2) phaseBL<true>(c, make_iseq<64>{});
      else phaseBL<false>(c, make_iseq<64>{});
    }
    if (__builtin_expect(redo(), 0)) {
      if (it) static_for([&](auto I) { vset<R_S + 32 + (I >> 2) * 8 + (I & 3)>(-INFINITY); }, make_iseq<16>{});
      asm volatile("s_nop 7");
      sm_redo<0, false>();
    }
    STA(5);
    KST(0, 9 + 3 * (it && !isY));
    float l[2];
    asm volatile("v_add_f32 %0, v%c2, v%c3\n\tv_add_f32 %1, v%c4, v%c5" : "=v"(l[0]), "=v"(l[1]) : "i"(R_RS), "i"(R_TS), "i"(R_RS + 1), "i"(R_TS + 1));
    const float cm[2] = {vget<R_M>(), vget<R_M + 1>()};
    l[0] = row_reduce<false>(l[0]); l[1] = row_reduce<false>(l[1]);
    EP p;
    if (it == 0) {
      const f32x4 ml = {cm[0], l[0], cm[1], l[1]};
      asm volatile("buffer_store_dwordx4 %0, v88, s[88:91], %1 offen sc1\n\ts_mov_b32 s101, %2" ::"v"(ml), "s"(oP), "s"(oP + wid * 16384) : "memory");
      phaseF(c, p, make_iseq<74>{});
      asm volatile(FL_POLL "\n\ts_waitcnt vmcnt(17)" ::: "memory");
    } else {
      if (!(mode & 2)) {
        p.fa[0] = __builtin_amdgcn_rcpf(l[0]); p.fa[1] = __builtin_amdgcn_rcpf(l[1]);
        phaseFL<1>(p);
      } else {
        const f32x4 ml = {vget<122>(), vget<123>(), vget<124>(), vget<125>()};
        if (__builtin_expect(__builtin_amdgcn_ballot_w64(ml[0] != cm[0] || ml[2] != cm[1]) == 0, 1)) {
          p.fa[0] = __builtin_amdgcn_rcpf(l[0] + ml[1]); p.fa[1] = __builtin_amdgcn_rcpf(l[1] + ml[3]);
          phaseFL<3>(p);
        } else {
#pragma unroll
          for (int e = 0; e < 2; ++e) {
            const float mm = fmaxf(cm[e], ml[2 * e]);
            const float wa = __builtin_amdgcn_exp2f(cm[e] - mm), wb = __builtin_amdgcn_exp2f(ml[2 * e] - mm);
            const float inv = __builtin_amdgcn_rcpf(l[e] * wa + ml[2 * e + 1] * wb);
            p.fa[e] = wa * inv; p.fb[e] = wb * inv;
          }
          phaseFL<2>(p);
        }
      }
    }
    KST(0, 10 + 3 * (it && !isY));
  }
  asm volatile(TICKET "\n\tv_xor_b32 v%c0, s100, v%c0\np_loop:" ::"i"(R_TB) : "memory");
  GAP();
  p_half<0, true>(make_iseq<32>{});
  asm volatile("s_cbranch_scc0 p_mid");
  p_half<1, true>(make_iseq<32>{});
  asm volatile("s_branch p_loop\np_loop2:");
  p_half<0, false>(make_iseq<32>{});
  asm volatile("p_mid:");
  p_half<1, false>(make_iseq<32>{});
  asm volatile("s_cbranch_scc1 p_loop2\n\ts_nop 5");
  GAP();
  p_cvt<1, 0>(); p_cvt<1, 1>(); p_cvt<1, 2>(); p_cvt<1, 3>();
  p_store<1, 1>();
  GAP();
  KST(0, 27);
  {
    constexpr unsigned rdy = 98304;
    int lz = threadIdx.x & 63; asm volatile("" : "+v"(lz));
    const int fr = lz & 15, fq = lz >> 4, qbF = md1 >> 28;
    if (wid == 0) {
#pragma unroll 1
      for (int q = 0; q < 0x100000; ++q) {
        unsigned lo, hi, tl, th;
        asm volatile("v_readfirstlane_b32 %0, v240\n\tv_readfirstlane_b32 %1, v241\n\ts_mov_b32 %2, s84\n\ts_mov_b32 %3, s85" : "=s"(lo), "=s"(hi), "=s"(tl), "=s"(th));
        if (__builtin_expect((((lo ^ tl) >> 8) | (hi ^ th)) == 0, 1)) break;
        asm volatile("s_sleep 4\n\tv_mov_b64 v[240:241], 1\n\t" TICKET "\n\ts_waitcnt vmcnt(0)" ::: "memory");
      }
      asm volatile("v_mov_b32 v241, 0\n\ts_mov_b64 exec, 1\n\tds_write_b64 %0, v[240:241]\n\ts_mov_b64 exec, -1" ::"v"(rdy) : "memory");
    }
    vsetu<79>(fo(1024) + qbF * 4352 + fr * 8);
    {
      const int jj = 4 * (fr >> 3) + (fr & 3);
      const unsigned one = ((fq ^ (fr >> 2)) & 1) ? 0u : (jj & 1) ? 0x3f800000u : 0x3f80u;
      static_for([&](auto I) { vsetu<96 + I>((jj >> 1) == I ? one : 0u); }, make_iseq<4>{});
    }
    unsigned vo = (fq >> 1) * 1024 + (fq & 1) * 256 + fr * 16;
    bf16* out = arg_late<bf16, 13>();
    __amdgpu_buffer_rsrc_t so_ = __builtin_amdgcn_make_buffer_rsrc((void*)out, (short)0, SEQ * OUTW * 2, 0x00020000);
    unsigned vw = ((qbF * 128 + (fr & 7)) * OUTW + DA + (fr >> 3) * 32 + fq * 8) * 2;
    asm volatile("" : "+s"(so_));
    asm volatile("s_waitcnt vmcnt(0) lgkmcnt(0)\n\ts_barrier" : "+v"(vo), "+v"(vw) :: "memory");
    fl_set<T_FS>();
    unsigned tk;
    asm volatile("ds_read_b32 %0, %1\n\ts_waitcnt lgkmcnt(0)" : "=v"(tk) : "v"(rdy) : "memory");
    tk = __builtin_amdgcn_readfirstlane(tk) & 255;
    KST(0, 30);
    if (doA) post_math(pj, pu, out, lds + 131072 + wid * 8192);
#define N3 0
#define N2 12
    constexpr int N1 = 28 - 3 * N3 - 2 * N2;
    static_assert(N1 >= 0 && N3 + N2 + N1 <= 16);
    const int nu = tk < N3 ? 3 : tk < N3 + N2 ? 2 : tk < N3 + N2 + N1 ? 1 : 0;
    const int u0 = 4 * (tk < N3 ? 3 * tk : tk < N3 + N2 ? N3 + 2 * tk : 2 * N3 + N2 + tk) + wid * nu;
    KST(0, 28);
    if (nu) {
      const unsigned so0 = O_S + qbF * 0x200000 + (16 + u0) * 2048, so[3] = {so0, so0, so0};
      unsigned pend = 0x5555u, cnt = 0x400000;
      while (true) {
        unsigned nw;
        if (wid == 3) {
          u64 m;
          const u64 ex = pend | (pend << 1);
          asm volatile(FL_ARM("%1") "\n\t" FL_POLL "\n\ts_waitcnt vmcnt(0)\n\tv_cmp_eq_u64 %0, s[84:85], v[124:125]" : "=s"(m) : "s"(ex) : "memory");
          nw = (unsigned)m & ((unsigned)m >> 1) & pend;
          if (nw) asm volatile("ds_write_b32 %0, %1 offset:4" ::"v"(rdy), "v"(~pend | nw) : "memory");
        } else {
          unsigned t;
          asm volatile("ds_read_b32 %0, %1 offset:4\n\ts_waitcnt lgkmcnt(0)" : "=v"(t) : "v"(rdy) : "memory");
          nw = __builtin_amdgcn_readfirstlane(t) & pend;
        }
        if (--cnt == 0) nw = pend;
        if (nw == 0) {
          if (wid == 3) asm volatile("s_sleep 4");
          else asm volatile("s_sleep 1");
          continue;
        }
        asm volatile("s_nop 4");
        static_for([&](auto S) {
          if ((nw >> (2 * S)) & 1) {

            r_load<0, 2 * S>(vo, so[0]); r_load<0, 2 * S + 1>(vo, so[0]);
            if (nu > 1) { r_load<1, 2 * S>(vo, so[1]); r_load<1, 2 * S + 1>(vo, so[1]); }
            if (nu > 2) { r_load<2, 2 * S>(vo, so[2]); r_load<2, 2 * S + 1>(vo, so[2]); }
          }
        }, make_iseq<8>{});
        pend &= ~nw;
        if (pend == 0) break;
      }
      asm volatile("s_waitcnt vmcnt(0)" ::: "memory");
      KST(0, 16);
      auto oo = [&](int U) { return ((((U >> 1) & 1) * 64 + ((U >> 2) & 3) * 16 + (U & 1) * 8) * OUTW + (U >> 4) * 64) * 2; };
      r_fin<0>(vw, so_, oo(u0));
      if (nu > 1) r_fin<1>(vw, so_, oo(u0 + 1));
      if (nu > 2) r_fin<2>(vw, so_, oo(u0 + 2));
      if (qbF == 15 && tk == 0) asm volatile(FL_SET("%0") ::"s"(fo(6144)) : "memory");
    }
    KST(0, 29);
  }
  asm volatile("s_waitcnt vmcnt(0)" ::: "memory");
  asm volatile("pf_code_end:");
  KST(0, 1);
}

extern "C" void kernel_launch(void* const* d_in, const int* in_sizes, int n_in, void* d_out, int out_size, void* d_ws,
                              size_t ws_size, hipStream_t stream) {
  const bf16* Aq = (const bf16*)d_in[0];
  const bf16* Ak = (const bf16*)d_in[1];
  const bf16* Av = (const bf16*)d_in[2];
  const bf16* Ash = (const bf16*)d_in[3];
  const bf16* Assmk = (const bf16*)d_in[4];
  const bf16* AssmD = (const bf16*)d_in[5];
  const bf16* Bx = (const bf16*)d_in[6];
  const bf16* Bckv = (const bf16*)d_in[7];
  const bf16* Bwq = (const bf16*)d_in[8];
  const bf16* Bwuk = (const bf16*)d_in[9];
  const bf16* Bwuv = (const bf16*)d_in[10];
  const bf16* Bwo = (const bf16*)d_in[11];
  bf16* out = (bf16*)d_out;
  hipFuncSetAttribute((const void*)fused_kernel, hipFuncAttributeMaxDynamicSharedMemorySize, 163840);
  fused_kernel<<<256, 256, 163840, stream>>>(Bckv, Bwuk, Bwuv, Bx, Bwq, (bf16*)d_ws, Ak, Av, Ash, Assmk, AssmD, Bwo, Aq, out);
}
```

```cpp
#include <hip/hip_runtime.h>
#include <hip/hip_bf16.h>
#include <stdint.h>
#include <utility>

typedef __hip_bfloat16 bf16;
typedef __attribute__((ext_vector_type(4))) short bf16x4;
typedef __attribute__((ext_vector_type(2))) unsigned u32x2;

constexpr int SEQ = 2048;
constexpr int DA = 448;
constexpr int NH = 16;
constexpr int HD = 256;
constexpr int DC = 1024;
constexpr int OUTW = 896;
constexpr unsigned TSB = NH * SEQ * HD * 2, O_K = TSB, O_S = 3 * TSB, O_P = 5 * TSB, PSTR = 67584, O_F = O_P + 128 * PSTR,
                   O_X = O_F + 400 * 4352, O_Z = O_X + DA * SEQ * 2, WSB = O_Z + DA * SEQ * 2;
constexpr unsigned fo(int w) { return O_F + (w >> 4) * 4352 + (w & 15) * 8; }

#define KST(kid, which)

__device__ __forceinline__ float bf2f(unsigned short u) { return __uint_as_float(((unsigned)u) << 16); }
__device__ __forceinline__ unsigned short f2bf(float f) { return __bfloat16_as_ushort(__float2bfloat16(f)); }
__device__ __forceinline__ float rbf(float f) { return bf2f(f2bf(f)); }
typedef __attribute__((ext_vector_type(2))) __bf16 bf16x2n;
typedef __attribute__((ext_vector_type(2))) float f32x2n;
__device__ __forceinline__ unsigned pk2(float a, float b) {
  return __builtin_bit_cast(unsigned, __builtin_convertvector((f32x2n){a, b}, bf16x2n));
}

__device__ __forceinline__ int off_b(int row, int ch) { return 256 * row + 16 * (ch ^ (((row & 3) << 2) | ((row >> 2) & 3))); }

template <int R> __device__ __forceinline__ void sset(unsigned x) { asm volatile("s_mov_b32 s%c0, %1" ::"i"(R), "s"(x)); }
template <int R> __device__ __forceinline__ void vsetu(unsigned x) { asm volatile("v_mov_b32 v%c0, %1" ::"i"(R), "v"(x)); }
template <int R> __device__ __forceinline__ void srd_set(const void* p, unsigned n) {
  asm volatile("s_mov_b64 s[%c0:%c1], %4\n\ts_mov_b32 s%c2, %5\n\ts_mov_b32 s%c3, 0x20000" ::"i"(R), "i"(R + 1), "i"(R + 2), "i"(R + 3), "s"(p), "s"(n));
}
template <int R, int L> __device__ __forceinline__ void tload() { asm volatile("v_readlane_b32 s%c0, v78, %1" ::"i"(R), "i"(L)); }
template <int L> __device__ __forceinline__ unsigned tget() { unsigned t; asm volatile("v_readlane_b32 %0, v78, %1" : "=s"(t) : "i"(L)); return t; }
#define FL_SET(S) "buffer_store_dwordx2 v[82:83], v81, s[88:91], " S " offen sc1"
#define FL_ARM(N) "v_cndmask_b32_e64 v80, -2.0, v79, " N
#define FL_POLL "buffer_load_dwordx2 v[124:125], v80, s[88:91], 0 offen sc1"
#define FL_CHECK(N) "s_mov_b32 s48, 0x100000\n7:\n\tv_cmp_eq_u64 vcc, s[84:85], v[124:125]\n\ts_and_b64 vcc, vcc, " N "\n\ts_cmp_eq_u64 vcc, " N \
                    "\n\ts_cbranch_scc1 8f\n\ts_sleep 2\n\t" FL_POLL "\n\ts_waitcnt vmcnt(0)\n\ts_sub_u32 s48, s48, 1\n\ts_cbranch_scc0 7b\n8:"
template <int L> __device__ __forceinline__ void fl_set() {
  asm volatile("v_readlane_b32 s48, v78, %0\n\ts_nop 4\n\t" FL_SET("s48") ::"i"(L) : "memory");
}
template <int L> __device__ __forceinline__ void fl_poll() {
  asm volatile("v_readlane_b32 s50, v78, %0\n\tv_readlane_b32 s51, v78, %1\n\ts_nop 1\n\t" FL_ARM("s[50:51]") "\n\t" FL_POLL ::"i"(L), "i"(L + 1) : "memory");
}
#define TICKET "buffer_atomic_add_x2 v[240:241], v81, s[88:91], s101 offen sc0"
__device__ __forceinline__ void fl_check() { asm volatile(FL_CHECK("s[50:51]") ::: "vcc", "scc", "memory"); }
template <int R> __device__ __forceinline__ float vget() { float t; asm volatile("v_mov_b32 %0, v%c1" : "=v"(t) : "i"(R)); return t; }
template <int R> __device__ __forceinline__ void vset(float x) { asm volatile("v_mov_b32 v%c0, %1" ::"i"(R), "v"(x)); }
template <int R> __device__ __forceinline__ float acc_read1() { float t; asm volatile("v_accvgpr_read_b32 %0, a[%c1]" : "=v"(t) : "i"(R)); return t; }
template <class F, int... I> __device__ __forceinline__ void static_for(F&& f, std::integer_sequence<int, I...>) {
  (f(std::integral_constant<int, I>{}), ...);
}
template <int... Is> using iseq = std::integer_sequence<int, Is...>;
template <int N> using make_iseq = std::make_integer_sequence<int, N>;

typedef __attribute__((ext_vector_type(4))) unsigned u32x4c;
typedef __attribute__((ext_vector_type(4))) float f32x4c;
typedef __attribute__((ext_vector_type(2))) float f32x2c;
typedef unsigned long long u64;
struct PR { u32x4c kk[7], vv[4]; };
__device__ __forceinline__ void prep_load(PR& p, int tid, int cg, int tb, const bf16* __restrict__ k, const bf16* __restrict__ v,
                                          const bf16* __restrict__ shift_k) {
  const __amdgpu_buffer_rsrc_t sk = __builtin_amdgcn_make_buffer_rsrc((void*)k, (short)0, SEQ * DA * 2, 0x00020000);
  const __amdgpu_buffer_rsrc_t sv = __builtin_amdgcn_make_buffer_rsrc((void*)v, (short)0, SEQ * DA * 2, 0x00020000);
  const __amdgpu_buffer_rsrc_t sw = __builtin_amdgcn_make_buffer_rsrc((void*)(shift_k + cg * 256), (short)0, 512, 0x00020000);
  const int vo = ((tb * 128 + (tid >> 3) * 4) * DA + cg * 64 + (tid & 7) * 8) * 2;
#pragma unroll
  for (int q = 0; q < 7; ++q)
    asm volatile("buffer_load_dwordx4 %0, %1, %2, 0 offen" : "=v"(p.kk[q]) : "v"(vo + (q - 3) * DA * 2), "s"(sk) : "memory");
#pragma unroll
  for (int q = 0; q < 4; ++q)
    asm volatile("buffer_load_dwordx4 %0, %1, %2, 0 offen offset:%3" : "=v"(p.vv[q]) : "v"(vo), "s"(sv), "i"(q * DA * 2) : "memory");
#pragma unroll
  for (int q = 0; q < 4; ++q)
    asm volatile("buffer_load_dwordx4 a[%c0:%c1], %2, %3, 0 offen offset:%4" ::"i"(4 * q), "i"(4 * q + 3), "v"((tid & 7) * 64), "s"(sw), "i"(q * 16) : "memory");
}
__device__ __forceinline__ float bfl(unsigned u) { return __uint_as_float(u << 16); }
__device__ __forceinline__ float bfh(unsigned u) { return __uint_as_float(u & 0xffff0000u); }
__device__ __forceinline__ f32x2c rbf2(f32x2c a) {
  return (f32x2c){__uint_as_float(pk2(0.f, a[0])), __uint_as_float(pk2(0.f, a[1]))};
}
template <int I> __device__ __forceinline__ void prep_math(const PR& p, int tid, char* lx) {
  const int rp = tid >> 3, c = tid & 7;
  f32x2c wt[4], kf[7];
  const unsigned w[4] = {__float_as_uint(acc_read1<4 * I>()), __float_as_uint(acc_read1<4 * I + 1>()), __float_as_uint(acc_read1<4 * I + 2>()),
                         __float_as_uint(acc_read1<4 * I + 3>())};
#pragma unroll
  for (int t = 0; t < 4; ++t)
    wt[t] = (t & 1) ? (f32x2c){bfh(w[t >> 1]), bfh(w[2 + (t >> 1)])} : (f32x2c){bfl(w[t >> 1]), bfl(w[2 + (t >> 1)])};
#pragma unroll
  for (int q = 0; q < 7; ++q) kf[q] = (f32x2c){bfl(p.kk[q][I]), bfh(p.kk[q][I])};
  f32x2c x[4];
#pragma unroll
  for (int e = 0; e < 4; ++e) {
    f32x2c s = rbf2(kf[e] * wt[3]);
    s = rbf2(s + rbf2(kf[e + 1] * wt[2]));
    s = rbf2(s + rbf2(kf[e + 2] * wt[1]));
    s = rbf2(s + rbf2(kf[e + 3] * wt[0]));
    x[e] = s * (f32x2c){bfl(p.vv[e][I]), bfh(p.vv[e][I])};
  }
#pragma unroll
  for (int u = 0; u < 2; ++u)
    *(u32x2*)(lx + (c * 8 + 2 * I + u) * 256 + (((31 - rp) * 8) ^ (c << 4))) = (u32x2){pk2(x[3][u], x[2][u]), pk2(x[1][u], x[0][u])};
}
__device__ __forceinline__ void prep_store(int tid, int cg, int tb, bf16* __restrict__ xr, const char* lx) {
  const __amdgpu_buffer_rsrc_t sx = __builtin_amdgcn_make_buffer_rsrc((void*)(xr + (size_t)cg * 64 * SEQ), (short)0, 64 * SEQ * 2, 0x00020000);
#pragma unroll
  for (int rd = 0; rd < 4; ++rd) {
    const int idx = tid + 256 * rd, ch = idx >> 4, q = idx & 15;
    u32x4c w = *(const u32x4c*)(lx + ch * 256 + 16 * (q ^ (ch >> 3)));
    __builtin_amdgcn_raw_buffer_store_b128(w, sx, (ch * SEQ + (15 - tb) * 128 + q * 8) * 2, 0, 16);
  }
}

#define CODE_TOUCH(LB, LE, wx, NW, NL, m0v) do { unsigned blo_, bhi_, elo_; \
    asm volatile("s_getpc_b64 s[48:49]\n\ts_add_u32 s48, s48, " LB "@rel32@lo+4\n\ts_addc_u32 s49, s49, " LB "@rel32@hi+12\n\t" \
                 "s_mov_b32 %0, s48\n\ts_mov_b32 %1, s49\n\t" \
                 "s_getpc_b64 s[48:49]\n\ts_add_u32 s48, s48, " LE "@rel32@lo+4\n\ts_mov_b32 %2, s48" \
                 : "=s"(blo_), "=s"(bhi_), "=s"(elo_) :: "scc"); \
    const unsigned n_ = elo_ - (blo_ & ~127u); \
    const __amdgpu_buffer_rsrc_t srd_ = __builtin_amdgcn_make_buffer_rsrc((void*)(((unsigned long long)bhi_ << 32) | (blo_ & ~127u)), (short)0, n_ < 262144u ? n_ : 0u, 0x00020000); \
    const unsigned l_ = threadIdx.x & 63, vo_ = l_ < (NL) ? ((wx) + l_ * (NW)) * 128u : 0x80000000u; \
    asm volatile("s_mov_b32 m0, %0\n\ts_nop 4\n\tbuffer_load_dword %1, %2, 0 offen lds" ::"s"(m0v), "v"(vo_), "s"(srd_) : "memory"); } while (0)
template <int I> __device__ __forceinline__ void d_store() {
  asm volatile("buffer_store_dwordx4 v[%c0:%c1], v%c2, s[88:91], s101 offen offset:%3 sc1" ::"i"(128 + 4 * I), "i"(131 + 4 * I), "i"(112 + ((I >> 2) & 1)),
               "i"((I & 3) * 1024) : "memory");
  if constexpr ((I & 7) == 7) asm volatile("s_addk_i32 s101, 0x4000" ::: "scc");
}
constexpr int NS_X = 19;
struct CV { unsigned pa[4], pb, px, vz, sz; float D; };
constexpr int cv_J(bool up, int q) { int J = up ? 0 : 7; while (q > J) { q -= J + 1; J += up ? 1 : -1; } return J; }
constexpr int cv_i(bool up, int q) { int J = up ? 0 : 7; while (q > J) { q -= J + 1; J += up ? 1 : -1; } return q; }
template <int R, int N> __device__ __forceinline__ void cv_rd(CV& c) {
  if constexpr (N < 16) {
    constexpr int i = N >> 1, o = 16 * R + 128 * (i & 1) + 2 * (N & 1), r = 32 + 32 * (R & 1) + 2 * N;
    asm volatile("ds_read2_b32 a[%c1:%c2], %0 offset0:%c3 offset1:%c4" ::"v"(c.pa[i >> 1]), "i"(r), "i"(r + 1), "i"(o), "i"(o + 1));
  } else {
    constexpr int m = N - 16, r = 192 + 32 * (R & 1) + 4 * m;
    asm volatile("ds_read_b128 a[%c1:%c2], %0 offset:%c3" ::"v"(c.pb), "i"(r), "i"(r + 3), "i"(64 * R + 512 * (7 - m)));
  }
}
template <int O> __device__ __forceinline__ void cv_xd(CV& c) {
  constexpr int e = O & 31, x = 96 + ((e >> 1) ^ 1);
  if constexpr (O >= 64) asm volatile("v_cvt_pk_bf16_f32 v%c0, 0, v%c0" ::"i"(160 + e));
  else if constexpr (O >= 32) asm volatile("v_mul_f32 v%c0, v%c0, %1" ::"i"(160 + e), "v"(c.D));
  else if constexpr (O & 1) asm volatile("v_lshlrev_b32 v%c0, 16, v%c1" ::"i"(160 + e), "i"(x));
  else asm volatile("v_and_b32 v%c0, 0xffff0000, v%c1" ::"i"(160 + e), "i"(x));
}
template <int O> __device__ __forceinline__ void cv_z(CV& c) {
  constexpr int J = O / 11, k = O % 11, y = 128 + 4 * J;
  if constexpr (k < 4) asm volatile("v_cvt_pk_bf16_f32 v%c0, 0, v%c0" ::"i"(y + k));
  else if constexpr (k < 8) asm volatile("v_add_f32 v%c0, v%c0, v%c1" ::"i"(y + k - 4), "i"(y + 28 + k));
  else if constexpr (k < 10) asm volatile("v_cvt_pk_bf16_f32 v%c0, v%c1, v%c2" ::"i"(y + k - 8), "i"(y + 2 * k - 16), "i"(y + 2 * k - 15));
  else asm volatile("buffer_store_dwordx2 v[%c0:%c1], %2, s[88:91], s101 offen sc1\n\ts_add_u32 s101, s101, 0x8000" ::"i"(y), "i"(y + 1), "v"(c.vz) : "memory");
}
constexpr int cv_z0(int Q) {
  int o = 0;
  for (int q = 0; q < Q; ++q)
    for (int k = 0; k < (q % 4 == 1 ? 0 : 3); ++k) { const int J = o / 11; if ((J + 1) * (J + 2) / 2 - 1 + (J ? 4 : 7) <= q) ++o; }
  return o;
}
constexpr int cv_xd0(int T) { int o = 0; for (int t = 0; t < T; ++t) o += t % 4 != 1 || (t >= 72 && t < 108); return o < 96 ? o : 96; }
static_assert(cv_xd0(144) == 96);
template <int B, int... Is> __device__ __forceinline__ void cv_zs(CV& c, iseq<Is...>) { (cv_z<B + Is>(c), ...); }
template <int R, int Q> __device__ __forceinline__ void cv_slot(CV& c) {
  constexpr int i = cv_i(R == 7, Q), J = cv_J(R == 7, Q), d = 128 + 4 * J, a = 32 + 32 * (R & 1) + 4 * i, b = 192 + 32 * (R & 1) + 4 * (J - i);
  constexpr int T = 36 * R + Q, vm = Q % 4 == 1 && Q < 32 ? Q / 4 : -1;
  if constexpr (Q == 0) asm volatile("s_waitcnt lgkmcnt(0)");
  if constexpr (R >= 6 && vm >= 0 && vm % 4 == 0) asm volatile("s_add_u32 m0, s79, %0" ::"i"(vm * 1024));
  if constexpr (R == 0 && i == 0)
    asm volatile("v_mfma_f32_16x16x32_bf16 v[%c0:%c1], a[%c2:%c3], a[%c4:%c5], 0" ::"i"(d), "i"(d + 3), "i"(a), "i"(a + 3), "i"(b), "i"(b + 3));
  else
    asm volatile("v_mfma_f32_16x16x32_bf16 v[%c0:%c1], a[%c2:%c3], a[%c4:%c5], v[%c0:%c1]" ::"i"(d), "i"(d + 3), "i"(a), "i"(a + 3), "i"(b), "i"(b + 3));
  if constexpr (Q < 24 && R < 7) cv_rd<R + 1, Q>(c);
  if constexpr (T % 7 == 3 && NS_X + 5 + T / 7 < 32) d_store<NS_X + 5 + T / 7>();
  if constexpr (R == 2 && Q >= 24 && Q < 32)
    asm volatile("ds_read_b64 v[%c1:%c2], %0 offset:%c3" ::"v"(c.px), "i"(96 + 2 * (Q - 24)), "i"(97 + 2 * (Q - 24)), "i"(512 * (31 - Q)));
  if constexpr (R >= 3 && R < 7) if constexpr (cv_xd0(T - 107) > cv_xd0(T - 108)) cv_xd<cv_xd0(T - 108)>(c);
  if constexpr ((R == 3 || R == 4) && vm >= 0) {
    constexpr int f = (R - 3) * 8 + vm;
    asm volatile("buffer_load_dwordx4 a[%c0:%c1], v84, s[88:91], s96 offen offset:%2 sc1" ::"i"(128 + 4 * f), "i"(131 + 4 * f), "i"((f & 3) * 1024) : "memory");
    if constexpr ((f & 3) == 3) asm volatile("s_addk_i32 s96, %0" ::"i"(f == 7 ? 28672 : 4096));
  }
  if constexpr (R == 4 && Q == 35) asm volatile(FL_POLL ::: "memory");
  if constexpr (R >= 6 && vm >= 0)
    asm volatile("buffer_load_dwordx4 v%c0, s[88:91], s98 offen offset:%1 sc1 lds" ::"i"(86 + vm / 4), "i"((vm & 3) * 1024) : "memory");
  if constexpr (R == 6 && Q == 35) asm volatile("s_add_u32 s98, s98, 0x8000\n\ts_xor_b32 s79, s79, 0x8000");
  if constexpr (R == 7) cv_zs<cv_z0(Q)>(c, make_iseq<cv_z0(Q + 1) - cv_z0(Q)>{});
  __builtin_amdgcn_sched_barrier(0);
}
template <int R, int... Q> __device__ __forceinline__ void cv_step(CV& c, iseq<Q...>) { (cv_slot<R, Q>(c), ...); }
template <int... N> __device__ __forceinline__ void cv_rd0(CV& c, iseq<N...>) { (cv_rd<0, N>(c), ...); }
template <int FLAG> __device__ __forceinline__ void conv_wave(unsigned sz, int ck, float Dd, char* lw) {
  int lane = threadIdx.x & 63; asm volatile("" : "+v"(lane));
  const int l16 = lane & 15, fq = lane >> 4;
  u32x4c w[5]; unsigned w4[5];
#pragma unroll
  for (int r = 0; r < 5; ++r) {
    w[r] = *(const u32x4c*)(lw + r * 1024 + lane * 16);
    w4[r] = *(const unsigned*)(lw + r * 1024 + lane * 16 + 16);
  }
  d_store<NS_X>(); d_store<NS_X + 1>();
#pragma unroll
  for (int r = 0; r < 5; ++r)
    *(u32x4c*)(lw + 10304 + r * 1024 + lane * 16) = (u32x4c){__builtin_amdgcn_alignbit(w[r][1], w[r][0], 16), __builtin_amdgcn_alignbit(w[r][2], w[r][1], 16),
                                                             __builtin_amdgcn_alignbit(w[r][3], w[r][2], 16), __builtin_amdgcn_alignbit(w4[r], w[r][3], 16)};
  d_store<NS_X + 2>();
  CV c;
#pragma unroll
  for (int i = 0; i < 4; ++i) c.pa[i] = (unsigned)(size_t)lw + ((l16 + 1) & 1) * 10304 + 4 * ((8 * fq + l16 + 1) >> 1) + 1024 * i;
  c.pb = (unsigned)(size_t)lw + 5120 + (2032 - 16 * l16 + 8 * fq) * 2 - 512 * 7;
  c.vz = (l16 >> 1) * 4096 + (((l16 & 1) * 2 + (fq >> 1)) ^ (ck >> 1)) * 16 + (fq & 1) * 8;
  c.px = (unsigned)(size_t)lw + 5120 + 2044 * 2 - 512 * 7 - (16 * l16 + 4 * fq) * 2;
  c.sz = sz; c.D = Dd;
  asm volatile("" : "+v"(c.vz), "+v"(c.px), "+v"(c.D) :: "memory");
  asm volatile("" : "+s"(c.sz));
  cv_rd0(c, make_iseq<24>{});
  d_store<NS_X + 3>(); d_store<NS_X + 4>();
  __builtin_amdgcn_sched_barrier(0);
  static_for([&](auto R) __attribute__((always_inline)) {
    cv_step<R>(c, make_iseq<36>{});
    if constexpr (R == 2) {
      asm volatile("s_waitcnt vmcnt(0)\n\ts_barrier" ::: "memory");
      fl_set<FLAG>();
      sset<101>(c.sz);
    }
    if constexpr (R == 5) {
      asm volatile("s_waitcnt vmcnt(0)" ::: "memory");
      fl_check();
    }
    __builtin_amdgcn_sched_barrier(0);
  }, make_iseq<8>{});
  asm volatile("s_nop 7");
  cv_zs<cv_z0(36)>(c, make_iseq<88 - cv_z0(36)>{});
  __builtin_amdgcn_sched_barrier(0);
}

__device__ __forceinline__ void post_dma(int cg, int tb, const bf16* __restrict__ q, unsigned m0w) {
  int lane = threadIdx.x & 63; asm volatile("" : "+v"(lane));
  const __amdgpu_buffer_rsrc_t sq = __builtin_amdgcn_make_buffer_rsrc((void*)(q + (size_t)tb * 32 * DA + cg * 64), (short)0, 32 * DA * 2, 0x00020000);
  const unsigned vq = ((lane >> 3) * DA + (lane & 7) * 8) * 2;
  asm volatile("s_mov_b32 m0, %0\n\ts_nop 4\n\tbuffer_load_dwordx4 %1, %2, 0 offen lds\n\tbuffer_load_dwordx4 %1, %2, %3 offen offset:1024 lds\n\t"
               "buffer_load_dwordx4 %1, %2, %4 offen offset:2048 lds\n\tbuffer_load_dwordx4 %1, %2, %5 offen offset:3072 lds"
               ::"s"(m0w + 4096), "v"(vq), "s"(sq), "s"(8 * DA * 2 - 1024), "s"(16 * DA * 2 - 2048), "s"(24 * DA * 2 - 3072) : "memory");
}
__device__ __forceinline__ void post_math(int cg, int tb, bf16* __restrict__ out, const char* lw) {
  int lane = threadIdx.x & 63; asm volatile("" : "+v"(lane));
  const int tl = lane >> 3, ck = lane & 7;
  const __amdgpu_buffer_rsrc_t so = __builtin_amdgcn_make_buffer_rsrc((void*)(out + (size_t)tb * 32 * OUTW + cg * 64), (short)0, 32 * OUTW * 2 - 2 * DA, 0x00020000);
  const unsigned vo = (tl * OUTW + ck * 8) * 2;
#pragma unroll
  for (int i = 0; i < 4; ++i) {
    const u32x4c qv = *(const u32x4c*)(lw + 4096 + i * 1024 + lane * 16);
    const char* lz = lw + ck * 64 + 16 * (i ^ (ck >> 1)) + tl * 2;
    u32x4c o;
#pragma unroll
    for (int e = 0; e < 4; ++e) {
      const float z0 = bf2f(*(const unsigned short*)(lz + 2 * e * 512));
      const float z1 = bf2f(*(const unsigned short*)(lz + (2 * e + 1) * 512));
      o[e] = pk2(bfl(qv[e]) * z0, bfh(qv[e]) * z1);
    }
    __builtin_amdgcn_raw_buffer_store_b128(o, so, vo + i * 8 * OUTW * 2, 0, 17);
  }
}

#define DI __device__ __forceinline__
#define GAP() __builtin_amdgcn_sched_barrier(0)
typedef __attribute__((ext_vector_type(4))) float f32x4g;
typedef __attribute__((ext_vector_type(4))) unsigned u32x4g;
constexpr int LDW = NH * HD;
struct GM { unsigned aA[2], aB[2][8]; };
template <bool W, int J, int V> DI void g_dma() {
  if constexpr (J == 0) asm volatile("buffer_load_dwordx4 v%c0, s[%c1:%c2], 0 offen lds" ::"i"(V), "i"(W ? 60 : 56), "i"(W ? 63 : 59) : "memory");
  else asm volatile("buffer_load_dwordx4 v%c0, s[%c1:%c2], s%c3 offen lds" ::"i"(V), "i"(W ? 60 : 56), "i"(W ? 63 : 59), "i"((W ? 70 : 63) + J) : "memory");
}
template <int P, int KS, int I> DI void g_wread(GM& g) {
  constexpr int nf = I >> 1, hf = I & 1, r = 192 - 64 * P + 8 * nf + 4 * KS + 2 * hf;
  asm volatile("ds_read_b64_tr_b16 a[%c1:%c2], %0 offset:%c3" ::"v"(g.aB[hf][nf]), "i"(r), "i"(r + 1), "i"(P * 32768 + KS * 16384));
}
template <int P, int M, int KS> DI void g_aread(GM& g) {
  constexpr int r = 96 + 8 * (M & 1) + 4 * KS;
  asm volatile("ds_read_b128 v[%c1:%c2], %0 offset:%c3" ::"v"(g.aA[KS]), "i"(r), "i"(r + 3), "i"(P * 32768 + M * 2048));
}
constexpr float CS = 0.0625f * 1.4426950408889634f;
constexpr int NS_Q = 19;
constexpr int e_vm(int G) { return (G * 3) % 8 < 3 ? G * 3 / 8 : -1; }
constexpr int e_st(int MODE, int G) {
  const int q = G & 15, k3 = ((G >> 4) - 1) * 3 + (q == 0 ? 0 : q == 4 ? 1 : 2), k2 = NS_Q + (G >> 4) * 2 + (q == 4);
  if (MODE == 5) return e_vm(G) >= 32 ? e_vm(G) - 32 : -1;
  if (MODE == 3) return G >= 33 && (G - 33) % 5 == 0 && (G - 33) / 5 < NS_X ? (G - 33) / 5 : -1;
  if (MODE == 2) return G >= 30 && (q == 0 || q == 4 || q == 14) && k3 - 2 < NS_Q ? k3 - 2 : -1;
  if (MODE == 4) return (q == 0 || q == 4) && k2 < 32 ? k2 : -1;
  return -1;
}
constexpr int e_nst(int MODE, int G0, int G1) { int n = 0; for (int G = G0; G < G1; ++G) n += e_st(MODE, G) >= 0; return n; }
static_assert(e_nst(2, 0, 128) == NS_Q && e_nst(3, 30, 128) == NS_X && e_nst(5, 0, 128) == 16 && e_nst(4, 0, 104) == 32 - NS_Q);
constexpr int e_slot(int MODE, int G) { return MODE == 3 ? (G % 3 == 0 && G < 30 ? G / 3 : -1) : e_vm(G) >= 0 && e_vm(G) < 32 ? 10 + e_vm(G) : -1; }
struct ES { int op0[129], nop[129]; };
constexpr ES e_sched(bool TR, int MODE) {
  ES z{};
  const bool SCL = MODE == 2 || MODE == 4;
  const int per = SCL ? 16 : 8, o1 = 32 * per;
  int o = MODE == 4 ? e_sched(false, 2).op0[128] : 0;
  for (int G = 0; G <= 128; ++G) {
    const int q = G & 15;
    int cap = 2 - (q == 1 || q == 2 || q == 15);
    if (SCL) cap -= (q >= 3 && q < 11 && (q & 1)) + (q == 7 || q == 11) + 2 * (G == 70 || G == 74 || (G & 63) == 61);
    cap += MODE == 2 ? 4 + (q == 10 || q == 13) : MODE == 3 ? 1 : MODE == 5 ? 2 : 0;
    if (e_st(MODE, G) >= 0 || (SCL ? q == 8 || q == 12 : e_slot(MODE, G) >= 0) || (MODE == 4 && G < 4)) cap = 0;
    if (G == 128) cap = 10000;
    z.op0[G] = o;
    while (o < o1) {
      const int i = o / per, rdy = MODE == 4 ? 0 : (TR ? 32 * (i >> 3) + 16 + 2 * (i & 7) + 1 : 4 * i + 3) + 6;
      if ((G < rdy && G < 128) || cap-- <= 0) break;
      ++o; ++z.nop[G];
    }
  }
  return z;
}
template <bool TR, int MODE> constexpr ES ESCH = e_sched(TR, MODE);
static_assert(ESCH<false, 4>.op0[96] == 512 && ESCH<false, 4>.op0[0] >= 24 * 16);
template <bool TR, bool SCL, int O> DI void e_op() {
  constexpr int per = SCL ? 16 : 8, i = O / per, k = O % per, r = 128 + 4 * i, t = 114 + 4 * (i & 1), c = k - (per - 4);
  if constexpr (k < 4) asm volatile("v_accvgpr_read_b32 v%c0, a[%c1]" ::"i"(t + k), "i"(4 * i + k));
  else if constexpr (c < 0) asm volatile("v_mul_f32 v%c0, s83, v%c0" ::"i"(k < 8 ? r + (k ^ 6) : t + k - 8));
  else {
    constexpr int d = c == 0 ? r + 3 : c == 1 ? r + 2 : r + c - 2, x = c == 0 ? r + 2 : c == 1 ? r : t + 2 * (c - 2);
    asm volatile("v_cvt_pk_bf16_f32 v%c0, v%c1, v%c2" ::"i"(d), "i"(x), "i"(x + 1));
  }
}
template <bool TR, bool SCL, int B, int... Is> DI void e_ops(iseq<Is...>) { (e_op<TR, SCL, B + Is>(), ...); }
template <int S> DI void e_m0() {
  if constexpr (S == 0 || S == 1 || S == 5 || S == 9) asm volatile("s_add_u32 m0, s78, %0" ::"i"(S * 1024));
  if constexpr (S >= 26 && (S - 26) % 4 == 0) asm volatile("s_add_u32 m0, s79, %0" ::"i"(((S - 26) >> 3) * 32768 + ((S - 26) & 4) * 1024));
}
template <int S> DI void e_load() {
  if constexpr (S == 0) asm volatile("buffer_load_dwordx4 v122, s[56:59], 0 offen lds" ::: "memory");
  else if constexpr (S < 5) asm volatile("buffer_load_dwordx4 v84, s[56:59], s64 offen offset:%0 lds" ::"i"((S - 1) * 1024) : "memory");
  else if constexpr (S < 9) asm volatile("buffer_load_dwordx4 v84, s[60:63], 0 offen offset:%0 sc1 lds" ::"i"((S - 5) * 1024) : "memory");
  else if constexpr (S == 9) asm volatile("buffer_load_dwordx4 v84, s[60:63], s62 offen sc1 lds" ::: "memory");
  else if constexpr (S < 26) {
    constexpr int f = S - 10;
    asm volatile("buffer_load_dwordx4 a[%c0:%c1], v84, s[88:91], s96 offen offset:%2 sc1" ::"i"(128 + 4 * f), "i"(131 + 4 * f), "i"((f & 3) * 1024) : "memory");
    if constexpr ((f & 3) == 3) asm volatile("s_addk_i32 s96, %0" ::"i"(f == 7 ? 28672 : 4096));
  } else {
    constexpr int f = S - 26;
    asm volatile("buffer_load_dwordx4 v%c0, s[88:91], s98 offen offset:%1 sc1 lds" ::"i"(86 + ((f >> 2) & 1)), "i"((f & 3) * 1024) : "memory");
    if constexpr (f == 7) asm volatile("s_add_u32 s98, s98, s97");
    if constexpr (f == 15) asm volatile(FL_POLL ::: "memory");
  }
}
template <bool TR, int P, int H, int G, int MODE> DI void g_gap(GM& g) {
  constexpr int m = 4 * H + (G >> 4), q = G & 15, n = q >> 1, ks = q & 1, GG = 64 * H + G;
  constexpr bool inV = TR ? m & 1 : n & 1, END = MODE == 3 || MODE == 5;
  constexpr int a = 96 + 8 * (m & 1) + 4 * ks, b = 192 - 64 * P + 8 * n + 4 * ks, d = (inV ? 128 : 0) + 4 * (TR ? 8 * (m >> 1) + n : 4 * m + (n >> 1));
  constexpr int J = END ? -1 : q == 8 ? 2 * (G >> 4) : q == 12 ? 2 * (G >> 4) + 1 : -1, gj = (J & 3) ^ ((J & 3) >> 1);
  if constexpr (END) { if constexpr (q == 0 && GG > 0) asm volatile("s_waitcnt lgkmcnt(0)"); e_m0<e_slot(MODE, GG)>(); }
  else if constexpr (q == 0 && G > 0) asm volatile("s_waitcnt lgkmcnt(4)");
  if constexpr (J == 0) asm volatile("s_add_u32 m0, s78, %0" ::"i"((P ^ H ^ 1) * 32768 + (1 - H) * 8192));
  else if constexpr (J == 4) asm volatile("s_add_u32 m0, s79, %0" ::"i"((P ^ H) * 32768 + (1 - H) * 16384));
  else if constexpr (J > 0 && (J & 3) == 1) asm volatile("s_bitset1_b32 m0, 12");
  else if constexpr (J > 0 && (J & 3) == 2) asm volatile("s_bitset1_b32 m0, %0" ::"i"(J < 4 ? 14 : 13));
  else if constexpr (J > 0) asm volatile("s_bitset0_b32 m0, 12");
#define GMF(D, X, Y, C) asm volatile("v_mfma_f32_16x16x32_bf16 " D "[%c0:%c1], " X ", " Y ", " C ::"i"(d), "i"(d + 3), "i"(a), "i"(a + 3), "i"(b), "i"(b + 3))
#define GMF2(D, C) do { if constexpr (TR) GMF(D, "v[%c2:%c3]", "a[%c4:%c5]", C); else GMF(D, "a[%c4:%c5]", "v[%c2:%c3]", C); } while (0)
  if constexpr ((MODE == 1 || MODE == 4) && ks == 0) { if constexpr (inV) GMF2("v", "0"); else GMF2("a", "0"); }
  else if constexpr (inV) GMF2("v", "v[%c0:%c1]");
  else GMF2("a", "a[%c0:%c1]");
#undef GMF2
#undef GMF
  if constexpr ((q == 1 || q == 2) && !(END && m == 7)) g_aread<(m < 7 ? P : P ^ 1), (m + 1) & 7, q - 1>(g);
  if constexpr (!END && q >= 3 && q < 11 && (q & 1)) g_wread<P ^ 1, H, (G >> 4) * 4 + (q - 3) / 2>(g);
  if constexpr (e_st(MODE, GG) >= 0) {
    static_assert(ESCH<TR, MODE>.op0[GG] >= (e_st(MODE, GG) + 1) * (END ? 8 : 16));
    d_store<e_st(MODE, GG)>();
  }
  if constexpr (J >= 0 && J < 4) g_dma<false, (1 - H) * 2 + (gj & 1) + (gj >> 1) * 4, 90>();
  else if constexpr (J >= 4) g_dma<true, (1 - H) * 4 + gj, 91>();
  if constexpr (MODE >= 2) e_ops<TR, !END, ESCH<TR, MODE>.op0[GG]>(make_iseq<ESCH<TR, MODE>.nop[GG]>{});
  if constexpr (END) {
    if constexpr (e_slot(MODE, GG) >= 0) e_load<e_slot(MODE, GG)>();
  } else {
    if constexpr (H == 1 && G == 6) asm volatile("v_lshl_add_u32 v90, s82, 7, v92\n\ts_and_b32 s82, s80, 15");
    if constexpr (H == 1 && G == 10) asm volatile("s_lshr_b64 s[80:81], s[80:81], 4\n\tv_lshl_add_u32 v91, s82, 19, v93");
    if constexpr (G == 61) {
      constexpr int nst = e_nst(MODE, H ? 29 : 0, GG + 1) + (MODE == 4 && !H ? e_nst(2, 93, 128) : 0);
      asm volatile("s_waitcnt vmcnt(%0) lgkmcnt(0)\n\ts_barrier" ::"i"(12 + nst) : "memory");
    }
  }
  if constexpr (END && GG == 127) {
    asm volatile("s_nop 7");
    e_ops<TR, false, ESCH<TR, MODE>.op0[128]>(make_iseq<ESCH<TR, MODE>.nop[128]>{});
  }
  GAP();
}
template <bool TR, int P, int H, int MODE, int... Gs> DI void g_half_(GM& g, iseq<Gs...>) { (g_gap<TR, P, H, Gs, MODE>(g), ...); }
template <bool TR, int P, int H, int MODE> DI void g_half(GM& g) { g_half_<TR, P, H, MODE>(g, make_iseq<64>{}); }
template <bool TR, int P, int MODE> DI void g_tile(GM& g) { g_half<TR, P, 0, MODE>(g); g_half<TR, P, 1, MODE>(g); }
template <int P, int... Is> DI void g_read0(GM& g, iseq<Is...>) {
  (g_wread<P, 0, Is>(g), ...); (g_wread<P, 1, Is>(g), ...);
  g_aread<P, 0, 0>(g); g_aread<P, 0, 1>(g);
}
DI void g_proW(unsigned g, unsigned mW) {
  asm volatile("v_lshl_add_u32 v91, %0, 18, v93\n\ts_mov_b32 m0, %1" ::"s"(g), "s"(mW));
  asm volatile("s_nop 0"); g_dma<true, 0, 91>();
  asm volatile("s_bitset1_b32 m0, 12\n\ts_nop 0"); g_dma<true, 1, 91>();
  asm volatile("s_bitset1_b32 m0, 13\n\ts_nop 0"); g_dma<true, 3, 91>();
  asm volatile("s_bitset0_b32 m0, 12\n\ts_nop 0"); g_dma<true, 2, 91>();
}
template <int PART = 3> DI void g_pro(unsigned i, unsigned mA, unsigned mW, unsigned lda) {
  i &= 3;
  if constexpr (PART & 1)
    asm volatile("v_add_u32 v90, %0, v92\n\tv_add_u32 v91, %1, v93\n\ts_mov_b32 m0, %2\n\ts_mov_b32 s54, %3" ::"s"(((i & 1) + (i >> 1) * 4) * 64 * lda),
                 "s"((i & 1) * 0x20000 + (i >> 1) * 256), "s"(mA + (i & 1) * 4096 + (i >> 1) * 16384), "s"(mW + i * 4096));
  if constexpr (PART == 1) return;
  g_dma<false, 0, 90>();
  asm volatile("s_bitset1_b32 m0, 13\n\ts_nop 0"); g_dma<false, 2, 90>();
  asm volatile("s_mov_b32 m0, s54\n\ts_nop 0"); g_dma<true, 0, 91>();
  asm volatile("s_bitset1_b32 m0, 14\n\ts_nop 0"); g_dma<true, 4, 91>();
  asm volatile("s_xor_b32 m0, s54, 0x8000\n\ts_nop 0\n\tbuffer_load_dwordx4 v91, s[60:63], s53 offen lds" ::: "memory", "scc");
}
DI void g_pro2(unsigned mA, unsigned mW) {
  asm volatile("v_add_u32 v90, 128, v92\n\ts_mov_b32 m0, %0" ::"s"(mA ^ 32768));
  asm volatile("s_nop 0"); g_dma<false, 0, 90>();
  asm volatile("s_bitset1_b32 m0, 12\n\ts_nop 0"); g_dma<false, 1, 90>();
  asm volatile("s_bitset1_b32 m0, 14\n\ts_nop 0"); g_dma<false, 5, 90>();
  asm volatile("s_bitset0_b32 m0, 12\n\ts_nop 0"); g_dma<false, 4, 90>();
  g_proW(3, mW ^ 49152);
}
template <bool TR, bool SCL, class HOOK> DI void g_loop(GM& g, unsigned evm, HOOK&& hook) {
  constexpr int nkt = SCL ? 7 : 16, P0 = SCL ? 0 : 1;
  asm volatile("" : "+s"(evm));
#pragma unroll 1
  for (int t = 1;; t += 2) {
    g_half<TR, P0 ^ 1, 0, 0>(g);
    if (__builtin_expect((evm >> t) & 1, 0)) { hook(t); GAP(); }
    g_half<TR, P0 ^ 1, 1, 0>(g);
    if (SCL && t + 2 >= nkt) break;
    g_half<TR, P0, 0, 0>(g);
    if constexpr (SCL) { if (__builtin_expect(t == 3, 0)) { hook(4); GAP(); } }
    else asm volatile("s_cmp_eq_u32 %0, 13\n\ts_cselect_b32 s78, 0x28000, s78" ::"s"(t) : "scc");
    g_half<TR, P0, 1, 0>(g);
    if (!SCL && t + 3 >= nkt) break;
  }
}

typedef __attribute__((ext_vector_type(4))) unsigned u32x4;
typedef __attribute__((ext_vector_type(4))) float f32x4;

constexpr int R_S = 128, R_P = 192, R_CI = 224, R_E = 96;
constexpr int R_TS = 112, R_RS = 114, R_M = 116;
constexpr int R_PD = 118, R_THR = 120, R_TMP = 121;
constexpr int r_dk(int q) { return q < 6 ? 90 + q : 120 + q; }
constexpr int S_V = 96, S_T = 97, S_K = 98, S_I = 99, S_EV = 100;
struct AC {
  unsigned rare;
  unsigned tb, vs, o0, o1;
};
DI bool redo() { u64 t; asm volatile("s_mov_b64 %0, s[54:55]" : "=s"(t)); return t != 0; }
template <int X, int F> DI void kread() {
  constexpr int r = 192 + 4 * (F & 15);
  asm volatile("ds_read_b128 a[%c0:%c1], v84 offset:%c2" ::"i"(r), "i"(r + 3), "i"(X * 32768 + F * 1024));
}
template <int X, int F> DI void vread() {
  constexpr int r = 192 + 4 * (F & 15);
  asm volatile("ds_read_b128 a[%c0:%c1], v85 offset:%c2" ::"i"(r), "i"(r + 3), "i"(X * 32768 + (F & 1) * 16384 + (F >> 1) * 1024));
}
template <int X, int... Fs> DI void kreads(iseq<Fs...>) { (kread<X, Fs>(), ...); }
template <int X, int J> DI void dma_m0() {
  if constexpr (J == 0) asm volatile("s_mov_b32 m0, s%c0" ::"i"(78 + X));
  else if constexpr (J == 4) asm volatile("s_or_b32 m0, s%c0, 0x1000" ::"i"(78 + X));
  else if constexpr (J == 8) asm volatile("s_bitset0_b32 m0, 16");
  else if constexpr (J == 12) asm volatile("s_bitset0_b32 m0, 12");
}
template <int J> DI void dma_at() {
  if constexpr (J < 8)
    asm volatile("buffer_load_dwordx4 v%c0, s[88:91], s96 offen offset:%1 sc1 lds" ::"i"(86 + (J >= 4)), "i"((J & 3) * 1024) : "memory");
  else
    asm volatile("buffer_load_dwordx4 v%c0, s[92:95], s98 offen offset:%1 sc1 lds" ::"i"(r_dk(J - 8)), "i"((J & 3) * 1024) : "memory");
}
template <int Q> DI void dma4k() {
  asm volatile("s_xor_b32 m0, s79, %0\n\ts_add_u32 s48, s98, %1\n\tbuffer_load_dwordx4 v%c2, s[88:91], s48 offen sc1 lds\n\t"
               "buffer_load_dwordx4 v%c2, s[88:91], s48 offen offset:1024 sc1 lds\n\tbuffer_load_dwordx4 v%c2, s[88:91], s48 offen offset:2048 sc1 lds\n\t"
               "buffer_load_dwordx4 v%c2, s[88:91], s48 offen offset:3072 sc1 lds"
               ::"i"((Q >> 1) * 32768 + (Q & 1) * 4096), "i"((Q >> 1) * 32768), "i"(86 + (Q & 1)) : "memory", "scc");
}
DI void acc_zero() { static_for([](auto I) { asm volatile("v_accvgpr_write_b32 a[%c0], 0" ::"i"((int)I)); }, make_iseq<128>{}); }
#define SMV 0
struct SMS { int ex[128], op0[128], nop[128], last; };
constexpr SMS sm_sched() {
  SMS z{};
  int et[32] = {}, e = 0;
  for (int t = 0; t < 128; ++t) {
    const int g = t & 15;
    const bool x = t < 48 ? (g >= 8 && (g & 1) && g < 15) : t < 64 ? (g >= 9 && g < 15) : (g >= 8 && g < 14);
    z.ex[t] = x && e < 32 ? e : -1;
    if (z.ex[t] >= 0) et[e++] = t;
  }
  int o = 0;
  for (int t = 0; t < 128; ++t) {
    const int g = t & 15;
    int cap = z.ex[t] >= 0 || t < 8 || (t < 48 && (g == 7 || (g >= 8 && !(g & 1)))) || t == 56 || t == 63 || t == 111 ? 0 : 1;
    z.op0[t] = o; z.nop[t] = 0;
    while (cap > 0 && o < 48 && et[2 * (o / 3) + (o % 3 == 0 ? 0 : 1)] + 2 <= t) {
      const int el = 2 * (o / 3) + o % 3;
      if (!(o % 3 < 2 && (el == 0 || el == 4))) --cap;
      ++o; ++z.nop[t]; z.last = t;
    }
  }
  for (int k = 16; k < 32; ++k) if (z.op0[et[k]] < 3 * ((k - 16) / 2 + 1)) z.last = 1000;
  return z;
}
constexpr SMS SM = sm_sched();
static_assert(SM.last <= 117);
constexpr int p_reg(int Y, int k) { return R_P + 16 * Y + 4 * ((k >> 3) * 2 + ((k >> 1) & 1)) + ((k >> 2) & 1) * 2 + (k & 1); }
template <int Y, int O> DI void sm_op() {
  constexpr int k = O / 3, e = 2 * k + O % 3, ts = R_TS + ((e >> 2) & 1);
  if constexpr (O % 3 == 2) asm volatile("v_cvt_pk_bf16_f32 v%c0, v%c1, v%c2" ::"i"(p_reg(Y, k)), "i"(R_E + ((2 * k) & 15)), "i"(R_E + ((2 * k + 1) & 15)));
  else if constexpr (e == 1 || e == 5) asm volatile("v_add_f32_e32 v%c0, v%c1, v%c2" ::"i"(ts), "i"(R_E + e - 1), "i"(R_E + e));
  else if constexpr (e != 0 && e != 4) asm volatile("v_add_f32_e32 v%c0, v%c0, v%c1" ::"i"(ts), "i"(R_E + (e & 15)));
}
template <int Y, int T> DI void sm_gap(AC& c) {
  if constexpr (SM.ex[T] >= 0) asm volatile("v_exp_f32 v%c0, v%c1" ::"i"(R_E + (SM.ex[T] & 15)), "i"(R_S + 32 * Y + SM.ex[T]));
  if constexpr (SM.nop[T] > 0) sm_op<Y, SM.op0[T]>();
  if constexpr (SM.nop[T] > 1) sm_op<Y, SM.op0[T] + 1>();
  if constexpr (SM.nop[T] > 2) sm_op<Y, SM.op0[T] + 2>();
  if constexpr (T == SM.last + 1) asm volatile("v_add_f32_e32 v%c0, v%c1, v%c2" ::"i"(R_TMP), "i"(R_TS), "i"(R_TS + 1));
  if constexpr (T == SM.last + 2) asm volatile("v_cmp_nle_f32_e64 s[54:55], v%c0, v%c1" ::"i"(R_TMP), "i"(R_THR));
}
template <int X, int MODE, int G> DI void gapA(AC& c) {
  constexpr bool FIRST = MODE == 1 || MODE == 2, NOQK = MODE == 3;
  constexpr int Xp = X ^ 1, kb = G >> 4, g = G & 15, qb = g >> 3, s = g & 7;
  if constexpr (MODE == 3 && G < 48 && G % 3 == 0) {
    constexpr int f = G / 3;
    asm volatile("buffer_load_dwordx4 a[%c0:%c1], v84, s[88:91], s75 offen offset:%2 sc1" ::"i"(128 + 4 * f), "i"(131 + 4 * f), "i"((f & 3) * 1024) : "memory");
    if constexpr ((f & 3) == 3) asm volatile("s_addk_i32 s75, %0" ::"i"(f == 7 ? 28672 : 4096));
  }
  if constexpr (G == 48) asm volatile("s_waitcnt vmcnt(%0) lgkmcnt(0)\n\ts_barrier" ::"i"(MODE == 3 ? 16 : MODE == 2 ? 12 : MODE == 1 ? 7 : 0) : "memory");
  else if constexpr (g == 0 && !NOQK) asm volatile("s_waitcnt lgkmcnt(0)");
  if constexpr (!NOQK) {
    constexpr int r = 192 + 4 * ((kb & 1) * 8 + s), q = 128 + 4 * g;
    constexpr int d = R_S + 32 * X + 4 * (kb * 2 + qb), ci = s == 0 ? R_CI + 4 * (kb * 2 + qb) : d;
    if constexpr (FIRST && s == 0)
      asm volatile("v_mfma_f32_16x16x32_bf16 v[%c0:%c1], a[%c2:%c3], a[%c4:%c5], 0" ::"i"(d), "i"(d + 3), "i"(r), "i"(r + 3), "i"(q), "i"(q + 3));
    else
      asm volatile("v_mfma_f32_16x16x32_bf16 v[%c0:%c1], a[%c2:%c3], a[%c4:%c5], v[%c6:%c7]" ::"i"(d), "i"(d + 3), "i"(r), "i"(r + 3), "i"(q), "i"(q + 3),
                   "i"(ci), "i"(ci + 3));
    if constexpr (g < 8 && kb < 3) kread<X, (kb + 1) * 8 + g>();
    constexpr int S0 = MODE == 1 ? 0 : SMV ? 40 : 14, S1 = MODE == 1 ? 1 : SMV ? 41 : 30, S2 = SMV ? 42 : 45, S3 = SMV ? 43 : 46;
    if constexpr (G == S0) asm volatile("s_add_u32 s96, s97, 0x1000000");
    if constexpr (G == S1) dma_m0<X, 0>();
    if constexpr (G == S2) asm volatile("s_mov_b32 s97, s98");
    if constexpr (G == S3) asm volatile("s_add_u32 s98, s98, s99");
    if constexpr (MODE != 1 && G >= 56 && !(G & 1)) dma_at<(G - 56) / 2>();
    if constexpr (MODE == 1 && (G & 11) == 9) dma_at<(G >> 4) * 2 + ((G >> 2) & 1)>();
    if constexpr (MODE == 1 && G == 31) dma_m0<X, 4>();
    if constexpr (G == 57) asm volatile("s_lshr_b32 s100, s100, 1\n\ts_add_u32 s56, s56, 1");
    if constexpr (G == 59) asm volatile("s_getpc_b64 s[86:87]\nev_pc%c0:\n\ts_add_u32 s86, s86, ev_ret%c0-ev_pc%c0\n\ts_addc_u32 s87, s87, 0" ::"i"(X * 4 + MODE));
    if constexpr (G == 61 && !FIRST) asm volatile("s_or_b32 s57, s54, s55\n\ts_mov_b32 %0, s57\n\ts_bitcmp1_b32 s100, 0" : "=s"(c.rare));
    if constexpr (G == 61 && FIRST) asm volatile("s_bitcmp1_b32 s100, 0");
  } else if constexpr (SM.nop[64 + G] > 0) asm volatile("s_nop 0");
  if constexpr (!FIRST && g < 8 && kb == 3) vread<Xp, g>();
  if constexpr (MODE == 2 && (G & 3) == 0) d_store<16 + G / 4>();
  if constexpr (!FIRST) sm_gap<Xp, 64 + G>(c);
  if constexpr (G == 63 && !NOQK) asm volatile("s_cbranch_scc1 ev_entry\nev_ret%c0:" ::"i"(X * 4 + MODE) : "vcc", "memory");
  GAP();
}
template <int R> DI void rescale1(float alpha) {
  float t;
  asm volatile("v_accvgpr_read_b32 %0, a[%c2]\n\tv_mul_f32 %0, %0, %1\n\tv_accvgpr_write_b32 a[%c2], %0" : "=&v"(t) : "v"(alpha), "i"(R));
}
template <int... Rs> DI void rescale_all(const float (&alpha)[2], iseq<Rs...>) { (rescale1<Rs>(alpha[(Rs >> 2) & 1]), ...); }
template <bool MAX> DI float row_reduce(float v) {
  auto p = __builtin_amdgcn_permlane32_swap(__float_as_uint(v), __float_as_uint(v), false, false);
  v = MAX ? fmaxf(__uint_as_float(p[0]), __uint_as_float(p[1])) : __uint_as_float(p[0]) + __uint_as_float(p[1]);
  p = __builtin_amdgcn_permlane16_swap(__float_as_uint(v), __float_as_uint(v), false, false);
  return MAX ? fmaxf(__uint_as_float(p[0]), __uint_as_float(p[1])) : __uint_as_float(p[0]) + __uint_as_float(p[1]);
}
template <int Y, int QB> DI float row_max() {
  constexpr int r = R_S + 32 * Y + 4 * QB;
  float mx;
  asm volatile("v_max_f32 %0, v%c1, v%c2\n\tv_max3_f32 %0, %0, v%c3, v%c4" : "=&v"(mx) : "i"(r), "i"(r + 1), "i"(r + 2), "i"(r + 3));
  static_for([&](auto K) {
    constexpr int q = r + 8 * (K + 1);
    float t = mx;
    asm volatile("v_max3_f32 %0, %0, v%c1, v%c2\n\tv_max3_f32 %0, %0, v%c3, v%c4" : "+v"(t) : "i"(q), "i"(q + 1), "i"(q + 2), "i"(q + 3));
    mx = t;
  }, make_iseq<3>{});
  return row_reduce<true>(mx);
}
template <int R> DI void v_sub(float d) { asm volatile("v_sub_f32 v%c0, v%c0, %1" ::"i"(R), "v"(d)); }
template <int B> DI void s_shift(const float (&d)[2]) { static_for([&](auto E) { v_sub<B + E>(d[(E >> 2) & 1]); }, make_iseq<32>{}); }
template <bool CI> DI void m_shift(const float (&d)[2]) {
  if constexpr (CI) s_shift<R_CI>(d);
  v_sub<R_M>(-d[0]); v_sub<R_M + 1>(-d[1]);
}
DI void set_mask0() {
  const int mr = __float_as_int(vget<89>());
  static_for([&](auto I) {
    constexpr int e = (I >> 2) * 8 + (I & 3), k = (I >> 2) * 16 + (I & 3);
    const float x = vget<R_S + e>();
    vset<R_S + e>(k > mr ? -INFINITY : x);
  }, make_iseq<16>{});
}
template <int X, bool CI = true> DI void sm_redo() {
  constexpr int Xp = X ^ 1;
  float d[2], dt[2], alpha[2], ts[2] = {0.f, 0.f};
  static_for([&](auto Q) {
    constexpr int qb = Q;
    const float pd = vget<R_PD + qb>();
    d[qb] = fmaxf(row_max<Xp, qb>() - pd, 0.f);
    dt[qb] = d[qb] + pd;
    vset<R_PD + qb>(d[qb]);
    alpha[qb] = __builtin_amdgcn_exp2f(-d[qb]);
    vset<R_RS + qb>(vget<R_RS + qb>() * alpha[qb]);
  }, make_iseq<2>{});
  vset<R_THR>(d[0] > 0.f || d[1] > 0.f ? -1.f : 4096.f);
  m_shift<CI>(d);
  static_for([&](auto K) {
    constexpr int k = K, qb = (k >> 1) & 1;
    const float e0 = __builtin_amdgcn_exp2f(vget<R_S + 32 * Xp + 2 * k>() - dt[qb]), e1 = __builtin_amdgcn_exp2f(vget<R_S + 32 * Xp + 2 * k + 1>() - dt[qb]);
    ts[qb] += e0; ts[qb] += e1;
    vset<p_reg(Xp, k)>(__uint_as_float(pk2(e0, e1)));
  }, make_iseq<16>{});
  vset<R_TS>(ts[0]); vset<R_TS + 1>(ts[1]);
  rescale_all(alpha, make_iseq<128>{});
  asm volatile("s_nop 1" ::: "memory");
  GAP();
}
template <int X, int MODE, int... Gs> DI void phaseA_(AC& c, iseq<Gs...>) { (gapA<X, MODE, Gs>(c), ...); }
template <int X, int MODE> DI void phaseA(AC& c) { phaseA_<X, MODE>(c, make_iseq<64>{}); }

template <int X, bool SMX, int G, bool FIRST = false> DI void gapB(AC& c) {
  constexpr int Xp = X ^ 1, blk = G >> 4, g = G & 15;
  if constexpr (g == 0) asm volatile("s_waitcnt lgkmcnt(0)");
  constexpr int qb = (g >> 1) & 1, s = g & 1, r = 192 + 4 * ((blk & 1) * 8 + (g >> 2) * 2 + s), o = 4 * ((blk * 4 + (g >> 2)) * 2 + qb);
  constexpr int p = R_P + 16 * Xp + 4 * (s * 2 + qb);
  if constexpr (FIRST && s == 0)
    asm volatile("v_mfma_f32_16x16x32_bf16 a[%c0:%c1], a[%c2:%c3], v[%c4:%c5], 0" ::"i"(o), "i"(o + 3), "i"(r), "i"(r + 3), "i"(p), "i"(p + 3));
  else
  asm volatile("v_mfma_f32_16x16x32_bf16 a[%c0:%c1], a[%c2:%c3], v[%c4:%c5], a[%c0:%c1]" ::"i"(o), "i"(o + 3), "i"(r), "i"(r + 3), "i"(p), "i"(p + 3));
  if constexpr (g < 8) {
    if constexpr (blk < 3) vread<Xp, (blk + 1) * 8 + g>();
    else if constexpr (SMX) kread<Xp, g>();
  }
  if constexpr (SMX) {
    if constexpr (G == 1 || G == 2) asm volatile("v_add_f32_e32 v%c0, v%c0, v%c1" ::"i"(R_RS + G - 1), "i"(R_TS + G - 1));
    if constexpr (g == 7 && blk < 3) dma_m0<X, blk * 4 + 4>();
    if constexpr (g >= 8 && !(g & 1) && blk < 3) dma_at<blk * 4 + 4 + (g - 8) / 2>();
    if constexpr (G == 55) asm volatile("s_cmp_eq_u32 s56, s76");
    if constexpr (G == 3) asm volatile("s_lshr_b32 s77, s77, 1");
    if constexpr (G == 4) asm volatile("s_bitcmp1_b32 s77, 0");
    if constexpr (G == 5) asm volatile("s_cselect_b64 s[52:53], s[62:63], 0");
    if constexpr (G == 6) asm volatile(FL_ARM("s[52:53]"));
    if constexpr (G == 56) asm volatile(FL_POLL ::: "memory");
    sm_gap<X, G>(c);
  }
  GAP();
}
template <int X, bool FIRST, int... Gs> DI void phaseB_(AC& c, iseq<Gs...>) { (gapB<X, true, Gs, FIRST>(c), ...); }
template <int X, bool FIRST = false> DI void phaseB(AC& c) { phaseB_<X, FIRST>(c, make_iseq<64>{}); }
constexpr int sm_nex() { int n = 0; for (int t = 0; t < 64; ++t) n += SM.ex[t] >= 0; return n; }
template <int X> DI void phaseB0(unsigned mode, bool vh) {
  dma_m0<X, 4>();
  kreads<X ^ 1>(make_iseq<8>{});
  if (vh) { dma_at<4>(); dma_at<5>(); dma_at<6>(); dma_at<7>(); }
  dma_m0<X, 8>();
  asm volatile("s_nop 3");
  float d[2];
  if (mode & 2) {
    d[0] = vget<122>(); d[1] = vget<123>();
    dma_at<8>();
  } else {
    if constexpr (X == 0) if (__builtin_expect(mode & 4, 0)) set_mask0();
    d[0] = row_max<X, 0>(); GAP(); dma_at<8>();
    d[1] = row_max<X, 1>();
#pragma unroll
    for (int qb = 0; qb < 2; ++qb) if (!(d[qb] > -3.0e38f)) d[qb] = 0.f;
  }
  GAP();
  vset<R_M>(d[0]); vset<R_M + 1>(d[1]);
  dma_at<9>();
  const f32x2c nd[2] = {{-d[0], -d[0]}, {-d[1], -d[1]}};
  static_for([&](auto E) {
    constexpr int e = E;
    const f32x2c v = nd[(e >> 1) & 1];
    asm volatile("v_pk_add_f32 v[%c0:%c1], v[%c0:%c1], %4\n\tv_mov_b64 v[%c2:%c3], %4" ::"i"(R_S + 32 * X + 2 * e), "i"(R_S + 32 * X + 2 * e + 1), "i"(R_CI + 2 * e), "i"(R_CI + 2 * e + 1), "v"(v));
    if constexpr (e == 7 || e == 15) dma_at<10 + e / 8>();
  }, make_iseq<16>{});
  static_assert(SM.op0[64] >= 3 * ((sm_nex() - 15) / 2) && SM.op0[64] <= 24);
  static_for([&](auto E) {
    constexpr int e = E;
    if constexpr (e < sm_nex()) asm volatile("v_exp_f32 v%c0, v%c1" ::"i"(R_E + e), "i"(R_S + 32 * X + e));
    if constexpr (e == 2) dma_m0<X, 12>();
    if constexpr (e == 5 || e == 10 || e == 15) dma_at<11 + e / 5>();
  }, make_iseq<16>{});
  static_for([&](auto O) { sm_op<X, O>(); }, make_iseq<SM.op0[64]>{});
  dma_at<15>();
  static_for([&](auto E) { asm volatile("v_exp_f32 v%c0, v%c1" ::"i"(R_E + (int)E), "i"(R_S + 32 * X + 16 + (int)E)); }, make_iseq<(sm_nex() > 16 ? sm_nex() - 16 : 0)>{});
  asm volatile("buffer_store_dwordx2 v[%c0:%c1], v88, s[88:91], s76 offen sc1\n\ts_bitcmp1_b32 s77, 0\n\ts_cselect_b64 s[52:53], s[62:63], 0\n\t"
               FL_ARM("s[52:53]") "\n\t" FL_POLL ::"i"(R_M), "i"(R_M + 1) : "memory", "scc");
  GAP();
}
enum : int { T_Q0, T_K0, T_OQ, T_OG, T_FPREP, T_FQ, T_FG, T_FS, T_ZO, T_CNT, T_NPRE = 10, T_NST = 12, T_TAB = 14, T_SLAB = 16, T_PART, T_IT = 18, T_ITN = 23 };
#define MSK(R, K) "v_cmp_gt_i32 s[48:49], " #K ", v89\n\tv_cmp_gt_i32 s[50:51], " #K "+1, v89\n\tv_cmp_gt_i32 s[52:53], " #K "+2, v89\n\t" \
                  "v_cmp_gt_i32 s[54:55], " #K "+3, v89\n\tv_cndmask_b32_e64 v[" #R "], v[" #R "], v122, s[48:49]\n\t" \
                  "v_cndmask_b32_e64 v[" #R "+1], v[" #R "+1], v122, s[50:51]\n\tv_cndmask_b32_e64 v[" #R "+2], v[" #R "+2], v122, s[52:53]\n\t" \
                  "v_cndmask_b32_e64 v[" #R "+3], v[" #R "+3], v122, s[54:55]\n\t"
DI void ev_code(AC& c) {
  GAP();
  asm volatile(
    "s_getpc_b64 s[82:83]\n\ts_add_u32 s82, s82, 12\n\ts_addc_u32 s83, s83, 0\n\ts_branch 9f\nev_entry:\n\t"
    "s_bitcmp1_b32 s60, s56\n\ts_cbranch_scc1 3f\n\ts_bitcmp1_b32 s74, s56\n\ts_cbranch_scc1 2f\n\t"
    "s_bitcmp1_b32 s58, s56\n\ts_cselect_b64 vcc, -1, 0\n\tv_cndmask_b32 v121, -2.0, v81, vcc\n\t"
    "buffer_store_dwordx2 v[82:83], v121, s[88:91], s70 offen sc1\n\tbuffer_store_dwordx2 v[82:83], v121, s[88:91], s71 offen sc1\n\t"
    "v_cmp_eq_u64 vcc, s[84:85], v[124:125]\n\ts_andn2_b64 vcc, s[62:63], vcc\n\ts_cbranch_scc1 5f\n"
    "0:\n\ts_add_u32 s98, s98, s59\n\ts_mov_b32 s59, s61\n\ts_mov_b32 s61, s72\n\ts_mov_b64 s[62:63], s[64:65]\n\ts_mov_b64 s[64:65], s[66:67]\n\t"
    "s_mov_b64 s[66:67], s[68:69]\n\ts_cmp_eq_u32 s56, 1\n\ts_cbranch_scc1 ev_first\n\ts_setpc_b64 s[86:87]\n"
    "5:\n\t" FL_CHECK("s[62:63]") "\n\ts_branch 0b\n"
    "6:\n\t" FL_CHECK("s[62:63]") "\n\ts_setpc_b64 s[86:87]\n"
    "2:\n\ts_mov_b32 s98, 0\n\t"
    "v_lshrrev_b32 v90, 7, v86\n\tv_and_b32 v91, 6, v90\n\tv_lshlrev_b32 v91, 4, v91\n\tv_and_b32 v92, 0x70, v86\n\tv_xor_b32 v91, v91, v92\n\t"
    "s_movk_i32 s48, 896\n\tv_mad_u32_u24 v94, v90, s48, v91\n\ts_mov_b64 s[92:93], s[80:81]\n\ts_mov_b32 s94, 229376\n\t"
    "v_add_u32 v90, 28672, v94\n\tv_add_u32 v91, 34816, v94\n\tv_add_u32 v92, 40960, v94\n\tv_add_u32 v93, 47104, v94\n\t"
    "v_add_u32 v95, 6144, v94\n\tv_add_u32 v126, 12288, v94\n\tv_add_u32 v127, 18432, v94\n\t"
    "v_mov_b32 v122, 0xff800000\n\t" MSK(228, 0) MSK(236, 16) MSK(244, 32) MSK(252, 48)
    "v_cmp_eq_u64 vcc, s[84:85], v[124:125]\n\ts_andn2_b64 vcc, s[62:63], vcc\n\ts_cbranch_scc1 6b\n\ts_setpc_b64 s[86:87]\n"
    "3:\n\tv_mov_b32 v122, 0xff800000\n\t" MSK(224, 0) MSK(232, 16) MSK(240, 32) MSK(248, 48)
    "v_readlane_b32 s54, v78, %0\n\ts_add_u32 m0, s78, 0x10000\n\t"
    "s_bfe_u32 s53, s73, 0x80010\n\ts_mulk_i32 s53, 3840\n\ts_mov_b64 s[48:49], s[80:81]\n\ts_add_u32 s50, s53, 3840\n\ts_min_u32 s50, s50, 229376\n\t"
    "s_mov_b32 s51, 0x20000\n\t"
    "v_lshlrev_b32 v121, 3, v84\n\tbuffer_load_dword v121, s[48:51], s53 offen lds\n\t"
    "s_bitcmp0_b32 s73, 3\n\ts_cbranch_scc1 4f\n\t"
    "buffer_load_dwordx4 v84, s[88:91], s54 offen sc1 lds\n\tbuffer_load_dwordx4 v84, s[88:91], s54 offen offset:1024 sc1 lds\n\t"
    "buffer_load_dwordx4 v84, s[88:91], s54 offen offset:2048 sc1 lds\n\tbuffer_load_dwordx4 v84, s[88:91], s54 offen offset:3072 sc1 lds\n"
    "4:\n\ts_cmp_eq_u32 s56, 1\n\ts_cbranch_scc1 ev_first\n\ts_setpc_b64 s[86:87]\n"
    ::"i"((int)T_ZO) : "scc");
  asm volatile("ev_zero:");
  acc_zero();
  asm volatile("s_setpc_b64 s[86:87]\nev_first:\n\ts_cmp_lg_u32 s57, 0\n\ts_cbranch_scc1 ev_zero\n\ts_bitcmp0_b32 s73, 4\n\ts_cbranch_scc1 ev_first1");
  GAP();
  phaseB<0, true>(c);
  asm volatile("s_branch att_loop\nev_first1:");
  phaseB<1, true>(c);
  asm volatile("s_branch att_mid\n9:");
  GAP();
}
DI void ev_call() { asm volatile("s_swappc_b64 s[86:87], s[82:83]" ::: "vcc", "scc", "memory"); GAP(); }

#define ACLOB_ALL "a0","a1","a2","a3","a4","a5","a6","a7","a8","a9","a10","a11","a12","a13","a14","a15","a16","a17","a18","a19","a20","a21","a22","a23","a24","a25","a26","a27","a28","a29","a30","a31","a32","a33","a34","a35","a36","a37","a38","a39","a40","a41","a42","a43","a44","a45","a46","a47","a48","a49","a50","a51","a52","a53","a54","a55","a56","a57","a58","a59","a60","a61","a62","a63","a64","a65","a66","a67","a68","a69","a70","a71","a72","a73","a74","a75","a76","a77","a78","a79","a80","a81","a82","a83","a84","a85","a86","a87","a88","a89","a90","a91","a92","a93","a94","a95","a96","a97","a98","a99","a100","a101","a102","a103","a104","a105","a106","a107","a108","a109","a110","a111","a112","a113","a114","a115","a116","a117","a118","a119","a120","a121","a122","a123","a124","a125","a126","a127","a128","a129","a130","a131","a132","a133","a134","a135","a136","a137","a138","a139","a140","a141","a142","a143","a144","a145","a146","a147","a148","a149","a150","a151","a152","a153","a154","a155","a156","a157","a158","a159","a160","a161","a162","a163","a164","a165","a166","a167","a168","a169","a170","a171","a172","a173","a174","a175","a176","a177","a178","a179","a180","a181","a182","a183","a184","a185","a186","a187","a188","a189","a190","a191","a192","a193","a194","a195","a196","a197","a198","a199","a200","a201","a202","a203","a204","a205","a206","a207","a208","a209","a210","a211","a212","a213","a214","a215","a216","a217","a218","a219","a220","a221","a222","a223","a224","a225","a226","a227","a228","a229","a230","a231","a232","a233","a234","a235","a236","a237","a238","a239","a240","a241","a242","a243","a244","a245","a246","a247","a248","a249","a250","a251","a252","a253","a254","a255"

struct EP { float fa[2], fb[2]; };
template <int I, int E, int MODE> DI void epi_pair(const EP& p) {
  constexpr int qb = I & 1, r = 16 * (I >> 1) + 4 * qb + (E >> 1) * 8 + (E & 1) * 2, t = 96 + 2 * E, w = 128 + 4 * I + E;
  if constexpr (MODE == 2) asm volatile("v_accvgpr_read_b32 v106, a[%c0]" ::"i"(w));
  asm volatile("v_accvgpr_read_b32 v%c0, a[%c2]\n\tv_accvgpr_read_b32 v%c1, a[%c3]" ::"i"(t), "i"(t + 1), "i"(r), "i"(r + 1));
  if constexpr (MODE) asm volatile("v_mul_f32 v%c0, v%c0, %2\n\tv_mul_f32 v%c1, v%c1, %2" ::"i"(t), "i"(t + 1), "v"(p.fa[qb]));
  if constexpr (MODE == 2)
    asm volatile("v_lshlrev_b32 v104, 16, v106\n\tv_and_b32 v105, 0xffff0000, v106\n\tv_fmac_f32 v%c0, v104, %2\n\tv_fmac_f32 v%c1, v105, %2"
                 ::"i"(t), "i"(t + 1), "v"(p.fb[qb]));
  asm volatile("v_cvt_pk_bf16_f32 v%c0, v%c1, v%c2" ::"i"(w), "i"(t), "i"(t + 1));
}
template <int I> DI void epi_store() {
  asm volatile("buffer_store_dwordx4 v[%c0:%c1], v84, s[88:91], s101 offen offset:%2 sc1" ::"i"(128 + 4 * I), "i"(131 + 4 * I), "i"((I & 3) * 1024) : "memory");
  if constexpr ((I & 3) == 3) asm volatile("s_addk_i32 s101, 4096");
}
constexpr int R_TB = 232, R_VS = 236;
template <int HF, int R> DI void p_read() {
  constexpr int mb = R >> 4, r = 192 - 64 * HF + 2 * R;
  asm volatile("ds_read_b64_tr_b16 a[%c1:%c2], v%c0 offset:%c3" ::"i"(R_TB + 2 * HF + mb), "i"(r), "i"(r + 1), "i"((R & 15) * 2048));
}
template <int QB, int E> DI void p_cvt() {
  asm volatile("v_cvt_pk_bf16_f32 v%c0, v%c1, v%c2" ::"i"(208 + 4 * QB + E), "i"(192 + 8 * QB + 2 * E), "i"(193 + 8 * QB + 2 * E));
}
template <int QB, int PR> DI void p_store() {
  asm volatile("buffer_store_dwordx4 v[%c0:%c1], v%c2, s[88:91], s96 offen offset:%3 sc1" ::"i"(208 + 4 * QB), "i"(211 + 4 * QB), "i"(R_VS + QB),
               "i"(PR * 128) : "memory");
}
constexpr int p_dma(int HF, int G) { return G == 3 && !HF ? 0 : G == 11 ? 1 - HF : G == 15 ? 2 - HF : G == 19 ? 3 - HF : G == 27 && HF ? 3 : -1; }
template <int HF, bool LD, int G> DI void p_gap() {
  constexpr int ch = G >> 3, t = G & 7, a = 192 - 64 * HF + 32 * (ch & 1) + 4 * t, b = 128 + 4 * (2 * t + (ch >> 1)), d = 192 + 4 * ch;
  if constexpr (t == 0)
    asm volatile("v_mfma_f32_16x16x32_bf16 v[%c0:%c1], a[%c2:%c3], v[%c4:%c5], 0" ::"i"(d), "i"(d + 3), "i"(a), "i"(a + 3), "i"(b), "i"(b + 3));
  else
    asm volatile("v_mfma_f32_16x16x32_bf16 v[%c0:%c1], a[%c2:%c3], v[%c4:%c5], v[%c0:%c1]" ::"i"(d), "i"(d + 3), "i"(a), "i"(a + 3), "i"(b), "i"(b + 3));
  p_read<HF ^ 1, G>();
  if constexpr (G == 0) asm volatile("v_xor_b32 v%c0, s100, v%c0" ::"i"(R_TB + 2 * HF + 1));
  if constexpr (G == 17) asm volatile("v_xor_b32 v%c0, s100, v%c0" ::"i"(R_TB + 2 * (HF ^ 1)));
  if constexpr (G >= 4 && G <= 8 && G != 7) p_cvt<1, (G < 7 ? G - 4 : 3)>();
  if constexpr (G == 24 || (G == 7 && HF == 0)) asm volatile("s_waitcnt lgkmcnt(8)");
  if constexpr (G == 7 && HF == 1) asm volatile("s_waitcnt vmcnt(%0) lgkmcnt(8)\n\ts_barrier" ::"i"(LD ? 13 : 5) : "memory");
  if constexpr (G == 10) p_store<1, HF ^ 1>();
  if constexpr (G >= 20 && G < 24) p_cvt<0, G - 20>();
  if constexpr (G == 25) p_store<0, HF>();
  if constexpr (LD && p_dma(HF, G) >= 0) dma_at<(HF ? 8 : 12) + p_dma(HF, G)>();
  if constexpr (LD && HF == 0 && G == 2) asm volatile("s_bitset0_b32 m0, 12");
  if constexpr (HF == 0 && G == 13) asm volatile("s_add_u32 s96, s96, 0x8000");
  if constexpr (HF == 0 && G == 26) asm volatile("s_add_u32 s98, s98, 128");
  if constexpr (LD && HF == 0 && G == 27) asm volatile("s_cmp_lt_u32 s98, 0x380");
  if constexpr (HF == 1 && G == 1) asm volatile("s_xor_b32 s100, s100, 0x10000");
  if constexpr (LD && HF == 1 && G == 2) asm volatile("s_xor_b32 s97, s97, s100");
  if constexpr (LD && HF == 1 && G == 9) asm volatile("s_or_b32 m0, s97, 0x1000");
  if constexpr (!LD && HF == 1 && G == 30) asm volatile("s_cmp_lt_u32 s98, 0x500");
  GAP();
}
template <int HF, bool LD, int... Gs> DI void p_half(iseq<Gs...>) { (p_gap<HF, LD, Gs>(), ...); }
template <int G> DI void gapF(AC& c, const EP& p) {
  if constexpr (G < 64) gapB<0, false, G>(c);
  if constexpr (G >= 10) epi_pair<(G - 10) / 4, (G - 10) & 3, 0>(p);
  if constexpr (G >= 14 && ((G - 14) & 3) == 0) epi_store<(G - 14) / 4>();
  if constexpr (G == 73) epi_store<15>();
  GAP();
}
template <int... Gs> DI void phaseF(AC& c, const EP& p, iseq<Gs...>) { (gapF<Gs>(c, p), ...); }
template <int G> DI void gapAL(AC& c) {
  constexpr int kb = G >> 3, s = G & 7, r = 192 + 4 * (G & 15), q = 160 + 4 * s, d = R_S + 36 + 8 * kb, ci = s == 0 ? R_CI + 4 + 8 * kb : d;
  if constexpr (G == 0) asm volatile("s_add_u32 s96, s97, 0x1000000\n\ts_mov_b32 m0, s79\n\ts_waitcnt vmcnt(9) lgkmcnt(0)\n\ts_barrier" ::: "memory");
  if constexpr (G >= 8 && (G & 3) == 0) asm volatile("s_waitcnt lgkmcnt(4)");
  if constexpr (G == 29) asm volatile("s_barrier" ::: "memory");
  asm volatile("v_mfma_f32_16x16x32_bf16 v[%c0:%c1], a[%c2:%c3], a[%c4:%c5], v[%c6:%c7]" ::"i"(d), "i"(d + 3), "i"(r), "i"(r + 3), "i"(q), "i"(q + 3),
               "i"(ci), "i"(ci + 3));
  if constexpr (G < 24) kread<1, G + 8>();
  else vread<0, G - 24>();
  if constexpr (G % 3 == 1 && G < 24) dma_at<G / 3>();
  if constexpr (G == 11) asm volatile("s_bitset1_b32 m0, 12");
  if constexpr (G == 28) asm volatile("buffer_load_dwordx4 v[122:125], v88, s[88:91], s76 offen sc1" ::: "memory");
  if constexpr (G == 30) asm volatile("s_xor_b32 m0, s79, 0x11000\n\ts_movk_i32 s98, 128");
  sm_gap<0, 64 + 2 * G>(c); sm_gap<0, 65 + 2 * G>(c);
  GAP();
}
template <int... Gs> DI void phaseAL(AC& c, iseq<Gs...>) { (gapAL<Gs>(c), ...); }
struct BLS { int ex[65], op[65], vm[65], su[65]; };
constexpr BLS bl_sched(bool LD) {
  BLS z{};
  z.op[0] = 1;
  for (int t = 0; t < 64; ++t) {
    const int g = t & 15, o = z.op[t];
    const bool x = t == 6 || (t < 48 && g > 8 && g != 12 && g != 15);
    const bool v = LD ? (t & 3) == 0 : (t & 11) == 8;
    const bool p = t >= 15 && o < 24 && (t & 3) != 0 && t != 47 && !x && z.ex[t - 1] > 2 * (o / 3) + (o % 3 != 0);
    z.ex[t + 1] = z.ex[t] + x; z.vm[t + 1] = z.vm[t] + v; z.op[t + 1] = o + p;
    z.su[t + 1] = z.su[t] + (t >= 1 && z.su[t] < (LD ? 16 : 10) && g != 15 && !v && !x && (LD || t > 55 || (t > 3 && !p)));
  }
  return z;
}
template <bool LD> constexpr BLS BL = bl_sched(LD);
static_assert(BL<true>.ex[64] == 16 && BL<true>.op[61] == 24 && BL<true>.vm[64] == 16 && BL<false>.vm[64] == 8);
static_assert(BL<true>.su[56] == 16 && BL<false>.su[62] == 10);
template <bool LD, int G> DI void gapBL(AC& c) {
  if constexpr (G == 48) asm volatile("s_waitcnt vmcnt(%0) lgkmcnt(0)\n\ts_barrier" ::"i"(BL<LD>.vm[48]) : "memory");
  gapB<1, false, G>(c);
  if constexpr (G >= 48 && G < 56) vread<1, G - 48>();
  if constexpr (BL<LD>.vm[G + 1] > BL<LD>.vm[G]) {
    constexpr int k = BL<LD>.vm[G] + (LD ? 0 : 16), I = 2 * (k & 7) + ((k >> 3) & 1);
    if constexpr (k < 16) {
      asm volatile("buffer_load_dwordx4 a[%c0:%c1], v84, s[88:91], s75 offen offset:%2 sc1" ::"i"(128 + 4 * I), "i"(131 + 4 * I), "i"((I & 3) * 1024) : "memory");
      if constexpr (k & 1) asm volatile("s_addk_i32 s75, %0" ::"i"(k == 7 ? -12288 : 4096));
    } else dma_at<k - 8>();
    if constexpr (k == 19) asm volatile("s_bitset0_b32 m0, 12");
  }
  if constexpr (G == 1 || G == 2) asm volatile("v_add_f32_e32 v%c0, v%c0, v%c1" ::"i"(R_RS + G - 1), "i"(R_TS + G - 1));
  if constexpr (G == 3) asm volatile("v_mov_b32 v%c0, 0" ::"i"(R_TS));
  if constexpr (BL<LD>.ex[G + 1] > BL<LD>.ex[G]) {
    constexpr int j = BL<LD>.ex[G];
    asm volatile("v_exp_f32 v%c0, v%c1" ::"i"(R_E + j), "i"(R_S + 36 + 8 * (j >> 2) + (j & 3)));
  }
  if constexpr (BL<LD>.op[G + 1] > BL<LD>.op[G]) {
    constexpr int o = BL<LD>.op[G], k = o / 3, j = 2 * k + o % 3;
    if constexpr (o % 3 == 2) asm volatile("v_cvt_pk_bf16_f32 v%c0, v%c1, v%c2" ::"i"(p_reg(1, (k >> 1) * 4 + 2 + (k & 1))), "i"(R_E + 2 * k), "i"(R_E + 2 * k + 1));
    else asm volatile("v_add_f32_e32 v%c0, v%c1, v%c2" ::"i"(R_TS + 1), "i"(j == 1 ? R_E : R_TS + 1), "i"(R_E + j));
  }
  if constexpr (G == 61) asm volatile("v_cmp_nle_f32_e64 s[54:55], v%c0, v%c1" ::"i"(R_TS + 1), "i"(R_THR));
  if constexpr (BL<LD>.su[G + 1] > BL<LD>.su[G]) {
    constexpr int k = BL<LD>.su[G];
    if constexpr (k < 4) asm volatile("v_xor_b32 v%c0, %1, %2" ::"i"(R_TB + k), "i"(32 * k), "v"(c.tb));
    else if constexpr (k < 6) asm volatile("v_add_u32 v%c0, %1, %2" ::"i"(R_VS + k - 4), "i"(4096 * (k - 4)), "v"(c.vs));
    else if constexpr (k == 6) tload<96, T_SLAB>();
    else if constexpr (k == 7) tload<101, T_CNT>();
    else if constexpr (k == 8) asm volatile("v_mov_b64 v[240:241], 1");
    else if constexpr (k == 9) asm volatile("s_mov_b32 s97, s79\n\ts_mov_b32 s100, 0x8000");
    else if constexpr (k < 14) asm volatile("v_mov_b32 v%c0, %1" ::"i"(k < 12 ? 214 + k : 218 + k), "v"(k & 1 ? c.o1 : c.o0));
    else asm volatile("v_mov_b64 v[%c0:%c1], 0" ::"i"(198 + 2 * k), "i"(199 + 2 * k));
  }
  GAP();
}
template <bool LD, int... Gs> DI void phaseBL(AC& c, iseq<Gs...>) { (gapBL<LD, Gs>(c), ...); }
template <int MODE, int G> DI void gapFL(const EP& p) {
  constexpr int x = G - (MODE == 3 ? 4 : 0), J = MODE == 1 ? (G < 24 && G % 3 == 1 ? G / 3 : -1) : G < 27 && G % 5 != 0 && G % 5 != 3 ? G / 5 * 3 + (G % 5) / 2 : -1;
  if constexpr (G == 0 && MODE == 1) asm volatile("s_or_b32 m0, s78, 0x1000\n\ts_movk_i32 s98, 256\n\ts_waitcnt lgkmcnt(0)");
  if constexpr (G == 0 && MODE > 1) asm volatile("s_waitcnt vmcnt(8) lgkmcnt(0)" ::: "memory");
  if constexpr (G == 16 && MODE > 1) asm volatile("s_waitcnt vmcnt(9)" ::: "memory");
  if constexpr (G >= 8 && G < 32 && (G & 3) == 0) asm volatile("s_waitcnt lgkmcnt(%0)" ::"i"(G < 28 ? 4 : 0));
  if constexpr (G < 32) {
    constexpr int o = 8 * (G >> 1) + 4, r = 192 + 4 * (G & 15), q = R_P + 20 + 8 * (G & 1);
    asm volatile("v_mfma_f32_16x16x32_bf16 a[%c0:%c1], a[%c2:%c3], v[%c4:%c5], a[%c0:%c1]" ::"i"(o), "i"(o + 3), "i"(r), "i"(r + 3), "i"(q), "i"(q + 3));
  }
  if constexpr (G < 24) vread<1, G + 8>();
  if constexpr (G == 28) asm volatile("s_waitcnt vmcnt(8)\n\ts_barrier\n\ts_or_b32 m0, s79, 0x1000\n\ts_movk_i32 s98, 384" ::: "memory");
  if constexpr (G == 29 || G == 31) dma_at<8 + (G - 29) / 2>();
  if constexpr (G == 32 || G == 40) {
    static_for([&](auto I) { p_read<0, 2 * (G - 32) + I>(); }, make_iseq<16>{});
    dma_at<10 + (G - 32) / 8>();
  }
  if constexpr (J >= 0) dma_at<8 + (J & 7)>();
  if constexpr (x >= 0) epi_pair<2 * ((x >> 2) & 7) + (x >> 5), x & 3, MODE>(p);
  if constexpr (MODE == 3 && G < 32) {
    constexpr int db = G & 15, qb = G >> 4, o = 4 * (db * 2 + qb), a = 224 + 4 * (db & 1), b = 128 + 4 * ((db >> 1) * 2 + qb);
    asm volatile("v_mfma_f32_16x16x32_bf16 a[%c0:%c1], v[%c2:%c3], a[%c4:%c5], a[%c0:%c1]" ::"i"(o), "i"(o + 3), "i"(a), "i"(a + 3), "i"(b), "i"(b + 3));
  }
  if constexpr (J == 3 || J == 11) asm volatile("s_bitset0_b32 m0, 12");
  if constexpr (J == 7 && MODE > 1) asm volatile("s_or_b32 m0, s78, 0x1000\n\ts_movk_i32 s98, 256");
  if constexpr (x == 63) asm volatile("s_waitcnt vmcnt(4) lgkmcnt(0)" ::: "memory");
  GAP();
}
template <int MODE, int... Gs> DI void phaseFL_(const EP& p, iseq<Gs...>) { (gapFL<MODE, Gs>(p), ...); }
template <int MODE> DI void phaseFL(const EP& p) { GAP(); phaseFL_<MODE>(p, make_iseq<MODE == 3 ? 68 : 64>{}); }
template <int U, int K> DI void r_load(unsigned vo, unsigned so) {
  asm volatile("buffer_load_dwordx4 a[%c0:%c1], %2, s[88:91], %3 offen offset:%4 sc1" ::"i"(64 * U + 4 * K), "i"(64 * U + 4 * K + 3), "v"(vo),
               "s"(so + (K >> 1) * 0x40000 + (U >> 1) * 4096), "i"((K & 1) * 512 + (U & 1) * 2048) : "memory");
}
template <int U, int K> DI void r_sum() {
  constexpr int d = 100 + 4 * (K & 1), b = 64 * U + 4 * K;
  if constexpr (K < 2) asm volatile("v_mfma_f32_16x16x32_bf16 v[%c0:%c1], v[96:99], a[%c2:%c3], 0" ::"i"(d), "i"(d + 3), "i"(b), "i"(b + 3));
  else asm volatile("v_mfma_f32_16x16x32_bf16 v[%c0:%c1], v[96:99], a[%c2:%c3], v[%c0:%c1]" ::"i"(d), "i"(d + 3), "i"(b), "i"(b + 3));
}
template <int U> DI void r_fin(unsigned vo, __amdgpu_buffer_rsrc_t srd, unsigned so) {
  asm volatile("" : "+s"(srd), "+s"(so));
  static_for([&](auto K) { r_sum<U, K>(); }, make_iseq<16>{});
  asm volatile("s_nop 7");
  static_for([&](auto E) { asm volatile("v_cvt_pk_bf16_f32 v%c0, v%c1, v%c2" ::"i"(116 + 4 * U + E), "i"(100 + 2 * E), "i"(101 + 2 * E)); }, make_iseq<4>{});
  asm volatile("buffer_store_dwordx4 v[%c3:%c4], %0, %1, %2 offen sc0 sc1" ::"v"(vo), "s"(srd), "s"(so), "i"(116 + 4 * U), "i"(119 + 4 * U) : "memory");
}

#define CMAX 12
#define BAL 0
#define NYE (BAL ? 16 : 15)
#define NYO (BAL ? 14 : 15)
#define NY0 15
struct TRow { unsigned v[64]; };
constexpr u64 kvmask(int m0, int m1) { u64 n = 0; for (int m = m0; m <= m1; ++m) n |= 0x101ull << m; return n; }
constexpr u64 k_order(bool isY, int rr) {
  const int cls = (rr + (rr >> 2)) & 3;
  return isY ? 0x012345feadcb6987ull : cls == 0 ? 0xfedcb9875a463210ull : cls == 1 ? 0xefadc6982b574310ull : cls == 2 ? 0xfebad7693c285410ull : 0xefcba8764d392510ull;
}
constexpr TRow t_row(int b, int wid) {
  TRow t{};
  const int j = b & 7, r = b >> 3, rr = r & 15, hsel = rr & 1, pj = rr >> 1, kv = pj & 1, h = j + 8 * hsel, u = hsel * 8 + j;
  const bool isY = r >= 16, noA = pj == 7, slow = BAL && (j & 1);
  const int pa = slow && (pj == 0 || pj == 7) ? 7 - pj : pj;
  const bool solo = pa == 7;
  const int jF = isY || !BAL ? j : slow && pa == 0 ? j - 1 : !slow && pa == 6 ? j + 1 : j, hF = jF + 8 * hsel;
  const int qbF = isY ? (solo ? (slow ? 6 : 7) : 15 - pa) : solo ? 8 : !BAL ? pa : slow ? (pa ? pa - 1 : 0) : pa + 1;
  const int mt = (rr >> 2) + (isY ? 4 : 0), g = j * 128, gF = jF * 128, NY = !(j & 1) ? NYE : pa ? NYO : NY0;
  const int nx0 = solo ? 0 : 32 - 2 * pa - NY, cx = nx0 < CMAX ? nx0 : CMAX, ex = nx0 - cx, qb0 = solo ? qbF : 15 - pa;
  const unsigned oQ = (unsigned)h << 20, oK = oQ + O_K, oP = O_P + (h * 8 + pa) * PSTR, oQF = (unsigned)hF << 20;
  if (wid == 4) {
    for (int l = 0; l < 64; ++l)
      t.v[l] = fo(l < 16 ? g + hsel * 16 + l : l == 16 ? g + 32 + hsel * 8 + (qb0 >> 1) : l == 17 ? gF + 32 + hsel * 8 + (qbF >> 1) :
                  l < 26 ? gF + hsel * 16 + ((l - 18) >> 2) * 8 + ((l - 18) & 3) : l == 26 ? g + 48 + hsel * 8 + pa : l < 32 ? g + 64 + hsel * 8 + pa :
                  l < 48 ? 2304 + pj * 16 + l - 32 : 2048 + pj * 16 + l - 48);
    return t;
  }
  auto put64 = [&](int i, u64 x) { t.v[i] = (unsigned)x; t.v[i + 1] = (unsigned)(x >> 32); };
  t.v[T_Q0] = oQ + (qb0 * 128 + wid * 16) * 512;
  t.v[T_K0] = oK + (isY ? cx : 0) * 32768;
  t.v[T_OQ] = oQ + pj * 131072 + (wid >> 1) * 65536 + (wid & 1) * 4096;
  t.v[T_OG] = oK + kv * TSB + mt * 131072 + (wid >> 1) * 65536 + (wid & 1) * (kv ? 8192 : 4096);
  t.v[T_FPREP] = fo(2048 + pj * 16 + u);
  t.v[T_FQ] = fo(g + 32 + hsel * 8 + pj);
  t.v[T_FG] = fo(g + hsel * 16 + kv * 8 + mt);
  t.v[T_FS] = fo(1024 + qbF * 16 + jF * 2 + hsel);
  t.v[T_CNT] = fo(2560 + qbF * 16);
  t.v[T_ZO] = O_Z + pj * 64 * SEQ * 2 + (u * 4 + wid) * 4096;
  const u64 own = 0x1ull << (kv * 8 + mt), conv = noA ? 0 : 0xffffull << 32;
  put64(T_NPRE, 0x10000ull | (isY ? (1ull << (cx >> 2)) | (1ull << ((cx + 1) >> 2)) : noA ? 0 : 0xffffull << 48));
  put64(T_NST, isY ? kvmask(cx >> 2, 3) | (solo ? 0 : 1ull << 27) : kvmask(0, 0));
  put64(T_TAB, k_order(isY, rr));
  t.v[T_SLAB] = O_S + qbF * 0x200000 + jF * 0x40000 + wid * 8192 + hsel * 1024;
  t.v[T_PART] = oP;
  const unsigned junk = fo(g + 96 + r), fconv = noA ? junk : fo(2304 + pj * 16 + u);
  for (int it = isY || solo; it < 2; ++it) {
    unsigned* p = t.v + T_IT + it * T_ITN;
    const int n = !it ? nx0 : isY && !solo ? NY : 2 * qbF + 2;
    u64 q[4] = {0, 0, 0, 0};
    unsigned jmp[3] = {0, 0, 0}, chk = 0;
    p[14] = junk;
    if (isY) {
      p[0] = 2; p[1] = 2; p[13] = t.v[T_FG];
      if (!solo) {
        chk = 1u << (14 - cx); p[0] |= chk; q[1] = kvmask(4, (31 - 2 * pa) >> 2) & ~own; jmp[1] = ex * 32768;
        chk |= 1u << (n - 2); q[2] = 1ull << 26;
      }
    } else if (it == 0) {
      chk = p[0] = 2u | (ex ? 1u << (cx - 2) : 0) | 1u << (n - 2);
      chk |= noA || qbF > 1 ? 0 : 1u << n;
      q[0] = kvmask(0, (cx - 1) >> 2);
      if (ex) { q[1] = kvmask(4, 4 + ((ex - 1) >> 2)); jmp[1] = (16 - cx) * 32768; }
      jmp[ex ? 2 : 1] = oQF - oQ - (ex ? 16 + ex : cx) * 32768;
      q[ex ? 2 : 1] = 0x20000ull | ((0x22ull << ((2 * qbF + 1) >> 2)) - 0x11ull) << 18;
      q[ex ? 3 : 2] = conv;
      p[1] = 2; p[13] = fconv; p[14] = fo(g + 64 + hsel * 8 + pa);
    } else if (solo) {
      chk = p[0] = 0x402; p[1] = 2; p[13] = fconv;
      q[0] = kvmask(0, 2); q[1] = kvmask(3, 4) | conv;
    } else {
      p[0] = p[1] = n > 4 ? 2 : 1; p[13] = fo(g + 48 + hsel * 8 + pa);
      chk = noA ? 0 : p[0];
      q[0] = conv;
    }
    p[2] = jmp[0]; p[4] = jmp[1]; p[15] = jmp[2];
    p[3] = !it ? 0 : n > 2 ? 1u << (n - 3) : 1u << 30;
    for (int k = 0; k < 4; ++k) { p[5 + 2 * k] = (unsigned)q[k]; p[6 + 2 * k] = (unsigned)(q[k] >> 32); }
    const int rk = (r >> 1) - (BAL && (r >> 1) > (slow ? 7 : 6));
    p[16] = 32 | (isY && !solo ? 2 : 0) | (n == 2 ? 4 : 0) | (isY || noA ? 0 : 8) | (n & 1) << 4 | (n - 1 - it) << 8 | (rk * 4 + wid) << 16 | hF << 24 | qbF << 28;
    p[17] = !it ? 0 : n > 2 ? 1u << (n - 2) : 1u << 31;
    p[18] = it ? oP + wid * 16384 : oQF + (qbF * 128 + wid * 16) * 512;
    p[19] = oP + (it ? 1032 : 1024);
    p[20] = chk >> 1;
    p[21] = (it ? oQF + O_K : oK) + (isY ? cx : 0) * 32768;
    p[22] = ((p[0] | p[3] | p[17]) & 0x3fffffffu) << 1;
  }
  return t;
}
struct TBlk { TRow row[5]; };
template <int B> constexpr TBlk t_blk = {{t_row(B, 0), t_row(B, 1), t_row(B, 2), t_row(B, 3), t_row(B, 4)}};
struct Tab { TBlk blk[256]; };
template <int... I> constexpr Tab t_make(iseq<I...>) { return Tab{{t_blk<I>...}}; }
__device__ const Tab g_tab = t_make(make_iseq<256>{});

template <int R> DI const bf16* sptr() { const bf16* p; asm volatile("s_mov_b64 %0, s[%c1:%c2]" : "=s"(p) : "i"(R), "i"(R + 1)); return p; }
template <class T, int I> DI T* arg_late() {
  unsigned lo, hi;
  asm volatile("v_readlane_b32 %0, v77, %2\n\tv_readlane_b32 %1, v77, %3\n\ts_nop 4" : "=s"(lo), "=s"(hi) : "i"(2 * I), "i"(2 * I + 1));
  return (T*)(((u64)hi << 32) | lo);
}
__global__ __launch_bounds__(256, 1) __attribute__((amdgpu_num_vgpr(77), amdgpu_num_sgpr(56))) void fused_kernel(
    const bf16* ckv_, const bf16* wuk_, const bf16* wuv_, const bf16* x_, const bf16* wq_, bf16* ws_, const bf16* ak_, const bf16* av_,
    const bf16* ash_, const bf16* assmk_, const bf16* assmD_, const bf16* wo_, const bf16* aq_, bf16* out_) {
  extern __shared__ __attribute__((aligned(16))) char lds[];
  const void* kap = (const void*)__builtin_amdgcn_kernarg_segment_ptr();
  asm volatile("at_code_begin:\n\ts_load_dwordx16 s[80:95], %0, 0\n\ts_load_dwordx2 s[96:97], %0, 0x40" ::"s"(kap) : ACLOB_ALL, "v255", "s101");
  asm volatile("s_getpc_b64 s[48:49]\nct_base:\n\ts_mov_b32 s50, pf_code_end-ct_base\n\ts_mov_b32 s51, 0x20000\n\ts_lshr_b32 s52, %1, 3\n\t"
               "v_and_b32 v116, 63, %0\n\tv_lshrrev_b32 v117, 6, %0\n\tv_lshl_add_u32 v116, v116, 7, v117\n\ts_lshl_b32 s52, s52, 9\n\t"
               "v_lshlrev_b32 v116, 7, v116\n\tbuffer_load_dword v117, v116, s[48:51], s52 offen" ::"v"(threadIdx.x), "s"(blockIdx.x) : "scc", "memory");
  GAP();
  const int tid = threadIdx.x, lane = tid & 63;
  const int wid = __builtin_amdgcn_readfirstlane(tid >> 6);
  const int b = blockIdx.x, j = b & 7, r = b >> 3, rr = r & 15, hsel = rr & 1, pj = rr >> 1, kv = pj & 1;
  const bool isY = r >= 16, doP = isY && pj != 7, doA = !isY && pj != 7;
  const int h = j + 8 * hsel, mt = (rr >> 2) + (isY ? 4 : 0), pu = (hsel * 8 + j) * 4 + wid;
  constexpr unsigned m0d = 131072 + 24576;
  const unsigned lA = isY ? 65536 : 0, lW = lA ^ 65536, po = isY ? 0 : 32768;
  unsigned mA = lA + wid * 1024 + po, mW = lW + wid * 1024 + po, ofA = isY ? pj * 256 * DA * 2 : mt * 256 * DC * 2, ofW = h * 512;
  const unsigned lda = isY ? DA : DC;
#define ROT 1
  unsigned g0 = (!ROT ? 0 : isY ? pj + 2 * hsel : rr + (rr >> 2)) & 3;
  {
    asm volatile("global_load_dword v78, %0, %2\n\tglobal_load_dword v79, %1, %2 offset:1024\n\tglobal_load_dword v77, %3, %4"
                 ::"v"(tid * 4), "v"(lane * 4), "s"(&g_tab.blk[b]), "v"((lane < 27 ? lane : 27) * 4), "s"(kap) : "memory");
    const unsigned ra = wid * 8 + (lane >> 3), sa = ((lane & 7) ^ (((wid & 1) * 4 + (lane >> 4)) & 7)) * 16;
    const unsigned vbA = ra * (lda * 2) + sa, vbB = (4 * wid + (lane >> 4)) * LDW * 2 + 16 * ((lane & 15) ^ (((lane >> 4) << 2) | wid));
    vsetu<92>(vbA); vsetu<93>(vbB); vsetu<94>(ra * (DC * 2) + sa);
    sset<65>(128 * lda);
    asm volatile("s_mov_b32 s74, 0x40000\n\ts_mov_b32 s53, 0x80000");
    sset<58>(256 * lda * 2); sset<62>((lda - 1) * LDW * 2 + 512);
    asm volatile("s_mov_b32 s59, 0x20000\n\ts_mov_b32 s63, 0x20000");
    g_pro<1>(g0, mA, mW, lda);
    asm volatile("" : "+s"(ofA), "+s"(ofW), "+s"(mA), "+s"(mW), "+s"(g0));
    GAP();
  }
  asm volatile("s_bitcmp1_b32 %0, 4\n\ts_waitcnt lgkmcnt(0)\n\ts_cselect_b64 s[60:61], s[84:85], s[82:83]\n\ts_bitcmp1_b32 %0, 7\n\t"
               "s_cselect_b64 s[60:61], s[88:89], s[60:61]\n\ts_cselect_b64 s[56:57], s[86:87], s[80:81]\n\t"
               "s_add_u32 s56, s56, %1\n\ts_addc_u32 s57, s57, 0\n\ts_add_u32 s60, s60, %2\n\ts_addc_u32 s61, s61, 0\n\t"
               "s_mov_b64 s[88:89], s[90:91]\n\ts_mov_b32 s90, %3\n\ts_mov_b32 s91, 0x20000" ::"s"(b), "s"(ofA), "s"(ofW), "i"(WSB) : "scc");
  KST(0, 0);
  AC c;
  {
    GM g;
    PR pr;
    const int ptb = hsel * 8 + j;
    if (doP) prep_load(pr, tid, pj, ptb, sptr<92>(), sptr<94>(), sptr<96>());
    g_pro<2>(g0, mA, mW, lda);
    g_pro(g0 + 1, mA, mW, lda);
    sset<64>(64 * lda); sset<67>(256 * lda); sset<68>(320 * lda);
    asm volatile("s_mov_b32 s71, 0x20000\n\ts_movk_i32 s72, 0x100\n\ts_mov_b32 s73, 0x20100");
    asm volatile("buffer_load_dwordx2 v[120:121], off, s[88:91], %0 sc1" ::"s"(fo(6144)) : "memory");
    if (doP) {
      asm volatile("s_waitcnt vmcnt(11)" ::: "memory");
#pragma unroll
      for (int q = 0; q < 7; ++q) asm volatile("" : "+v"(pr.kk[q]));
#pragma unroll
      for (int q = 0; q < 4; ++q) asm volatile("" : "+v"(pr.vv[q]));
      static_for([&](auto Ic) __attribute__((always_inline)) {
        constexpr int I = Ic;
        if constexpr (I < 2) g_pro(g0 + 2 + I, mA, mW, lda);
        if constexpr (I == 2) g_pro2(mA, mW);
        GAP();
        prep_math<I>(pr, tid, lds + 131072);
        GAP();
      }, make_iseq<4>{});
      asm volatile("s_waitcnt lgkmcnt(0)\n\ts_barrier" ::: "memory");
      prep_store(tid, pj, ptb, (bf16*)sptr<88>() + O_X / 2, lds + 131072);
    }
    GAP();
    {
      const int fr = lane & 15, fq = lane >> 4, q = (lane & 15) >> 2, p = lane & 3;
#pragma unroll
      for (int k2 = 0; k2 < 2; ++k2)
        g.aA[k2] = lA + (wid >> 1) * 16384 + (fr >> 3) * 1024 + (fr & 7) * 128 + (((4 * k2 + fq) ^ (fr >> 1)) * 16);
#pragma unroll
      for (int hf = 0; hf < 2; ++hf)
#pragma unroll
        for (int nf = 0; nf < 8; ++nf)
          g.aB[hf][nf] = lW + (wid & 1) * 8192 + off_b(8 * fq + 4 * hf + q, nf * 2 + (p >> 1)) + 8 * (p & 1);
      asm volatile("" : "+v"(g.aA[0]), "+v"(g.aA[1]));
#pragma unroll
      for (int nf = 0; nf < 8; ++nf) asm volatile("" : "+v"(g.aB[0][nf]), "+v"(g.aB[1][nf]));
      const unsigned one = fq != q ? 0u : (p & 1) ? 0x3f800000u : 0x3f80u;
      c.o0 = (p & 2) ? 0u : one; c.o1 = (p & 2) ? one : 0u;
      c.tb = (lane >> 2) * 128 + 16 * ((p >> 1) ^ ((lane >> 2) & 6)) + 8 * (p & 1);
      c.vs = (fr >> 3) * 2048 + (fq & 1) * 512 + (fq >> 1) * 256 + (fr & 7) * 16;
      asm volatile("" : "+v"(c.o0), "+v"(c.o1), "+v"(c.tb), "+v"(c.vs));
      vsetu<81>(tid == 0 ? 0u : 0x80000000u);
      vsetu<84>(lane * 16); vsetu<85>(lane * 16 + 65536); vsetu<86>(wid * 8192 + lane * 16); vsetu<87>(wid * 8192 + lane * 16 + 4096);
      vsetu<88>(65536 + wid * 256 + fr * 16); vsetu<89>(16 * wid + fr - 4 * fq);
      asm volatile("s_mov_b64 s[92:93], s[88:89]\n\ts_mov_b64 s[94:95], s[90:91]\n\ts_mov_b32 s75, 0x60000\n\t"
                   "s_mov_b32 s76, 0x40100\n\ts_mov_b32 s77, 0x60100");
      sset<66>(192 * lda); sset<69>(384 * lda); sset<70>(448 * lda);
      sset<78>(lA + wid * 1024); sset<79>(lW + wid * 1024);
    }
    if (doP) asm volatile("s_waitcnt vmcnt(12)" ::: "memory");
    else {
      g_pro(g0 + 2, mA, mW, lda); g_pro(g0 + 3, mA, mW, lda); g_pro2(mA, mW);
      asm volatile("s_waitcnt vmcnt(8)" ::: "memory");
    }
    asm volatile("s_barrier" ::: "memory");
    if (isY) g_read0<0>(g, make_iseq<16>{});
    else g_read0<1>(g, make_iseq<16>{});
    GAP();
    {
      u64 z = (((u64)__float_as_uint(vget<121>()) << 32) | __float_as_uint(vget<120>())) + 0x9e3779b97f4a7c15ull;
      z = (z ^ (z >> 30)) * 0xbf58476d1ce4e5b9ull; z = (z ^ (z >> 27)) * 0x94d049bb133111ebull; z ^= z >> 31;
      vsetu<82>((unsigned)z); vsetu<83>((unsigned)(z >> 32));
      asm volatile("v_readlane_b32 s80, v78, %0\n\tv_readlane_b32 s81, v78, %1\n\tv_readfirstlane_b32 s84, v82\n\tv_readfirstlane_b32 s85, v83"
                   ::"i"((int)T_TAB), "i"(T_TAB + 1));
      if (isY) asm volatile("s_mov_b64 s[80:81], 0x6543210");
    }
    unsigned k2;
    asm volatile("s_bfe_u32 s82, s80, 0x40008\n\ts_lshr_b64 s[80:81], s[80:81], 12\n\ts_lshl_b32 %0, s82, 1" : "=s"(k2) :: "scc");
    asm volatile("s_waitcnt lgkmcnt(0)\n\ts_barrier" ::: "memory");
    g_proW(k2, mW);
    GAP();
    if (isY) {
      g_tile<false, 0, 1>(g);
      g_loop<false, true>(g, doP ? 0x22u : 0x20u, [&](int t) __attribute__((always_inline)) {
        if (t == 1) fl_set<T_FPREP>();
        else if (t == 4) { tload<80, T_TAB>(); tload<81, T_TAB + 1>(); srd_set<60>((kv ? arg_late<const bf16, 2>() : arg_late<const bf16, 1>()) + h * 256, (DC - 1) * LDW * 2 + 512); }
        else {
          srd_set<56>(arg_late<const bf16, 0>() + (size_t)mt * 256 * DC, 256 * DC * 2);
          asm volatile("v_mov_b32 v92, v94");
          static_for([&](auto J) { asm volatile("s_mov_b32 s%c0, %1" ::"i"(64 + J), "i"((J + 1) * 64 * DC)); }, make_iseq<7>{});
        }
      });
      tload<101, T_OQ>(); sset<83>(__float_as_uint(CS));
      asm volatile("v_mov_b32 v112, v84\n\tv_add_u32 v113, 8192, v84");
      GAP();
      g_tile<false, 0, 2>(g);
      KST(0, 3);
    }
#define TT 3
    auto kvtile = [&](auto TRc) __attribute__((always_inline)) {
      constexpr bool TR = TRc;
      if (isY) g_tile<TR, 1, 4>(g);
      else g_tile<TR, 1, 1>(g);
      g_loop<TR, false>(g, 0x2000u | (isY || doA ? 2u : BAL && (j & 1) ? 0u : 1u << TT), [&](int t) __attribute__((always_inline)) {
        int l = threadIdx.x & 63; asm volatile("" : "+v"(l));
        if (t == 13) {
          asm volatile("s_mov_b32 s79, 0x28000");
          fl_poll<T_NPRE>();
          if ((tget<T_IT + T_ITN + 16>() & 0xf000000) == 0)
            asm volatile("v_readlane_b32 s48, v78, %0\n\tv_and_b32 v122, 0xffffff00, v82\n\tv_mov_b32 v123, v83\n\ts_nop 2\n\t"
                         "buffer_store_dwordx2 v[122:123], v81, s[88:91], s48 offen sc1" ::"i"((int)T_CNT) : "memory");
          if (!doA && !isY) {
            CODE_TOUCH("at_code_begin", "pf_code_end", hsel * 8 + wid * 2, 16u, 64, m0d);
            CODE_TOUCH("at_code_begin", "pf_code_end", hsel * 8 + wid * 2 + 1, 16u, 64, m0d);
          }
          if (doA) {
            const char* tp = arg_late<const char, 9>() + (size_t)(pj * 64 + pu) * (SEQ * 2) + (l & 31) * 128;
            asm volatile("s_mov_b32 m0, %1\n\ts_nop 0\n\tglobal_load_lds_dword %0, off" ::"v"(tp), "i"(m0d) : "memory");
          }
        } else if (isY) fl_set<T_FQ>();
        else if (doA) post_dma(pj, pu, arg_late<const bf16, 12>(), 131072 + wid * 8192);
        else {
          const char* tp = arg_late<const char, 0>() + 1024 * DC * 2 + (pu * 256 + l) * 128;
          asm volatile("s_mov_b32 m0, %0\n\ts_nop 0" ::"i"(m0d));
#pragma unroll
          for (int i = 0; i < (BAL ? 8 : 4); ++i) asm volatile("global_load_lds_dword %0, off" ::"v"(tp + (i & 3) * 8192 + (i >> 2) * 131072) : "memory");
        }
      });
      tload<101, T_OG>(); tload<96, T_Q0>(); tload<98, T_K0>();
      fl_check();
      tload<50, T_NST>(); tload<51, T_NST + 1>();
      asm volatile("v_mov_b32 v112, v84\n\tv_add_u32 v113, %0, v84\n\tv_mov_b32 v90, v87\n\tv_mov_b32 v91, v87\n\tv_mov_b32 v92, v87\n\tv_mov_b32 v93, v87\n\t"
                   "v_mov_b32 v94, v86\n\tv_mov_b32 v95, v86\n\tv_mov_b32 v126, v86\n\tv_mov_b32 v127, v86\n\ts_mov_b32 s99, 0x8000\n\t" FL_ARM("s[50:51]")
                   ::"i"(TR ? 4096 : 8192));
      if (doA) {
        const int d = pj * 64 + pu;
        const bf16* dp = arg_late<const bf16, 10>() + d;
        asm volatile("global_load_ushort v123, %0, off" ::"v"(dp) : "memory");
        srd_set<56>(arg_late<const bf16, 9>() + (size_t)d * SEQ, SEQ * 2);
        srd_set<60>(arg_late<const char, 5>() + O_X + d * SEQ * 2, SEQ * 2);
        sset<78>(65536 + wid * 15488);
        asm volatile("s_movk_i32 s64, 992\n\tv_subrev_u32 v122, 32, v84");
        GAP();
        g_tile<TR, 0, 3>(g);
      } else {
        sset<79>(isY ? wid * 8192 : 0x30000);
        if (tget<T_IT + T_ITN + 16>() & 16) asm volatile("s_add_u32 s98, s98, 0x8000\n\ts_mov_b32 s97, 0xffff8000" ::: "scc");
        else asm volatile("s_mov_b32 s97, 0x8000");
        GAP();
        g_tile<TR, 0, 5>(g);
      }
    };
    if (kv) kvtile(std::true_type{});
    else kvtile(std::false_type{});
  }
  KST(0, 4);
  asm volatile("s_waitcnt vmcnt(16)" ::: "memory");
  const unsigned md0 = tget<T_IT + 16>(), md1 = tget<T_IT + T_ITN + 16>();
  sset<79>(wid * 8192 + (md0 & 16) * 2048);
  if (doA) {
    const float Dd = bfl(__float_as_uint(vget<123>()));
    KST(0, 5);
    conv_wave<T_FG>(O_Z + pj * 64 * SEQ * 2 + ((pu & 7) * 8 + (pu >> 3)) * 64, pu >> 3, Dd, lds + 65536 + wid * 15488);
    const char* tp = arg_late<const char, 11>() + (size_t)h * (HD * DA * 2) + ((pj * 4 + wid) * 64 + lane) * 128;
    asm volatile("s_mov_b32 m0, %1\n\ts_nop 0\n\tglobal_load_lds_dword %0, off\n\ts_waitcnt vmcnt(17)" ::"v"(tp), "i"(m0d) : "memory");
    KST(0, 6);
  } else if (!isY) {
    static_for([&](auto I) { d_store<16 + I>(); }, make_iseq<16>{});
    tload<98, T_K0>();
    asm volatile("s_waitcnt vmcnt(0)\n\ts_barrier" ::: "memory");
    fl_set<T_FG>();
    KST(0, 5);
    fl_check();
    CODE_TOUCH("att_begin", "pf_code_end", hsel * 4 + wid, 8u, 64, m0d);
    dma4k<0>(); dma4k<1>(); dma4k<2>(); dma4k<3>();
    asm volatile("s_waitcnt vmcnt(0)" ::: "memory");
    KST(0, 6);
  } else {
    tload<76, T_PART>();
    fl_check();
    asm volatile("buffer_load_dwordx2 v[122:123], v88, s[88:91], s76 offen offset:1024 sc1" ::: "memory");
  }
  sset<78>(65536 + wid * 8192); sset<79>(98304 + wid * 8192);
  { const bf16* wp = arg_late<const bf16, 11>() + ((md1 >> 24) & 15) * HD * DA; asm volatile("s_mov_b64 s[80:81], %0" ::"s"(wp)); }
  asm volatile("att_begin:");
  ev_code(c);
  asm volatile("s_waitcnt lgkmcnt(0)\n\ts_barrier" ::: "memory");
#pragma unroll 1
  for (int it = md0 ? 0 : 1; it < 2; ++it) {
    const unsigned mode = it ? md1 : md0;
    const bool odd = mode & 16;
    if (odd) kreads<1>(make_iseq<8>{});
    else kreads<0>(make_iseq<8>{});
    const unsigned oP = tget<T_PART>();
    if (it == 0) {
      static_for([&](auto I) { tload<57 + I, T_IT + I>(); }, make_iseq<21>{});
      tload<S_T, T_IT + 21>(); tload<S_EV, T_IT + 22>();
    } else {
      static_for([&](auto I) { tload<57 + I, T_IT + T_ITN + I>(); }, make_iseq<21>{});
      tload<S_T, T_IT + T_ITN + 21>(); tload<S_EV, T_IT + T_ITN + 22>();
    }
    asm volatile("s_add_u32 s98, s97, 0x8000\n\ts_mov_b32 s56, -1\n\tv_mov_b64 v[114:115], 0\n\tv_mov_b64 v[118:119], 0\n\t"
                 "v_mov_b32 v120, 0x45800000" ::: "scc");
    KST(0, 8 + 3 * (it && !isY));
    GAP();
#define STA(k)
#define STI()
#define LOOP_ENDS "s_bfe_u32 s76, s73, 0x80008"
    STA(0);
    if (odd) {
      if (isY) phaseA<1, 2>(c);
      else phaseA<1, 1>(c);
      STA(1);
      STA(2);
      phaseB0<1>(mode, isY);
      STA(3);
      asm volatile(LOOP_ENDS "\n\ts_branch att_mid" ::: "scc");
    } else {
      if (isY) phaseA<0, 2>(c);
      else phaseA<0, 1>(c);
      STA(1);
      if (__builtin_expect(mode & 4, 0)) { asm volatile("s_mov_b32 s56, 30"); ev_call(); asm volatile("s_mov_b32 s56, 31"); ev_call(); acc_zero(); }
      STA(2);
      phaseB0<0>(mode, isY);
      if (__builtin_expect(mode & 4, 0)) { ev_call(); asm volatile("s_branch att_done"); }
      STA(3);
    }
    GAP();
    asm volatile(LOOP_ENDS "\natt_loop:" ::: "scc");
    GAP();
    phaseA<1, 0>(c);
    STI();
    if (__builtin_expect(c.rare != 0, 0)) sm_redo<1>();
    phaseB<1>(c);
    asm volatile("s_cbranch_scc1 att_done\natt_mid:");
    phaseA<0, 0>(c);
    STI();
    if (__builtin_expect(c.rare != 0, 0)) sm_redo<0>();
    phaseB<0>(c);
    asm volatile("s_cbranch_scc0 att_loop\natt_done:");
    GAP();
    STA(4);
    if (it == 0) phaseA<0, 3>(c);
    else {
      sset<76>(oP);
      GAP();
      phaseAL(c, make_iseq<32>{});
      if (__builtin_expect(redo(), 0)) sm_redo<1, false>();
      if (mode & 2) phaseBL<true>(c, make_iseq<64>{});
      else phaseBL<false>(c, make_iseq<64>{});
    }
    if (__builtin_expect(redo(), 0)) {
      if (it) static_for([&](auto I) { vset<R_S + 32 + (I >> 2) * 8 + (I & 3)>(-INFINITY); }, make_iseq<16>{});
      asm volatile("s_nop 7");
      sm_redo<0, false>();
    }
    STA(5);
    KST(0, 9 + 3 * (it && !isY));
    float l[2];
    asm volatile("v_add_f32 %0, v%c2, v%c3\n\tv_add_f32 %1, v%c4, v%c5" : "=v"(l[0]), "=v"(l[1]) : "i"(R_RS), "i"(R_TS), "i"(R_RS + 1), "i"(R_TS + 1));
    const float cm[2] = {vget<R_M>(), vget<R_M + 1>()};
    l[0] = row_reduce<false>(l[0]); l[1] = row_reduce<false>(l[1]);
    EP p;
    if (it == 0) {
      const f32x4 ml = {cm[0], l[0], cm[1], l[1]};
      asm volatile("buffer_store_dwordx4 %0, v88, s[88:91], %1 offen sc1\n\ts_mov_b32 s101, %2" ::"v"(ml), "s"(oP), "s"(oP + wid * 16384) : "memory");
      phaseF(c, p, make_iseq<74>{});
      asm volatile(FL_POLL "\n\ts_waitcnt vmcnt(17)" ::: "memory");
    } else {
      if (!(mode & 2)) {
        p.fa[0] = __builtin_amdgcn_rcpf(l[0]); p.fa[1] = __builtin_amdgcn_rcpf(l[1]);
        phaseFL<1>(p);
      } else {
        const f32x4 ml = {vget<122>(), vget<123>(), vget<124>(), vget<125>()};
        if (__builtin_expect(__builtin_amdgcn_ballot_w64(ml[0] != cm[0] || ml[2] != cm[1]) == 0, 1)) {
          p.fa[0] = __builtin_amdgcn_rcpf(l[0] + ml[1]); p.fa[1] = __builtin_amdgcn_rcpf(l[1] + ml[3]);
          phaseFL<3>(p);
        } else {
#pragma unroll
          for (int e = 0; e < 2; ++e) {
            const float mm = fmaxf(cm[e], ml[2 * e]);
            const float wa = __builtin_amdgcn_exp2f(cm[e] - mm), wb = __builtin_amdgcn_exp2f(ml[2 * e] - mm);
            const float inv = __builtin_amdgcn_rcpf(l[e] * wa + ml[2 * e + 1] * wb);
            p.fa[e] = wa * inv; p.fb[e] = wb * inv;
          }
          phaseFL<2>(p);
        }
      }
    }
    KST(0, 10 + 3 * (it && !isY));
  }
  asm volatile(TICKET "\n\tv_xor_b32 v%c0, s100, v%c0\np_loop:" ::"i"(R_TB) : "memory");
  GAP();
  p_half<0, true>(make_iseq<32>{});
  asm volatile("s_cbranch_scc0 p_mid");
  p_half<1, true>(make_iseq<32>{});
  asm volatile("s_branch p_loop\np_loop2:");
  p_half<0, false>(make_iseq<32>{});
  asm volatile("p_mid:");
  p_half<1, false>(make_iseq<32>{});
  asm volatile("s_cbranch_scc1 p_loop2\n\ts_nop 5");
  GAP();
  p_cvt<1, 0>(); p_cvt<1, 1>(); p_cvt<1, 2>(); p_cvt<1, 3>();
  p_store<1, 1>();
  GAP();
  KST(0, 27);
  {
    constexpr unsigned rdy = 98304;
    int lz = threadIdx.x & 63; asm volatile("" : "+v"(lz));
    const int fr = lz & 15, fq = lz >> 4, qbF = md1 >> 28;
    if (wid == 0) {
#pragma unroll 1
      for (int q = 0; q < 0x100000; ++q) {
        unsigned lo, hi, tl, th;
        asm volatile("v_readfirstlane_b32 %0, v240\n\tv_readfirstlane_b32 %1, v241\n\ts_mov_b32 %2, s84\n\ts_mov_b32 %3, s85" : "=s"(lo), "=s"(hi), "=s"(tl), "=s"(th));
        if (__builtin_expect((((lo ^ tl) >> 8) | (hi ^ th)) == 0, 1)) break;
        asm volatile("s_sleep 4\n\tv_mov_b64 v[240:241], 1\n\t" TICKET "\n\ts_waitcnt vmcnt(0)" ::: "memory");
      }
      asm volatile("v_mov_b32 v241, 0\n\ts_mov_b64 exec, 1\n\tds_write_b64 %0, v[240:241]\n\ts_mov_b64 exec, -1" ::"v"(rdy) : "memory");
    }
    vsetu<79>(fo(1024) + qbF * 4352 + fr * 8);
    {
      const int jj = 4 * (fr >> 3) + (fr & 3);
      const unsigned one = ((fq ^ (fr >> 2)) & 1) ? 0u : (jj & 1) ? 0x3f800000u : 0x3f80u;
      static_for([&](auto I) { vsetu<96 + I>((jj >> 1) == I ? one : 0u); }, make_iseq<4>{});
    }
    unsigned vo = (fq >> 1) * 1024 + (fq & 1) * 256 + fr * 16;
    bf16* out = arg_late<bf16, 13>();
    __amdgpu_buffer_rsrc_t so_ = __builtin_amdgcn_make_buffer_rsrc((void*)out, (short)0, SEQ * OUTW * 2, 0x00020000);
    unsigned vw = ((qbF * 128 + (fr & 7)) * OUTW + DA + (fr >> 3) * 32 + fq * 8) * 2;
    asm volatile("" : "+s"(so_));
    asm volatile("s_waitcnt vmcnt(0) lgkmcnt(0)\n\ts_barrier" : "+v"(vo), "+v"(vw) :: "memory");
    fl_set<T_FS>();
    unsigned tk;
    asm volatile("ds_read_b32 %0, %1\n\ts_waitcnt lgkmcnt(0)" : "=v"(tk) : "v"(rdy) : "memory");
    tk = __builtin_amdgcn_readfirstlane(tk) & 255;
    KST(0, 30);
    if (doA) post_math(pj, pu, out, lds + 131072 + wid * 8192);
#define N3 0
#define N2 12
    constexpr int N1 = 28 - 3 * N3 - 2 * N2;
    static_assert(N1 >= 0 && N3 + N2 + N1 <= 16);
    const int nu = tk < N3 ? 3 : tk < N3 + N2 ? 2 : tk < N3 + N2 + N1 ? 1 : 0;
    const int u0 = 4 * (tk < N3 ? 3 * tk : tk < N3 + N2 ? N3 + 2 * tk : 2 * N3 + N2 + tk) + wid * nu;
    KST(0, 28);
    if (nu) {
      const unsigned so0 = O_S + qbF * 0x200000 + (16 + u0) * 2048, so[3] = {so0, so0, so0};
      unsigned pend = 0x5555u, cnt = 0x400000;
      while (true) {
        unsigned nw;
        if (wid == 3) {
          u64 m;
          const u64 ex = pend | (pend << 1);
          asm volatile(FL_ARM("%1") "\n\t" FL_POLL "\n\ts_waitcnt vmcnt(0)\n\tv_cmp_eq_u64 %0, s[84:85], v[124:125]" : "=s"(m) : "s"(ex) : "memory");
          nw = (unsigned)m & ((unsigned)m >> 1) & pend;
          if (nw) asm volatile("ds_write_b32 %0, %1 offset:4" ::"v"(rdy), "v"(~pend | nw) : "memory");
        } else {
          unsigned t;
          asm volatile("ds_read_b32 %0, %1 offset:4\n\ts_waitcnt lgkmcnt(0)" : "=v"(t) : "v"(rdy) : "memory");
          nw = __builtin_amdgcn_readfirstlane(t) & pend;
        }
        if (--cnt == 0) nw = pend;
        if (nw == 0) {
          if (wid == 3) asm volatile("s_sleep 4");
          else asm volatile("s_sleep 1");
          continue;
        }
        asm volatile("s_nop 4");
        static_for([&](auto S) {
          if ((nw >> (2 * S)) & 1) {

            r_load<0, 2 * S>(vo, so[0]); r_load<0, 2 * S + 1>(vo, so[0]);
            if (nu > 1) { r_load<1, 2 * S>(vo, so[1]); r_load<1, 2 * S + 1>(vo, so[1]); }
            if (nu > 2) { r_load<2, 2 * S>(vo, so[2]); r_load<2, 2 * S + 1>(vo, so[2]); }
          }
        }, make_iseq<8>{});
        pend &= ~nw;
        if (pend == 0) break;
      }
      asm volatile("s_waitcnt vmcnt(0)" ::: "memory");
      KST(0, 16);
      auto oo = [&](int U) { return ((((U >> 1) & 1) * 64 + ((U >> 2) & 3) * 16 + (U & 1) * 8) * OUTW + (U >> 4) * 64) * 2; };
      r_fin<0>(vw, so_, oo(u0));
      if (nu > 1) r_fin<1>(vw, so_, oo(u0 + 1));
      if (nu > 2) r_fin<2>(vw, so_, oo(u0 + 2));
      if (qbF == 15 && tk == 0) asm volatile(FL_SET("%0") ::"s"(fo(6144)) : "memory");
    }
    KST(0, 29);
  }
  asm volatile("s_waitcnt vmcnt(0)" ::: "memory");
  asm volatile("pf_code_end:");
  KST(0, 1);
}

extern "C" void kernel_launch(void* const* d_in, const int* in_sizes, int n_in, void* d_out, int out_size, void* d_ws,
                              size_t ws_size, hipStream_t stream) {
  const bf16* Aq = (const bf16*)d_in[0];
  const bf16* Ak = (const bf16*)d_in[1];
  const bf16* Av = (const bf16*)d_in[2];
  const bf16* Ash = (const bf16*)d_in[3];
  const bf16* Assmk = (const bf16*)d_in[4];
  const bf16* AssmD = (const bf16*)d_in[5];
  const bf16* Bx = (const bf16*)d_in[6];
  const bf16* Bckv = (const bf16*)d_in[7];
  const bf16* Bwq = (const bf16*)d_in[8];
  const bf16* Bwuk = (const bf16*)d_in[9];
  const bf16* Bwuv = (const bf16*)d_in[10];
  const bf16* Bwo = (const bf16*)d_in[11];
  bf16* out = (bf16*)d_out;
  hipFuncSetAttribute((const void*)fused_kernel, hipFuncAttributeMaxDynamicSharedMemorySize, 163840);
  fused_kernel<<<256, 256, 163840, stream>>>(Bckv, Bwuk, Bwuv, Bx, Bwq, (bf16*)d_ws, Ak, Av, Ash, Assmk, AssmD, Bwo, Aq, out);
}
```

```cpp
#include <hip/hip_runtime.h>
#include <hip/hip_bf16.h>
#include <stdint.h>
#include <utility>

typedef __hip_bfloat16 bf16;
typedef __attribute__((ext_vector_type(4))) short bf16x4;
typedef __attribute__((ext_vector_type(2))) unsigned u32x2;

constexpr int SEQ = 2048;
constexpr int DA = 448;
constexpr int NH = 16;
constexpr int HD = 256;
constexpr int DC = 1024;
constexpr int OUTW = 896;
constexpr unsigned TSB = NH * SEQ * HD * 2, O_K = TSB, O_S = 3 * TSB, O_P = 5 * TSB, PSTR = 67584, O_F = O_P + 128 * PSTR,
                   O_X = O_F + 400 * 4352, O_Z = O_X + DA * SEQ * 2, WSB = O_Z + DA * SEQ * 2;
constexpr unsigned fo(int w) { return O_F + (w >> 4) * 4352 + (w & 15) * 8; }

#define KST(kid, which)

__device__ __forceinline__ float bf2f(unsigned short u) { return __uint_as_float(((unsigned)u) << 16); }
__device__ __forceinline__ unsigned short f2bf(float f) { return __bfloat16_as_ushort(__float2bfloat16(f)); }
__device__ __forceinline__ float rbf(float f) { return bf2f(f2bf(f)); }
typedef __attribute__((ext_vector_type(2))) __bf16 bf16x2n;
typedef __attribute__((ext_vector_type(2))) float f32x2n;
__device__ __forceinline__ unsigned pk2(float a, float b) {
  return __builtin_bit_cast(unsigned, __builtin_convertvector((f32x2n){a, b}, bf16x2n));
}

__device__ __forceinline__ int off_b(int row, int ch) { return 256 * row + 16 * (ch ^ (((row & 3) << 2) | ((row >> 2) & 3))); }

template <int R> __device__ __forceinline__ void sset(unsigned x) { asm volatile("s_mov_b32 s%c0, %1" ::"i"(R), "s"(x)); }
template <int R> __device__ __forceinline__ void vsetu(unsigned x) { asm volatile("v_mov_b32 v%c0, %1" ::"i"(R), "v"(x)); }
template <int R> __device__ __forceinline__ void srd_set(const void* p, unsigned n) {
  asm volatile("s_mov_b64 s[%c0:%c1], %4\n\ts_mov_b32 s%c2, %5\n\ts_mov_b32 s%c3, 0x20000" ::"i"(R), "i"(R + 1), "i"(R + 2), "i"(R + 3), "s"(p), "s"(n));
}
template <int R, int L> __device__ __forceinline__ void tload() { asm volatile("v_readlane_b32 s%c0, v78, %1" ::"i"(R), "i"(L)); }
template <int L> __device__ __forceinline__ unsigned tget() { unsigned t; asm volatile("v_readlane_b32 %0, v78, %1" : "=s"(t) : "i"(L)); return t; }
#define FL_SET(S) "buffer_store_dwordx2 v[82:83], v81, s[88:91], " S " offen sc1"
#define FL_ARM(N) "v_cndmask_b32_e64 v80, -2.0, v79, " N
#define FL_POLL "buffer_load_dwordx2 v[124:125], v80, s[88:91], 0 offen sc1"
#define FL_CHECK(N) "s_mov_b32 s48, 0x100000\n7:\n\tv_cmp_eq_u64 vcc, s[84:85], v[124:125]\n\ts_and_b64 vcc, vcc, " N "\n\ts_cmp_eq_u64 vcc, " N \
                    "\n\ts_cbranch_scc1 8f\n\ts_sleep 2\n\t" FL_POLL "\n\ts_waitcnt vmcnt(0)\n\ts_sub_u32 s48, s48, 1\n\ts_cbranch_scc0 7b\n8:"
template <int L> __device__ __forceinline__ void fl_set() {
  asm volatile("v_readlane_b32 s48, v78, %0\n\ts_nop 4\n\t" FL_SET("s48") ::"i"(L) : "memory");
}
template <int L> __device__ __forceinline__ void fl_poll() {
  asm volatile("v_readlane_b32 s50, v78, %0\n\tv_readlane_b32 s51, v78, %1\n\ts_nop 1\n\t" FL_ARM("s[50:51]") "\n\t" FL_POLL ::"i"(L), "i"(L + 1) : "memory");
}
#define TICKET "buffer_atomic_add_x2 v[240:241], v81, s[88:91], s101 offen sc0"
__device__ __forceinline__ void fl_check() { asm volatile(FL_CHECK("s[50:51]") ::: "vcc", "scc", "memory"); }
template <int R> __device__ __forceinline__ float vget() { float t; asm volatile("v_mov_b32 %0, v%c1" : "=v"(t) : "i"(R)); return t; }
template <int R> __device__ __forceinline__ void vset(float x) { asm volatile("v_mov_b32 v%c0, %1" ::"i"(R), "v"(x)); }
template <int R> __device__ __forceinline__ float acc_read1() { float t; asm volatile("v_accvgpr_read_b32 %0, a[%c1]" : "=v"(t) : "i"(R)); return t; }
template <class F, int... I> __device__ __forceinline__ void static_for(F&& f, std::integer_sequence<int, I...>) {
  (f(std::integral_constant<int, I>{}), ...);
}
template <int... Is> using iseq = std::integer_sequence<int, Is...>;
template <int N> using make_iseq = std::make_integer_sequence<int, N>;

typedef __attribute__((ext_vector_type(4))) unsigned u32x4c;
typedef __attribute__((ext_vector_type(4))) float f32x4c;
typedef __attribute__((ext_vector_type(2))) float f32x2c;
typedef unsigned long long u64;
struct PR { u32x4c kk[7], vv[4]; };
__device__ __forceinline__ void prep_load(PR& p, int tid, int cg, int tb, const bf16* __restrict__ k, const bf16* __restrict__ v,
                                          const bf16* __restrict__ shift_k) {
  const __amdgpu_buffer_rsrc_t sk = __builtin_amdgcn_make_buffer_rsrc((void*)k, (short)0, SEQ * DA * 2, 0x00020000);
  const __amdgpu_buffer_rsrc_t sv = __builtin_amdgcn_make_buffer_rsrc((void*)v, (short)0, SEQ * DA * 2, 0x00020000);
  const __amdgpu_buffer_rsrc_t sw = __builtin_amdgcn_make_buffer_rsrc((void*)(shift_k + cg * 256), (short)0, 512, 0x00020000);
  const int vo = ((tb * 128 + (tid >> 3) * 4) * DA + cg * 64 + (tid & 7) * 8) * 2;
#pragma unroll
  for (int q = 0; q < 7; ++q)
    asm volatile("buffer_load_dwordx4 %0, %1, %2, 0 offen" : "=v"(p.kk[q]) : "v"(vo + (q - 3) * DA * 2), "s"(sk) : "memory");
#pragma unroll
  for (int q = 0; q < 4; ++q)
    asm volatile("buffer_load_dwordx4 %0, %1, %2, 0 offen offset:%3" : "=v"(p.vv[q]) : "v"(vo), "s"(sv), "i"(q * DA * 2) : "memory");
#pragma unroll
  for (int q = 0; q < 4; ++q)
    asm volatile("buffer_load_dwordx4 a[%c0:%c1], %2, %3, 0 offen offset:%4" ::"i"(4 * q), "i"(4 * q + 3), "v"((tid & 7) * 64), "s"(sw), "i"(q * 16) : "memory");
}
__device__ __forceinline__ float bfl(unsigned u) { return __uint_as_float(u << 16); }
__device__ __forceinline__ float bfh(unsigned u) { return __uint_as_float(u & 0xffff0000u); }
__device__ __forceinline__ f32x2c rbf2(f32x2c a) {
  return (f32x2c){__uint_as_float(pk2(0.f, a[0])), __uint_as_float(pk2(0.f, a[1]))};
}
template <int I> __device__ __forceinline__ void prep_math(const PR& p, int tid, char* lx) {
  const int rp = tid >> 3, c = tid & 7;
  f32x2c wt[4], kf[7];
  const unsigned w[4] = {__float_as_uint(acc_read1<4 * I>()), __float_as_uint(acc_read1<4 * I + 1>()), __float_as_uint(acc_read1<4 * I + 2>()),
                         __float_as_uint(acc_read1<4 * I + 3>())};
#pragma unroll
  for (int t = 0; t < 4; ++t)
    wt[t] = (t & 1) ? (f32x2c){bfh(w[t >> 1]), bfh(w[2 + (t >> 1)])} : (f32x2c){bfl(w[t >> 1]), bfl(w[2 + (t >> 1)])};
#pragma unroll
  for (int q = 0; q < 7; ++q) kf[q] = (f32x2c){bfl(p.kk[q][I]), bfh(p.kk[q][I])};
  f32x2c x[4];
#pragma unroll
  for (int e = 0; e < 4; ++e) {
    f32x2c s = rbf2(kf[e] * wt[3]);
    s = rbf2(s + rbf2(kf[e + 1] * wt[2]));
    s = rbf2(s + rbf2(kf[e + 2] * wt[1]));
    s = rbf2(s + rbf2(kf[e + 3] * wt[0]));
    x[e] = s * (f32x2c){bfl(p.vv[e][I]), bfh(p.vv[e][I])};
  }
#pragma unroll
  for (int u = 0; u < 2; ++u)
    *(u32x2*)(lx + (c * 8 + 2 * I + u) * 256 + (((31 - rp) * 8) ^ (c << 4))) = (u32x2){pk2(x[3][u], x[2][u]), pk2(x[1][u], x[0][u])};
}
__device__ __forceinline__ void prep_store(int tid, int cg, int tb, bf16* __restrict__ xr, const char* lx) {
  const __amdgpu_buffer_rsrc_t sx = __builtin_amdgcn_make_buffer_rsrc((void*)(xr + (size_t)cg * 64 * SEQ), (short)0, 64 * SEQ * 2, 0x00020000);
#pragma unroll
  for (int rd = 0; rd < 4; ++rd) {
    const int idx = tid + 256 * rd, ch = idx >> 4, q = idx & 15;
    u32x4c w = *(const u32x4c*)(lx + ch * 256 + 16 * (q ^ (ch >> 3)));
    __builtin_amdgcn_raw_buffer_store_b128(w, sx, (ch * SEQ + (15 - tb) * 128 + q * 8) * 2, 0, 16);
  }
}

#define CODE_TOUCH(LB, LE, wx, NW, NL, m0v) do { unsigned blo_, bhi_, elo_; \
    asm volatile("s_getpc_b64 s[48:49]\n\ts_add_u32 s48, s48, " LB "@rel32@lo+4\n\ts_addc_u32 s49, s49, " LB "@rel32@hi+12\n\t" \
                 "s_mov_b32 %0, s48\n\ts_mov_b32 %1, s49\n\t" \
                 "s_getpc_b64 s[48:49]\n\ts_add_u32 s48, s48, " LE "@rel32@lo+4\n\ts_mov_b32 %2, s48" \
                 : "=s"(blo_), "=s"(bhi_), "=s"(elo_) :: "scc"); \
    const unsigned n_ = elo_ - (blo_ & ~127u); \
    const __amdgpu_buffer_rsrc_t srd_ = __builtin_amdgcn_make_buffer_rsrc((void*)(((unsigned long long)bhi_ << 32) | (blo_ & ~127u)), (short)0, n_ < 262144u ? n_ : 0u, 0x00020000); \
    const unsigned l_ = threadIdx.x & 63, vo_ = l_ < (NL) ? ((wx) + l_ * (NW)) * 128u : 0x80000000u; \
    asm volatile("s_mov_b32 m0, %0\n\ts_nop 4\n\tbuffer_load_dword %1, %2, 0 offen lds" ::"s"(m0v), "v"(vo_), "s"(srd_) : "memory"); } while (0)
template <int I> __device__ __forceinline__ void d_store() {
  asm volatile("buffer_store_dwordx4 v[%c0:%c1], v%c2, s[88:91], s101 offen offset:%3 sc1" ::"i"(128 + 4 * I), "i"(131 + 4 * I), "i"(112 + ((I >> 2) & 1)),
               "i"((I & 3) * 1024) : "memory");
  if constexpr ((I & 7) == 7) asm volatile("s_addk_i32 s101, 0x4000" ::: "scc");
}
constexpr int NS_X = 19;
struct CV { unsigned pa[4], pb, px, vz, sz; float D; };
constexpr int cv_J(bool up, int q) { int J = up ? 0 : 7; while (q > J) { q -= J + 1; J += up ? 1 : -1; } return J; }
constexpr int cv_i(bool up, int q) { int J = up ? 0 : 7; while (q > J) { q -= J + 1; J += up ? 1 : -1; } return q; }
template <int R, int N> __device__ __forceinline__ void cv_rd(CV& c) {
  if constexpr (N < 16) {
    constexpr int i = N >> 1, o = 16 * R + 128 * (i & 1) + 2 * (N & 1), r = 32 + 32 * (R & 1) + 2 * N;
    asm volatile("ds_read2_b32 a[%c1:%c2], %0 offset0:%c3 offset1:%c4" ::"v"(c.pa[i >> 1]), "i"(r), "i"(r + 1), "i"(o), "i"(o + 1));
  } else {
    constexpr int m = N - 16, r = 192 + 32 * (R & 1) + 4 * m;
    asm volatile("ds_read_b128 a[%c1:%c2], %0 offset:%c3" ::"v"(c.pb), "i"(r), "i"(r + 3), "i"(64 * R + 512 * (7 - m)));
  }
}
template <int O> __device__ __forceinline__ void cv_xd(CV& c) {
  constexpr int e = O & 31, x = 96 + ((e >> 1) ^ 1);
  if constexpr (O >= 64) asm volatile("v_cvt_pk_bf16_f32 v%c0, 0, v%c0" ::"i"(160 + e));
  else if constexpr (O >= 32) asm volatile("v_mul_f32 v%c0, v%c0, %1" ::"i"(160 + e), "v"(c.D));
  else if constexpr (O & 1) asm volatile("v_lshlrev_b32 v%c0, 16, v%c1" ::"i"(160 + e), "i"(x));
  else asm volatile("v_and_b32 v%c0, 0xffff0000, v%c1" ::"i"(160 + e), "i"(x));
}
template <int O> __device__ __forceinline__ void cv_z(CV& c) {
  constexpr int J = O / 11, k = O % 11, y = 128 + 4 * J;
  if constexpr (k < 4) asm volatile("v_cvt_pk_bf16_f32 v%c0, 0, v%c0" ::"i"(y + k));
  else if constexpr (k < 8) asm volatile("v_add_f32 v%c0, v%c0, v%c1" ::"i"(y + k - 4), "i"(y + 28 + k));
  else if constexpr (k < 10) asm volatile("v_cvt_pk_bf16_f32 v%c0, v%c1, v%c2" ::"i"(y + k - 8), "i"(y + 2 * k - 16), "i"(y + 2 * k - 15));
  else asm volatile("buffer_store_dwordx2 v[%c0:%c1], %2, s[88:91], s101 offen sc1\n\ts_add_u32 s101, s101, 0x8000" ::"i"(y), "i"(y + 1), "v"(c.vz) : "memory");
}
constexpr int cv_z0(int Q) {
  int o = 0;
  for (int q = 0; q < Q; ++q)
    for (int k = 0; k < (q % 4 == 1 ? 0 : 3); ++k) { const int J = o / 11; if ((J + 1) * (J + 2) / 2 - 1 + (J ? 4 : 7) <= q) ++o; }
  return o;
}
constexpr int cv_xd0(int T) { int o = 0; for (int t = 0; t < T; ++t) o += t % 4 != 1 || (t >= 72 && t < 108); return o < 96 ? o : 96; }
static_assert(cv_xd0(144) == 96);
template <int B, int... Is> __device__ __forceinline__ void cv_zs(CV& c, iseq<Is...>) { (cv_z<B + Is>(c), ...); }
template <int R, int Q> __device__ __forceinline__ void cv_slot(CV& c) {
  constexpr int i = cv_i(R == 7, Q), J = cv_J(R == 7, Q), d = 128 + 4 * J, a = 32 + 32 * (R & 1) + 4 * i, b = 192 + 32 * (R & 1) + 4 * (J - i);
  constexpr int T = 36 * R + Q, vm = Q % 4 == 1 && Q < 32 ? Q / 4 : -1;
  if constexpr (Q == 0) asm volatile("s_waitcnt lgkmcnt(0)");
  if constexpr (R >= 6 && vm >= 0 && vm % 4 == 0) asm volatile("s_add_u32 m0, s79, %0" ::"i"(vm * 1024));
  if constexpr (R == 0 && i == 0)
    asm volatile("v_mfma_f32_16x16x32_bf16 v[%c0:%c1], a[%c2:%c3], a[%c4:%c5], 0" ::"i"(d), "i"(d + 3), "i"(a), "i"(a + 3), "i"(b), "i"(b + 3));
  else
    asm volatile("v_mfma_f32_16x16x32_bf16 v[%c0:%c1], a[%c2:%c3], a[%c4:%c5], v[%c0:%c1]" ::"i"(d), "i"(d + 3), "i"(a), "i"(a + 3), "i"(b), "i"(b + 3));
  if constexpr (Q < 24 && R < 7) cv_rd<R + 1, Q>(c);
  if constexpr (T % 7 == 3 && NS_X + 5 + T / 7 < 32) d_store<NS_X + 5 + T / 7>();
  if constexpr (R == 2 && Q >= 24 && Q < 32)
    asm volatile("ds_read_b64 v[%c1:%c2], %0 offset:%c3" ::"v"(c.px), "i"(96 + 2 * (Q - 24)), "i"(97 + 2 * (Q - 24)), "i"(512 * (31 - Q)));
  if constexpr (R >= 3 && R < 7) if constexpr (cv_xd0(T - 107) > cv_xd0(T - 108)) cv_xd<cv_xd0(T - 108)>(c);
  if constexpr ((R == 3 || R == 4) && vm >= 0) {
    constexpr int f = (R - 3) * 8 + vm;
    asm volatile("buffer_load_dwordx4 a[%c0:%c1], v84, s[88:91], s96 offen offset:%2 sc1" ::"i"(128 + 4 * f), "i"(131 + 4 * f), "i"((f & 3) * 1024) : "memory");
    if constexpr ((f & 3) == 3) asm volatile("s_addk_i32 s96, %0" ::"i"(f == 7 ? 28672 : 4096));
  }
  if constexpr (R == 4 && Q == 35) asm volatile(FL_POLL ::: "memory");
  if constexpr (R >= 6 && vm >= 0)
    asm volatile("buffer_load_dwordx4 v%c0, s[88:91], s98 offen offset:%1 sc1 lds" ::"i"(86 + vm / 4), "i"((vm & 3) * 1024) : "memory");
  if constexpr (R == 6 && Q == 35) asm volatile("s_add_u32 s98, s98, 0x8000\n\ts_xor_b32 s79, s79, 0x8000");
  if constexpr (R == 7) cv_zs<cv_z0(Q)>(c, make_iseq<cv_z0(Q + 1) - cv_z0(Q)>{});
  __builtin_amdgcn_sched_barrier(0);
}
template <int R, int... Q> __device__ __forceinline__ void cv_step(CV& c, iseq<Q...>) { (cv_slot<R, Q>(c), ...); }
template <int... N> __device__ __forceinline__ void cv_rd0(CV& c, iseq<N...>) { (cv_rd<0, N>(c), ...); }
template <int FLAG> __device__ __forceinline__ void conv_wave(unsigned sz, int ck, float Dd, char* lw) {
  int lane = threadIdx.x & 63; asm volatile("" : "+v"(lane));
  const int l16 = lane & 15, fq = lane >> 4;
  u32x4c w[5]; unsigned w4[5];
#pragma unroll
  for (int r = 0; r < 5; ++r) {
    w[r] = *(const u32x4c*)(lw + r * 1024 + lane * 16);
    w4[r] = *(const unsigned*)(lw + r * 1024 + lane * 16 + 16);
  }
  d_store<NS_X>(); d_store<NS_X + 1>();
#pragma unroll
  for (int r = 0; r < 5; ++r)
    *(u32x4c*)(lw + 10304 + r * 1024 + lane * 16) = (u32x4c){__builtin_amdgcn_alignbit(w[r][1], w[r][0], 16), __builtin_amdgcn_alignbit(w[r][2], w[r][1], 16),
                                                             __builtin_amdgcn_alignbit(w[r][3], w[r][2], 16), __builtin_amdgcn_alignbit(w4[r], w[r][3], 16)};
  d_store<NS_X + 2>();
  CV c;
#pragma unroll
  for (int i = 0; i < 4; ++i) c.pa[i] = (unsigned)(size_t)lw + ((l16 + 1) & 1) * 10304 + 4 * ((8 * fq + l16 + 1) >> 1) + 1024 * i;
  c.pb = (unsigned)(size_t)lw + 5120 + (2032 - 16 * l16 + 8 * fq) * 2 - 512 * 7;
  c.vz = (l16 >> 1) * 4096 + (((l16 & 1) * 2 + (fq >> 1)) ^ (ck >> 1)) * 16 + (fq & 1) * 8;
  c.px = (unsigned)(size_t)lw + 5120 + 2044 * 2 - 512 * 7 - (16 * l16 + 4 * fq) * 2;
  c.sz = sz; c.D = Dd;
  asm volatile("" : "+v"(c.vz), "+v"(c.px), "+v"(c.D) :: "memory");
  asm volatile("" : "+s"(c.sz));
  cv_rd0(c, make_iseq<24>{});
  d_store<NS_X + 3>(); d_store<NS_X + 4>();
  __builtin_amdgcn_sched_barrier(0);
  static_for([&](auto R) __attribute__((always_inline)) {
    cv_step<R>(c, make_iseq<36>{});
    if constexpr (R == 2) {
      asm volatile("s_waitcnt vmcnt(0)\n\ts_barrier" ::: "memory");
      fl_set<FLAG>();
      sset<101>(c.sz);
    }
    if constexpr (R == 5) {
      asm volatile("s_waitcnt vmcnt(0)" ::: "memory");
      fl_check();
    }
    __builtin_amdgcn_sched_barrier(0);
  }, make_iseq<8>{});
  asm volatile("s_nop 7");
  cv_zs<cv_z0(36)>(c, make_iseq<88 - cv_z0(36)>{});
  __builtin_amdgcn_sched_barrier(0);
}

__device__ __forceinline__ void post_dma(int cg, int tb, const bf16* __restrict__ q, unsigned m0w) {
  int lane = threadIdx.x & 63; asm volatile("" : "+v"(lane));
  const __amdgpu_buffer_rsrc_t sq = __builtin_amdgcn_make_buffer_rsrc((void*)(q + (size_t)tb * 32 * DA + cg * 64), (short)0, 32 * DA * 2, 0x00020000);
  const unsigned vq = ((lane >> 3) * DA + (lane & 7) * 8) * 2;
  asm volatile("s_mov_b32 m0, %0\n\ts_nop 4\n\tbuffer_load_dwordx4 %1, %2, 0 offen lds\n\tbuffer_load_dwordx4 %1, %2, %3 offen offset:1024 lds\n\t"
               "buffer_load_dwordx4 %1, %2, %4 offen offset:2048 lds\n\tbuffer_load_dwordx4 %1, %2, %5 offen offset:3072 lds"
               ::"s"(m0w + 4096), "v"(vq), "s"(sq), "s"(8 * DA * 2 - 1024), "s"(16 * DA * 2 - 2048), "s"(24 * DA * 2 - 3072) : "memory");
}
__device__ __forceinline__ void post_math(int cg, int tb, bf16* __restrict__ out, const char* lw) {
  int lane = threadIdx.x & 63; asm volatile("" : "+v"(lane));
  const int tl = lane >> 3, ck = lane & 7;
  const __amdgpu_buffer_rsrc_t so = __builtin_amdgcn_make_buffer_rsrc((void*)(out + (size_t)tb * 32 * OUTW + cg * 64), (short)0, 32 * OUTW * 2 - 2 * DA, 0x00020000);
  const unsigned vo = (tl * OUTW + ck * 8) * 2;
#pragma unroll
  for (int i = 0; i < 4; ++i) {
    const u32x4c qv = *(const u32x4c*)(lw + 4096 + i * 1024 + lane * 16);
    const char* lz = lw + ck * 64 + 16 * (i ^ (ck >> 1)) + tl * 2;
    u32x4c o;
#pragma unroll
    for (int e = 0; e < 4; ++e) {
      const float z0 = bf2f(*(const unsigned short*)(lz + 2 * e * 512));
      const float z1 = bf2f(*(const unsigned short*)(lz + (2 * e + 1) * 512));
      o[e] = pk2(bfl(qv[e]) * z0, bfh(qv[e]) * z1);
    }
    __builtin_amdgcn_raw_buffer_store_b128(o, so, vo + i * 8 * OUTW * 2, 0, 17);
  }
}

#define DI __device__ __forceinline__
#define GAP() __builtin_amdgcn_sched_barrier(0)
typedef __attribute__((ext_vector_type(4))) float f32x4g;
typedef __attribute__((ext_vector_type(4))) unsigned u32x4g;
constexpr int LDW = NH * HD;
struct GM { unsigned aA[2], aB[2][8]; };
template <bool W, int J, int V> DI void g_dma() {
  if constexpr (J == 0) asm volatile("buffer_load_dwordx4 v%c0, s[%c1:%c2], 0 offen lds" ::"i"(V), "i"(W ? 60 : 56), "i"(W ? 63 : 59) : "memory");
  else asm volatile("buffer_load_dwordx4 v%c0, s[%c1:%c2], s%c3 offen lds" ::"i"(V), "i"(W ? 60 : 56), "i"(W ? 63 : 59), "i"((W ? 70 : 63) + J) : "memory");
}
template <int P, int KS, int I> DI void g_wread(GM& g) {
  constexpr int nf = I >> 1, hf = I & 1, r = 192 - 64 * P + 8 * nf + 4 * KS + 2 * hf;
  asm volatile("ds_read_b64_tr_b16 a[%c1:%c2], %0 offset:%c3" ::"v"(g.aB[hf][nf]), "i"(r), "i"(r + 1), "i"(P * 32768 + KS * 16384));
}
template <int P, int M, int KS> DI void g_aread(GM& g) {
  constexpr int r = 96 + 8 * (M & 1) + 4 * KS;
  asm volatile("ds_read_b128 v[%c1:%c2], %0 offset:%c3" ::"v"(g.aA[KS]), "i"(r), "i"(r + 3), "i"(P * 32768 + M * 2048));
}
constexpr float CS = 0.0625f * 1.4426950408889634f;
constexpr int NS_Q = 19;
constexpr int e_vm(int G) { return (G * 3) % 8 < 3 ? G * 3 / 8 : -1; }
constexpr int e_st(int MODE, int G) {
  const int q = G & 15, k3 = ((G >> 4) - 1) * 3 + (q == 0 ? 0 : q == 4 ? 1 : 2), k2 = NS_Q + (G >> 4) * 2 + (q == 4);
  if (MODE == 5) return e_vm(G) >= 32 ? e_vm(G) - 32 : -1;
  if (MODE == 3) return G >= 33 && (G - 33) % 5 == 0 && (G - 33) / 5 < NS_X ? (G - 33) / 5 : -1;
  if (MODE == 2) return G >= 30 && (q == 0 || q == 4 || q == 14) && k3 - 2 < NS_Q ? k3 - 2 : -1;
  if (MODE == 4) return (q == 0 || q == 4) && k2 < 32 ? k2 : -1;
  return -1;
}
constexpr int e_nst(int MODE, int G0, int G1) { int n = 0; for (int G = G0; G < G1; ++G) n += e_st(MODE, G) >= 0; return n; }
static_assert(e_nst(2, 0, 128) == NS_Q && e_nst(3, 30, 128) == NS_X && e_nst(5, 0, 128) == 16 && e_nst(4, 0, 104) == 32 - NS_Q);
constexpr int e_slot(int MODE, int G) { return MODE == 3 ? (G % 3 == 0 && G < 30 ? G / 3 : -1) : e_vm(G) >= 0 && e_vm(G) < 32 ? 10 + e_vm(G) : -1; }
struct ES { int op0[129], nop[129]; };
constexpr ES e_sched(bool TR, int MODE) {
  ES z{};
  const bool SCL = MODE == 2 || MODE == 4;
  const int per = SCL ? 16 : 8, o1 = 32 * per;
  int o = MODE == 4 ? e_sched(false, 2).op0[128] : 0;
  for (int G = 0; G <= 128; ++G) {
    const int q = G & 15;
    int cap = 2 - (q == 1 || q == 2 || q == 15);
    if (SCL) cap -= (q >= 3 && q < 11 && (q & 1)) + (q == 7 || q == 11) + 2 * (G == 70 || G == 74 || (G & 63) == 61);
    cap += MODE == 2 ? 4 + (q == 10 || q == 13) : MODE == 3 ? 1 : MODE == 5 ? 2 : 0;
    if (e_st(MODE, G) >= 0 || (SCL ? q == 8 || q == 12 : e_slot(MODE, G) >= 0) || (MODE == 4 && G < 4)) cap = 0;
    if (G == 128) cap = 10000;
    z.op0[G] = o;
    while (o < o1) {
      const int i = o / per, rdy = MODE == 4 ? 0 : (TR ? 32 * (i >> 3) + 16 + 2 * (i & 7) + 1 : 4 * i + 3) + 6;
      if ((G < rdy && G < 128) || cap-- <= 0) break;
      ++o; ++z.nop[G];
    }
  }
  return z;
}
template <bool TR, int MODE> constexpr ES ESCH = e_sched(TR, MODE);
static_assert(ESCH<false, 4>.op0[96] == 512 && ESCH<false, 4>.op0[0] >= 24 * 16);
template <bool TR, bool SCL, int O> DI void e_op() {
  constexpr int per = SCL ? 16 : 8, i = O / per, k = O % per, r = 128 + 4 * i, t = 114 + 4 * (i & 1), c = k - (per - 4);
  if constexpr (k < 4) asm volatile("v_accvgpr_read_b32 v%c0, a[%c1]" ::"i"(t + k), "i"(4 * i + k));
  else if constexpr (c < 0) asm volatile("v_mul_f32 v%c0, s83, v%c0" ::"i"(k < 8 ? r + (k ^ 6) : t + k - 8));
  else {
    constexpr int d = c == 0 ? r + 3 : c == 1 ? r + 2 : r + c - 2, x = c == 0 ? r + 2 : c == 1 ? r : t + 2 * (c - 2);
    asm volatile("v_cvt_pk_bf16_f32 v%c0, v%c1, v%c2" ::"i"(d), "i"(x), "i"(x + 1));
  }
}
template <bool TR, bool SCL, int B, int... Is> DI void e_ops(iseq<Is...>) { (e_op<TR, SCL, B + Is>(), ...); }
template <int S> DI void e_m0() {
  if constexpr (S == 0 || S == 1 || S == 5 || S == 9) asm volatile("s_add_u32 m0, s78, %0" ::"i"(S * 1024));
  if constexpr (S >= 26 && (S - 26) % 4 == 0) asm volatile("s_add_u32 m0, s79, %0" ::"i"(((S - 26) >> 3) * 32768 + ((S - 26) & 4) * 1024));
}
template <int S> DI void e_load() {
  if constexpr (S == 0) asm volatile("buffer_load_dwordx4 v122, s[56:59], 0 offen lds" ::: "memory");
  else if constexpr (S < 5) asm volatile("buffer_load_dwordx4 v84, s[56:59], s64 offen offset:%0 lds" ::"i"((S - 1) * 1024) : "memory");
  else if constexpr (S < 9) asm volatile("buffer_load_dwordx4 v84, s[60:63], 0 offen offset:%0 sc1 lds" ::"i"((S - 5) * 1024) : "memory");
  else if constexpr (S == 9) asm volatile("buffer_load_dwordx4 v84, s[60:63], s62 offen sc1 lds" ::: "memory");
  else if constexpr (S < 26) {
    constexpr int f = S - 10;
    asm volatile("buffer_load_dwordx4 a[%c0:%c1], v84, s[88:91], s96 offen offset:%2 sc1" ::"i"(128 + 4 * f), "i"(131 + 4 * f), "i"((f & 3) * 1024) : "memory");
    if constexpr ((f & 3) == 3) asm volatile("s_addk_i32 s96, %0" ::"i"(f == 7 ? 28672 : 4096));
  } else {
    constexpr int f = S - 26;
    asm volatile("buffer_load_dwordx4 v%c0, s[88:91], s98 offen offset:%1 sc1 lds" ::"i"(86 + ((f >> 2) & 1)), "i"((f & 3) * 1024) : "memory");
    if constexpr (f == 7) asm volatile("s_add_u32 s98, s98, s97");
    if constexpr (f == 15) asm volatile(FL_POLL ::: "memory");
  }
}
template <bool TR, int P, int H, int G, int MODE> DI void g_gap(GM& g) {
  constexpr int m = 4 * H + (G >> 4), q = G & 15, n = q >> 1, ks = q & 1, GG = 64 * H + G;
  constexpr bool inV = TR ? m & 1 : n & 1, END = MODE == 3 || MODE == 5;
  constexpr int a = 96 + 8 * (m & 1) + 4 * ks, b = 192 - 64 * P + 8 * n + 4 * ks, d = (inV ? 128 : 0) + 4 * (TR ? 8 * (m >> 1) + n : 4 * m + (n >> 1));
  constexpr int J = END ? -1 : q == 8 ? 2 * (G >> 4) : q == 12 ? 2 * (G >> 4) + 1 : -1, gj = (J & 3) ^ ((J & 3) >> 1);
  if constexpr (END) { if constexpr (q == 0 && GG > 0) asm volatile("s_waitcnt lgkmcnt(0)"); e_m0<e_slot(MODE, GG)>(); }
  else if constexpr (q == 0 && G > 0) asm volatile("s_waitcnt lgkmcnt(4)");
  if constexpr (J == 0) asm volatile("s_add_u32 m0, s78, %0" ::"i"((P ^ H ^ 1) * 32768 + (1 - H) * 8192));
  else if constexpr (J == 4) asm volatile("s_add_u32 m0, s79, %0" ::"i"((P ^ H) * 32768 + (1 - H) * 16384));
  else if constexpr (J > 0 && (J & 3) == 1) asm volatile("s_bitset1_b32 m0, 12");
  else if constexpr (J > 0 && (J & 3) == 2) asm volatile("s_bitset1_b32 m0, %0" ::"i"(J < 4 ? 14 : 13));
  else if constexpr (J > 0) asm volatile("s_bitset0_b32 m0, 12");
#define GMF(D, X, Y, C) asm volatile("v_mfma_f32_16x16x32_bf16 " D "[%c0:%c1], " X ", " Y ", " C ::"i"(d), "i"(d + 3), "i"(a), "i"(a + 3), "i"(b), "i"(b + 3))
#define GMF2(D, C) do { if constexpr (TR) GMF(D, "v[%c2:%c3]", "a[%c4:%c5]", C); else GMF(D, "a[%c4:%c5]", "v[%c2:%c3]", C); } while (0)
  if constexpr ((MODE == 1 || MODE == 4) && ks == 0) { if constexpr (inV) GMF2("v", "0"); else GMF2("a", "0"); }
  else if constexpr (inV) GMF2("v", "v[%c0:%c1]");
  else GMF2("a", "a[%c0:%c1]");
#undef GMF2
#undef GMF
  if constexpr ((q == 1 || q == 2) && !(END && m == 7)) g_aread<(m < 7 ? P : P ^ 1), (m + 1) & 7, q - 1>(g);
  if constexpr (!END && q >= 3 && q < 11 && (q & 1)) g_wread<P ^ 1, H, (G >> 4) * 4 + (q - 3) / 2>(g);
  if constexpr (e_st(MODE, GG) >= 0) {
    static_assert(ESCH<TR, MODE>.op0[GG] >= (e_st(MODE, GG) + 1) * (END ? 8 : 16));
    d_store<e_st(MODE, GG)>();
  }
  if constexpr (J >= 0 && J < 4) g_dma<false, (1 - H) * 2 + (gj & 1) + (gj >> 1) * 4, 90>();
  else if constexpr (J >= 4) g_dma<true, (1 - H) * 4 + gj, 91>();
  if constexpr (MODE >= 2) e_ops<TR, !END, ESCH<TR, MODE>.op0[GG]>(make_iseq<ESCH<TR, MODE>.nop[GG]>{});
  if constexpr (END) {
    if constexpr (e_slot(MODE, GG) >= 0) e_load<e_slot(MODE, GG)>();
  } else {
    if constexpr (H == 1 && G == 6) asm volatile("v_lshl_add_u32 v90, s82, 7, v92\n\ts_and_b32 s82, s80, 15");
    if constexpr (H == 1 && G == 10) asm volatile("s_lshr_b64 s[80:81], s[80:81], 4\n\tv_lshl_add_u32 v91, s82, 19, v93");
    if constexpr (G == 61) {
      constexpr int nst = e_nst(MODE, H ? 29 : 0, GG + 1) + (MODE == 4 && !H ? e_nst(2, 93, 128) : 0);
      asm volatile("s_waitcnt vmcnt(%0) lgkmcnt(0)\n\ts_barrier" ::"i"(12 + nst) : "memory");
    }
  }
  if constexpr (END && GG == 127) {
    asm volatile("s_nop 7");
    e_ops<TR, false, ESCH<TR, MODE>.op0[128]>(make_iseq<ESCH<TR, MODE>.nop[128]>{});
  }
  GAP();
}
template <bool TR, int P, int H, int MODE, int... Gs> DI void g_half_(GM& g, iseq<Gs...>) { (g_gap<TR, P, H, Gs, MODE>(g), ...); }
template <bool TR, int P, int H, int MODE> DI void g_half(GM& g) { g_half_<TR, P, H, MODE>(g, make_iseq<64>{}); }
template <bool TR, int P, int MODE> DI void g_tile(GM& g) { g_half<TR, P, 0, MODE>(g); g_half<TR, P, 1, MODE>(g); }
template <int P, int... Is> DI void g_read0(GM& g, iseq<Is...>) {
  (g_wread<P, 0, Is>(g), ...); (g_wread<P, 1, Is>(g), ...);
  g_aread<P, 0, 0>(g); g_aread<P, 0, 1>(g);
}
DI void g_proW(unsigned g, unsigned mW) {
  asm volatile("v_lshl_add_u32 v91, %0, 18, v93\n\ts_mov_b32 m0, %1" ::"s"(g), "s"(mW));
  asm volatile("s_nop 0"); g_dma<true, 0, 91>();
  asm volatile("s_bitset1_b32 m0, 12\n\ts_nop 0"); g_dma<true, 1, 91>();
  asm volatile("s_bitset1_b32 m0, 13\n\ts_nop 0"); g_dma<true, 3, 91>();
  asm volatile("s_bitset0_b32 m0, 12\n\ts_nop 0"); g_dma<true, 2, 91>();
}
template <int PART = 3> DI void g_pro(unsigned i, unsigned mA, unsigned mW, unsigned lda) {
  i &= 3;
  if constexpr (PART & 1)
    asm volatile("v_add_u32 v90, %0, v92\n\tv_add_u32 v91, %1, v93\n\ts_mov_b32 m0, %2\n\ts_mov_b32 s54, %3" ::"s"(((i & 1) + (i >> 1) * 4) * 64 * lda),
                 "s"((i & 1) * 0x20000 + (i >> 1) * 256), "s"(mA + (i & 1) * 4096 + (i >> 1) * 16384), "s"(mW + i * 4096));
  if constexpr (PART == 1) return;
  g_dma<false, 0, 90>();
  asm volatile("s_bitset1_b32 m0, 13\n\ts_nop 0"); g_dma<false, 2, 90>();
  asm volatile("s_mov_b32 m0, s54\n\ts_nop 0"); g_dma<true, 0, 91>();
  asm volatile("s_bitset1_b32 m0, 14\n\ts_nop 0"); g_dma<true, 4, 91>();
  asm volatile("s_xor_b32 m0, s54, 0x8000\n\ts_nop 0\n\tbuffer_load_dwordx4 v91, s[60:63], s53 offen lds" ::: "memory", "scc");
}
DI void g_pro2(unsigned mA, unsigned mW) {
  asm volatile("v_add_u32 v90, 128, v92\n\ts_mov_b32 m0, %0" ::"s"(mA ^ 32768));
  asm volatile("s_nop 0"); g_dma<false, 0, 90>();
  asm volatile("s_bitset1_b32 m0, 12\n\ts_nop 0"); g_dma<false, 1, 90>();
  asm volatile("s_bitset1_b32 m0, 14\n\ts_nop 0"); g_dma<false, 5, 90>();
  asm volatile("s_bitset0_b32 m0, 12\n\ts_nop 0"); g_dma<false, 4, 90>();
  g_proW(3, mW ^ 49152);
}
template <bool TR, bool SCL, class HOOK> DI void g_loop(GM& g, unsigned evm, HOOK&& hook) {
  constexpr int nkt = SCL ? 7 : 16, P0 = SCL ? 0 : 1;
  asm volatile("" : "+s"(evm));
#pragma unroll 1
  for (int t = 1;; t += 2) {
    g_half<TR, P0 ^ 1, 0, 0>(g);
    if (__builtin_expect((evm >> t) & 1, 0)) { hook(t); GAP(); }
    g_half<TR, P0 ^ 1, 1, 0>(g);
    if (SCL && t + 2 >= nkt) break;
    g_half<TR, P0, 0, 0>(g);
    if constexpr (SCL) { if (__builtin_expect(t == 3, 0)) { hook(4); GAP(); } }
    else asm volatile("s_cmp_eq_u32 %0, 13\n\ts_cselect_b32 s78, 0x28000, s78" ::"s"(t) : "scc");
    g_half<TR, P0, 1, 0>(g);
    if (!SCL && t + 3 >= nkt) break;
  }
}

typedef __attribute__((ext_vector_type(4))) unsigned u32x4;
typedef __attribute__((ext_vector_type(4))) float f32x4;

constexpr int R_S = 128, R_P = 192, R_CI = 224, R_E = 96;
constexpr int R_TS = 112, R_RS = 114, R_M = 116;
constexpr int R_PD = 118, R_THR = 120, R_TMP = 121;
constexpr int r_dk(int q) { return q < 6 ? 90 + q : 120 + q; }
constexpr int S_V = 96, S_T = 97, S_K = 98, S_I = 99, S_EV = 100;
struct AC {
  unsigned rare;
  unsigned tb, vs, o0, o1;
};
DI bool redo() { u64 t; asm volatile("s_mov_b64 %0, s[54:55]" : "=s"(t)); return t != 0; }
template <int X, int F> DI void kread() {
  constexpr int r = 192 + 4 * (F & 15);
  asm volatile("ds_read_b128 a[%c0:%c1], v84 offset:%c2" ::"i"(r), "i"(r + 3), "i"(X * 32768 + F * 1024));
}
template <int X, int F> DI void vread() {
  constexpr int r = 192 + 4 * (F & 15);
  asm volatile("ds_read_b128 a[%c0:%c1], v85 offset:%c2" ::"i"(r), "i"(r + 3), "i"(X * 32768 + (F & 1) * 16384 + (F >> 1) * 1024));
}
template <int X, int... Fs> DI void kreads(iseq<Fs...>) { (kread<X, Fs>(), ...); }
template <int X, int J> DI void dma_m0() {
  if constexpr (J == 0) asm volatile("s_mov_b32 m0, s%c0" ::"i"(78 + X));
  else if constexpr (J == 4) asm volatile("s_or_b32 m0, s%c0, 0x1000" ::"i"(78 + X));
  else if constexpr (J == 8) asm volatile("s_bitset0_b32 m0, 16");
  else if constexpr (J == 12) asm volatile("s_bitset0_b32 m0, 12");
}
template <int J> DI void dma_at() {
  if constexpr (J < 8)
    asm volatile("buffer_load_dwordx4 v%c0, s[88:91], s96 offen offset:%1 sc1 lds" ::"i"(86 + (J >= 4)), "i"((J & 3) * 1024) : "memory");
  else
    asm volatile("buffer_load_dwordx4 v%c0, s[92:95], s98 offen offset:%1 sc1 lds" ::"i"(r_dk(J - 8)), "i"((J & 3) * 1024) : "memory");
}
template <int Q> DI void dma4k() {
  asm volatile("s_xor_b32 m0, s79, %0\n\ts_add_u32 s48, s98, %1\n\tbuffer_load_dwordx4 v%c2, s[88:91], s48 offen sc1 lds\n\t"
               "buffer_load_dwordx4 v%c2, s[88:91], s48 offen offset:1024 sc1 lds\n\tbuffer_load_dwordx4 v%c2, s[88:91], s48 offen offset:2048 sc1 lds\n\t"
               "buffer_load_dwordx4 v%c2, s[88:91], s48 offen offset:3072 sc1 lds"
               ::"i"((Q >> 1) * 32768 + (Q & 1) * 4096), "i"((Q >> 1) * 32768), "i"(86 + (Q & 1)) : "memory", "scc");
}
DI void acc_zero() { static_for([](auto I) { asm volatile("v_accvgpr_write_b32 a[%c0], 0" ::"i"((int)I)); }, make_iseq<128>{}); }
#define SMV 0
struct SMS { int ex[128], op0[128], nop[128], last; };
constexpr SMS sm_sched() {
  SMS z{};
  int et[32] = {}, e = 0;
  for (int t = 0; t < 128; ++t) {
    const int g = t & 15;
    const bool x = t < 48 ? (g >= 8 && (g & 1) && g < 15) : t < 64 ? (g >= 9 && g < 15) : (g >= 8 && g < 14);
    z.ex[t] = x && e < 32 ? e : -1;
    if (z.ex[t] >= 0) et[e++] = t;
  }
  int o = 0;
  for (int t = 0; t < 128; ++t) {
    const int g = t & 15;
    int cap = z.ex[t] >= 0 || t < 8 || (t < 48 && (g == 7 || (g >= 8 && !(g & 1)))) || t == 56 || t == 63 || t == 111 ? 0 : 1;
    z.op0[t] = o; z.nop[t] = 0;
    while (cap > 0 && o < 48 && et[2 * (o / 3) + (o % 3 == 0 ? 0 : 1)] + 2 <= t) {
      const int el = 2 * (o / 3) + o % 3;
      if (!(o % 3 < 2 && (el == 0 || el == 4))) --cap;
      ++o; ++z.nop[t]; z.last = t;
    }
  }
  for (int k = 16; k < 32; ++k) if (z.op0[et[k]] < 3 * ((k - 16) / 2 + 1)) z.last = 1000;
  return z;
}
constexpr SMS SM = sm_sched();
static_assert(SM.last <= 117);
constexpr int p_reg(int Y, int k) { return R_P + 16 * Y + 4 * ((k >> 3) * 2 + ((k >> 1) & 1)) + ((k >> 2) & 1) * 2 + (k & 1); }
template <int Y, int O> DI void sm_op() {
  constexpr int k = O / 3, e = 2 * k + O % 3, ts = R_TS + ((e >> 2) & 1);
  if constexpr (O % 3 == 2) asm volatile("v_cvt_pk_bf16_f32 v%c0, v%c1, v%c2" ::"i"(p_reg(Y, k)), "i"(R_E + ((2 * k) & 15)), "i"(R_E + ((2 * k + 1) & 15)));
  else if constexpr (e == 1 || e == 5) asm volatile("v_add_f32_e32 v%c0, v%c1, v%c2" ::"i"(ts), "i"(R_E + e - 1), "i"(R_E + e));
  else if constexpr (e != 0 && e != 4) asm volatile("v_add_f32_e32 v%c0, v%c0, v%c1" ::"i"(ts), "i"(R_E + (e & 15)));
}
template <int Y, int T> DI void sm_gap(AC& c) {
  if constexpr (SM.ex[T] >= 0) asm volatile("v_exp_f32 v%c0, v%c1" ::"i"(R_E + (SM.ex[T] & 15)), "i"(R_S + 32 * Y + SM.ex[T]));
  if constexpr (SM.nop[T] > 0) sm_op<Y, SM.op0[T]>();
  if constexpr (SM.nop[T] > 1) sm_op<Y, SM.op0[T] + 1>();
  if constexpr (SM.nop[T] > 2) sm_op<Y, SM.op0[T] + 2>();
  if constexpr (T == SM.last + 1) asm volatile("v_add_f32_e32 v%c0, v%c1, v%c2" ::"i"(R_TMP), "i"(R_TS), "i"(R_TS + 1));
  if constexpr (T == SM.last + 2) asm volatile("v_cmp_nle_f32_e64 s[54:55], v%c0, v%c1" ::"i"(R_TMP), "i"(R_THR));
}
template <int X, int MODE, int G> DI void gapA(AC& c) {
  constexpr bool FIRST = MODE == 1 || MODE == 2, NOQK = MODE == 3;
  constexpr int Xp = X ^ 1, kb = G >> 4, g = G & 15, qb = g >> 3, s = g & 7;
  if constexpr (MODE == 3 && G < 48 && G % 3 == 0) {
    constexpr int f = G / 3;
    asm volatile("buffer_load_dwordx4 a[%c0:%c1], v84, s[88:91], s75 offen offset:%2 sc1" ::"i"(128 + 4 * f), "i"(131 + 4 * f), "i"((f & 3) * 1024) : "memory");
    if constexpr ((f & 3) == 3) asm volatile("s_addk_i32 s75, %0" ::"i"(f == 7 ? 28672 : 4096));
  }
  if constexpr (G == 48) asm volatile("s_waitcnt vmcnt(%0) lgkmcnt(0)\n\ts_barrier" ::"i"(MODE == 3 ? 16 : MODE == 2 ? 12 : MODE == 1 ? 7 : 0) : "memory");
  else if constexpr (g == 0 && !NOQK) asm volatile("s_waitcnt lgkmcnt(0)");
  if constexpr (!NOQK) {
    constexpr int r = 192 + 4 * ((kb & 1) * 8 + s), q = 128 + 4 * g;
    constexpr int d = R_S + 32 * X + 4 * (kb * 2 + qb), ci = s == 0 ? R_CI + 4 * (kb * 2 + qb) : d;
    if constexpr (FIRST && s == 0)
      asm volatile("v_mfma_f32_16x16x32_bf16 v[%c0:%c1], a[%c2:%c3], a[%c4:%c5], 0" ::"i"(d), "i"(d + 3), "i"(r), "i"(r + 3), "i"(q), "i"(q + 3));
    else
      asm volatile("v_mfma_f32_16x16x32_bf16 v[%c0:%c1], a[%c2:%c3], a[%c4:%c5], v[%c6:%c7]" ::"i"(d), "i"(d + 3), "i"(r), "i"(r + 3), "i"(q), "i"(q + 3),
                   "i"(ci), "i"(ci + 3));
    if constexpr (g < 8 && kb < 3) kread<X, (kb + 1) * 8 + g>();
    constexpr int S0 = MODE == 1 ? 0 : SMV ? 40 : 14, S1 = MODE == 1 ? 1 : SMV ? 41 : 30, S2 = SMV ? 42 : 45, S3 = SMV ? 43 : 46;
    if constexpr (G == S0) asm volatile("s_add_u32 s96, s97, 0x1000000");
    if constexpr (G == S1) dma_m0<X, 0>();
    if constexpr (G == S2) asm volatile("s_mov_b32 s97, s98");
    if constexpr (G == S3) asm volatile("s_add_u32 s98, s98, s99");
    if constexpr (MODE != 1 && G >= 56 && !(G & 1)) dma_at<(G - 56) / 2>();
    if constexpr (MODE == 1 && (G & 11) == 9) dma_at<(G >> 4) * 2 + ((G >> 2) & 1)>();
    if constexpr (MODE == 1 && G == 31) dma_m0<X, 4>();
    if constexpr (G == 57) asm volatile("s_lshr_b32 s100, s100, 1\n\ts_add_u32 s56, s56, 1");
    if constexpr (G == 59) asm volatile("s_getpc_b64 s[86:87]\nev_pc%c0:\n\ts_add_u32 s86, s86, ev_ret%c0-ev_pc%c0\n\ts_addc_u32 s87, s87, 0" ::"i"(X * 4 + MODE));
    if constexpr (G == 61 && !FIRST) asm volatile("s_or_b32 s57, s54, s55\n\ts_mov_b32 %0, s57\n\ts_bitcmp1_b32 s100, 0" : "=s"(c.rare));
    if constexpr (G == 61 && FIRST) asm volatile("s_bitcmp1_b32 s100, 0");
  } else if constexpr (SM.nop[64 + G] > 0) asm volatile("s_nop 0");
  if constexpr (!FIRST && g < 8 && kb == 3) vread<Xp, g>();
  if constexpr (MODE == 2 && (G & 3) == 0) d_store<16 + G / 4>();
  if constexpr (!FIRST) sm_gap<Xp, 64 + G>(c);
  if constexpr (G == 63 && !NOQK) asm volatile("s_cbranch_scc1 ev_entry\nev_ret%c0:" ::"i"(X * 4 + MODE) : "vcc", "memory");
  GAP();
}
template <int R> DI void rescale1(float alpha) {
  float t;
  asm volatile("v_accvgpr_read_b32 %0, a[%c2]\n\tv_mul_f32 %0, %0, %1\n\tv_accvgpr_write_b32 a[%c2], %0" : "=&v"(t) : "v"(alpha), "i"(R));
}
template <int... Rs> DI void rescale_all(const float (&alpha)[2], iseq<Rs...>) { (rescale1<Rs>(alpha[(Rs >> 2) & 1]), ...); }
template <bool MAX> DI float row_reduce(float v) {
  auto p = __builtin_amdgcn_permlane32_swap(__float_as_uint(v), __float_as_uint(v), false, false);
  v = MAX ? fmaxf(__uint_as_float(p[0]), __uint_as_float(p[1])) : __uint_as_float(p[0]) + __uint_as_float(p[1]);
  p = __builtin_amdgcn_permlane16_swap(__float_as_uint(v), __float_as_uint(v), false, false);
  return MAX ? fmaxf(__uint_as_float(p[0]), __uint_as_float(p[1])) : __uint_as_float(p[0]) + __uint_as_float(p[1]);
}
template <int Y, int QB> DI float row_max() {
  constexpr int r = R_S + 32 * Y + 4 * QB;
  float mx;
  asm volatile("v_max_f32 %0, v%c1, v%c2\n\tv_max3_f32 %0, %0, v%c3, v%c4" : "=&v"(mx) : "i"(r), "i"(r + 1), "i"(r + 2), "i"(r + 3));
  static_for([&](auto K) {
    constexpr int q = r + 8 * (K + 1);
    float t = mx;
    asm volatile("v_max3_f32 %0, %0, v%c1, v%c2\n\tv_max3_f32 %0, %0, v%c3, v%c4" : "+v"(t) : "i"(q), "i"(q + 1), "i"(q + 2), "i"(q + 3));
    mx = t;
  }, make_iseq<3>{});
  return row_reduce<true>(mx);
}
template <int R> DI void v_sub(float d) { asm volatile("v_sub_f32 v%c0, v%c0, %1" ::"i"(R), "v"(d)); }
template <int B> DI void s_shift(const float (&d)[2]) { static_for([&](auto E) { v_sub<B + E>(d[(E >> 2) & 1]); }, make_iseq<32>{}); }
template <bool CI> DI void m_shift(const float (&d)[2]) {
  if constexpr (CI) s_shift<R_CI>(d);
  v_sub<R_M>(-d[0]); v_sub<R_M + 1>(-d[1]);
}
DI void set_mask0() {
  const int mr = __float_as_int(vget<89>());
  static_for([&](auto I) {
    constexpr int e = (I >> 2) * 8 + (I & 3), k = (I >> 2) * 16 + (I & 3);
    const float x = vget<R_S + e>();
    vset<R_S + e>(k > mr ? -INFINITY : x);
  }, make_iseq<16>{});
}
template <int X, bool CI = true> DI void sm_redo() {
  constexpr int Xp = X ^ 1;
  float d[2], dt[2], alpha[2], ts[2] = {0.f, 0.f};
  static_for([&](auto Q) {
    constexpr int qb = Q;
    const float pd = vget<R_PD + qb>();
    d[qb] = fmaxf(row_max<Xp, qb>() - pd, 0.f);
    dt[qb] = d[qb] + pd;
    vset<R_PD + qb>(d[qb]);
    alpha[qb] = __builtin_amdgcn_exp2f(-d[qb]);
    vset<R_RS + qb>(vget<R_RS + qb>() * alpha[qb]);
  }, make_iseq<2>{});
  vset<R_THR>(d[0] > 0.f || d[1] > 0.f ? -1.f : 4096.f);
  m_shift<CI>(d);
  static_for([&](auto K) {
    constexpr int k = K, qb = (k >> 1) & 1;
    const float e0 = __builtin_amdgcn_exp2f(vget<R_S + 32 * Xp + 2 * k>() - dt[qb]), e1 = __builtin_amdgcn_exp2f(vget<R_S + 32 * Xp + 2 * k + 1>() - dt[qb]);
    ts[qb] += e0; ts[qb] += e1;
    vset<p_reg(Xp, k)>(__uint_as_float(pk2(e0, e1)));
  }, make_iseq<16>{});
  vset<R_TS>(ts[0]); vset<R_TS + 1>(ts[1]);
  rescale_all(alpha, make_iseq<128>{});
  asm volatile("s_nop 1" ::: "memory");
  GAP();
}
template <int X, int MODE, int... Gs> DI void phaseA_(AC& c, iseq<Gs...>) { (gapA<X, MODE, Gs>(c), ...); }
template <int X, int MODE> DI void phaseA(AC& c) { phaseA_<X, MODE>(c, make_iseq<64>{}); }

template <int X, bool SMX, int G, bool FIRST = false> DI void gapB(AC& c) {
  constexpr int Xp = X ^ 1, blk = G >> 4, g = G & 15;
  if constexpr (g == 0) asm volatile("s_waitcnt lgkmcnt(0)");
  constexpr int qb = (g >> 1) & 1, s = g & 1, r = 192 + 4 * ((blk & 1) * 8 + (g >> 2) * 2 + s), o = 4 * ((blk * 4 + (g >> 2)) * 2 + qb);
  constexpr int p = R_P + 16 * Xp + 4 * (s * 2 + qb);
  if constexpr (FIRST && s == 0)
    asm volatile("v_mfma_f32_16x16x32_bf16 a[%c0:%c1], a[%c2:%c3], v[%c4:%c5], 0" ::"i"(o), "i"(o + 3), "i"(r), "i"(r + 3), "i"(p), "i"(p + 3));
  else
  asm volatile("v_mfma_f32_16x16x32_bf16 a[%c0:%c1], a[%c2:%c3], v[%c4:%c5], a[%c0:%c1]" ::"i"(o), "i"(o + 3), "i"(r), "i"(r + 3), "i"(p), "i"(p + 3));
  if constexpr (g < 8) {
    if constexpr (blk < 3) vread<Xp, (blk + 1) * 8 + g>();
    else if constexpr (SMX) kread<Xp, g>();
  }
  if constexpr (SMX) {
    if constexpr (G == 1 || G == 2) asm volatile("v_add_f32_e32 v%c0, v%c0, v%c1" ::"i"(R_RS + G - 1), "i"(R_TS + G - 1));
    if constexpr (g == 7 && blk < 3) dma_m0<X, blk * 4 + 4>();
    if constexpr (g >= 8 && !(g & 1) && blk < 3) dma_at<blk * 4 + 4 + (g - 8) / 2>();
    if constexpr (G == 55) asm volatile("s_cmp_eq_u32 s56, s76");
    if constexpr (G == 3) asm volatile("s_lshr_b32 s77, s77, 1");
    if constexpr (G == 4) asm volatile("s_bitcmp1_b32 s77, 0");
    if constexpr (G == 5) asm volatile("s_cselect_b64 s[52:53], s[62:63], 0");
    if constexpr (G == 6) asm volatile(FL_ARM("s[52:53]"));
    if constexpr (G == 56) asm volatile(FL_POLL ::: "memory");
    sm_gap<X, G>(c);
  }
  GAP();
}
template <int X, bool FIRST, int... Gs> DI void phaseB_(AC& c, iseq<Gs...>) { (gapB<X, true, Gs, FIRST>(c), ...); }
template <int X, bool FIRST = false> DI void phaseB(AC& c) { phaseB_<X, FIRST>(c, make_iseq<64>{}); }
constexpr int sm_nex() { int n = 0; for (int t = 0; t < 64; ++t) n += SM.ex[t] >= 0; return n; }
template <int X> DI void phaseB0(unsigned mode, bool vh) {
  dma_m0<X, 4>();
  kreads<X ^ 1>(make_iseq<8>{});
  if (vh) { dma_at<4>(); dma_at<5>(); dma_at<6>(); dma_at<7>(); }
  dma_m0<X, 8>();
  asm volatile("s_nop 3");
  float d[2];
  if (mode & 2) {
    d[0] = vget<122>(); d[1] = vget<123>();
    dma_at<8>();
  } else {
    if constexpr (X == 0) if (__builtin_expect(mode & 4, 0)) set_mask0();
    d[0] = row_max<X, 0>(); GAP(); dma_at<8>();
    d[1] = row_max<X, 1>();
#pragma unroll
    for (int qb = 0; qb < 2; ++qb) if (!(d[qb] > -3.0e38f)) d[qb] = 0.f;
  }
  GAP();
  vset<R_M>(d[0]); vset<R_M + 1>(d[1]);
  dma_at<9>();
  const f32x2c nd[2] = {{-d[0], -d[0]}, {-d[1], -d[1]}};
  static_for([&](auto E) {
    constexpr int e = E;
    const f32x2c v = nd[(e >> 1) & 1];
    asm volatile("v_pk_add_f32 v[%c0:%c1], v[%c0:%c1], %4\n\tv_mov_b64 v[%c2:%c3], %4" ::"i"(R_S + 32 * X + 2 * e), "i"(R_S + 32 * X + 2 * e + 1), "i"(R_CI + 2 * e), "i"(R_CI + 2 * e + 1), "v"(v));
    if constexpr (e == 7 || e == 15) dma_at<10 + e / 8>();
  }, make_iseq<16>{});
  static_assert(SM.op0[64] >= 3 * ((sm_nex() - 15) / 2) && SM.op0[64] <= 24);
  static_for([&](auto E) {
    constexpr int e = E;
    if constexpr (e < sm_nex()) asm volatile("v_exp_f32 v%c0, v%c1" ::"i"(R_E + e), "i"(R_S + 32 * X + e));
    if constexpr (e == 2) dma_m0<X, 12>();
    if constexpr (e == 5 || e == 10 || e == 15) dma_at<11 + e / 5>();
  }, make_iseq<16>{});
  static_for([&](auto O) { sm_op<X, O>(); }, make_iseq<SM.op0[64]>{});
  dma_at<15>();
  static_for([&](auto E) { asm volatile("v_exp_f32 v%c0, v%c1" ::"i"(R_E + (int)E), "i"(R_S + 32 * X + 16 + (int)E)); }, make_iseq<(sm_nex() > 16 ? sm_nex() - 16 : 0)>{});
  asm volatile("buffer_store_dwordx2 v[%c0:%c1], v88, s[88:91], s76 offen sc1\n\ts_bitcmp1_b32 s77, 0\n\ts_cselect_b64 s[52:53], s[62:63], 0\n\t"
               FL_ARM("s[52:53]") "\n\t" FL_POLL ::"i"(R_M), "i"(R_M + 1) : "memory", "scc");
  GAP();
}
enum : int { T_Q0, T_K0, T_OQ, T_OG, T_FPREP, T_FQ, T_FG, T_FS, T_ZO, T_CNT, T_NPRE = 10, T_NST = 12, T_TAB = 14, T_SLAB = 16, T_PART, T_IT = 18, T_ITN = 23 };
#define MSK(R, K) "v_cmp_gt_i32 s[48:49], " #K ", v89\n\tv_cmp_gt_i32 s[50:51], " #K "+1, v89\n\tv_cmp_gt_i32 s[52:53], " #K "+2, v89\n\t" \
                  "v_cmp_gt_i32 s[54:55], " #K "+3, v89\n\tv_cndmask_b32_e64 v[" #R "], v[" #R "], v122, s[48:49]\n\t" \
                  "v_cndmask_b32_e64 v[" #R "+1], v[" #R "+1], v122, s[50:51]\n\tv_cndmask_b32_e64 v[" #R "+2], v[" #R "+2], v122, s[52:53]\n\t" \
                  "v_cndmask_b32_e64 v[" #R "+3], v[" #R "+3], v122, s[54:55]\n\t"
DI void ev_code(AC& c) {
  GAP();
  asm volatile(
    "s_getpc_b64 s[82:83]\n\ts_add_u32 s82, s82, 12\n\ts_addc_u32 s83, s83, 0\n\ts_branch 9f\nev_entry:\n\t"
    "s_bitcmp1_b32 s60, s56\n\ts_cbranch_scc1 3f\n\ts_bitcmp1_b32 s74, s56\n\ts_cbranch_scc1 2f\n\t"
    "s_bitcmp1_b32 s58, s56\n\ts_cselect_b64 vcc, -1, 0\n\tv_cndmask_b32 v121, -2.0, v81, vcc\n\t"
    "buffer_store_dwordx2 v[82:83], v121, s[88:91], s70 offen sc1\n\tbuffer_store_dwordx2 v[82:83], v121, s[88:91], s71 offen sc1\n\t"
    "v_cmp_eq_u64 vcc, s[84:85], v[124:125]\n\ts_andn2_b64 vcc, s[62:63], vcc\n\ts_cbranch_scc1 5f\n"
    "0:\n\ts_add_u32 s98, s98, s59\n\ts_mov_b32 s59, s61\n\ts_mov_b32 s61, s72\n\ts_mov_b64 s[62:63], s[64:65]\n\ts_mov_b64 s[64:65], s[66:67]\n\t"
    "s_mov_b64 s[66:67], s[68:69]\n\ts_cmp_eq_u32 s56, 1\n\ts_cbranch_scc1 ev_first\n\ts_setpc_b64 s[86:87]\n"
    "5:\n\t" FL_CHECK("s[62:63]") "\n\ts_branch 0b\n"
    "6:\n\t" FL_CHECK("s[62:63]") "\n\ts_setpc_b64 s[86:87]\n"
    "2:\n\ts_mov_b32 s98, 0\n\t"
    "v_lshrrev_b32 v90, 7, v86\n\tv_and_b32 v91, 6, v90\n\tv_lshlrev_b32 v91, 4, v91\n\tv_and_b32 v92, 0x70, v86\n\tv_xor_b32 v91, v91, v92\n\t"
    "s_movk_i32 s48, 896\n\tv_mad_u32_u24 v94, v90, s48, v91\n\ts_mov_b64 s[92:93], s[80:81]\n\ts_mov_b32 s94, 229376\n\t"
    "v_add_u32 v90, 28672, v94\n\tv_add_u32 v91, 34816, v94\n\tv_add_u32 v92, 40960, v94\n\tv_add_u32 v93, 47104, v94\n\t"
    "v_add_u32 v95, 6144, v94\n\tv_add_u32 v126, 12288, v94\n\tv_add_u32 v127, 18432, v94\n\t"
    "v_mov_b32 v122, 0xff800000\n\t" MSK(228, 0) MSK(236, 16) MSK(244, 32) MSK(252, 48)
    "v_cmp_eq_u64 vcc, s[84:85], v[124:125]\n\ts_andn2_b64 vcc, s[62:63], vcc\n\ts_cbranch_scc1 6b\n\ts_setpc_b64 s[86:87]\n"
    "3:\n\tv_mov_b32 v122, 0xff800000\n\t" MSK(224, 0) MSK(232, 16) MSK(240, 32) MSK(248, 48)
    "v_readlane_b32 s54, v78, %0\n\ts_add_u32 m0, s78, 0x10000\n\t"
    "s_bfe_u32 s53, s73, 0x80010\n\ts_mulk_i32 s53, 3840\n\ts_mov_b64 s[48:49], s[80:81]\n\ts_add_u32 s50, s53, 3840\n\ts_min_u32 s50, s50, 229376\n\t"
    "s_mov_b32 s51, 0x20000\n\t"
    "v_lshlrev_b32 v121, 3, v84\n\tbuffer_load_dword v121, s[48:51], s53 offen lds\n\t"
    "s_bitcmp0_b32 s73, 3\n\ts_cbranch_scc1 4f\n\t"
    "buffer_load_dwordx4 v84, s[88:91], s54 offen sc1 lds\n\tbuffer_load_dwordx4 v84, s[88:91], s54 offen offset:1024 sc1 lds\n\t"
    "buffer_load_dwordx4 v84, s[88:91], s54 offen offset:2048 sc1 lds\n\tbuffer_load_dwordx4 v84, s[88:91], s54 offen offset:3072 sc1 lds\n"
    "4:\n\ts_cmp_eq_u32 s56, 1\n\ts_cbranch_scc1 ev_first\n\ts_setpc_b64 s[86:87]\n"
    ::"i"((int)T_ZO) : "scc");
  asm volatile("ev_zero:");
  acc_zero();
  asm volatile("s_setpc_b64 s[86:87]\nev_first:\n\ts_cmp_lg_u32 s57, 0\n\ts_cbranch_scc1 ev_zero\n\ts_bitcmp0_b32 s73, 4\n\ts_cbranch_scc1 ev_first1");
  GAP();
  phaseB<0, true>(c);
  asm volatile("s_branch att_loop\nev_first1:");
  phaseB<1, true>(c);
  asm volatile("s_branch att_mid\n9:");
  GAP();
}
DI void ev_call() { asm volatile("s_swappc_b64 s[86:87], s[82:83]" ::: "vcc", "scc", "memory"); GAP(); }

#define ACLOB_ALL "a0","a1","a2","a3","a4","a5","a6","a7","a8","a9","a10","a11","a12","a13","a14","a15","a16","a17","a18","a19","a20","a21","a22","a23","a24","a25","a26","a27","a28","a29","a30","a31","a32","a33","a34","a35","a36","a37","a38","a39","a40","a41","a42","a43","a44","a45","a46","a47","a48","a49","a50","a51","a52","a53","a54","a55","a56","a57","a58","a59","a60","a61","a62","a63","a64","a65","a66","a67","a68","a69","a70","a71","a72","a73","a74","a75","a76","a77","a78","a79","a80","a81","a82","a83","a84","a85","a86","a87","a88","a89","a90","a91","a92","a93","a94","a95","a96","a97","a98","a99","a100","a101","a102","a103","a104","a105","a106","a107","a108","a109","a110","a111","a112","a113","a114","a115","a116","a117","a118","a119","a120","a121","a122","a123","a124","a125","a126","a127","a128","a129","a130","a131","a132","a133","a134","a135","a136","a137","a138","a139","a140","a141","a142","a143","a144","a145","a146","a147","a148","a149","a150","a151","a152","a153","a154","a155","a156","a157","a158","a159","a160","a161","a162","a163","a164","a165","a166","a167","a168","a169","a170","a171","a172","a173","a174","a175","a176","a177","a178","a179","a180","a181","a182","a183","a184","a185","a186","a187","a188","a189","a190","a191","a192","a193","a194","a195","a196","a197","a198","a199","a200","a201","a202","a203","a204","a205","a206","a207","a208","a209","a210","a211","a212","a213","a214","a215","a216","a217","a218","a219","a220","a221","a222","a223","a224","a225","a226","a227","a228","a229","a230","a231","a232","a233","a234","a235","a236","a237","a238","a239","a240","a241","a242","a243","a244","a245","a246","a247","a248","a249","a250","a251","a252","a253","a254","a255"

struct EP { float fa[2], fb[2]; };
template <int I, int E, int MODE> DI void epi_pair(const EP& p) {
  constexpr int qb = I & 1, r = 16 * (I >> 1) + 4 * qb + (E >> 1) * 8 + (E & 1) * 2, t = 96 + 2 * E, w = 128 + 4 * I + E;
  if constexpr (MODE == 2) asm volatile("v_accvgpr_read_b32 v106, a[%c0]" ::"i"(w));
  asm volatile("v_accvgpr_read_b32 v%c0, a[%c2]\n\tv_accvgpr_read_b32 v%c1, a[%c3]" ::"i"(t), "i"(t + 1), "i"(r), "i"(r + 1));
  if constexpr (MODE) asm volatile("v_mul_f32 v%c0, v%c0, %2\n\tv_mul_f32 v%c1, v%c1, %2" ::"i"(t), "i"(t + 1), "v"(p.fa[qb]));
  if constexpr (MODE == 2)
    asm volatile("v_lshlrev_b32 v104, 16, v106\n\tv_and_b32 v105, 0xffff0000, v106\n\tv_fmac_f32 v%c0, v104, %2\n\tv_fmac_f32 v%c1, v105, %2"
                 ::"i"(t), "i"(t + 1), "v"(p.fb[qb]));
  asm volatile("v_cvt_pk_bf16_f32 v%c0, v%c1, v%c2" ::"i"(w), "i"(t), "i"(t + 1));
}
template <int I> DI void epi_store() {
  asm volatile("buffer_store_dwordx4 v[%c0:%c1], v84, s[88:91], s101 offen offset:%2 sc1" ::"i"(128 + 4 * I), "i"(131 + 4 * I), "i"((I & 3) * 1024) : "memory");
  if constexpr ((I & 3) == 3) asm volatile("s_addk_i32 s101, 4096");
}
constexpr int R_TB = 232, R_VS = 236;
template <int HF, int R> DI void p_read() {
  constexpr int mb = R >> 4, r = 192 - 64 * HF + 2 * R;
  asm volatile("ds_read_b64_tr_b16 a[%c1:%c2], v%c0 offset:%c3" ::"i"(R_TB + 2 * HF + mb), "i"(r), "i"(r + 1), "i"((R & 15) * 2048));
}
template <int QB, int E> DI void p_cvt() {
  asm volatile("v_cvt_pk_bf16_f32 v%c0, v%c1, v%c2" ::"i"(208 + 4 * QB + E), "i"(192 + 8 * QB + 2 * E), "i"(193 + 8 * QB + 2 * E));
}
template <int QB, int PR> DI void p_store() {
  asm volatile("buffer_store_dwordx4 v[%c0:%c1], v%c2, s[88:91], s96 offen offset:%3 sc1" ::"i"(208 + 4 * QB), "i"(211 + 4 * QB), "i"(R_VS + QB),
               "i"(PR * 128) : "memory");
}
constexpr int p_dma(int HF, int G) { return G == 3 && !HF ? 0 : G == 11 ? 1 - HF : G == 15 ? 2 - HF : G == 19 ? 3 - HF : G == 27 && HF ? 3 : -1; }
template <int HF, bool LD, int G> DI void p_gap() {
  constexpr int ch = G >> 3, t = G & 7, a = 192 - 64 * HF + 32 * (ch & 1) + 4 * t, b = 128 + 4 * (2 * t + (ch >> 1)), d = 192 + 4 * ch;
  if constexpr (t == 0)
    asm volatile("v_mfma_f32_16x16x32_bf16 v[%c0:%c1], a[%c2:%c3], v[%c4:%c5], 0" ::"i"(d), "i"(d + 3), "i"(a), "i"(a + 3), "i"(b), "i"(b + 3));
  else
    asm volatile("v_mfma_f32_16x16x32_bf16 v[%c0:%c1], a[%c2:%c3], v[%c4:%c5], v[%c0:%c1]" ::"i"(d), "i"(d + 3), "i"(a), "i"(a + 3), "i"(b), "i"(b + 3));
  p_read<HF ^ 1, G>();
  if constexpr (G == 0) asm volatile("v_xor_b32 v%c0, s100, v%c0" ::"i"(R_TB + 2 * HF + 1));
  if constexpr (G == 17) asm volatile("v_xor_b32 v%c0, s100, v%c0" ::"i"(R_TB + 2 * (HF ^ 1)));
  if constexpr (G >= 4 && G <= 8 && G != 7) p_cvt<1, (G < 7 ? G - 4 : 3)>();
  if constexpr (G == 24 || (G == 7 && HF == 0)) asm volatile("s_waitcnt lgkmcnt(8)");
  if constexpr (G == 7 && HF == 1) asm volatile("s_waitcnt vmcnt(%0) lgkmcnt(8)\n\ts_barrier" ::"i"(LD ? 13 : 5) : "memory");
  if constexpr (G == 10) p_store<1, HF ^ 1>();
  if constexpr (G >= 20 && G < 24) p_cvt<0, G - 20>();
  if constexpr (G == 25) p_store<0, HF>();
  if constexpr (LD && p_dma(HF, G) >= 0) dma_at<(HF ? 8 : 12) + p_dma(HF, G)>();
  if constexpr (LD && HF == 0 && G == 2) asm volatile("s_bitset0_b32 m0, 12");
  if constexpr (HF == 0 && G == 13) asm volatile("s_add_u32 s96, s96, 0x8000");
  if constexpr (HF == 0 && G == 26) asm volatile("s_add_u32 s98, s98, 128");
  if constexpr (LD && HF == 0 && G == 27) asm volatile("s_cmp_lt_u32 s98, 0x380");
  if constexpr (HF == 1 && G == 1) asm volatile("s_xor_b32 s100, s100, 0x10000");
  if constexpr (LD && HF == 1 && G == 2) asm volatile("s_xor_b32 s97, s97, s100");
  if constexpr (LD && HF == 1 && G == 9) asm volatile("s_or_b32 m0, s97, 0x1000");
  if constexpr (!LD && HF == 1 && G == 30) asm volatile("s_cmp_lt_u32 s98, 0x500");
  GAP();
}
template <int HF, bool LD, int... Gs> DI void p_half(iseq<Gs...>) { (p_gap<HF, LD, Gs>(), ...); }
template <int G> DI void gapF(AC& c, const EP& p) {
  if constexpr (G < 64) gapB<0, false, G>(c);
  if constexpr (G >= 10) epi_pair<(G - 10) / 4, (G - 10) & 3, 0>(p);
  if constexpr (G >= 14 && ((G - 14) & 3) == 0) epi_store<(G - 14) / 4>();
  if constexpr (G == 73) epi_store<15>();
  GAP();
}
template <int... Gs> DI void phaseF(AC& c, const EP& p, iseq<Gs...>) { (gapF<Gs>(c, p), ...); }
template <int G> DI void gapAL(AC& c) {
  constexpr int kb = G >> 3, s = G & 7, r = 192 + 4 * (G & 15), q = 160 + 4 * s, d = R_S + 36 + 8 * kb, ci = s == 0 ? R_CI + 4 + 8 * kb : d;
  if constexpr (G == 0) asm volatile("s_add_u32 s96, s97, 0x1000000\n\ts_mov_b32 m0, s79\n\ts_waitcnt vmcnt(9) lgkmcnt(0)\n\ts_barrier" ::: "memory");
  if constexpr (G >= 8 && (G & 3) == 0) asm volatile("s_waitcnt lgkmcnt(4)");
  if constexpr (G == 29) asm volatile("s_barrier" ::: "memory");
  asm volatile("v_mfma_f32_16x16x32_bf16 v[%c0:%c1], a[%c2:%c3], a[%c4:%c5], v[%c6:%c7]" ::"i"(d), "i"(d + 3), "i"(r), "i"(r + 3), "i"(q), "i"(q + 3),
               "i"(ci), "i"(ci + 3));
  if constexpr (G < 24) kread<1, G + 8>();
  else vread<0, G - 24>();
  if constexpr (G % 3 == 1 && G < 24) dma_at<G / 3>();
  if constexpr (G == 11) asm volatile("s_bitset1_b32 m0, 12");
  if constexpr (G == 28) asm volatile("buffer_load_dwordx4 v[122:125], v88, s[88:91], s76 offen sc1" ::: "memory");
  if constexpr (G == 30) asm volatile("s_xor_b32 m0, s79, 0x11000\n\ts_movk_i32 s98, 128");
  sm_gap<0, 64 + 2 * G>(c); sm_gap<0, 65 + 2 * G>(c);
  GAP();
}
template <int... Gs> DI void phaseAL(AC& c, iseq<Gs...>) { (gapAL<Gs>(c), ...); }
struct BLS { int ex[65], op[65], vm[65], su[65]; };
constexpr BLS bl_sched(bool LD) {
  BLS z{};
  z.op[0] = 1;
  for (int t = 0; t < 64; ++t) {
    const int g = t & 15, o = z.op[t];
    const bool x = t == 6 || (t < 48 && g > 8 && g != 12 && g != 15);
    const bool v = LD ? (t & 3) == 0 : (t & 11) == 8;
    const bool p = t >= 15 && o < 24 && (t & 3) != 0 && t != 47 && !x && z.ex[t - 1] > 2 * (o / 3) + (o % 3 != 0);
    z.ex[t + 1] = z.ex[t] + x; z.vm[t + 1] = z.vm[t] + v; z.op[t + 1] = o + p;
    z.su[t + 1] = z.su[t] + (t >= 1 && z.su[t] < (LD ? 16 : 10) && g != 15 && !v && !x && (LD || t > 55 || (t > 3 && !p)));
  }
  return z;
}
template <bool LD> constexpr BLS BL = bl_sched(LD);
static_assert(BL<true>.ex[64] == 16 && BL<true>.op[61] == 24 && BL<true>.vm[64] == 16 && BL<false>.vm[64] == 8);
static_assert(BL<true>.su[56] == 16 && BL<false>.su[62] == 10);
template <bool LD, int G> DI void gapBL(AC& c) {
  if constexpr (G == 48) asm volatile("s_waitcnt vmcnt(%0) lgkmcnt(0)\n\ts_barrier" ::"i"(BL<LD>.vm[48]) : "memory");
  gapB<1, false, G>(c);
  if constexpr (G >= 48 && G < 56) vread<1, G - 48>();
  if constexpr (BL<LD>.vm[G + 1] > BL<LD>.vm[G]) {
    constexpr int k = BL<LD>.vm[G] + (LD ? 0 : 16), I = 2 * (k & 7) + ((k >> 3) & 1);
    if constexpr (k < 16) {
      asm volatile("buffer_load_dwordx4 a[%c0:%c1], v84, s[88:91], s75 offen offset:%2 sc1" ::"i"(128 + 4 * I), "i"(131 + 4 * I), "i"((I & 3) * 1024) : "memory");
      if constexpr (k & 1) asm volatile("s_addk_i32 s75, %0" ::"i"(k == 7 ? -12288 : 4096));
    } else dma_at<k - 8>();
    if constexpr (k == 19) asm volatile("s_bitset0_b32 m0, 12");
  }
  if constexpr (G == 1 || G == 2) asm volatile("v_add_f32_e32 v%c0, v%c0, v%c1" ::"i"(R_RS + G - 1), "i"(R_TS + G - 1));
  if constexpr (G == 3) asm volatile("v_mov_b32 v%c0, 0" ::"i"(R_TS));
  if constexpr (BL<LD>.ex[G + 1] > BL<LD>.ex[G]) {
    constexpr int j = BL<LD>.ex[G];
    asm volatile("v_exp_f32 v%c0, v%c1" ::"i"(R_E + j), "i"(R_S + 36 + 8 * (j >> 2) + (j & 3)));
  }
  if constexpr (BL<LD>.op[G + 1] > BL<LD>.op[G]) {
    constexpr int o = BL<LD>.op[G], k = o / 3, j = 2 * k + o % 3;
    if constexpr (o % 3 == 2) asm volatile("v_cvt_pk_bf16_f32 v%c0, v%c1, v%c2" ::"i"(p_reg(1, (k >> 1) * 4 + 2 + (k & 1))), "i"(R_E + 2 * k), "i"(R_E + 2 * k + 1));
    else asm volatile("v_add_f32_e32 v%c0, v%c1, v%c2" ::"i"(R_TS + 1), "i"(j == 1 ? R_E : R_TS + 1), "i"(R_E + j));
  }
  if constexpr (G == 61) asm volatile("v_cmp_nle_f32_e64 s[54:55], v%c0, v%c1" ::"i"(R_TS + 1), "i"(R_THR));
  if constexpr (BL<LD>.su[G + 1] > BL<LD>.su[G]) {
    constexpr int k = BL<LD>.su[G];
    if constexpr (k < 4) asm volatile("v_xor_b32 v%c0, %1, %2" ::"i"(R_TB + k), "i"(32 * k), "v"(c.tb));
    else if constexpr (k < 6) asm volatile("v_add_u32 v%c0, %1, %2" ::"i"(R_VS + k - 4), "i"(4096 * (k - 4)), "v"(c.vs));
    else if constexpr (k == 6) tload<96, T_SLAB>();
    else if constexpr (k == 7) tload<101, T_CNT>();
    else if constexpr (k == 8) asm volatile("v_mov_b64 v[240:241], 1");
    else if constexpr (k == 9) asm volatile("s_mov_b32 s97, s79\n\ts_mov_b32 s100, 0x8000");
    else if constexpr (k < 14) asm volatile("v_mov_b32 v%c0, %1" ::"i"(k < 12 ? 214 + k : 218 + k), "v"(k & 1 ? c.o1 : c.o0));
    else asm volatile("v_mov_b64 v[%c0:%c1], 0" ::"i"(198 + 2 * k), "i"(199 + 2 * k));
  }
  GAP();
}
template <bool LD, int... Gs> DI void phaseBL(AC& c, iseq<Gs...>) { (gapBL<LD, Gs>(c), ...); }
template <int MODE, int G> DI void gapFL(const EP& p) {
  constexpr int x = G - (MODE == 3 ? 4 : 0), J = MODE == 1 ? (G < 24 && G % 3 == 1 ? G / 3 : -1) : G < 27 && G % 5 != 0 && G % 5 != 3 ? G / 5 * 3 + (G % 5) / 2 : -1;
  if constexpr (G == 0 && MODE == 1) asm volatile("s_or_b32 m0, s78, 0x1000\n\ts_movk_i32 s98, 256\n\ts_waitcnt lgkmcnt(0)");
  if constexpr (G == 0 && MODE > 1) asm volatile("s_waitcnt vmcnt(8) lgkmcnt(0)" ::: "memory");
  if constexpr (G == 16 && MODE > 1) asm volatile("s_waitcnt vmcnt(9)" ::: "memory");
  if constexpr (G >= 8 && G < 32 && (G & 3) == 0) asm volatile("s_waitcnt lgkmcnt(%0)" ::"i"(G < 28 ? 4 : 0));
  if constexpr (G < 32) {
    constexpr int o = 8 * (G >> 1) + 4, r = 192 + 4 * (G & 15), q = R_P + 20 + 8 * (G & 1);
    asm volatile("v_mfma_f32_16x16x32_bf16 a[%c0:%c1], a[%c2:%c3], v[%c4:%c5], a[%c0:%c1]" ::"i"(o), "i"(o + 3), "i"(r), "i"(r + 3), "i"(q), "i"(q + 3));
  }
  if constexpr (G < 24) vread<1, G + 8>();
  if constexpr (G == 28) asm volatile("s_waitcnt vmcnt(8)\n\ts_barrier\n\ts_or_b32 m0, s79, 0x1000\n\ts_movk_i32 s98, 384" ::: "memory");
  if constexpr (G == 29 || G == 31) dma_at<8 + (G - 29) / 2>();
  if constexpr (G == 32 || G == 40) {
    static_for([&](auto I) { p_read<0, 2 * (G - 32) + I>(); }, make_iseq<16>{});
    dma_at<10 + (G - 32) / 8>();
  }
  if constexpr (J >= 0) dma_at<8 + (J & 7)>();
  if constexpr (x >= 0) epi_pair<2 * ((x >> 2) & 7) + (x >> 5), x & 3, MODE>(p);
  if constexpr (MODE == 3 && G < 32) {
    constexpr int db = G & 15, qb = G >> 4, o = 4 * (db * 2 + qb), a = 224 + 4 * (db & 1), b = 128 + 4 * ((db >> 1) * 2 + qb);
    asm volatile("v_mfma_f32_16x16x32_bf16 a[%c0:%c1], v[%c2:%c3], a[%c4:%c5], a[%c0:%c1]" ::"i"(o), "i"(o + 3), "i"(a), "i"(a + 3), "i"(b), "i"(b + 3));
  }
  if constexpr (J == 3 || J == 11) asm volatile("s_bitset0_b32 m0, 12");
  if constexpr (J == 7 && MODE > 1) asm volatile("s_or_b32 m0, s78, 0x1000\n\ts_movk_i32 s98, 256");
  if constexpr (x == 63) asm volatile("s_waitcnt vmcnt(4) lgkmcnt(0)" ::: "memory");
  GAP();
}
template <int MODE, int... Gs> DI void phaseFL_(const EP& p, iseq<Gs...>) { (gapFL<MODE, Gs>(p), ...); }
template <int MODE> DI void phaseFL(const EP& p) { GAP(); phaseFL_<MODE>(p, make_iseq<MODE == 3 ? 68 : 64>{}); }
template <int U, int K> DI void r_load(unsigned vo, unsigned so) {
  asm volatile("buffer_load_dwordx4 a[%c0:%c1], %2, s[88:91], %3 offen offset:%4 sc1" ::"i"(64 * U + 4 * K), "i"(64 * U + 4 * K + 3), "v"(vo),
               "s"(so + (K >> 1) * 0x40000 + (U >> 1) * 4096), "i"((K & 1) * 512 + (U & 1) * 2048) : "memory");
}
template <int U, int K> DI void r_sum() {
  constexpr int d = 100 + 4 * (K & 1), b = 64 * U + 4 * K;
  if constexpr (K < 2) asm volatile("v_mfma_f32_16x16x32_bf16 v[%c0:%c1], v[96:99], a[%c2:%c3], 0" ::"i"(d), "i"(d + 3), "i"(b), "i"(b + 3));
  else asm volatile("v_mfma_f32_16x16x32_bf16 v[%c0:%c1], v[96:99], a[%c2:%c3], v[%c0:%c1]" ::"i"(d), "i"(d + 3), "i"(b), "i"(b + 3));
}
template <int U> DI void r_fin(unsigned vo, __amdgpu_buffer_rsrc_t srd, unsigned so) {
  asm volatile("" : "+s"(srd), "+s"(so));
  static_for([&](auto K) { r_sum<U, K>(); }, make_iseq<16>{});
  asm volatile("s_nop 7");
  static_for([&](auto E) { asm volatile("v_cvt_pk_bf16_f32 v%c0, v%c1, v%c2" ::"i"(116 + 4 * U + E), "i"(100 + 2 * E), "i"(101 + 2 * E)); }, make_iseq<4>{});
  asm volatile("buffer_store_dwordx4 v[%c3:%c4], %0, %1, %2 offen sc0 sc1" ::"v"(vo), "s"(srd), "s"(so), "i"(116 + 4 * U), "i"(119 + 4 * U) : "memory");
}

#define CMAX 12
#define BAL 0
#define NYE (BAL ? 16 : 15)
#define NYO (BAL ? 14 : 15)
#define NY0 15
struct TRow { unsigned v[64]; };
constexpr u64 kvmask(int m0, int m1) { u64 n = 0; for (int m = m0; m <= m1; ++m) n |= 0x101ull << m; return n; }
constexpr u64 k_order(bool isY, int rr) {
  const int cls = (rr + (rr >> 2)) & 3;
#define NCM 3
  u64 t = 0;
  int n = 0;
  auto put = [&](int k) { t |= (u64)k << (4 * n++); };
  if (isY) {
    for (int k = 1; k <= 4; ++k) put(NCM + 4 + (k & 3));
    for (int k = 1; k <= 4; ++k) put(NCM + 8 + (k & 3));
    for (int k = NCM + 12; k < 16; ++k) put(k);
    for (int k = NCM + 3; k >= 0; --k) put(k);
    return t;
  }
  for (int k = 0; k < NCM; ++k) put(k);
  for (int k = 0; k < 3; ++k) { put(NCM + 4 * k + cls); put(NCM + ((cls + k + 1) & 3)); }
  for (int k = 1; k < 4; ++k) put(NCM + 4 + ((cls + k) & 3));
  for (int k = 1; k < 4; ++k) put(NCM + 8 + ((cls + k) & 3));
  for (int k = NCM + 12; k < 16; ++k) put(NCM == 2 ? k ^ (cls & 1) : k);
  return t;
}
static_assert(NCM != 2 || (k_order(false, 1) == 0xefadc6982b574310ull && k_order(true, 0) == 0x012345feadcb6987ull));
constexpr TRow t_row(int b, int wid) {
  TRow t{};
  const int j = b & 7, r = b >> 3, rr = r & 15, hsel = rr & 1, pj = rr >> 1, kv = pj & 1, h = j + 8 * hsel, u = hsel * 8 + j;
  const bool isY = r >= 16, noA = pj == 7, slow = BAL && (j & 1);
  const int pa = slow && (pj == 0 || pj == 7) ? 7 - pj : pj;
  const bool solo = pa == 7;
  const int jF = isY || !BAL ? j : slow && pa == 0 ? j - 1 : !slow && pa == 6 ? j + 1 : j, hF = jF + 8 * hsel;
  const int qbF = isY ? (solo ? (slow ? 6 : 7) : 15 - pa) : solo ? 8 : !BAL ? pa : slow ? (pa ? pa - 1 : 0) : pa + 1;
  const int mt = (rr >> 2) + (isY ? 4 : 0), g = j * 128, gF = jF * 128, NY = !(j & 1) ? NYE : pa ? NYO : NY0;
  const int nx0 = solo ? 0 : 32 - 2 * pa - NY, cx = nx0 < CMAX ? nx0 : CMAX, ex = nx0 - cx, qb0 = solo ? qbF : 15 - pa;
  const unsigned oQ = (unsigned)h << 20, oK = oQ + O_K, oP = O_P + (h * 8 + pa) * PSTR, oQF = (unsigned)hF << 20;
  if (wid == 4) {
    for (int l = 0; l < 64; ++l)
      t.v[l] = fo(l < 16 ? g + hsel * 16 + l : l == 16 ? g + 32 + hsel * 8 + (qb0 >> 1) : l == 17 ? gF + 32 + hsel * 8 + (qbF >> 1) :
                  l < 26 ? gF + hsel * 16 + ((l - 18) >> 2) * 8 + ((l - 18) & 3) : l == 26 ? g + 48 + hsel * 8 + pa : l < 32 ? g + 64 + hsel * 8 + pa :
                  l < 48 ? 2304 + pj * 16 + l - 32 : 2048 + pj * 16 + l - 48);
    return t;
  }
  auto put64 = [&](int i, u64 x) { t.v[i] = (unsigned)x; t.v[i + 1] = (unsigned)(x >> 32); };
  t.v[T_Q0] = oQ + (qb0 * 128 + wid * 16) * 512;
  t.v[T_K0] = oK + (isY ? cx : 0) * 32768;
  t.v[T_OQ] = oQ + pj * 131072 + (wid >> 1) * 65536 + (wid & 1) * 4096;
  t.v[T_OG] = oK + kv * TSB + mt * 131072 + (wid >> 1) * 65536 + (wid & 1) * (kv ? 8192 : 4096);
  t.v[T_FPREP] = fo(2048 + pj * 16 + u);
  t.v[T_FQ] = fo(g + 32 + hsel * 8 + pj);
  t.v[T_FG] = fo(g + hsel * 16 + kv * 8 + mt);
  t.v[T_FS] = fo(1024 + qbF * 16 + jF * 2 + hsel);
  t.v[T_CNT] = fo(2560 + qbF * 16);
  t.v[T_ZO] = O_Z + pj * 64 * SEQ * 2 + (u * 4 + wid) * 4096;
  const u64 own = 0x1ull << (kv * 8 + mt), conv = noA ? 0 : 0xffffull << 32;
  put64(T_NPRE, 0x10000ull | (isY ? (1ull << (cx >> 2)) | (1ull << ((cx + 1) >> 2)) : noA ? 0 : 0xffffull << 48));
  put64(T_NST, isY ? kvmask(cx >> 2, 3) | (solo ? 0 : 1ull << 27) : kvmask(0, 0));
  put64(T_TAB, k_order(isY, rr));
  t.v[T_SLAB] = O_S + qbF * 0x200000 + jF * 0x40000 + wid * 8192 + hsel * 1024;
  t.v[T_PART] = oP;
  const unsigned junk = fo(g + 96 + r), fconv = noA ? junk : fo(2304 + pj * 16 + u);
  for (int it = isY || solo; it < 2; ++it) {
    unsigned* p = t.v + T_IT + it * T_ITN;
    const int n = !it ? nx0 : isY && !solo ? NY : 2 * qbF + 2;
    u64 q[4] = {0, 0, 0, 0};
    unsigned jmp[3] = {0, 0, 0}, chk = 0;
    p[14] = junk;
    if (isY) {
      p[0] = 2; p[1] = 2; p[13] = t.v[T_FG];
      if (!solo) {
        chk = 1u << (14 - cx); p[0] |= chk; q[1] = kvmask(4, (31 - 2 * pa) >> 2) & ~own; jmp[1] = ex * 32768;
        chk |= 1u << (n - 2); q[2] = 1ull << 26;
      }
    } else if (it == 0) {
      chk = p[0] = 2u | (ex ? 1u << (cx - 2) : 0) | 1u << (n - 2);
      chk |= noA || qbF > 1 ? 0 : 1u << n;
      q[0] = kvmask(0, (cx - 1) >> 2);
      if (ex) { q[1] = kvmask(4, 4 + ((ex - 1) >> 2)); jmp[1] = (16 - cx) * 32768; }
      jmp[ex ? 2 : 1] = oQF - oQ - (ex ? 16 + ex : cx) * 32768;
      q[ex ? 2 : 1] = 0x20000ull | ((0x22ull << ((2 * qbF + 1) >> 2)) - 0x11ull) << 18;
      q[ex ? 3 : 2] = conv;
      p[1] = 2; p[13] = fconv; p[14] = fo(g + 64 + hsel * 8 + pa);
    } else if (solo) {
      chk = p[0] = 0x402; p[1] = 2; p[13] = fconv;
      q[0] = kvmask(0, 2); q[1] = kvmask(3, 4) | conv;
    } else {
      p[0] = p[1] = n > 4 ? 2 : 1; p[13] = fo(g + 48 + hsel * 8 + pa);
      chk = noA ? 0 : p[0];
      q[0] = conv;
    }
    p[2] = jmp[0]; p[4] = jmp[1]; p[15] = jmp[2];
    p[3] = !it ? 0 : n > 2 ? 1u << (n - 3) : 1u << 30;
    for (int k = 0; k < 4; ++k) { p[5 + 2 * k] = (unsigned)q[k]; p[6 + 2 * k] = (unsigned)(q[k] >> 32); }
    const int rk = (r >> 1) - (BAL && (r >> 1) > (slow ? 7 : 6));
    p[16] = 32 | (isY && !solo ? 2 : 0) | (n == 2 ? 4 : 0) | (isY || noA ? 0 : 8) | (n & 1) << 4 | (n - 1 - it) << 8 | (rk * 4 + wid) << 16 | hF << 24 | qbF << 28;
    p[17] = !it ? 0 : n > 2 ? 1u << (n - 2) : 1u << 31;
    p[18] = it ? oP + wid * 16384 : oQF + (qbF * 128 + wid * 16) * 512;
    p[19] = oP + (it ? 1032 : 1024);
    p[20] = chk >> 1;
    p[21] = (it ? oQF + O_K : oK) + (isY ? cx : 0) * 32768;
    p[22] = ((p[0] | p[3] | p[17]) & 0x3fffffffu) << 1;
  }
  return t;
}
struct TBlk { TRow row[5]; };
template <int B> constexpr TBlk t_blk = {{t_row(B, 0), t_row(B, 1), t_row(B, 2), t_row(B, 3), t_row(B, 4)}};
struct Tab { TBlk blk[256]; };
template <int... I> constexpr Tab t_make(iseq<I...>) { return Tab{{t_blk<I>...}}; }
__device__ const Tab g_tab = t_make(make_iseq<256>{});

template <int R> DI const bf16* sptr() { const bf16* p; asm volatile("s_mov_b64 %0, s[%c1:%c2]" : "=s"(p) : "i"(R), "i"(R + 1)); return p; }
template <class T, int I> DI T* arg_late() {
  unsigned lo, hi;
  asm volatile("v_readlane_b32 %0, v77, %2\n\tv_readlane_b32 %1, v77, %3\n\ts_nop 4" : "=s"(lo), "=s"(hi) : "i"(2 * I), "i"(2 * I + 1));
  return (T*)(((u64)hi << 32) | lo);
}
__global__ __launch_bounds__(256, 1) __attribute__((amdgpu_num_vgpr(77), amdgpu_num_sgpr(56))) void fused_kernel(
    const bf16* ckv_, const bf16* wuk_, const bf16* wuv_, const bf16* x_, const bf16* wq_, bf16* ws_, const bf16* ak_, const bf16* av_,
    const bf16* ash_, const bf16* assmk_, const bf16* assmD_, const bf16* wo_, const bf16* aq_, bf16* out_) {
  extern __shared__ __attribute__((aligned(16))) char lds[];
  const void* kap = (const void*)__builtin_amdgcn_kernarg_segment_ptr();
  asm volatile("at_code_begin:\n\ts_load_dwordx16 s[80:95], %0, 0\n\ts_load_dwordx2 s[96:97], %0, 0x40" ::"s"(kap) : ACLOB_ALL, "v255", "s101");
  asm volatile("s_getpc_b64 s[48:49]\nct_base:\n\ts_mov_b32 s50, pf_code_end-ct_base\n\ts_mov_b32 s51, 0x20000\n\ts_lshr_b32 s52, %1, 3\n\t"
               "v_and_b32 v116, 63, %0\n\tv_lshrrev_b32 v117, 6, %0\n\tv_lshl_add_u32 v116, v116, 7, v117\n\ts_lshl_b32 s52, s52, 9\n\t"
               "v_lshlrev_b32 v116, 7, v116\n\tbuffer_load_dword v117, v116, s[48:51], s52 offen" ::"v"(threadIdx.x), "s"(blockIdx.x) : "scc", "memory");
  GAP();
  const int tid = threadIdx.x, lane = tid & 63;
  const int wid = __builtin_amdgcn_readfirstlane(tid >> 6);
  const int b = blockIdx.x, j = b & 7, r = b >> 3, rr = r & 15, hsel = rr & 1, pj = rr >> 1, kv = pj & 1;
  const bool isY = r >= 16, doP = isY && pj != 7, doA = !isY && pj != 7;
  const int h = j + 8 * hsel, mt = (rr >> 2) + (isY ? 4 : 0), pu = (hsel * 8 + j) * 4 + wid;
  constexpr unsigned m0d = 131072 + 24576;
  const unsigned lA = isY ? 65536 : 0, lW = lA ^ 65536, po = isY ? 0 : 32768;
  unsigned mA = lA + wid * 1024 + po, mW = lW + wid * 1024 + po, ofA = isY ? pj * 256 * DA * 2 : mt * 256 * DC * 2, ofW = h * 512;
  const unsigned lda = isY ? DA : DC;
#define ROT 1
  unsigned g0 = (!ROT ? 0 : isY ? pj + 2 * hsel : rr + (rr >> 2)) & 3;
  {
    asm volatile("global_load_dword v78, %0, %2\n\tglobal_load_dword v79, %1, %2 offset:1024\n\tglobal_load_dword v77, %3, %4"
                 ::"v"(tid * 4), "v"(lane * 4), "s"(&g_tab.blk[b]), "v"((lane < 27 ? lane : 27) * 4), "s"(kap) : "memory");
    const unsigned ra = wid * 8 + (lane >> 3), sa = ((lane & 7) ^ (((wid & 1) * 4 + (lane >> 4)) & 7)) * 16;
    const unsigned vbA = ra * (lda * 2) + sa, vbB = (4 * wid + (lane >> 4)) * LDW * 2 + 16 * ((lane & 15) ^ (((lane >> 4) << 2) | wid));
    vsetu<92>(vbA); vsetu<93>(vbB); vsetu<94>(ra * (DC * 2) + sa);
    sset<65>(128 * lda);
    asm volatile("s_mov_b32 s74, 0x40000\n\ts_mov_b32 s53, 0x80000");
    sset<58>(256 * lda * 2); sset<62>((lda - 1) * LDW * 2 + 512);
    asm volatile("s_mov_b32 s59, 0x20000\n\ts_mov_b32 s63, 0x20000");
    g_pro<1>(g0, mA, mW, lda);
    asm volatile("" : "+s"(ofA), "+s"(ofW), "+s"(mA), "+s"(mW), "+s"(g0));
    GAP();
  }
  asm volatile("s_bitcmp1_b32 %0, 4\n\ts_waitcnt lgkmcnt(0)\n\ts_cselect_b64 s[60:61], s[84:85], s[82:83]\n\ts_bitcmp1_b32 %0, 7\n\t"
               "s_cselect_b64 s[60:61], s[88:89], s[60:61]\n\ts_cselect_b64 s[56:57], s[86:87], s[80:81]\n\t"
               "s_add_u32 s56, s56, %1\n\ts_addc_u32 s57, s57, 0\n\ts_add_u32 s60, s60, %2\n\ts_addc_u32 s61, s61, 0\n\t"
               "s_mov_b64 s[88:89], s[90:91]\n\ts_mov_b32 s90, %3\n\ts_mov_b32 s91, 0x20000" ::"s"(b), "s"(ofA), "s"(ofW), "i"(WSB) : "scc");
  KST(0, 0);
  AC c;
  {
    GM g;
    PR pr;
    const int ptb = hsel * 8 + j;
    if (doP) prep_load(pr, tid, pj, ptb, sptr<92>(), sptr<94>(), sptr<96>());
    g_pro<2>(g0, mA, mW, lda);
    g_pro(g0 + 1, mA, mW, lda);
    sset<64>(64 * lda); sset<67>(256 * lda); sset<68>(320 * lda);
    asm volatile("s_mov_b32 s71, 0x20000\n\ts_movk_i32 s72, 0x100\n\ts_mov_b32 s73, 0x20100");
    asm volatile("buffer_load_dwordx2 v[120:121], off, s[88:91], %0 sc1" ::"s"(fo(6144)) : "memory");
    if (doP) {
      asm volatile("s_waitcnt vmcnt(11)" ::: "memory");
#pragma unroll
      for (int q = 0; q < 7; ++q) asm volatile("" : "+v"(pr.kk[q]));
#pragma unroll
      for (int q = 0; q < 4; ++q) asm volatile("" : "+v"(pr.vv[q]));
      static_for([&](auto Ic) __attribute__((always_inline)) {
        constexpr int I = Ic;
        if constexpr (I < 2) g_pro(g0 + 2 + I, mA, mW, lda);
        if constexpr (I == 2) g_pro2(mA, mW);
        GAP();
        prep_math<I>(pr, tid, lds + 131072);
        GAP();
      }, make_iseq<4>{});
      asm volatile("s_waitcnt lgkmcnt(0)\n\ts_barrier" ::: "memory");
      prep_store(tid, pj, ptb, (bf16*)sptr<88>() + O_X / 2, lds + 131072);
    }
    GAP();
    {
      const int fr = lane & 15, fq = lane >> 4, q = (lane & 15) >> 2, p = lane & 3;
#pragma unroll
      for (int k2 = 0; k2 < 2; ++k2)
        g.aA[k2] = lA + (wid >> 1) * 16384 + (fr >> 3) * 1024 + (fr & 7) * 128 + (((4 * k2 + fq) ^ (fr >> 1)) * 16);
#pragma unroll
      for (int hf = 0; hf < 2; ++hf)
#pragma unroll
        for (int nf = 0; nf < 8; ++nf)
          g.aB[hf][nf] = lW + (wid & 1) * 8192 + off_b(8 * fq + 4 * hf + q, nf * 2 + (p >> 1)) + 8 * (p & 1);
      asm volatile("" : "+v"(g.aA[0]), "+v"(g.aA[1]));
#pragma unroll
      for (int nf = 0; nf < 8; ++nf) asm volatile("" : "+v"(g.aB[0][nf]), "+v"(g.aB[1][nf]));
      const unsigned one = fq != q ? 0u : (p & 1) ? 0x3f800000u : 0x3f80u;
      c.o0 = (p & 2) ? 0u : one; c.o1 = (p & 2) ? one : 0u;
      c.tb = (lane >> 2) * 128 + 16 * ((p >> 1) ^ ((lane >> 2) & 6)) + 8 * (p & 1);
      c.vs = (fr >> 3) * 2048 + (fq & 1) * 512 + (fq >> 1) * 256 + (fr & 7) * 16;
      asm volatile("" : "+v"(c.o0), "+v"(c.o1), "+v"(c.tb), "+v"(c.vs));
      vsetu<81>(tid == 0 ? 0u : 0x80000000u);
      vsetu<84>(lane * 16); vsetu<85>(lane * 16 + 65536); vsetu<86>(wid * 8192 + lane * 16); vsetu<87>(wid * 8192 + lane * 16 + 4096);
      vsetu<88>(65536 + wid * 256 + fr * 16); vsetu<89>(16 * wid + fr - 4 * fq);
      asm volatile("s_mov_b64 s[92:93], s[88:89]\n\ts_mov_b64 s[94:95], s[90:91]\n\ts_mov_b32 s75, 0x60000\n\t"
                   "s_mov_b32 s76, 0x40100\n\ts_mov_b32 s77, 0x60100");
      sset<66>(192 * lda); sset<69>(384 * lda); sset<70>(448 * lda);
      sset<78>(lA + wid * 1024); sset<79>(lW + wid * 1024);
    }
    if (doP) asm volatile("s_waitcnt vmcnt(12)" ::: "memory");
    else {
      g_pro(g0 + 2, mA, mW, lda); g_pro(g0 + 3, mA, mW, lda); g_pro2(mA, mW);
      asm volatile("s_waitcnt vmcnt(8)" ::: "memory");
    }
    asm volatile("s_barrier" ::: "memory");
    if (isY) g_read0<0>(g, make_iseq<16>{});
    else g_read0<1>(g, make_iseq<16>{});
    GAP();
    {
      u64 z = (((u64)__float_as_uint(vget<121>()) << 32) | __float_as_uint(vget<120>())) + 0x9e3779b97f4a7c15ull;
      z = (z ^ (z >> 30)) * 0xbf58476d1ce4e5b9ull; z = (z ^ (z >> 27)) * 0x94d049bb133111ebull; z ^= z >> 31;
      vsetu<82>((unsigned)z); vsetu<83>((unsigned)(z >> 32));
      asm volatile("v_readlane_b32 s80, v78, %0\n\tv_readlane_b32 s81, v78, %1\n\tv_readfirstlane_b32 s84, v82\n\tv_readfirstlane_b32 s85, v83"
                   ::"i"((int)T_TAB), "i"(T_TAB + 1));
      if (isY) asm volatile("s_mov_b64 s[80:81], 0x6543210");
    }
    unsigned k2;
    asm volatile("s_bfe_u32 s82, s80, 0x40008\n\ts_lshr_b64 s[80:81], s[80:81], 12\n\ts_lshl_b32 %0, s82, 1" : "=s"(k2) :: "scc");
    asm volatile("s_waitcnt lgkmcnt(0)\n\ts_barrier" ::: "memory");
    g_proW(k2, mW);
    GAP();
    if (isY) {
      g_tile<false, 0, 1>(g);
      g_loop<false, true>(g, doP ? 0x22u : 0x20u, [&](int t) __attribute__((always_inline)) {
        if (t == 1) fl_set<T_FPREP>();
        else if (t == 4) { tload<80, T_TAB>(); tload<81, T_TAB + 1>(); srd_set<60>((kv ? arg_late<const bf16, 2>() : arg_late<const bf16, 1>()) + h * 256, (DC - 1) * LDW * 2 + 512); }
        else {
          srd_set<56>(arg_late<const bf16, 0>() + (size_t)mt * 256 * DC, 256 * DC * 2);
          asm volatile("v_mov_b32 v92, v94");
          static_for([&](auto J) { asm volatile("s_mov_b32 s%c0, %1" ::"i"(64 + J), "i"((J + 1) * 64 * DC)); }, make_iseq<7>{});
        }
      });
      tload<101, T_OQ>(); sset<83>(__float_as_uint(CS));
      asm volatile("v_mov_b32 v112, v84\n\tv_add_u32 v113, 8192, v84");
      GAP();
      g_tile<false, 0, 2>(g);
      KST(0, 3);
    }
#define TT 3
    auto kvtile = [&](auto TRc) __attribute__((always_inline)) {
      constexpr bool TR = TRc;
      if (isY) g_tile<TR, 1, 4>(g);
      else g_tile<TR, 1, 1>(g);
      g_loop<TR, false>(g, 0x2000u | (isY || doA ? 2u : BAL && (j & 1) ? 0u : 1u << TT), [&](int t) __attribute__((always_inline)) {
        int l = threadIdx.x & 63; asm volatile("" : "+v"(l));
        if (t == 13) {
          asm volatile("s_mov_b32 s79, 0x28000");
          fl_poll<T_NPRE>();
          if ((tget<T_IT + T_ITN + 16>() & 0xf000000) == 0)
            asm volatile("v_readlane_b32 s48, v78, %0\n\tv_and_b32 v122, 0xffffff00, v82\n\tv_mov_b32 v123, v83\n\ts_nop 2\n\t"
                         "buffer_store_dwordx2 v[122:123], v81, s[88:91], s48 offen sc1" ::"i"((int)T_CNT) : "memory");
          if (!doA && !isY) {
            CODE_TOUCH("at_code_begin", "pf_code_end", hsel * 8 + wid * 2, 16u, 64, m0d);
            CODE_TOUCH("at_code_begin", "pf_code_end", hsel * 8 + wid * 2 + 1, 16u, 64, m0d);
          }
          if (doA) {
            const char* tp = arg_late<const char, 9>() + (size_t)(pj * 64 + pu) * (SEQ * 2) + (l & 31) * 128;
            asm volatile("s_mov_b32 m0, %1\n\ts_nop 0\n\tglobal_load_lds_dword %0, off" ::"v"(tp), "i"(m0d) : "memory");
          }
        } else if (isY) fl_set<T_FQ>();
        else if (doA) post_dma(pj, pu, arg_late<const bf16, 12>(), 131072 + wid * 8192);
        else {
          const char* tp = arg_late<const char, 0>() + 1024 * DC * 2 + (pu * 256 + l) * 128;
          asm volatile("s_mov_b32 m0, %0\n\ts_nop 0" ::"i"(m0d));
#pragma unroll
          for (int i = 0; i < (BAL ? 8 : 4); ++i) asm volatile("global_load_lds_dword %0, off" ::"v"(tp + (i & 3) * 8192 + (i >> 2) * 131072) : "memory");
        }
      });
      tload<101, T_OG>(); tload<96, T_Q0>(); tload<98, T_K0>();
      fl_check();
      tload<50, T_NST>(); tload<51, T_NST + 1>();
      asm volatile("v_mov_b32 v112, v84\n\tv_add_u32 v113, %0, v84\n\tv_mov_b32 v90, v87\n\tv_mov_b32 v91, v87\n\tv_mov_b32 v92, v87\n\tv_mov_b32 v93, v87\n\t"
                   "v_mov_b32 v94, v86\n\tv_mov_b32 v95, v86\n\tv_mov_b32 v126, v86\n\tv_mov_b32 v127, v86\n\ts_mov_b32 s99, 0x8000\n\t" FL_ARM("s[50:51]")
                   ::"i"(TR ? 4096 : 8192));
      if (doA) {
        const int d = pj * 64 + pu;
        const bf16* dp = arg_late<const bf16, 10>() + d;
        asm volatile("global_load_ushort v123, %0, off" ::"v"(dp) : "memory");
        srd_set<56>(arg_late<const bf16, 9>() + (size_t)d * SEQ, SEQ * 2);
        srd_set<60>(arg_late<const char, 5>() + O_X + d * SEQ * 2, SEQ * 2);
        sset<78>(65536 + wid * 15488);
        asm volatile("s_movk_i32 s64, 992\n\tv_subrev_u32 v122, 32, v84");
        GAP();
        g_tile<TR, 0, 3>(g);
      } else {
        sset<79>(isY ? wid * 8192 : 0x30000);
        if (tget<T_IT + T_ITN + 16>() & 16) asm volatile("s_add_u32 s98, s98, 0x8000\n\ts_mov_b32 s97, 0xffff8000" ::: "scc");
        else asm volatile("s_mov_b32 s97, 0x8000");
        GAP();
        g_tile<TR, 0, 5>(g);
      }
    };
    if (kv) kvtile(std::true_type{});
    else kvtile(std::false_type{});
  }
  KST(0, 4);
  asm volatile("s_waitcnt vmcnt(16)" ::: "memory");
  const unsigned md0 = tget<T_IT + 16>(), md1 = tget<T_IT + T_ITN + 16>();
  sset<79>(wid * 8192 + (md0 & 16) * 2048);
  if (doA) {
    const float Dd = bfl(__float_as_uint(vget<123>()));
    KST(0, 5);
    conv_wave<T_FG>(O_Z + pj * 64 * SEQ * 2 + ((pu & 7) * 8 + (pu >> 3)) * 64, pu >> 3, Dd, lds + 65536 + wid * 15488);
    const char* tp = arg_late<const char, 11>() + (size_t)h * (HD * DA * 2) + ((pj * 4 + wid) * 64 + lane) * 128;
    asm volatile("s_mov_b32 m0, %1\n\ts_nop 0\n\tglobal_load_lds_dword %0, off\n\ts_waitcnt vmcnt(17)" ::"v"(tp), "i"(m0d) : "memory");
    KST(0, 6);
  } else if (!isY) {
    static_for([&](auto I) { d_store<16 + I>(); }, make_iseq<16>{});
    tload<98, T_K0>();
    asm volatile("s_waitcnt vmcnt(0)\n\ts_barrier" ::: "memory");
    fl_set<T_FG>();
    KST(0, 5);
    fl_check();
    CODE_TOUCH("att_begin", "pf_code_end", hsel * 4 + wid, 8u, 64, m0d);
    dma4k<0>(); dma4k<1>(); dma4k<2>(); dma4k<3>();
    asm volatile("s_waitcnt vmcnt(0)" ::: "memory");
    KST(0, 6);
  } else {
    tload<76, T_PART>();
    fl_check();
    asm volatile("buffer_load_dwordx2 v[122:123], v88, s[88:91], s76 offen offset:1024 sc1" ::: "memory");
  }
  sset<78>(65536 + wid * 8192); sset<79>(98304 + wid * 8192);
  { const bf16* wp = arg_late<const bf16, 11>() + ((md1 >> 24) & 15) * HD * DA; asm volatile("s_mov_b64 s[80:81], %0" ::"s"(wp)); }
  asm volatile("att_begin:");
  ev_code(c);
  asm volatile("s_waitcnt lgkmcnt(0)\n\ts_barrier" ::: "memory");
#pragma unroll 1
  for (int it = md0 ? 0 : 1; it < 2; ++it) {
    const unsigned mode = it ? md1 : md0;
    const bool odd = mode & 16;
    if (odd) kreads<1>(make_iseq<8>{});
    else kreads<0>(make_iseq<8>{});
    const unsigned oP = tget<T_PART>();
    if (it == 0) {
      static_for([&](auto I) { tload<57 + I, T_IT + I>(); }, make_iseq<21>{});
      tload<S_T, T_IT + 21>(); tload<S_EV, T_IT + 22>();
    } else {
      static_for([&](auto I) { tload<57 + I, T_IT + T_ITN + I>(); }, make_iseq<21>{});
      tload<S_T, T_IT + T_ITN + 21>(); tload<S_EV, T_IT + T_ITN + 22>();
    }
    asm volatile("s_add_u32 s98, s97, 0x8000\n\ts_mov_b32 s56, -1\n\tv_mov_b64 v[114:115], 0\n\tv_mov_b64 v[118:119], 0\n\t"
                 "v_mov_b32 v120, 0x45800000" ::: "scc");
    KST(0, 8 + 3 * (it && !isY));
    GAP();
#define STA(k)
#define STI()
#define LOOP_ENDS "s_bfe_u32 s76, s73, 0x80008"
    STA(0);
    if (odd) {
      if (isY) phaseA<1, 2>(c);
      else phaseA<1, 1>(c);
      STA(1);
      STA(2);
      phaseB0<1>(mode, isY);
      STA(3);
      asm volatile(LOOP_ENDS "\n\ts_branch att_mid" ::: "scc");
    } else {
      if (isY) phaseA<0, 2>(c);
      else phaseA<0, 1>(c);
      STA(1);
      if (__builtin_expect(mode & 4, 0)) { asm volatile("s_mov_b32 s56, 30"); ev_call(); asm volatile("s_mov_b32 s56, 31"); ev_call(); acc_zero(); }
      STA(2);
      phaseB0<0>(mode, isY);
      if (__builtin_expect(mode & 4, 0)) { ev_call(); asm volatile("s_branch att_done"); }
      STA(3);
    }
    GAP();
    asm volatile(LOOP_ENDS "\natt_loop:" ::: "scc");
    GAP();
    phaseA<1, 0>(c);
    STI();
    if (__builtin_expect(c.rare != 0, 0)) sm_redo<1>();
    phaseB<1>(c);
    asm volatile("s_cbranch_scc1 att_done\natt_mid:");
    phaseA<0, 0>(c);
    STI();
    if (__builtin_expect(c.rare != 0, 0)) sm_redo<0>();
    phaseB<0>(c);
    asm volatile("s_cbranch_scc0 att_loop\natt_done:");
    GAP();
    STA(4);
    if (it == 0) phaseA<0, 3>(c);
    else {
      sset<76>(oP);
      GAP();
      phaseAL(c, make_iseq<32>{});
      if (__builtin_expect(redo(), 0)) sm_redo<1, false>();
      if (mode & 2) phaseBL<true>(c, make_iseq<64>{});
      else phaseBL<false>(c, make_iseq<64>{});
    }
    if (__builtin_expect(redo(), 0)) {
      if (it) static_for([&](auto I) { vset<R_S + 32 + (I >> 2) * 8 + (I & 3)>(-INFINITY); }, make_iseq<16>{});
      asm volatile("s_nop 7");
      sm_redo<0, false>();
    }
    STA(5);
    KST(0, 9 + 3 * (it && !isY));
    float l[2];
    asm volatile("v_add_f32 %0, v%c2, v%c3\n\tv_add_f32 %1, v%c4, v%c5" : "=v"(l[0]), "=v"(l[1]) : "i"(R_RS), "i"(R_TS), "i"(R_RS + 1), "i"(R_TS + 1));
    const float cm[2] = {vget<R_M>(), vget<R_M + 1>()};
    l[0] = row_reduce<false>(l[0]); l[1] = row_reduce<false>(l[1]);
    EP p;
    if (it == 0) {
      const f32x4 ml = {cm[0], l[0], cm[1], l[1]};
      asm volatile("buffer_store_dwordx4 %0, v88, s[88:91], %1 offen sc1\n\ts_mov_b32 s101, %2" ::"v"(ml), "s"(oP), "s"(oP + wid * 16384) : "memory");
      phaseF(c, p, make_iseq<74>{});
      asm volatile(FL_POLL "\n\ts_waitcnt vmcnt(17)" ::: "memory");
    } else {
      if (!(mode & 2)) {
        p.fa[0] = __builtin_amdgcn_rcpf(l[0]); p.fa[1] = __builtin_amdgcn_rcpf(l[1]);
        phaseFL<1>(p);
      } else {
        const f32x4 ml = {vget<122>(), vget<123>(), vget<124>(), vget<125>()};
        if (__builtin_expect(__builtin_amdgcn_ballot_w64(ml[0] != cm[0] || ml[2] != cm[1]) == 0, 1)) {
          p.fa[0] = __builtin_amdgcn_rcpf(l[0] + ml[1]); p.fa[1] = __builtin_amdgcn_rcpf(l[1] + ml[3]);
          phaseFL<3>(p);
        } else {
#pragma unroll
          for (int e = 0; e < 2; ++e) {
            const float mm = fmaxf(cm[e], ml[2 * e]);
            const float wa = __builtin_amdgcn_exp2f(cm[e] - mm), wb = __builtin_amdgcn_exp2f(ml[2 * e] - mm);
            const float inv = __builtin_amdgcn_rcpf(l[e] * wa + ml[2 * e + 1] * wb);
            p.fa[e] = wa * inv; p.fb[e] = wb * inv;
          }
          phaseFL<2>(p);
        }
      }
    }
    KST(0, 10 + 3 * (it && !isY));
  }
  asm volatile(TICKET "\n\tv_xor_b32 v%c0, s100, v%c0\np_loop:" ::"i"(R_TB) : "memory");
  GAP();
  p_half<0, true>(make_iseq<32>{});
  asm volatile("s_cbranch_scc0 p_mid");
  p_half<1, true>(make_iseq<32>{});
  asm volatile("s_branch p_loop\np_loop2:");
  p_half<0, false>(make_iseq<32>{});
  asm volatile("p_mid:");
  p_half<1, false>(make_iseq<32>{});
  asm volatile("s_cbranch_scc1 p_loop2\n\ts_nop 5");
  GAP();
  p_cvt<1, 0>(); p_cvt<1, 1>(); p_cvt<1, 2>(); p_cvt<1, 3>();
  p_store<1, 1>();
  GAP();
  KST(0, 27);
  {
    constexpr unsigned rdy = 98304;
    int lz = threadIdx.x & 63; asm volatile("" : "+v"(lz));
    const int fr = lz & 15, fq = lz >> 4, qbF = md1 >> 28;
    if (wid == 0) {
#pragma unroll 1
      for (int q = 0; q < 0x100000; ++q) {
        unsigned lo, hi, tl, th;
        asm volatile("v_readfirstlane_b32 %0, v240\n\tv_readfirstlane_b32 %1, v241\n\ts_mov_b32 %2, s84\n\ts_mov_b32 %3, s85" : "=s"(lo), "=s"(hi), "=s"(tl), "=s"(th));
        if (__builtin_expect((((lo ^ tl) >> 8) | (hi ^ th)) == 0, 1)) break;
        asm volatile("s_sleep 4\n\tv_mov_b64 v[240:241], 1\n\t" TICKET "\n\ts_waitcnt vmcnt(0)" ::: "memory");
      }
      asm volatile("v_mov_b32 v241, 0\n\ts_mov_b64 exec, 1\n\tds_write_b64 %0, v[240:241]\n\ts_mov_b64 exec, -1" ::"v"(rdy) : "memory");
    }
    vsetu<79>(fo(1024) + qbF * 4352 + fr * 8);
    {
      const int jj = 4 * (fr >> 3) + (fr & 3);
      const unsigned one = ((fq ^ (fr >> 2)) & 1) ? 0u : (jj & 1) ? 0x3f800000u : 0x3f80u;
      static_for([&](auto I) { vsetu<96 + I>((jj >> 1) == I ? one : 0u); }, make_iseq<4>{});
    }
    unsigned vo = (fq >> 1) * 1024 + (fq & 1) * 256 + fr * 16;
    bf16* out = arg_late<bf16, 13>();
    __amdgpu_buffer_rsrc_t so_ = __builtin_amdgcn_make_buffer_rsrc((void*)out, (short)0, SEQ * OUTW * 2, 0x00020000);
    unsigned vw = ((qbF * 128 + (fr & 7)) * OUTW + DA + (fr >> 3) * 32 + fq * 8) * 2;
    asm volatile("" : "+s"(so_));
    asm volatile("s_waitcnt vmcnt(0) lgkmcnt(0)\n\ts_barrier" : "+v"(vo), "+v"(vw) :: "memory");
    fl_set<T_FS>();
    unsigned tk;
    asm volatile("ds_read_b32 %0, %1\n\ts_waitcnt lgkmcnt(0)" : "=v"(tk) : "v"(rdy) : "memory");
    tk = __builtin_amdgcn_readfirstlane(tk) & 255;
    KST(0, 30);
    if (doA) post_math(pj, pu, out, lds + 131072 + wid * 8192);
#define N3 0
#define N2 12
    constexpr int N1 = 28 - 3 * N3 - 2 * N2;
    static_assert(N1 >= 0 && N3 + N2 + N1 <= 16);
    const int nu = tk < N3 ? 3 : tk < N3 + N2 ? 2 : tk < N3 + N2 + N1 ? 1 : 0;
    const int u0 = 4 * (tk < N3 ? 3 * tk : tk < N3 + N2 ? N3 + 2 * tk : 2 * N3 + N2 + tk) + wid * nu;
    KST(0, 28);
    if (nu) {
      const unsigned so0 = O_S + qbF * 0x200000 + (16 + u0) * 2048, so[3] = {so0, so0, so0};
      unsigned pend = 0x5555u, cnt = 0x400000;
      while (true) {
        unsigned nw;
        if (wid == 3) {
          u64 m;
          const u64 ex = pend | (pend << 1);
          asm volatile(FL_ARM("%1") "\n\t" FL_POLL "\n\ts_waitcnt vmcnt(0)\n\tv_cmp_eq_u64 %0, s[84:85], v[124:125]" : "=s"(m) : "s"(ex) : "memory");
          nw = (unsigned)m & ((unsigned)m >> 1) & pend;
          if (nw) asm volatile("ds_write_b32 %0, %1 offset:4" ::"v"(rdy), "v"(~pend | nw) : "memory");
        } else {
          unsigned t;
          asm volatile("ds_read_b32 %0, %1 offset:4\n\ts_waitcnt lgkmcnt(0)" : "=v"(t) : "v"(rdy) : "memory");
          nw = __builtin_amdgcn_readfirstlane(t) & pend;
        }
        if (--cnt == 0) nw = pend;
        if (nw == 0) {
          if (wid == 3) asm volatile("s_sleep 4");
          else asm volatile("s_sleep 1");
          continue;
        }
        asm volatile("s_nop 4");
        static_for([&](auto S) {
          if ((nw >> (2 * S)) & 1) {

            r_load<0, 2 * S>(vo, so[0]); r_load<0, 2 * S + 1>(vo, so[0]);
            if (nu > 1) { r_load<1, 2 * S>(vo, so[1]); r_load<1, 2 * S + 1>(vo, so[1]); }
            if (nu > 2) { r_load<2, 2 * S>(vo, so[2]); r_load<2, 2 * S + 1>(vo, so[2]); }
          }
        }, make_iseq<8>{});
        pend &= ~nw;
        if (pend == 0) break;
      }
      asm volatile("s_waitcnt vmcnt(0)" ::: "memory");
      KST(0, 16);
      auto oo = [&](int U) { return ((((U >> 1) & 1) * 64 + ((U >> 2) & 3) * 16 + (U & 1) * 8) * OUTW + (U >> 4) * 64) * 2; };
      r_fin<0>(vw, so_, oo(u0));
      if (nu > 1) r_fin<1>(vw, so_, oo(u0 + 1));
      if (nu > 2) r_fin<2>(vw, so_, oo(u0 + 2));
      if (qbF == 15 && tk == 0) asm volatile(FL_SET("%0") ::"s"(fo(6144)) : "memory");
    }
    KST(0, 29);
  }
  asm volatile("s_waitcnt vmcnt(0)" ::: "memory");
  asm volatile("pf_code_end:");
  KST(0, 1);
}

extern "C" void kernel_launch(void* const* d_in, const int* in_sizes, int n_in, void* d_out, int out_size, void* d_ws,
                              size_t ws_size, hipStream_t stream) {
  const bf16* Aq = (const bf16*)d_in[0];
  const bf16* Ak = (const bf16*)d_in[1];
  const bf16* Av = (const bf16*)d_in[2];
  const bf16* Ash = (const bf16*)d_in[3];
  const bf16* Assmk = (const bf16*)d_in[4];
  const bf16* AssmD = (const bf16*)d_in[5];
  const bf16* Bx = (const bf16*)d_in[6];
  const bf16* Bckv = (const bf16*)d_in[7];
  const bf16* Bwq = (const bf16*)d_in[8];
  const bf16* Bwuk = (const bf16*)d_in[9];
  const bf16* Bwuv = (const bf16*)d_in[10];
  const bf16* Bwo = (const bf16*)d_in[11];
  bf16* out = (bf16*)d_out;
  hipFuncSetAttribute((const void*)fused_kernel, hipFuncAttributeMaxDynamicSharedMemorySize, 163840);
  fused_kernel<<<256, 256, 163840, stream>>>(Bckv, Bwuk, Bwuv, Bx, Bwq, (bf16*)d_ws, Ak, Av, Ash, Assmk, AssmD, Bwo, Aq, out);
}
```

```cpp
#include <hip/hip_runtime.h>
#include <hip/hip_bf16.h>
#include <stdint.h>
#include <utility>

typedef __hip_bfloat16 bf16;
typedef __attribute__((ext_vector_type(4))) short bf16x4;
typedef __attribute__((ext_vector_type(2))) unsigned u32x2;

constexpr int SEQ = 2048;
constexpr int DA = 448;
constexpr int NH = 16;
constexpr int HD = 256;
constexpr int DC = 1024;
constexpr int OUTW = 896;
constexpr unsigned TSB = NH * SEQ * HD * 2, O_K = TSB, O_S = 3 * TSB, O_P = 5 * TSB, PSTR = 67584, O_F = O_P + 128 * PSTR,
                   O_X = O_F + 400 * 4352, O_Z = O_X + DA * SEQ * 2, WSB = O_Z + DA * SEQ * 2;
constexpr unsigned fo(int w) { return O_F + (w >> 4) * 4352 + (w & 15) * 8; }

#define KST(kid, which)

__device__ __forceinline__ float bf2f(unsigned short u) { return __uint_as_float(((unsigned)u) << 16); }
__device__ __forceinline__ unsigned short f2bf(float f) { return __bfloat16_as_ushort(__float2bfloat16(f)); }
__device__ __forceinline__ float rbf(float f) { return bf2f(f2bf(f)); }
typedef __attribute__((ext_vector_type(2))) __bf16 bf16x2n;
typedef __attribute__((ext_vector_type(2))) float f32x2n;
__device__ __forceinline__ unsigned pk2(float a, float b) {
  return __builtin_bit_cast(unsigned, __builtin_convertvector((f32x2n){a, b}, bf16x2n));
}

__device__ __forceinline__ int off_b(int row, int ch) { return 256 * row + 16 * (ch ^ (((row & 3) << 2) | ((row >> 2) & 3))); }

template <int R> __device__ __forceinline__ void sset(unsigned x) { asm volatile("s_mov_b32 s%c0, %1" ::"i"(R), "s"(x)); }
template <int R> __device__ __forceinline__ void vsetu(unsigned x) { asm volatile("v_mov_b32 v%c0, %1" ::"i"(R), "v"(x)); }
template <int R> __device__ __forceinline__ void srd_set(const void* p, unsigned n) {
  asm volatile("s_mov_b64 s[%c0:%c1], %4\n\ts_mov_b32 s%c2, %5\n\ts_mov_b32 s%c3, 0x20000" ::"i"(R), "i"(R + 1), "i"(R + 2), "i"(R + 3), "s"(p), "s"(n));
}
template <int R, int L> __device__ __forceinline__ void tload() { asm volatile("v_readlane_b32 s%c0, v78, %1" ::"i"(R), "i"(L)); }
template <int L> __device__ __forceinline__ unsigned tget() { unsigned t; asm volatile("v_readlane_b32 %0, v78, %1" : "=s"(t) : "i"(L)); return t; }
#define FL_SET(S) "buffer_store_dwordx2 v[82:83], v81, s[88:91], " S " offen sc1"
#define FL_ARM(N) "v_cndmask_b32_e64 v80, -2.0, v79, " N
#define FL_POLL "buffer_load_dwordx2 v[124:125], v80, s[88:91], 0 offen sc1"
#define FL_CHECK(N) "s_mov_b32 s48, 0x100000\n7:\n\tv_cmp_eq_u64 vcc, s[84:85], v[124:125]\n\ts_and_b64 vcc, vcc, " N "\n\ts_cmp_eq_u64 vcc, " N \
                    "\n\ts_cbranch_scc1 8f\n\ts_sleep 2\n\t" FL_POLL "\n\ts_waitcnt vmcnt(0)\n\ts_sub_u32 s48, s48, 1\n\ts_cbranch_scc0 7b\n8:"
template <int L> __device__ __forceinline__ void fl_set() {
  asm volatile("v_readlane_b32 s48, v78, %0\n\ts_nop 4\n\t" FL_SET("s48") ::"i"(L) : "memory");
}
template <int L> __device__ __forceinline__ void fl_poll() {
  asm volatile("v_readlane_b32 s50, v78, %0\n\tv_readlane_b32 s51, v78, %1\n\ts_nop 1\n\t" FL_ARM("s[50:51]") "\n\t" FL_POLL ::"i"(L), "i"(L + 1) : "memory");
}
#define TICKET "buffer_atomic_add_x2 v[240:241], v81, s[88:91], s101 offen sc0"
__device__ __forceinline__ void fl_check() { asm volatile(FL_CHECK("s[50:51]") ::: "vcc", "scc", "memory"); }
template <int R, int K> __device__ __forceinline__ unsigned vxor() { unsigned t; asm volatile("v_xor_b32 %0, %1, v%c2" : "=v"(t) : "i"(K), "i"(R)); return t; }
template <int R> __device__ __forceinline__ float vget() { float t; asm volatile("v_mov_b32 %0, v%c1" : "=v"(t) : "i"(R)); return t; }
template <int R> __device__ __forceinline__ void vset(float x) { asm volatile("v_mov_b32 v%c0, %1" ::"i"(R), "v"(x)); }
template <int R> __device__ __forceinline__ float acc_read1() { float t; asm volatile("v_accvgpr_read_b32 %0, a[%c1]" : "=v"(t) : "i"(R)); return t; }
template <class F, int... I> __device__ __forceinline__ void static_for(F&& f, std::integer_sequence<int, I...>) {
  (f(std::integral_constant<int, I>{}), ...);
}
template <int... Is> using iseq = std::integer_sequence<int, Is...>;
template <int N> using make_iseq = std::make_integer_sequence<int, N>;

typedef __attribute__((ext_vector_type(4))) unsigned u32x4c;
typedef __attribute__((ext_vector_type(4))) float f32x4c;
typedef __attribute__((ext_vector_type(2))) float f32x2c;
typedef unsigned long long u64;
struct PR { u32x4c kk[7], vv[4]; };
__device__ __forceinline__ void prep_load(PR& p, int tid, int cg, int tb, const bf16* __restrict__ k, const bf16* __restrict__ v,
                                          const bf16* __restrict__ shift_k) {
  const __amdgpu_buffer_rsrc_t sk = __builtin_amdgcn_make_buffer_rsrc((void*)k, (short)0, SEQ * DA * 2, 0x00020000);
  const __amdgpu_buffer_rsrc_t sv = __builtin_amdgcn_make_buffer_rsrc((void*)v, (short)0, SEQ * DA * 2, 0x00020000);
  const __amdgpu_buffer_rsrc_t sw = __builtin_amdgcn_make_buffer_rsrc((void*)(shift_k + cg * 256), (short)0, 512, 0x00020000);
  const int vo = ((tb * 128 + (tid >> 3) * 4) * DA + cg * 64 + (tid & 7) * 8) * 2;
#pragma unroll
  for (int q = 0; q < 7; ++q)
    asm volatile("buffer_load_dwordx4 %0, %1, %2, 0 offen" : "=v"(p.kk[q]) : "v"(vo + (q - 3) * DA * 2), "s"(sk) : "memory");
#pragma unroll
  for (int q = 0; q < 4; ++q)
    asm volatile("buffer_load_dwordx4 %0, %1, %2, 0 offen offset:%3" : "=v"(p.vv[q]) : "v"(vo), "s"(sv), "i"(q * DA * 2) : "memory");
#pragma unroll
  for (int q = 0; q < 4; ++q)
    asm volatile("buffer_load_dwordx4 a[%c0:%c1], %2, %3, 0 offen offset:%4" ::"i"(4 * q), "i"(4 * q + 3), "v"((tid & 7) * 64), "s"(sw), "i"(q * 16) : "memory");
}
__device__ __forceinline__ float bfl(unsigned u) { return __uint_as_float(u << 16); }
__device__ __forceinline__ float bfh(unsigned u) { return __uint_as_float(u & 0xffff0000u); }
__device__ __forceinline__ f32x2c rbf2(f32x2c a) {
  return (f32x2c){__uint_as_float(pk2(0.f, a[0])), __uint_as_float(pk2(0.f, a[1]))};
}
template <int I> __device__ __forceinline__ void prep_math(const PR& p, int tid, char* lx) {
  const int rp = tid >> 3, c = tid & 7;
  f32x2c wt[4], kf[7];
  const unsigned w[4] = {__float_as_uint(acc_read1<4 * I>()), __float_as_uint(acc_read1<4 * I + 1>()), __float_as_uint(acc_read1<4 * I + 2>()),
                         __float_as_uint(acc_read1<4 * I + 3>())};
#pragma unroll
  for (int t = 0; t < 4; ++t)
    wt[t] = (t & 1) ? (f32x2c){bfh(w[t >> 1]), bfh(w[2 + (t >> 1)])} : (f32x2c){bfl(w[t >> 1]), bfl(w[2 + (t >> 1)])};
#pragma unroll
  for (int q = 0; q < 7; ++q) kf[q] = (f32x2c){bfl(p.kk[q][I]), bfh(p.kk[q][I])};
  f32x2c x[4];
#pragma unroll
  for (int e = 0; e < 4; ++e) {
    f32x2c s = rbf2(kf[e] * wt[3]);
    s = rbf2(s + rbf2(kf[e + 1] * wt[2]));
    s = rbf2(s + rbf2(kf[e + 2] * wt[1]));
    s = rbf2(s + rbf2(kf[e + 3] * wt[0]));
    x[e] = s * (f32x2c){bfl(p.vv[e][I]), bfh(p.vv[e][I])};
  }
#pragma unroll
  for (int u = 0; u < 2; ++u)
    *(u32x2*)(lx + (c * 8 + 2 * I + u) * 256 + (((31 - rp) * 8) ^ (c << 4))) = (u32x2){pk2(x[3][u], x[2][u]), pk2(x[1][u], x[0][u])};
}
__device__ __forceinline__ void prep_store(int tid, int cg, int tb, bf16* __restrict__ xr, const char* lx) {
  const __amdgpu_buffer_rsrc_t sx = __builtin_amdgcn_make_buffer_rsrc((void*)(xr + (size_t)cg * 64 * SEQ), (short)0, 64 * SEQ * 2, 0x00020000);
#pragma unroll
  for (int rd = 0; rd < 4; ++rd) {
    const int idx = tid + 256 * rd, ch = idx >> 4, q = idx & 15;
    u32x4c w = *(const u32x4c*)(lx + ch * 256 + 16 * (q ^ (ch >> 3)));
    __builtin_amdgcn_raw_buffer_store_b128(w, sx, (ch * SEQ + (15 - tb) * 128 + q * 8) * 2, 0, 16);
  }
}

#define CODE_TOUCH(LB, LE, wx, NW, NL, m0v) do { unsigned blo_, bhi_, elo_; \
    asm volatile("s_getpc_b64 s[48:49]\n\ts_add_u32 s48, s48, " LB "@rel32@lo+4\n\ts_addc_u32 s49, s49, " LB "@rel32@hi+12\n\t" \
                 "s_mov_b32 %0, s48\n\ts_mov_b32 %1, s49\n\t" \
                 "s_getpc_b64 s[48:49]\n\ts_add_u32 s48, s48, " LE "@rel32@lo+4\n\ts_mov_b32 %2, s48" \
                 : "=s"(blo_), "=s"(bhi_), "=s"(elo_) :: "scc"); \
    const unsigned n_ = elo_ - (blo_ & ~127u); \
    const __amdgpu_buffer_rsrc_t srd_ = __builtin_amdgcn_make_buffer_rsrc((void*)(((unsigned long long)bhi_ << 32) | (blo_ & ~127u)), (short)0, n_ < 262144u ? n_ : 0u, 0x00020000); \
    const unsigned l_ = threadIdx.x & 63, vo_ = l_ < (NL) ? ((wx) + l_ * (NW)) * 128u : 0x80000000u; \
    asm volatile("s_mov_b32 m0, %0\n\ts_nop 4\n\tbuffer_load_dword %1, %2, 0 offen lds" ::"s"(m0v), "v"(vo_), "s"(srd_) : "memory"); } while (0)
template <int I> __device__ __forceinline__ void d_store() {
  asm volatile("buffer_store_dwordx4 v[%c0:%c1], v%c2, s[88:91], s101 offen offset:%3 sc1" ::"i"(128 + 4 * I), "i"(131 + 4 * I), "i"(112 + ((I >> 2) & 1)),
               "i"((I & 3) * 1024) : "memory");
  if constexpr ((I & 7) == 7) asm volatile("s_addk_i32 s101, 0x4000" ::: "scc");
}
constexpr int NS_X = 19;
struct CV { unsigned pa[4], pb, px, vz, sz; float D; };
constexpr int cv_J(bool up, int q) { int J = up ? 0 : 7; while (q > J) { q -= J + 1; J += up ? 1 : -1; } return J; }
constexpr int cv_i(bool up, int q) { int J = up ? 0 : 7; while (q > J) { q -= J + 1; J += up ? 1 : -1; } return q; }
template <int R, int N> __device__ __forceinline__ void cv_rd(CV& c) {
  if constexpr (N < 16) {
    constexpr int i = N >> 1, o = 16 * R + 128 * (i & 1) + 2 * (N & 1), r = 32 + 32 * (R & 1) + 2 * N;
    asm volatile("ds_read2_b32 a[%c1:%c2], %0 offset0:%c3 offset1:%c4" ::"v"(c.pa[i >> 1]), "i"(r), "i"(r + 1), "i"(o), "i"(o + 1));
  } else {
    constexpr int m = N - 16, r = 192 + 32 * (R & 1) + 4 * m;
    asm volatile("ds_read_b128 a[%c1:%c2], %0 offset:%c3" ::"v"(c.pb), "i"(r), "i"(r + 3), "i"(64 * R + 512 * (7 - m)));
  }
}
template <int O> __device__ __forceinline__ void cv_xd(CV& c) {
  constexpr int e = O & 31, x = 96 + ((e >> 1) ^ 1);
  if constexpr (O >= 64) asm volatile("v_cvt_pk_bf16_f32 v%c0, 0, v%c0" ::"i"(160 + e));
  else if constexpr (O >= 32) asm volatile("v_mul_f32 v%c0, v%c0, %1" ::"i"(160 + e), "v"(c.D));
  else if constexpr (O & 1) asm volatile("v_lshlrev_b32 v%c0, 16, v%c1" ::"i"(160 + e), "i"(x));
  else asm volatile("v_and_b32 v%c0, 0xffff0000, v%c1" ::"i"(160 + e), "i"(x));
}
template <int O> __device__ __forceinline__ void cv_z(CV& c) {
  constexpr int J = O / 11, k = O % 11, y = 128 + 4 * J;
  if constexpr (k < 4) asm volatile("v_cvt_pk_bf16_f32 v%c0, 0, v%c0" ::"i"(y + k));
  else if constexpr (k < 8) asm volatile("v_add_f32 v%c0, v%c0, v%c1" ::"i"(y + k - 4), "i"(y + 28 + k));
  else if constexpr (k < 10) asm volatile("v_cvt_pk_bf16_f32 v%c0, v%c1, v%c2" ::"i"(y + k - 8), "i"(y + 2 * k - 16), "i"(y + 2 * k - 15));
  else asm volatile("buffer_store_dwordx2 v[%c0:%c1], %2, s[88:91], s101 offen sc1\n\ts_add_u32 s101, s101, 0x8000" ::"i"(y), "i"(y + 1), "v"(c.vz) : "memory");
}
constexpr int cv_z0(int Q) {
  int o = 0;
  for (int q = 0; q < Q; ++q)
    for (int k = 0; k < (q % 4 == 1 ? 0 : 3); ++k) { const int J = o / 11; if ((J + 1) * (J + 2) / 2 - 1 + (J ? 4 : 7) <= q) ++o; }
  return o;
}
constexpr int cv_xd0(int T) { int o = 0; for (int t = 0; t < T; ++t) o += t % 4 != 1 || (t >= 72 && t < 108); return o < 96 ? o : 96; }
static_assert(cv_xd0(144) == 96);
template <int B, int... Is> __device__ __forceinline__ void cv_zs(CV& c, iseq<Is...>) { (cv_z<B + Is>(c), ...); }
template <int R, int Q> __device__ __forceinline__ void cv_slot(CV& c) {
  constexpr int i = cv_i(R == 7, Q), J = cv_J(R == 7, Q), d = 128 + 4 * J, a = 32 + 32 * (R & 1) + 4 * i, b = 192 + 32 * (R & 1) + 4 * (J - i);
  constexpr int T = 36 * R + Q, vm = Q % 4 == 1 && Q < 32 ? Q / 4 : -1;
  if constexpr (Q == 0) asm volatile("s_waitcnt lgkmcnt(0)");
  if constexpr (R >= 6 && vm >= 0 && vm % 4 == 0) asm volatile("s_add_u32 m0, s79, %0" ::"i"(vm * 1024));
  if constexpr (R == 0 && i == 0)
    asm volatile("v_mfma_f32_16x16x32_bf16 v[%c0:%c1], a[%c2:%c3], a[%c4:%c5], 0" ::"i"(d), "i"(d + 3), "i"(a), "i"(a + 3), "i"(b), "i"(b + 3));
  else
    asm volatile("v_mfma_f32_16x16x32_bf16 v[%c0:%c1], a[%c2:%c3], a[%c4:%c5], v[%c0:%c1]" ::"i"(d), "i"(d + 3), "i"(a), "i"(a + 3), "i"(b), "i"(b + 3));
  if constexpr (Q < 24 && R < 7) cv_rd<R + 1, Q>(c);
  if constexpr (T % 7 == 3 && NS_X + 5 + T / 7 < 32) d_store<NS_X + 5 + T / 7>();
  if constexpr (R == 2 && Q >= 24 && Q < 32)
    asm volatile("ds_read_b64 v[%c1:%c2], %0 offset:%c3" ::"v"(c.px), "i"(96 + 2 * (Q - 24)), "i"(97 + 2 * (Q - 24)), "i"(512 * (31 - Q)));
  if constexpr (R >= 3 && R < 7) if constexpr (cv_xd0(T - 107) > cv_xd0(T - 108)) cv_xd<cv_xd0(T - 108)>(c);
  if constexpr ((R == 3 || R == 4) && vm >= 0) {
    constexpr int f = (R - 3) * 8 + vm;
    asm volatile("buffer_load_dwordx4 a[%c0:%c1], v84, s[88:91], s96 offen offset:%2 sc1" ::"i"(128 + 4 * f), "i"(131 + 4 * f), "i"((f & 3) * 1024) : "memory");
    if constexpr ((f & 3) == 3) asm volatile("s_addk_i32 s96, %0" ::"i"(f == 7 ? 28672 : 4096));
  }
  if constexpr (R == 4 && Q == 35) asm volatile(FL_POLL ::: "memory");
  if constexpr (R >= 6 && vm >= 0)
    asm volatile("buffer_load_dwordx4 v%c0, s[88:91], s98 offen offset:%1 sc1 lds" ::"i"(86 + vm / 4), "i"((vm & 3) * 1024) : "memory");
  if constexpr (R == 6 && Q == 35) asm volatile("s_add_u32 s98, s98, 0x8000\n\ts_xor_b32 s79, s79, 0x8000");
  if constexpr (R == 7) cv_zs<cv_z0(Q)>(c, make_iseq<cv_z0(Q + 1) - cv_z0(Q)>{});
  __builtin_amdgcn_sched_barrier(0);
}
template <int R, int... Q> __device__ __forceinline__ void cv_step(CV& c, iseq<Q...>) { (cv_slot<R, Q>(c), ...); }
template <int... N> __device__ __forceinline__ void cv_rd0(CV& c, iseq<N...>) { (cv_rd<0, N>(c), ...); }
template <int FLAG> __device__ __forceinline__ void conv_wave(unsigned sz, int ck, float Dd, char* lw) {
  int lane = threadIdx.x & 63; asm volatile("" : "+v"(lane));
  const int l16 = lane & 15, fq = lane >> 4;
  u32x4c w[5]; unsigned w4[5];
#pragma unroll
  for (int r = 0; r < 5; ++r) {
    w[r] = *(const u32x4c*)(lw + r * 1024 + lane * 16);
    w4[r] = *(const unsigned*)(lw + r * 1024 + lane * 16 + 16);
  }
  d_store<NS_X>(); d_store<NS_X + 1>();
#pragma unroll
  for (int r = 0; r < 5; ++r)
    *(u32x4c*)(lw + 10304 + r * 1024 + lane * 16) = (u32x4c){__builtin_amdgcn_alignbit(w[r][1], w[r][0], 16), __builtin_amdgcn_alignbit(w[r][2], w[r][1], 16),
                                                             __builtin_amdgcn_alignbit(w[r][3], w[r][2], 16), __builtin_amdgcn_alignbit(w4[r], w[r][3], 16)};
  d_store<NS_X + 2>();
  CV c;
#pragma unroll
  for (int i = 0; i < 4; ++i) c.pa[i] = (unsigned)(size_t)lw + ((l16 + 1) & 1) * 10304 + 4 * ((8 * fq + l16 + 1) >> 1) + 1024 * i;
  c.pb = (unsigned)(size_t)lw + 5120 + (2032 - 16 * l16 + 8 * fq) * 2 - 512 * 7;
  c.vz = (l16 >> 1) * 4096 + (((l16 & 1) * 2 + (fq >> 1)) ^ (ck >> 1)) * 16 + (fq & 1) * 8;
  c.px = (unsigned)(size_t)lw + 5120 + 2044 * 2 - 512 * 7 - (16 * l16 + 4 * fq) * 2;
  c.sz = sz; c.D = Dd;
  asm volatile("" : "+v"(c.vz), "+v"(c.px), "+v"(c.D) :: "memory");
  asm volatile("" : "+s"(c.sz));
  cv_rd0(c, make_iseq<24>{});
  d_store<NS_X + 3>(); d_store<NS_X + 4>();
  __builtin_amdgcn_sched_barrier(0);
  static_for([&](auto R) __attribute__((always_inline)) {
    cv_step<R>(c, make_iseq<36>{});
    if constexpr (R == 2) {
      asm volatile("s_waitcnt vmcnt(0)\n\ts_barrier" ::: "memory");
      fl_set<FLAG>();
      sset<101>(c.sz);
    }
    if constexpr (R == 5) {
      asm volatile("s_waitcnt vmcnt(0)" ::: "memory");
      fl_check();
    }
    __builtin_amdgcn_sched_barrier(0);
  }, make_iseq<8>{});
  asm volatile("s_nop 7");
  cv_zs<cv_z0(36)>(c, make_iseq<88 - cv_z0(36)>{});
  __builtin_amdgcn_sched_barrier(0);
}

__device__ __forceinline__ void post_dma(int cg, int tb, const bf16* __restrict__ q, unsigned m0w) {
  int lane = threadIdx.x & 63; asm volatile("" : "+v"(lane));
  const __amdgpu_buffer_rsrc_t sq = __builtin_amdgcn_make_buffer_rsrc((void*)(q + (size_t)tb * 32 * DA + cg * 64), (short)0, 32 * DA * 2, 0x00020000);
  const unsigned vq = ((lane >> 3) * DA + (lane & 7) * 8) * 2;
  asm volatile("s_mov_b32 m0, %0\n\ts_nop 4\n\tbuffer_load_dwordx4 %1, %2, 0 offen lds\n\tbuffer_load_dwordx4 %1, %2, %3 offen offset:1024 lds\n\t"
               "buffer_load_dwordx4 %1, %2, %4 offen offset:2048 lds\n\tbuffer_load_dwordx4 %1, %2, %5 offen offset:3072 lds"
               ::"s"(m0w + 4096), "v"(vq), "s"(sq), "s"(8 * DA * 2 - 1024), "s"(16 * DA * 2 - 2048), "s"(24 * DA * 2 - 3072) : "memory");
}
__device__ __forceinline__ void post_math(int cg, int tb, bf16* __restrict__ out, const char* lw) {
  int lane = threadIdx.x & 63; asm volatile("" : "+v"(lane));
  const int tl = lane >> 3, ck = lane & 7;
  const __amdgpu_buffer_rsrc_t so = __builtin_amdgcn_make_buffer_rsrc((void*)(out + (size_t)tb * 32 * OUTW + cg * 64), (short)0, 32 * OUTW * 2 - 2 * DA, 0x00020000);
  const unsigned vo = (tl * OUTW + ck * 8) * 2;
#pragma unroll
  for (int i = 0; i < 4; ++i) {
    const u32x4c qv = *(const u32x4c*)(lw + 4096 + i * 1024 + lane * 16);
    const char* lz = lw + ck * 64 + 16 * (i ^ (ck >> 1)) + tl * 2;
    u32x4c o;
#pragma unroll
    for (int e = 0; e < 4; ++e) {
      const float z0 = bf2f(*(const unsigned short*)(lz + 2 * e * 512));
      const float z1 = bf2f(*(const unsigned short*)(lz + (2 * e + 1) * 512));
      o[e] = pk2(bfl(qv[e]) * z0, bfh(qv[e]) * z1);
    }
    __builtin_amdgcn_raw_buffer_store_b128(o, so, vo + i * 8 * OUTW * 2, 0, 17);
  }
}

#define DI __device__ __forceinline__
#define GAP() __builtin_amdgcn_sched_barrier(0)
typedef __attribute__((ext_vector_type(4))) float f32x4g;
typedef __attribute__((ext_vector_type(4))) unsigned u32x4g;
constexpr int LDW = NH * HD;
struct GM { unsigned aA[2], aB[2][8]; };
template <bool W, int J, int V> DI void g_dma() {
  if constexpr (J == 0) asm volatile("buffer_load_dwordx4 v%c0, s[%c1:%c2], 0 offen lds" ::"i"(V), "i"(W ? 60 : 56), "i"(W ? 63 : 59) : "memory");
  else asm volatile("buffer_load_dwordx4 v%c0, s[%c1:%c2], s%c3 offen lds" ::"i"(V), "i"(W ? 60 : 56), "i"(W ? 63 : 59), "i"((W ? 70 : 63) + J) : "memory");
}
template <int P, int KS, int I> DI void g_wread(GM& g) {
  constexpr int nf = I >> 1, hf = I & 1, r = 192 - 64 * P + 8 * nf + 4 * KS + 2 * hf;
  asm volatile("ds_read_b64_tr_b16 a[%c1:%c2], %0 offset:%c3" ::"v"(g.aB[hf][nf]), "i"(r), "i"(r + 1), "i"(P * 32768 + KS * 16384));
}
template <int P, int M, int KS> DI void g_aread(GM& g) {
  constexpr int r = 96 + 8 * (M & 1) + 4 * KS;
  asm volatile("ds_read_b128 v[%c1:%c2], %0 offset:%c3" ::"v"(g.aA[KS]), "i"(r), "i"(r + 3), "i"(P * 32768 + M * 2048));
}
constexpr float CS = 0.0625f * 1.4426950408889634f;
constexpr int NS_Q = 19;
constexpr int e_vm(int G) { return (G * 3) % 8 < 3 ? G * 3 / 8 : -1; }
constexpr int e_st(int MODE, int G) {
  const int q = G & 15, k3 = ((G >> 4) - 1) * 3 + (q == 0 ? 0 : q == 4 ? 1 : 2), k2 = NS_Q + (G >> 4) * 2 + (q == 4);
  if (MODE == 5) return e_vm(G) >= 32 ? e_vm(G) - 32 : -1;
  if (MODE == 3) return G >= 33 && (G - 33) % 5 == 0 && (G - 33) / 5 < NS_X ? (G - 33) / 5 : -1;
  if (MODE == 2) return G >= 30 && (q == 0 || q == 4 || q == 14) && k3 - 2 < NS_Q ? k3 - 2 : -1;
  if (MODE == 4) return (q == 0 || q == 4) && k2 < 32 ? k2 : -1;
  return -1;
}
constexpr int e_nst(int MODE, int G0, int G1) { int n = 0; for (int G = G0; G < G1; ++G) n += e_st(MODE, G) >= 0; return n; }
static_assert(e_nst(2, 0, 128) == NS_Q && e_nst(3, 30, 128) == NS_X && e_nst(5, 0, 128) == 16 && e_nst(4, 0, 104) == 32 - NS_Q);
constexpr int e_slot(int MODE, int G) { return MODE == 3 ? (G % 3 == 0 && G < 30 ? G / 3 : -1) : e_vm(G) >= 0 && e_vm(G) < 32 ? 10 + e_vm(G) : -1; }
struct ES { int op0[129], nop[129]; };
constexpr ES e_sched(bool TR, int MODE) {
  ES z{};
  const bool SCL = MODE == 2 || MODE == 4;
  const int per = SCL ? 16 : 8, o1 = 32 * per;
  int o = MODE == 4 ? e_sched(false, 2).op0[128] : 0;
  for (int G = 0; G <= 128; ++G) {
    const int q = G & 15;
    int cap = 2 - (q == 1 || q == 2 || q == 15);
    if (SCL) cap -= (q >= 3 && q < 11 && (q & 1)) + (q == 7 || q == 11) + 2 * (G == 70 || G == 74 || (G & 63) == 61);
    cap += MODE == 2 ? 4 + (q == 10 || q == 13) : MODE == 3 ? 1 : MODE == 5 ? 2 : 0;
    if (e_st(MODE, G) >= 0 || (SCL ? q == 8 || q == 12 : e_slot(MODE, G) >= 0) || (MODE == 4 && G < 4)) cap = 0;
    if (G == 128) cap = 10000;
    z.op0[G] = o;
    while (o < o1) {
      const int i = o / per, rdy = MODE == 4 ? 0 : (TR ? 32 * (i >> 3) + 16 + 2 * (i & 7) + 1 : 4 * i + 3) + 6;
      if ((G < rdy && G < 128) || cap-- <= 0) break;
      ++o; ++z.nop[G];
    }
  }
  return z;
}
template <bool TR, int MODE> constexpr ES ESCH = e_sched(TR, MODE);
static_assert(ESCH<false, 4>.op0[96] == 512 && ESCH<false, 4>.op0[0] >= 24 * 16);
template <bool TR, bool SCL, int O> DI void e_op() {
  constexpr int per = SCL ? 16 : 8, i = O / per, k = O % per, r = 128 + 4 * i, t = 114 + 4 * (i & 1), c = k - (per - 4);
  if constexpr (k < 4) asm volatile("v_accvgpr_read_b32 v%c0, a[%c1]" ::"i"(t + k), "i"(4 * i + k));
  else if constexpr (c < 0) asm volatile("v_mul_f32 v%c0, s83, v%c0" ::"i"(k < 8 ? r + (k ^ 6) : t + k - 8));
  else {
    constexpr int d = c == 0 ? r + 3 : c == 1 ? r + 2 : r + c - 2, x = c == 0 ? r + 2 : c == 1 ? r : t + 2 * (c - 2);
    asm volatile("v_cvt_pk_bf16_f32 v%c0, v%c1, v%c2" ::"i"(d), "i"(x), "i"(x + 1));
  }
}
template <bool TR, bool SCL, int B, int... Is> DI void e_ops(iseq<Is...>) { (e_op<TR, SCL, B + Is>(), ...); }
template <int S> DI void e_m0() {
  if constexpr (S == 0 || S == 1 || S == 5 || S == 9) asm volatile("s_add_u32 m0, s78, %0" ::"i"(S * 1024));
  if constexpr (S >= 26 && (S - 26) % 4 == 0) asm volatile("s_add_u32 m0, s79, %0" ::"i"(((S - 26) >> 3) * 32768 + ((S - 26) & 4) * 1024));
}
template <int S> DI void e_load() {
  if constexpr (S == 0) asm volatile("buffer_load_dwordx4 v122, s[56:59], 0 offen lds" ::: "memory");
  else if constexpr (S < 5) asm volatile("buffer_load_dwordx4 v84, s[56:59], s64 offen offset:%0 lds" ::"i"((S - 1) * 1024) : "memory");
  else if constexpr (S < 9) asm volatile("buffer_load_dwordx4 v84, s[60:63], 0 offen offset:%0 sc1 lds" ::"i"((S - 5) * 1024) : "memory");
  else if constexpr (S == 9) asm volatile("buffer_load_dwordx4 v84, s[60:63], s62 offen sc1 lds" ::: "memory");
  else if constexpr (S < 26) {
    constexpr int f = S - 10;
    asm volatile("buffer_load_dwordx4 a[%c0:%c1], v84, s[88:91], s96 offen offset:%2 sc1" ::"i"(128 + 4 * f), "i"(131 + 4 * f), "i"((f & 3) * 1024) : "memory");
    if constexpr ((f & 3) == 3) asm volatile("s_addk_i32 s96, %0" ::"i"(f == 7 ? 28672 : 4096));
  } else {
    constexpr int f = S - 26;
    asm volatile("buffer_load_dwordx4 v%c0, s[88:91], s98 offen offset:%1 sc1 lds" ::"i"(86 + ((f >> 2) & 1)), "i"((f & 3) * 1024) : "memory");
    if constexpr (f == 7) asm volatile("s_add_u32 s98, s98, s97");
    if constexpr (f == 15) asm volatile(FL_POLL ::: "memory");
  }
}
template <bool TR, int P, int H, int G, int MODE> DI void g_gap(GM& g) {
  constexpr int m = 4 * H + (G >> 4), q = G & 15, n = q >> 1, ks = q & 1, GG = 64 * H + G;
  constexpr bool inV = TR ? m & 1 : n & 1, END = MODE == 3 || MODE == 5;
  constexpr int a = 96 + 8 * (m & 1) + 4 * ks, b = 192 - 64 * P + 8 * n + 4 * ks, d = (inV ? 128 : 0) + 4 * (TR ? 8 * (m >> 1) + n : 4 * m + (n >> 1));
  constexpr int J = END ? -1 : q == 8 ? 2 * (G >> 4) : q == 12 ? 2 * (G >> 4) + 1 : -1, gj = (J & 3) ^ ((J & 3) >> 1);
  if constexpr (END) { if constexpr (q == 0 && GG > 0) asm volatile("s_waitcnt lgkmcnt(0)"); e_m0<e_slot(MODE, GG)>(); }
  else if constexpr (q == 0 && G > 0) asm volatile("s_waitcnt lgkmcnt(4)");
  if constexpr (J == 0) asm volatile("s_add_u32 m0, s78, %0" ::"i"((P ^ H ^ 1) * 32768 + (1 - H) * 8192));
  else if constexpr (J == 4) asm volatile("s_add_u32 m0, s79, %0" ::"i"((P ^ H) * 32768 + (1 - H) * 16384));
  else if constexpr (J > 0 && (J & 3) == 1) asm volatile("s_bitset1_b32 m0, 12");
  else if constexpr (J > 0 && (J & 3) == 2) asm volatile("s_bitset1_b32 m0, %0" ::"i"(J < 4 ? 14 : 13));
  else if constexpr (J > 0) asm volatile("s_bitset0_b32 m0, 12");
#define GMF(D, X, Y, C) asm volatile("v_mfma_f32_16x16x32_bf16 " D "[%c0:%c1], " X ", " Y ", " C ::"i"(d), "i"(d + 3), "i"(a), "i"(a + 3), "i"(b), "i"(b + 3))
#define GMF2(D, C) do { if constexpr (TR) GMF(D, "v[%c2:%c3]", "a[%c4:%c5]", C); else GMF(D, "a[%c4:%c5]", "v[%c2:%c3]", C); } while (0)
  if constexpr ((MODE == 1 || MODE == 4) && ks == 0) { if constexpr (inV) GMF2("v", "0"); else GMF2("a", "0"); }
  else if constexpr (inV) GMF2("v", "v[%c0:%c1]");
  else GMF2("a", "a[%c0:%c1]");
#undef GMF2
#undef GMF
  if constexpr ((q == 1 || q == 2) && !(END && m == 7)) g_aread<(m < 7 ? P : P ^ 1), (m + 1) & 7, q - 1>(g);
  if constexpr (!END && q >= 3 && q < 11 && (q & 1)) g_wread<P ^ 1, H, (G >> 4) * 4 + (q - 3) / 2>(g);
  if constexpr (MODE == 1 && GG < 7) {
    if constexpr (GG == 0) asm volatile("s_barrier" ::: "memory");
    if constexpr (GG == 2) asm volatile("s_add_u32 m0, s79, %0" ::"i"(P * 32768));
    if constexpr (GG >= 3) g_dma<true, (GG - 3) ^ ((GG - 3) >> 1), 91>();
    if constexpr (GG == 3 || GG == 4) asm volatile("s_bitset1_b32 m0, %0" ::"i"(9 + GG));
    if constexpr (GG == 5) asm volatile("s_bitset0_b32 m0, 12");
  }
  if constexpr (e_st(MODE, GG) >= 0) {
    static_assert(ESCH<TR, MODE>.op0[GG] >= (e_st(MODE, GG) + 1) * (END ? 8 : 16));
    d_store<e_st(MODE, GG)>();
  }
  if constexpr (J >= 0 && J < 4) g_dma<false, (1 - H) * 2 + (gj & 1) + (gj >> 1) * 4, 90>();
  else if constexpr (J >= 4) g_dma<true, (1 - H) * 4 + gj, 91>();
  if constexpr (MODE >= 2) e_ops<TR, !END, ESCH<TR, MODE>.op0[GG]>(make_iseq<ESCH<TR, MODE>.nop[GG]>{});
  if constexpr (END) {
    if constexpr (e_slot(MODE, GG) >= 0) e_load<e_slot(MODE, GG)>();
  } else {
    if constexpr (H == 1 && G == 6) asm volatile("v_lshl_add_u32 v90, s82, 7, v92\n\ts_and_b32 s82, s80, 15");
    if constexpr (H == 1 && G == 10) asm volatile("s_lshr_b64 s[80:81], s[80:81], 4\n\tv_lshl_add_u32 v91, s82, 19, v93");
    if constexpr (G == 61) {
      constexpr int nst = e_nst(MODE, H ? 29 : 0, GG + 1) + (MODE == 4 && !H ? e_nst(2, 93, 128) : 0);
      asm volatile("s_waitcnt vmcnt(%0) lgkmcnt(0)\n\ts_barrier" ::"i"(12 + nst) : "memory");
    }
  }
  if constexpr (END && GG == 127) {
    asm volatile("s_nop 7");
    e_ops<TR, false, ESCH<TR, MODE>.op0[128]>(make_iseq<ESCH<TR, MODE>.nop[128]>{});
  }
  GAP();
}
template <bool TR, int P, int H, int MODE, int... Gs> DI void g_half_(GM& g, iseq<Gs...>) { (g_gap<TR, P, H, Gs, MODE>(g), ...); }
template <bool TR, int P, int H, int MODE> DI void g_half(GM& g) { g_half_<TR, P, H, MODE>(g, make_iseq<64>{}); }
template <bool TR, int P, int MODE> DI void g_tile(GM& g) { g_half<TR, P, 0, MODE>(g); g_half<TR, P, 1, MODE>(g); }
template <int P, int... Is> DI void g_read0(GM& g, iseq<Is...>) {
  (g_wread<P, 0, Is>(g), ...); (g_wread<P, 1, Is>(g), ...);
  g_aread<P, 0, 0>(g); g_aread<P, 0, 1>(g);
}
DI void g_proW(unsigned g, unsigned mW) {
  asm volatile("v_lshl_add_u32 v91, %0, 18, v93\n\ts_mov_b32 m0, %1" ::"s"(g), "s"(mW));
  asm volatile("s_nop 0"); g_dma<true, 0, 91>();
  asm volatile("s_bitset1_b32 m0, 12\n\ts_nop 0"); g_dma<true, 1, 91>();
  asm volatile("s_bitset1_b32 m0, 13\n\ts_nop 0"); g_dma<true, 3, 91>();
  asm volatile("s_bitset0_b32 m0, 12\n\ts_nop 0"); g_dma<true, 2, 91>();
}
template <int PART = 3> DI void g_pro(unsigned i, unsigned mA, unsigned mW, unsigned lda) {
  i &= 3;
  if constexpr (PART & 1)
    asm volatile("v_add_u32 v90, %0, v92\n\tv_add_u32 v91, %1, v93\n\ts_mov_b32 m0, %2\n\ts_mov_b32 s54, %3" ::"s"(((i & 1) + (i >> 1) * 4) * 64 * lda),
                 "s"((i & 1) * 0x20000 + (i >> 1) * 256), "s"(mA + (i & 1) * 4096 + (i >> 1) * 16384), "s"(mW + i * 4096));
  if constexpr (PART == 1) return;
  g_dma<false, 0, 90>();
  asm volatile("s_bitset1_b32 m0, 13\n\ts_nop 0"); g_dma<false, 2, 90>();
  asm volatile("s_mov_b32 m0, s54\n\ts_nop 0"); g_dma<true, 0, 91>();
  asm volatile("s_bitset1_b32 m0, 14\n\ts_nop 0"); g_dma<true, 4, 91>();
  asm volatile("s_xor_b32 m0, s54, 0x8000\n\ts_nop 0\n\tbuffer_load_dwordx4 v91, s[60:63], s53 offen lds" ::: "memory", "scc");
}
DI void g_pro2(unsigned mA, unsigned mW) {
  asm volatile("v_add_u32 v90, 128, v92\n\ts_mov_b32 m0, %0" ::"s"(mA ^ 32768));
  asm volatile("s_nop 0"); g_dma<false, 0, 90>();
  asm volatile("s_bitset1_b32 m0, 12\n\ts_nop 0"); g_dma<false, 1, 90>();
  asm volatile("s_bitset1_b32 m0, 14\n\ts_nop 0"); g_dma<false, 5, 90>();
  asm volatile("s_bitset0_b32 m0, 12\n\ts_nop 0"); g_dma<false, 4, 90>();
  g_proW(3, mW ^ 49152);
}
template <bool TR, bool SCL, class HOOK> DI void g_loop(GM& g, unsigned evm, HOOK&& hook) {
  constexpr int nkt = SCL ? 7 : 16, P0 = SCL ? 0 : 1;
  asm volatile("" : "+s"(evm));
#pragma unroll 1
  for (int t = 1;; t += 2) {
    g_half<TR, P0 ^ 1, 0, 0>(g);
    if (__builtin_expect((evm >> t) & 1, 0)) { hook(t); GAP(); }
    g_half<TR, P0 ^ 1, 1, 0>(g);
    if (SCL && t + 2 >= nkt) break;
    g_half<TR, P0, 0, 0>(g);
    if constexpr (SCL) { if (__builtin_expect(t == 3, 0)) { hook(4); GAP(); } }
    else asm volatile("s_cmp_eq_u32 %0, 13\n\ts_cselect_b32 s78, 0x28000, s78" ::"s"(t) : "scc");
    g_half<TR, P0, 1, 0>(g);
    if (!SCL && t + 3 >= nkt) break;
  }
}

typedef __attribute__((ext_vector_type(4))) unsigned u32x4;
typedef __attribute__((ext_vector_type(4))) float f32x4;

constexpr int R_S = 128, R_P = 192, R_CI = 224, R_E = 96;
constexpr int R_TS = 112, R_RS = 114, R_M = 116;
constexpr int R_PD = 118, R_THR = 120, R_TMP = 121;
constexpr int r_dk(int q) { return q < 6 ? 90 + q : 120 + q; }
constexpr int S_V = 96, S_T = 97, S_K = 98, S_I = 99, S_EV = 100;
struct AC {
  unsigned rare;
  unsigned tb, vs, o0, o1;
};
DI bool redo() { u64 t; asm volatile("s_mov_b64 %0, s[54:55]" : "=s"(t)); return t != 0; }
template <int X, int F> DI void kread() {
  constexpr int r = 192 + 4 * (F & 15);
  asm volatile("ds_read_b128 a[%c0:%c1], v84 offset:%c2" ::"i"(r), "i"(r + 3), "i"(X * 32768 + F * 1024));
}
template <int X, int F> DI void vread() {
  constexpr int r = 192 + 4 * (F & 15);
  asm volatile("ds_read_b128 a[%c0:%c1], v85 offset:%c2" ::"i"(r), "i"(r + 3), "i"(X * 32768 + (F & 1) * 16384 + (F >> 1) * 1024));
}
template <int X, int... Fs> DI void kreads(iseq<Fs...>) { (kread<X, Fs>(), ...); }
template <int X, int J> DI void dma_m0() {
  if constexpr (J == 0) asm volatile("s_mov_b32 m0, s%c0" ::"i"(78 + X));
  else if constexpr (J == 4) asm volatile("s_or_b32 m0, s%c0, 0x1000" ::"i"(78 + X));
  else if constexpr (J == 8) asm volatile("s_bitset0_b32 m0, 16");
  else if constexpr (J == 12) asm volatile("s_bitset0_b32 m0, 12");
}
template <int J> DI void dma_at() {
  if constexpr (J < 8)
    asm volatile("buffer_load_dwordx4 v%c0, s[88:91], s96 offen offset:%1 sc1 lds" ::"i"(86 + (J >= 4)), "i"((J & 3) * 1024) : "memory");
  else
    asm volatile("buffer_load_dwordx4 v%c0, s[92:95], s98 offen offset:%1 sc1 lds" ::"i"(r_dk(J - 8)), "i"((J & 3) * 1024) : "memory");
}
template <int Q> DI void dma4k() {
  asm volatile("s_xor_b32 m0, s79, %0\n\ts_add_u32 s48, s98, %1\n\tbuffer_load_dwordx4 v%c2, s[88:91], s48 offen sc1 lds\n\t"
               "buffer_load_dwordx4 v%c2, s[88:91], s48 offen offset:1024 sc1 lds\n\tbuffer_load_dwordx4 v%c2, s[88:91], s48 offen offset:2048 sc1 lds\n\t"
               "buffer_load_dwordx4 v%c2, s[88:91], s48 offen offset:3072 sc1 lds"
               ::"i"((Q >> 1) * 32768 + (Q & 1) * 4096), "i"((Q >> 1) * 32768), "i"(86 + (Q & 1)) : "memory", "scc");
}
DI void acc_zero() { static_for([](auto I) { asm volatile("v_accvgpr_write_b32 a[%c0], 0" ::"i"((int)I)); }, make_iseq<128>{}); }
#define SMV 0
struct SMS { int ex[128], op0[128], nop[128], last; };
constexpr SMS sm_sched() {
  SMS z{};
  int et[32] = {}, e = 0;
  for (int t = 0; t < 128; ++t) {
    const int g = t & 15;
    const bool x = t < 48 ? (g >= 8 && (g & 1) && g < 15) : t < 64 ? (g >= 9 && g < 15) : (g >= 8 && g < 14);
    z.ex[t] = x && e < 32 ? e : -1;
    if (z.ex[t] >= 0) et[e++] = t;
  }
  int o = 0;
  for (int t = 0; t < 128; ++t) {
    const int g = t & 15;
    int cap = z.ex[t] >= 0 || t < 8 || (t < 48 && (g == 7 || (g >= 8 && !(g & 1)))) || t == 56 || t == 63 || t == 111 ? 0 : 1;
    z.op0[t] = o; z.nop[t] = 0;
    while (cap > 0 && o < 48 && et[2 * (o / 3) + (o % 3 == 0 ? 0 : 1)] + 2 <= t) {
      const int el = 2 * (o / 3) + o % 3;
      if (!(o % 3 < 2 && (el == 0 || el == 4))) --cap;
      ++o; ++z.nop[t]; z.last = t;
    }
  }
  for (int k = 16; k < 32; ++k) if (z.op0[et[k]] < 3 * ((k - 16) / 2 + 1)) z.last = 1000;
  return z;
}
constexpr SMS SM = sm_sched();
static_assert(SM.last <= 117);
constexpr int p_reg(int Y, int k) { return R_P + 16 * Y + 4 * ((k >> 3) * 2 + ((k >> 1) & 1)) + ((k >> 2) & 1) * 2 + (k & 1); }
template <int Y, int O> DI void sm_op() {
  constexpr int k = O / 3, e = 2 * k + O % 3, ts = R_TS + ((e >> 2) & 1);
  if constexpr (O % 3 == 2) asm volatile("v_cvt_pk_bf16_f32 v%c0, v%c1, v%c2" ::"i"(p_reg(Y, k)), "i"(R_E + ((2 * k) & 15)), "i"(R_E + ((2 * k + 1) & 15)));
  else if constexpr (e == 1 || e == 5) asm volatile("v_add_f32_e32 v%c0, v%c1, v%c2" ::"i"(ts), "i"(R_E + e - 1), "i"(R_E + e));
  else if constexpr (e != 0 && e != 4) asm volatile("v_add_f32_e32 v%c0, v%c0, v%c1" ::"i"(ts), "i"(R_E + (e & 15)));
}
template <int Y, int T> DI void sm_gap(AC& c) {
  if constexpr (SM.ex[T] >= 0) asm volatile("v_exp_f32 v%c0, v%c1" ::"i"(R_E + (SM.ex[T] & 15)), "i"(R_S + 32 * Y + SM.ex[T]));
  if constexpr (SM.nop[T] > 0) sm_op<Y, SM.op0[T]>();
  if constexpr (SM.nop[T] > 1) sm_op<Y, SM.op0[T] + 1>();
  if constexpr (SM.nop[T] > 2) sm_op<Y, SM.op0[T] + 2>();
  if constexpr (T == SM.last + 1) asm volatile("v_add_f32_e32 v%c0, v%c1, v%c2" ::"i"(R_TMP), "i"(R_TS), "i"(R_TS + 1));
  if constexpr (T == SM.last + 2) asm volatile("v_cmp_nle_f32_e64 s[54:55], v%c0, v%c1" ::"i"(R_TMP), "i"(R_THR));
}
template <int X, int MODE, int G> DI void gapA(AC& c) {
  constexpr bool FIRST = MODE == 1 || MODE == 2, NOQK = MODE == 3;
  constexpr int Xp = X ^ 1, kb = G >> 4, g = G & 15, qb = g >> 3, s = g & 7;
  if constexpr (MODE == 3 && G < 48 && G % 3 == 0) {
    constexpr int f = G / 3;
    asm volatile("buffer_load_dwordx4 a[%c0:%c1], v84, s[88:91], s75 offen offset:%2 sc1" ::"i"(128 + 4 * f), "i"(131 + 4 * f), "i"((f & 3) * 1024) : "memory");
    if constexpr ((f & 3) == 3) asm volatile("s_addk_i32 s75, %0" ::"i"(f == 7 ? 28672 : 4096));
  }
  if constexpr (G == 48) asm volatile("s_waitcnt vmcnt(%0) lgkmcnt(0)\n\ts_barrier" ::"i"(MODE == 3 ? 16 : MODE == 2 ? 12 : MODE == 1 ? 7 : 0) : "memory");
  else if constexpr (g == 0 && !NOQK) asm volatile("s_waitcnt lgkmcnt(0)");
  if constexpr (!NOQK) {
    constexpr int r = 192 + 4 * ((kb & 1) * 8 + s), q = 128 + 4 * g;
    constexpr int d = R_S + 32 * X + 4 * (kb * 2 + qb), ci = s == 0 ? R_CI + 4 * (kb * 2 + qb) : d;
    if constexpr (FIRST && s == 0)
      asm volatile("v_mfma_f32_16x16x32_bf16 v[%c0:%c1], a[%c2:%c3], a[%c4:%c5], 0" ::"i"(d), "i"(d + 3), "i"(r), "i"(r + 3), "i"(q), "i"(q + 3));
    else
      asm volatile("v_mfma_f32_16x16x32_bf16 v[%c0:%c1], a[%c2:%c3], a[%c4:%c5], v[%c6:%c7]" ::"i"(d), "i"(d + 3), "i"(r), "i"(r + 3), "i"(q), "i"(q + 3),
                   "i"(ci), "i"(ci + 3));
    if constexpr (g < 8 && kb < 3) kread<X, (kb + 1) * 8 + g>();
    constexpr int S0 = MODE == 1 ? 0 : SMV ? 40 : 14, S1 = MODE == 1 ? 1 : SMV ? 41 : 30, S2 = SMV ? 42 : 45, S3 = SMV ? 43 : 46;
    if constexpr (G == S0) asm volatile("s_add_u32 s96, s97, 0x1000000");
    if constexpr (G == S1) dma_m0<X, 0>();
    if constexpr (G == S2) asm volatile("s_mov_b32 s97, s98");
    if constexpr (G == S3) asm volatile("s_add_u32 s98, s98, s99");
    if constexpr (MODE != 1 && G >= 56 && !(G & 1)) dma_at<(G - 56) / 2>();
    if constexpr (MODE == 1 && (G & 11) == 9) dma_at<(G >> 4) * 2 + ((G >> 2) & 1)>();
    if constexpr (MODE == 1 && G == 31) dma_m0<X, 4>();
    if constexpr (G == 57) asm volatile("s_lshr_b32 s100, s100, 1\n\ts_add_u32 s56, s56, 1");
    if constexpr (G == 59) asm volatile("s_getpc_b64 s[86:87]\nev_pc%c0:\n\ts_add_u32 s86, s86, ev_ret%c0-ev_pc%c0\n\ts_addc_u32 s87, s87, 0" ::"i"(X * 4 + MODE));
    if constexpr (G == 61 && !FIRST) asm volatile("s_or_b32 s57, s54, s55\n\ts_mov_b32 %0, s57\n\ts_bitcmp1_b32 s100, 0" : "=s"(c.rare));
    if constexpr (G == 61 && FIRST) asm volatile("s_bitcmp1_b32 s100, 0");
  } else if constexpr (SM.nop[64 + G] > 0) asm volatile("s_nop 0");
  if constexpr (!FIRST && g < 8 && kb == 3) vread<Xp, g>();
  if constexpr (MODE == 2 && (G & 3) == 0) d_store<16 + G / 4>();
  if constexpr (!FIRST) sm_gap<Xp, 64 + G>(c);
  if constexpr (G == 63 && !NOQK) asm volatile("s_cbranch_scc1 ev_entry\nev_ret%c0:" ::"i"(X * 4 + MODE) : "vcc", "memory");
  GAP();
}
template <int R> DI void rescale1(float alpha) {
  float t;
  asm volatile("v_accvgpr_read_b32 %0, a[%c2]\n\tv_mul_f32 %0, %0, %1\n\tv_accvgpr_write_b32 a[%c2], %0" : "=&v"(t) : "v"(alpha), "i"(R));
}
template <int... Rs> DI void rescale_all(const float (&alpha)[2], iseq<Rs...>) { (rescale1<Rs>(alpha[(Rs >> 2) & 1]), ...); }
template <bool MAX> DI float row_reduce(float v) {
  auto p = __builtin_amdgcn_permlane32_swap(__float_as_uint(v), __float_as_uint(v), false, false);
  v = MAX ? fmaxf(__uint_as_float(p[0]), __uint_as_float(p[1])) : __uint_as_float(p[0]) + __uint_as_float(p[1]);
  p = __builtin_amdgcn_permlane16_swap(__float_as_uint(v), __float_as_uint(v), false, false);
  return MAX ? fmaxf(__uint_as_float(p[0]), __uint_as_float(p[1])) : __uint_as_float(p[0]) + __uint_as_float(p[1]);
}
template <int Y, int QB> DI float row_max() {
  constexpr int r = R_S + 32 * Y + 4 * QB;
  float mx;
  asm volatile("v_max_f32 %0, v%c1, v%c2\n\tv_max3_f32 %0, %0, v%c3, v%c4" : "=&v"(mx) : "i"(r), "i"(r + 1), "i"(r + 2), "i"(r + 3));
  static_for([&](auto K) {
    constexpr int q = r + 8 * (K + 1);
    float t = mx;
    asm volatile("v_max3_f32 %0, %0, v%c1, v%c2\n\tv_max3_f32 %0, %0, v%c3, v%c4" : "+v"(t) : "i"(q), "i"(q + 1), "i"(q + 2), "i"(q + 3));
    mx = t;
  }, make_iseq<3>{});
  return row_reduce<true>(mx);
}
template <int R> DI void v_sub(float d) { asm volatile("v_sub_f32 v%c0, v%c0, %1" ::"i"(R), "v"(d)); }
template <int B> DI void s_shift(const float (&d)[2]) { static_for([&](auto E) { v_sub<B + E>(d[(E >> 2) & 1]); }, make_iseq<32>{}); }
template <bool CI> DI void m_shift(const float (&d)[2]) {
  if constexpr (CI) s_shift<R_CI>(d);
  v_sub<R_M>(-d[0]); v_sub<R_M + 1>(-d[1]);
}
DI void set_mask0() {
  const int mr = __float_as_int(vget<89>());
  static_for([&](auto I) {
    constexpr int e = (I >> 2) * 8 + (I & 3), k = (I >> 2) * 16 + (I & 3);
    const float x = vget<R_S + e>();
    vset<R_S + e>(k > mr ? -INFINITY : x);
  }, make_iseq<16>{});
}
template <int X, bool CI = true> DI void sm_redo() {
  constexpr int Xp = X ^ 1;
  float d[2], dt[2], alpha[2], ts[2] = {0.f, 0.f};
  static_for([&](auto Q) {
    constexpr int qb = Q;
    const float pd = vget<R_PD + qb>();
    d[qb] = fmaxf(row_max<Xp, qb>() - pd, 0.f);
    dt[qb] = d[qb] + pd;
    vset<R_PD + qb>(d[qb]);
    alpha[qb] = __builtin_amdgcn_exp2f(-d[qb]);
    vset<R_RS + qb>(vget<R_RS + qb>() * alpha[qb]);
  }, make_iseq<2>{});
  vset<R_THR>(d[0] > 0.f || d[1] > 0.f ? -1.f : 4096.f);
  m_shift<CI>(d);
  static_for([&](auto K) {
    constexpr int k = K, qb = (k >> 1) & 1;
    const float e0 = __builtin_amdgcn_exp2f(vget<R_S + 32 * Xp + 2 * k>() - dt[qb]), e1 = __builtin_amdgcn_exp2f(vget<R_S + 32 * Xp + 2 * k + 1>() - dt[qb]);
    ts[qb] += e0; ts[qb] += e1;
    vset<p_reg(Xp, k)>(__uint_as_float(pk2(e0, e1)));
  }, make_iseq<16>{});
  vset<R_TS>(ts[0]); vset<R_TS + 1>(ts[1]);
  rescale_all(alpha, make_iseq<128>{});
  asm volatile("s_nop 1" ::: "memory");
  GAP();
}
template <int X, int MODE, int... Gs> DI void phaseA_(AC& c, iseq<Gs...>) { (gapA<X, MODE, Gs>(c), ...); }
template <int X, int MODE> DI void phaseA(AC& c) { phaseA_<X, MODE>(c, make_iseq<64>{}); }

template <int X, bool SMX, int G, bool FIRST = false> DI void gapB(AC& c) {
  constexpr int Xp = X ^ 1, blk = G >> 4, g = G & 15;
  if constexpr (g == 0) asm volatile("s_waitcnt lgkmcnt(0)");
  constexpr int qb = (g >> 1) & 1, s = g & 1, r = 192 + 4 * ((blk & 1) * 8 + (g >> 2) * 2 + s), o = 4 * ((blk * 4 + (g >> 2)) * 2 + qb);
  constexpr int p = R_P + 16 * Xp + 4 * (s * 2 + qb);
  if constexpr (FIRST && s == 0)
    asm volatile("v_mfma_f32_16x16x32_bf16 a[%c0:%c1], a[%c2:%c3], v[%c4:%c5], 0" ::"i"(o), "i"(o + 3), "i"(r), "i"(r + 3), "i"(p), "i"(p + 3));
  else
  asm volatile("v_mfma_f32_16x16x32_bf16 a[%c0:%c1], a[%c2:%c3], v[%c4:%c5], a[%c0:%c1]" ::"i"(o), "i"(o + 3), "i"(r), "i"(r + 3), "i"(p), "i"(p + 3));
  if constexpr (g < 8) {
    if constexpr (blk < 3) vread<Xp, (blk + 1) * 8 + g>();
    else if constexpr (SMX) kread<Xp, g>();
  }
  if constexpr (SMX) {
    if constexpr (G == 1 || G == 2) asm volatile("v_add_f32_e32 v%c0, v%c0, v%c1" ::"i"(R_RS + G - 1), "i"(R_TS + G - 1));
    if constexpr (g == 7 && blk < 3) dma_m0<X, blk * 4 + 4>();
    if constexpr (g >= 8 && !(g & 1) && blk < 3) dma_at<blk * 4 + 4 + (g - 8) / 2>();
    if constexpr (G == 55) asm volatile("s_cmp_eq_u32 s56, s76");
    if constexpr (G == 3) asm volatile("s_lshr_b32 s77, s77, 1");
    if constexpr (G == 4) asm volatile("s_bitcmp1_b32 s77, 0");
    if constexpr (G == 5) asm volatile("s_cselect_b64 s[52:53], s[62:63], 0");
    if constexpr (G == 6) asm volatile(FL_ARM("s[52:53]"));
    if constexpr (G == 56) asm volatile(FL_POLL ::: "memory");
    sm_gap<X, G>(c);
  }
  GAP();
}
template <int X, bool FIRST, int... Gs> DI void phaseB_(AC& c, iseq<Gs...>) { (gapB<X, true, Gs, FIRST>(c), ...); }
template <int X, bool FIRST = false> DI void phaseB(AC& c) { phaseB_<X, FIRST>(c, make_iseq<64>{}); }
constexpr int sm_nex() { int n = 0; for (int t = 0; t < 64; ++t) n += SM.ex[t] >= 0; return n; }
template <int X> DI void phaseB0(unsigned mode, bool vh) {
  dma_m0<X, 4>();
  kreads<X ^ 1>(make_iseq<8>{});
  if (vh) { dma_at<4>(); dma_at<5>(); dma_at<6>(); dma_at<7>(); }
  dma_m0<X, 8>();
  asm volatile("s_nop 3");
  float d[2];
  if (mode & 2) {
    d[0] = vget<122>(); d[1] = vget<123>();
    dma_at<8>();
  } else {
    if constexpr (X == 0) if (__builtin_expect(mode & 4, 0)) set_mask0();
    d[0] = row_max<X, 0>(); GAP(); dma_at<8>();
    d[1] = row_max<X, 1>();
#pragma unroll
    for (int qb = 0; qb < 2; ++qb) if (!(d[qb] > -3.0e38f)) d[qb] = 0.f;
  }
  GAP();
  vset<R_M>(d[0]); vset<R_M + 1>(d[1]);
  dma_at<9>();
  const f32x2c nd[2] = {{-d[0], -d[0]}, {-d[1], -d[1]}};
  static_for([&](auto E) {
    constexpr int e = E;
    const f32x2c v = nd[(e >> 1) & 1];
    asm volatile("v_pk_add_f32 v[%c0:%c1], v[%c0:%c1], %4\n\tv_mov_b64 v[%c2:%c3], %4" ::"i"(R_S + 32 * X + 2 * e), "i"(R_S + 32 * X + 2 * e + 1), "i"(R_CI + 2 * e), "i"(R_CI + 2 * e + 1), "v"(v));
    if constexpr (e == 7 || e == 15) dma_at<10 + e / 8>();
  }, make_iseq<16>{});
  static_assert(SM.op0[64] >= 3 * ((sm_nex() - 15) / 2) && SM.op0[64] <= 24);
  static_for([&](auto E) {
    constexpr int e = E;
    if constexpr (e < sm_nex()) asm volatile("v_exp_f32 v%c0, v%c1" ::"i"(R_E + e), "i"(R_S + 32 * X + e));
    if constexpr (e == 2) dma_m0<X, 12>();
    if constexpr (e == 5 || e == 10 || e == 15) dma_at<11 + e / 5>();
  }, make_iseq<16>{});
  static_for([&](auto O) { sm_op<X, O>(); }, make_iseq<SM.op0[64]>{});
  dma_at<15>();
  static_for([&](auto E) { asm volatile("v_exp_f32 v%c0, v%c1" ::"i"(R_E + (int)E), "i"(R_S + 32 * X + 16 + (int)E)); }, make_iseq<(sm_nex() > 16 ? sm_nex() - 16 : 0)>{});
  asm volatile("buffer_store_dwordx2 v[%c0:%c1], v88, s[88:91], s76 offen sc1\n\ts_bitcmp1_b32 s77, 0\n\ts_cselect_b64 s[52:53], s[62:63], 0\n\t"
               FL_ARM("s[52:53]") "\n\t" FL_POLL ::"i"(R_M), "i"(R_M + 1) : "memory", "scc");
  GAP();
}
enum : int { T_Q0, T_K0, T_OQ, T_OG, T_FPREP, T_FQ, T_FG, T_FS, T_ZO, T_CNT, T_NPRE = 10, T_NST = 12, T_TAB = 14, T_SLAB = 16, T_PART, T_IT = 18, T_ITN = 23 };
#define MSK(R, K) "v_cmp_gt_i32 s[48:49], " #K ", v89\n\tv_cmp_gt_i32 s[50:51], " #K "+1, v89\n\tv_cmp_gt_i32 s[52:53], " #K "+2, v89\n\t" \
                  "v_cmp_gt_i32 s[54:55], " #K "+3, v89\n\tv_cndmask_b32_e64 v[" #R "], v[" #R "], v122, s[48:49]\n\t" \
                  "v_cndmask_b32_e64 v[" #R "+1], v[" #R "+1], v122, s[50:51]\n\tv_cndmask_b32_e64 v[" #R "+2], v[" #R "+2], v122, s[52:53]\n\t" \
                  "v_cndmask_b32_e64 v[" #R "+3], v[" #R "+3], v122, s[54:55]\n\t"
DI void ev_code(AC& c) {
  GAP();
  asm volatile(
    "s_getpc_b64 s[82:83]\n\ts_add_u32 s82, s82, 12\n\ts_addc_u32 s83, s83, 0\n\ts_branch 9f\nev_entry:\n\t"
    "s_bitcmp1_b32 s60, s56\n\ts_cbranch_scc1 3f\n\ts_bitcmp1_b32 s74, s56\n\ts_cbranch_scc1 2f\n\t"
    "s_bitcmp1_b32 s58, s56\n\ts_cselect_b64 vcc, -1, 0\n\tv_cndmask_b32 v121, -2.0, v81, vcc\n\t"
    "buffer_store_dwordx2 v[82:83], v121, s[88:91], s70 offen sc1\n\tbuffer_store_dwordx2 v[82:83], v121, s[88:91], s71 offen sc1\n\t"
    "v_cmp_eq_u64 vcc, s[84:85], v[124:125]\n\ts_andn2_b64 vcc, s[62:63], vcc\n\ts_cbranch_scc1 5f\n"
    "0:\n\ts_add_u32 s98, s98, s59\n\ts_mov_b32 s59, s61\n\ts_mov_b32 s61, s72\n\ts_mov_b64 s[62:63], s[64:65]\n\ts_mov_b64 s[64:65], s[66:67]\n\t"
    "s_mov_b64 s[66:67], s[68:69]\n\ts_cmp_eq_u32 s56, 1\n\ts_cbranch_scc1 ev_first\n\ts_setpc_b64 s[86:87]\n"
    "5:\n\t" FL_CHECK("s[62:63]") "\n\ts_branch 0b\n"
    "6:\n\t" FL_CHECK("s[62:63]") "\n\ts_setpc_b64 s[86:87]\n"
    "2:\n\ts_mov_b32 s98, 0\n\t"
    "v_lshrrev_b32 v90, 7, v86\n\tv_and_b32 v91, 6, v90\n\tv_lshlrev_b32 v91, 4, v91\n\tv_and_b32 v92, 0x70, v86\n\tv_xor_b32 v91, v91, v92\n\t"
    "s_movk_i32 s48, 896\n\tv_mad_u32_u24 v94, v90, s48, v91\n\ts_mov_b64 s[92:93], s[80:81]\n\ts_mov_b32 s94, 229376\n\t"
    "v_add_u32 v90, 28672, v94\n\tv_add_u32 v91, 34816, v94\n\tv_add_u32 v92, 40960, v94\n\tv_add_u32 v93, 47104, v94\n\t"
    "v_add_u32 v95, 6144, v94\n\tv_add_u32 v126, 12288, v94\n\tv_add_u32 v127, 18432, v94\n\t"
    "v_mov_b32 v122, 0xff800000\n\t" MSK(228, 0) MSK(236, 16) MSK(244, 32) MSK(252, 48)
    "v_cmp_eq_u64 vcc, s[84:85], v[124:125]\n\ts_andn2_b64 vcc, s[62:63], vcc\n\ts_cbranch_scc1 6b\n\ts_setpc_b64 s[86:87]\n"
    "3:\n\tv_mov_b32 v122, 0xff800000\n\t" MSK(224, 0) MSK(232, 16) MSK(240, 32) MSK(248, 48)
    "v_readlane_b32 s54, v78, %0\n\ts_add_u32 m0, s78, 0x10000\n\t"
    "s_bfe_u32 s53, s73, 0x80010\n\ts_mulk_i32 s53, 3840\n\ts_mov_b64 s[48:49], s[80:81]\n\ts_add_u32 s50, s53, 3840\n\ts_min_u32 s50, s50, 229376\n\t"
    "s_mov_b32 s51, 0x20000\n\t"
    "v_lshlrev_b32 v121, 3, v84\n\tbuffer_load_dword v121, s[48:51], s53 offen lds\n\t"
    "s_bitcmp0_b32 s73, 3\n\ts_cbranch_scc1 4f\n\t"
    "buffer_load_dwordx4 v84, s[88:91], s54 offen sc1 lds\n\tbuffer_load_dwordx4 v84, s[88:91], s54 offen offset:1024 sc1 lds\n\t"
    "buffer_load_dwordx4 v84, s[88:91], s54 offen offset:2048 sc1 lds\n\tbuffer_load_dwordx4 v84, s[88:91], s54 offen offset:3072 sc1 lds\n"
    "4:\n\ts_cmp_eq_u32 s56, 1\n\ts_cbranch_scc1 ev_first\n\ts_setpc_b64 s[86:87]\n"
    ::"i"((int)T_ZO) : "scc");
  asm volatile("ev_zero:");
  acc_zero();
  asm volatile("s_setpc_b64 s[86:87]\nev_first:\n\ts_cmp_lg_u32 s57, 0\n\ts_cbranch_scc1 ev_zero\n\ts_bitcmp0_b32 s73, 4\n\ts_cbranch_scc1 ev_first1");
  GAP();
  phaseB<0, true>(c);
  asm volatile("s_branch att_loop\nev_first1:");
  phaseB<1, true>(c);
  asm volatile("s_branch att_mid\n9:");
  GAP();
}
DI void ev_call() { asm volatile("s_swappc_b64 s[86:87], s[82:83]" ::: "vcc", "scc", "memory"); GAP(); }

#define ACLOB_ALL "a0","a1","a2","a3","a4","a5","a6","a7","a8","a9","a10","a11","a12","a13","a14","a15","a16","a17","a18","a19","a20","a21","a22","a23","a24","a25","a26","a27","a28","a29","a30","a31","a32","a33","a34","a35","a36","a37","a38","a39","a40","a41","a42","a43","a44","a45","a46","a47","a48","a49","a50","a51","a52","a53","a54","a55","a56","a57","a58","a59","a60","a61","a62","a63","a64","a65","a66","a67","a68","a69","a70","a71","a72","a73","a74","a75","a76","a77","a78","a79","a80","a81","a82","a83","a84","a85","a86","a87","a88","a89","a90","a91","a92","a93","a94","a95","a96","a97","a98","a99","a100","a101","a102","a103","a104","a105","a106","a107","a108","a109","a110","a111","a112","a113","a114","a115","a116","a117","a118","a119","a120","a121","a122","a123","a124","a125","a126","a127","a128","a129","a130","a131","a132","a133","a134","a135","a136","a137","a138","a139","a140","a141","a142","a143","a144","a145","a146","a147","a148","a149","a150","a151","a152","a153","a154","a155","a156","a157","a158","a159","a160","a161","a162","a163","a164","a165","a166","a167","a168","a169","a170","a171","a172","a173","a174","a175","a176","a177","a178","a179","a180","a181","a182","a183","a184","a185","a186","a187","a188","a189","a190","a191","a192","a193","a194","a195","a196","a197","a198","a199","a200","a201","a202","a203","a204","a205","a206","a207","a208","a209","a210","a211","a212","a213","a214","a215","a216","a217","a218","a219","a220","a221","a222","a223","a224","a225","a226","a227","a228","a229","a230","a231","a232","a233","a234","a235","a236","a237","a238","a239","a240","a241","a242","a243","a244","a245","a246","a247","a248","a249","a250","a251","a252","a253","a254","a255"

struct EP { float fa[2], fb[2]; };
template <int I, int E, int MODE> DI void epi_pair(const EP& p) {
  constexpr int qb = I & 1, r = 16 * (I >> 1) + 4 * qb + (E >> 1) * 8 + (E & 1) * 2, t = 96 + 2 * E, w = 128 + 4 * I + E;
  if constexpr (MODE == 2) asm volatile("v_accvgpr_read_b32 v106, a[%c0]" ::"i"(w));
  asm volatile("v_accvgpr_read_b32 v%c0, a[%c2]\n\tv_accvgpr_read_b32 v%c1, a[%c3]" ::"i"(t), "i"(t + 1), "i"(r), "i"(r + 1));
  if constexpr (MODE) asm volatile("v_mul_f32 v%c0, v%c0, %2\n\tv_mul_f32 v%c1, v%c1, %2" ::"i"(t), "i"(t + 1), "v"(p.fa[qb]));
  if constexpr (MODE == 2)
    asm volatile("v_lshlrev_b32 v104, 16, v106\n\tv_and_b32 v105, 0xffff0000, v106\n\tv_fmac_f32 v%c0, v104, %2\n\tv_fmac_f32 v%c1, v105, %2"
                 ::"i"(t), "i"(t + 1), "v"(p.fb[qb]));
  asm volatile("v_cvt_pk_bf16_f32 v%c0, v%c1, v%c2" ::"i"(w), "i"(t), "i"(t + 1));
}
template <int I> DI void epi_store() {
  asm volatile("buffer_store_dwordx4 v[%c0:%c1], v84, s[88:91], s101 offen offset:%2 sc1" ::"i"(128 + 4 * I), "i"(131 + 4 * I), "i"((I & 3) * 1024) : "memory");
  if constexpr ((I & 3) == 3) asm volatile("s_addk_i32 s101, 4096");
}
constexpr int R_TB = 232, R_VS = 236;
template <int HF, int R> DI void p_read() {
  constexpr int mb = R >> 4, r = 192 - 64 * HF + 2 * R;
  asm volatile("ds_read_b64_tr_b16 a[%c1:%c2], v%c0 offset:%c3" ::"i"(R_TB + 2 * HF + mb), "i"(r), "i"(r + 1), "i"((R & 15) * 2048));
}
template <int QB, int E> DI void p_cvt() {
  asm volatile("v_cvt_pk_bf16_f32 v%c0, v%c1, v%c2" ::"i"(208 + 4 * QB + E), "i"(192 + 8 * QB + 2 * E), "i"(193 + 8 * QB + 2 * E));
}
template <int QB, int PR> DI void p_store() {
  asm volatile("buffer_store_dwordx4 v[%c0:%c1], v%c2, s[88:91], s96 offen offset:%3 sc1" ::"i"(208 + 4 * QB), "i"(211 + 4 * QB), "i"(R_VS + QB),
               "i"(PR * 128) : "memory");
}
constexpr int p_dma(int HF, int G) { return G == 3 && !HF ? 0 : G == 11 ? 1 - HF : G == 15 ? 2 - HF : G == 19 ? 3 - HF : G == 27 && HF ? 3 : -1; }
template <int HF, bool LD, int G> DI void p_gap() {
  constexpr int ch = G >> 3, t = G & 7, a = 192 - 64 * HF + 32 * (ch & 1) + 4 * t, b = 128 + 4 * (2 * t + (ch >> 1)), d = 192 + 4 * ch;
  if constexpr (t == 0)
    asm volatile("v_mfma_f32_16x16x32_bf16 v[%c0:%c1], a[%c2:%c3], v[%c4:%c5], 0" ::"i"(d), "i"(d + 3), "i"(a), "i"(a + 3), "i"(b), "i"(b + 3));
  else
    asm volatile("v_mfma_f32_16x16x32_bf16 v[%c0:%c1], a[%c2:%c3], v[%c4:%c5], v[%c0:%c1]" ::"i"(d), "i"(d + 3), "i"(a), "i"(a + 3), "i"(b), "i"(b + 3));
  p_read<HF ^ 1, G>();
  if constexpr (G == 0) asm volatile("v_xor_b32 v%c0, s100, v%c0" ::"i"(R_TB + 2 * HF + 1));
  if constexpr (G == 17) asm volatile("v_xor_b32 v%c0, s100, v%c0" ::"i"(R_TB + 2 * (HF ^ 1)));
  if constexpr (G >= 4 && G <= 8 && G != 7) p_cvt<1, (G < 7 ? G - 4 : 3)>();
  if constexpr (G == 24 || (G == 7 && HF == 0)) asm volatile("s_waitcnt lgkmcnt(8)");
  if constexpr (G == 7 && HF == 1) asm volatile("s_waitcnt vmcnt(%0) lgkmcnt(8)\n\ts_barrier" ::"i"(LD ? 13 : 5) : "memory");
  if constexpr (G == 10) p_store<1, HF ^ 1>();
  if constexpr (G >= 20 && G < 24) p_cvt<0, G - 20>();
  if constexpr (G == 25) p_store<0, HF>();
  if constexpr (LD && p_dma(HF, G) >= 0) dma_at<(HF ? 8 : 12) + p_dma(HF, G)>();
  if constexpr (LD && HF == 0 && G == 2) asm volatile("s_bitset0_b32 m0, 12");
  if constexpr (HF == 0 && G == 13) asm volatile("s_add_u32 s96, s96, 0x8000");
  if constexpr (HF == 0 && G == 26) asm volatile("s_add_u32 s98, s98, 128");
  if constexpr (LD && HF == 0 && G == 27) asm volatile("s_cmp_lt_u32 s98, 0x380");
  if constexpr (HF == 1 && G == 1) asm volatile("s_xor_b32 s100, s100, 0x10000");
  if constexpr (LD && HF == 1 && G == 2) asm volatile("s_xor_b32 s97, s97, s100");
  if constexpr (LD && HF == 1 && G == 9) asm volatile("s_or_b32 m0, s97, 0x1000");
  if constexpr (!LD && HF == 1 && G == 30) asm volatile("s_cmp_lt_u32 s98, 0x500");
  GAP();
}
template <int HF, bool LD, int... Gs> DI void p_half(iseq<Gs...>) { (p_gap<HF, LD, Gs>(), ...); }
template <int G> DI void gapF(AC& c, const EP& p) {
  if constexpr (G < 64) gapB<0, false, G>(c);
  if constexpr (G >= 10) epi_pair<(G - 10) / 4, (G - 10) & 3, 0>(p);
  if constexpr (G >= 14 && ((G - 14) & 3) == 0) epi_store<(G - 14) / 4>();
  if constexpr (G == 73) epi_store<15>();
  GAP();
}
template <int... Gs> DI void phaseF(AC& c, const EP& p, iseq<Gs...>) { (gapF<Gs>(c, p), ...); }
template <int G> DI void gapAL(AC& c) {
  constexpr int kb = G >> 3, s = G & 7, r = 192 + 4 * (G & 15), q = 160 + 4 * s, d = R_S + 36 + 8 * kb, ci = s == 0 ? R_CI + 4 + 8 * kb : d;
  if constexpr (G == 0) asm volatile("s_add_u32 s96, s97, 0x1000000\n\ts_mov_b32 m0, s79\n\ts_waitcnt vmcnt(9) lgkmcnt(0)\n\ts_barrier" ::: "memory");
  if constexpr (G >= 8 && (G & 3) == 0) asm volatile("s_waitcnt lgkmcnt(4)");
  if constexpr (G == 29) asm volatile("s_barrier" ::: "memory");
  asm volatile("v_mfma_f32_16x16x32_bf16 v[%c0:%c1], a[%c2:%c3], a[%c4:%c5], v[%c6:%c7]" ::"i"(d), "i"(d + 3), "i"(r), "i"(r + 3), "i"(q), "i"(q + 3),
               "i"(ci), "i"(ci + 3));
  if constexpr (G < 24) kread<1, G + 8>();
  else vread<0, G - 24>();
  if constexpr (G % 3 == 1 && G < 24) dma_at<G / 3>();
  if constexpr (G == 11) asm volatile("s_bitset1_b32 m0, 12");
  if constexpr (G == 28) asm volatile("buffer_load_dwordx4 v[122:125], v88, s[88:91], s76 offen sc1" ::: "memory");
  if constexpr (G == 30) asm volatile("s_xor_b32 m0, s79, 0x11000\n\ts_movk_i32 s98, 128");
  sm_gap<0, 64 + 2 * G>(c); sm_gap<0, 65 + 2 * G>(c);
  GAP();
}
template <int... Gs> DI void phaseAL(AC& c, iseq<Gs...>) { (gapAL<Gs>(c), ...); }
struct BLS { int ex[65], op[65], vm[65], su[65]; };
constexpr BLS bl_sched(bool LD) {
  BLS z{};
  z.op[0] = 1;
  for (int t = 0; t < 64; ++t) {
    const int g = t & 15, o = z.op[t];
    const bool x = t == 6 || (t < 48 && g > 8 && g != 12 && g != 15);
    const bool v = LD ? (t & 3) == 0 : (t & 11) == 8;
    const bool p = t >= 15 && o < 24 && (t & 3) != 0 && t != 47 && !x && z.ex[t - 1] > 2 * (o / 3) + (o % 3 != 0);
    z.ex[t + 1] = z.ex[t] + x; z.vm[t + 1] = z.vm[t] + v; z.op[t + 1] = o + p;
    z.su[t + 1] = z.su[t] + (t >= 1 && z.su[t] < (LD ? 16 : 10) && g != 15 && !v && !x && (LD || t > 55 || (t > 3 && !p)));
  }
  return z;
}
template <bool LD> constexpr BLS BL = bl_sched(LD);
static_assert(BL<true>.ex[64] == 16 && BL<true>.op[61] == 24 && BL<true>.vm[64] == 16 && BL<false>.vm[64] == 8);
static_assert(BL<true>.su[56] == 16 && BL<false>.su[62] == 10);
template <bool LD, int G> DI void gapBL(AC& c) {
  if constexpr (G == 48) asm volatile("s_waitcnt vmcnt(%0) lgkmcnt(0)\n\ts_barrier" ::"i"(BL<LD>.vm[48]) : "memory");
  gapB<1, false, G>(c);
  if constexpr (G >= 48 && G < 56) vread<1, G - 48>();
  if constexpr (BL<LD>.vm[G + 1] > BL<LD>.vm[G]) {
    constexpr int k = BL<LD>.vm[G] + (LD ? 0 : 16), I = 2 * (k & 7) + ((k >> 3) & 1);
    if constexpr (k < 16) {
      asm volatile("buffer_load_dwordx4 a[%c0:%c1], v84, s[88:91], s75 offen offset:%2 sc1" ::"i"(128 + 4 * I), "i"(131 + 4 * I), "i"((I & 3) * 1024) : "memory");
      if constexpr (k & 1) asm volatile("s_addk_i32 s75, %0" ::"i"(k == 7 ? -12288 : 4096));
    } else dma_at<k - 8>();
    if constexpr (k == 19) asm volatile("s_bitset0_b32 m0, 12");
  }
  if constexpr (G == 1 || G == 2) asm volatile("v_add_f32_e32 v%c0, v%c0, v%c1" ::"i"(R_RS + G - 1), "i"(R_TS + G - 1));
  if constexpr (G == 3) asm volatile("v_mov_b32 v%c0, 0" ::"i"(R_TS));
  if constexpr (BL<LD>.ex[G + 1] > BL<LD>.ex[G]) {
    constexpr int j = BL<LD>.ex[G];
    asm volatile("v_exp_f32 v%c0, v%c1" ::"i"(R_E + j), "i"(R_S + 36 + 8 * (j >> 2) + (j & 3)));
  }
  if constexpr (BL<LD>.op[G + 1] > BL<LD>.op[G]) {
    constexpr int o = BL<LD>.op[G], k = o / 3, j = 2 * k + o % 3;
    if constexpr (o % 3 == 2) asm volatile("v_cvt_pk_bf16_f32 v%c0, v%c1, v%c2" ::"i"(p_reg(1, (k >> 1) * 4 + 2 + (k & 1))), "i"(R_E + 2 * k), "i"(R_E + 2 * k + 1));
    else asm volatile("v_add_f32_e32 v%c0, v%c1, v%c2" ::"i"(R_TS + 1), "i"(j == 1 ? R_E : R_TS + 1), "i"(R_E + j));
  }
  if constexpr (G == 61) asm volatile("v_cmp_nle_f32_e64 s[54:55], v%c0, v%c1" ::"i"(R_TS + 1), "i"(R_THR));
  if constexpr (BL<LD>.su[G + 1] > BL<LD>.su[G]) {
    constexpr int k = BL<LD>.su[G];
    if constexpr (k < 4) asm volatile("v_xor_b32 v%c0, %1, %2" ::"i"(R_TB + k), "i"(32 * k), "v"(c.tb));
    else if constexpr (k < 6) asm volatile("v_add_u32 v%c0, %1, %2" ::"i"(R_VS + k - 4), "i"(4096 * (k - 4)), "v"(c.vs));
    else if constexpr (k == 6) tload<96, T_SLAB>();
    else if constexpr (k == 7) tload<101, T_CNT>();
    else if constexpr (k == 8) asm volatile("v_mov_b64 v[240:241], 1");
    else if constexpr (k == 9) asm volatile("s_mov_b32 s97, s79\n\ts_mov_b32 s100, 0x8000");
    else if constexpr (k < 14) asm volatile("v_mov_b32 v%c0, %1" ::"i"(k < 12 ? 214 + k : 218 + k), "v"(k & 1 ? c.o1 : c.o0));
    else asm volatile("v_mov_b64 v[%c0:%c1], 0" ::"i"(198 + 2 * k), "i"(199 + 2 * k));
  }
  GAP();
}
template <bool LD, int... Gs> DI void phaseBL(AC& c, iseq<Gs...>) { (gapBL<LD, Gs>(c), ...); }
template <int MODE, int G> DI void gapFL(const EP& p) {
  constexpr int x = G - (MODE == 3 ? 4 : 0), J = MODE == 1 ? (G < 24 && G % 3 == 1 ? G / 3 : -1) : G < 27 && G % 5 != 0 && G % 5 != 3 ? G / 5 * 3 + (G % 5) / 2 : -1;
  if constexpr (G == 0 && MODE == 1) asm volatile("s_or_b32 m0, s78, 0x1000\n\ts_movk_i32 s98, 256\n\ts_waitcnt lgkmcnt(0)");
  if constexpr (G == 0 && MODE > 1) asm volatile("s_waitcnt vmcnt(8) lgkmcnt(0)" ::: "memory");
  if constexpr (G == 16 && MODE > 1) asm volatile("s_waitcnt vmcnt(9)" ::: "memory");
  if constexpr (G >= 8 && G < 32 && (G & 3) == 0) asm volatile("s_waitcnt lgkmcnt(%0)" ::"i"(G < 28 ? 4 : 0));
  if constexpr (G < 32) {
    constexpr int o = 8 * (G >> 1) + 4, r = 192 + 4 * (G & 15), q = R_P + 20 + 8 * (G & 1);
    asm volatile("v_mfma_f32_16x16x32_bf16 a[%c0:%c1], a[%c2:%c3], v[%c4:%c5], a[%c0:%c1]" ::"i"(o), "i"(o + 3), "i"(r), "i"(r + 3), "i"(q), "i"(q + 3));
  }
  if constexpr (G < 24) vread<1, G + 8>();
  if constexpr (G == 28) asm volatile("s_waitcnt vmcnt(8)\n\ts_barrier\n\ts_or_b32 m0, s79, 0x1000\n\ts_movk_i32 s98, 384" ::: "memory");
  if constexpr (G == 29 || G == 31) dma_at<8 + (G - 29) / 2>();
  if constexpr (G == 32 || G == 40) {
    static_for([&](auto I) { p_read<0, 2 * (G - 32) + I>(); }, make_iseq<16>{});
    dma_at<10 + (G - 32) / 8>();
  }
  if constexpr (J >= 0) dma_at<8 + (J & 7)>();
  if constexpr (x >= 0) epi_pair<2 * ((x >> 2) & 7) + (x >> 5), x & 3, MODE>(p);
  if constexpr (MODE == 3 && G < 32) {
    constexpr int db = G & 15, qb = G >> 4, o = 4 * (db * 2 + qb), a = 224 + 4 * (db & 1), b = 128 + 4 * ((db >> 1) * 2 + qb);
    asm volatile("v_mfma_f32_16x16x32_bf16 a[%c0:%c1], v[%c2:%c3], a[%c4:%c5], a[%c0:%c1]" ::"i"(o), "i"(o + 3), "i"(a), "i"(a + 3), "i"(b), "i"(b + 3));
  }
  if constexpr (J == 3 || J == 11) asm volatile("s_bitset0_b32 m0, 12");
  if constexpr (J == 7 && MODE > 1) asm volatile("s_or_b32 m0, s78, 0x1000\n\ts_movk_i32 s98, 256");
  if constexpr (x == 63) asm volatile("s_waitcnt vmcnt(4) lgkmcnt(0)" ::: "memory");
  GAP();
}
template <int MODE, int... Gs> DI void phaseFL_(const EP& p, iseq<Gs...>) { (gapFL<MODE, Gs>(p), ...); }
template <int MODE> DI void phaseFL(const EP& p) { GAP(); phaseFL_<MODE>(p, make_iseq<MODE == 3 ? 68 : 64>{}); }
template <int U, int K> DI void r_load(unsigned vo, unsigned so) {
  asm volatile("buffer_load_dwordx4 a[%c0:%c1], %2, s[88:91], %3 offen offset:%4 sc1" ::"i"(64 * U + 4 * K), "i"(64 * U + 4 * K + 3), "v"(vo),
               "s"(so + (K >> 1) * 0x40000 + (U >> 1) * 4096), "i"((K & 1) * 512 + (U & 1) * 2048) : "memory");
}
template <int U, int K> DI void r_sum() {
  constexpr int d = 100 + 4 * (K & 1), b = 64 * U + 4 * K;
  if constexpr (K < 2) asm volatile("v_mfma_f32_16x16x32_bf16 v[%c0:%c1], v[96:99], a[%c2:%c3], 0" ::"i"(d), "i"(d + 3), "i"(b), "i"(b + 3));
  else asm volatile("v_mfma_f32_16x16x32_bf16 v[%c0:%c1], v[96:99], a[%c2:%c3], v[%c0:%c1]" ::"i"(d), "i"(d + 3), "i"(b), "i"(b + 3));
}
template <int U> DI void r_fin(unsigned vo, __amdgpu_buffer_rsrc_t srd, unsigned so) {
  asm volatile("" : "+s"(srd), "+s"(so));
  static_for([&](auto K) { r_sum<U, K>(); }, make_iseq<16>{});
  asm volatile("s_nop 7");
  static_for([&](auto E) { asm volatile("v_cvt_pk_bf16_f32 v%c0, v%c1, v%c2" ::"i"(116 + 4 * U + E), "i"(100 + 2 * E), "i"(101 + 2 * E)); }, make_iseq<4>{});
  asm volatile("buffer_store_dwordx4 v[%c3:%c4], %0, %1, %2 offen sc0 sc1" ::"v"(vo), "s"(srd), "s"(so), "i"(116 + 4 * U), "i"(119 + 4 * U) : "memory");
}

#define CMAX 12
#define BAL 0
#define NYE (BAL ? 16 : 15)
#define NYO (BAL ? 14 : 15)
#define NY0 15
struct TRow { unsigned v[64]; };
constexpr u64 kvmask(int m0, int m1) { u64 n = 0; for (int m = m0; m <= m1; ++m) n |= 0x101ull << m; return n; }
constexpr u64 k_order(bool isY, int rr) {
  const int cls = (rr + (rr >> 2)) & 3;
#define NCM 3
  u64 t = 0;
  int n = 0;
  auto put = [&](int k) { t |= (u64)k << (4 * n++); };
  if (isY) {
    for (int k = 1; k <= 4; ++k) put(NCM + 4 + (k & 3));
    for (int k = 1; k <= 4; ++k) put(NCM + 8 + (k & 3));
    for (int k = NCM + 12; k < 16; ++k) put(k);
    for (int k = NCM + 3; k >= 0; --k) put(k);
    return t;
  }
  for (int k = 0; k < NCM; ++k) put(k);
  for (int k = 0; k < 3; ++k) { put(NCM + 4 * k + cls); put(NCM + ((cls + k + 1) & 3)); }
  for (int k = 1; k < 4; ++k) put(NCM + 4 + ((cls + k) & 3));
  for (int k = 1; k < 4; ++k) put(NCM + 8 + ((cls + k) & 3));
  for (int k = NCM + 12; k < 16; ++k) put(NCM == 2 ? k ^ (cls & 1) : k);
  return t;
}
static_assert(NCM >= 3);
constexpr TRow t_row(int b, int wid) {
  TRow t{};
  const int j = b & 7, r = b >> 3, rr = r & 15, hsel = rr & 1, pj = rr >> 1, kv = pj & 1, h = j + 8 * hsel, u = hsel * 8 + j;
  const bool isY = r >= 16, noA = pj == 7, slow = BAL && (j & 1);
  const int pa = slow && (pj == 0 || pj == 7) ? 7 - pj : pj;
  const bool solo = pa == 7;
  const int jF = isY || !BAL ? j : slow && pa == 0 ? j - 1 : !slow && pa == 6 ? j + 1 : j, hF = jF + 8 * hsel;
  const int qbF = isY ? (solo ? (slow ? 6 : 7) : 15 - pa) : solo ? 8 : !BAL ? pa : slow ? (pa ? pa - 1 : 0) : pa + 1;
  const int mt = (rr >> 2) + (isY ? 4 : 0), g = j * 128, gF = jF * 128, NY = !(j & 1) ? NYE : pa ? NYO : NY0;
  const int nx0 = solo ? 0 : 32 - 2 * pa - NY, cx = nx0 < CMAX ? nx0 : CMAX, ex = nx0 - cx, qb0 = solo ? qbF : 15 - pa;
  const unsigned oQ = (unsigned)h << 20, oK = oQ + O_K, oP = O_P + (h * 8 + pa) * PSTR, oQF = (unsigned)hF << 20;
  if (wid == 4) {
    for (int l = 0; l < 64; ++l)
      t.v[l] = fo(l < 16 ? g + hsel * 16 + l : l == 16 ? g + 32 + hsel * 8 + (qb0 >> 1) : l == 17 ? gF + 32 + hsel * 8 + (qbF >> 1) :
                  l < 26 ? gF + hsel * 16 + ((l - 18) >> 2) * 8 + ((l - 18) & 3) : l == 26 ? g + 48 + hsel * 8 + pa : l < 32 ? g + 64 + hsel * 8 + pa :
                  l < 48 ? 2304 + pj * 16 + l - 32 : 2048 + pj * 16 + l - 48);
    return t;
  }
  auto put64 = [&](int i, u64 x) { t.v[i] = (unsigned)x; t.v[i + 1] = (unsigned)(x >> 32); };
  t.v[T_Q0] = oQ + (qb0 * 128 + wid * 16) * 512;
  t.v[T_K0] = oK + (isY ? cx : 0) * 32768;
  t.v[T_OQ] = oQ + pj * 131072 + (wid >> 1) * 65536 + (wid & 1) * 4096;
  t.v[T_OG] = oK + kv * TSB + mt * 131072 + (wid >> 1) * 65536 + (wid & 1) * (kv ? 8192 : 4096);
  t.v[T_FPREP] = fo(2048 + pj * 16 + u);
  t.v[T_FQ] = fo(g + 32 + hsel * 8 + pj);
  t.v[T_FG] = fo(g + hsel * 16 + kv * 8 + mt);
  t.v[T_FS] = fo(1024 + qbF * 16 + jF * 2 + hsel);
  t.v[T_CNT] = fo(2560 + qbF * 16);
  t.v[T_ZO] = O_Z + pj * 64 * SEQ * 2 + (u * 4 + wid) * 4096;
  const u64 own = 0x1ull << (kv * 8 + mt), conv = noA ? 0 : 0xffffull << 32;
  put64(T_NPRE, 0x10000ull | (isY ? (1ull << (cx >> 2)) | (1ull << ((cx + 1) >> 2)) : noA ? 0 : 0xffffull << 48));
  put64(T_NST, isY ? kvmask(cx >> 2, 3) | (solo ? 0 : 1ull << 27) : kvmask(0, 0));
  put64(T_TAB, k_order(isY, rr));
  t.v[T_SLAB] = O_S + qbF * 0x200000 + jF * 0x40000 + wid * 8192 + hsel * 1024;
  t.v[T_PART] = oP;
  const unsigned junk = fo(g + 96 + r), fconv = noA ? junk : fo(2304 + pj * 16 + u);
  for (int it = isY || solo; it < 2; ++it) {
    unsigned* p = t.v + T_IT + it * T_ITN;
    const int n = !it ? nx0 : isY && !solo ? NY : 2 * qbF + 2;
    u64 q[4] = {0, 0, 0, 0};
    unsigned jmp[3] = {0, 0, 0}, chk = 0;
    p[14] = junk;
    if (isY) {
      p[0] = 2; p[1] = 2; p[13] = t.v[T_FG];
      if (!solo) {
        chk = 1u << (14 - cx); p[0] |= chk; q[1] = kvmask(4, (31 - 2 * pa) >> 2) & ~own; jmp[1] = ex * 32768;
        chk |= 1u << (n - 2); q[2] = 1ull << 26;
      }
    } else if (it == 0) {
      chk = p[0] = 2u | (ex ? 1u << (cx - 2) : 0) | 1u << (n - 2);
      chk |= noA || qbF > 1 ? 0 : 1u << n;
      q[0] = kvmask(0, (cx - 1) >> 2);
      if (ex) { q[1] = kvmask(4, 4 + ((ex - 1) >> 2)); jmp[1] = (16 - cx) * 32768; }
      jmp[ex ? 2 : 1] = oQF - oQ - (ex ? 16 + ex : cx) * 32768;
      q[ex ? 2 : 1] = 0x20000ull | ((0x22ull << ((2 * qbF + 1) >> 2)) - 0x11ull) << 18;
      q[ex ? 3 : 2] = conv;
      p[1] = 2; p[13] = fconv; p[14] = fo(g + 64 + hsel * 8 + pa);
    } else if (solo) {
      chk = p[0] = 0x402; p[1] = 2; p[13] = fconv;
      q[0] = kvmask(0, 2); q[1] = kvmask(3, 4) | conv;
    } else {
      p[0] = p[1] = n > 4 ? 2 : 1; p[13] = fo(g + 48 + hsel * 8 + pa);
      chk = noA ? 0 : p[0];
      q[0] = conv;
    }
    p[2] = jmp[0]; p[4] = jmp[1]; p[15] = jmp[2];
    p[3] = !it ? 0 : n > 2 ? 1u << (n - 3) : 1u << 30;
    for (int k = 0; k < 4; ++k) { p[5 + 2 * k] = (unsigned)q[k]; p[6 + 2 * k] = (unsigned)(q[k] >> 32); }
    const int rk = (r >> 1) - (BAL && (r >> 1) > (slow ? 7 : 6));
    p[16] = 32 | (isY && !solo ? 2 : 0) | (n == 2 ? 4 : 0) | (isY || noA ? 0 : 8) | (n & 1) << 4 | (n - 1 - it) << 8 | (rk * 4 + wid) << 16 | hF << 24 | qbF << 28;
    p[17] = !it ? 0 : n > 2 ? 1u << (n - 2) : 1u << 31;
    p[18] = it ? oP + wid * 16384 : oQF + (qbF * 128 + wid * 16) * 512;
    p[19] = oP + (it ? 1032 : 1024);
    p[20] = chk >> 1;
    p[21] = (it ? oQF + O_K : oK) + (isY ? cx : 0) * 32768;
    p[22] = ((p[0] | p[3] | p[17]) & 0x3fffffffu) << 1;
  }
  return t;
}
struct TBlk { TRow row[5]; };
template <int B> constexpr TBlk t_blk = {{t_row(B, 0), t_row(B, 1), t_row(B, 2), t_row(B, 3), t_row(B, 4)}};
struct Tab { TBlk blk[256]; };
template <int... I> constexpr Tab t_make(iseq<I...>) { return Tab{{t_blk<I>...}}; }
__device__ const Tab g_tab = t_make(make_iseq<256>{});

template <int R> DI const bf16* sptr() { const bf16* p; asm volatile("s_mov_b64 %0, s[%c1:%c2]" : "=s"(p) : "i"(R), "i"(R + 1)); return p; }
template <class T, int I> DI T* arg_late() {
  unsigned lo, hi;
  asm volatile("v_readlane_b32 %0, v77, %2\n\tv_readlane_b32 %1, v77, %3\n\ts_nop 4" : "=s"(lo), "=s"(hi) : "i"(2 * I), "i"(2 * I + 1));
  return (T*)(((u64)hi << 32) | lo);
}
__global__ __launch_bounds__(256, 1) __attribute__((amdgpu_num_vgpr(77), amdgpu_num_sgpr(56))) void fused_kernel(
    const bf16* ckv_, const bf16* wuk_, const bf16* wuv_, const bf16* x_, const bf16* wq_, bf16* ws_, const bf16* ak_, const bf16* av_,
    const bf16* ash_, const bf16* assmk_, const bf16* assmD_, const bf16* wo_, const bf16* aq_, bf16* out_) {
  extern __shared__ __attribute__((aligned(16))) char lds[];
  const void* kap = (const void*)__builtin_amdgcn_kernarg_segment_ptr();
  asm volatile("at_code_begin:\n\ts_load_dwordx16 s[80:95], %0, 0\n\ts_load_dwordx2 s[96:97], %0, 0x40" ::"s"(kap) : ACLOB_ALL, "v255", "s101");
  asm volatile("s_getpc_b64 s[48:49]\nct_base:\n\ts_mov_b32 s50, pf_code_end-ct_base\n\ts_mov_b32 s51, 0x20000\n\ts_lshr_b32 s52, %1, 3\n\t"
               "v_and_b32 v116, 63, %0\n\tv_lshrrev_b32 v117, 6, %0\n\tv_lshl_add_u32 v116, v116, 7, v117\n\ts_lshl_b32 s52, s52, 9\n\t"
               "v_lshlrev_b32 v116, 7, v116\n\tbuffer_load_dword v117, v116, s[48:51], s52 offen" ::"v"(threadIdx.x), "s"(blockIdx.x) : "scc", "memory");
  GAP();
  const int tid = threadIdx.x, lane = tid & 63;
  const int wid = __builtin_amdgcn_readfirstlane(tid >> 6);
  const int b = blockIdx.x, j = b & 7, r = b >> 3, rr = r & 15, hsel = rr & 1, pj = rr >> 1, kv = pj & 1;
  const bool isY = r >= 16, doP = isY && pj != 7, doA = !isY && pj != 7;
  const int h = j + 8 * hsel, mt = (rr >> 2) + (isY ? 4 : 0), pu = (hsel * 8 + j) * 4 + wid;
  constexpr unsigned m0d = 131072 + 24576;
  const unsigned lA = isY ? 65536 : 0, lW = lA ^ 65536, po = isY ? 0 : 32768;
  unsigned mA = lA + wid * 1024 + po, mW = lW + wid * 1024 + po, ofA = isY ? pj * 256 * DA * 2 : mt * 256 * DC * 2, ofW = h * 512;
  const unsigned lda = isY ? DA : DC;
#define ROT 1
  unsigned g0 = (!ROT ? 0 : isY ? pj + 2 * hsel : rr + (rr >> 2)) & 3;
  {
    asm volatile("global_load_dword v78, %0, %2\n\tglobal_load_dword v79, %1, %2 offset:1024\n\tglobal_load_dword v77, %3, %4"
                 ::"v"(tid * 4), "v"(lane * 4), "s"(&g_tab.blk[b]), "v"((lane < 27 ? lane : 27) * 4), "s"(kap) : "memory");
    const unsigned ra = wid * 8 + (lane >> 3), sa = ((lane & 7) ^ (((wid & 1) * 4 + (lane >> 4)) & 7)) * 16;
    const unsigned vbA = ra * (lda * 2) + sa, vbB = (4 * wid + (lane >> 4)) * LDW * 2 + 16 * ((lane & 15) ^ (((lane >> 4) << 2) | wid));
    vsetu<92>(vbA); vsetu<93>(vbB); vsetu<94>(ra * (DC * 2) + sa);
    sset<65>(128 * lda);
    asm volatile("s_mov_b32 s74, 0x40000\n\ts_mov_b32 s53, 0x80000");
    sset<58>(256 * lda * 2); sset<62>((lda - 1) * LDW * 2 + 512);
    asm volatile("s_mov_b32 s59, 0x20000\n\ts_mov_b32 s63, 0x20000");
    g_pro<1>(g0, mA, mW, lda);
    asm volatile("" : "+s"(ofA), "+s"(ofW), "+s"(mA), "+s"(mW), "+s"(g0));
    GAP();
  }
  asm volatile("s_bitcmp1_b32 %0, 4\n\ts_waitcnt lgkmcnt(0)\n\ts_cselect_b64 s[60:61], s[84:85], s[82:83]\n\ts_bitcmp1_b32 %0, 7\n\t"
               "s_cselect_b64 s[60:61], s[88:89], s[60:61]\n\ts_cselect_b64 s[56:57], s[86:87], s[80:81]\n\t"
               "s_add_u32 s56, s56, %1\n\ts_addc_u32 s57, s57, 0\n\ts_add_u32 s60, s60, %2\n\ts_addc_u32 s61, s61, 0\n\t"
               "s_mov_b64 s[88:89], s[90:91]\n\ts_mov_b32 s90, %3\n\ts_mov_b32 s91, 0x20000" ::"s"(b), "s"(ofA), "s"(ofW), "i"(WSB) : "scc");
  KST(0, 0);
  AC c;
  {
    GM g;
    PR pr;
    const int ptb = hsel * 8 + j;
    if (doP) prep_load(pr, tid, pj, ptb, sptr<92>(), sptr<94>(), sptr<96>());
    g_pro<2>(g0, mA, mW, lda);
    g_pro(g0 + 1, mA, mW, lda);
    sset<64>(64 * lda); sset<67>(256 * lda); sset<68>(320 * lda);
    asm volatile("s_mov_b32 s71, 0x20000\n\ts_movk_i32 s72, 0x100\n\ts_mov_b32 s73, 0x20100");
    asm volatile("buffer_load_dwordx2 v[120:121], off, s[88:91], %0 sc1" ::"s"(fo(6144)) : "memory");
    GAP();
    {
      const int fr = lane & 15, fq = lane >> 4, q = (lane & 15) >> 2, p = lane & 3;
      vsetu<128>(lA + (wid >> 1) * 16384 + (fr >> 3) * 1024 + (fr & 7) * 128 + ((fq ^ (fr >> 1)) * 16));
      vsetu<129>(lW + (wid & 1) * 8192 + off_b(8 * fq + q, p >> 1) + 8 * (p & 1));
      vsetu<130>(lW + (wid & 1) * 8192 + off_b(8 * fq + 4 + q, p >> 1) + 8 * (p & 1));
      const unsigned one = fq != q ? 0u : (p & 1) ? 0x3f800000u : 0x3f80u;
      vsetu<131>((p & 2) ? 0u : one); vsetu<132>((p & 2) ? one : 0u);
      vsetu<133>((lane >> 2) * 128 + 16 * ((p >> 1) ^ ((lane >> 2) & 6)) + 8 * (p & 1));
      vsetu<134>((fr >> 3) * 2048 + (fq & 1) * 512 + (fq >> 1) * 256 + (fr & 7) * 16);
      vsetu<81>(tid == 0 ? 0u : 0x80000000u);
      vsetu<84>(lane * 16); vsetu<85>(lane * 16 + 65536); vsetu<86>(wid * 8192 + lane * 16); vsetu<87>(wid * 8192 + lane * 16 + 4096);
      vsetu<88>(65536 + wid * 256 + fr * 16); vsetu<89>(16 * wid + fr - 4 * fq);
      asm volatile("s_mov_b64 s[92:93], s[88:89]\n\ts_mov_b64 s[94:95], s[90:91]\n\ts_mov_b32 s75, 0x60000\n\t"
                   "s_mov_b32 s76, 0x40100\n\ts_mov_b32 s77, 0x60100\n\ts_mov_b32 s82, 2");
      sset<66>(192 * lda); sset<69>(384 * lda); sset<70>(448 * lda);
      sset<78>(lA + wid * 1024); sset<79>(lW + wid * 1024);
    }
    GAP();
    if (doP) {
      asm volatile("s_waitcnt vmcnt(11)" ::: "memory");
#pragma unroll
      for (int q = 0; q < 7; ++q) asm volatile("" : "+v"(pr.kk[q]));
#pragma unroll
      for (int q = 0; q < 4; ++q) asm volatile("" : "+v"(pr.vv[q]));
      static_for([&](auto Ic) __attribute__((always_inline)) {
        constexpr int I = Ic;
        if constexpr (I < 2) g_pro(g0 + 2 + I, mA, mW, lda);
        if constexpr (I == 2) g_pro2(mA, mW);
        GAP();
        prep_math<I>(pr, tid, lds + 131072);
        GAP();
      }, make_iseq<4>{});
    } else { g_pro(g0 + 2, mA, mW, lda); g_pro(g0 + 3, mA, mW, lda); g_pro2(mA, mW); }
    GAP();
    g.aA[0] = vxor<128, 0>(); g.aA[1] = vxor<128, 64>();
    static_for([&](auto I) { g.aB[I >> 3][I & 7] = vxor<129 + (I >> 3), 32 * (I & 7)>(); }, make_iseq<16>{});
    c.o0 = vxor<131, 0>(); c.o1 = vxor<132, 0>(); c.tb = vxor<133, 0>(); c.vs = vxor<134, 0>();
    asm volatile("v_add_u32 v91, 0x100000, v93\n\ts_waitcnt vmcnt(8) lgkmcnt(0)\n\ts_barrier" ::: "memory");
    if (isY) g_read0<0>(g, make_iseq<16>{});
    else g_read0<1>(g, make_iseq<16>{});
    GAP();
    asm volatile("v_readfirstlane_b32 s48, v120\n\tv_readfirstlane_b32 s49, v121\n\tv_readlane_b32 s80, v78, %0\n\tv_readlane_b32 s81, v78, %1\n\t"
                 "s_mov_b32 s50, 0x4c957f2d\n\ts_mul_i32 s84, s48, s50\n\ts_mul_hi_u32 s85, s48, s50\n\ts_mul_i32 s49, s49, s50\n\t"
                 "s_mul_i32 s48, s48, 0x5851f42d\n\ts_add_u32 s85, s85, s49\n\ts_add_u32 s85, s85, s48\n\ts_add_u32 s84, s84, 0xf767814f\n\t"
                 "s_addc_u32 s85, s85, 0x14057b7e\n\ts_lshr_b64 s[80:81], s[80:81], 12\n\tv_mov_b32 v82, s84\n\tv_mov_b32 v83, s85"
                 ::"i"((int)T_TAB), "i"(T_TAB + 1) : "scc");
    if (isY) asm volatile("s_mov_b64 s[80:81], 0x6543");
    if (doP) prep_store(tid, pj, ptb, (bf16*)sptr<88>() + O_X / 2, lds + 131072);
    asm volatile("s_waitcnt lgkmcnt(0)" ::: "memory");
    GAP();
    if (isY) {
      g_tile<false, 0, 1>(g);
      g_loop<false, true>(g, doP ? 0x22u : 0x20u, [&](int t) __attribute__((always_inline)) {
        if (t == 1) fl_set<T_FPREP>();
        else if (t == 4) { tload<80, T_TAB>(); tload<81, T_TAB + 1>(); srd_set<60>((kv ? arg_late<const bf16, 2>() : arg_late<const bf16, 1>()) + h * 256, (DC - 1) * LDW * 2 + 512); }
        else {
          srd_set<56>(arg_late<const bf16, 0>() + (size_t)mt * 256 * DC, 256 * DC * 2);
          asm volatile("v_mov_b32 v92, v94");
          static_for([&](auto J) { asm volatile("s_mov_b32 s%c0, %1" ::"i"(64 + J), "i"((J + 1) * 64 * DC)); }, make_iseq<7>{});
        }
      });
      tload<101, T_OQ>(); sset<83>(__float_as_uint(CS));
      asm volatile("v_mov_b32 v112, v84\n\tv_add_u32 v113, 8192, v84");
      GAP();
      g_tile<false, 0, 2>(g);
      KST(0, 3);
    }
#define TT 3
    auto kvtile = [&](auto TRc) __attribute__((always_inline)) {
      constexpr bool TR = TRc;
      if (isY) g_tile<TR, 1, 4>(g);
      else g_tile<TR, 1, 1>(g);
      g_loop<TR, false>(g, 0x2000u | (isY || doA ? 2u : BAL && (j & 1) ? 0u : 1u << TT), [&](int t) __attribute__((always_inline)) {
        int l = threadIdx.x & 63; asm volatile("" : "+v"(l));
        if (t == 13) {
          asm volatile("s_mov_b32 s79, 0x28000");
          fl_poll<T_NPRE>();
          if ((tget<T_IT + T_ITN + 16>() & 0xf000000) == 0)
            asm volatile("v_readlane_b32 s48, v78, %0\n\tv_and_b32 v122, 0xffffff00, v82\n\tv_mov_b32 v123, v83\n\ts_nop 2\n\t"
                         "buffer_store_dwordx2 v[122:123], v81, s[88:91], s48 offen sc1" ::"i"((int)T_CNT) : "memory");
          if (!doA && !isY) {
            CODE_TOUCH("at_code_begin", "pf_code_end", hsel * 8 + wid * 2, 16u, 64, m0d);
            CODE_TOUCH("at_code_begin", "pf_code_end", hsel * 8 + wid * 2 + 1, 16u, 64, m0d);
          }
          if (doA) {
            const char* tp = arg_late<const char, 9>() + (size_t)(pj * 64 + pu) * (SEQ * 2) + (l & 31) * 128;
            asm volatile("s_mov_b32 m0, %1\n\ts_nop 0\n\tglobal_load_lds_dword %0, off" ::"v"(tp), "i"(m0d) : "memory");
          }
        } else if (isY) fl_set<T_FQ>();
        else if (doA) post_dma(pj, pu, arg_late<const bf16, 12>(), 131072 + wid * 8192);
        else {
          const char* tp = arg_late<const char, 0>() + 1024 * DC * 2 + (pu * 256 + l) * 128;
          asm volatile("s_mov_b32 m0, %0\n\ts_nop 0" ::"i"(m0d));
#pragma unroll
          for (int i = 0; i < (BAL ? 8 : 4); ++i) asm volatile("global_load_lds_dword %0, off" ::"v"(tp + (i & 3) * 8192 + (i >> 2) * 131072) : "memory");
        }
      });
      tload<101, T_OG>(); tload<96, T_Q0>(); tload<98, T_K0>();
      fl_check();
      tload<50, T_NST>(); tload<51, T_NST + 1>();
      asm volatile("v_mov_b32 v112, v84\n\tv_add_u32 v113, %0, v84\n\tv_mov_b32 v90, v87\n\tv_mov_b32 v91, v87\n\tv_mov_b32 v92, v87\n\tv_mov_b32 v93, v87\n\t"
                   "v_mov_b32 v94, v86\n\tv_mov_b32 v95, v86\n\tv_mov_b32 v126, v86\n\tv_mov_b32 v127, v86\n\ts_mov_b32 s99, 0x8000\n\t" FL_ARM("s[50:51]")
                   ::"i"(TR ? 4096 : 8192));
      if (doA) {
        const int d = pj * 64 + pu;
        const bf16* dp = arg_late<const bf16, 10>() + d;
        asm volatile("global_load_ushort v123, %0, off" ::"v"(dp) : "memory");
        srd_set<56>(arg_late<const bf16, 9>() + (size_t)d * SEQ, SEQ * 2);
        srd_set<60>(arg_late<const char, 5>() + O_X + d * SEQ * 2, SEQ * 2);
        sset<78>(65536 + wid * 15488);
        asm volatile("s_movk_i32 s64, 992\n\tv_subrev_u32 v122, 32, v84");
        GAP();
        g_tile<TR, 0, 3>(g);
      } else {
        sset<79>(isY ? wid * 8192 : 0x30000);
        if (tget<T_IT + T_ITN + 16>() & 16) asm volatile("s_add_u32 s98, s98, 0x8000\n\ts_mov_b32 s97, 0xffff8000" ::: "scc");
        else asm volatile("s_mov_b32 s97, 0x8000");
        GAP();
        g_tile<TR, 0, 5>(g);
      }
    };
    if (kv) kvtile(std::true_type{});
    else kvtile(std::false_type{});
  }
  KST(0, 4);
  asm volatile("s_waitcnt vmcnt(16)" ::: "memory");
  const unsigned md0 = tget<T_IT + 16>(), md1 = tget<T_IT + T_ITN + 16>();
  sset<79>(wid * 8192 + (md0 & 16) * 2048);
  if (doA) {
    const float Dd = bfl(__float_as_uint(vget<123>()));
    KST(0, 5);
    conv_wave<T_FG>(O_Z + pj * 64 * SEQ * 2 + ((pu & 7) * 8 + (pu >> 3)) * 64, pu >> 3, Dd, lds + 65536 + wid * 15488);
    const char* tp = arg_late<const char, 11>() + (size_t)h * (HD * DA * 2) + ((pj * 4 + wid) * 64 + lane) * 128;
    asm volatile("s_mov_b32 m0, %1\n\ts_nop 0\n\tglobal_load_lds_dword %0, off\n\ts_waitcnt vmcnt(17)" ::"v"(tp), "i"(m0d) : "memory");
    KST(0, 6);
  } else if (!isY) {
    static_for([&](auto I) { d_store<16 + I>(); }, make_iseq<16>{});
    tload<98, T_K0>();
    asm volatile("s_waitcnt vmcnt(0)\n\ts_barrier" ::: "memory");
    fl_set<T_FG>();
    KST(0, 5);
    fl_check();
    CODE_TOUCH("att_begin", "pf_code_end", hsel * 4 + wid, 8u, 64, m0d);
    dma4k<0>(); dma4k<1>(); dma4k<2>(); dma4k<3>();
    asm volatile("s_waitcnt vmcnt(0)" ::: "memory");
    KST(0, 6);
  } else {
    tload<76, T_PART>();
    fl_check();
    asm volatile("buffer_load_dwordx2 v[122:123], v88, s[88:91], s76 offen offset:1024 sc1" ::: "memory");
  }
  sset<78>(65536 + wid * 8192); sset<79>(98304 + wid * 8192);
  { const bf16* wp = arg_late<const bf16, 11>() + ((md1 >> 24) & 15) * HD * DA; asm volatile("s_mov_b64 s[80:81], %0" ::"s"(wp)); }
  asm volatile("att_begin:");
  ev_code(c);
  asm volatile("s_waitcnt lgkmcnt(0)\n\ts_barrier" ::: "memory");
#pragma unroll 1
  for (int it = md0 ? 0 : 1; it < 2; ++it) {
    const unsigned mode = it ? md1 : md0;
    const bool odd = mode & 16;
    if (odd) kreads<1>(make_iseq<8>{});
    else kreads<0>(make_iseq<8>{});
    const unsigned oP = tget<T_PART>();
    if (it == 0) {
      static_for([&](auto I) { tload<57 + I, T_IT + I>(); }, make_iseq<21>{});
      tload<S_T, T_IT + 21>(); tload<S_EV, T_IT + 22>();
    } else {
      static_for([&](auto I) { tload<57 + I, T_IT + T_ITN + I>(); }, make_iseq<21>{});
      tload<S_T, T_IT + T_ITN + 21>(); tload<S_EV, T_IT + T_ITN + 22>();
    }
    asm volatile("s_add_u32 s98, s97, 0x8000\n\ts_mov_b32 s56, -1\n\tv_mov_b64 v[114:115], 0\n\tv_mov_b64 v[118:119], 0\n\t"
                 "v_mov_b32 v120, 0x45800000" ::: "scc");
    KST(0, 8 + 3 * (it && !isY));
    GAP();
#define STA(k)
#define STI()
#define LOOP_ENDS "s_bfe_u32 s76, s73, 0x80008"
    STA(0);
    if (odd) {
      if (isY) phaseA<1, 2>(c);
      else phaseA<1, 1>(c);
      STA(1);
      STA(2);
      phaseB0<1>(mode, isY);
      STA(3);
      asm volatile(LOOP_ENDS "\n\ts_branch att_mid" ::: "scc");
    } else {
      if (isY) phaseA<0, 2>(c);
      else phaseA<0, 1>(c);
      STA(1);
      if (__builtin_expect(mode & 4, 0)) { asm volatile("s_mov_b32 s56, 30"); ev_call(); asm volatile("s_mov_b32 s56, 31"); ev_call(); acc_zero(); }
      STA(2);
      phaseB0<0>(mode, isY);
      if (__builtin_expect(mode & 4, 0)) { ev_call(); asm volatile("s_branch att_done"); }
      STA(3);
    }
    GAP();
    asm volatile(LOOP_ENDS "\natt_loop:" ::: "scc");
    GAP();
    phaseA<1, 0>(c);
    STI();
    if (__builtin_expect(c.rare != 0, 0)) sm_redo<1>();
    phaseB<1>(c);
    asm volatile("s_cbranch_scc1 att_done\natt_mid:");
    phaseA<0, 0>(c);
    STI();
    if (__builtin_expect(c.rare != 0, 0)) sm_redo<0>();
    phaseB<0>(c);
    asm volatile("s_cbranch_scc0 att_loop\natt_done:");
    GAP();
    STA(4);
    if (it == 0) phaseA<0, 3>(c);
    else {
      sset<76>(oP);
      GAP();
      phaseAL(c, make_iseq<32>{});
      if (__builtin_expect(redo(), 0)) sm_redo<1, false>();
      if (mode & 2) phaseBL<true>(c, make_iseq<64>{});
      else phaseBL<false>(c, make_iseq<64>{});
    }
    if (__builtin_expect(redo(), 0)) {
      if (it) static_for([&](auto I) { vset<R_S + 32 + (I >> 2) * 8 + (I & 3)>(-INFINITY); }, make_iseq<16>{});
      asm volatile("s_nop 7");
      sm_redo<0, false>();
    }
    STA(5);
    KST(0, 9 + 3 * (it && !isY));
    float l[2];
    asm volatile("v_add_f32 %0, v%c2, v%c3\n\tv_add_f32 %1, v%c4, v%c5" : "=v"(l[0]), "=v"(l[1]) : "i"(R_RS), "i"(R_TS), "i"(R_RS + 1), "i"(R_TS + 1));
    const float cm[2] = {vget<R_M>(), vget<R_M + 1>()};
    l[0] = row_reduce<false>(l[0]); l[1] = row_reduce<false>(l[1]);
    EP p;
    if (it == 0) {
      const f32x4 ml = {cm[0], l[0], cm[1], l[1]};
      asm volatile("buffer_store_dwordx4 %0, v88, s[88:91], %1 offen sc1\n\ts_mov_b32 s101, %2" ::"v"(ml), "s"(oP), "s"(oP + wid * 16384) : "memory");
      phaseF(c, p, make_iseq<74>{});
      asm volatile(FL_POLL "\n\ts_waitcnt vmcnt(17)" ::: "memory");
    } else {
      if (!(mode & 2)) {
        p.fa[0] = __builtin_amdgcn_rcpf(l[0]); p.fa[1] = __builtin_amdgcn_rcpf(l[1]);
        phaseFL<1>(p);
      } else {
        const f32x4 ml = {vget<122>(), vget<123>(), vget<124>(), vget<125>()};
        if (__builtin_expect(__builtin_amdgcn_ballot_w64(ml[0] != cm[0] || ml[2] != cm[1]) == 0, 1)) {
          p.fa[0] = __builtin_amdgcn_rcpf(l[0] + ml[1]); p.fa[1] = __builtin_amdgcn_rcpf(l[1] + ml[3]);
          phaseFL<3>(p);
        } else {
#pragma unroll
          for (int e = 0; e < 2; ++e) {
            const float mm = fmaxf(cm[e], ml[2 * e]);
            const float wa = __builtin_amdgcn_exp2f(cm[e] - mm), wb = __builtin_amdgcn_exp2f(ml[2 * e] - mm);
            const float inv = __builtin_amdgcn_rcpf(l[e] * wa + ml[2 * e + 1] * wb);
            p.fa[e] = wa * inv; p.fb[e] = wb * inv;
          }
          phaseFL<2>(p);
        }
      }
    }
    KST(0, 10 + 3 * (it && !isY));
  }
  asm volatile(TICKET "\n\tv_xor_b32 v%c0, s100, v%c0\np_loop:" ::"i"(R_TB) : "memory");
  GAP();
  p_half<0, true>(make_iseq<32>{});
  asm volatile("s_cbranch_scc0 p_mid");
  p_half<1, true>(make_iseq<32>{});
  asm volatile("s_branch p_loop\np_loop2:");
  p_half<0, false>(make_iseq<32>{});
  asm volatile("p_mid:");
  p_half<1, false>(make_iseq<32>{});
  asm volatile("s_cbranch_scc1 p_loop2\n\ts_nop 5");
  GAP();
  p_cvt<1, 0>(); p_cvt<1, 1>(); p_cvt<1, 2>(); p_cvt<1, 3>();
  p_store<1, 1>();
  GAP();
  KST(0, 27);
  {
    constexpr unsigned rdy = 98304;
    int lz = threadIdx.x & 63; asm volatile("" : "+v"(lz));
    const int fr = lz & 15, fq = lz >> 4, qbF = md1 >> 28;
    if (wid == 0) {
#pragma unroll 1
      for (int q = 0; q < 0x100000; ++q) {
        unsigned lo, hi, tl, th;
        asm volatile("v_readfirstlane_b32 %0, v240\n\tv_readfirstlane_b32 %1, v241\n\ts_mov_b32 %2, s84\n\ts_mov_b32 %3, s85" : "=s"(lo), "=s"(hi), "=s"(tl), "=s"(th));
        if (__builtin_expect((((lo ^ tl) >> 8) | (hi ^ th)) == 0, 1)) break;
        asm volatile("s_sleep 4\n\tv_mov_b64 v[240:241], 1\n\t" TICKET "\n\ts_waitcnt vmcnt(0)" ::: "memory");
      }
      asm volatile("v_mov_b32 v241, 0\n\ts_mov_b64 exec, 1\n\tds_write_b64 %0, v[240:241]\n\ts_mov_b64 exec, -1" ::"v"(rdy) : "memory");
    }
    vsetu<79>(fo(1024) + qbF * 4352 + fr * 8);
    {
      const int jj = 4 * (fr >> 3) + (fr & 3);
      const unsigned one = ((fq ^ (fr >> 2)) & 1) ? 0u : (jj & 1) ? 0x3f800000u : 0x3f80u;
      static_for([&](auto I) { vsetu<96 + I>((jj >> 1) == I ? one : 0u); }, make_iseq<4>{});
    }
    unsigned vo = (fq >> 1) * 1024 + (fq & 1) * 256 + fr * 16;
    bf16* out = arg_late<bf16, 13>();
    __amdgpu_buffer_rsrc_t so_ = __builtin_amdgcn_make_buffer_rsrc((void*)out, (short)0, SEQ * OUTW * 2, 0x00020000);
    unsigned vw = ((qbF * 128 + (fr & 7)) * OUTW + DA + (fr >> 3) * 32 + fq * 8) * 2;
    asm volatile("" : "+s"(so_));
    asm volatile("s_waitcnt vmcnt(0) lgkmcnt(0)\n\ts_barrier" : "+v"(vo), "+v"(vw) :: "memory");
    fl_set<T_FS>();
    unsigned tk;
    asm volatile("ds_read_b32 %0, %1\n\ts_waitcnt lgkmcnt(0)" : "=v"(tk) : "v"(rdy) : "memory");
    tk = __builtin_amdgcn_readfirstlane(tk) & 255;
    KST(0, 30);
    if (doA) post_math(pj, pu, out, lds + 131072 + wid * 8192);
#define N3 0
#define N2 12
    constexpr int N1 = 28 - 3 * N3 - 2 * N2;
    static_assert(N1 >= 0 && N3 + N2 + N1 <= 16);
    const int nu = tk < N3 ? 3 : tk < N3 + N2 ? 2 : tk < N3 + N2 + N1 ? 1 : 0;
    const int u0 = 4 * (tk < N3 ? 3 * tk : tk < N3 + N2 ? N3 + 2 * tk : 2 * N3 + N2 + tk) + wid * nu;
    KST(0, 28);
    if (nu) {
      const unsigned so0 = O_S + qbF * 0x200000 + (16 + u0) * 2048, so[3] = {so0, so0, so0};
      unsigned pend = 0x5555u, cnt = 0x400000;
      while (true) {
        unsigned nw;
        if (wid == 3) {
          u64 m;
          const u64 ex = pend | (pend << 1);
          asm volatile(FL_ARM("%1") "\n\t" FL_POLL "\n\ts_waitcnt vmcnt(0)\n\tv_cmp_eq_u64 %0, s[84:85], v[124:125]" : "=s"(m) : "s"(ex) : "memory");
          nw = (unsigned)m & ((unsigned)m >> 1) & pend;
          if (nw) asm volatile("ds_write_b32 %0, %1 offset:4" ::"v"(rdy), "v"(~pend | nw) : "memory");
        } else {
          unsigned t;
          asm volatile("ds_read_b32 %0, %1 offset:4\n\ts_waitcnt lgkmcnt(0)" : "=v"(t) : "v"(rdy) : "memory");
          nw = __builtin_amdgcn_readfirstlane(t) & pend;
        }
        if (--cnt == 0) nw = pend;
        if (nw == 0) {
          if (wid == 3) asm volatile("s_sleep 4");
          else asm volatile("s_sleep 1");
          continue;
        }
        asm volatile("s_nop 4");
        static_for([&](auto S) {
          if ((nw >> (2 * S)) & 1) {

            r_load<0, 2 * S>(vo, so[0]); r_load<0, 2 * S + 1>(vo, so[0]);
            if (nu > 1) { r_load<1, 2 * S>(vo, so[1]); r_load<1, 2 * S + 1>(vo, so[1]); }
            if (nu > 2) { r_load<2, 2 * S>(vo, so[2]); r_load<2, 2 * S + 1>(vo, so[2]); }
          }
        }, make_iseq<8>{});
        pend &= ~nw;
        if (pend == 0) break;
      }
      asm volatile("s_waitcnt vmcnt(0)" ::: "memory");
      KST(0, 16);
      auto oo = [&](int U) { return ((((U >> 1) & 1) * 64 + ((U >> 2) & 3) * 16 + (U & 1) * 8) * OUTW + (U >> 4) * 64) * 2; };
      r_fin<0>(vw, so_, oo(u0));
      if (nu > 1) r_fin<1>(vw, so_, oo(u0 + 1));
      if (nu > 2) r_fin<2>(vw, so_, oo(u0 + 2));
      if (qbF == 15 && tk == 0) asm volatile(FL_SET("%0") ::"s"(fo(6144)) : "memory");
    }
    KST(0, 29);
  }
  asm volatile("s_waitcnt vmcnt(0)" ::: "memory");
  asm volatile("pf_code_end:");
  KST(0, 1);
}

extern "C" void kernel_launch(void* const* d_in, const int* in_sizes, int n_in, void* d_out, int out_size, void* d_ws,
                              size_t ws_size, hipStream_t stream) {
  const bf16* Aq = (const bf16*)d_in[0];
  const bf16* Ak = (const bf16*)d_in[1];
  const bf16* Av = (const bf16*)d_in[2];
  const bf16* Ash = (const bf16*)d_in[3];
  const bf16* Assmk = (const bf16*)d_in[4];
  const bf16* AssmD = (const bf16*)d_in[5];
  const bf16* Bx = (const bf16*)d_in[6];
  const bf16* Bckv = (const bf16*)d_in[7];
  const bf16* Bwq = (const bf16*)d_in[8];
  const bf16* Bwuk = (const bf16*)d_in[9];
  const bf16* Bwuv = (const bf16*)d_in[10];
  const bf16* Bwo = (const bf16*)d_in[11];
  bf16* out = (bf16*)d_out;
  hipFuncSetAttribute((const void*)fused_kernel, hipFuncAttributeMaxDynamicSharedMemorySize, 163840);
  fused_kernel<<<256, 256, 163840, stream>>>(Bckv, Bwuk, Bwuv, Bx, Bwq, (bf16*)d_ws, Ak, Av, Ash, Assmk, AssmD, Bwo, Aq, out);
}
```

```cpp
#include <hip/hip_runtime.h>
#include <hip/hip_bf16.h>
#include <stdint.h>
#include <utility>

typedef __hip_bfloat16 bf16;
typedef __attribute__((ext_vector_type(4))) short bf16x4;
typedef __attribute__((ext_vector_type(2))) unsigned u32x2;

constexpr int SEQ = 2048;
constexpr int DA = 448;
constexpr int NH = 16;
constexpr int HD = 256;
constexpr int DC = 1024;
constexpr int OUTW = 896;
constexpr unsigned TSB = NH * SEQ * HD * 2, O_K = TSB, O_S = 3 * TSB, O_P = 5 * TSB, PSTR = 67584, O_F = O_P + 128 * PSTR,
                   O_X = O_F + 400 * 4352, O_Z = O_X + DA * SEQ * 2, WSB = O_Z + DA * SEQ * 2;
constexpr unsigned fo(int w) { return O_F + (w >> 4) * 4352 + (w & 15) * 8; }

#define KST(kid, which)

__device__ __forceinline__ float bf2f(unsigned short u) { return __uint_as_float(((unsigned)u) << 16); }
__device__ __forceinline__ unsigned short f2bf(float f) { return __bfloat16_as_ushort(__float2bfloat16(f)); }
__device__ __forceinline__ float rbf(float f) { return bf2f(f2bf(f)); }
typedef __attribute__((ext_vector_type(2))) __bf16 bf16x2n;
typedef __attribute__((ext_vector_type(2))) float f32x2n;
__device__ __forceinline__ unsigned pk2(float a, float b) {
  return __builtin_bit_cast(unsigned, __builtin_convertvector((f32x2n){a, b}, bf16x2n));
}

__device__ __forceinline__ int off_b(int row, int ch) { return 256 * row + 16 * (ch ^ (((row & 3) << 2) | ((row >> 2) & 3))); }

template <int R> __device__ __forceinline__ void sset(unsigned x) { asm volatile("s_mov_b32 s%c0, %1" ::"i"(R), "s"(x)); }
template <int R> __device__ __forceinline__ void vsetu(unsigned x) { asm volatile("v_mov_b32 v%c0, %1" ::"i"(R), "v"(x)); }
template <int R> __device__ __forceinline__ void srd_set(const void* p, unsigned n) {
  asm volatile("s_mov_b64 s[%c0:%c1], %4\n\ts_mov_b32 s%c2, %5\n\ts_mov_b32 s%c3, 0x20000" ::"i"(R), "i"(R + 1), "i"(R + 2), "i"(R + 3), "s"(p), "s"(n));
}
template <int R, int L> __device__ __forceinline__ void tload() { asm volatile("v_readlane_b32 s%c0, v78, %1" ::"i"(R), "i"(L)); }
template <int L> __device__ __forceinline__ unsigned tget() { unsigned t; asm volatile("v_readlane_b32 %0, v78, %1" : "=s"(t) : "i"(L)); return t; }
#define FL_SET(S) "buffer_store_dwordx2 v[82:83], v81, s[88:91], " S " offen sc1"
#define FL_ARM(N) "v_cndmask_b32_e64 v80, -2.0, v79, " N
#define FL_POLL "buffer_load_dwordx2 v[124:125], v80, s[88:91], 0 offen sc1"
#define FL_CHECK(N) "s_mov_b32 s48, 0x100000\n7:\n\tv_cmp_eq_u64 vcc, s[84:85], v[124:125]\n\ts_and_b64 vcc, vcc, " N "\n\ts_cmp_eq_u64 vcc, " N \
                    "\n\ts_cbranch_scc1 8f\n\ts_sleep 2\n\t" FL_POLL "\n\ts_waitcnt vmcnt(0)\n\ts_sub_u32 s48, s48, 1\n\ts_cbranch_scc0 7b\n8:"
template <int L> __device__ __forceinline__ void fl_set() {
  asm volatile("v_readlane_b32 s48, v78, %0\n\ts_nop 4\n\t" FL_SET("s48") ::"i"(L) : "memory");
}
template <int L> __device__ __forceinline__ void fl_poll() {
  asm volatile("v_readlane_b32 s50, v78, %0\n\tv_readlane_b32 s51, v78, %1\n\ts_nop 1\n\t" FL_ARM("s[50:51]") "\n\t" FL_POLL ::"i"(L), "i"(L + 1) : "memory");
}
#define TICKET "buffer_atomic_add_x2 v[240:241], v81, s[88:91], s101 offen sc0"
__device__ __forceinline__ void fl_check() { asm volatile(FL_CHECK("s[50:51]") ::: "vcc", "scc", "memory"); }
template <int R, int K> __device__ __forceinline__ unsigned vxor() { unsigned t; asm volatile("v_xor_b32 %0, %1, v%c2" : "=v"(t) : "i"(K), "i"(R)); return t; }
template <int R> __device__ __forceinline__ float vget() { float t; asm volatile("v_mov_b32 %0, v%c1" : "=v"(t) : "i"(R)); return t; }
template <int R> __device__ __forceinline__ void vset(float x) { asm volatile("v_mov_b32 v%c0, %1" ::"i"(R), "v"(x)); }
template <int R> __device__ __forceinline__ float acc_read1() { float t; asm volatile("v_accvgpr_read_b32 %0, a[%c1]" : "=v"(t) : "i"(R)); return t; }
template <class F, int... I> __device__ __forceinline__ void static_for(F&& f, std::integer_sequence<int, I...>) {
  (f(std::integral_constant<int, I>{}), ...);
}
template <int... Is> using iseq = std::integer_sequence<int, Is...>;
template <int N> using make_iseq = std::make_integer_sequence<int, N>;

typedef __attribute__((ext_vector_type(4))) unsigned u32x4c;
typedef __attribute__((ext_vector_type(4))) float f32x4c;
typedef __attribute__((ext_vector_type(2))) float f32x2c;
typedef unsigned long long u64;
struct PR { u32x4c kk[7], vv[4]; };
__device__ __forceinline__ void prep_load(PR& p, int tid, int cg, int tb, const bf16* __restrict__ k, const bf16* __restrict__ v,
                                          const bf16* __restrict__ shift_k) {
  const __amdgpu_buffer_rsrc_t sk = __builtin_amdgcn_make_buffer_rsrc((void*)k, (short)0, SEQ * DA * 2, 0x00020000);
  const __amdgpu_buffer_rsrc_t sv = __builtin_amdgcn_make_buffer_rsrc((void*)v, (short)0, SEQ * DA * 2, 0x00020000);
  const __amdgpu_buffer_rsrc_t sw = __builtin_amdgcn_make_buffer_rsrc((void*)(shift_k + cg * 256), (short)0, 512, 0x00020000);
  const int vo = ((tb * 128 + (tid >> 3) * 4) * DA + cg * 64 + (tid & 7) * 8) * 2;
#pragma unroll
  for (int q = 0; q < 7; ++q)
    asm volatile("buffer_load_dwordx4 %0, %1, %2, 0 offen" : "=v"(p.kk[q]) : "v"(vo + (q - 3) * DA * 2), "s"(sk) : "memory");
#pragma unroll
  for (int q = 0; q < 4; ++q)
    asm volatile("buffer_load_dwordx4 %0, %1, %2, 0 offen offset:%3" : "=v"(p.vv[q]) : "v"(vo), "s"(sv), "i"(q * DA * 2) : "memory");
#pragma unroll
  for (int q = 0; q < 4; ++q)
    asm volatile("buffer_load_dwordx4 a[%c0:%c1], %2, %3, 0 offen offset:%4" ::"i"(4 * q), "i"(4 * q + 3), "v"((tid & 7) * 64), "s"(sw), "i"(q * 16) : "memory");
}
__device__ __forceinline__ float bfl(unsigned u) { return __uint_as_float(u << 16); }
__device__ __forceinline__ float bfh(unsigned u) { return __uint_as_float(u & 0xffff0000u); }
__device__ __forceinline__ f32x2c rbf2(f32x2c a) {
  return (f32x2c){__uint_as_float(pk2(0.f, a[0])), __uint_as_float(pk2(0.f, a[1]))};
}
template <int I> __device__ __forceinline__ void prep_math(const PR& p, int tid, char* lx) {
  const int rp = tid >> 3, c = tid & 7;
  f32x2c wt[4], kf[7];
  const unsigned w[4] = {__float_as_uint(acc_read1<4 * I>()), __float_as_uint(acc_read1<4 * I + 1>()), __float_as_uint(acc_read1<4 * I + 2>()),
                         __float_as_uint(acc_read1<4 * I + 3>())};
#pragma unroll
  for (int t = 0; t < 4; ++t)
    wt[t] = (t & 1) ? (f32x2c){bfh(w[t >> 1]), bfh(w[2 + (t >> 1)])} : (f32x2c){bfl(w[t >> 1]), bfl(w[2 + (t >> 1)])};
#pragma unroll
  for (int q = 0; q < 7; ++q) kf[q] = (f32x2c){bfl(p.kk[q][I]), bfh(p.kk[q][I])};
  f32x2c x[4];
#pragma unroll
  for (int e = 0; e < 4; ++e) {
    f32x2c s = rbf2(kf[e] * wt[3]);
    s = rbf2(s + rbf2(kf[e + 1] * wt[2]));
    s = rbf2(s + rbf2(kf[e + 2] * wt[1]));
    s = rbf2(s + rbf2(kf[e + 3] * wt[0]));
    x[e] = s * (f32x2c){bfl(p.vv[e][I]), bfh(p.vv[e][I])};
  }
#pragma unroll
  for (int u = 0; u < 2; ++u)
    *(u32x2*)(lx + (c * 8 + 2 * I + u) * 256 + (((31 - rp) * 8) ^ (c << 4))) = (u32x2){pk2(x[3][u], x[2][u]), pk2(x[1][u], x[0][u])};
}
__device__ __forceinline__ void prep_store(int tid, int cg, int tb, bf16* __restrict__ xr, const char* lx) {
  const __amdgpu_buffer_rsrc_t sx = __builtin_amdgcn_make_buffer_rsrc((void*)(xr + (size_t)cg * 64 * SEQ), (short)0, 64 * SEQ * 2, 0x00020000);
#pragma unroll
  for (int rd = 0; rd < 4; ++rd) {
    const int idx = tid + 256 * rd, ch = idx >> 4, q = idx & 15;
    u32x4c w = *(const u32x4c*)(lx + ch * 256 + 16 * (q ^ (ch >> 3)));
    __builtin_amdgcn_raw_buffer_store_b128(w, sx, (ch * SEQ + (15 - tb) * 128 + q * 8) * 2, 0, 16);
  }
}

#define CODE_TOUCH(LB, LE, wx, NW, NL, m0v) do { unsigned blo_, bhi_, elo_; \
    asm volatile("s_getpc_b64 s[48:49]\n\ts_add_u32 s48, s48, " LB "@rel32@lo+4\n\ts_addc_u32 s49, s49, " LB "@rel32@hi+12\n\t" \
                 "s_mov_b32 %0, s48\n\ts_mov_b32 %1, s49\n\t" \
                 "s_getpc_b64 s[48:49]\n\ts_add_u32 s48, s48, " LE "@rel32@lo+4\n\ts_mov_b32 %2, s48" \
                 : "=s"(blo_), "=s"(bhi_), "=s"(elo_) :: "scc"); \
    const unsigned n_ = elo_ - (blo_ & ~127u); \
    const __amdgpu_buffer_rsrc_t srd_ = __builtin_amdgcn_make_buffer_rsrc((void*)(((unsigned long long)bhi_ << 32) | (blo_ & ~127u)), (short)0, n_ < 262144u ? n_ : 0u, 0x00020000); \
    const unsigned l_ = threadIdx.x & 63, vo_ = l_ < (NL) ? ((wx) + l_ * (NW)) * 128u : 0x80000000u; \
    asm volatile("s_mov_b32 m0, %0\n\ts_nop 4\n\tbuffer_load_dword %1, %2, 0 offen lds" ::"s"(m0v), "v"(vo_), "s"(srd_) : "memory"); } while (0)
template <int I> __device__ __forceinline__ void d_store() {
  asm volatile("buffer_store_dwordx4 v[%c0:%c1], v%c2, s[88:91], s101 offen offset:%3 sc1" ::"i"(128 + 4 * I), "i"(131 + 4 * I), "i"(112 + ((I >> 2) & 1)),
               "i"((I & 3) * 1024) : "memory");
  if constexpr ((I & 7) == 7) asm volatile("s_addk_i32 s101, 0x4000" ::: "scc");
}
constexpr int NS_X = 19;
struct CV { unsigned pa[4], pb, px, vz, sz; float D; };
constexpr int cv_J(bool up, int q) { int J = up ? 0 : 7; while (q > J) { q -= J + 1; J += up ? 1 : -1; } return J; }
constexpr int cv_i(bool up, int q) { int J = up ? 0 : 7; while (q > J) { q -= J + 1; J += up ? 1 : -1; } return q; }
template <int R, int N> __device__ __forceinline__ void cv_rd(CV& c) {
  if constexpr (N < 16) {
    constexpr int i = N >> 1, o = 16 * R + 128 * (i & 1) + 2 * (N & 1), r = 32 + 32 * (R & 1) + 2 * N;
    asm volatile("ds_read2_b32 a[%c1:%c2], %0 offset0:%c3 offset1:%c4" ::"v"(c.pa[i >> 1]), "i"(r), "i"(r + 1), "i"(o), "i"(o + 1));
  } else {
    constexpr int m = N - 16, r = 192 + 32 * (R & 1) + 4 * m;
    asm volatile("ds_read_b128 a[%c1:%c2], %0 offset:%c3" ::"v"(c.pb), "i"(r), "i"(r + 3), "i"(64 * R + 512 * (7 - m)));
  }
}
template <int O> __device__ __forceinline__ void cv_xd(CV& c) {
  constexpr int e = O & 31, x = 96 + ((e >> 1) ^ 1);
  if constexpr (O >= 64) asm volatile("v_cvt_pk_bf16_f32 v%c0, 0, v%c0" ::"i"(160 + e));
  else if constexpr (O >= 32) asm volatile("v_mul_f32 v%c0, v%c0, %1" ::"i"(160 + e), "v"(c.D));
  else if constexpr (O & 1) asm volatile("v_lshlrev_b32 v%c0, 16, v%c1" ::"i"(160 + e), "i"(x));
  else asm volatile("v_and_b32 v%c0, 0xffff0000, v%c1" ::"i"(160 + e), "i"(x));
}
template <int O> __device__ __forceinline__ void cv_z(CV& c) {
  constexpr int J = O / 11, k = O % 11, y = 128 + 4 * J;
  if constexpr (k < 4) asm volatile("v_cvt_pk_bf16_f32 v%c0, 0, v%c0" ::"i"(y + k));
  else if constexpr (k < 8) asm volatile("v_add_f32 v%c0, v%c0, v%c1" ::"i"(y + k - 4), "i"(y + 28 + k));
  else if constexpr (k < 10) asm volatile("v_cvt_pk_bf16_f32 v%c0, v%c1, v%c2" ::"i"(y + k - 8), "i"(y + 2 * k - 16), "i"(y + 2 * k - 15));
  else asm volatile("buffer_store_dwordx2 v[%c0:%c1], %2, s[88:91], s101 offen sc1\n\ts_add_u32 s101, s101, 0x8000" ::"i"(y), "i"(y + 1), "v"(c.vz) : "memory");
}
constexpr int cv_z0(int Q) {
  int o = 0;
  for (int q = 0; q < Q; ++q)
    for (int k = 0; k < (q % 4 == 1 ? 0 : 3); ++k) { const int J = o / 11; if ((J + 1) * (J + 2) / 2 - 1 + (J ? 4 : 7) <= q) ++o; }
  return o;
}
constexpr int cv_xd0(int T) { int o = 0; for (int t = 0; t < T; ++t) o += t % 4 != 1 || (t >= 72 && t < 108); return o < 96 ? o : 96; }
static_assert(cv_xd0(144) == 96);
template <int B, int... Is> __device__ __forceinline__ void cv_zs(CV& c, iseq<Is...>) { (cv_z<B + Is>(c), ...); }
template <int R, int Q> __device__ __forceinline__ void cv_slot(CV& c) {
  constexpr int i = cv_i(R == 7, Q), J = cv_J(R == 7, Q), d = 128 + 4 * J, a = 32 + 32 * (R & 1) + 4 * i, b = 192 + 32 * (R & 1) + 4 * (J - i);
  constexpr int T = 36 * R + Q, vm = Q % 4 == 1 && Q < 32 ? Q / 4 : -1;
  if constexpr (Q == 0) asm volatile("s_waitcnt lgkmcnt(0)");
  if constexpr (R >= 6 && vm >= 0 && vm % 4 == 0) asm volatile("s_add_u32 m0, s79, %0" ::"i"(vm * 1024));
  if constexpr (R == 0 && i == 0)
    asm volatile("v_mfma_f32_16x16x32_bf16 v[%c0:%c1], a[%c2:%c3], a[%c4:%c5], 0" ::"i"(d), "i"(d + 3), "i"(a), "i"(a + 3), "i"(b), "i"(b + 3));
  else
    asm volatile("v_mfma_f32_16x16x32_bf16 v[%c0:%c1], a[%c2:%c3], a[%c4:%c5], v[%c0:%c1]" ::"i"(d), "i"(d + 3), "i"(a), "i"(a + 3), "i"(b), "i"(b + 3));
  if constexpr (Q < 24 && R < 7) cv_rd<R + 1, Q>(c);
  if constexpr (T % 7 == 3 && NS_X + 5 + T / 7 < 32) d_store<NS_X + 5 + T / 7>();
  if constexpr (R == 2 && Q >= 24 && Q < 32)
    asm volatile("ds_read_b64 v[%c1:%c2], %0 offset:%c3" ::"v"(c.px), "i"(96 + 2 * (Q - 24)), "i"(97 + 2 * (Q - 24)), "i"(512 * (31 - Q)));
  if constexpr (R >= 3 && R < 7) if constexpr (cv_xd0(T - 107) > cv_xd0(T - 108)) cv_xd<cv_xd0(T - 108)>(c);
  if constexpr ((R == 3 || R == 4) && vm >= 0) {
    constexpr int f = (R - 3) * 8 + vm;
    asm volatile("buffer_load_dwordx4 a[%c0:%c1], v84, s[88:91], s96 offen offset:%2 sc1" ::"i"(128 + 4 * f), "i"(131 + 4 * f), "i"((f & 3) * 1024) : "memory");
    if constexpr ((f & 3) == 3) asm volatile("s_addk_i32 s96, %0" ::"i"(f == 7 ? 28672 : 4096));
  }
  if constexpr (R == 4 && Q == 35) asm volatile(FL_POLL ::: "memory");
  if constexpr (R >= 6 && vm >= 0)
    asm volatile("buffer_load_dwordx4 v%c0, s[88:91], s98 offen offset:%1 sc1 lds" ::"i"(86 + vm / 4), "i"((vm & 3) * 1024) : "memory");
  if constexpr (R == 6 && Q == 35) asm volatile("s_add_u32 s98, s98, 0x8000\n\ts_xor_b32 s79, s79, 0x8000");
  if constexpr (R == 7) cv_zs<cv_z0(Q)>(c, make_iseq<cv_z0(Q + 1) - cv_z0(Q)>{});
  __builtin_amdgcn_sched_barrier(0);
}
template <int R, int... Q> __device__ __forceinline__ void cv_step(CV& c, iseq<Q...>) { (cv_slot<R, Q>(c), ...); }
template <int... N> __device__ __forceinline__ void cv_rd0(CV& c, iseq<N...>) { (cv_rd<0, N>(c), ...); }
template <int FLAG> __device__ __forceinline__ void conv_wave(unsigned sz, int ck, float Dd, char* lw) {
  int lane = threadIdx.x & 63; asm volatile("" : "+v"(lane));
  const int l16 = lane & 15, fq = lane >> 4;
  u32x4c w[5]; unsigned w4[5];
#pragma unroll
  for (int r = 0; r < 5; ++r) {
    w[r] = *(const u32x4c*)(lw + r * 1024 + lane * 16);
    w4[r] = *(const unsigned*)(lw + r * 1024 + lane * 16 + 16);
  }
  d_store<NS_X>(); d_store<NS_X + 1>();
#pragma unroll
  for (int r = 0; r < 5; ++r)
    *(u32x4c*)(lw + 10304 + r * 1024 + lane * 16) = (u32x4c){__builtin_amdgcn_alignbit(w[r][1], w[r][0], 16), __builtin_amdgcn_alignbit(w[r][2], w[r][1], 16),
                                                             __builtin_amdgcn_alignbit(w[r][3], w[r][2], 16), __builtin_amdgcn_alignbit(w4[r], w[r][3], 16)};
  d_store<NS_X + 2>();
  CV c;
#pragma unroll
  for (int i = 0; i < 4; ++i) c.pa[i] = (unsigned)(size_t)lw + ((l16 + 1) & 1) * 10304 + 4 * ((8 * fq + l16 + 1) >> 1) + 1024 * i;
  c.pb = (unsigned)(size_t)lw + 5120 + (2032 - 16 * l16 + 8 * fq) * 2 - 512 * 7;
  c.vz = (l16 >> 1) * 4096 + (((l16 & 1) * 2 + (fq >> 1)) ^ (ck >> 1)) * 16 + (fq & 1) * 8;
  c.px = (unsigned)(size_t)lw + 5120 + 2044 * 2 - 512 * 7 - (16 * l16 + 4 * fq) * 2;
  c.sz = sz; c.D = Dd;
  asm volatile("" : "+v"(c.vz), "+v"(c.px), "+v"(c.D) :: "memory");
  asm volatile("" : "+s"(c.sz));
  cv_rd0(c, make_iseq<24>{});
  d_store<NS_X + 3>(); d_store<NS_X + 4>();
  __builtin_amdgcn_sched_barrier(0);
  static_for([&](auto R) __attribute__((always_inline)) {
    cv_step<R>(c, make_iseq<36>{});
    if constexpr (R == 2) {
      asm volatile("s_waitcnt vmcnt(0)\n\ts_barrier" ::: "memory");
      fl_set<FLAG>();
      sset<101>(c.sz);
    }
    if constexpr (R == 5) {
      asm volatile("s_waitcnt vmcnt(0)" ::: "memory");
      fl_check();
    }
    __builtin_amdgcn_sched_barrier(0);
  }, make_iseq<8>{});
  asm volatile("s_nop 7");
  cv_zs<cv_z0(36)>(c, make_iseq<88 - cv_z0(36)>{});
  __builtin_amdgcn_sched_barrier(0);
}

__device__ __forceinline__ void post_dma(int cg, int tb, const bf16* __restrict__ q, unsigned m0w) {
  int lane = threadIdx.x & 63; asm volatile("" : "+v"(lane));
  const __amdgpu_buffer_rsrc_t sq = __builtin_amdgcn_make_buffer_rsrc((void*)(q + (size_t)tb * 32 * DA + cg * 64), (short)0, 32 * DA * 2, 0x00020000);
  const unsigned vq = ((lane >> 3) * DA + (lane & 7) * 8) * 2;
  asm volatile("s_mov_b32 m0, %0\n\ts_nop 4\n\tbuffer_load_dwordx4 %1, %2, 0 offen lds\n\tbuffer_load_dwordx4 %1, %2, %3 offen offset:1024 lds\n\t"
               "buffer_load_dwordx4 %1, %2, %4 offen offset:2048 lds\n\tbuffer_load_dwordx4 %1, %2, %5 offen offset:3072 lds"
               ::"s"(m0w + 4096), "v"(vq), "s"(sq), "s"(8 * DA * 2 - 1024), "s"(16 * DA * 2 - 2048), "s"(24 * DA * 2 - 3072) : "memory");
}
__device__ __forceinline__ void post_math(int cg, int tb, bf16* __restrict__ out, const char* lw) {
  int lane = threadIdx.x & 63; asm volatile("" : "+v"(lane));
  const int tl = lane >> 3, ck = lane & 7;
  const __amdgpu_buffer_rsrc_t so = __builtin_amdgcn_make_buffer_rsrc((void*)(out + (size_t)tb * 32 * OUTW + cg * 64), (short)0, 32 * OUTW * 2 - 2 * DA, 0x00020000);
  const unsigned vo = (tl * OUTW + ck * 8) * 2;
#pragma unroll
  for (int i = 0; i < 4; ++i) {
    const u32x4c qv = *(const u32x4c*)(lw + 4096 + i * 1024 + lane * 16);
    const char* lz = lw + ck * 64 + 16 * (i ^ (ck >> 1)) + tl * 2;
    u32x4c o;
#pragma unroll
    for (int e = 0; e < 4; ++e) {
      const float z0 = bf2f(*(const unsigned short*)(lz + 2 * e * 512));
      const float z1 = bf2f(*(const unsigned short*)(lz + (2 * e + 1) * 512));
      o[e] = pk2(bfl(qv[e]) * z0, bfh(qv[e]) * z1);
    }
    __builtin_amdgcn_raw_buffer_store_b128(o, so, vo + i * 8 * OUTW * 2, 0, 17);
  }
}

#define DI __device__ __forceinline__
#define GAP() __builtin_amdgcn_sched_barrier(0)
typedef __attribute__((ext_vector_type(4))) float f32x4g;
typedef __attribute__((ext_vector_type(4))) unsigned u32x4g;
constexpr int LDW = NH * HD;
struct GM { unsigned aA[2], aB[2][8]; };
template <bool W, int J, int V> DI void g_dma() {
  if constexpr (J == 0) asm volatile("buffer_load_dwordx4 v%c0, s[%c1:%c2], 0 offen lds" ::"i"(V), "i"(W ? 60 : 56), "i"(W ? 63 : 59) : "memory");
  else asm volatile("buffer_load_dwordx4 v%c0, s[%c1:%c2], s%c3 offen lds" ::"i"(V), "i"(W ? 60 : 56), "i"(W ? 63 : 59), "i"((W ? 70 : 63) + J) : "memory");
}
template <int P, int KS, int I> DI void g_wread(GM& g) {
  constexpr int nf = I >> 1, hf = I & 1, r = 192 - 64 * P + 8 * nf + 4 * KS + 2 * hf;
  asm volatile("ds_read_b64_tr_b16 a[%c1:%c2], %0 offset:%c3" ::"v"(g.aB[hf][nf]), "i"(r), "i"(r + 1), "i"(P * 32768 + KS * 16384));
}
template <int P, int M, int KS> DI void g_aread(GM& g) {
  constexpr int r = 96 + 8 * (M & 1) + 4 * KS;
  asm volatile("ds_read_b128 v[%c1:%c2], %0 offset:%c3" ::"v"(g.aA[KS]), "i"(r), "i"(r + 3), "i"(P * 32768 + M * 2048));
}
constexpr float CS = 0.0625f * 1.4426950408889634f;
constexpr int NS_Q = 19;
constexpr int e_vm(int G) { return (G * 3) % 8 < 3 ? G * 3 / 8 : -1; }
constexpr int e_st(int MODE, int G) {
  const int q = G & 15, k3 = ((G >> 4) - 1) * 3 + (q == 0 ? 0 : q == 4 ? 1 : 2), k2 = NS_Q + (G >> 4) * 2 + (q == 4);
  if (MODE == 5) return e_vm(G) >= 32 ? e_vm(G) - 32 : -1;
  if (MODE == 3) return G >= 33 && (G - 33) % 5 == 0 && (G - 33) / 5 < NS_X ? (G - 33) / 5 : -1;
  if (MODE == 2) return G >= 30 && (q == 0 || q == 4 || q == 14) && k3 - 2 < NS_Q ? k3 - 2 : -1;
  if (MODE == 4) return (q == 0 || q == 4) && k2 < 32 ? k2 : -1;
  return -1;
}
constexpr int e_nst(int MODE, int G0, int G1) { int n = 0; for (int G = G0; G < G1; ++G) n += e_st(MODE, G) >= 0; return n; }
static_assert(e_nst(2, 0, 128) == NS_Q && e_nst(3, 30, 128) == NS_X && e_nst(5, 0, 128) == 16 && e_nst(4, 0, 104) == 32 - NS_Q);
constexpr int e_slot(int MODE, int G) { return MODE == 3 ? (G % 3 == 0 && G < 30 ? G / 3 : -1) : e_vm(G) >= 0 && e_vm(G) < 32 ? 10 + e_vm(G) : -1; }
struct ES { int op0[129], nop[129]; };
constexpr ES e_sched(bool TR, int MODE) {
  ES z{};
  const bool SCL = MODE == 2 || MODE == 4;
  const int per = SCL ? 16 : 8, o1 = 32 * per;
  int o = MODE == 4 ? e_sched(false, 2).op0[128] : 0;
  for (int G = 0; G <= 128; ++G) {
    const int q = G & 15;
    int cap = 2 - (q == 1 || q == 2 || q == 15);
    if (SCL) cap -= (q >= 3 && q < 11 && (q & 1)) + (q == 7 || q == 11) + 2 * (G == 70 || G == 74 || (G & 63) == 61);
    cap += MODE == 2 ? 4 + (q == 10 || q == 13) : MODE == 3 ? 1 : MODE == 5 ? 2 : 0;
    if (e_st(MODE, G) >= 0 || (SCL ? q == 8 || q == 12 : e_slot(MODE, G) >= 0) || (MODE == 4 && G < 4)) cap = 0;
    if (G == 128) cap = 10000;
    z.op0[G] = o;
    while (o < o1) {
      const int i = o / per, rdy = MODE == 4 ? 0 : (TR ? 32 * (i >> 3) + 16 + 2 * (i & 7) + 1 : 4 * i + 3) + 6;
      if ((G < rdy && G < 128) || cap-- <= 0) break;
      ++o; ++z.nop[G];
    }
  }
  return z;
}
template <bool TR, int MODE> constexpr ES ESCH = e_sched(TR, MODE);
static_assert(ESCH<false, 4>.op0[96] == 512 && ESCH<false, 4>.op0[0] >= 24 * 16);
template <bool TR, bool SCL, int O> DI void e_op() {
  constexpr int per = SCL ? 16 : 8, i = O / per, k = O % per, r = 128 + 4 * i, t = 114 + 4 * (i & 1), c = k - (per - 4);
  if constexpr (k < 4) asm volatile("v_accvgpr_read_b32 v%c0, a[%c1]" ::"i"(t + k), "i"(4 * i + k));
  else if constexpr (c < 0) asm volatile("v_mul_f32 v%c0, s83, v%c0" ::"i"(k < 8 ? r + (k ^ 6) : t + k - 8));
  else {
    constexpr int d = c == 0 ? r + 3 : c == 1 ? r + 2 : r + c - 2, x = c == 0 ? r + 2 : c == 1 ? r : t + 2 * (c - 2);
    asm volatile("v_cvt_pk_bf16_f32 v%c0, v%c1, v%c2" ::"i"(d), "i"(x), "i"(x + 1));
  }
}
template <bool TR, bool SCL, int B, int... Is> DI void e_ops(iseq<Is...>) { (e_op<TR, SCL, B + Is>(), ...); }
template <int S> DI void e_m0() {
  if constexpr (S == 0 || S == 1 || S == 5 || S == 9) asm volatile("s_add_u32 m0, s78, %0" ::"i"(S * 1024));
  if constexpr (S >= 26 && (S - 26) % 4 == 0) asm volatile("s_add_u32 m0, s79, %0" ::"i"(((S - 26) >> 3) * 32768 + ((S - 26) & 4) * 1024));
}
template <int S> DI void e_load() {
  if constexpr (S == 0) asm volatile("buffer_load_dwordx4 v122, s[56:59], 0 offen lds" ::: "memory");
  else if constexpr (S < 5) asm volatile("buffer_load_dwordx4 v84, s[56:59], s64 offen offset:%0 lds" ::"i"((S - 1) * 1024) : "memory");
  else if constexpr (S < 9) asm volatile("buffer_load_dwordx4 v84, s[60:63], 0 offen offset:%0 sc1 lds" ::"i"((S - 5) * 1024) : "memory");
  else if constexpr (S == 9) asm volatile("buffer_load_dwordx4 v84, s[60:63], s62 offen sc1 lds" ::: "memory");
  else if constexpr (S < 26) {
    constexpr int f = S - 10;
    asm volatile("buffer_load_dwordx4 a[%c0:%c1], v84, s[88:91], s96 offen offset:%2 sc1" ::"i"(128 + 4 * f), "i"(131 + 4 * f), "i"((f & 3) * 1024) : "memory");
    if constexpr ((f & 3) == 3) asm volatile("s_addk_i32 s96, %0" ::"i"(f == 7 ? 28672 : 4096));
  } else {
    constexpr int f = S - 26;
    asm volatile("buffer_load_dwordx4 v%c0, s[88:91], s98 offen offset:%1 sc1 lds" ::"i"(86 + ((f >> 2) & 1)), "i"((f & 3) * 1024) : "memory");
    if constexpr (f == 7) asm volatile("s_add_u32 s98, s98, s97");
    if constexpr (f == 15) asm volatile(FL_POLL ::: "memory");
  }
}
template <bool TR, int P, int H, int G, int MODE> DI void g_gap(GM& g) {
  constexpr int m = 4 * H + (G >> 4), q = G & 15, n = q >> 1, ks = q & 1, GG = 64 * H + G;
  constexpr bool inV = TR ? m & 1 : n & 1, END = MODE == 3 || MODE == 5;
  constexpr int a = 96 + 8 * (m & 1) + 4 * ks, b = 192 - 64 * P + 8 * n + 4 * ks, d = (inV ? 128 : 0) + 4 * (TR ? 8 * (m >> 1) + n : 4 * m + (n >> 1));
  constexpr int J = END ? -1 : q == 8 ? 2 * (G >> 4) : q == 12 ? 2 * (G >> 4) + 1 : -1, gj = (J & 3) ^ ((J & 3) >> 1);
  if constexpr (END) { if constexpr (q == 0 && GG > 0) asm volatile("s_waitcnt lgkmcnt(0)"); e_m0<e_slot(MODE, GG)>(); }
  else if constexpr (q == 0 && G > 0) asm volatile("s_waitcnt lgkmcnt(4)");
  if constexpr (J == 0) asm volatile("s_add_u32 m0, s78, %0" ::"i"((P ^ H ^ 1) * 32768 + (1 - H) * 8192));
  else if constexpr (J == 4) asm volatile("s_add_u32 m0, s79, %0" ::"i"((P ^ H) * 32768 + (1 - H) * 16384));
  else if constexpr (J > 0 && (J & 3) == 1) asm volatile("s_bitset1_b32 m0, 12");
  else if constexpr (J > 0 && (J & 3) == 2) asm volatile("s_bitset1_b32 m0, %0" ::"i"(J < 4 ? 14 : 13));
  else if constexpr (J > 0) asm volatile("s_bitset0_b32 m0, 12");
#define GMF(D, X, Y, C) asm volatile("v_mfma_f32_16x16x32_bf16 " D "[%c0:%c1], " X ", " Y ", " C ::"i"(d), "i"(d + 3), "i"(a), "i"(a + 3), "i"(b), "i"(b + 3))
#define GMF2(D, C) do { if constexpr (TR) GMF(D, "v[%c2:%c3]", "a[%c4:%c5]", C); else GMF(D, "a[%c4:%c5]", "v[%c2:%c3]", C); } while (0)
  if constexpr ((MODE == 1 || MODE == 4) && ks == 0) { if constexpr (inV) GMF2("v", "0"); else GMF2("a", "0"); }
  else if constexpr (inV) GMF2("v", "v[%c0:%c1]");
  else GMF2("a", "a[%c0:%c1]");
#undef GMF2
#undef GMF
  if constexpr ((q == 1 || q == 2) && !(END && m == 7)) g_aread<(m < 7 ? P : P ^ 1), (m + 1) & 7, q - 1>(g);
  if constexpr (!END && q >= 3 && q < 11 && (q & 1)) g_wread<P ^ 1, H, (G >> 4) * 4 + (q - 3) / 2>(g);
  if constexpr (MODE == 1 && GG < 7) {
    if constexpr (GG == 0) asm volatile("s_barrier" ::: "memory");
    if constexpr (GG == 2) asm volatile("s_add_u32 m0, s79, %0" ::"i"(P * 32768));
    if constexpr (GG >= 3) g_dma<true, (GG - 3) ^ ((GG - 3) >> 1), 91>();
    if constexpr (GG == 3 || GG == 4) asm volatile("s_bitset1_b32 m0, %0" ::"i"(9 + GG));
    if constexpr (GG == 5) asm volatile("s_bitset0_b32 m0, 12");
  }
  if constexpr (e_st(MODE, GG) >= 0) {
    static_assert(ESCH<TR, MODE>.op0[GG] >= (e_st(MODE, GG) + 1) * (END ? 8 : 16));
    d_store<e_st(MODE, GG)>();
  }
  if constexpr (J >= 0 && J < 4) g_dma<false, (1 - H) * 2 + (gj & 1) + (gj >> 1) * 4, 90>();
  else if constexpr (J >= 4) g_dma<true, (1 - H) * 4 + gj, 91>();
  if constexpr (MODE >= 2) e_ops<TR, !END, ESCH<TR, MODE>.op0[GG]>(make_iseq<ESCH<TR, MODE>.nop[GG]>{});
  if constexpr (END) {
    if constexpr (e_slot(MODE, GG) >= 0) e_load<e_slot(MODE, GG)>();
  } else {
    if constexpr (H == 1 && G == 6) asm volatile("v_lshl_add_u32 v90, s82, 7, v92\n\ts_and_b32 s82, s80, 15");
    if constexpr (H == 1 && G == 10) asm volatile("s_lshr_b64 s[80:81], s[80:81], 4\n\tv_lshl_add_u32 v91, s82, 19, v93");
    if constexpr (G == 61) {
      constexpr int nst = e_nst(MODE, H ? 29 : 0, GG + 1) + (MODE == 4 && !H ? e_nst(2, 93, 128) : 0);
      asm volatile("s_waitcnt vmcnt(%0) lgkmcnt(0)\n\ts_barrier" ::"i"(12 + nst) : "memory");
    }
  }
  if constexpr (END && GG == 127) {
    asm volatile("s_nop 7");
    e_ops<TR, false, ESCH<TR, MODE>.op0[128]>(make_iseq<ESCH<TR, MODE>.nop[128]>{});
  }
  GAP();
}
template <bool TR, int P, int H, int MODE, int... Gs> DI void g_half_(GM& g, iseq<Gs...>) { (g_gap<TR, P, H, Gs, MODE>(g), ...); }
template <bool TR, int P, int H, int MODE> DI void g_half(GM& g) { g_half_<TR, P, H, MODE>(g, make_iseq<64>{}); }
template <bool TR, int P, int MODE> DI void g_tile(GM& g) { g_half<TR, P, 0, MODE>(g); g_half<TR, P, 1, MODE>(g); }
template <int P, int... Is> DI void g_read0(GM& g, iseq<Is...>) {
  (g_wread<P, 0, Is>(g), ...); (g_wread<P, 1, Is>(g), ...);
  g_aread<P, 0, 0>(g); g_aread<P, 0, 1>(g);
}
DI void g_proW(unsigned g, unsigned mW) {
  asm volatile("v_lshl_add_u32 v91, %0, 18, v93\n\ts_mov_b32 m0, %1" ::"s"(g), "s"(mW));
  asm volatile("s_nop 0"); g_dma<true, 0, 91>();
  asm volatile("s_bitset1_b32 m0, 12\n\ts_nop 0"); g_dma<true, 1, 91>();
  asm volatile("s_bitset1_b32 m0, 13\n\ts_nop 0"); g_dma<true, 3, 91>();
  asm volatile("s_bitset0_b32 m0, 12\n\ts_nop 0"); g_dma<true, 2, 91>();
}
template <int PART = 3> DI void g_pro(unsigned i, unsigned mA, unsigned mW, unsigned lda) {
  i &= 3;
  if constexpr (PART & 1)
    asm volatile("v_add_u32 v90, %0, v92\n\tv_add_u32 v91, %1, v93\n\ts_mov_b32 m0, %2\n\ts_mov_b32 s54, %3" ::"s"(((i & 1) + (i >> 1) * 4) * 64 * lda),
                 "s"((i & 1) * 0x20000 + (i >> 1) * 256), "s"(mA + (i & 1) * 4096 + (i >> 1) * 16384), "s"(mW + i * 4096));
  if constexpr (PART == 1) return;
  g_dma<false, 0, 90>();
  asm volatile("s_bitset1_b32 m0, 13\n\ts_nop 0"); g_dma<false, 2, 90>();
  asm volatile("s_mov_b32 m0, s54\n\ts_nop 0"); g_dma<true, 0, 91>();
  asm volatile("s_bitset1_b32 m0, 14\n\ts_nop 0"); g_dma<true, 4, 91>();
  asm volatile("s_xor_b32 m0, s54, 0x8000\n\ts_nop 0\n\tbuffer_load_dwordx4 v91, s[60:63], s53 offen lds" ::: "memory", "scc");
}
DI void g_pro2(unsigned mA, unsigned mW) {
  asm volatile("v_add_u32 v90, 128, v92\n\ts_mov_b32 m0, %0" ::"s"(mA ^ 32768));
  asm volatile("s_nop 0"); g_dma<false, 0, 90>();
  asm volatile("s_bitset1_b32 m0, 12\n\ts_nop 0"); g_dma<false, 1, 90>();
  asm volatile("s_bitset1_b32 m0, 14\n\ts_nop 0"); g_dma<false, 5, 90>();
  asm volatile("s_bitset0_b32 m0, 12\n\ts_nop 0"); g_dma<false, 4, 90>();
  g_proW(3, mW ^ 49152);
}
template <bool TR, bool SCL, class HOOK> DI void g_loop(GM& g, unsigned evm, HOOK&& hook) {
  constexpr int nkt = SCL ? 7 : 16, P0 = SCL ? 0 : 1;
  asm volatile("" : "+s"(evm));
#pragma unroll 1
  for (int t = 1;; t += 2) {
    g_half<TR, P0 ^ 1, 0, 0>(g);
    if (__builtin_expect((evm >> t) & 1, 0)) { hook(t); GAP(); }
    g_half<TR, P0 ^ 1, 1, 0>(g);
    if (SCL && t + 2 >= nkt) break;
    g_half<TR, P0, 0, 0>(g);
    if constexpr (SCL) { if (__builtin_expect(t == 3, 0)) { hook(4); GAP(); } }
    else asm volatile("s_cmp_eq_u32 %0, 13\n\ts_cselect_b32 s78, 0x28000, s78" ::"s"(t) : "scc");
    g_half<TR, P0, 1, 0>(g);
    if (!SCL && t + 3 >= nkt) break;
  }
}

typedef __attribute__((ext_vector_type(4))) unsigned u32x4;
typedef __attribute__((ext_vector_type(4))) float f32x4;

constexpr int R_S = 128, R_P = 192, R_CI = 224, R_E = 96;
constexpr int R_TS = 112, R_RS = 114, R_M = 116;
constexpr int R_PD = 118, R_THR = 120, R_TMP = 121;
constexpr int r_dk(int q) { return q < 6 ? 90 + q : 120 + q; }
constexpr int S_V = 96, S_T = 97, S_K = 98, S_I = 99, S_EV = 100;
struct AC {
  unsigned rare;
  unsigned tb, vs, o0, o1;
};
DI bool redo() { u64 t; asm volatile("s_mov_b64 %0, s[54:55]" : "=s"(t)); return t != 0; }
template <int X, int F> DI void kread() {
  constexpr int r = 192 + 4 * (F & 15);
  asm volatile("ds_read_b128 a[%c0:%c1], v84 offset:%c2" ::"i"(r), "i"(r + 3), "i"(X * 32768 + F * 1024));
}
template <int X, int F> DI void vread() {
  constexpr int r = 192 + 4 * (F & 15);
  asm volatile("ds_read_b128 a[%c0:%c1], v85 offset:%c2" ::"i"(r), "i"(r + 3), "i"(X * 32768 + (F & 1) * 16384 + (F >> 1) * 1024));
}
template <int X, int... Fs> DI void kreads(iseq<Fs...>) { (kread<X, Fs>(), ...); }
template <int X, int J> DI void dma_m0() {
  if constexpr (J == 0) asm volatile("s_mov_b32 m0, s%c0" ::"i"(78 + X));
  else if constexpr (J == 4) asm volatile("s_or_b32 m0, s%c0, 0x1000" ::"i"(78 + X));
  else if constexpr (J == 8) asm volatile("s_bitset0_b32 m0, 16");
  else if constexpr (J == 12) asm volatile("s_bitset0_b32 m0, 12");
}
template <int J> DI void dma_at() {
  if constexpr (J < 8)
    asm volatile("buffer_load_dwordx4 v%c0, s[88:91], s96 offen offset:%1 sc1 lds" ::"i"(86 + (J >= 4)), "i"((J & 3) * 1024) : "memory");
  else
    asm volatile("buffer_load_dwordx4 v%c0, s[92:95], s98 offen offset:%1 sc1 lds" ::"i"(r_dk(J - 8)), "i"((J & 3) * 1024) : "memory");
}
template <int Q> DI void dma4k() {
  asm volatile("s_xor_b32 m0, s79, %0\n\ts_add_u32 s48, s98, %1\n\tbuffer_load_dwordx4 v%c2, s[88:91], s48 offen sc1 lds\n\t"
               "buffer_load_dwordx4 v%c2, s[88:91], s48 offen offset:1024 sc1 lds\n\tbuffer_load_dwordx4 v%c2, s[88:91], s48 offen offset:2048 sc1 lds\n\t"
               "buffer_load_dwordx4 v%c2, s[88:91], s48 offen offset:3072 sc1 lds"
               ::"i"((Q >> 1) * 32768 + (Q & 1) * 4096), "i"((Q >> 1) * 32768), "i"(86 + (Q & 1)) : "memory", "scc");
}
DI void acc_zero() { static_for([](auto I) { asm volatile("v_accvgpr_write_b32 a[%c0], 0" ::"i"((int)I)); }, make_iseq<128>{}); }
#define SMV 0
struct SMS { int ex[128], op0[128], nop[128], last; };
constexpr SMS sm_sched() {
  SMS z{};
  int et[32] = {}, e = 0;
  for (int t = 0; t < 128; ++t) {
    const int g = t & 15;
    const bool x = t < 48 ? (g >= 8 && (g & 1) && g < 15) : t < 64 ? (g >= 9 && g < 15) : (g >= 8 && g < 14);
    z.ex[t] = x && e < 32 ? e : -1;
    if (z.ex[t] >= 0) et[e++] = t;
  }
  int o = 0;
  for (int t = 0; t < 128; ++t) {
    const int g = t & 15;
    int cap = z.ex[t] >= 0 || t < 8 || (t < 48 && (g == 7 || (g >= 8 && !(g & 1)))) || t == 56 || t == 63 || t == 111 ? 0 : 1;
    z.op0[t] = o; z.nop[t] = 0;
    while (cap > 0 && o < 48 && et[2 * (o / 3) + (o % 3 == 0 ? 0 : 1)] + 2 <= t) {
      const int el = 2 * (o / 3) + o % 3;
      if (!(o % 3 < 2 && (el == 0 || el == 4))) --cap;
      ++o; ++z.nop[t]; z.last = t;
    }
  }
  for (int k = 16; k < 32; ++k) if (z.op0[et[k]] < 3 * ((k - 16) / 2 + 1)) z.last = 1000;
  return z;
}
constexpr SMS SM = sm_sched();
static_assert(SM.last <= 117);
constexpr int p_reg(int Y, int k) { return R_P + 16 * Y + 4 * ((k >> 3) * 2 + ((k >> 1) & 1)) + ((k >> 2) & 1) * 2 + (k & 1); }
template <int Y, int O> DI void sm_op() {
  constexpr int k = O / 3, e = 2 * k + O % 3, ts = R_TS + ((e >> 2) & 1);
  if constexpr (O % 3 == 2) asm volatile("v_cvt_pk_bf16_f32 v%c0, v%c1, v%c2" ::"i"(p_reg(Y, k)), "i"(R_E + ((2 * k) & 15)), "i"(R_E + ((2 * k + 1) & 15)));
  else if constexpr (e == 1 || e == 5) asm volatile("v_add_f32_e32 v%c0, v%c1, v%c2" ::"i"(ts), "i"(R_E + e - 1), "i"(R_E + e));
  else if constexpr (e != 0 && e != 4) asm volatile("v_add_f32_e32 v%c0, v%c0, v%c1" ::"i"(ts), "i"(R_E + (e & 15)));
}
template <int Y, int T> DI void sm_gap(AC& c) {
  if constexpr (SM.ex[T] >= 0) asm volatile("v_exp_f32 v%c0, v%c1" ::"i"(R_E + (SM.ex[T] & 15)), "i"(R_S + 32 * Y + SM.ex[T]));
  if constexpr (SM.nop[T] > 0) sm_op<Y, SM.op0[T]>();
  if constexpr (SM.nop[T] > 1) sm_op<Y, SM.op0[T] + 1>();
  if constexpr (SM.nop[T] > 2) sm_op<Y, SM.op0[T] + 2>();
  if constexpr (T == SM.last + 1) asm volatile("v_add_f32_e32 v%c0, v%c1, v%c2" ::"i"(R_TMP), "i"(R_TS), "i"(R_TS + 1));
  if constexpr (T == SM.last + 2) asm volatile("v_cmp_nle_f32_e64 s[54:55], v%c0, v%c1" ::"i"(R_TMP), "i"(R_THR));
}
template <int X, int MODE, int G> DI void gapA(AC& c) {
  constexpr bool FIRST = MODE == 1 || MODE == 2, NOQK = MODE == 3;
  constexpr int Xp = X ^ 1, kb = G >> 4, g = G & 15, qb = g >> 3, s = g & 7;
  if constexpr (MODE == 3 && G < 48 && G % 3 == 0) {
    constexpr int f = G / 3;
    asm volatile("buffer_load_dwordx4 a[%c0:%c1], v84, s[88:91], s75 offen offset:%2 sc1" ::"i"(128 + 4 * f), "i"(131 + 4 * f), "i"((f & 3) * 1024) : "memory");
    if constexpr ((f & 3) == 3) asm volatile("s_addk_i32 s75, %0" ::"i"(f == 7 ? 28672 : 4096));
  }
  if constexpr (G == 48) asm volatile("s_waitcnt vmcnt(%0) lgkmcnt(0)\n\ts_barrier" ::"i"(MODE == 3 ? 16 : MODE == 2 ? 12 : MODE == 1 ? 7 : 0) : "memory");
  else if constexpr (g == 0 && !NOQK) asm volatile("s_waitcnt lgkmcnt(0)");
  if constexpr (!NOQK) {
    constexpr int r = 192 + 4 * ((kb & 1) * 8 + s), q = 128 + 4 * g;
    constexpr int d = R_S + 32 * X + 4 * (kb * 2 + qb), ci = s == 0 ? R_CI + 4 * (kb * 2 + qb) : d;
    if constexpr (FIRST && s == 0)
      asm volatile("v_mfma_f32_16x16x32_bf16 v[%c0:%c1], a[%c2:%c3], a[%c4:%c5], 0" ::"i"(d), "i"(d + 3), "i"(r), "i"(r + 3), "i"(q), "i"(q + 3));
    else
      asm volatile("v_mfma_f32_16x16x32_bf16 v[%c0:%c1], a[%c2:%c3], a[%c4:%c5], v[%c6:%c7]" ::"i"(d), "i"(d + 3), "i"(r), "i"(r + 3), "i"(q), "i"(q + 3),
                   "i"(ci), "i"(ci + 3));
    if constexpr (g < 8 && kb < 3) kread<X, (kb + 1) * 8 + g>();
    constexpr int S0 = MODE == 1 ? 0 : SMV ? 40 : 14, S1 = MODE == 1 ? 1 : SMV ? 41 : 30, S2 = SMV ? 42 : 45, S3 = SMV ? 43 : 46;
    if constexpr (G == S0) asm volatile("s_add_u32 s96, s97, 0x1000000");
    if constexpr (G == S1) dma_m0<X, 0>();
    if constexpr (G == S2) asm volatile("s_mov_b32 s97, s98");
    if constexpr (G == S3) asm volatile("s_add_u32 s98, s98, s99");
    if constexpr (MODE != 1 && G >= 56 && !(G & 1)) dma_at<(G - 56) / 2>();
    if constexpr (MODE == 1 && (G & 11) == 9) dma_at<(G >> 4) * 2 + ((G >> 2) & 1)>();
    if constexpr (MODE == 1 && G == 31) dma_m0<X, 4>();
    if constexpr (G == 57) asm volatile("s_lshr_b32 s100, s100, 1\n\ts_add_u32 s56, s56, 1");
    if constexpr (G == 59) asm volatile("s_getpc_b64 s[86:87]\nev_pc%c0:\n\ts_add_u32 s86, s86, ev_ret%c0-ev_pc%c0\n\ts_addc_u32 s87, s87, 0" ::"i"(X * 4 + MODE));
    if constexpr (G == 61 && !FIRST) asm volatile("s_or_b32 s57, s54, s55\n\ts_mov_b32 %0, s57\n\ts_bitcmp1_b32 s100, 0" : "=s"(c.rare));
    if constexpr (G == 61 && FIRST) asm volatile("s_bitcmp1_b32 s100, 0");
  } else if constexpr (SM.nop[64 + G] > 0) asm volatile("s_nop 0");
  if constexpr (!FIRST && g < 8 && kb == 3) vread<Xp, g>();
  if constexpr (MODE == 2 && (G & 3) == 0) d_store<16 + G / 4>();
  if constexpr (!FIRST) sm_gap<Xp, 64 + G>(c);
  if constexpr (G == 63 && !NOQK) asm volatile("s_cbranch_scc1 ev_entry\nev_ret%c0:" ::"i"(X * 4 + MODE) : "vcc", "memory");
  GAP();
}
template <int R> DI void rescale1(float alpha) {
  float t;
  asm volatile("v_accvgpr_read_b32 %0, a[%c2]\n\tv_mul_f32 %0, %0, %1\n\tv_accvgpr_write_b32 a[%c2], %0" : "=&v"(t) : "v"(alpha), "i"(R));
}
template <int... Rs> DI void rescale_all(const float (&alpha)[2], iseq<Rs...>) { (rescale1<Rs>(alpha[(Rs >> 2) & 1]), ...); }
template <bool MAX> DI float row_reduce(float v) {
  auto p = __builtin_amdgcn_permlane32_swap(__float_as_uint(v), __float_as_uint(v), false, false);
  v = MAX ? fmaxf(__uint_as_float(p[0]), __uint_as_float(p[1])) : __uint_as_float(p[0]) + __uint_as_float(p[1]);
  p = __builtin_amdgcn_permlane16_swap(__float_as_uint(v), __float_as_uint(v), false, false);
  return MAX ? fmaxf(__uint_as_float(p[0]), __uint_as_float(p[1])) : __uint_as_float(p[0]) + __uint_as_float(p[1]);
}
template <int Y, int QB> DI float row_max() {
  constexpr int r = R_S + 32 * Y + 4 * QB;
  float mx;
  asm volatile("v_max_f32 %0, v%c1, v%c2\n\tv_max3_f32 %0, %0, v%c3, v%c4" : "=&v"(mx) : "i"(r), "i"(r + 1), "i"(r + 2), "i"(r + 3));
  static_for([&](auto K) {
    constexpr int q = r + 8 * (K + 1);
    float t = mx;
    asm volatile("v_max3_f32 %0, %0, v%c1, v%c2\n\tv_max3_f32 %0, %0, v%c3, v%c4" : "+v"(t) : "i"(q), "i"(q + 1), "i"(q + 2), "i"(q + 3));
    mx = t;
  }, make_iseq<3>{});
  return row_reduce<true>(mx);
}
template <int R> DI void v_sub(float d) { asm volatile("v_sub_f32 v%c0, v%c0, %1" ::"i"(R), "v"(d)); }
template <int B> DI void s_shift(const float (&d)[2]) { static_for([&](auto E) { v_sub<B + E>(d[(E >> 2) & 1]); }, make_iseq<32>{}); }
template <bool CI> DI void m_shift(const float (&d)[2]) {
  if constexpr (CI) s_shift<R_CI>(d);
  v_sub<R_M>(-d[0]); v_sub<R_M + 1>(-d[1]);
}
DI void set_mask0() {
  const int mr = __float_as_int(vget<89>());
  static_for([&](auto I) {
    constexpr int e = (I >> 2) * 8 + (I & 3), k = (I >> 2) * 16 + (I & 3);
    const float x = vget<R_S + e>();
    vset<R_S + e>(k > mr ? -INFINITY : x);
  }, make_iseq<16>{});
}
template <int X, bool CI = true> DI void sm_redo() {
  constexpr int Xp = X ^ 1;
  float d[2], dt[2], alpha[2], ts[2] = {0.f, 0.f};
  static_for([&](auto Q) {
    constexpr int qb = Q;
    const float pd = vget<R_PD + qb>();
    d[qb] = fmaxf(row_max<Xp, qb>() - pd, 0.f);
    dt[qb] = d[qb] + pd;
    vset<R_PD + qb>(d[qb]);
    alpha[qb] = __builtin_amdgcn_exp2f(-d[qb]);
    vset<R_RS + qb>(vget<R_RS + qb>() * alpha[qb]);
  }, make_iseq<2>{});
  vset<R_THR>(d[0] > 0.f || d[1] > 0.f ? -1.f : 4096.f);
  m_shift<CI>(d);
  static_for([&](auto K) {
    constexpr int k = K, qb = (k >> 1) & 1;
    const float e0 = __builtin_amdgcn_exp2f(vget<R_S + 32 * Xp + 2 * k>() - dt[qb]), e1 = __builtin_amdgcn_exp2f(vget<R_S + 32 * Xp + 2 * k + 1>() - dt[qb]);
    ts[qb] += e0; ts[qb] += e1;
    vset<p_reg(Xp, k)>(__uint_as_float(pk2(e0, e1)));
  }, make_iseq<16>{});
  vset<R_TS>(ts[0]); vset<R_TS + 1>(ts[1]);
  rescale_all(alpha, make_iseq<128>{});
  asm volatile("s_nop 1" ::: "memory");
  GAP();
}
template <int X, int MODE, int... Gs> DI void phaseA_(AC& c, iseq<Gs...>) { (gapA<X, MODE, Gs>(c), ...); }
template <int X, int MODE> DI void phaseA(AC& c) { phaseA_<X, MODE>(c, make_iseq<64>{}); }

template <int X, bool SMX, int G, bool FIRST = false> DI void gapB(AC& c) {
  constexpr int Xp = X ^ 1, blk = G >> 4, g = G & 15;
  if constexpr (g == 0) asm volatile("s_waitcnt lgkmcnt(0)");
  constexpr int qb = (g >> 1) & 1, s = g & 1, r = 192 + 4 * ((blk & 1) * 8 + (g >> 2) * 2 + s), o = 4 * ((blk * 4 + (g >> 2)) * 2 + qb);
  constexpr int p = R_P + 16 * Xp + 4 * (s * 2 + qb);
  if constexpr (FIRST && s == 0)
    asm volatile("v_mfma_f32_16x16x32_bf16 a[%c0:%c1], a[%c2:%c3], v[%c4:%c5], 0" ::"i"(o), "i"(o + 3), "i"(r), "i"(r + 3), "i"(p), "i"(p + 3));
  else
  asm volatile("v_mfma_f32_16x16x32_bf16 a[%c0:%c1], a[%c2:%c3], v[%c4:%c5], a[%c0:%c1]" ::"i"(o), "i"(o + 3), "i"(r), "i"(r + 3), "i"(p), "i"(p + 3));
  if constexpr (g < 8) {
    if constexpr (blk < 3) vread<Xp, (blk + 1) * 8 + g>();
    else if constexpr (SMX) kread<Xp, g>();
  }
  if constexpr (SMX) {
    if constexpr (G == 1 || G == 2) asm volatile("v_add_f32_e32 v%c0, v%c0, v%c1" ::"i"(R_RS + G - 1), "i"(R_TS + G - 1));
    if constexpr (g == 7 && blk < 3) dma_m0<X, blk * 4 + 4>();
    if constexpr (g >= 8 && !(g & 1) && blk < 3) dma_at<blk * 4 + 4 + (g - 8) / 2>();
    if constexpr (G == 55) asm volatile("s_cmp_eq_u32 s56, s76");
    if constexpr (G == 3) asm volatile("s_lshr_b32 s77, s77, 1");
    if constexpr (G == 4) asm volatile("s_bitcmp1_b32 s77, 0");
    if constexpr (G == 5) asm volatile("s_cselect_b64 s[52:53], s[62:63], 0");
    if constexpr (G == 6) asm volatile(FL_ARM("s[52:53]"));
    if constexpr (G == 56) asm volatile(FL_POLL ::: "memory");
    sm_gap<X, G>(c);
  }
  GAP();
}
template <int X, bool FIRST, int... Gs> DI void phaseB_(AC& c, iseq<Gs...>) { (gapB<X, true, Gs, FIRST>(c), ...); }
template <int X, bool FIRST = false> DI void phaseB(AC& c) { phaseB_<X, FIRST>(c, make_iseq<64>{}); }
constexpr int sm_nex() { int n = 0; for (int t = 0; t < 64; ++t) n += SM.ex[t] >= 0; return n; }
template <int X> DI void phaseB0(unsigned mode, bool vh) {
  dma_m0<X, 4>();
  kreads<X ^ 1>(make_iseq<8>{});
  if (vh) { dma_at<4>(); dma_at<5>(); dma_at<6>(); dma_at<7>(); }
  dma_m0<X, 8>();
  asm volatile("s_nop 3");
  float d[2];
  if (mode & 2) {
    d[0] = vget<122>(); d[1] = vget<123>();
    dma_at<8>();
  } else {
    if constexpr (X == 0) if (__builtin_expect(mode & 4, 0)) set_mask0();
    d[0] = row_max<X, 0>(); GAP(); dma_at<8>();
    d[1] = row_max<X, 1>();
#pragma unroll
    for (int qb = 0; qb < 2; ++qb) if (!(d[qb] > -3.0e38f)) d[qb] = 0.f;
  }
  GAP();
  vset<R_M>(d[0]); vset<R_M + 1>(d[1]);
  dma_at<9>();
  const f32x2c nd[2] = {{-d[0], -d[0]}, {-d[1], -d[1]}};
  static_for([&](auto E) {
    constexpr int e = E;
    const f32x2c v = nd[(e >> 1) & 1];
    asm volatile("v_pk_add_f32 v[%c0:%c1], v[%c0:%c1], %4\n\tv_mov_b64 v[%c2:%c3], %4" ::"i"(R_S + 32 * X + 2 * e), "i"(R_S + 32 * X + 2 * e + 1), "i"(R_CI + 2 * e), "i"(R_CI + 2 * e + 1), "v"(v));
    if constexpr (e == 7 || e == 15) dma_at<10 + e / 8>();
  }, make_iseq<16>{});
  static_assert(SM.op0[64] >= 3 * ((sm_nex() - 15) / 2) && SM.op0[64] <= 24);
  static_for([&](auto E) {
    constexpr int e = E;
    if constexpr (e < sm_nex()) asm volatile("v_exp_f32 v%c0, v%c1" ::"i"(R_E + e), "i"(R_S + 32 * X + e));
    if constexpr (e == 2) dma_m0<X, 12>();
    if constexpr (e == 5 || e == 10 || e == 15) dma_at<11 + e / 5>();
  }, make_iseq<16>{});
  static_for([&](auto O) { sm_op<X, O>(); }, make_iseq<SM.op0[64]>{});
  dma_at<15>();
  static_for([&](auto E) { asm volatile("v_exp_f32 v%c0, v%c1" ::"i"(R_E + (int)E), "i"(R_S + 32 * X + 16 + (int)E)); }, make_iseq<(sm_nex() > 16 ? sm_nex() - 16 : 0)>{});
  asm volatile("buffer_store_dwordx2 v[%c0:%c1], v88, s[88:91], s76 offen sc1\n\ts_bitcmp1_b32 s77, 0\n\ts_cselect_b64 s[52:53], s[62:63], 0\n\t"
               FL_ARM("s[52:53]") "\n\t" FL_POLL ::"i"(R_M), "i"(R_M + 1) : "memory", "scc");
  GAP();
}
enum : int { T_Q0, T_K0, T_OQ, T_OG, T_FPREP, T_FQ, T_FG, T_FS, T_ZO, T_CNT, T_NPRE = 10, T_NST = 12, T_TAB = 14, T_SLAB = 16, T_PART, T_IT = 18, T_ITN = 23 };
#define MSK(R, K) "v_cmp_gt_i32 s[48:49], " #K ", v89\n\tv_cmp_gt_i32 s[50:51], " #K "+1, v89\n\tv_cmp_gt_i32 s[52:53], " #K "+2, v89\n\t" \
                  "v_cmp_gt_i32 s[54:55], " #K "+3, v89\n\tv_cndmask_b32_e64 v[" #R "], v[" #R "], v122, s[48:49]\n\t" \
                  "v_cndmask_b32_e64 v[" #R "+1], v[" #R "+1], v122, s[50:51]\n\tv_cndmask_b32_e64 v[" #R "+2], v[" #R "+2], v122, s[52:53]\n\t" \
                  "v_cndmask_b32_e64 v[" #R "+3], v[" #R "+3], v122, s[54:55]\n\t"
DI void ev_code(AC& c) {
  GAP();
  asm volatile(
    "s_getpc_b64 s[82:83]\n\ts_add_u32 s82, s82, 12\n\ts_addc_u32 s83, s83, 0\n\ts_branch 9f\nev_entry:\n\t"
    "s_bitcmp1_b32 s60, s56\n\ts_cbranch_scc1 3f\n\ts_bitcmp1_b32 s74, s56\n\ts_cbranch_scc1 2f\n\t"
    "s_bitcmp1_b32 s58, s56\n\ts_cselect_b64 vcc, -1, 0\n\tv_cndmask_b32 v121, -2.0, v81, vcc\n\t"
    "buffer_store_dwordx2 v[82:83], v121, s[88:91], s70 offen sc1\n\tbuffer_store_dwordx2 v[82:83], v121, s[88:91], s71 offen sc1\n\t"
    "v_cmp_eq_u64 vcc, s[84:85], v[124:125]\n\ts_andn2_b64 vcc, s[62:63], vcc\n\ts_cbranch_scc1 5f\n"
    "0:\n\ts_add_u32 s98, s98, s59\n\ts_mov_b32 s59, s61\n\ts_mov_b32 s61, s72\n\ts_mov_b64 s[62:63], s[64:65]\n\ts_mov_b64 s[64:65], s[66:67]\n\t"
    "s_mov_b64 s[66:67], s[68:69]\n\ts_cmp_eq_u32 s56, 1\n\ts_cbranch_scc1 ev_first\n\ts_setpc_b64 s[86:87]\n"
    "5:\n\t" FL_CHECK("s[62:63]") "\n\ts_branch 0b\n"
    "6:\n\t" FL_CHECK("s[62:63]") "\n\ts_setpc_b64 s[86:87]\n"
    "2:\n\ts_mov_b32 s98, 0\n\t"
    "v_lshrrev_b32 v90, 7, v86\n\tv_and_b32 v91, 6, v90\n\tv_lshlrev_b32 v91, 4, v91\n\tv_and_b32 v92, 0x70, v86\n\tv_xor_b32 v91, v91, v92\n\t"
    "s_movk_i32 s48, 896\n\tv_mad_u32_u24 v94, v90, s48, v91\n\ts_mov_b64 s[92:93], s[80:81]\n\ts_mov_b32 s94, 229376\n\t"
    "v_add_u32 v90, 28672, v94\n\tv_add_u32 v91, 34816, v94\n\tv_add_u32 v92, 40960, v94\n\tv_add_u32 v93, 47104, v94\n\t"
    "v_add_u32 v95, 6144, v94\n\tv_add_u32 v126, 12288, v94\n\tv_add_u32 v127, 18432, v94\n\t"
    "v_mov_b32 v122, 0xff800000\n\t" MSK(228, 0) MSK(236, 16) MSK(244, 32) MSK(252, 48)
    "v_cmp_eq_u64 vcc, s[84:85], v[124:125]\n\ts_andn2_b64 vcc, s[62:63], vcc\n\ts_cbranch_scc1 6b\n\ts_setpc_b64 s[86:87]\n"
    "3:\n\tv_mov_b32 v122, 0xff800000\n\t" MSK(224, 0) MSK(232, 16) MSK(240, 32) MSK(248, 48)
    "v_readlane_b32 s54, v78, %0\n\ts_add_u32 m0, s78, 0x10000\n\t"
    "s_bfe_u32 s53, s73, 0x80010\n\ts_mulk_i32 s53, 3840\n\ts_mov_b64 s[48:49], s[80:81]\n\ts_add_u32 s50, s53, 3840\n\ts_min_u32 s50, s50, 229376\n\t"
    "s_mov_b32 s51, 0x20000\n\t"
    "v_lshlrev_b32 v121, 3, v84\n\tbuffer_load_dword v121, s[48:51], s53 offen lds\n\t"
    "s_bitcmp0_b32 s73, 3\n\ts_cbranch_scc1 4f\n\t"
    "buffer_load_dwordx4 v84, s[88:91], s54 offen sc1 lds\n\tbuffer_load_dwordx4 v84, s[88:91], s54 offen offset:1024 sc1 lds\n\t"
    "buffer_load_dwordx4 v84, s[88:91], s54 offen offset:2048 sc1 lds\n\tbuffer_load_dwordx4 v84, s[88:91], s54 offen offset:3072 sc1 lds\n"
    "4:\n\ts_cmp_eq_u32 s56, 1\n\ts_cbranch_scc1 ev_first\n\ts_setpc_b64 s[86:87]\n"
    ::"i"((int)T_ZO) : "scc");
  asm volatile("ev_zero:");
  acc_zero();
  asm volatile("s_setpc_b64 s[86:87]\nev_first:\n\ts_cmp_lg_u32 s57, 0\n\ts_cbranch_scc1 ev_zero\n\ts_bitcmp0_b32 s73, 4\n\ts_cbranch_scc1 ev_first1");
  GAP();
  phaseB<0, true>(c);
  asm volatile("s_branch att_loop\nev_first1:");
  phaseB<1, true>(c);
  asm volatile("s_branch att_mid\n9:");
  GAP();
}
DI void ev_call() { asm volatile("s_swappc_b64 s[86:87], s[82:83]" ::: "vcc", "scc", "memory"); GAP(); }

#define ACLOB_ALL "a0","a1","a2","a3","a4","a5","a6","a7","a8","a9","a10","a11","a12","a13","a14","a15","a16","a17","a18","a19","a20","a21","a22","a23","a24","a25","a26","a27","a28","a29","a30","a31","a32","a33","a34","a35","a36","a37","a38","a39","a40","a41","a42","a43","a44","a45","a46","a47","a48","a49","a50","a51","a52","a53","a54","a55","a56","a57","a58","a59","a60","a61","a62","a63","a64","a65","a66","a67","a68","a69","a70","a71","a72","a73","a74","a75","a76","a77","a78","a79","a80","a81","a82","a83","a84","a85","a86","a87","a88","a89","a90","a91","a92","a93","a94","a95","a96","a97","a98","a99","a100","a101","a102","a103","a104","a105","a106","a107","a108","a109","a110","a111","a112","a113","a114","a115","a116","a117","a118","a119","a120","a121","a122","a123","a124","a125","a126","a127","a128","a129","a130","a131","a132","a133","a134","a135","a136","a137","a138","a139","a140","a141","a142","a143","a144","a145","a146","a147","a148","a149","a150","a151","a152","a153","a154","a155","a156","a157","a158","a159","a160","a161","a162","a163","a164","a165","a166","a167","a168","a169","a170","a171","a172","a173","a174","a175","a176","a177","a178","a179","a180","a181","a182","a183","a184","a185","a186","a187","a188","a189","a190","a191","a192","a193","a194","a195","a196","a197","a198","a199","a200","a201","a202","a203","a204","a205","a206","a207","a208","a209","a210","a211","a212","a213","a214","a215","a216","a217","a218","a219","a220","a221","a222","a223","a224","a225","a226","a227","a228","a229","a230","a231","a232","a233","a234","a235","a236","a237","a238","a239","a240","a241","a242","a243","a244","a245","a246","a247","a248","a249","a250","a251","a252","a253","a254","a255"

struct EP { float fa[2], fb[2]; };
template <int I, int E, int MODE> DI void epi_pair(const EP& p) {
  constexpr int qb = I & 1, r = 16 * (I >> 1) + 4 * qb + (E >> 1) * 8 + (E & 1) * 2, t = 96 + 2 * E, w = 128 + 4 * I + E;
  if constexpr (MODE == 2) asm volatile("v_accvgpr_read_b32 v106, a[%c0]" ::"i"(w));
  asm volatile("v_accvgpr_read_b32 v%c0, a[%c2]\n\tv_accvgpr_read_b32 v%c1, a[%c3]" ::"i"(t), "i"(t + 1), "i"(r), "i"(r + 1));
  if constexpr (MODE) asm volatile("v_mul_f32 v%c0, v%c0, %2\n\tv_mul_f32 v%c1, v%c1, %2" ::"i"(t), "i"(t + 1), "v"(p.fa[qb]));
  if constexpr (MODE == 2)
    asm volatile("v_lshlrev_b32 v104, 16, v106\n\tv_and_b32 v105, 0xffff0000, v106\n\tv_fmac_f32 v%c0, v104, %2\n\tv_fmac_f32 v%c1, v105, %2"
                 ::"i"(t), "i"(t + 1), "v"(p.fb[qb]));
  asm volatile("v_cvt_pk_bf16_f32 v%c0, v%c1, v%c2" ::"i"(w), "i"(t), "i"(t + 1));
}
template <int I> DI void epi_store() {
  asm volatile("buffer_store_dwordx4 v[%c0:%c1], v84, s[88:91], s101 offen offset:%2 sc1" ::"i"(128 + 4 * I), "i"(131 + 4 * I), "i"((I & 3) * 1024) : "memory");
  if constexpr ((I & 3) == 3) asm volatile("s_addk_i32 s101, 4096");
}
constexpr int R_TB = 232, R_VS = 236;
template <int HF, int R> DI void p_read() {
  constexpr int mb = R >> 4, r = 192 - 64 * HF + 2 * R;
  asm volatile("ds_read_b64_tr_b16 a[%c1:%c2], v%c0 offset:%c3" ::"i"(R_TB + 2 * HF + mb), "i"(r), "i"(r + 1), "i"((R & 15) * 2048));
}
template <int QB, int E> DI void p_cvt() {
  asm volatile("v_cvt_pk_bf16_f32 v%c0, v%c1, v%c2" ::"i"(208 + 4 * QB + E), "i"(192 + 8 * QB + 2 * E), "i"(193 + 8 * QB + 2 * E));
}
template <int QB, int PR> DI void p_store() {
  asm volatile("buffer_store_dwordx4 v[%c0:%c1], v%c2, s[88:91], s96 offen offset:%3 sc1" ::"i"(208 + 4 * QB), "i"(211 + 4 * QB), "i"(R_VS + QB),
               "i"(PR * 128) : "memory");
}
constexpr int p_dma(int HF, int G) { return G == 3 && !HF ? 0 : G == 11 ? 1 - HF : G == 15 ? 2 - HF : G == 19 ? 3 - HF : G == 27 && HF ? 3 : -1; }
template <int HF, bool LD, int G> DI void p_gap() {
  constexpr int ch = G >> 3, t = G & 7, a = 192 - 64 * HF + 32 * (ch & 1) + 4 * t, b = 128 + 4 * (2 * t + (ch >> 1)), d = 192 + 4 * ch;
  if constexpr (t == 0)
    asm volatile("v_mfma_f32_16x16x32_bf16 v[%c0:%c1], a[%c2:%c3], v[%c4:%c5], 0" ::"i"(d), "i"(d + 3), "i"(a), "i"(a + 3), "i"(b), "i"(b + 3));
  else
    asm volatile("v_mfma_f32_16x16x32_bf16 v[%c0:%c1], a[%c2:%c3], v[%c4:%c5], v[%c0:%c1]" ::"i"(d), "i"(d + 3), "i"(a), "i"(a + 3), "i"(b), "i"(b + 3));
  p_read<HF ^ 1, G>();
  if constexpr (G == 0) asm volatile("v_xor_b32 v%c0, s100, v%c0" ::"i"(R_TB + 2 * HF + 1));
  if constexpr (G == 17) asm volatile("v_xor_b32 v%c0, s100, v%c0" ::"i"(R_TB + 2 * (HF ^ 1)));
  if constexpr (G >= 4 && G <= 8 && G != 7) p_cvt<1, (G < 7 ? G - 4 : 3)>();
  if constexpr (G == 24 || (G == 7 && HF == 0)) asm volatile("s_waitcnt lgkmcnt(8)");
  if constexpr (G == 7 && HF == 1) asm volatile("s_waitcnt vmcnt(%0) lgkmcnt(8)\n\ts_barrier" ::"i"(LD ? 13 : 5) : "memory");
  if constexpr (G == 10) p_store<1, HF ^ 1>();
  if constexpr (G >= 20 && G < 24) p_cvt<0, G - 20>();
  if constexpr (G == 25) p_store<0, HF>();
  if constexpr (LD && p_dma(HF, G) >= 0) dma_at<(HF ? 8 : 12) + p_dma(HF, G)>();
  if constexpr (LD && HF == 0 && G == 2) asm volatile("s_bitset0_b32 m0, 12");
  if constexpr (HF == 0 && G == 13) asm volatile("s_add_u32 s96, s96, 0x8000");
  if constexpr (HF == 0 && G == 26) asm volatile("s_add_u32 s98, s98, 128");
  if constexpr (LD && HF == 0 && G == 27) asm volatile("s_cmp_lt_u32 s98, 0x380");
  if constexpr (HF == 1 && G == 1) asm volatile("s_xor_b32 s100, s100, 0x10000");
  if constexpr (LD && HF == 1 && G == 2) asm volatile("s_xor_b32 s97, s97, s100");
  if constexpr (LD && HF == 1 && G == 9) asm volatile("s_or_b32 m0, s97, 0x1000");
  if constexpr (!LD && HF == 1 && G == 30) asm volatile("s_cmp_lt_u32 s98, 0x500");
  GAP();
}
template <int HF, bool LD, int... Gs> DI void p_half(iseq<Gs...>) { (p_gap<HF, LD, Gs>(), ...); }
template <int G> DI void gapF(AC& c, const EP& p) {
  if constexpr (G < 64) gapB<0, false, G>(c);
  if constexpr (G >= 10) epi_pair<(G - 10) / 4, (G - 10) & 3, 0>(p);
  if constexpr (G >= 14 && ((G - 14) & 3) == 0) epi_store<(G - 14) / 4>();
  if constexpr (G == 73) epi_store<15>();
  GAP();
}
template <int... Gs> DI void phaseF(AC& c, const EP& p, iseq<Gs...>) { (gapF<Gs>(c, p), ...); }
template <int G> DI void gapAL(AC& c) {
  constexpr int kb = G >> 3, s = G & 7, r = 192 + 4 * (G & 15), q = 160 + 4 * s, d = R_S + 36 + 8 * kb, ci = s == 0 ? R_CI + 4 + 8 * kb : d;
  if constexpr (G == 0) asm volatile("s_add_u32 s96, s97, 0x1000000\n\ts_mov_b32 m0, s79\n\ts_waitcnt vmcnt(9) lgkmcnt(0)\n\ts_barrier" ::: "memory");
  if constexpr (G >= 8 && (G & 3) == 0) asm volatile("s_waitcnt lgkmcnt(4)");
  if constexpr (G == 29) asm volatile("s_barrier" ::: "memory");
  asm volatile("v_mfma_f32_16x16x32_bf16 v[%c0:%c1], a[%c2:%c3], a[%c4:%c5], v[%c6:%c7]" ::"i"(d), "i"(d + 3), "i"(r), "i"(r + 3), "i"(q), "i"(q + 3),
               "i"(ci), "i"(ci + 3));
  if constexpr (G < 24) kread<1, G + 8>();
  else vread<0, G - 24>();
  if constexpr (G % 3 == 1 && G < 24) dma_at<G / 3>();
  if constexpr (G == 11) asm volatile("s_bitset1_b32 m0, 12");
  if constexpr (G == 28) asm volatile("buffer_load_dwordx4 v[122:125], v88, s[88:91], s76 offen sc1" ::: "memory");
  if constexpr (G == 30) asm volatile("s_xor_b32 m0, s79, 0x11000\n\ts_movk_i32 s98, 128");
  sm_gap<0, 64 + 2 * G>(c); sm_gap<0, 65 + 2 * G>(c);
  GAP();
}
template <int... Gs> DI void phaseAL(AC& c, iseq<Gs...>) { (gapAL<Gs>(c), ...); }
struct BLS { int ex[65], op[65], vm[65], su[65]; };
constexpr BLS bl_sched(bool LD) {
  BLS z{};
  z.op[0] = 1;
  for (int t = 0; t < 64; ++t) {
    const int g = t & 15, o = z.op[t];
    const bool x = t == 6 || (t < 48 && g > 8 && g != 12 && g != 15);
    const bool v = LD ? (t & 3) == 0 : (t & 11) == 8;
    const bool p = t >= 15 && o < 24 && (t & 3) != 0 && t != 47 && !x && z.ex[t - 1] > 2 * (o / 3) + (o % 3 != 0);
    z.ex[t + 1] = z.ex[t] + x; z.vm[t + 1] = z.vm[t] + v; z.op[t + 1] = o + p;
    z.su[t + 1] = z.su[t] + (t >= 1 && z.su[t] < (LD ? 16 : 10) && g != 15 && !v && !x && (LD || t > 55 || (t > 3 && !p)));
  }
  return z;
}
template <bool LD> constexpr BLS BL = bl_sched(LD);
static_assert(BL<true>.ex[64] == 16 && BL<true>.op[61] == 24 && BL<true>.vm[64] == 16 && BL<false>.vm[64] == 8);
static_assert(BL<true>.su[56] == 16 && BL<false>.su[62] == 10);
template <bool LD, int G> DI void gapBL(AC& c) {
  if constexpr (G == 48) asm volatile("s_waitcnt vmcnt(%0) lgkmcnt(0)\n\ts_barrier" ::"i"(BL<LD>.vm[48]) : "memory");
  gapB<1, false, G>(c);
  if constexpr (G >= 48 && G < 56) vread<1, G - 48>();
  if constexpr (BL<LD>.vm[G + 1] > BL<LD>.vm[G]) {
    constexpr int k = BL<LD>.vm[G] + (LD ? 0 : 16), I = 2 * (k & 7) + ((k >> 3) & 1);
    if constexpr (k < 16) {
      asm volatile("buffer_load_dwordx4 a[%c0:%c1], v84, s[88:91], s75 offen offset:%2 sc1" ::"i"(128 + 4 * I), "i"(131 + 4 * I), "i"((I & 3) * 1024) : "memory");
      if constexpr (k & 1) asm volatile("s_addk_i32 s75, %0" ::"i"(k == 7 ? -12288 : 4096));
    } else dma_at<k - 8>();
    if constexpr (k == 19) asm volatile("s_bitset0_b32 m0, 12");
  }
  if constexpr (G == 1 || G == 2) asm volatile("v_add_f32_e32 v%c0, v%c0, v%c1" ::"i"(R_RS + G - 1), "i"(R_TS + G - 1));
  if constexpr (G == 3) asm volatile("v_mov_b32 v%c0, 0" ::"i"(R_TS));
  if constexpr (BL<LD>.ex[G + 1] > BL<LD>.ex[G]) {
    constexpr int j = BL<LD>.ex[G];
    asm volatile("v_exp_f32 v%c0, v%c1" ::"i"(R_E + j), "i"(R_S + 36 + 8 * (j >> 2) + (j & 3)));
  }
  if constexpr (BL<LD>.op[G + 1] > BL<LD>.op[G]) {
    constexpr int o = BL<LD>.op[G], k = o / 3, j = 2 * k + o % 3;
    if constexpr (o % 3 == 2) asm volatile("v_cvt_pk_bf16_f32 v%c0, v%c1, v%c2" ::"i"(p_reg(1, (k >> 1) * 4 + 2 + (k & 1))), "i"(R_E + 2 * k), "i"(R_E + 2 * k + 1));
    else asm volatile("v_add_f32_e32 v%c0, v%c1, v%c2" ::"i"(R_TS + 1), "i"(j == 1 ? R_E : R_TS + 1), "i"(R_E + j));
  }
  if constexpr (G == 61) asm volatile("v_cmp_nle_f32_e64 s[54:55], v%c0, v%c1" ::"i"(R_TS + 1), "i"(R_THR));
  if constexpr (BL<LD>.su[G + 1] > BL<LD>.su[G]) {
    constexpr int k = BL<LD>.su[G];
    if constexpr (k < 4) asm volatile("v_xor_b32 v%c0, %1, %2" ::"i"(R_TB + k), "i"(32 * k), "v"(c.tb));
    else if constexpr (k < 6) asm volatile("v_add_u32 v%c0, %1, %2" ::"i"(R_VS + k - 4), "i"(4096 * (k - 4)), "v"(c.vs));
    else if constexpr (k == 6) tload<96, T_SLAB>();
    else if constexpr (k == 7) tload<101, T_CNT>();
    else if constexpr (k == 8) asm volatile("v_mov_b64 v[240:241], 1");
    else if constexpr (k == 9) asm volatile("s_mov_b32 s97, s79\n\ts_mov_b32 s100, 0x8000");
    else if constexpr (k < 14) asm volatile("v_mov_b32 v%c0, %1" ::"i"(k < 12 ? 214 + k : 218 + k), "v"(k & 1 ? c.o1 : c.o0));
    else asm volatile("v_mov_b64 v[%c0:%c1], 0" ::"i"(198 + 2 * k), "i"(199 + 2 * k));
  }
  GAP();
}
template <bool LD, int... Gs> DI void phaseBL(AC& c, iseq<Gs...>) { (gapBL<LD, Gs>(c), ...); }
template <int MODE, int G> DI void gapFL(const EP& p) {
  constexpr int x = G - (MODE == 3 ? 4 : 0), J = MODE == 1 ? (G < 24 && G % 3 == 1 ? G / 3 : -1) : G < 27 && G % 5 != 0 && G % 5 != 3 ? G / 5 * 3 + (G % 5) / 2 : -1;
  if constexpr (G == 0 && MODE == 1) asm volatile("s_or_b32 m0, s78, 0x1000\n\ts_movk_i32 s98, 256\n\ts_waitcnt lgkmcnt(0)");
  if constexpr (G == 0 && MODE > 1) asm volatile("s_waitcnt vmcnt(8) lgkmcnt(0)" ::: "memory");
  if constexpr (G == 16 && MODE > 1) asm volatile("s_waitcnt vmcnt(9)" ::: "memory");
  if constexpr (G >= 8 && G < 32 && (G & 3) == 0) asm volatile("s_waitcnt lgkmcnt(%0)" ::"i"(G < 28 ? 4 : 0));
  if constexpr (G < 32) {
    constexpr int o = 8 * (G >> 1) + 4, r = 192 + 4 * (G & 15), q = R_P + 20 + 8 * (G & 1);
    asm volatile("v_mfma_f32_16x16x32_bf16 a[%c0:%c1], a[%c2:%c3], v[%c4:%c5], a[%c0:%c1]" ::"i"(o), "i"(o + 3), "i"(r), "i"(r + 3), "i"(q), "i"(q + 3));
  }
  if constexpr (G < 24) vread<1, G + 8>();
  if constexpr (G == 28) asm volatile("s_waitcnt vmcnt(8)\n\ts_barrier\n\ts_or_b32 m0, s79, 0x1000\n\ts_movk_i32 s98, 384" ::: "memory");
  if constexpr (G == 29 || G == 31) dma_at<8 + (G - 29) / 2>();
  if constexpr (G == 32 || G == 40) {
    static_for([&](auto I) { p_read<0, 2 * (G - 32) + I>(); }, make_iseq<16>{});
    dma_at<10 + (G - 32) / 8>();
  }
  if constexpr (J >= 0) dma_at<8 + (J & 7)>();
  if constexpr (x >= 0) epi_pair<2 * ((x >> 2) & 7) + (x >> 5), x & 3, MODE>(p);
  if constexpr (MODE == 3 && G < 32) {
    constexpr int db = G & 15, qb = G >> 4, o = 4 * (db * 2 + qb), a = 224 + 4 * (db & 1), b = 128 + 4 * ((db >> 1) * 2 + qb);
    asm volatile("v_mfma_f32_16x16x32_bf16 a[%c0:%c1], v[%c2:%c3], a[%c4:%c5], a[%c0:%c1]" ::"i"(o), "i"(o + 3), "i"(a), "i"(a + 3), "i"(b), "i"(b + 3));
  }
  if constexpr (J == 3 || J == 11) asm volatile("s_bitset0_b32 m0, 12");
  if constexpr (J == 7 && MODE > 1) asm volatile("s_or_b32 m0, s78, 0x1000\n\ts_movk_i32 s98, 256");
  if constexpr (x == 63) asm volatile("s_waitcnt vmcnt(4) lgkmcnt(0)" ::: "memory");
  GAP();
}
template <int MODE, int... Gs> DI void phaseFL_(const EP& p, iseq<Gs...>) { (gapFL<MODE, Gs>(p), ...); }
template <int MODE> DI void phaseFL(const EP& p) { GAP(); phaseFL_<MODE>(p, make_iseq<MODE == 3 ? 68 : 64>{}); }
template <int U, int K> DI void r_load(unsigned vo, unsigned so) {
  asm volatile("buffer_load_dwordx4 a[%c0:%c1], %2, s[88:91], %3 offen offset:%4 sc1" ::"i"(64 * U + 4 * K), "i"(64 * U + 4 * K + 3), "v"(vo),
               "s"(so + (K >> 1) * 0x40000 + (U >> 1) * 4096), "i"((K & 1) * 512 + (U & 1) * 2048) : "memory");
}
template <int U, int K> DI void r_sum() {
  constexpr int d = 128 + 8 * U + 4 * (K & 1), b = 64 * U + 4 * K;
  if constexpr (K < 2) asm volatile("v_mfma_f32_16x16x32_bf16 v[%c0:%c1], v[96:99], a[%c2:%c3], 0" ::"i"(d), "i"(d + 3), "i"(b), "i"(b + 3));
  else asm volatile("v_mfma_f32_16x16x32_bf16 v[%c0:%c1], v[96:99], a[%c2:%c3], v[%c0:%c1]" ::"i"(d), "i"(d + 3), "i"(b), "i"(b + 3));
}
template <int U> DI void r_sums() { static_for([&](auto K) { r_sum<U, K>(); }, make_iseq<16>{}); }
template <int U> DI void r_fin(unsigned vo, __amdgpu_buffer_rsrc_t srd, unsigned so) {
  static_for([&](auto E) { asm volatile("v_cvt_pk_bf16_f32 v%c0, v%c1, v%c2" ::"i"(128 + 8 * U + E), "i"(128 + 8 * U + 2 * E), "i"(129 + 8 * U + 2 * E)); }, make_iseq<4>{});
  asm volatile("s_nop 0\n\tbuffer_store_dwordx4 v[%c3:%c4], %0, %1, %2 offen sc0 sc1" ::"v"(vo), "s"(srd), "s"(so), "i"(128 + 8 * U), "i"(131 + 8 * U) : "memory");
}

#define CMAX 12
#define BAL 0
#define NYE (BAL ? 16 : 15)
#define NYO (BAL ? 14 : 15)
#define NY0 15
struct TRow { unsigned v[64]; };
constexpr u64 kvmask(int m0, int m1) { u64 n = 0; for (int m = m0; m <= m1; ++m) n |= 0x101ull << m; return n; }
constexpr u64 k_order(bool isY, int rr) {
  const int cls = (rr + (rr >> 2)) & 3;
#define NCM 3
  u64 t = 0;
  int n = 0;
  auto put = [&](int k) { t |= (u64)k << (4 * n++); };
  if (isY) {
    for (int k = 1; k <= 4; ++k) put(NCM + 4 + (k & 3));
    for (int k = 1; k <= 4; ++k) put(NCM + 8 + (k & 3));
    for (int k = NCM + 12; k < 16; ++k) put(k);
    for (int k = NCM + 3; k >= 0; --k) put(k);
    return t;
  }
  for (int k = 0; k < NCM; ++k) put(k);
  for (int k = 0; k < 3; ++k) { put(NCM + 4 * k + cls); put(NCM + ((cls + k + 1) & 3)); }
  for (int k = 1; k < 4; ++k) put(NCM + 4 + ((cls + k) & 3));
  for (int k = 1; k < 4; ++k) put(NCM + 8 + ((cls + k) & 3));
  for (int k = NCM + 12; k < 16; ++k) put(NCM == 2 ? k ^ (cls & 1) : k);
  return t;
}
static_assert(NCM >= 3);
constexpr TRow t_row(int b, int wid) {
  TRow t{};
  const int j = b & 7, r = b >> 3, rr = r & 15, hsel = rr & 1, pj = rr >> 1, kv = pj & 1, h = j + 8 * hsel, u = hsel * 8 + j;
  const bool isY = r >= 16, noA = pj == 7, slow = BAL && (j & 1);
  const int pa = slow && (pj == 0 || pj == 7) ? 7 - pj : pj;
  const bool solo = pa == 7;
  const int jF = isY || !BAL ? j : slow && pa == 0 ? j - 1 : !slow && pa == 6 ? j + 1 : j, hF = jF + 8 * hsel;
  const int qbF = isY ? (solo ? (slow ? 6 : 7) : 15 - pa) : solo ? 8 : !BAL ? pa : slow ? (pa ? pa - 1 : 0) : pa + 1;
  const int mt = (rr >> 2) + (isY ? 4 : 0), g = j * 128, gF = jF * 128, NY = !(j & 1) ? NYE : pa ? NYO : NY0;
  const int nx0 = solo ? 0 : 32 - 2 * pa - NY, cx = nx0 < CMAX ? nx0 : CMAX, ex = nx0 - cx, qb0 = solo ? qbF : 15 - pa;
  const unsigned oQ = (unsigned)h << 20, oK = oQ + O_K, oP = O_P + (h * 8 + pa) * PSTR, oQF = (unsigned)hF << 20;
  if (wid == 4) {
    for (int l = 0; l < 64; ++l)
      t.v[l] = fo(l < 16 ? g + hsel * 16 + l : l == 16 ? g + 32 + hsel * 8 + (qb0 >> 1) : l == 17 ? gF + 32 + hsel * 8 + (qbF >> 1) :
                  l < 26 ? gF + hsel * 16 + ((l - 18) >> 2) * 8 + ((l - 18) & 3) : l == 26 ? g + 48 + hsel * 8 + pa : l < 32 ? g + 64 + hsel * 8 + pa :
                  l < 48 ? 2304 + pj * 16 + l - 32 : 2048 + pj * 16 + l - 48);
    return t;
  }
  auto put64 = [&](int i, u64 x) { t.v[i] = (unsigned)x; t.v[i + 1] = (unsigned)(x >> 32); };
  t.v[T_Q0] = oQ + (qb0 * 128 + wid * 16) * 512;
  t.v[T_K0] = oK + (isY ? cx : 0) * 32768;
  t.v[T_OQ] = oQ + pj * 131072 + (wid >> 1) * 65536 + (wid & 1) * 4096;
  t.v[T_OG] = oK + kv * TSB + mt * 131072 + (wid >> 1) * 65536 + (wid & 1) * (kv ? 8192 : 4096);
  t.v[T_FPREP] = fo(2048 + pj * 16 + u);
  t.v[T_FQ] = fo(g + 32 + hsel * 8 + pj);
  t.v[T_FG] = fo(g + hsel * 16 + kv * 8 + mt);
  t.v[T_FS] = fo(1024 + qbF * 16 + jF * 2 + hsel);
  t.v[T_CNT] = fo(2560 + qbF * 16);
  t.v[T_ZO] = O_Z + pj * 64 * SEQ * 2 + (u * 4 + wid) * 4096;
  const u64 own = 0x1ull << (kv * 8 + mt), conv = noA ? 0 : 0xffffull << 32;
  put64(T_NPRE, 0x10000ull | (isY ? (1ull << (cx >> 2)) | (1ull << ((cx + 1) >> 2)) : noA ? 0 : 0xffffull << 48));
  put64(T_NST, isY ? kvmask(cx >> 2, 3) | (solo ? 0 : 1ull << 27) : kvmask(0, 0));
  put64(T_TAB, k_order(isY, rr));
  t.v[T_SLAB] = O_S + qbF * 0x200000 + jF * 0x40000 + wid * 8192 + hsel * 1024;
  t.v[T_PART] = oP;
  const unsigned junk = fo(g + 96 + r), fconv = noA ? junk : fo(2304 + pj * 16 + u);
  for (int it = isY || solo; it < 2; ++it) {
    unsigned* p = t.v + T_IT + it * T_ITN;
    const int n = !it ? nx0 : isY && !solo ? NY : 2 * qbF + 2;
    u64 q[4] = {0, 0, 0, 0};
    unsigned jmp[3] = {0, 0, 0}, chk = 0;
    p[14] = junk;
    if (isY) {
      p[0] = 2; p[1] = 2; p[13] = t.v[T_FG];
      if (!solo) {
        chk = 1u << (14 - cx); p[0] |= chk; q[1] = kvmask(4, (31 - 2 * pa) >> 2) & ~own; jmp[1] = ex * 32768;
        chk |= 1u << (n - 2); q[2] = 1ull << 26;
      }
    } else if (it == 0) {
      chk = p[0] = 2u | (ex ? 1u << (cx - 2) : 0) | 1u << (n - 2);
      chk |= noA || qbF > 1 ? 0 : 1u << n;
      q[0] = kvmask(0, (cx - 1) >> 2);
      if (ex) { q[1] = kvmask(4, 4 + ((ex - 1) >> 2)); jmp[1] = (16 - cx) * 32768; }
      jmp[ex ? 2 : 1] = oQF - oQ - (ex ? 16 + ex : cx) * 32768;
      q[ex ? 2 : 1] = 0x20000ull | ((0x22ull << ((2 * qbF + 1) >> 2)) - 0x11ull) << 18;
      q[ex ? 3 : 2] = conv;
      p[1] = 2; p[13] = fconv; p[14] = fo(g + 64 + hsel * 8 + pa);
    } else if (solo) {
      chk = p[0] = 0x402; p[1] = 2; p[13] = fconv;
      q[0] = kvmask(0, 2); q[1] = kvmask(3, 4) | conv;
    } else {
      p[0] = p[1] = n > 4 ? 2 : 1; p[13] = fo(g + 48 + hsel * 8 + pa);
      chk = noA ? 0 : p[0];
      q[0] = conv;
    }
    p[2] = jmp[0]; p[4] = jmp[1]; p[15] = jmp[2];
    p[3] = !it ? 0 : n > 2 ? 1u << (n - 3) : 1u << 30;
    for (int k = 0; k < 4; ++k) { p[5 + 2 * k] = (unsigned)q[k]; p[6 + 2 * k] = (unsigned)(q[k] >> 32); }
    const int rk = (r >> 1) - (BAL && (r >> 1) > (slow ? 7 : 6));
    p[16] = 32 | (isY && !solo ? 2 : 0) | (n == 2 ? 4 : 0) | (isY || noA ? 0 : 8) | (n & 1) << 4 | (n - 1 - it) << 8 | (rk * 4 + wid) << 16 | hF << 24 | qbF << 28;
    p[17] = !it ? 0 : n > 2 ? 1u << (n - 2) : 1u << 31;
    p[18] = it ? oP + wid * 16384 : oQF + (qbF * 128 + wid * 16) * 512;
    p[19] = oP + (it ? 1032 : 1024);
    p[20] = chk >> 1;
    p[21] = (it ? oQF + O_K : oK) + (isY ? cx : 0) * 32768;
    p[22] = ((p[0] | p[3] | p[17]) & 0x3fffffffu) << 1;
  }
  return t;
}
struct TBlk { TRow row[5]; };
template <int B> constexpr TBlk t_blk = {{t_row(B, 0), t_row(B, 1), t_row(B, 2), t_row(B, 3), t_row(B, 4)}};
struct Tab { TBlk blk[256]; };
template <int... I> constexpr Tab t_make(iseq<I...>) { return Tab{{t_blk<I>...}}; }
__device__ const Tab g_tab = t_make(make_iseq<256>{});

template <int R> DI const bf16* sptr() { const bf16* p; asm volatile("s_mov_b64 %0, s[%c1:%c2]" : "=s"(p) : "i"(R), "i"(R + 1)); return p; }
template <class T, int I> DI T* arg_late() {
  unsigned lo, hi;
  asm volatile("v_readlane_b32 %0, v77, %2\n\tv_readlane_b32 %1, v77, %3\n\ts_nop 4" : "=s"(lo), "=s"(hi) : "i"(2 * I), "i"(2 * I + 1));
  return (T*)(((u64)hi << 32) | lo);
}
__global__ __launch_bounds__(256, 1) __attribute__((amdgpu_num_vgpr(77), amdgpu_num_sgpr(56))) void fused_kernel(
    const bf16* ckv_, const bf16* wuk_, const bf16* wuv_, const bf16* x_, const bf16* wq_, bf16* ws_, const bf16* ak_, const bf16* av_,
    const bf16* ash_, const bf16* assmk_, const bf16* assmD_, const bf16* wo_, const bf16* aq_, bf16* out_) {
  extern __shared__ __attribute__((aligned(16))) char lds[];
  const void* kap = (const void*)__builtin_amdgcn_kernarg_segment_ptr();
  asm volatile("at_code_begin:\n\ts_load_dwordx16 s[80:95], %0, 0\n\ts_load_dwordx2 s[96:97], %0, 0x40" ::"s"(kap) : ACLOB_ALL, "v255", "s101");
  asm volatile("s_getpc_b64 s[48:49]\nct_base:\n\ts_mov_b32 s50, pf_code_end-ct_base\n\ts_mov_b32 s51, 0x20000\n\ts_lshr_b32 s52, %1, 3\n\t"
               "v_and_b32 v116, 63, %0\n\tv_lshrrev_b32 v117, 6, %0\n\tv_lshl_add_u32 v116, v116, 7, v117\n\ts_lshl_b32 s52, s52, 9\n\t"
               "v_lshlrev_b32 v116, 7, v116\n\tbuffer_load_dword v117, v116, s[48:51], s52 offen" ::"v"(threadIdx.x), "s"(blockIdx.x) : "scc", "memory");
  GAP();
  const int tid = threadIdx.x, lane = tid & 63;
  const int wid = __builtin_amdgcn_readfirstlane(tid >> 6);
  const int b = blockIdx.x, j = b & 7, r = b >> 3, rr = r & 15, hsel = rr & 1, pj = rr >> 1, kv = pj & 1;
  const bool isY = r >= 16, doP = isY && pj != 7, doA = !isY && pj != 7;
  const int h = j + 8 * hsel, mt = (rr >> 2) + (isY ? 4 : 0), pu = (hsel * 8 + j) * 4 + wid;
  constexpr unsigned m0d = 131072 + 24576;
  const unsigned lA = isY ? 65536 : 0, lW = lA ^ 65536, po = isY ? 0 : 32768;
  unsigned mA = lA + wid * 1024 + po, mW = lW + wid * 1024 + po, ofA = isY ? pj * 256 * DA * 2 : mt * 256 * DC * 2, ofW = h * 512;
  const unsigned lda = isY ? DA : DC;
#define ROT 1
  unsigned g0 = (!ROT ? 0 : isY ? pj + 2 * hsel : rr + (rr >> 2)) & 3;
  {
    asm volatile("global_load_dword v78, %0, %2\n\tglobal_load_dword v79, %1, %2 offset:1024\n\tglobal_load_dword v77, %3, %4"
                 ::"v"(tid * 4), "v"(lane * 4), "s"(&g_tab.blk[b]), "v"((lane < 27 ? lane : 27) * 4), "s"(kap) : "memory");
    const unsigned ra = wid * 8 + (lane >> 3), sa = ((lane & 7) ^ (((wid & 1) * 4 + (lane >> 4)) & 7)) * 16;
    const unsigned vbA = ra * (lda * 2) + sa, vbB = (4 * wid + (lane >> 4)) * LDW * 2 + 16 * ((lane & 15) ^ (((lane >> 4) << 2) | wid));
    vsetu<92>(vbA); vsetu<93>(vbB); vsetu<94>(ra * (DC * 2) + sa);
    sset<65>(128 * lda);
    asm volatile("s_mov_b32 s74, 0x40000\n\ts_mov_b32 s53, 0x80000");
    sset<58>(256 * lda * 2); sset<62>((lda - 1) * LDW * 2 + 512);
    asm volatile("s_mov_b32 s59, 0x20000\n\ts_mov_b32 s63, 0x20000");
    g_pro<1>(g0, mA, mW, lda);
    asm volatile("" : "+s"(ofA), "+s"(ofW), "+s"(mA), "+s"(mW), "+s"(g0));
    GAP();
  }
  asm volatile("s_bitcmp1_b32 %0, 4\n\ts_waitcnt lgkmcnt(0)\n\ts_cselect_b64 s[60:61], s[84:85], s[82:83]\n\ts_bitcmp1_b32 %0, 7\n\t"
               "s_cselect_b64 s[60:61], s[88:89], s[60:61]\n\ts_cselect_b64 s[56:57], s[86:87], s[80:81]\n\t"
               "s_add_u32 s56, s56, %1\n\ts_addc_u32 s57, s57, 0\n\ts_add_u32 s60, s60, %2\n\ts_addc_u32 s61, s61, 0\n\t"
               "s_mov_b64 s[88:89], s[90:91]\n\ts_mov_b32 s90, %3\n\ts_mov_b32 s91, 0x20000" ::"s"(b), "s"(ofA), "s"(ofW), "i"(WSB) : "scc");
  KST(0, 0);
  AC c;
  {
    GM g;
    PR pr;
    const int ptb = hsel * 8 + j;
    if (doP) prep_load(pr, tid, pj, ptb, sptr<92>(), sptr<94>(), sptr<96>());
    g_pro<2>(g0, mA, mW, lda);
    g_pro(g0 + 1, mA, mW, lda);
    sset<64>(64 * lda); sset<67>(256 * lda); sset<68>(320 * lda);
    asm volatile("s_mov_b32 s71, 0x20000\n\ts_movk_i32 s72, 0x100\n\ts_mov_b32 s73, 0x20100");
    asm volatile("buffer_load_dwordx2 v[120:121], off, s[88:91], %0 sc1" ::"s"(fo(6144)) : "memory");
    GAP();
    {
      const int fr = lane & 15, fq = lane >> 4, q = (lane & 15) >> 2, p = lane & 3;
      vsetu<128>(lA + (wid >> 1) * 16384 + (fr >> 3) * 1024 + (fr & 7) * 128 + ((fq ^ (fr >> 1)) * 16));
      vsetu<129>(lW + (wid & 1) * 8192 + off_b(8 * fq + q, p >> 1) + 8 * (p & 1));
      vsetu<130>(lW + (wid & 1) * 8192 + off_b(8 * fq + 4 + q, p >> 1) + 8 * (p & 1));
      const unsigned one = fq != q ? 0u : (p & 1) ? 0x3f800000u : 0x3f80u;
      vsetu<131>((p & 2) ? 0u : one); vsetu<132>((p & 2) ? one : 0u);
      vsetu<133>((lane >> 2) * 128 + 16 * ((p >> 1) ^ ((lane >> 2) & 6)) + 8 * (p & 1));
      vsetu<134>((fr >> 3) * 2048 + (fq & 1) * 512 + (fq >> 1) * 256 + (fr & 7) * 16);
      vsetu<81>(tid == 0 ? 0u : 0x80000000u);
      vsetu<84>(lane * 16); vsetu<85>(lane * 16 + 65536); vsetu<86>(wid * 8192 + lane * 16); vsetu<87>(wid * 8192 + lane * 16 + 4096);
      vsetu<88>(65536 + wid * 256 + fr * 16); vsetu<89>(16 * wid + fr - 4 * fq);
      asm volatile("s_mov_b64 s[92:93], s[88:89]\n\ts_mov_b64 s[94:95], s[90:91]\n\ts_mov_b32 s75, 0x60000\n\t"
                   "s_mov_b32 s76, 0x40100\n\ts_mov_b32 s77, 0x60100\n\ts_mov_b32 s82, 2");
      sset<66>(192 * lda); sset<69>(384 * lda); sset<70>(448 * lda);
      sset<78>(lA + wid * 1024); sset<79>(lW + wid * 1024);
    }
    GAP();
    if (doP) {
      asm volatile("s_waitcnt vmcnt(11)" ::: "memory");
#pragma unroll
      for (int q = 0; q < 7; ++q) asm volatile("" : "+v"(pr.kk[q]));
#pragma unroll
      for (int q = 0; q < 4; ++q) asm volatile("" : "+v"(pr.vv[q]));
      static_for([&](auto Ic) __attribute__((always_inline)) {
        constexpr int I = Ic;
        if constexpr (I < 2) g_pro(g0 + 2 + I, mA, mW, lda);
        if constexpr (I == 2) g_pro2(mA, mW);
        GAP();
        prep_math<I>(pr, tid, lds + 131072);
        GAP();
      }, make_iseq<4>{});
    } else { g_pro(g0 + 2, mA, mW, lda); g_pro(g0 + 3, mA, mW, lda); g_pro2(mA, mW); }
    GAP();
    g.aA[0] = vxor<128, 0>(); g.aA[1] = vxor<128, 64>();
    static_for([&](auto I) { g.aB[I >> 3][I & 7] = vxor<129 + (I >> 3), 32 * (I & 7)>(); }, make_iseq<16>{});
    c.o0 = vxor<131, 0>(); c.o1 = vxor<132, 0>(); c.tb = vxor<133, 0>(); c.vs = vxor<134, 0>();
    asm volatile("v_add_u32 v91, 0x100000, v93\n\ts_waitcnt vmcnt(8) lgkmcnt(0)\n\ts_barrier" ::: "memory");
    if (isY) g_read0<0>(g, make_iseq<16>{});
    else g_read0<1>(g, make_iseq<16>{});
    GAP();
    asm volatile("v_readfirstlane_b32 s48, v120\n\tv_readfirstlane_b32 s49, v121\n\tv_readlane_b32 s80, v78, %0\n\tv_readlane_b32 s81, v78, %1\n\t"
                 "s_mov_b32 s50, 0x4c957f2d\n\ts_mul_i32 s84, s48, s50\n\ts_mul_hi_u32 s85, s48, s50\n\ts_mul_i32 s49, s49, s50\n\t"
                 "s_mul_i32 s48, s48, 0x5851f42d\n\ts_add_u32 s85, s85, s49\n\ts_add_u32 s85, s85, s48\n\ts_add_u32 s84, s84, 0xf767814f\n\t"
                 "s_addc_u32 s85, s85, 0x14057b7e\n\ts_lshr_b64 s[80:81], s[80:81], 12\n\tv_mov_b32 v82, s84\n\tv_mov_b32 v83, s85"
                 ::"i"((int)T_TAB), "i"(T_TAB + 1) : "scc");
    if (isY) asm volatile("s_mov_b64 s[80:81], 0x6543");
    if (doP) prep_store(tid, pj, ptb, (bf16*)sptr<88>() + O_X / 2, lds + 131072);
    asm volatile("s_waitcnt lgkmcnt(0)" ::: "memory");
    GAP();
    if (isY) {
      g_tile<false, 0, 1>(g);
      g_loop<false, true>(g, doP ? 0x22u : 0x20u, [&](int t) __attribute__((always_inline)) {
        if (t == 1) fl_set<T_FPREP>();
        else if (t == 4) { tload<80, T_TAB>(); tload<81, T_TAB + 1>(); srd_set<60>((kv ? arg_late<const bf16, 2>() : arg_late<const bf16, 1>()) + h * 256, (DC - 1) * LDW * 2 + 512); }
        else {
          srd_set<56>(arg_late<const bf16, 0>() + (size_t)mt * 256 * DC, 256 * DC * 2);
          asm volatile("v_mov_b32 v92, v94");
          static_for([&](auto J) { asm volatile("s_mov_b32 s%c0, %1" ::"i"(64 + J), "i"((J + 1) * 64 * DC)); }, make_iseq<7>{});
        }
      });
      tload<101, T_OQ>(); sset<83>(__float_as_uint(CS));
      asm volatile("v_mov_b32 v112, v84\n\tv_add_u32 v113, 8192, v84");
      GAP();
      g_tile<false, 0, 2>(g);
      KST(0, 3);
    }
#define TT 3
    auto kvtile = [&](auto TRc) __attribute__((always_inline)) {
      constexpr bool TR = TRc;
      if (isY) g_tile<TR, 1, 4>(g);
      else g_tile<TR, 1, 1>(g);
      g_loop<TR, false>(g, 0x2000u | (isY || doA ? 2u : BAL && (j & 1) ? 0u : 1u << TT), [&](int t) __attribute__((always_inline)) {
        int l = threadIdx.x & 63; asm volatile("" : "+v"(l));
        if (t == 13) {
          asm volatile("s_mov_b32 s79, 0x28000");
          fl_poll<T_NPRE>();
          if ((tget<T_IT + T_ITN + 16>() & 0xf000000) == 0)
            asm volatile("v_readlane_b32 s48, v78, %0\n\tv_and_b32 v122, 0xffffff00, v82\n\tv_mov_b32 v123, v83\n\ts_nop 2\n\t"
                         "buffer_store_dwordx2 v[122:123], v81, s[88:91], s48 offen sc1" ::"i"((int)T_CNT) : "memory");
          if (!doA && !isY) {
            CODE_TOUCH("at_code_begin", "pf_code_end", hsel * 8 + wid * 2, 16u, 64, m0d);
            CODE_TOUCH("at_code_begin", "pf_code_end", hsel * 8 + wid * 2 + 1, 16u, 64, m0d);
          }
          if (doA) {
            const char* tp = arg_late<const char, 9>() + (size_t)(pj * 64 + pu) * (SEQ * 2) + (l & 31) * 128;
            asm volatile("s_mov_b32 m0, %1\n\ts_nop 0\n\tglobal_load_lds_dword %0, off" ::"v"(tp), "i"(m0d) : "memory");
          }
        } else if (isY) fl_set<T_FQ>();
        else if (doA) post_dma(pj, pu, arg_late<const bf16, 12>(), 131072 + wid * 8192);
        else {
          const char* tp = arg_late<const char, 0>() + 1024 * DC * 2 + (pu * 256 + l) * 128;
          asm volatile("s_mov_b32 m0, %0\n\ts_nop 0" ::"i"(m0d));
#pragma unroll
          for (int i = 0; i < (BAL ? 8 : 4); ++i) asm volatile("global_load_lds_dword %0, off" ::"v"(tp + (i & 3) * 8192 + (i >> 2) * 131072) : "memory");
        }
      });
      tload<101, T_OG>(); tload<96, T_Q0>(); tload<98, T_K0>();
      fl_check();
      tload<50, T_NST>(); tload<51, T_NST + 1>();
      asm volatile("v_mov_b32 v112, v84\n\tv_add_u32 v113, %0, v84\n\tv_mov_b32 v90, v87\n\tv_mov_b32 v91, v87\n\tv_mov_b32 v92, v87\n\tv_mov_b32 v93, v87\n\t"
                   "v_mov_b32 v94, v86\n\tv_mov_b32 v95, v86\n\tv_mov_b32 v126, v86\n\tv_mov_b32 v127, v86\n\ts_mov_b32 s99, 0x8000\n\t" FL_ARM("s[50:51]")
                   ::"i"(TR ? 4096 : 8192));
      if (doA) {
        const int d = pj * 64 + pu;
        const bf16* dp = arg_late<const bf16, 10>() + d;
        asm volatile("global_load_ushort v123, %0, off" ::"v"(dp) : "memory");
        srd_set<56>(arg_late<const bf16, 9>() + (size_t)d * SEQ, SEQ * 2);
        srd_set<60>(arg_late<const char, 5>() + O_X + d * SEQ * 2, SEQ * 2);
        sset<78>(65536 + wid * 15488);
        asm volatile("s_movk_i32 s64, 992\n\tv_subrev_u32 v122, 32, v84");
        GAP();
        g_tile<TR, 0, 3>(g);
      } else {
        sset<79>(isY ? wid * 8192 : 0x30000);
        if (tget<T_IT + T_ITN + 16>() & 16) asm volatile("s_add_u32 s98, s98, 0x8000\n\ts_mov_b32 s97, 0xffff8000" ::: "scc");
        else asm volatile("s_mov_b32 s97, 0x8000");
        GAP();
        g_tile<TR, 0, 5>(g);
      }
    };
    if (kv) kvtile(std::true_type{});
    else kvtile(std::false_type{});
  }
  KST(0, 4);
  asm volatile("s_waitcnt vmcnt(16)" ::: "memory");
  const unsigned md0 = tget<T_IT + 16>(), md1 = tget<T_IT + T_ITN + 16>();
  sset<79>(wid * 8192 + (md0 & 16) * 2048);
  if (doA) {
    const float Dd = bfl(__float_as_uint(vget<123>()));
    KST(0, 5);
    conv_wave<T_FG>(O_Z + pj * 64 * SEQ * 2 + ((pu & 7) * 8 + (pu >> 3)) * 64, pu >> 3, Dd, lds + 65536 + wid * 15488);
    const char* tp = arg_late<const char, 11>() + (size_t)h * (HD * DA * 2) + ((pj * 4 + wid) * 64 + lane) * 128;
    asm volatile("s_mov_b32 m0, %1\n\ts_nop 0\n\tglobal_load_lds_dword %0, off\n\ts_waitcnt vmcnt(17)" ::"v"(tp), "i"(m0d) : "memory");
    KST(0, 6);
  } else if (!isY) {
    static_for([&](auto I) { d_store<16 + I>(); }, make_iseq<16>{});
    tload<98, T_K0>();
    asm volatile("s_waitcnt vmcnt(0)\n\ts_barrier" ::: "memory");
    fl_set<T_FG>();
    KST(0, 5);
    fl_check();
    CODE_TOUCH("att_begin", "pf_code_end", hsel * 4 + wid, 8u, 64, m0d);
    dma4k<0>(); dma4k<1>(); dma4k<2>(); dma4k<3>();
    asm volatile("s_waitcnt vmcnt(0)" ::: "memory");
    KST(0, 6);
  } else {
    tload<76, T_PART>();
    fl_check();
    asm volatile("buffer_load_dwordx2 v[122:123], v88, s[88:91], s76 offen offset:1024 sc1" ::: "memory");
  }
  sset<78>(65536 + wid * 8192); sset<79>(98304 + wid * 8192);
  { const bf16* wp = arg_late<const bf16, 11>() + ((md1 >> 24) & 15) * HD * DA; asm volatile("s_mov_b64 s[80:81], %0" ::"s"(wp)); }
  asm volatile("att_begin:");
  ev_code(c);
  asm volatile("s_waitcnt lgkmcnt(0)\n\ts_barrier" ::: "memory");
#pragma unroll 1
  for (int it = md0 ? 0 : 1; it < 2; ++it) {
    const unsigned mode = it ? md1 : md0;
    const bool odd = mode & 16;
    if (odd) kreads<1>(make_iseq<8>{});
    else kreads<0>(make_iseq<8>{});
    const unsigned oP = tget<T_PART>();
    if (it == 0) {
      static_for([&](auto I) { tload<57 + I, T_IT + I>(); }, make_iseq<21>{});
      tload<S_T, T_IT + 21>(); tload<S_EV, T_IT + 22>();
    } else {
      static_for([&](auto I) { tload<57 + I, T_IT + T_ITN + I>(); }, make_iseq<21>{});
      tload<S_T, T_IT + T_ITN + 21>(); tload<S_EV, T_IT + T_ITN + 22>();
    }
    asm volatile("s_add_u32 s98, s97, 0x8000\n\ts_mov_b32 s56, -1\n\tv_mov_b64 v[114:115], 0\n\tv_mov_b64 v[118:119], 0\n\t"
                 "v_mov_b32 v120, 0x45800000" ::: "scc");
    KST(0, 8 + 3 * (it && !isY));
    GAP();
#define STA(k)
#define STI()
#define LOOP_ENDS "s_bfe_u32 s76, s73, 0x80008"
    STA(0);
    if (odd) {
      if (isY) phaseA<1, 2>(c);
      else phaseA<1, 1>(c);
      STA(1);
      STA(2);
      phaseB0<1>(mode, isY);
      STA(3);
      asm volatile(LOOP_ENDS "\n\ts_branch att_mid" ::: "scc");
    } else {
      if (isY) phaseA<0, 2>(c);
      else phaseA<0, 1>(c);
      STA(1);
      if (__builtin_expect(mode & 4, 0)) { asm volatile("s_mov_b32 s56, 30"); ev_call(); asm volatile("s_mov_b32 s56, 31"); ev_call(); acc_zero(); }
      STA(2);
      phaseB0<0>(mode, isY);
      if (__builtin_expect(mode & 4, 0)) { ev_call(); asm volatile("s_branch att_done"); }
      STA(3);
    }
    GAP();
    asm volatile(LOOP_ENDS "\natt_loop:" ::: "scc");
    GAP();
    phaseA<1, 0>(c);
    STI();
    if (__builtin_expect(c.rare != 0, 0)) sm_redo<1>();
    phaseB<1>(c);
    asm volatile("s_cbranch_scc1 att_done\natt_mid:");
    phaseA<0, 0>(c);
    STI();
    if (__builtin_expect(c.rare != 0, 0)) sm_redo<0>();
    phaseB<0>(c);
    asm volatile("s_cbranch_scc0 att_loop\natt_done:");
    GAP();
    STA(4);
    if (it == 0) phaseA<0, 3>(c);
    else {
      sset<76>(oP);
      GAP();
      phaseAL(c, make_iseq<32>{});
      if (__builtin_expect(redo(), 0)) sm_redo<1, false>();
      if (mode & 2) phaseBL<true>(c, make_iseq<64>{});
      else phaseBL<false>(c, make_iseq<64>{});
    }
    if (__builtin_expect(redo(), 0)) {
      if (it) static_for([&](auto I) { vset<R_S + 32 + (I >> 2) * 8 + (I & 3)>(-INFINITY); }, make_iseq<16>{});
      asm volatile("s_nop 7");
      sm_redo<0, false>();
    }
    STA(5);
    KST(0, 9 + 3 * (it && !isY));
    float l[2];
    asm volatile("v_add_f32 %0, v%c2, v%c3\n\tv_add_f32 %1, v%c4, v%c5" : "=v"(l[0]), "=v"(l[1]) : "i"(R_RS), "i"(R_TS), "i"(R_RS + 1), "i"(R_TS + 1));
    const float cm[2] = {vget<R_M>(), vget<R_M + 1>()};
    l[0] = row_reduce<false>(l[0]); l[1] = row_reduce<false>(l[1]);
    EP p;
    if (it == 0) {
      const f32x4 ml = {cm[0], l[0], cm[1], l[1]};
      asm volatile("buffer_store_dwordx4 %0, v88, s[88:91], %1 offen sc1\n\ts_mov_b32 s101, %2" ::"v"(ml), "s"(oP), "s"(oP + wid * 16384) : "memory");
      phaseF(c, p, make_iseq<74>{});
      asm volatile(FL_POLL "\n\ts_waitcnt vmcnt(17)" ::: "memory");
    } else {
      if (!(mode & 2)) {
        p.fa[0] = __builtin_amdgcn_rcpf(l[0]); p.fa[1] = __builtin_amdgcn_rcpf(l[1]);
        phaseFL<1>(p);
      } else {
        const f32x4 ml = {vget<122>(), vget<123>(), vget<124>(), vget<125>()};
        if (__builtin_expect(__builtin_amdgcn_ballot_w64(ml[0] != cm[0] || ml[2] != cm[1]) == 0, 1)) {
          p.fa[0] = __builtin_amdgcn_rcpf(l[0] + ml[1]); p.fa[1] = __builtin_amdgcn_rcpf(l[1] + ml[3]);
          phaseFL<3>(p);
        } else {
#pragma unroll
          for (int e = 0; e < 2; ++e) {
            const float mm = fmaxf(cm[e], ml[2 * e]);
            const float wa = __builtin_amdgcn_exp2f(cm[e] - mm), wb = __builtin_amdgcn_exp2f(ml[2 * e] - mm);
            const float inv = __builtin_amdgcn_rcpf(l[e] * wa + ml[2 * e + 1] * wb);
            p.fa[e] = wa * inv; p.fb[e] = wb * inv;
          }
          phaseFL<2>(p);
        }
      }
    }
    KST(0, 10 + 3 * (it && !isY));
  }
  asm volatile(TICKET "\n\tv_xor_b32 v%c0, s100, v%c0\np_loop:" ::"i"(R_TB) : "memory");
  GAP();
  p_half<0, true>(make_iseq<32>{});
  asm volatile("s_cbranch_scc0 p_mid");
  p_half<1, true>(make_iseq<32>{});
  asm volatile("s_branch p_loop\np_loop2:");
  p_half<0, false>(make_iseq<32>{});
  asm volatile("p_mid:");
  p_half<1, false>(make_iseq<32>{});
  asm volatile("s_cbranch_scc1 p_loop2\n\ts_nop 5");
  GAP();
  p_cvt<1, 0>(); p_cvt<1, 1>(); p_cvt<1, 2>(); p_cvt<1, 3>();
  p_store<1, 1>();
  GAP();
  KST(0, 27);
  {
    constexpr unsigned rdy = 98304;
    int lz = threadIdx.x & 63; asm volatile("" : "+v"(lz));
    const int fr = lz & 15, fq = lz >> 4, qbF = md1 >> 28;
    if (wid == 0) {
#pragma unroll 1
      for (int q = 0; q < 0x100000; ++q) {
        unsigned lo, hi, tl, th;
        asm volatile("v_readfirstlane_b32 %0, v240\n\tv_readfirstlane_b32 %1, v241\n\ts_mov_b32 %2, s84\n\ts_mov_b32 %3, s85" : "=s"(lo), "=s"(hi), "=s"(tl), "=s"(th));
        if (__builtin_expect((((lo ^ tl) >> 8) | (hi ^ th)) == 0, 1)) break;
        asm volatile("s_sleep 4\n\tv_mov_b64 v[240:241], 1\n\t" TICKET "\n\ts_waitcnt vmcnt(0)" ::: "memory");
      }
      asm volatile("v_mov_b32 v241, 0\n\ts_mov_b64 exec, 1\n\tds_write_b64 %0, v[240:241]\n\ts_mov_b64 exec, -1" ::"v"(rdy) : "memory");
    }
    vsetu<79>(fo(1024) + qbF * 4352 + fr * 8);
    {
      const int jj = 4 * (fr >> 3) + (fr & 3);
      const unsigned one = ((fq ^ (fr >> 2)) & 1) ? 0u : (jj & 1) ? 0x3f800000u : 0x3f80u;
      static_for([&](auto I) { vsetu<96 + I>((jj >> 1) == I ? one : 0u); }, make_iseq<4>{});
    }
    unsigned vo = (fq >> 1) * 1024 + (fq & 1) * 256 + fr * 16;
    bf16* out = arg_late<bf16, 13>();
    __amdgpu_buffer_rsrc_t so_ = __builtin_amdgcn_make_buffer_rsrc((void*)out, (short)0, SEQ * OUTW * 2, 0x00020000);
    unsigned vw = ((qbF * 128 + (fr & 7)) * OUTW + DA + (fr >> 3) * 32 + fq * 8) * 2;
    asm volatile("" : "+s"(so_));
    asm volatile("s_waitcnt vmcnt(0) lgkmcnt(0)\n\ts_barrier" : "+v"(vo), "+v"(vw) :: "memory");
    fl_set<T_FS>();
    unsigned tk;
    asm volatile("ds_read_b32 %0, %1\n\ts_waitcnt lgkmcnt(0)" : "=v"(tk) : "v"(rdy) : "memory");
    tk = __builtin_amdgcn_readfirstlane(tk) & 255;
    KST(0, 30);
    if (doA) post_math(pj, pu, out, lds + 131072 + wid * 8192);
#define N3 0
#define N2 12
    constexpr int N1 = 28 - 3 * N3 - 2 * N2;
    static_assert(N1 >= 0 && N3 + N2 + N1 <= 16);
    const int nu = tk < N3 ? 3 : tk < N3 + N2 ? 2 : tk < N3 + N2 + N1 ? 1 : 0;
    const int u0 = 4 * (tk < N3 ? 3 * tk : tk < N3 + N2 ? N3 + 2 * tk : 2 * N3 + N2 + tk) + wid * nu;
    KST(0, 28);
    if (nu) {
      const unsigned so0 = O_S + qbF * 0x200000 + (16 + u0) * 2048, so[3] = {so0, so0, so0};
      unsigned pend = 0x5555u, cnt = 0x400000;
      while (true) {
        unsigned nw;
        if (wid == 3) {
          u64 m;
          const u64 ex = pend | (pend << 1);
          asm volatile(FL_ARM("%1") "\n\t" FL_POLL "\n\ts_waitcnt vmcnt(0)\n\tv_cmp_eq_u64 %0, s[84:85], v[124:125]" : "=s"(m) : "s"(ex) : "memory");
          nw = (unsigned)m & ((unsigned)m >> 1) & pend;
          if (nw) asm volatile("ds_write_b32 %0, %1 offset:4" ::"v"(rdy), "v"(~pend | nw) : "memory");
        } else {
          unsigned t;
          asm volatile("ds_read_b32 %0, %1 offset:4\n\ts_waitcnt lgkmcnt(0)" : "=v"(t) : "v"(rdy) : "memory");
          nw = __builtin_amdgcn_readfirstlane(t) & pend;
        }
        if (--cnt == 0) nw = pend;
        if (nw == 0) {
          if (wid == 3) asm volatile("s_sleep 4");
          else asm volatile("s_sleep 1");
          continue;
        }
        asm volatile("s_nop 4");
        static_for([&](auto S) {
          if ((nw >> (2 * S)) & 1) {

            r_load<0, 2 * S>(vo, so[0]); r_load<0, 2 * S + 1>(vo, so[0]);
            if (nu > 1) { r_load<1, 2 * S>(vo, so[1]); r_load<1, 2 * S + 1>(vo, so[1]); }
            if (nu > 2) { r_load<2, 2 * S>(vo, so[2]); r_load<2, 2 * S + 1>(vo, so[2]); }
          }
        }, make_iseq<8>{});
        pend &= ~nw;
        if (pend == 0) break;
      }
      auto oo = [&](int U) { return ((((U >> 1) & 1) * 64 + ((U >> 2) & 3) * 16 + (U & 1) * 8) * OUTW + (U >> 4) * 64) * 2; };
      unsigned o0 = oo(u0), o1 = oo(u0 + 1), o2 = oo(u0 + 2);
      asm volatile("s_waitcnt vmcnt(0)" : "+s"(o0), "+s"(o1), "+s"(o2), "+s"(so_) :: "memory");
      KST(0, 16);
      if (nu > 2) r_sums<2>();
      if (nu > 1) r_sums<1>();
      r_sums<0>();
      if (nu > 2) r_fin<2>(vw, so_, o2);
      if (nu > 1) r_fin<1>(vw, so_, o1);
      asm volatile("s_nop 7");
      r_fin<0>(vw, so_, o0);
      if (qbF == 15 && tk == 0) asm volatile(FL_SET("%0") ::"s"(fo(6144)) : "memory");
    }
    KST(0, 29);
  }
  asm volatile("pf_code_end:");
  KST(0, 1);
}

extern "C" void kernel_launch(void* const* d_in, const int* in_sizes, int n_in, void* d_out, int out_size, void* d_ws,
                              size_t ws_size, hipStream_t stream) {
  const bf16* Aq = (const bf16*)d_in[0];
  const bf16* Ak = (const bf16*)d_in[1];
  const bf16* Av = (const bf16*)d_in[2];
  const bf16* Ash = (const bf16*)d_in[3];
  const bf16* Assmk = (const bf16*)d_in[4];
  const bf16* AssmD = (const bf16*)d_in[5];
  const bf16* Bx = (const bf16*)d_in[6];
  const bf16* Bckv = (const bf16*)d_in[7];
  const bf16* Bwq = (const bf16*)d_in[8];
  const bf16* Bwuk = (const bf16*)d_in[9];
  const bf16* Bwuv = (const bf16*)d_in[10];
  const bf16* Bwo = (const bf16*)d_in[11];
  bf16* out = (bf16*)d_out;
  hipFuncSetAttribute((const void*)fused_kernel, hipFuncAttributeMaxDynamicSharedMemorySize, 163840);
  fused_kernel<<<256, 256, 163840, stream>>>(Bckv, Bwuk, Bwuv, Bx, Bwq, (bf16*)d_ws, Ak, Av, Ash, Assmk, AssmD, Bwo, Aq, out);
}
```

```cpp
#include <hip/hip_runtime.h>
#include <hip/hip_bf16.h>
#include <stdint.h>
#include <utility>

typedef __hip_bfloat16 bf16;
typedef __attribute__((ext_vector_type(4))) short bf16x4;
typedef __attribute__((ext_vector_type(2))) unsigned u32x2;

constexpr int SEQ = 2048;
constexpr int DA = 448;
constexpr int NH = 16;
constexpr int HD = 256;
constexpr int DC = 1024;
constexpr int OUTW = 896;
constexpr unsigned TSB = NH * SEQ * HD * 2, O_K = TSB, O_S = 3 * TSB, O_P = 5 * TSB, PSTR = 67584, O_F = O_P + 128 * PSTR,
                   O_X = O_F + 400 * 4352, O_Z = O_X + DA * SEQ * 2, WSB = O_Z + DA * SEQ * 2;
constexpr unsigned fo(int w) { return O_F + (w >> 4) * 4352 + (w & 15) * 8; }

#define KST(kid, which)

__device__ __forceinline__ float bf2f(unsigned short u) { return __uint_as_float(((unsigned)u) << 16); }
__device__ __forceinline__ unsigned short f2bf(float f) { return __bfloat16_as_ushort(__float2bfloat16(f)); }
__device__ __forceinline__ float rbf(float f) { return bf2f(f2bf(f)); }
typedef __attribute__((ext_vector_type(2))) __bf16 bf16x2n;
typedef __attribute__((ext_vector_type(2))) float f32x2n;
__device__ __forceinline__ unsigned pk2(float a, float b) {
  return __builtin_bit_cast(unsigned, __builtin_convertvector((f32x2n){a, b}, bf16x2n));
}

__device__ __forceinline__ int off_b(int row, int ch) { return 256 * row + 16 * (ch ^ (((row & 3) << 2) | ((row >> 2) & 3))); }

template <int R> __device__ __forceinline__ void sset(unsigned x) { asm volatile("s_mov_b32 s%c0, %1" ::"i"(R), "s"(x)); }
template <int R> __device__ __forceinline__ void vsetu(unsigned x) { asm volatile("v_mov_b32 v%c0, %1" ::"i"(R), "v"(x)); }
template <int R> __device__ __forceinline__ void srd_set(const void* p, unsigned n) {
  asm volatile("s_mov_b64 s[%c0:%c1], %4\n\ts_mov_b32 s%c2, %5\n\ts_mov_b32 s%c3, 0x20000" ::"i"(R), "i"(R + 1), "i"(R + 2), "i"(R + 3), "s"(p), "s"(n));
}
template <int R, int L> __device__ __forceinline__ void tload() { asm volatile("v_readlane_b32 s%c0, v78, %1" ::"i"(R), "i"(L)); }
template <int L> __device__ __forceinline__ unsigned tget() { unsigned t; asm volatile("v_readlane_b32 %0, v78, %1" : "=s"(t) : "i"(L)); return t; }
#define FL_SET(S) "buffer_store_dwordx2 v[82:83], v81, s[88:91], " S " offen sc1"
#define FL_ARM(N) "v_cndmask_b32_e64 v80, -2.0, v79, " N
#define FL_POLL "buffer_load_dwordx2 v[124:125], v80, s[88:91], 0 offen sc1"
#define FL_CHECK(N) "s_mov_b32 s48, 0x100000\n7:\n\tv_cmp_eq_u64 vcc, s[84:85], v[124:125]\n\ts_and_b64 vcc, vcc, " N "\n\ts_cmp_eq_u64 vcc, " N \
                    "\n\ts_cbranch_scc1 8f\n\ts_sleep 2\n\t" FL_POLL "\n\ts_waitcnt vmcnt(0)\n\ts_sub_u32 s48, s48, 1\n\ts_cbranch_scc0 7b\n8:"
template <int L> __device__ __forceinline__ void fl_set() {
  asm volatile("v_readlane_b32 s48, v78, %0\n\ts_nop 4\n\t" FL_SET("s48") ::"i"(L) : "memory");
}
template <int L> __device__ __forceinline__ void fl_poll() {
  asm volatile("v_readlane_b32 s50, v78, %0\n\tv_readlane_b32 s51, v78, %1\n\ts_nop 1\n\t" FL_ARM("s[50:51]") "\n\t" FL_POLL ::"i"(L), "i"(L + 1) : "memory");
}
#define TICKET "buffer_atomic_add_x2 v[240:241], v81, s[88:91], s101 offen sc0"
__device__ __forceinline__ void fl_check() { asm volatile(FL_CHECK("s[50:51]") ::: "vcc", "scc", "memory"); }
template <int R, int K> __device__ __forceinline__ unsigned vxor() { unsigned t; asm volatile("v_xor_b32 %0, %1, v%c2" : "=v"(t) : "i"(K), "i"(R)); return t; }
template <int R> __device__ __forceinline__ float vget() { float t; asm volatile("v_mov_b32 %0, v%c1" : "=v"(t) : "i"(R)); return t; }
template <int R> __device__ __forceinline__ void vset(float x) { asm volatile("v_mov_b32 v%c0, %1" ::"i"(R), "v"(x)); }
template <int R> __device__ __forceinline__ float acc_read1() { float t; asm volatile("v_accvgpr_read_b32 %0, a[%c1]" : "=v"(t) : "i"(R)); return t; }
template <class F, int... I> __device__ __forceinline__ void static_for(F&& f, std::integer_sequence<int, I...>) {
  (f(std::integral_constant<int, I>{}), ...);
}
template <int... Is> using iseq = std::integer_sequence<int, Is...>;
template <int N> using make_iseq = std::make_integer_sequence<int, N>;

typedef __attribute__((ext_vector_type(4))) unsigned u32x4c;
typedef __attribute__((ext_vector_type(4))) float f32x4c;
typedef __attribute__((ext_vector_type(2))) float f32x2c;
typedef unsigned long long u64;
struct PR { u32x4c kk[7], vv[4]; };
__device__ __forceinline__ void prep_load(PR& p, int tid, int cg, int tb, const bf16* __restrict__ k, const bf16* __restrict__ v,
                                          const bf16* __restrict__ shift_k) {
  const __amdgpu_buffer_rsrc_t sk = __builtin_amdgcn_make_buffer_rsrc((void*)k, (short)0, SEQ * DA * 2, 0x00020000);
  const __amdgpu_buffer_rsrc_t sv = __builtin_amdgcn_make_buffer_rsrc((void*)v, (short)0, SEQ * DA * 2, 0x00020000);
  const __amdgpu_buffer_rsrc_t sw = __builtin_amdgcn_make_buffer_rsrc((void*)(shift_k + cg * 256), (short)0, 512, 0x00020000);
  const int vo = ((tb * 128 + (tid >> 3) * 4) * DA + cg * 64 + (tid & 7) * 8) * 2;
#pragma unroll
  for (int q = 0; q < 7; ++q)
    asm volatile("buffer_load_dwordx4 %0, %1, %2, 0 offen" : "=v"(p.kk[q]) : "v"(vo + (q - 3) * DA * 2), "s"(sk) : "memory");
#pragma unroll
  for (int q = 0; q < 4; ++q)
    asm volatile("buffer_load_dwordx4 %0, %1, %2, 0 offen offset:%3" : "=v"(p.vv[q]) : "v"(vo), "s"(sv), "i"(q * DA * 2) : "memory");
#pragma unroll
  for (int q = 0; q < 4; ++q)
    asm volatile("buffer_load_dwordx4 a[%c0:%c1], %2, %3, 0 offen offset:%4" ::"i"(4 * q), "i"(4 * q + 3), "v"((tid & 7) * 64), "s"(sw), "i"(q * 16) : "memory");
}
__device__ __forceinline__ float bfl(unsigned u) { return __uint_as_float(u << 16); }
__device__ __forceinline__ float bfh(unsigned u) { return __uint_as_float(u & 0xffff0000u); }
__device__ __forceinline__ f32x2c rbf2(f32x2c a) {
  return (f32x2c){__uint_as_float(pk2(0.f, a[0])), __uint_as_float(pk2(0.f, a[1]))};
}
template <int I> __device__ __forceinline__ void prep_math(const PR& p, int tid, char* lx) {
  const int rp = tid >> 3, c = tid & 7;
  f32x2c wt[4], kf[7];
  const unsigned w[4] = {__float_as_uint(acc_read1<4 * I>()), __float_as_uint(acc_read1<4 * I + 1>()), __float_as_uint(acc_read1<4 * I + 2>()),
                         __float_as_uint(acc_read1<4 * I + 3>())};
#pragma unroll
  for (int t = 0; t < 4; ++t)
    wt[t] = (t & 1) ? (f32x2c){bfh(w[t >> 1]), bfh(w[2 + (t >> 1)])} : (f32x2c){bfl(w[t >> 1]), bfl(w[2 + (t >> 1)])};
#pragma unroll
  for (int q = 0; q < 7; ++q) kf[q] = (f32x2c){bfl(p.kk[q][I]), bfh(p.kk[q][I])};
  f32x2c x[4];
#pragma unroll
  for (int e = 0; e < 4; ++e) {
    f32x2c s = rbf2(kf[e] * wt[3]);
    s = rbf2(s + rbf2(kf[e + 1] * wt[2]));
    s = rbf2(s + rbf2(kf[e + 2] * wt[1]));
    s = rbf2(s + rbf2(kf[e + 3] * wt[0]));
    x[e] = s * (f32x2c){bfl(p.vv[e][I]), bfh(p.vv[e][I])};
  }
#pragma unroll
  for (int u = 0; u < 2; ++u)
    *(u32x2*)(lx + (c * 8 + 2 * I + u) * 256 + (((31 - rp) * 8) ^ (c << 4))) = (u32x2){pk2(x[3][u], x[2][u]), pk2(x[1][u], x[0][u])};
}
__device__ __forceinline__ void prep_store(int tid, int cg, int tb, bf16* __restrict__ xr, const char* lx) {
  const __amdgpu_buffer_rsrc_t sx = __builtin_amdgcn_make_buffer_rsrc((void*)(xr + (size_t)cg * 64 * SEQ), (short)0, 64 * SEQ * 2, 0x00020000);
#pragma unroll
  for (int rd = 0; rd < 4; ++rd) {
    const int idx = tid + 256 * rd, ch = idx >> 4, q = idx & 15;
    u32x4c w = *(const u32x4c*)(lx + ch * 256 + 16 * (q ^ (ch >> 3)));
    __builtin_amdgcn_raw_buffer_store_b128(w, sx, (ch * SEQ + (15 - tb) * 128 + q * 8) * 2, 0, 16);
  }
}

#define CODE_TOUCH(LB, LE, wx, NW, NL, m0v) do { unsigned blo_, bhi_, elo_; \
    asm volatile("s_getpc_b64 s[48:49]\n\ts_add_u32 s48, s48, " LB "@rel32@lo+4\n\ts_addc_u32 s49, s49, " LB "@rel32@hi+12\n\t" \
                 "s_mov_b32 %0, s48\n\ts_mov_b32 %1, s49\n\t" \
                 "s_getpc_b64 s[48:49]\n\ts_add_u32 s48, s48, " LE "@rel32@lo+4\n\ts_mov_b32 %2, s48" \
                 : "=s"(blo_), "=s"(bhi_), "=s"(elo_) :: "scc"); \
    const unsigned n_ = elo_ - (blo_ & ~127u); \
    const __amdgpu_buffer_rsrc_t srd_ = __builtin_amdgcn_make_buffer_rsrc((void*)(((unsigned long long)bhi_ << 32) | (blo_ & ~127u)), (short)0, n_ < 262144u ? n_ : 0u, 0x00020000); \
    const unsigned l_ = threadIdx.x & 63, vo_ = l_ < (NL) ? ((wx) + l_ * (NW)) * 128u : 0x80000000u; \
    asm volatile("s_mov_b32 m0, %0\n\ts_nop 4\n\tbuffer_load_dword %1, %2, 0 offen lds" ::"s"(m0v), "v"(vo_), "s"(srd_) : "memory"); } while (0)
template <int I> __device__ __forceinline__ void d_store() {
  asm volatile("buffer_store_dwordx4 v[%c0:%c1], v%c2, s[88:91], s101 offen offset:%3 sc1" ::"i"(128 + 4 * I), "i"(131 + 4 * I), "i"(112 + ((I >> 2) & 1)),
               "i"((I & 3) * 1024) : "memory");
  if constexpr ((I & 7) == 7) asm volatile("s_addk_i32 s101, 0x4000" ::: "scc");
}
constexpr int NS_X = 19;
struct CV { unsigned pa[4], pb, px, vz, sz; float D; };
constexpr int cv_J(bool up, int q) { int J = up ? 0 : 7; while (q > J) { q -= J + 1; J += up ? 1 : -1; } return J; }
constexpr int cv_i(bool up, int q) { int J = up ? 0 : 7; while (q > J) { q -= J + 1; J += up ? 1 : -1; } return q; }
template <int R, int N> __device__ __forceinline__ void cv_rd(CV& c) {
  if constexpr (N < 16) {
    constexpr int i = N >> 1, o = 16 * R + 128 * (i & 1) + 2 * (N & 1), r = 32 + 32 * (R & 1) + 2 * N;
    asm volatile("ds_read2_b32 a[%c1:%c2], %0 offset0:%c3 offset1:%c4" ::"v"(c.pa[i >> 1]), "i"(r), "i"(r + 1), "i"(o), "i"(o + 1));
  } else {
    constexpr int m = N - 16, r = 192 + 32 * (R & 1) + 4 * m;
    asm volatile("ds_read_b128 a[%c1:%c2], %0 offset:%c3" ::"v"(c.pb), "i"(r), "i"(r + 3), "i"(64 * R + 512 * (7 - m)));
  }
}
template <int O> __device__ __forceinline__ void cv_xd(CV& c) {
  constexpr int e = O & 31, x = 96 + ((e >> 1) ^ 1);
  if constexpr (O >= 64) asm volatile("v_cvt_pk_bf16_f32 v%c0, 0, v%c0" ::"i"(160 + e));
  else if constexpr (O >= 32) asm volatile("v_mul_f32 v%c0, v%c0, %1" ::"i"(160 + e), "v"(c.D));
  else if constexpr (O & 1) asm volatile("v_lshlrev_b32 v%c0, 16, v%c1" ::"i"(160 + e), "i"(x));
  else asm volatile("v_and_b32 v%c0, 0xffff0000, v%c1" ::"i"(160 + e), "i"(x));
}
template <int O> __device__ __forceinline__ void cv_z(CV& c) {
  constexpr int J = O / 11, k = O % 11, y = 128 + 4 * J;
  if constexpr (k < 4) asm volatile("v_cvt_pk_bf16_f32 v%c0, 0, v%c0" ::"i"(y + k));
  else if constexpr (k < 8) asm volatile("v_add_f32 v%c0, v%c0, v%c1" ::"i"(y + k - 4), "i"(y + 28 + k));
  else if constexpr (k < 10) asm volatile("v_cvt_pk_bf16_f32 v%c0, v%c1, v%c2" ::"i"(y + k - 8), "i"(y + 2 * k - 16), "i"(y + 2 * k - 15));
  else asm volatile("buffer_store_dwordx2 v[%c0:%c1], %2, s[88:91], s101 offen sc1\n\ts_add_u32 s101, s101, 0x8000" ::"i"(y), "i"(y + 1), "v"(c.vz) : "memory");
}
constexpr int cv_z0(int Q) {
  int o = 0;
  for (int q = 0; q < Q; ++q)
    for (int k = 0; k < (q % 4 == 1 ? 0 : 3); ++k) { const int J = o / 11; if ((J + 1) * (J + 2) / 2 - 1 + (J ? 4 : 7) <= q) ++o; }
  return o;
}
constexpr int cv_xd0(int T) { int o = 0; for (int t = 0; t < T; ++t) o += t % 4 != 1 || (t >= 72 && t < 108); return o < 96 ? o : 96; }
static_assert(cv_xd0(144) == 96);
template <int B, int... Is> __device__ __forceinline__ void cv_zs(CV& c, iseq<Is...>) { (cv_z<B + Is>(c), ...); }
template <int R, int Q> __device__ __forceinline__ void cv_slot(CV& c) {
  constexpr int i = cv_i(R == 7, Q), J = cv_J(R == 7, Q), d = 128 + 4 * J, a = 32 + 32 * (R & 1) + 4 * i, b = 192 + 32 * (R & 1) + 4 * (J - i);
  constexpr int T = 36 * R + Q, vm = Q % 4 == 1 && Q < 32 ? Q / 4 : -1;
  if constexpr (Q == 0) asm volatile("s_waitcnt lgkmcnt(0)");
  if constexpr (R >= 6 && vm >= 0 && vm % 4 == 0) asm volatile("s_add_u32 m0, s79, %0" ::"i"(vm * 1024));
  if constexpr (R == 0 && i == 0)
    asm volatile("v_mfma_f32_16x16x32_bf16 v[%c0:%c1], a[%c2:%c3], a[%c4:%c5], 0" ::"i"(d), "i"(d + 3), "i"(a), "i"(a + 3), "i"(b), "i"(b + 3));
  else
    asm volatile("v_mfma_f32_16x16x32_bf16 v[%c0:%c1], a[%c2:%c3], a[%c4:%c5], v[%c0:%c1]" ::"i"(d), "i"(d + 3), "i"(a), "i"(a + 3), "i"(b), "i"(b + 3));
  if constexpr (Q < 24 && R < 7) cv_rd<R + 1, Q>(c);
  if constexpr (T % 7 == 3 && NS_X + 5 + T / 7 < 32) d_store<NS_X + 5 + T / 7>();
  if constexpr (R == 2 && Q >= 24 && Q < 32)
    asm volatile("ds_read_b64 v[%c1:%c2], %0 offset:%c3" ::"v"(c.px), "i"(96 + 2 * (Q - 24)), "i"(97 + 2 * (Q - 24)), "i"(512 * (31 - Q)));
  if constexpr (R >= 3 && R < 7) if constexpr (cv_xd0(T - 107) > cv_xd0(T - 108)) cv_xd<cv_xd0(T - 108)>(c);
  if constexpr ((R == 3 || R == 4) && vm >= 0) {
    constexpr int f = (R - 3) * 8 + vm;
    asm volatile("buffer_load_dwordx4 a[%c0:%c1], v84, s[88:91], s96 offen offset:%2 sc1" ::"i"(128 + 4 * f), "i"(131 + 4 * f), "i"((f & 3) * 1024) : "memory");
    if constexpr ((f & 3) == 3) asm volatile("s_addk_i32 s96, %0" ::"i"(f == 7 ? 28672 : 4096));
  }
  if constexpr (R == 4 && Q == 35) asm volatile(FL_POLL ::: "memory");
  if constexpr (R >= 6 && vm >= 0)
    asm volatile("buffer_load_dwordx4 v%c0, s[88:91], s98 offen offset:%1 sc1 lds" ::"i"(86 + vm / 4), "i"((vm & 3) * 1024) : "memory");
  if constexpr (R == 6 && Q == 35) asm volatile("s_add_u32 s98, s98, 0x8000\n\ts_xor_b32 s79, s79, 0x8000");
  if constexpr (R == 7) cv_zs<cv_z0(Q)>(c, make_iseq<cv_z0(Q + 1) - cv_z0(Q)>{});
  __builtin_amdgcn_sched_barrier(0);
}
template <int R, int... Q> __device__ __forceinline__ void cv_step(CV& c, iseq<Q...>) { (cv_slot<R, Q>(c), ...); }
template <int... N> __device__ __forceinline__ void cv_rd0(CV& c, iseq<N...>) { (cv_rd<0, N>(c), ...); }
template <int FLAG> __device__ __forceinline__ void conv_wave(unsigned sz, int ck, float Dd, char* lw) {
  int lane = threadIdx.x & 63; asm volatile("" : "+v"(lane));
  const int l16 = lane & 15, fq = lane >> 4;
  u32x4c w[5]; unsigned w4[5];
#pragma unroll
  for (int r = 0; r < 5; ++r) {
    w[r] = *(const u32x4c*)(lw + r * 1024 + lane * 16);
    w4[r] = *(const unsigned*)(lw + r * 1024 + lane * 16 + 16);
  }
  d_store<NS_X>(); d_store<NS_X + 1>();
#pragma unroll
  for (int r = 0; r < 5; ++r)
    *(u32x4c*)(lw + 10304 + r * 1024 + lane * 16) = (u32x4c){__builtin_amdgcn_alignbit(w[r][1], w[r][0], 16), __builtin_amdgcn_alignbit(w[r][2], w[r][1], 16),
                                                             __builtin_amdgcn_alignbit(w[r][3], w[r][2], 16), __builtin_amdgcn_alignbit(w4[r], w[r][3], 16)};
  d_store<NS_X + 2>();
  CV c;
#pragma unroll
  for (int i = 0; i < 4; ++i) c.pa[i] = (unsigned)(size_t)lw + ((l16 + 1) & 1) * 10304 + 4 * ((8 * fq + l16 + 1) >> 1) + 1024 * i;
  c.pb = (unsigned)(size_t)lw + 5120 + (2032 - 16 * l16 + 8 * fq) * 2 - 512 * 7;
  c.vz = (l16 >> 1) * 4096 + (((l16 & 1) * 2 + (fq >> 1)) ^ (ck >> 1)) * 16 + (fq & 1) * 8;
  c.px = (unsigned)(size_t)lw + 5120 + 2044 * 2 - 512 * 7 - (16 * l16 + 4 * fq) * 2;
  c.sz = sz; c.D = Dd;
  asm volatile("" : "+v"(c.vz), "+v"(c.px), "+v"(c.D) :: "memory");
  asm volatile("" : "+s"(c.sz));
  cv_rd0(c, make_iseq<24>{});
  d_store<NS_X + 3>(); d_store<NS_X + 4>();
  __builtin_amdgcn_sched_barrier(0);
  static_for([&](auto R) __attribute__((always_inline)) {
    cv_step<R>(c, make_iseq<36>{});
    if constexpr (R == 2) {
      asm volatile("s_waitcnt vmcnt(0)\n\ts_barrier" ::: "memory");
      fl_set<FLAG>();
      sset<101>(c.sz);
    }
    if constexpr (R == 5) {
      asm volatile("s_waitcnt vmcnt(0)" ::: "memory");
      fl_check();
    }
    __builtin_amdgcn_sched_barrier(0);
  }, make_iseq<8>{});
  asm volatile("s_nop 7");
  cv_zs<cv_z0(36)>(c, make_iseq<88 - cv_z0(36)>{});
  __builtin_amdgcn_sched_barrier(0);
}

__device__ __forceinline__ void post_dma(int cg, int tb, const bf16* __restrict__ q, unsigned m0w) {
  int lane = threadIdx.x & 63; asm volatile("" : "+v"(lane));
  const __amdgpu_buffer_rsrc_t sq = __builtin_amdgcn_make_buffer_rsrc((void*)(q + (size_t)tb * 32 * DA + cg * 64), (short)0, 32 * DA * 2, 0x00020000);
  const unsigned vq = ((lane >> 3) * DA + (lane & 7) * 8) * 2;
  asm volatile("s_mov_b32 m0, %0\n\ts_nop 4\n\tbuffer_load_dwordx4 %1, %2, 0 offen lds\n\tbuffer_load_dwordx4 %1, %2, %3 offen offset:1024 lds\n\t"
               "buffer_load_dwordx4 %1, %2, %4 offen offset:2048 lds\n\tbuffer_load_dwordx4 %1, %2, %5 offen offset:3072 lds"
               ::"s"(m0w + 4096), "v"(vq), "s"(sq), "s"(8 * DA * 2 - 1024), "s"(16 * DA * 2 - 2048), "s"(24 * DA * 2 - 3072) : "memory");
}
__device__ __forceinline__ void post_math(int cg, int tb, bf16* __restrict__ out, const char* lw) {
  int lane = threadIdx.x & 63; asm volatile("" : "+v"(lane));
  const int tl = lane >> 3, ck = lane & 7;
  const __amdgpu_buffer_rsrc_t so = __builtin_amdgcn_make_buffer_rsrc((void*)(out + (size_t)tb * 32 * OUTW + cg * 64), (short)0, 32 * OUTW * 2 - 2 * DA, 0x00020000);
  const unsigned vo = (tl * OUTW + ck * 8) * 2;
#pragma unroll
  for (int i = 0; i < 4; ++i) {
    const u32x4c qv = *(const u32x4c*)(lw + 4096 + i * 1024 + lane * 16);
    const char* lz = lw + ck * 64 + 16 * (i ^ (ck >> 1)) + tl * 2;
    u32x4c o;
#pragma unroll
    for (int e = 0; e < 4; ++e) {
      const float z0 = bf2f(*(const unsigned short*)(lz + 2 * e * 512));
      const float z1 = bf2f(*(const unsigned short*)(lz + (2 * e + 1) * 512));
      o[e] = pk2(bfl(qv[e]) * z0, bfh(qv[e]) * z1);
    }
    __builtin_amdgcn_raw_buffer_store_b128(o, so, vo + i * 8 * OUTW * 2, 0, 17);
  }
}

#define DI __device__ __forceinline__
#define GAP() __builtin_amdgcn_sched_barrier(0)
typedef __attribute__((ext_vector_type(4))) float f32x4g;
typedef __attribute__((ext_vector_type(4))) unsigned u32x4g;
constexpr int LDW = NH * HD;
struct GM { unsigned aA[2], aB[2][8]; };
template <bool W, int J, int V> DI void g_dma() {
  if constexpr (J == 0) asm volatile("buffer_load_dwordx4 v%c0, s[%c1:%c2], 0 offen lds" ::"i"(V), "i"(W ? 60 : 56), "i"(W ? 63 : 59) : "memory");
  else asm volatile("buffer_load_dwordx4 v%c0, s[%c1:%c2], s%c3 offen lds" ::"i"(V), "i"(W ? 60 : 56), "i"(W ? 63 : 59), "i"((W ? 70 : 63) + J) : "memory");
}
template <int P, int KS, int I> DI void g_wread(GM& g) {
  constexpr int nf = I >> 1, hf = I & 1, r = 192 - 64 * P + 8 * nf + 4 * KS + 2 * hf;
  asm volatile("ds_read_b64_tr_b16 a[%c1:%c2], %0 offset:%c3" ::"v"(g.aB[hf][nf]), "i"(r), "i"(r + 1), "i"(P * 32768 + KS * 16384));
}
template <int P, int M, int KS> DI void g_aread(GM& g) {
  constexpr int r = 96 + 8 * (M & 1) + 4 * KS;
  asm volatile("ds_read_b128 v[%c1:%c2], %0 offset:%c3" ::"v"(g.aA[KS]), "i"(r), "i"(r + 3), "i"(P * 32768 + M * 2048));
}
constexpr float CS = 0.0625f * 1.4426950408889634f;
constexpr int NS_Q = 19;
constexpr int e_vm(int G) { return (G * 3) % 8 < 3 ? G * 3 / 8 : -1; }
constexpr int e_st(int MODE, int G) {
  const int q = G & 15, k3 = ((G >> 4) - 1) * 3 + (q == 0 ? 0 : q == 4 ? 1 : 2), k2 = NS_Q + (G >> 4) * 2 + (q == 4);
  if (MODE == 5) return e_vm(G) >= 32 ? e_vm(G) - 32 : -1;
  if (MODE == 3) return G >= 33 && (G - 33) % 5 == 0 && (G - 33) / 5 < NS_X ? (G - 33) / 5 : -1;
  if (MODE == 2) return G >= 30 && (q == 0 || q == 4 || q == 14) && k3 - 2 < NS_Q ? k3 - 2 : -1;
  if (MODE == 4) return (q == 0 || q == 4) && k2 < 32 ? k2 : -1;
  return -1;
}
constexpr int e_nst(int MODE, int G0, int G1) { int n = 0; for (int G = G0; G < G1; ++G) n += e_st(MODE, G) >= 0; return n; }
static_assert(e_nst(2, 0, 128) == NS_Q && e_nst(3, 30, 128) == NS_X && e_nst(5, 0, 128) == 16 && e_nst(4, 0, 104) == 32 - NS_Q);
constexpr int e_slot(int MODE, int G) { return MODE == 3 ? (G % 3 == 0 && G < 30 ? G / 3 : -1) : e_vm(G) >= 0 && e_vm(G) < 32 ? 10 + e_vm(G) : -1; }
struct ES { int op0[129], nop[129]; };
constexpr ES e_sched(bool TR, int MODE) {
  ES z{};
  const bool SCL = MODE == 2 || MODE == 4;
  const int per = SCL ? 16 : 8, o1 = 32 * per;
  int o = MODE == 4 ? e_sched(false, 2).op0[128] : 0;
  for (int G = 0; G <= 128; ++G) {
    const int q = G & 15;
    int cap = 2 - (q == 1 || q == 2 || q == 15);
    if (SCL) cap -= (q >= 3 && q < 11 && (q & 1)) + (q == 7 || q == 11) + 2 * (G == 70 || G == 74 || (G & 63) == 61);
    cap += MODE == 2 ? 4 + (q == 10 || q == 13) : MODE == 3 ? 1 : MODE == 5 ? 2 : 0;
    if (e_st(MODE, G) >= 0 || (SCL ? q == 8 || q == 12 : e_slot(MODE, G) >= 0) || (MODE == 4 && G < 4)) cap = 0;
    if (G == 128) cap = 10000;
    z.op0[G] = o;
    while (o < o1) {
      const int i = o / per, rdy = MODE == 4 ? 0 : (TR ? 32 * (i >> 3) + 16 + 2 * (i & 7) + 1 : 4 * i + 3) + 6;
      if ((G < rdy && G < 128) || cap-- <= 0) break;
      ++o; ++z.nop[G];
    }
  }
  return z;
}
template <bool TR, int MODE> constexpr ES ESCH = e_sched(TR, MODE);
static_assert(ESCH<false, 4>.op0[96] == 512 && ESCH<false, 4>.op0[0] >= 24 * 16);
template <bool TR, bool SCL, int O> DI void e_op() {
  constexpr int per = SCL ? 16 : 8, i = O / per, k = O % per, r = 128 + 4 * i, t = 114 + 4 * (i & 1), c = k - (per - 4);
  if constexpr (k < 4) asm volatile("v_accvgpr_read_b32 v%c0, a[%c1]" ::"i"(t + k), "i"(4 * i + k));
  else if constexpr (c < 0) asm volatile("v_mul_f32 v%c0, s83, v%c0" ::"i"(k < 8 ? r + (k ^ 6) : t + k - 8));
  else {
    constexpr int d = c == 0 ? r + 3 : c == 1 ? r + 2 : r + c - 2, x = c == 0 ? r + 2 : c == 1 ? r : t + 2 * (c - 2);
    asm volatile("v_cvt_pk_bf16_f32 v%c0, v%c1, v%c2" ::"i"(d), "i"(x), "i"(x + 1));
  }
}
template <bool TR, bool SCL, int B, int... Is> DI void e_ops(iseq<Is...>) { (e_op<TR, SCL, B + Is>(), ...); }
template <int S> DI void e_m0() {
  if constexpr (S == 0 || S == 1 || S == 5 || S == 9) asm volatile("s_add_u32 m0, s78, %0" ::"i"(S * 1024));
  if constexpr (S >= 26 && (S - 26) % 4 == 0) asm volatile("s_add_u32 m0, s79, %0" ::"i"(((S - 26) >> 3) * 32768 + ((S - 26) & 4) * 1024));
}
template <int S> DI void e_load() {
  if constexpr (S == 0) asm volatile("buffer_load_dwordx4 v122, s[56:59], 0 offen lds" ::: "memory");
  else if constexpr (S < 5) asm volatile("buffer_load_dwordx4 v84, s[56:59], s64 offen offset:%0 lds" ::"i"((S - 1) * 1024) : "memory");
  else if constexpr (S < 9) asm volatile("buffer_load_dwordx4 v84, s[60:63], 0 offen offset:%0 sc1 lds" ::"i"((S - 5) * 1024) : "memory");
  else if constexpr (S == 9) asm volatile("buffer_load_dwordx4 v84, s[60:63], s62 offen sc1 lds" ::: "memory");
  else if constexpr (S < 26) {
    constexpr int f = S - 10;
    asm volatile("buffer_load_dwordx4 a[%c0:%c1], v84, s[88:91], s96 offen offset:%2 sc1" ::"i"(128 + 4 * f), "i"(131 + 4 * f), "i"((f & 3) * 1024) : "memory");
    if constexpr ((f & 3) == 3) asm volatile("s_addk_i32 s96, %0" ::"i"(f == 7 ? 28672 : 4096));
  } else {
    constexpr int f = S - 26;
    asm volatile("buffer_load_dwordx4 v%c0, s[88:91], s98 offen offset:%1 sc1 lds" ::"i"(86 + ((f >> 2) & 1)), "i"((f & 3) * 1024) : "memory");
    if constexpr (f == 7) asm volatile("s_add_u32 s98, s98, s97");
    if constexpr (f == 15) asm volatile(FL_POLL ::: "memory");
  }
}
template <bool TR, int P, int H, int G, int MODE> DI void g_gap(GM& g) {
  constexpr int m = 4 * H + (G >> 4), q = G & 15, n = q >> 1, ks = q & 1, GG = 64 * H + G;
  constexpr bool inV = TR ? m & 1 : n & 1, END = MODE == 3 || MODE == 5;
  constexpr int a = 96 + 8 * (m & 1) + 4 * ks, b = 192 - 64 * P + 8 * n + 4 * ks, d = (inV ? 128 : 0) + 4 * (TR ? 8 * (m >> 1) + n : 4 * m + (n >> 1));
  constexpr int J = END ? -1 : q == 8 ? 2 * (G >> 4) : q == 12 ? 2 * (G >> 4) + 1 : -1, gj = (J & 3) ^ ((J & 3) >> 1);
  if constexpr (END) { if constexpr (q == 0 && GG > 0) asm volatile("s_waitcnt lgkmcnt(0)"); e_m0<e_slot(MODE, GG)>(); }
  else if constexpr (q == 0 && G > 0) asm volatile("s_waitcnt lgkmcnt(4)");
  if constexpr (J == 0) asm volatile("s_add_u32 m0, s78, %0" ::"i"((P ^ H ^ 1) * 32768 + (1 - H) * 8192));
  else if constexpr (J == 4) asm volatile("s_add_u32 m0, s79, %0" ::"i"((P ^ H) * 32768 + (1 - H) * 16384));
  else if constexpr (J > 0 && (J & 3) == 1) asm volatile("s_bitset1_b32 m0, 12");
  else if constexpr (J > 0 && (J & 3) == 2) asm volatile("s_bitset1_b32 m0, %0" ::"i"(J < 4 ? 14 : 13));
  else if constexpr (J > 0) asm volatile("s_bitset0_b32 m0, 12");
#define GMF(D, X, Y, C) asm volatile("v_mfma_f32_16x16x32_bf16 " D "[%c0:%c1], " X ", " Y ", " C ::"i"(d), "i"(d + 3), "i"(a), "i"(a + 3), "i"(b), "i"(b + 3))
#define GMF2(D, C) do { if constexpr (TR) GMF(D, "v[%c2:%c3]", "a[%c4:%c5]", C); else GMF(D, "a[%c4:%c5]", "v[%c2:%c3]", C); } while (0)
  if constexpr ((MODE == 1 || MODE == 4) && ks == 0) { if constexpr (inV) GMF2("v", "0"); else GMF2("a", "0"); }
  else if constexpr (inV) GMF2("v", "v[%c0:%c1]");
  else GMF2("a", "a[%c0:%c1]");
#undef GMF2
#undef GMF
  if constexpr ((q == 1 || q == 2) && !(END && m == 7)) g_aread<(m < 7 ? P : P ^ 1), (m + 1) & 7, q - 1>(g);
  if constexpr (!END && q >= 3 && q < 11 && (q & 1)) g_wread<P ^ 1, H, (G >> 4) * 4 + (q - 3) / 2>(g);
  if constexpr (MODE == 1 && GG < 7) {
    if constexpr (GG == 0) asm volatile("s_barrier" ::: "memory");
    if constexpr (GG == 2) asm volatile("s_add_u32 m0, s79, %0" ::"i"(P * 32768));
    if constexpr (GG >= 3) g_dma<true, (GG - 3) ^ ((GG - 3) >> 1), 91>();
    if constexpr (GG == 3 || GG == 4) asm volatile("s_bitset1_b32 m0, %0" ::"i"(9 + GG));
    if constexpr (GG == 5) asm volatile("s_bitset0_b32 m0, 12");
  }
  if constexpr (e_st(MODE, GG) >= 0) {
    static_assert(ESCH<TR, MODE>.op0[GG] >= (e_st(MODE, GG) + 1) * (END ? 8 : 16));
    d_store<e_st(MODE, GG)>();
  }
  if constexpr (J >= 0 && J < 4) g_dma<false, (1 - H) * 2 + (gj & 1) + (gj >> 1) * 4, 90>();
  else if constexpr (J >= 4) g_dma<true, (1 - H) * 4 + gj, 91>();
  if constexpr (MODE >= 2) e_ops<TR, !END, ESCH<TR, MODE>.op0[GG]>(make_iseq<ESCH<TR, MODE>.nop[GG]>{});
  if constexpr (END) {
    if constexpr (e_slot(MODE, GG) >= 0) e_load<e_slot(MODE, GG)>();
  } else {
    if constexpr (H == 1 && G == 6) asm volatile("v_lshl_add_u32 v90, s82, 7, v92\n\ts_and_b32 s82, s80, 15");
    if constexpr (H == 1 && G == 10) asm volatile("s_lshr_b64 s[80:81], s[80:81], 4\n\tv_lshl_add_u32 v91, s82, 19, v93");
    if constexpr (G == 61) {
      constexpr int nst = e_nst(MODE, H ? 29 : 0, GG + 1) + (MODE == 4 && !H ? e_nst(2, 93, 128) : 0);
      asm volatile("s_waitcnt vmcnt(%0) lgkmcnt(0)\n\ts_barrier" ::"i"(12 + nst) : "memory");
    }
  }
  if constexpr (END && GG == 127) {
    asm volatile("s_nop 7");
    e_ops<TR, false, ESCH<TR, MODE>.op0[128]>(make_iseq<ESCH<TR, MODE>.nop[128]>{});
  }
  GAP();
}
template <bool TR, int P, int H, int MODE, int... Gs> DI void g_half_(GM& g, iseq<Gs...>) { (g_gap<TR, P, H, Gs, MODE>(g), ...); }
template <bool TR, int P, int H, int MODE> DI void g_half(GM& g) { g_half_<TR, P, H, MODE>(g, make_iseq<64>{}); }
template <bool TR, int P, int MODE> DI void g_tile(GM& g) { g_half<TR, P, 0, MODE>(g); g_half<TR, P, 1, MODE>(g); }
template <int P, int... Is> DI void g_read0(GM& g, iseq<Is...>) {
  (g_wread<P, 0, Is>(g), ...); (g_wread<P, 1, Is>(g), ...);
  g_aread<P, 0, 0>(g); g_aread<P, 0, 1>(g);
}
DI void g_proW(unsigned g, unsigned mW) {
  asm volatile("v_lshl_add_u32 v91, %0, 18, v93\n\ts_mov_b32 m0, %1" ::"s"(g), "s"(mW));
  asm volatile("s_nop 0"); g_dma<true, 0, 91>();
  asm volatile("s_bitset1_b32 m0, 12\n\ts_nop 0"); g_dma<true, 1, 91>();
  asm volatile("s_bitset1_b32 m0, 13\n\ts_nop 0"); g_dma<true, 3, 91>();
  asm volatile("s_bitset0_b32 m0, 12\n\ts_nop 0"); g_dma<true, 2, 91>();
}
template <int PART = 3> DI void g_pro(unsigned i, unsigned mA, unsigned mW, unsigned lda) {
  i &= 3;
  if constexpr (PART & 1)
    asm volatile("v_add_u32 v90, %0, v92\n\tv_add_u32 v91, %1, v93\n\ts_mov_b32 m0, %2\n\ts_mov_b32 s54, %3" ::"s"(((i & 1) + (i >> 1) * 4) * 64 * lda),
                 "s"((i & 1) * 0x20000 + (i >> 1) * 256), "s"(mA + (i & 1) * 4096 + (i >> 1) * 16384), "s"(mW + i * 4096));
  if constexpr (PART == 1) return;
  g_dma<false, 0, 90>();
  asm volatile("s_bitset1_b32 m0, 13\n\ts_nop 0"); g_dma<false, 2, 90>();
  asm volatile("s_mov_b32 m0, s54\n\ts_nop 0"); g_dma<true, 0, 91>();
  asm volatile("s_bitset1_b32 m0, 14\n\ts_nop 0"); g_dma<true, 4, 91>();
  asm volatile("s_xor_b32 m0, s54, 0x8000\n\ts_nop 0\n\tbuffer_load_dwordx4 v91, s[60:63], s53 offen lds" ::: "memory", "scc");
}
DI void g_pro2(unsigned mA, unsigned mW) {
  asm volatile("v_add_u32 v90, 128, v92\n\ts_mov_b32 m0, %0" ::"s"(mA ^ 32768));
  asm volatile("s_nop 0"); g_dma<false, 0, 90>();
  asm volatile("s_bitset1_b32 m0, 12\n\ts_nop 0"); g_dma<false, 1, 90>();
  asm volatile("s_bitset1_b32 m0, 14\n\ts_nop 0"); g_dma<false, 5, 90>();
  asm volatile("s_bitset0_b32 m0, 12\n\ts_nop 0"); g_dma<false, 4, 90>();
  g_proW(3, mW ^ 49152);
}
template <bool TR, bool SCL, class H0, class H1> DI void g_loop(GM& g, H0&& early, H1&& late) {
  constexpr int nkt = SCL ? 7 : 16, P0 = SCL ? 0 : 1;
#pragma unroll 1
  for (int t = 1;; t += 2) {
    asm volatile("" : "+s"(t));
    g_half<TR, P0 ^ 1, 0, 0>(g);
    if (__builtin_expect(t == (SCL ? 5 : 13), 0)) { late(); GAP(); }
    g_half<TR, P0 ^ 1, 1, 0>(g);
    if (SCL && t + 2 >= nkt) break;
    g_half<TR, P0, 0, 0>(g);
    if (__builtin_expect(t == (SCL ? 3 : 1), 0)) { early(); GAP(); }
    if constexpr (!SCL) asm volatile("s_mov_b32 s78, s83");
    g_half<TR, P0, 1, 0>(g);
    if (!SCL && t + 3 >= nkt) break;
  }
}

typedef __attribute__((ext_vector_type(4))) unsigned u32x4;
typedef __attribute__((ext_vector_type(4))) float f32x4;

constexpr int R_S = 128, R_P = 192, R_CI = 224, R_E = 96;
constexpr int R_TS = 112, R_RS = 114, R_M = 116;
constexpr int R_PD = 118, R_THR = 120, R_TMP = 121;
constexpr int r_dk(int q) { return q < 6 ? 90 + q : 120 + q; }
constexpr int S_V = 96, S_T = 97, S_K = 98, S_I = 99, S_EV = 100;
struct AC {
  unsigned rare;
  unsigned tb, vs, o0, o1;
};
DI bool redo() { u64 t; asm volatile("s_mov_b64 %0, s[54:55]" : "=s"(t)); return t != 0; }
template <int X, int F> DI void kread() {
  constexpr int r = 192 + 4 * (F & 15);
  asm volatile("ds_read_b128 a[%c0:%c1], v84 offset:%c2" ::"i"(r), "i"(r + 3), "i"(X * 32768 + F * 1024));
}
template <int X, int F> DI void vread() {
  constexpr int r = 192 + 4 * (F & 15);
  asm volatile("ds_read_b128 a[%c0:%c1], v85 offset:%c2" ::"i"(r), "i"(r + 3), "i"(X * 32768 + (F & 1) * 16384 + (F >> 1) * 1024));
}
template <int X, int... Fs> DI void kreads(iseq<Fs...>) { (kread<X, Fs>(), ...); }
template <int X, int J> DI void dma_m0() {
  if constexpr (J == 0) asm volatile("s_mov_b32 m0, s%c0" ::"i"(78 + X));
  else if constexpr (J == 4) asm volatile("s_or_b32 m0, s%c0, 0x1000" ::"i"(78 + X));
  else if constexpr (J == 8) asm volatile("s_bitset0_b32 m0, 16");
  else if constexpr (J == 12) asm volatile("s_bitset0_b32 m0, 12");
}
template <int J> DI void dma_at() {
  if constexpr (J < 8)
    asm volatile("buffer_load_dwordx4 v%c0, s[88:91], s96 offen offset:%1 sc1 lds" ::"i"(86 + (J >= 4)), "i"((J & 3) * 1024) : "memory");
  else
    asm volatile("buffer_load_dwordx4 v%c0, s[92:95], s98 offen offset:%1 sc1 lds" ::"i"(r_dk(J - 8)), "i"((J & 3) * 1024) : "memory");
}
template <int Q> DI void dma4k() {
  asm volatile("s_xor_b32 m0, s79, %0\n\ts_add_u32 s48, s98, %1\n\tbuffer_load_dwordx4 v%c2, s[88:91], s48 offen sc1 lds\n\t"
               "buffer_load_dwordx4 v%c2, s[88:91], s48 offen offset:1024 sc1 lds\n\tbuffer_load_dwordx4 v%c2, s[88:91], s48 offen offset:2048 sc1 lds\n\t"
               "buffer_load_dwordx4 v%c2, s[88:91], s48 offen offset:3072 sc1 lds"
               ::"i"((Q >> 1) * 32768 + (Q & 1) * 4096), "i"((Q >> 1) * 32768), "i"(86 + (Q & 1)) : "memory", "scc");
}
DI void acc_zero() { static_for([](auto I) { asm volatile("v_accvgpr_write_b32 a[%c0], 0" ::"i"((int)I)); }, make_iseq<128>{}); }
#define SMV 0
struct SMS { int ex[128], op0[128], nop[128], last; };
constexpr SMS sm_sched() {
  SMS z{};
  int et[32] = {}, e = 0;
  for (int t = 0; t < 128; ++t) {
    const int g = t & 15;
    const bool x = t < 48 ? (g >= 8 && (g & 1) && g < 15) : t < 64 ? (g >= 9 && g < 15) : (g >= 8 && g < 14);
    z.ex[t] = x && e < 32 ? e : -1;
    if (z.ex[t] >= 0) et[e++] = t;
  }
  int o = 0;
  for (int t = 0; t < 128; ++t) {
    const int g = t & 15;
    int cap = z.ex[t] >= 0 || t < 8 || (t < 48 && (g == 7 || (g >= 8 && !(g & 1)))) || t == 56 || t == 63 || t == 111 ? 0 : 1;
    z.op0[t] = o; z.nop[t] = 0;
    while (cap > 0 && o < 48 && et[2 * (o / 3) + (o % 3 == 0 ? 0 : 1)] + 2 <= t) {
      const int el = 2 * (o / 3) + o % 3;
      if (!(o % 3 < 2 && (el == 0 || el == 4))) --cap;
      ++o; ++z.nop[t]; z.last = t;
    }
  }
  for (int k = 16; k < 32; ++k) if (z.op0[et[k]] < 3 * ((k - 16) / 2 + 1)) z.last = 1000;
  return z;
}
constexpr SMS SM = sm_sched();
static_assert(SM.last <= 117);
constexpr int p_reg(int Y, int k) { return R_P + 16 * Y + 4 * ((k >> 3) * 2 + ((k >> 1) & 1)) + ((k >> 2) & 1) * 2 + (k & 1); }
template <int Y, int O> DI void sm_op() {
  constexpr int k = O / 3, e = 2 * k + O % 3, ts = R_TS + ((e >> 2) & 1);
  if constexpr (O % 3 == 2) asm volatile("v_cvt_pk_bf16_f32 v%c0, v%c1, v%c2" ::"i"(p_reg(Y, k)), "i"(R_E + ((2 * k) & 15)), "i"(R_E + ((2 * k + 1) & 15)));
  else if constexpr (e == 1 || e == 5) asm volatile("v_add_f32_e32 v%c0, v%c1, v%c2" ::"i"(ts), "i"(R_E + e - 1), "i"(R_E + e));
  else if constexpr (e != 0 && e != 4) asm volatile("v_add_f32_e32 v%c0, v%c0, v%c1" ::"i"(ts), "i"(R_E + (e & 15)));
}
template <int Y, int T> DI void sm_gap(AC& c) {
  if constexpr (SM.ex[T] >= 0) asm volatile("v_exp_f32 v%c0, v%c1" ::"i"(R_E + (SM.ex[T] & 15)), "i"(R_S + 32 * Y + SM.ex[T]));
  if constexpr (SM.nop[T] > 0) sm_op<Y, SM.op0[T]>();
  if constexpr (SM.nop[T] > 1) sm_op<Y, SM.op0[T] + 1>();
  if constexpr (SM.nop[T] > 2) sm_op<Y, SM.op0[T] + 2>();
  if constexpr (T == SM.last + 1) asm volatile("v_add_f32_e32 v%c0, v%c1, v%c2" ::"i"(R_TMP), "i"(R_TS), "i"(R_TS + 1));
  if constexpr (T == SM.last + 2) asm volatile("v_cmp_nle_f32_e64 s[54:55], v%c0, v%c1" ::"i"(R_TMP), "i"(R_THR));
}
template <int X, int MODE, int G> DI void gapA(AC& c) {
  constexpr bool FIRST = MODE == 1 || MODE == 2, NOQK = MODE == 3;
  constexpr int Xp = X ^ 1, kb = G >> 4, g = G & 15, qb = g >> 3, s = g & 7;
  if constexpr (MODE == 3 && G < 48 && G % 3 == 0) {
    constexpr int f = G / 3;
    asm volatile("buffer_load_dwordx4 a[%c0:%c1], v84, s[88:91], s75 offen offset:%2 sc1" ::"i"(128 + 4 * f), "i"(131 + 4 * f), "i"((f & 3) * 1024) : "memory");
    if constexpr ((f & 3) == 3) asm volatile("s_addk_i32 s75, %0" ::"i"(f == 7 ? 28672 : 4096));
  }
  if constexpr (G == 48) asm volatile("s_waitcnt vmcnt(%0) lgkmcnt(0)\n\ts_barrier" ::"i"(MODE == 3 ? 16 : MODE == 2 ? 12 : MODE == 1 ? 7 : 0) : "memory");
  else if constexpr (g == 0 && !NOQK) asm volatile("s_waitcnt lgkmcnt(0)");
  if constexpr (!NOQK) {
    constexpr int r = 192 + 4 * ((kb & 1) * 8 + s), q = 128 + 4 * g;
    constexpr int d = R_S + 32 * X + 4 * (kb * 2 + qb), ci = s == 0 ? R_CI + 4 * (kb * 2 + qb) : d;
    if constexpr (FIRST && s == 0)
      asm volatile("v_mfma_f32_16x16x32_bf16 v[%c0:%c1], a[%c2:%c3], a[%c4:%c5], 0" ::"i"(d), "i"(d + 3), "i"(r), "i"(r + 3), "i"(q), "i"(q + 3));
    else
      asm volatile("v_mfma_f32_16x16x32_bf16 v[%c0:%c1], a[%c2:%c3], a[%c4:%c5], v[%c6:%c7]" ::"i"(d), "i"(d + 3), "i"(r), "i"(r + 3), "i"(q), "i"(q + 3),
                   "i"(ci), "i"(ci + 3));
    if constexpr (g < 8 && kb < 3) kread<X, (kb + 1) * 8 + g>();
    constexpr int S0 = MODE == 1 ? 0 : SMV ? 40 : 14, S1 = MODE == 1 ? 1 : SMV ? 41 : 30, S2 = SMV ? 42 : 45, S3 = SMV ? 43 : 46;
    if constexpr (G == S0) asm volatile("s_add_u32 s96, s97, 0x1000000");
    if constexpr (G == S1) dma_m0<X, 0>();
    if constexpr (G == S2) asm volatile("s_mov_b32 s97, s98");
    if constexpr (G == S3) asm volatile("s_add_u32 s98, s98, s99");
    if constexpr (MODE != 1 && G >= 56 && !(G & 1)) dma_at<(G - 56) / 2>();
    if constexpr (MODE == 1 && (G & 11) == 9) dma_at<(G >> 4) * 2 + ((G >> 2) & 1)>();
    if constexpr (MODE == 1 && G == 31) dma_m0<X, 4>();
    if constexpr (G == 57) asm volatile("s_lshr_b32 s100, s100, 1\n\ts_add_u32 s56, s56, 1");
    if constexpr (G == 59) asm volatile("s_getpc_b64 s[86:87]\nev_pc%c0:\n\ts_add_u32 s86, s86, ev_ret%c0-ev_pc%c0\n\ts_addc_u32 s87, s87, 0" ::"i"(X * 4 + MODE));
    if constexpr (G == 61 && !FIRST) asm volatile("s_or_b32 s57, s54, s55\n\ts_mov_b32 %0, s57\n\ts_bitcmp1_b32 s100, 0" : "=s"(c.rare));
    if constexpr (G == 61 && FIRST) asm volatile("s_bitcmp1_b32 s100, 0");
  } else if constexpr (SM.nop[64 + G] > 0) asm volatile("s_nop 0");
  if constexpr (!FIRST && g < 8 && kb == 3) vread<Xp, g>();
  if constexpr (MODE == 2 && (G & 3) == 0) d_store<16 + G / 4>();
  if constexpr (!FIRST) sm_gap<Xp, 64 + G>(c);
  if constexpr (G == 63 && !NOQK) asm volatile("s_cbranch_scc1 ev_entry\nev_ret%c0:" ::"i"(X * 4 + MODE) : "vcc", "memory");
  GAP();
}
template <int R> DI void rescale1(float alpha) {
  float t;
  asm volatile("v_accvgpr_read_b32 %0, a[%c2]\n\tv_mul_f32 %0, %0, %1\n\tv_accvgpr_write_b32 a[%c2], %0" : "=&v"(t) : "v"(alpha), "i"(R));
}
template <int... Rs> DI void rescale_all(const float (&alpha)[2], iseq<Rs...>) { (rescale1<Rs>(alpha[(Rs >> 2) & 1]), ...); }
template <bool MAX> DI float row_reduce(float v) {
  auto p = __builtin_amdgcn_permlane32_swap(__float_as_uint(v), __float_as_uint(v), false, false);
  v = MAX ? fmaxf(__uint_as_float(p[0]), __uint_as_float(p[1])) : __uint_as_float(p[0]) + __uint_as_float(p[1]);
  p = __builtin_amdgcn_permlane16_swap(__float_as_uint(v), __float_as_uint(v), false, false);
  return MAX ? fmaxf(__uint_as_float(p[0]), __uint_as_float(p[1])) : __uint_as_float(p[0]) + __uint_as_float(p[1]);
}
template <int Y, int QB> DI float row_max() {
  constexpr int r = R_S + 32 * Y + 4 * QB;
  float mx;
  asm volatile("v_max_f32 %0, v%c1, v%c2\n\tv_max3_f32 %0, %0, v%c3, v%c4" : "=&v"(mx) : "i"(r), "i"(r + 1), "i"(r + 2), "i"(r + 3));
  static_for([&](auto K) {
    constexpr int q = r + 8 * (K + 1);
    float t = mx;
    asm volatile("v_max3_f32 %0, %0, v%c1, v%c2\n\tv_max3_f32 %0, %0, v%c3, v%c4" : "+v"(t) : "i"(q), "i"(q + 1), "i"(q + 2), "i"(q + 3));
    mx = t;
  }, make_iseq<3>{});
  return row_reduce<true>(mx);
}
template <int R> DI void v_sub(float d) { asm volatile("v_sub_f32 v%c0, v%c0, %1" ::"i"(R), "v"(d)); }
template <int B> DI void s_shift(const float (&d)[2]) { static_for([&](auto E) { v_sub<B + E>(d[(E >> 2) & 1]); }, make_iseq<32>{}); }
template <bool CI> DI void m_shift(const float (&d)[2]) {
  if constexpr (CI) s_shift<R_CI>(d);
  v_sub<R_M>(-d[0]); v_sub<R_M + 1>(-d[1]);
}
DI void set_mask0() {
  const int mr = __float_as_int(vget<89>());
  static_for([&](auto I) {
    constexpr int e = (I >> 2) * 8 + (I & 3), k = (I >> 2) * 16 + (I & 3);
    const float x = vget<R_S + e>();
    vset<R_S + e>(k > mr ? -INFINITY : x);
  }, make_iseq<16>{});
}
template <int X, bool CI = true> DI void sm_redo() {
  constexpr int Xp = X ^ 1;
  float d[2], dt[2], alpha[2], ts[2] = {0.f, 0.f};
  static_for([&](auto Q) {
    constexpr int qb = Q;
    const float pd = vget<R_PD + qb>();
    d[qb] = fmaxf(row_max<Xp, qb>() - pd, 0.f);
    dt[qb] = d[qb] + pd;
    vset<R_PD + qb>(d[qb]);
    alpha[qb] = __builtin_amdgcn_exp2f(-d[qb]);
    vset<R_RS + qb>(vget<R_RS + qb>() * alpha[qb]);
  }, make_iseq<2>{});
  vset<R_THR>(d[0] > 0.f || d[1] > 0.f ? -1.f : 4096.f);
  m_shift<CI>(d);
  static_for([&](auto K) {
    constexpr int k = K, qb = (k >> 1) & 1;
    const float e0 = __builtin_amdgcn_exp2f(vget<R_S + 32 * Xp + 2 * k>() - dt[qb]), e1 = __builtin_amdgcn_exp2f(vget<R_S + 32 * Xp + 2 * k + 1>() - dt[qb]);
    ts[qb] += e0; ts[qb] += e1;
    vset<p_reg(Xp, k)>(__uint_as_float(pk2(e0, e1)));
  }, make_iseq<16>{});
  vset<R_TS>(ts[0]); vset<R_TS + 1>(ts[1]);
  rescale_all(alpha, make_iseq<128>{});
  asm volatile("s_nop 1" ::: "memory");
  GAP();
}
template <int X, int MODE, int... Gs> DI void phaseA_(AC& c, iseq<Gs...>) { (gapA<X, MODE, Gs>(c), ...); }
template <int X, int MODE> DI void phaseA(AC& c) { phaseA_<X, MODE>(c, make_iseq<64>{}); }

template <int X, bool SMX, int G, bool FIRST = false> DI void gapB(AC& c) {
  constexpr int Xp = X ^ 1, blk = G >> 4, g = G & 15;
  if constexpr (g == 0) asm volatile("s_waitcnt lgkmcnt(0)");
  constexpr int qb = (g >> 1) & 1, s = g & 1, r = 192 + 4 * ((blk & 1) * 8 + (g >> 2) * 2 + s), o = 4 * ((blk * 4 + (g >> 2)) * 2 + qb);
  constexpr int p = R_P + 16 * Xp + 4 * (s * 2 + qb);
  if constexpr (FIRST && s == 0)
    asm volatile("v_mfma_f32_16x16x32_bf16 a[%c0:%c1], a[%c2:%c3], v[%c4:%c5], 0" ::"i"(o), "i"(o + 3), "i"(r), "i"(r + 3), "i"(p), "i"(p + 3));
  else
  asm volatile("v_mfma_f32_16x16x32_bf16 a[%c0:%c1], a[%c2:%c3], v[%c4:%c5], a[%c0:%c1]" ::"i"(o), "i"(o + 3), "i"(r), "i"(r + 3), "i"(p), "i"(p + 3));
  if constexpr (g < 8) {
    if constexpr (blk < 3) vread<Xp, (blk + 1) * 8 + g>();
    else if constexpr (SMX) kread<Xp, g>();
  }
  if constexpr (SMX) {
    if constexpr (G == 1 || G == 2) asm volatile("v_add_f32_e32 v%c0, v%c0, v%c1" ::"i"(R_RS + G - 1), "i"(R_TS + G - 1));
    if constexpr (g == 7 && blk < 3) dma_m0<X, blk * 4 + 4>();
    if constexpr (g >= 8 && !(g & 1) && blk < 3) dma_at<blk * 4 + 4 + (g - 8) / 2>();
    if constexpr (G == 55) asm volatile("s_cmp_eq_u32 s56, s76");
    if constexpr (G == 3) asm volatile("s_lshr_b32 s77, s77, 1");
    if constexpr (G == 4) asm volatile("s_bitcmp1_b32 s77, 0");
    if constexpr (G == 5) asm volatile("s_cselect_b64 s[52:53], s[62:63], 0");
    if constexpr (G == 6) asm volatile(FL_ARM("s[52:53]"));
    if constexpr (G == 56) asm volatile(FL_POLL ::: "memory");
    sm_gap<X, G>(c);
  }
  GAP();
}
template <int X, bool FIRST, int... Gs> DI void phaseB_(AC& c, iseq<Gs...>) { (gapB<X, true, Gs, FIRST>(c), ...); }
template <int X, bool FIRST = false> DI void phaseB(AC& c) { phaseB_<X, FIRST>(c, make_iseq<64>{}); }
constexpr int sm_nex() { int n = 0; for (int t = 0; t < 64; ++t) n += SM.ex[t] >= 0; return n; }
template <int X> DI void phaseB0(unsigned mode, bool vh) {
  dma_m0<X, 4>();
  kreads<X ^ 1>(make_iseq<8>{});
  if (vh) { dma_at<4>(); dma_at<5>(); dma_at<6>(); dma_at<7>(); }
  dma_m0<X, 8>();
  asm volatile("s_nop 3");
  float d[2];
  if (mode & 2) {
    d[0] = vget<122>(); d[1] = vget<123>();
    dma_at<8>();
  } else {
    if constexpr (X == 0) if (__builtin_expect(mode & 4, 0)) set_mask0();
    d[0] = row_max<X, 0>(); GAP(); dma_at<8>();
    d[1] = row_max<X, 1>();
#pragma unroll
    for (int qb = 0; qb < 2; ++qb) if (!(d[qb] > -3.0e38f)) d[qb] = 0.f;
  }
  GAP();
  vset<R_M>(d[0]); vset<R_M + 1>(d[1]);
  dma_at<9>();
  const f32x2c nd[2] = {{-d[0], -d[0]}, {-d[1], -d[1]}};
  static_for([&](auto E) {
    constexpr int e = E;
    const f32x2c v = nd[(e >> 1) & 1];
    asm volatile("v_pk_add_f32 v[%c0:%c1], v[%c0:%c1], %4\n\tv_mov_b64 v[%c2:%c3], %4" ::"i"(R_S + 32 * X + 2 * e), "i"(R_S + 32 * X + 2 * e + 1), "i"(R_CI + 2 * e), "i"(R_CI + 2 * e + 1), "v"(v));
    if constexpr (e == 7 || e == 15) dma_at<10 + e / 8>();
  }, make_iseq<16>{});
  static_assert(SM.op0[64] >= 3 * ((sm_nex() - 15) / 2) && SM.op0[64] <= 24);
  static_for([&](auto E) {
    constexpr int e = E;
    if constexpr (e < sm_nex()) asm volatile("v_exp_f32 v%c0, v%c1" ::"i"(R_E + e), "i"(R_S + 32 * X + e));
    if constexpr (e == 2) dma_m0<X, 12>();
    if constexpr (e == 5 || e == 10 || e == 15) dma_at<11 + e / 5>();
  }, make_iseq<16>{});
  static_for([&](auto O) { sm_op<X, O>(); }, make_iseq<SM.op0[64]>{});
  dma_at<15>();
  static_for([&](auto E) { asm volatile("v_exp_f32 v%c0, v%c1" ::"i"(R_E + (int)E), "i"(R_S + 32 * X + 16 + (int)E)); }, make_iseq<(sm_nex() > 16 ? sm_nex() - 16 : 0)>{});
  asm volatile("buffer_store_dwordx2 v[%c0:%c1], v88, s[88:91], s76 offen sc1\n\ts_bitcmp1_b32 s77, 0\n\ts_cselect_b64 s[52:53], s[62:63], 0\n\t"
               FL_ARM("s[52:53]") "\n\t" FL_POLL ::"i"(R_M), "i"(R_M + 1) : "memory", "scc");
  GAP();
}
enum : int { T_Q0, T_K0, T_OQ, T_OG, T_FPREP, T_FQ, T_FG, T_FS, T_ZO, T_CNT, T_NPRE = 10, T_NST = 12, T_TAB = 14, T_SLAB = 16, T_PART, T_IT = 18, T_ITN = 23 };
#define MSK(R, K) "v_cmp_gt_i32 s[48:49], " #K ", v89\n\tv_cmp_gt_i32 s[50:51], " #K "+1, v89\n\tv_cmp_gt_i32 s[52:53], " #K "+2, v89\n\t" \
                  "v_cmp_gt_i32 s[54:55], " #K "+3, v89\n\tv_cndmask_b32_e64 v[" #R "], v[" #R "], v122, s[48:49]\n\t" \
                  "v_cndmask_b32_e64 v[" #R "+1], v[" #R "+1], v122, s[50:51]\n\tv_cndmask_b32_e64 v[" #R "+2], v[" #R "+2], v122, s[52:53]\n\t" \
                  "v_cndmask_b32_e64 v[" #R "+3], v[" #R "+3], v122, s[54:55]\n\t"
DI void ev_code(AC& c) {
  GAP();
  asm volatile(
    "s_getpc_b64 s[82:83]\n\ts_add_u32 s82, s82, 12\n\ts_addc_u32 s83, s83, 0\n\ts_branch 9f\nev_entry:\n\t"
    "s_bitcmp1_b32 s60, s56\n\ts_cbranch_scc1 3f\n\ts_bitcmp1_b32 s74, s56\n\ts_cbranch_scc1 2f\n\t"
    "s_bitcmp1_b32 s58, s56\n\ts_cselect_b64 vcc, -1, 0\n\tv_cndmask_b32 v121, -2.0, v81, vcc\n\t"
    "buffer_store_dwordx2 v[82:83], v121, s[88:91], s70 offen sc1\n\tbuffer_store_dwordx2 v[82:83], v121, s[88:91], s71 offen sc1\n\t"
    "v_cmp_eq_u64 vcc, s[84:85], v[124:125]\n\ts_andn2_b64 vcc, s[62:63], vcc\n\ts_cbranch_scc1 5f\n"
    "0:\n\ts_add_u32 s98, s98, s59\n\ts_mov_b32 s59, s61\n\ts_mov_b32 s61, s72\n\ts_mov_b64 s[62:63], s[64:65]\n\ts_mov_b64 s[64:65], s[66:67]\n\t"
    "s_mov_b64 s[66:67], s[68:69]\n\ts_cmp_eq_u32 s56, 1\n\ts_cbranch_scc1 ev_first\n\ts_setpc_b64 s[86:87]\n"
    "5:\n\t" FL_CHECK("s[62:63]") "\n\ts_branch 0b\n"
    "6:\n\t" FL_CHECK("s[62:63]") "\n\ts_setpc_b64 s[86:87]\n"
    "2:\n\ts_mov_b32 s98, 0\n\t"
    "v_lshrrev_b32 v90, 7, v86\n\tv_and_b32 v91, 6, v90\n\tv_lshlrev_b32 v91, 4, v91\n\tv_and_b32 v92, 0x70, v86\n\tv_xor_b32 v91, v91, v92\n\t"
    "s_movk_i32 s48, 896\n\tv_mad_u32_u24 v94, v90, s48, v91\n\ts_mov_b64 s[92:93], s[80:81]\n\ts_mov_b32 s94, 229376\n\t"
    "v_add_u32 v90, 28672, v94\n\tv_add_u32 v91, 34816, v94\n\tv_add_u32 v92, 40960, v94\n\tv_add_u32 v93, 47104, v94\n\t"
    "v_add_u32 v95, 6144, v94\n\tv_add_u32 v126, 12288, v94\n\tv_add_u32 v127, 18432, v94\n\t"
    "v_mov_b32 v122, 0xff800000\n\t" MSK(228, 0) MSK(236, 16) MSK(244, 32) MSK(252, 48)
    "v_cmp_eq_u64 vcc, s[84:85], v[124:125]\n\ts_andn2_b64 vcc, s[62:63], vcc\n\ts_cbranch_scc1 6b\n\ts_setpc_b64 s[86:87]\n"
    "3:\n\tv_mov_b32 v122, 0xff800000\n\t" MSK(224, 0) MSK(232, 16) MSK(240, 32) MSK(248, 48)
    "v_readlane_b32 s54, v78, %0\n\ts_add_u32 m0, s78, 0x10000\n\t"
    "s_bfe_u32 s53, s73, 0x80010\n\ts_mulk_i32 s53, 3840\n\ts_mov_b64 s[48:49], s[80:81]\n\ts_add_u32 s50, s53, 3840\n\ts_min_u32 s50, s50, 229376\n\t"
    "s_mov_b32 s51, 0x20000\n\t"
    "v_lshlrev_b32 v121, 3, v84\n\tbuffer_load_dword v121, s[48:51], s53 offen lds\n\t"
    "s_bitcmp0_b32 s73, 3\n\ts_cbranch_scc1 4f\n\t"
    "buffer_load_dwordx4 v84, s[88:91], s54 offen sc1 lds\n\tbuffer_load_dwordx4 v84, s[88:91], s54 offen offset:1024 sc1 lds\n\t"
    "buffer_load_dwordx4 v84, s[88:91], s54 offen offset:2048 sc1 lds\n\tbuffer_load_dwordx4 v84, s[88:91], s54 offen offset:3072 sc1 lds\n"
    "4:\n\ts_cmp_eq_u32 s56, 1\n\ts_cbranch_scc1 ev_first\n\ts_setpc_b64 s[86:87]\n"
    ::"i"((int)T_ZO) : "scc");
  asm volatile("ev_zero:");
  acc_zero();
  asm volatile("s_setpc_b64 s[86:87]\nev_first:\n\ts_cmp_lg_u32 s57, 0\n\ts_cbranch_scc1 ev_zero\n\ts_bitcmp0_b32 s73, 4\n\ts_cbranch_scc1 ev_first1");
  GAP();
  phaseB<0, true>(c);
  asm volatile("s_branch att_loop\nev_first1:");
  phaseB<1, true>(c);
  asm volatile("s_branch att_mid\n9:");
  GAP();
}
DI void ev_call() { asm volatile("s_swappc_b64 s[86:87], s[82:83]" ::: "vcc", "scc", "memory"); GAP(); }

#define ACLOB_ALL "a0","a1","a2","a3","a4","a5","a6","a7","a8","a9","a10","a11","a12","a13","a14","a15","a16","a17","a18","a19","a20","a21","a22","a23","a24","a25","a26","a27","a28","a29","a30","a31","a32","a33","a34","a35","a36","a37","a38","a39","a40","a41","a42","a43","a44","a45","a46","a47","a48","a49","a50","a51","a52","a53","a54","a55","a56","a57","a58","a59","a60","a61","a62","a63","a64","a65","a66","a67","a68","a69","a70","a71","a72","a73","a74","a75","a76","a77","a78","a79","a80","a81","a82","a83","a84","a85","a86","a87","a88","a89","a90","a91","a92","a93","a94","a95","a96","a97","a98","a99","a100","a101","a102","a103","a104","a105","a106","a107","a108","a109","a110","a111","a112","a113","a114","a115","a116","a117","a118","a119","a120","a121","a122","a123","a124","a125","a126","a127","a128","a129","a130","a131","a132","a133","a134","a135","a136","a137","a138","a139","a140","a141","a142","a143","a144","a145","a146","a147","a148","a149","a150","a151","a152","a153","a154","a155","a156","a157","a158","a159","a160","a161","a162","a163","a164","a165","a166","a167","a168","a169","a170","a171","a172","a173","a174","a175","a176","a177","a178","a179","a180","a181","a182","a183","a184","a185","a186","a187","a188","a189","a190","a191","a192","a193","a194","a195","a196","a197","a198","a199","a200","a201","a202","a203","a204","a205","a206","a207","a208","a209","a210","a211","a212","a213","a214","a215","a216","a217","a218","a219","a220","a221","a222","a223","a224","a225","a226","a227","a228","a229","a230","a231","a232","a233","a234","a235","a236","a237","a238","a239","a240","a241","a242","a243","a244","a245","a246","a247","a248","a249","a250","a251","a252","a253","a254","a255"

struct EP { float fa[2], fb[2]; };
template <int I, int E, int MODE> DI void epi_pair(const EP& p) {
  constexpr int qb = I & 1, r = 16 * (I >> 1) + 4 * qb + (E >> 1) * 8 + (E & 1) * 2, t = 96 + 2 * E, w = 128 + 4 * I + E;
  if constexpr (MODE == 2) asm volatile("v_accvgpr_read_b32 v106, a[%c0]" ::"i"(w));
  asm volatile("v_accvgpr_read_b32 v%c0, a[%c2]\n\tv_accvgpr_read_b32 v%c1, a[%c3]" ::"i"(t), "i"(t + 1), "i"(r), "i"(r + 1));
  if constexpr (MODE) asm volatile("v_mul_f32 v%c0, v%c0, %2\n\tv_mul_f32 v%c1, v%c1, %2" ::"i"(t), "i"(t + 1), "v"(p.fa[qb]));
  if constexpr (MODE == 2)
    asm volatile("v_lshlrev_b32 v104, 16, v106\n\tv_and_b32 v105, 0xffff0000, v106\n\tv_fmac_f32 v%c0, v104, %2\n\tv_fmac_f32 v%c1, v105, %2"
                 ::"i"(t), "i"(t + 1), "v"(p.fb[qb]));
  asm volatile("v_cvt_pk_bf16_f32 v%c0, v%c1, v%c2" ::"i"(w), "i"(t), "i"(t + 1));
}
template <int I> DI void epi_store() {
  asm volatile("buffer_store_dwordx4 v[%c0:%c1], v84, s[88:91], s101 offen offset:%2 sc1" ::"i"(128 + 4 * I), "i"(131 + 4 * I), "i"((I & 3) * 1024) : "memory");
  if constexpr ((I & 3) == 3) asm volatile("s_addk_i32 s101, 4096");
}
constexpr int R_TB = 232, R_VS = 236;
template <int HF, int R> DI void p_read() {
  constexpr int mb = R >> 4, r = 192 - 64 * HF + 2 * R;
  asm volatile("ds_read_b64_tr_b16 a[%c1:%c2], v%c0 offset:%c3" ::"i"(R_TB + 2 * HF + mb), "i"(r), "i"(r + 1), "i"((R & 15) * 2048));
}
template <int QB, int E> DI void p_cvt() {
  asm volatile("v_cvt_pk_bf16_f32 v%c0, v%c1, v%c2" ::"i"(208 + 4 * QB + E), "i"(192 + 8 * QB + 2 * E), "i"(193 + 8 * QB + 2 * E));
}
template <int QB, int PR> DI void p_store() {
  asm volatile("buffer_store_dwordx4 v[%c0:%c1], v%c2, s[88:91], s96 offen offset:%3 sc1" ::"i"(208 + 4 * QB), "i"(211 + 4 * QB), "i"(R_VS + QB),
               "i"(PR * 128) : "memory");
}
constexpr int p_dma(int HF, int G) { return G == 3 && !HF ? 0 : G == 11 ? 1 - HF : G == 15 ? 2 - HF : G == 19 ? 3 - HF : G == 27 && HF ? 3 : -1; }
template <int HF, bool LD, int G> DI void p_gap() {
  constexpr int ch = G >> 3, t = G & 7, a = 192 - 64 * HF + 32 * (ch & 1) + 4 * t, b = 128 + 4 * (2 * t + (ch >> 1)), d = 192 + 4 * ch;
  if constexpr (t == 0)
    asm volatile("v_mfma_f32_16x16x32_bf16 v[%c0:%c1], a[%c2:%c3], v[%c4:%c5], 0" ::"i"(d), "i"(d + 3), "i"(a), "i"(a + 3), "i"(b), "i"(b + 3));
  else
    asm volatile("v_mfma_f32_16x16x32_bf16 v[%c0:%c1], a[%c2:%c3], v[%c4:%c5], v[%c0:%c1]" ::"i"(d), "i"(d + 3), "i"(a), "i"(a + 3), "i"(b), "i"(b + 3));
  p_read<HF ^ 1, G>();
  if constexpr (G == 0) asm volatile("v_xor_b32 v%c0, s100, v%c0" ::"i"(R_TB + 2 * HF + 1));
  if constexpr (G == 17) asm volatile("v_xor_b32 v%c0, s100, v%c0" ::"i"(R_TB + 2 * (HF ^ 1)));
  if constexpr (G >= 4 && G <= 8 && G != 7) p_cvt<1, (G < 7 ? G - 4 : 3)>();
  if constexpr (G == 24 || (G == 7 && HF == 0)) asm volatile("s_waitcnt lgkmcnt(8)");
  if constexpr (G == 7 && HF == 1) asm volatile("s_waitcnt vmcnt(%0) lgkmcnt(8)\n\ts_barrier" ::"i"(LD ? 13 : 5) : "memory");
  if constexpr (G == 10) p_store<1, HF ^ 1>();
  if constexpr (G >= 20 && G < 24) p_cvt<0, G - 20>();
  if constexpr (G == 25) p_store<0, HF>();
  if constexpr (LD && p_dma(HF, G) >= 0) dma_at<(HF ? 8 : 12) + p_dma(HF, G)>();
  if constexpr (LD && HF == 0 && G == 2) asm volatile("s_bitset0_b32 m0, 12");
  if constexpr (HF == 0 && G == 13) asm volatile("s_add_u32 s96, s96, 0x8000");
  if constexpr (HF == 0 && G == 26) asm volatile("s_add_u32 s98, s98, 128");
  if constexpr (LD && HF == 0 && G == 27) asm volatile("s_cmp_lt_u32 s98, 0x380");
  if constexpr (HF == 1 && G == 1) asm volatile("s_xor_b32 s100, s100, 0x10000");
  if constexpr (LD && HF == 1 && G == 2) asm volatile("s_xor_b32 s97, s97, s100");
  if constexpr (LD && HF == 1 && G == 9) asm volatile("s_or_b32 m0, s97, 0x1000");
  if constexpr (!LD && HF == 1 && G == 30) asm volatile("s_cmp_lt_u32 s98, 0x500");
  GAP();
}
template <int HF, bool LD, int... Gs> DI void p_half(iseq<Gs...>) { (p_gap<HF, LD, Gs>(), ...); }
template <int G> DI void gapF(AC& c, const EP& p) {
  if constexpr (G < 64) gapB<0, false, G>(c);
  if constexpr (G >= 10) epi_pair<(G - 10) / 4, (G - 10) & 3, 0>(p);
  if constexpr (G >= 14 && ((G - 14) & 3) == 0) epi_store<(G - 14) / 4>();
  if constexpr (G == 73) epi_store<15>();
  GAP();
}
template <int... Gs> DI void phaseF(AC& c, const EP& p, iseq<Gs...>) { (gapF<Gs>(c, p), ...); }
template <int G> DI void gapAL(AC& c) {
  constexpr int kb = G >> 3, s = G & 7, r = 192 + 4 * (G & 15), q = 160 + 4 * s, d = R_S + 36 + 8 * kb, ci = s == 0 ? R_CI + 4 + 8 * kb : d;
  if constexpr (G == 0) asm volatile("s_add_u32 s96, s97, 0x1000000\n\ts_mov_b32 m0, s79\n\ts_waitcnt vmcnt(9) lgkmcnt(0)\n\ts_barrier" ::: "memory");
  if constexpr (G >= 8 && (G & 3) == 0) asm volatile("s_waitcnt lgkmcnt(4)");
  if constexpr (G == 29) asm volatile("s_barrier" ::: "memory");
  asm volatile("v_mfma_f32_16x16x32_bf16 v[%c0:%c1], a[%c2:%c3], a[%c4:%c5], v[%c6:%c7]" ::"i"(d), "i"(d + 3), "i"(r), "i"(r + 3), "i"(q), "i"(q + 3),
               "i"(ci), "i"(ci + 3));
  if constexpr (G < 24) kread<1, G + 8>();
  else vread<0, G - 24>();
  if constexpr (G % 3 == 1 && G < 24) dma_at<G / 3>();
  if constexpr (G == 11) asm volatile("s_bitset1_b32 m0, 12");
  if constexpr (G == 28) asm volatile("buffer_load_dwordx4 v[122:125], v88, s[88:91], s76 offen sc1" ::: "memory");
  if constexpr (G == 30) asm volatile("s_xor_b32 m0, s79, 0x11000\n\ts_movk_i32 s98, 128");
  sm_gap<0, 64 + 2 * G>(c); sm_gap<0, 65 + 2 * G>(c);
  GAP();
}
template <int... Gs> DI void phaseAL(AC& c, iseq<Gs...>) { (gapAL<Gs>(c), ...); }
struct BLS { int ex[65], op[65], vm[65], su[65]; };
constexpr BLS bl_sched(bool LD) {
  BLS z{};
  z.op[0] = 1;
  for (int t = 0; t < 64; ++t) {
    const int g = t & 15, o = z.op[t];
    const bool x = t == 6 || (t < 48 && g > 8 && g != 12 && g != 15);
    const bool v = LD ? (t & 3) == 0 : (t & 11) == 8;
    const bool p = t >= 15 && o < 24 && (t & 3) != 0 && t != 47 && !x && z.ex[t - 1] > 2 * (o / 3) + (o % 3 != 0);
    z.ex[t + 1] = z.ex[t] + x; z.vm[t + 1] = z.vm[t] + v; z.op[t + 1] = o + p;
    z.su[t + 1] = z.su[t] + (t >= 1 && z.su[t] < (LD ? 16 : 10) && g != 15 && !v && !x && (LD || t > 55 || (t > 3 && !p)));
  }
  return z;
}
template <bool LD> constexpr BLS BL = bl_sched(LD);
static_assert(BL<true>.ex[64] == 16 && BL<true>.op[61] == 24 && BL<true>.vm[64] == 16 && BL<false>.vm[64] == 8);
static_assert(BL<true>.su[56] == 16 && BL<false>.su[62] == 10);
template <bool LD, int G> DI void gapBL(AC& c) {
  if constexpr (G == 48) asm volatile("s_waitcnt vmcnt(%0) lgkmcnt(0)\n\ts_barrier" ::"i"(BL<LD>.vm[48]) : "memory");
  gapB<1, false, G>(c);
  if constexpr (G >= 48 && G < 56) vread<1, G - 48>();
  if constexpr (BL<LD>.vm[G + 1] > BL<LD>.vm[G]) {
    constexpr int k = BL<LD>.vm[G] + (LD ? 0 : 16), I = 2 * (k & 7) + ((k >> 3) & 1);
    if constexpr (k < 16) {
      asm volatile("buffer_load_dwordx4 a[%c0:%c1], v84, s[88:91], s75 offen offset:%2 sc1" ::"i"(128 + 4 * I), "i"(131 + 4 * I), "i"((I & 3) * 1024) : "memory");
      if constexpr (k & 1) asm volatile("s_addk_i32 s75, %0" ::"i"(k == 7 ? -12288 : 4096));
    } else dma_at<k - 8>();
    if constexpr (k == 19) asm volatile("s_bitset0_b32 m0, 12");
  }
  if constexpr (G == 1 || G == 2) asm volatile("v_add_f32_e32 v%c0, v%c0, v%c1" ::"i"(R_RS + G - 1), "i"(R_TS + G - 1));
  if constexpr (G == 3) asm volatile("v_mov_b32 v%c0, 0" ::"i"(R_TS));
  if constexpr (BL<LD>.ex[G + 1] > BL<LD>.ex[G]) {
    constexpr int j = BL<LD>.ex[G];
    asm volatile("v_exp_f32 v%c0, v%c1" ::"i"(R_E + j), "i"(R_S + 36 + 8 * (j >> 2) + (j & 3)));
  }
  if constexpr (BL<LD>.op[G + 1] > BL<LD>.op[G]) {
    constexpr int o = BL<LD>.op[G], k = o / 3, j = 2 * k + o % 3;
    if constexpr (o % 3 == 2) asm volatile("v_cvt_pk_bf16_f32 v%c0, v%c1, v%c2" ::"i"(p_reg(1, (k >> 1) * 4 + 2 + (k & 1))), "i"(R_E + 2 * k), "i"(R_E + 2 * k + 1));
    else asm volatile("v_add_f32_e32 v%c0, v%c1, v%c2" ::"i"(R_TS + 1), "i"(j == 1 ? R_E : R_TS + 1), "i"(R_E + j));
  }
  if constexpr (G == 61) asm volatile("v_cmp_nle_f32_e64 s[54:55], v%c0, v%c1" ::"i"(R_TS + 1), "i"(R_THR));
  if constexpr (BL<LD>.su[G + 1] > BL<LD>.su[G]) {
    constexpr int k = BL<LD>.su[G];
    if constexpr (k < 4) asm volatile("v_xor_b32 v%c0, %1, %2" ::"i"(R_TB + k), "i"(32 * k), "v"(c.tb));
    else if constexpr (k < 6) asm volatile("v_add_u32 v%c0, %1, %2" ::"i"(R_VS + k - 4), "i"(4096 * (k - 4)), "v"(c.vs));
    else if constexpr (k == 6) tload<96, T_SLAB>();
    else if constexpr (k == 7) tload<101, T_CNT>();
    else if constexpr (k == 8) asm volatile("v_mov_b64 v[240:241], 1");
    else if constexpr (k == 9) asm volatile("s_mov_b32 s97, s79\n\ts_mov_b32 s100, 0x8000");
    else if constexpr (k < 14) asm volatile("v_mov_b32 v%c0, %1" ::"i"(k < 12 ? 214 + k : 218 + k), "v"(k & 1 ? c.o1 : c.o0));
    else asm volatile("v_mov_b64 v[%c0:%c1], 0" ::"i"(198 + 2 * k), "i"(199 + 2 * k));
  }
  GAP();
}
template <bool LD, int... Gs> DI void phaseBL(AC& c, iseq<Gs...>) { (gapBL<LD, Gs>(c), ...); }
template <int MODE, int G> DI void gapFL(const EP& p) {
  constexpr int x = G - (MODE == 3 ? 4 : 0), J = MODE == 1 ? (G < 24 && G % 3 == 1 ? G / 3 : -1) : G < 27 && G % 5 != 0 && G % 5 != 3 ? G / 5 * 3 + (G % 5) / 2 : -1;
  if constexpr (G == 0 && MODE == 1) asm volatile("s_or_b32 m0, s78, 0x1000\n\ts_movk_i32 s98, 256\n\ts_waitcnt lgkmcnt(0)");
  if constexpr (G == 0 && MODE > 1) asm volatile("s_waitcnt vmcnt(8) lgkmcnt(0)" ::: "memory");
  if constexpr (G == 16 && MODE > 1) asm volatile("s_waitcnt vmcnt(9)" ::: "memory");
  if constexpr (G >= 8 && G < 32 && (G & 3) == 0) asm volatile("s_waitcnt lgkmcnt(%0)" ::"i"(G < 28 ? 4 : 0));
  if constexpr (G < 32) {
    constexpr int o = 8 * (G >> 1) + 4, r = 192 + 4 * (G & 15), q = R_P + 20 + 8 * (G & 1);
    asm volatile("v_mfma_f32_16x16x32_bf16 a[%c0:%c1], a[%c2:%c3], v[%c4:%c5], a[%c0:%c1]" ::"i"(o), "i"(o + 3), "i"(r), "i"(r + 3), "i"(q), "i"(q + 3));
  }
  if constexpr (G < 24) vread<1, G + 8>();
  if constexpr (G == 28) asm volatile("s_waitcnt vmcnt(8)\n\ts_barrier\n\ts_or_b32 m0, s79, 0x1000\n\ts_movk_i32 s98, 384" ::: "memory");
  if constexpr (G == 29 || G == 31) dma_at<8 + (G - 29) / 2>();
  if constexpr (G == 32 || G == 40) {
    static_for([&](auto I) { p_read<0, 2 * (G - 32) + I>(); }, make_iseq<16>{});
    dma_at<10 + (G - 32) / 8>();
  }
  if constexpr (J >= 0) dma_at<8 + (J & 7)>();
  if constexpr (x >= 0) epi_pair<2 * ((x >> 2) & 7) + (x >> 5), x & 3, MODE>(p);
  if constexpr (MODE == 3 && G < 32) {
    constexpr int db = G & 15, qb = G >> 4, o = 4 * (db * 2 + qb), a = 224 + 4 * (db & 1), b = 128 + 4 * ((db >> 1) * 2 + qb);
    asm volatile("v_mfma_f32_16x16x32_bf16 a[%c0:%c1], v[%c2:%c3], a[%c4:%c5], a[%c0:%c1]" ::"i"(o), "i"(o + 3), "i"(a), "i"(a + 3), "i"(b), "i"(b + 3));
  }
  if constexpr (J == 3 || J == 11) asm volatile("s_bitset0_b32 m0, 12");
  if constexpr (J == 7 && MODE > 1) asm volatile("s_or_b32 m0, s78, 0x1000\n\ts_movk_i32 s98, 256");
  if constexpr (x == 63) asm volatile("s_waitcnt vmcnt(4) lgkmcnt(0)" ::: "memory");
  GAP();
}
template <int MODE, int... Gs> DI void phaseFL_(const EP& p, iseq<Gs...>) { (gapFL<MODE, Gs>(p), ...); }
template <int MODE> DI void phaseFL(const EP& p) { GAP(); phaseFL_<MODE>(p, make_iseq<MODE == 3 ? 68 : 64>{}); }
template <int U, int K> DI void r_load(unsigned vo, unsigned so) {
  asm volatile("buffer_load_dwordx4 a[%c0:%c1], %2, s[88:91], %3 offen offset:%4 sc1" ::"i"(64 * U + 4 * K), "i"(64 * U + 4 * K + 3), "v"(vo),
               "s"(so + (K >> 1) * 0x40000 + (U >> 1) * 4096), "i"((K & 1) * 512 + (U & 1) * 2048) : "memory");
}
template <int U, int K> DI void r_sum() {
  constexpr int d = 128 + 8 * U + 4 * (K & 1), b = 64 * U + 4 * K;
  if constexpr (K < 2) asm volatile("v_mfma_f32_16x16x32_bf16 v[%c0:%c1], v[96:99], a[%c2:%c3], 0" ::"i"(d), "i"(d + 3), "i"(b), "i"(b + 3));
  else asm volatile("v_mfma_f32_16x16x32_bf16 v[%c0:%c1], v[96:99], a[%c2:%c3], v[%c0:%c1]" ::"i"(d), "i"(d + 3), "i"(b), "i"(b + 3));
}
template <int U> DI void r_sums() { static_for([&](auto K) { r_sum<U, K>(); }, make_iseq<16>{}); }
template <int U> DI void r_fin(unsigned vo, __amdgpu_buffer_rsrc_t srd, unsigned so) {
  static_for([&](auto E) { asm volatile("v_cvt_pk_bf16_f32 v%c0, v%c1, v%c2" ::"i"(128 + 8 * U + E), "i"(128 + 8 * U + 2 * E), "i"(129 + 8 * U + 2 * E)); }, make_iseq<4>{});
  asm volatile("s_nop 0\n\tbuffer_store_dwordx4 v[%c3:%c4], %0, %1, %2 offen sc0 sc1" ::"v"(vo), "s"(srd), "s"(so), "i"(128 + 8 * U), "i"(131 + 8 * U) : "memory");
}

#define CMAX 12
#define BAL 0
#define NYM 0
#define NYE (BAL ? 16 : 15)
#define NYO (BAL ? 14 : 15)
#define NY0 15
struct TRow { unsigned v[64]; };
constexpr u64 kvmask(int m0, int m1) { u64 n = 0; for (int m = m0; m <= m1; ++m) n |= 0x101ull << m; return n; }
constexpr u64 k_order(bool isY, int rr) {
  const int cls = (rr + (rr >> 2)) & 3;
#define NCM 3
  u64 t = 0;
  int n = 0;
  auto put = [&](int k) { t |= (u64)k << (4 * n++); };
  if (isY) {
    for (int k = 1; k <= 4; ++k) put(NCM + 4 + (k & 3));
    for (int k = 1; k <= 4; ++k) put(NCM + 8 + (k & 3));
    for (int k = NCM + 12; k < 16; ++k) put(k);
    for (int k = NCM + 3; k >= 0; --k) put(k);
    return t;
  }
  for (int k = 0; k < NCM; ++k) put(k);
  for (int k = 0; k < 3; ++k) { put(NCM + 4 * k + cls); put(NCM + ((cls + k + 1) & 3)); }
  for (int k = 1; k < 4; ++k) put(NCM + 4 + ((cls + k) & 3));
  for (int k = 1; k < 4; ++k) put(NCM + 8 + ((cls + k) & 3));
  for (int k = NCM + 12; k < 16; ++k) put(NCM == 2 ? k ^ (cls & 1) : k);
  return t;
}
static_assert(NCM >= 3);
constexpr TRow t_row(int b, int wid) {
  TRow t{};
  const int j = b & 7, r = b >> 3, rr = r & 15, hsel = rr & 1, pj = rr >> 1, kv = pj & 1, h = j + 8 * hsel, u = hsel * 8 + j;
  const bool isY = r >= 16, noA = pj == 7, slow = BAL && (j & 1);
  const int pa = slow && (pj == 0 || pj == 7) ? 7 - pj : pj;
  const bool solo = pa == 7;
  const int jF = isY || !BAL ? j : slow && pa == 0 ? j - 1 : !slow && pa == 6 ? j + 1 : j, hF = jF + 8 * hsel;
  const int qbF = isY ? (solo ? (slow ? 6 : 7) : 15 - pa) : solo ? 8 : !BAL ? pa : slow ? (pa ? pa - 1 : 0) : pa + 1;
  const int mt = (rr >> 2) + (isY ? 4 : 0), g = j * 128, gF = jF * 128, NY = (!(j & 1) ? NYE : pa ? NYO : NY0) - ((NYM >> pa) & 1);
  const int nx0 = solo ? 0 : 32 - 2 * pa - NY, cx = nx0 < CMAX ? nx0 : CMAX, ex = nx0 - cx, qb0 = solo ? qbF : 15 - pa;
  const unsigned oQ = (unsigned)h << 20, oK = oQ + O_K, oP = O_P + (h * 8 + pa) * PSTR, oQF = (unsigned)hF << 20;
  if (wid == 4) {
    for (int l = 0; l < 64; ++l)
      t.v[l] = fo(l < 16 ? g + hsel * 16 + l : l == 16 ? g + 32 + hsel * 8 + (qb0 >> 1) : l == 17 ? gF + 32 + hsel * 8 + (qbF >> 1) :
                  l < 26 ? gF + hsel * 16 + ((l - 18) >> 2) * 8 + ((l - 18) & 3) : l == 26 ? g + 48 + hsel * 8 + pa : l < 32 ? g + 64 + hsel * 8 + pa :
                  l < 48 ? 2304 + pj * 16 + l - 32 : 2048 + pj * 16 + l - 48);
    return t;
  }
  auto put64 = [&](int i, u64 x) { t.v[i] = (unsigned)x; t.v[i + 1] = (unsigned)(x >> 32); };
  t.v[T_Q0] = oQ + (qb0 * 128 + wid * 16) * 512;
  t.v[T_K0] = oK + (isY ? cx : 0) * 32768;
  t.v[T_OQ] = oQ + pj * 131072 + (wid >> 1) * 65536 + (wid & 1) * 4096;
  t.v[T_OG] = oK + kv * TSB + mt * 131072 + (wid >> 1) * 65536 + (wid & 1) * (kv ? 8192 : 4096);
  t.v[T_FPREP] = fo(2048 + pj * 16 + u);
  t.v[T_FQ] = fo(g + 32 + hsel * 8 + pj);
  t.v[T_FG] = fo(g + hsel * 16 + kv * 8 + mt);
  t.v[T_FS] = fo(1024 + qbF * 16 + jF * 2 + hsel);
  t.v[T_CNT] = fo(2560 + qbF * 16);
  t.v[T_ZO] = O_Z + pj * 64 * SEQ * 2 + (u * 4 + wid) * 4096;
  const u64 own = 0x1ull << (kv * 8 + mt), conv = noA ? 0 : 0xffffull << 32;
  put64(T_NPRE, 0x10000ull | (isY ? (1ull << (cx >> 2)) | (1ull << ((cx + 1) >> 2)) : noA ? 0 : 0xffffull << 48));
  put64(T_NST, isY ? kvmask(cx >> 2, 3) | (solo ? 0 : 1ull << 27) : kvmask(0, 0));
  put64(T_TAB, k_order(isY, rr));
  t.v[T_SLAB] = O_S + qbF * 0x200000 + jF * 0x40000 + wid * 8192 + hsel * 1024;
  t.v[T_PART] = oP;
  const unsigned junk = fo(g + 96 + r), fconv = noA ? junk : fo(2304 + pj * 16 + u);
  for (int it = isY || solo; it < 2; ++it) {
    unsigned* p = t.v + T_IT + it * T_ITN;
    const int n = !it ? nx0 : isY && !solo ? NY : 2 * qbF + 2;
    u64 q[4] = {0, 0, 0, 0};
    unsigned jmp[3] = {0, 0, 0}, chk = 0;
    p[14] = junk;
    if (isY) {
      p[0] = 2; p[1] = 2; p[13] = t.v[T_FG];
      if (!solo) {
        chk = 1u << (14 - cx); p[0] |= chk; q[1] = kvmask(4, (31 - 2 * pa) >> 2) & ~own; jmp[1] = ex * 32768;
        chk |= 1u << (n - 2); q[2] = 1ull << 26;
      }
    } else if (it == 0) {
      chk = p[0] = 2u | (ex ? 1u << (cx - 2) : 0) | 1u << (n - 2);
      chk |= noA || qbF > 1 ? 0 : 1u << n;
      q[0] = kvmask(0, (cx - 1) >> 2);
      if (ex) { q[1] = kvmask(4, 4 + ((ex - 1) >> 2)); jmp[1] = (16 - cx) * 32768; }
      jmp[ex ? 2 : 1] = oQF - oQ - (ex ? 16 + ex : cx) * 32768;
      q[ex ? 2 : 1] = 0x20000ull | ((0x22ull << ((2 * qbF + 1) >> 2)) - 0x11ull) << 18;
      q[ex ? 3 : 2] = conv;
      p[1] = 2; p[13] = fconv; p[14] = fo(g + 64 + hsel * 8 + pa);
    } else if (solo) {
      chk = p[0] = 0x402; p[1] = 2; p[13] = fconv;
      q[0] = kvmask(0, 2); q[1] = kvmask(3, 4) | conv;
    } else {
      p[0] = p[1] = n > 4 ? 2 : 1; p[13] = fo(g + 48 + hsel * 8 + pa);
      chk = noA ? 0 : p[0];
      q[0] = conv;
    }
    p[2] = jmp[0]; p[4] = jmp[1]; p[15] = jmp[2];
    p[3] = !it ? 0 : n > 2 ? 1u << (n - 3) : 1u << 30;
    for (int k = 0; k < 4; ++k) { p[5 + 2 * k] = (unsigned)q[k]; p[6 + 2 * k] = (unsigned)(q[k] >> 32); }
    const int rk = (r >> 1) - (BAL && (r >> 1) > (slow ? 7 : 6));
    p[16] = 32 | (isY && !solo ? 2 : 0) | (n == 2 ? 4 : 0) | (isY || noA ? 0 : 8) | (n & 1) << 4 | (n - 1 - it) << 8 | (rk * 4 + wid) << 16 | hF << 24 | qbF << 28;
    p[17] = !it ? 0 : n > 2 ? 1u << (n - 2) : 1u << 31;
    p[18] = it ? oP + wid * 16384 : oQF + (qbF * 128 + wid * 16) * 512;
    p[19] = oP + (it ? 1032 : 1024);
    p[20] = chk >> 1;
    p[21] = (it ? oQF + O_K : oK) + (isY ? cx : 0) * 32768;
    p[22] = ((p[0] | p[3] | p[17]) & 0x3fffffffu) << 1;
  }
  return t;
}
struct TBlk { TRow row[5]; };
template <int B> constexpr TBlk t_blk = {{t_row(B, 0), t_row(B, 1), t_row(B, 2), t_row(B, 3), t_row(B, 4)}};
struct Tab { TBlk blk[256]; };
template <int... I> constexpr Tab t_make(iseq<I...>) { return Tab{{t_blk<I>...}}; }
__device__ const Tab g_tab = t_make(make_iseq<256>{});

template <int R> DI const bf16* sptr() { const bf16* p; asm volatile("s_mov_b64 %0, s[%c1:%c2]" : "=s"(p) : "i"(R), "i"(R + 1)); return p; }
template <class T, int I> DI T* arg_late() {
  unsigned lo, hi;
  asm volatile("v_readlane_b32 %0, v77, %2\n\tv_readlane_b32 %1, v77, %3\n\ts_nop 4" : "=s"(lo), "=s"(hi) : "i"(2 * I), "i"(2 * I + 1));
  return (T*)(((u64)hi << 32) | lo);
}
__global__ __launch_bounds__(256, 1) __attribute__((amdgpu_num_vgpr(77), amdgpu_num_sgpr(56))) void fused_kernel(
    const bf16* ckv_, const bf16* wuk_, const bf16* wuv_, const bf16* x_, const bf16* wq_, bf16* ws_, const bf16* ak_, const bf16* av_,
    const bf16* ash_, const bf16* assmk_, const bf16* assmD_, const bf16* wo_, const bf16* aq_, bf16* out_) {
  extern __shared__ __attribute__((aligned(16))) char lds[];
  const void* kap = (const void*)__builtin_amdgcn_kernarg_segment_ptr();
  asm volatile("at_code_begin:\n\ts_load_dwordx16 s[80:95], %0, 0\n\ts_load_dwordx2 s[96:97], %0, 0x40" ::"s"(kap) : ACLOB_ALL, "v255", "s101");
  asm volatile("s_getpc_b64 s[48:49]\nct_base:\n\ts_mov_b32 s50, pf_code_end-ct_base\n\ts_mov_b32 s51, 0x20000\n\ts_lshr_b32 s52, %1, 3\n\t"
               "v_and_b32 v116, 63, %0\n\tv_lshrrev_b32 v117, 6, %0\n\tv_lshl_add_u32 v116, v116, 7, v117\n\ts_lshl_b32 s52, s52, 9\n\t"
               "v_lshlrev_b32 v116, 7, v116\n\tbuffer_load_dword v117, v116, s[48:51], s52 offen" ::"v"(threadIdx.x), "s"(blockIdx.x) : "scc", "memory");
  GAP();
  const int tid = threadIdx.x, lane = tid & 63;
  const int wid = __builtin_amdgcn_readfirstlane(tid >> 6);
  const int b = blockIdx.x, j = b & 7, r = b >> 3, rr = r & 15, hsel = rr & 1, pj = rr >> 1, kv = pj & 1;
  const bool isY = r >= 16, doP = isY && pj != 7, doA = !isY && pj != 7;
  const int h = j + 8 * hsel, mt = (rr >> 2) + (isY ? 4 : 0), pu = (hsel * 8 + j) * 4 + wid;
  constexpr unsigned m0d = 131072 + 24576;
  const unsigned lA = isY ? 65536 : 0, lW = lA ^ 65536, po = isY ? 0 : 32768;
  unsigned mA = lA + wid * 1024 + po, mW = lW + wid * 1024 + po, ofA = isY ? pj * 256 * DA * 2 : mt * 256 * DC * 2, ofW = h * 512;
  const unsigned lda = isY ? DA : DC;
#define ROT 1
  unsigned g0 = (!ROT ? 0 : isY ? pj + 2 * hsel : rr + (rr >> 2)) & 3;
  {
    asm volatile("global_load_dword v78, %0, %2\n\tglobal_load_dword v79, %1, %2 offset:1024\n\tglobal_load_dword v77, %3, %4"
                 ::"v"(tid * 4), "v"(lane * 4), "s"(&g_tab.blk[b]), "v"((lane < 27 ? lane : 27) * 4), "s"(kap) : "memory");
    const unsigned ra = wid * 8 + (lane >> 3), sa = ((lane & 7) ^ (((wid & 1) * 4 + (lane >> 4)) & 7)) * 16;
    const unsigned vbA = ra * (lda * 2) + sa, vbB = (4 * wid + (lane >> 4)) * LDW * 2 + 16 * ((lane & 15) ^ (((lane >> 4) << 2) | wid));
    vsetu<92>(vbA); vsetu<93>(vbB); vsetu<94>(ra * (DC * 2) + sa);
    sset<65>(128 * lda);
    asm volatile("s_mov_b32 s74, 0x40000\n\ts_mov_b32 s53, 0x80000");
    sset<58>(256 * lda * 2); sset<62>((lda - 1) * LDW * 2 + 512);
    asm volatile("s_mov_b32 s59, 0x20000\n\ts_mov_b32 s63, 0x20000");
    g_pro<1>(g0, mA, mW, lda);
    asm volatile("" : "+s"(ofA), "+s"(ofW), "+s"(mA), "+s"(mW), "+s"(g0));
    GAP();
  }
  asm volatile("s_bitcmp1_b32 %0, 4\n\ts_waitcnt lgkmcnt(0)\n\ts_cselect_b64 s[98:99], s[84:85], s[82:83]\n\ts_bitcmp1_b32 %0, 7\n\t"
               "s_cselect_b64 s[60:61], s[88:89], s[98:99]\n\ts_cselect_b64 s[56:57], s[86:87], s[80:81]\n\t"
               "s_add_u32 s56, s56, %1\n\ts_addc_u32 s57, s57, 0\n\ts_add_u32 s60, s60, %2\n\ts_addc_u32 s61, s61, 0\n\t"
               "s_mov_b64 s[88:89], s[90:91]\n\ts_mov_b32 s90, %3\n\ts_mov_b32 s91, 0x20000" ::"s"(b), "s"(ofA), "s"(ofW), "i"(WSB) : "scc");
  KST(0, 0);
  AC c;
  {
    GM g;
    PR pr;
    const int ptb = hsel * 8 + j;
    if (doP) prep_load(pr, tid, pj, ptb, sptr<92>(), sptr<94>(), sptr<96>());
    g_pro<2>(g0, mA, mW, lda);
    g_pro(g0 + 1, mA, mW, lda);
    sset<64>(64 * lda); sset<67>(256 * lda); sset<68>(320 * lda);
    asm volatile("s_mov_b32 s71, 0x20000\n\ts_movk_i32 s72, 0x100\n\ts_mov_b32 s73, 0x20100");
    asm volatile("buffer_load_dwordx2 v[120:121], off, s[88:91], %0 sc1" ::"s"(fo(6144)) : "memory");
    GAP();
    {
      const int fr = lane & 15, fq = lane >> 4, q = (lane & 15) >> 2, p = lane & 3;
      vsetu<128>(lA + (wid >> 1) * 16384 + (fr >> 3) * 1024 + (fr & 7) * 128 + ((fq ^ (fr >> 1)) * 16));
      vsetu<129>(lW + (wid & 1) * 8192 + off_b(8 * fq + q, p >> 1) + 8 * (p & 1));
      vsetu<130>(lW + (wid & 1) * 8192 + off_b(8 * fq + 4 + q, p >> 1) + 8 * (p & 1));
      const unsigned one = fq != q ? 0u : (p & 1) ? 0x3f800000u : 0x3f80u;
      vsetu<131>((p & 2) ? 0u : one); vsetu<132>((p & 2) ? one : 0u);
      vsetu<133>((lane >> 2) * 128 + 16 * ((p >> 1) ^ ((lane >> 2) & 6)) + 8 * (p & 1));
      vsetu<134>((fr >> 3) * 2048 + (fq & 1) * 512 + (fq >> 1) * 256 + (fr & 7) * 16);
      vsetu<81>(tid == 0 ? 0u : 0x80000000u);
      vsetu<84>(lane * 16); vsetu<85>(lane * 16 + 65536); vsetu<86>(wid * 8192 + lane * 16); vsetu<87>(wid * 8192 + lane * 16 + 4096);
      vsetu<88>(65536 + wid * 256 + fr * 16); vsetu<89>(16 * wid + fr - 4 * fq);
      asm volatile("s_mov_b64 s[92:93], s[88:89]\n\ts_mov_b64 s[94:95], s[90:91]\n\ts_mov_b32 s75, 0x60000\n\t"
                   "s_mov_b32 s76, 0x40100\n\ts_mov_b32 s77, 0x60100\n\ts_mov_b32 s82, 2");
      asm volatile("s_add_u32 s86, s80, %0\n\ts_addc_u32 s87, s81, 0\n\ts_add_u32 s98, s98, %1\n\ts_addc_u32 s99, s99, 0"
                   ::"s"(mt * 256 * DC * 2), "s"(ofW) : "scc");
      sset<66>(192 * lda); sset<69>(384 * lda); sset<70>(448 * lda);
      sset<78>(lA + wid * 1024); sset<79>(lW + wid * 1024);
    }
    GAP();
    if (doP) {
      asm volatile("s_waitcnt vmcnt(11)" ::: "memory");
#pragma unroll
      for (int q = 0; q < 7; ++q) asm volatile("" : "+v"(pr.kk[q]));
#pragma unroll
      for (int q = 0; q < 4; ++q) asm volatile("" : "+v"(pr.vv[q]));
      static_for([&](auto Ic) __attribute__((always_inline)) {
        constexpr int I = Ic;
        if constexpr (I < 2) g_pro(g0 + 2 + I, mA, mW, lda);
        if constexpr (I == 2) g_pro2(mA, mW);
        GAP();
        prep_math<I>(pr, tid, lds + 131072);
        GAP();
      }, make_iseq<4>{});
    } else { g_pro(g0 + 2, mA, mW, lda); g_pro(g0 + 3, mA, mW, lda); g_pro2(mA, mW); }
    GAP();
    g.aA[0] = vxor<128, 0>(); g.aA[1] = vxor<128, 64>();
    static_for([&](auto I) { g.aB[I >> 3][I & 7] = vxor<129 + (I >> 3), 32 * (I & 7)>(); }, make_iseq<16>{});
    c.o0 = vxor<131, 0>(); c.o1 = vxor<132, 0>(); c.tb = vxor<133, 0>(); c.vs = vxor<134, 0>();
    asm volatile("v_add_u32 v91, 0x100000, v93\n\ts_waitcnt vmcnt(8) lgkmcnt(0)\n\ts_barrier" ::: "memory");
    if (isY) g_read0<0>(g, make_iseq<16>{});
    else g_read0<1>(g, make_iseq<16>{});
    GAP();
    asm volatile("v_readfirstlane_b32 s48, v120\n\tv_readfirstlane_b32 s49, v121\n\tv_readlane_b32 s80, v78, %0\n\tv_readlane_b32 s81, v78, %1\n\t"
                 "s_mov_b32 s50, 0x4c957f2d\n\ts_mul_i32 s84, s48, s50\n\ts_mul_hi_u32 s85, s48, s50\n\ts_mul_i32 s49, s49, s50\n\t"
                 "s_mul_i32 s48, s48, 0x5851f42d\n\ts_add_u32 s85, s85, s49\n\ts_add_u32 s85, s85, s48\n\ts_add_u32 s84, s84, 0xf767814f\n\t"
                 "s_addc_u32 s85, s85, 0x14057b7e\n\ts_lshr_b64 s[80:81], s[80:81], 12\n\tv_mov_b32 v82, s84\n\tv_mov_b32 v83, s85"
                 ::"i"((int)T_TAB), "i"(T_TAB + 1) : "scc");
    if (isY) asm volatile("s_mov_b64 s[80:81], 0x6543");
    if (doP) prep_store(tid, pj, ptb, (bf16*)sptr<88>() + O_X / 2, lds + 131072);
    asm volatile("s_waitcnt lgkmcnt(0)" ::: "memory");
    GAP();
    if (isY) {
      g_tile<false, 0, 1>(g);
      g_loop<false, true>(g, [&]() __attribute__((always_inline)) {
        tload<80, T_TAB>(); tload<81, T_TAB + 1>();
        fl_set<T_FPREP>();
        asm volatile("s_mov_b64 s[60:61], s[98:99]\n\ts_mov_b32 s62, %0" ::"i"((DC - 1) * LDW * 2 + 512));
      }, [&]() __attribute__((always_inline)) {
        asm volatile("s_mov_b64 s[56:57], s[86:87]\n\ts_mov_b32 s58, %0\n\tv_mov_b32 v92, v94" ::"i"(256 * DC * 2));
        static_for([&](auto J) { asm volatile("s_mov_b32 s%c0, %1" ::"i"(64 + J), "i"((J + 1) * 64 * DC)); }, make_iseq<7>{});
      });
      tload<101, T_OQ>(); sset<83>(__float_as_uint(CS));
      asm volatile("v_mov_b32 v112, v84\n\tv_add_u32 v113, 8192, v84");
      GAP();
      g_tile<false, 0, 2>(g);
      KST(0, 3);
    }
    auto kvtile = [&](auto TRc) __attribute__((always_inline)) {
      constexpr bool TR = TRc;
      if (isY) g_tile<TR, 1, 4>(g);
      else g_tile<TR, 1, 1>(g);
      asm volatile("s_mov_b32 s83, s78");
      g_loop<TR, false>(g, [&]() __attribute__((always_inline)) {
        asm volatile("v_readlane_b32 s49, v78, %1\n\tv_readlane_b32 s48, v78, %0\n\tv_and_b32 v122, 0xffffff00, v82\n\tv_mov_b32 v123, v83\n\t"
                     "s_and_b32 s49, s49, 0xf000000\n\ts_cselect_b32 s49, -2.0, 0\n\tv_or_b32 v121, s49, v81\n\ts_nop 0\n\t"
                     "buffer_store_dwordx2 v[122:123], v121, s[88:91], s48 offen sc1" ::"i"((int)T_CNT), "i"(T_IT + T_ITN + 16) : "memory", "scc");
        if (isY) fl_set<T_FQ>();
        else {
          int pv = pu; asm volatile("" : "+s"(pv));
          if (doA) post_dma(pj, pv, arg_late<const bf16, 12>(), 131072 + wid * 8192);
          else if (!(BAL && (j & 1))) {
            const char* tp = arg_late<const char, 0>() + 1024 * DC * 2 + pv * 32768 + __float_as_uint(vget<84>()) * 8;
            asm volatile("s_mov_b32 m0, %0\n\ts_nop 0" ::"i"(m0d));
#pragma unroll
            for (int i = 0; i < (BAL ? 8 : 4); ++i) asm volatile("global_load_lds_dword %0, off" ::"v"(tp + (i & 3) * 8192 + (i >> 2) * 131072) : "memory");
          }
        }
      }, [&]() __attribute__((always_inline)) {
        asm volatile("s_mov_b32 s79, 0x28000\n\ts_mov_b32 s83, s79");
        fl_poll<T_NPRE>();
        if (!isY) {
          int pv = pu; asm volatile("" : "+s"(pv));
          if (doA) {
            const char* tp = arg_late<const char, 9>() + (size_t)(pj * 64 + pv) * (SEQ * 2) + __float_as_uint(vget<84>()) * 4;
            asm volatile("s_mov_b32 m0, %1\n\ts_nop 0\n\tglobal_load_lds_dword %0, off" ::"v"(tp), "i"(m0d) : "memory");
          } else {
            CODE_TOUCH("at_code_begin", "pf_code_end", (pv >> 5) * 8 + (pv & 3) * 2, 16u, 64, m0d);
            CODE_TOUCH("at_code_begin", "pf_code_end", (pv >> 5) * 8 + (pv & 3) * 2 + 1, 16u, 64, m0d);
          }
        }
      });
      tload<101, T_OG>(); tload<96, T_Q0>(); tload<98, T_K0>();
      fl_check();
      tload<50, T_NST>(); tload<51, T_NST + 1>();
      asm volatile("v_mov_b32 v112, v84\n\tv_add_u32 v113, %0, v84\n\tv_mov_b32 v90, v87\n\tv_mov_b32 v91, v87\n\tv_mov_b32 v92, v87\n\tv_mov_b32 v93, v87\n\t"
                   "v_mov_b32 v94, v86\n\tv_mov_b32 v95, v86\n\tv_mov_b32 v126, v86\n\tv_mov_b32 v127, v86\n\ts_mov_b32 s99, 0x8000\n\t" FL_ARM("s[50:51]")
                   ::"i"(TR ? 4096 : 8192));
      if (doA) {
        const int d = pj * 64 + pu;
        const bf16* dp = arg_late<const bf16, 10>() + d;
        asm volatile("global_load_ushort v123, %0, off" ::"v"(dp) : "memory");
        srd_set<56>(arg_late<const bf16, 9>() + (size_t)d * SEQ, SEQ * 2);
        srd_set<60>(arg_late<const char, 5>() + O_X + d * SEQ * 2, SEQ * 2);
        sset<78>(65536 + wid * 15488);
        asm volatile("s_movk_i32 s64, 992\n\tv_subrev_u32 v122, 32, v84");
        GAP();
        g_tile<TR, 0, 3>(g);
      } else {
        sset<79>(isY ? wid * 8192 : 0x30000);
        if (tget<T_IT + T_ITN + 16>() & 16) asm volatile("s_add_u32 s98, s98, 0x8000\n\ts_mov_b32 s97, 0xffff8000" ::: "scc");
        else asm volatile("s_mov_b32 s97, 0x8000");
        GAP();
        g_tile<TR, 0, 5>(g);
      }
    };
    if (kv) kvtile(std::true_type{});
    else kvtile(std::false_type{});
  }
  KST(0, 4);
  asm volatile("s_waitcnt vmcnt(16)" ::: "memory");
  const unsigned md0 = tget<T_IT + 16>(), md1 = tget<T_IT + T_ITN + 16>();
  sset<79>(wid * 8192 + (md0 & 16) * 2048);
  if (doA) {
    const float Dd = bfl(__float_as_uint(vget<123>()));
    KST(0, 5);
    conv_wave<T_FG>(O_Z + pj * 64 * SEQ * 2 + ((pu & 7) * 8 + (pu >> 3)) * 64, pu >> 3, Dd, lds + 65536 + wid * 15488);
    const char* tp = arg_late<const char, 11>() + (size_t)h * (HD * DA * 2) + ((pj * 4 + wid) * 64 + lane) * 128;
    asm volatile("s_mov_b32 m0, %1\n\ts_nop 0\n\tglobal_load_lds_dword %0, off\n\ts_waitcnt vmcnt(17)" ::"v"(tp), "i"(m0d) : "memory");
    KST(0, 6);
  } else if (!isY) {
    static_for([&](auto I) { d_store<16 + I>(); }, make_iseq<16>{});
    tload<98, T_K0>();
    asm volatile("s_waitcnt vmcnt(0)\n\ts_barrier" ::: "memory");
    fl_set<T_FG>();
    KST(0, 5);
    fl_check();
    CODE_TOUCH("att_begin", "pf_code_end", hsel * 4 + wid, 8u, 64, m0d);
    dma4k<0>(); dma4k<1>(); dma4k<2>(); dma4k<3>();
    asm volatile("s_waitcnt vmcnt(0)" ::: "memory");
    KST(0, 6);
  } else {
    tload<76, T_PART>();
    fl_check();
    asm volatile("buffer_load_dwordx2 v[122:123], v88, s[88:91], s76 offen offset:1024 sc1" ::: "memory");
  }
  sset<78>(65536 + wid * 8192); sset<79>(98304 + wid * 8192);
  { const bf16* wp = arg_late<const bf16, 11>() + ((md1 >> 24) & 15) * HD * DA; asm volatile("s_mov_b64 s[80:81], %0" ::"s"(wp)); }
  asm volatile("att_begin:");
  ev_code(c);
  asm volatile("s_waitcnt lgkmcnt(0)\n\ts_barrier" ::: "memory");
#pragma unroll 1
  for (int it = md0 ? 0 : 1; it < 2; ++it) {
    const unsigned mode = it ? md1 : md0;
    const bool odd = mode & 16;
    if (odd) kreads<1>(make_iseq<8>{});
    else kreads<0>(make_iseq<8>{});
    const unsigned oP = tget<T_PART>();
    if (it == 0) {
      static_for([&](auto I) { tload<57 + I, T_IT + I>(); }, make_iseq<21>{});
      tload<S_T, T_IT + 21>(); tload<S_EV, T_IT + 22>();
    } else {
      static_for([&](auto I) { tload<57 + I, T_IT + T_ITN + I>(); }, make_iseq<21>{});
      tload<S_T, T_IT + T_ITN + 21>(); tload<S_EV, T_IT + T_ITN + 22>();
    }
    asm volatile("s_add_u32 s98, s97, 0x8000\n\ts_mov_b32 s56, -1\n\tv_mov_b64 v[114:115], 0\n\tv_mov_b64 v[118:119], 0\n\t"
                 "v_mov_b32 v120, 0x45800000" ::: "scc");
    KST(0, 8 + 3 * (it && !isY));
    GAP();
#define STA(k)
#define STI()
#define LOOP_ENDS "s_bfe_u32 s76, s73, 0x80008"
    STA(0);
    if (odd) {
      if (isY) phaseA<1, 2>(c);
      else phaseA<1, 1>(c);
      STA(1);
      STA(2);
      phaseB0<1>(mode, isY);
      STA(3);
      asm volatile(LOOP_ENDS "\n\ts_branch att_mid" ::: "scc");
    } else {
      if (isY) phaseA<0, 2>(c);
      else phaseA<0, 1>(c);
      STA(1);
      if (__builtin_expect(mode & 4, 0)) { asm volatile("s_mov_b32 s56, 30"); ev_call(); asm volatile("s_mov_b32 s56, 31"); ev_call(); acc_zero(); }
      STA(2);
      phaseB0<0>(mode, isY);
      if (__builtin_expect(mode & 4, 0)) { ev_call(); asm volatile("s_branch att_done"); }
      STA(3);
    }
    GAP();
    asm volatile(LOOP_ENDS "\natt_loop:" ::: "scc");
    GAP();
    phaseA<1, 0>(c);
    STI();
    if (__builtin_expect(c.rare != 0, 0)) sm_redo<1>();
    phaseB<1>(c);
    asm volatile("s_cbranch_scc1 att_done\natt_mid:");
    phaseA<0, 0>(c);
    STI();
    if (__builtin_expect(c.rare != 0, 0)) sm_redo<0>();
    phaseB<0>(c);
    asm volatile("s_cbranch_scc0 att_loop\natt_done:");
    GAP();
    STA(4);
    if (it == 0) phaseA<0, 3>(c);
    else {
      sset<76>(oP);
      GAP();
      phaseAL(c, make_iseq<32>{});
      if (__builtin_expect(redo(), 0)) sm_redo<1, false>();
      if (mode & 2) phaseBL<true>(c, make_iseq<64>{});
      else phaseBL<false>(c, make_iseq<64>{});
    }
    if (__builtin_expect(redo(), 0)) {
      if (it) static_for([&](auto I) { vset<R_S + 32 + (I >> 2) * 8 + (I & 3)>(-INFINITY); }, make_iseq<16>{});
      asm volatile("s_nop 7");
      sm_redo<0, false>();
    }
    STA(5);
    KST(0, 9 + 3 * (it && !isY));
    float l[2];
    asm volatile("v_add_f32 %0, v%c2, v%c3\n\tv_add_f32 %1, v%c4, v%c5" : "=v"(l[0]), "=v"(l[1]) : "i"(R_RS), "i"(R_TS), "i"(R_RS + 1), "i"(R_TS + 1));
    const float cm[2] = {vget<R_M>(), vget<R_M + 1>()};
    l[0] = row_reduce<false>(l[0]); l[1] = row_reduce<false>(l[1]);
    EP p;
    if (it == 0) {
      const f32x4 ml = {cm[0], l[0], cm[1], l[1]};
      asm volatile("buffer_store_dwordx4 %0, v88, s[88:91], %1 offen sc1\n\ts_mov_b32 s101, %2" ::"v"(ml), "s"(oP), "s"(oP + wid * 16384) : "memory");
      phaseF(c, p, make_iseq<74>{});
      asm volatile(FL_POLL "\n\ts_waitcnt vmcnt(17)" ::: "memory");
    } else {
      if (!(mode & 2)) {
        p.fa[0] = __builtin_amdgcn_rcpf(l[0]); p.fa[1] = __builtin_amdgcn_rcpf(l[1]);
        phaseFL<1>(p);
      } else {
        const f32x4 ml = {vget<122>(), vget<123>(), vget<124>(), vget<125>()};
        if (__builtin_expect(__builtin_amdgcn_ballot_w64(ml[0] != cm[0] || ml[2] != cm[1]) == 0, 1)) {
          p.fa[0] = __builtin_amdgcn_rcpf(l[0] + ml[1]); p.fa[1] = __builtin_amdgcn_rcpf(l[1] + ml[3]);
          phaseFL<3>(p);
        } else {
#pragma unroll
          for (int e = 0; e < 2; ++e) {
            const float mm = fmaxf(cm[e], ml[2 * e]);
            const float wa = __builtin_amdgcn_exp2f(cm[e] - mm), wb = __builtin_amdgcn_exp2f(ml[2 * e] - mm);
            const float inv = __builtin_amdgcn_rcpf(l[e] * wa + ml[2 * e + 1] * wb);
            p.fa[e] = wa * inv; p.fb[e] = wb * inv;
          }
          phaseFL<2>(p);
        }
      }
    }
    KST(0, 10 + 3 * (it && !isY));
  }
  asm volatile(TICKET "\n\tv_xor_b32 v%c0, s100, v%c0\np_loop:" ::"i"(R_TB) : "memory");
  GAP();
  p_half<0, true>(make_iseq<32>{});
  asm volatile("s_cbranch_scc0 p_mid");
  p_half<1, true>(make_iseq<32>{});
  asm volatile("s_branch p_loop\np_loop2:");
  p_half<0, false>(make_iseq<32>{});
  asm volatile("p_mid:");
  p_half<1, false>(make_iseq<32>{});
  asm volatile("s_cbranch_scc1 p_loop2\n\ts_nop 5");
  GAP();
  p_cvt<1, 0>(); p_cvt<1, 1>(); p_cvt<1, 2>(); p_cvt<1, 3>();
  p_store<1, 1>();
  GAP();
  KST(0, 27);
  {
    constexpr unsigned rdy = 98304;
    int lz = threadIdx.x & 63; asm volatile("" : "+v"(lz));
    const int fr = lz & 15, fq = lz >> 4, qbF = md1 >> 28;
    if (wid == 0) {
#pragma unroll 1
      for (int q = 0; q < 0x100000; ++q) {
        unsigned lo, hi, tl, th;
        asm volatile("v_readfirstlane_b32 %0, v240\n\tv_readfirstlane_b32 %1, v241\n\ts_mov_b32 %2, s84\n\ts_mov_b32 %3, s85" : "=s"(lo), "=s"(hi), "=s"(tl), "=s"(th));
        if (__builtin_expect((((lo ^ tl) >> 8) | (hi ^ th)) == 0, 1)) break;
        asm volatile("s_sleep 4\n\tv_mov_b64 v[240:241], 1\n\t" TICKET "\n\ts_waitcnt vmcnt(0)" ::: "memory");
      }
      asm volatile("v_mov_b32 v241, 0\n\ts_mov_b64 exec, 1\n\tds_write_b64 %0, v[240:241]\n\ts_mov_b64 exec, -1" ::"v"(rdy) : "memory");
    }
    vsetu<79>(fo(1024) + qbF * 4352 + fr * 8);
    {
      const int jj = 4 * (fr >> 3) + (fr & 3);
      const unsigned one = ((fq ^ (fr >> 2)) & 1) ? 0u : (jj & 1) ? 0x3f800000u : 0x3f80u;
      static_for([&](auto I) { vsetu<96 + I>((jj >> 1) == I ? one : 0u); }, make_iseq<4>{});
    }
    unsigned vo = (fq >> 1) * 1024 + (fq & 1) * 256 + fr * 16;
    bf16* out = arg_late<bf16, 13>();
    __amdgpu_buffer_rsrc_t so_ = __builtin_amdgcn_make_buffer_rsrc((void*)out, (short)0, SEQ * OUTW * 2, 0x00020000);
    unsigned vw = ((qbF * 128 + (fr & 7)) * OUTW + DA + (fr >> 3) * 32 + fq * 8) * 2;
    asm volatile("" : "+s"(so_));
    asm volatile("s_waitcnt vmcnt(0) lgkmcnt(0)\n\ts_barrier" : "+v"(vo), "+v"(vw) :: "memory");
    fl_set<T_FS>();
    unsigned tk;
    asm volatile("ds_read_b32 %0, %1\n\ts_waitcnt lgkmcnt(0)" : "=v"(tk) : "v"(rdy) : "memory");
    tk = __builtin_amdgcn_readfirstlane(tk) & 255;
    KST(0, 30);
    if (doA) post_math(pj, pu, out, lds + 131072 + wid * 8192);
#define N3 0
#define N2 12
    constexpr int N1 = 28 - 3 * N3 - 2 * N2;
    static_assert(N1 >= 0 && N3 + N2 + N1 <= 16);
    const int nu = tk < N3 ? 3 : tk < N3 + N2 ? 2 : tk < N3 + N2 + N1 ? 1 : 0;
    const int u0 = 4 * (tk < N3 ? 3 * tk : tk < N3 + N2 ? N3 + 2 * tk : 2 * N3 + N2 + tk) + wid * nu;
    KST(0, 28);
    if (nu) {
      const unsigned so0 = O_S + qbF * 0x200000 + (16 + u0) * 2048, so[3] = {so0, so0, so0};
      unsigned pend = 0x5555u, cnt = 0x400000;
      while (true) {
        unsigned nw;
        if (wid == 3) {
          u64 m;
          const u64 ex = pend | (pend << 1);
          asm volatile(FL_ARM("%1") "\n\t" FL_POLL "\n\ts_waitcnt vmcnt(0)\n\tv_cmp_eq_u64 %0, s[84:85], v[124:125]" : "=s"(m) : "s"(ex) : "memory");
          nw = (unsigned)m & ((unsigned)m >> 1) & pend;
          if (nw) asm volatile("ds_write_b32 %0, %1 offset:4" ::"v"(rdy), "v"(~pend | nw) : "memory");
        } else {
          unsigned t;
          asm volatile("ds_read_b32 %0, %1 offset:4\n\ts_waitcnt lgkmcnt(0)" : "=v"(t) : "v"(rdy) : "memory");
          nw = __builtin_amdgcn_readfirstlane(t) & pend;
        }
        if (--cnt == 0) nw = pend;
        if (nw == 0) {
          if (wid == 3) asm volatile("s_sleep 4");
          else asm volatile("s_sleep 1");
          continue;
        }
        asm volatile("s_nop 4");
        static_for([&](auto S) {
          if ((nw >> (2 * S)) & 1) {

            r_load<0, 2 * S>(vo, so[0]); r_load<0, 2 * S + 1>(vo, so[0]);
            if (nu > 1) { r_load<1, 2 * S>(vo, so[1]); r_load<1, 2 * S + 1>(vo, so[1]); }
            if (nu > 2) { r_load<2, 2 * S>(vo, so[2]); r_load<2, 2 * S + 1>(vo, so[2]); }
          }
        }, make_iseq<8>{});
        pend &= ~nw;
        if (pend == 0) break;
      }
      auto oo = [&](int U) { return ((((U >> 1) & 1) * 64 + ((U >> 2) & 3) * 16 + (U & 1) * 8) * OUTW + (U >> 4) * 64) * 2; };
      unsigned o0 = oo(u0), o1 = oo(u0 + 1), o2 = oo(u0 + 2);
      asm volatile("s_waitcnt vmcnt(0)" : "+s"(o0), "+s"(o1), "+s"(o2), "+s"(so_) :: "memory");
      KST(0, 16);
      if (nu > 2) r_sums<2>();
      if (nu > 1) r_sums<1>();
      r_sums<0>();
      if (nu > 2) r_fin<2>(vw, so_, o2);
      if (nu > 1) r_fin<1>(vw, so_, o1);
      asm volatile("s_nop 7");
      r_fin<0>(vw, so_, o0);
      if (qbF == 15 && tk == 0) asm volatile(FL_SET("%0") ::"s"(fo(6144)) : "memory");
    }
    KST(0, 29);
  }
  asm volatile("pf_code_end:");
  KST(0, 1);
}

extern "C" void kernel_launch(void* const* d_in, const int* in_sizes, int n_in, void* d_out, int out_size, void* d_ws,
                              size_t ws_size, hipStream_t stream) {
  const bf16* Aq = (const bf16*)d_in[0];
  const bf16* Ak = (const bf16*)d_in[1];
  const bf16* Av = (const bf16*)d_in[2];
  const bf16* Ash = (const bf16*)d_in[3];
  const bf16* Assmk = (const bf16*)d_in[4];
  const bf16* AssmD = (const bf16*)d_in[5];
  const bf16* Bx = (const bf16*)d_in[6];
  const bf16* Bckv = (const bf16*)d_in[7];
  const bf16* Bwq = (const bf16*)d_in[8];
  const bf16* Bwuk = (const bf16*)d_in[9];
  const bf16* Bwuv = (const bf16*)d_in[10];
  const bf16* Bwo = (const bf16*)d_in[11];
  bf16* out = (bf16*)d_out;
  hipFuncSetAttribute((const void*)fused_kernel, hipFuncAttributeMaxDynamicSharedMemorySize, 163840);
  fused_kernel<<<256, 256, 163840, stream>>>(Bckv, Bwuk, Bwuv, Bx, Bwq, (bf16*)d_ws, Ak, Av, Ash, Assmk, AssmD, Bwo, Aq, out);
}
```

```cpp
#include <hip/hip_runtime.h>
#include <hip/hip_bf16.h>
#include <stdint.h>
#include <utility>

typedef __hip_bfloat16 bf16;
typedef __attribute__((ext_vector_type(4))) short bf16x4;
typedef __attribute__((ext_vector_type(2))) unsigned u32x2;

constexpr int SEQ = 2048;
constexpr int DA = 448;
constexpr int NH = 16;
constexpr int HD = 256;
constexpr int DC = 1024;
constexpr int OUTW = 896;
constexpr unsigned TSB = NH * SEQ * HD * 2, O_K = TSB, O_S = 3 * TSB, O_P = 5 * TSB, PSTR = 67584, O_F = O_P + 128 * PSTR,
                   O_X = O_F + 400 * 4352, O_Z = O_X + DA * SEQ * 2, WSB = O_Z + DA * SEQ * 2;
constexpr unsigned fo(int w) { return O_F + (w >> 4) * 4352 + (w & 15) * 8; }

#define KST(kid, which)

__device__ __forceinline__ float bf2f(unsigned short u) { return __uint_as_float(((unsigned)u) << 16); }
__device__ __forceinline__ unsigned short f2bf(float f) { return __bfloat16_as_ushort(__float2bfloat16(f)); }
__device__ __forceinline__ float rbf(float f) { return bf2f(f2bf(f)); }
typedef __attribute__((ext_vector_type(2))) __bf16 bf16x2n;
typedef __attribute__((ext_vector_type(2))) float f32x2n;
__device__ __forceinline__ unsigned pk2(float a, float b) {
  return __builtin_bit_cast(unsigned, __builtin_convertvector((f32x2n){a, b}, bf16x2n));
}

__device__ __forceinline__ int off_b(int row, int ch) { return 256 * row + 16 * (ch ^ (((row & 3) << 2) | ((row >> 2) & 3))); }

template <int R> __device__ __forceinline__ void sset(unsigned x) { asm volatile("s_mov_b32 s%c0, %1" ::"i"(R), "s"(x)); }
template <int R> __device__ __forceinline__ void vsetu(unsigned x) { asm volatile("v_mov_b32 v%c0, %1" ::"i"(R), "v"(x)); }
template <int R> __device__ __forceinline__ void srd_set(const void* p, unsigned n) {
  asm volatile("s_mov_b64 s[%c0:%c1], %4\n\ts_mov_b32 s%c2, %5\n\ts_mov_b32 s%c3, 0x20000" ::"i"(R), "i"(R + 1), "i"(R + 2), "i"(R + 3), "s"(p), "s"(n));
}
template <int R, int L> __device__ __forceinline__ void tload() { asm volatile("v_readlane_b32 s%c0, v78, %1" ::"i"(R), "i"(L)); }
template <int L> __device__ __forceinline__ unsigned tget() { unsigned t; asm volatile("v_readlane_b32 %0, v78, %1" : "=s"(t) : "i"(L)); return t; }
#define FL_SET(S) "buffer_store_dwordx2 v[82:83], v81, s[88:91], " S " offen sc1"
#define FL_ARM(N) "v_cndmask_b32_e64 v80, -2.0, v79, " N
#define FL_POLL "buffer_load_dwordx2 v[124:125], v80, s[88:91], 0 offen sc1"
#define FL_CHECK(N) "s_mov_b32 s48, 0x100000\n7:\n\tv_cmp_eq_u64 vcc, s[84:85], v[124:125]\n\ts_and_b64 vcc, vcc, " N "\n\ts_cmp_eq_u64 vcc, " N \
                    "\n\ts_cbranch_scc1 8f\n\ts_sleep 2\n\t" FL_POLL "\n\ts_waitcnt vmcnt(0)\n\ts_sub_u32 s48, s48, 1\n\ts_cbranch_scc0 7b\n8:"
template <int L> __device__ __forceinline__ void fl_set() {
  asm volatile("v_readlane_b32 s48, v78, %0\n\ts_nop 4\n\t" FL_SET("s48") ::"i"(L) : "memory");
}
template <int L> __device__ __forceinline__ void fl_poll() {
  asm volatile("v_readlane_b32 s50, v78, %0\n\tv_readlane_b32 s51, v78, %1\n\ts_nop 1\n\t" FL_ARM("s[50:51]") "\n\t" FL_POLL ::"i"(L), "i"(L + 1) : "memory");
}
#define TICKET "buffer_atomic_add_x2 v[240:241], v81, s[88:91], s101 offen sc0"
__device__ __forceinline__ void fl_check() { asm volatile(FL_CHECK("s[50:51]") ::: "vcc", "scc", "memory"); }
template <int R, int K> __device__ __forceinline__ unsigned vxor() { unsigned t; asm volatile("v_xor_b32 %0, %1, v%c2" : "=v"(t) : "i"(K), "i"(R)); return t; }
template <int R> __device__ __forceinline__ float vget() { float t; asm volatile("v_mov_b32 %0, v%c1" : "=v"(t) : "i"(R)); return t; }
template <int R> __device__ __forceinline__ void vset(float x) { asm volatile("v_mov_b32 v%c0, %1" ::"i"(R), "v"(x)); }
template <int R> __device__ __forceinline__ float acc_read1() { float t; asm volatile("v_accvgpr_read_b32 %0, a[%c1]" : "=v"(t) : "i"(R)); return t; }
template <class F, int... I> __device__ __forceinline__ void static_for(F&& f, std::integer_sequence<int, I...>) {
  (f(std::integral_constant<int, I>{}), ...);
}
template <int... Is> using iseq = std::integer_sequence<int, Is...>;
template <int N> using make_iseq = std::make_integer_sequence<int, N>;

typedef __attribute__((ext_vector_type(4))) unsigned u32x4c;
typedef __attribute__((ext_vector_type(4))) float f32x4c;
typedef __attribute__((ext_vector_type(2))) float f32x2c;
typedef unsigned long long u64;
struct PR { u32x4c kk[7], vv[4]; };
__device__ __forceinline__ void prep_load(PR& p, int tid, int cg, int tb, const bf16* __restrict__ k, const bf16* __restrict__ v,
                                          const bf16* __restrict__ shift_k) {
  const __amdgpu_buffer_rsrc_t sk = __builtin_amdgcn_make_buffer_rsrc((void*)k, (short)0, SEQ * DA * 2, 0x00020000);
  const __amdgpu_buffer_rsrc_t sv = __builtin_amdgcn_make_buffer_rsrc((void*)v, (short)0, SEQ * DA * 2, 0x00020000);
  const __amdgpu_buffer_rsrc_t sw = __builtin_amdgcn_make_buffer_rsrc((void*)(shift_k + cg * 256), (short)0, 512, 0x00020000);
  const int vo = ((tb * 128 + (tid >> 3) * 4) * DA + cg * 64 + (tid & 7) * 8) * 2;
#pragma unroll
  for (int q = 0; q < 7; ++q)
    asm volatile("buffer_load_dwordx4 %0, %1, %2, 0 offen" : "=v"(p.kk[q]) : "v"(vo + (q - 3) * DA * 2), "s"(sk) : "memory");
#pragma unroll
  for (int q = 0; q < 4; ++q)
    asm volatile("buffer_load_dwordx4 %0, %1, %2, 0 offen offset:%3" : "=v"(p.vv[q]) : "v"(vo), "s"(sv), "i"(q * DA * 2) : "memory");
#pragma unroll
  for (int q = 0; q < 4; ++q)
    asm volatile("buffer_load_dwordx4 a[%c0:%c1], %2, %3, 0 offen offset:%4" ::"i"(4 * q), "i"(4 * q + 3), "v"((tid & 7) * 64), "s"(sw), "i"(q * 16) : "memory");
}
__device__ __forceinline__ float bfl(unsigned u) { return __uint_as_float(u << 16); }
__device__ __forceinline__ float bfh(unsigned u) { return __uint_as_float(u & 0xffff0000u); }
__device__ __forceinline__ f32x2c rbf2(f32x2c a) {
  return (f32x2c){__uint_as_float(pk2(0.f, a[0])), __uint_as_float(pk2(0.f, a[1]))};
}
template <int I> __device__ __forceinline__ void prep_math(const PR& p, int tid, char* lx) {
  const int rp = tid >> 3, c = tid & 7;
  f32x2c wt[4], kf[7];
  const unsigned w[4] = {__float_as_uint(acc_read1<4 * I>()), __float_as_uint(acc_read1<4 * I + 1>()), __float_as_uint(acc_read1<4 * I + 2>()),
                         __float_as_uint(acc_read1<4 * I + 3>())};
#pragma unroll
  for (int t = 0; t < 4; ++t)
    wt[t] = (t & 1) ? (f32x2c){bfh(w[t >> 1]), bfh(w[2 + (t >> 1)])} : (f32x2c){bfl(w[t >> 1]), bfl(w[2 + (t >> 1)])};
#pragma unroll
  for (int q = 0; q < 7; ++q) kf[q] = (f32x2c){bfl(p.kk[q][I]), bfh(p.kk[q][I])};
  f32x2c x[4];
#pragma unroll
  for (int e = 0; e < 4; ++e) {
    f32x2c s = rbf2(kf[e] * wt[3]);
    s = rbf2(s + rbf2(kf[e + 1] * wt[2]));
    s = rbf2(s + rbf2(kf[e + 2] * wt[1]));
    s = rbf2(s + rbf2(kf[e + 3] * wt[0]));
    x[e] = s * (f32x2c){bfl(p.vv[e][I]), bfh(p.vv[e][I])};
  }
#pragma unroll
  for (int u = 0; u < 2; ++u)
    *(u32x2*)(lx + (c * 8 + 2 * I + u) * 256 + (((31 - rp) * 8) ^ (c << 4))) = (u32x2){pk2(x[3][u], x[2][u]), pk2(x[1][u], x[0][u])};
}
__device__ __forceinline__ void prep_store(int tid, int cg, int tb, bf16* __restrict__ xr, const char* lx) {
  const __amdgpu_buffer_rsrc_t sx = __builtin_amdgcn_make_buffer_rsrc((void*)(xr + (size_t)cg * 64 * SEQ), (short)0, 64 * SEQ * 2, 0x00020000);
#pragma unroll
  for (int rd = 0; rd < 4; ++rd) {
    const int idx = tid + 256 * rd, ch = idx >> 4, q = idx & 15;
    u32x4c w = *(const u32x4c*)(lx + ch * 256 + 16 * (q ^ (ch >> 3)));
    __builtin_amdgcn_raw_buffer_store_b128(w, sx, (ch * SEQ + (15 - tb) * 128 + q * 8) * 2, 0, 16);
  }
}

#define CODE_TOUCH(LB, LE, wx, NW, NL, m0v) do { unsigned blo_, bhi_, elo_; \
    asm volatile("s_getpc_b64 s[48:49]\n\ts_add_u32 s48, s48, " LB "@rel32@lo+4\n\ts_addc_u32 s49, s49, " LB "@rel32@hi+12\n\t" \
                 "s_mov_b32 %0, s48\n\ts_mov_b32 %1, s49\n\t" \
                 "s_getpc_b64 s[48:49]\n\ts_add_u32 s48, s48, " LE "@rel32@lo+4\n\ts_mov_b32 %2, s48" \
                 : "=s"(blo_), "=s"(bhi_), "=s"(elo_) :: "scc"); \
    const unsigned n_ = elo_ - (blo_ & ~127u); \
    const __amdgpu_buffer_rsrc_t srd_ = __builtin_amdgcn_make_buffer_rsrc((void*)(((unsigned long long)bhi_ << 32) | (blo_ & ~127u)), (short)0, n_ < 262144u ? n_ : 0u, 0x00020000); \
    const unsigned l_ = threadIdx.x & 63, vo_ = l_ < (NL) ? ((wx) + l_ * (NW)) * 128u : 0x80000000u; \
    asm volatile("s_mov_b32 m0, %0\n\ts_nop 4\n\tbuffer_load_dword %1, %2, 0 offen lds" ::"s"(m0v), "v"(vo_), "s"(srd_) : "memory"); } while (0)
template <int I> __device__ __forceinline__ void d_store() {
  asm volatile("buffer_store_dwordx4 v[%c0:%c1], v%c2, s[88:91], s101 offen offset:%3 sc1" ::"i"(128 + 4 * I), "i"(131 + 4 * I), "i"(112 + ((I >> 2) & 1)),
               "i"((I & 3) * 1024) : "memory");
  if constexpr ((I & 7) == 7) asm volatile("s_addk_i32 s101, 0x4000" ::: "scc");
}
constexpr int NS_X = 19;
struct CV { unsigned pa[4], pb, px, vz, sz; float D; };
constexpr int cv_J(bool up, int q) { int J = up ? 0 : 7; while (q > J) { q -= J + 1; J += up ? 1 : -1; } return J; }
constexpr int cv_i(bool up, int q) { int J = up ? 0 : 7; while (q > J) { q -= J + 1; J += up ? 1 : -1; } return q; }
template <int R, int N> __device__ __forceinline__ void cv_rd(CV& c) {
  if constexpr (N < 16) {
    constexpr int i = N >> 1, o = 16 * R + 128 * (i & 1) + 2 * (N & 1), r = 32 + 32 * (R & 1) + 2 * N;
    asm volatile("ds_read2_b32 a[%c1:%c2], %0 offset0:%c3 offset1:%c4" ::"v"(c.pa[i >> 1]), "i"(r), "i"(r + 1), "i"(o), "i"(o + 1));
  } else {
    constexpr int m = N - 16, r = 192 + 32 * (R & 1) + 4 * m;
    asm volatile("ds_read_b128 a[%c1:%c2], %0 offset:%c3" ::"v"(c.pb), "i"(r), "i"(r + 3), "i"(64 * R + 512 * (7 - m)));
  }
}
template <int O> __device__ __forceinline__ void cv_xd(CV& c) {
  constexpr int e = O & 31, x = 96 + ((e >> 1) ^ 1);
  if constexpr (O >= 64) asm volatile("v_cvt_pk_bf16_f32 v%c0, 0, v%c0" ::"i"(160 + e));
  else if constexpr (O >= 32) asm volatile("v_mul_f32 v%c0, v%c0, %1" ::"i"(160 + e), "v"(c.D));
  else if constexpr (O & 1) asm volatile("v_lshlrev_b32 v%c0, 16, v%c1" ::"i"(160 + e), "i"(x));
  else asm volatile("v_and_b32 v%c0, 0xffff0000, v%c1" ::"i"(160 + e), "i"(x));
}
template <int O> __device__ __forceinline__ void cv_z(CV& c) {
  constexpr int J = O / 11, k = O % 11, y = 128 + 4 * J;
  if constexpr (k < 4) asm volatile("v_cvt_pk_bf16_f32 v%c0, 0, v%c0" ::"i"(y + k));
  else if constexpr (k < 8) asm volatile("v_add_f32 v%c0, v%c0, v%c1" ::"i"(y + k - 4), "i"(y + 28 + k));
  else if constexpr (k < 10) asm volatile("v_cvt_pk_bf16_f32 v%c0, v%c1, v%c2" ::"i"(y + k - 8), "i"(y + 2 * k - 16), "i"(y + 2 * k - 15));
  else asm volatile("buffer_store_dwordx2 v[%c0:%c1], %2, s[88:91], s101 offen sc1\n\ts_add_u32 s101, s101, 0x8000" ::"i"(y), "i"(y + 1), "v"(c.vz) : "memory");
}
constexpr int cv_z0(int Q) {
  int o = 0;
  for (int q = 0; q < Q; ++q)
    for (int k = 0; k < (q % 4 == 1 ? 0 : 3); ++k) { const int J = o / 11; if ((J + 1) * (J + 2) / 2 - 1 + (J ? 4 : 7) <= q) ++o; }
  return o;
}
constexpr int cv_xd0(int T) { int o = 0; for (int t = 0; t < T; ++t) o += t % 4 != 1 || (t >= 72 && t < 108); return o < 96 ? o : 96; }
static_assert(cv_xd0(144) == 96);
template <int B, int... Is> __device__ __forceinline__ void cv_zs(CV& c, iseq<Is...>) { (cv_z<B + Is>(c), ...); }
template <int R, int Q> __device__ __forceinline__ void cv_slot(CV& c) {
  constexpr int i = cv_i(R == 7, Q), J = cv_J(R == 7, Q), d = 128 + 4 * J, a = 32 + 32 * (R & 1) + 4 * i, b = 192 + 32 * (R & 1) + 4 * (J - i);
  constexpr int T = 36 * R + Q, vm = Q % 4 == 1 && Q < 32 ? Q / 4 : -1;
  if constexpr (Q == 0) asm volatile("s_waitcnt lgkmcnt(0)");
  if constexpr (R >= 6 && vm >= 0 && vm % 4 == 0) asm volatile("s_add_u32 m0, s79, %0" ::"i"(vm * 1024));
  if constexpr (R == 0 && i == 0)
    asm volatile("v_mfma_f32_16x16x32_bf16 v[%c0:%c1], a[%c2:%c3], a[%c4:%c5], 0" ::"i"(d), "i"(d + 3), "i"(a), "i"(a + 3), "i"(b), "i"(b + 3));
  else
    asm volatile("v_mfma_f32_16x16x32_bf16 v[%c0:%c1], a[%c2:%c3], a[%c4:%c5], v[%c0:%c1]" ::"i"(d), "i"(d + 3), "i"(a), "i"(a + 3), "i"(b), "i"(b + 3));
  if constexpr (Q < 24 && R < 7) cv_rd<R + 1, Q>(c);
  if constexpr (T % 7 == 3 && NS_X + 5 + T / 7 < 32) d_store<NS_X + 5 + T / 7>();
  if constexpr (R == 2 && Q >= 24 && Q < 32)
    asm volatile("ds_read_b64 v[%c1:%c2], %0 offset:%c3" ::"v"(c.px), "i"(96 + 2 * (Q - 24)), "i"(97 + 2 * (Q - 24)), "i"(512 * (31 - Q)));
  if constexpr (R >= 3 && R < 7) if constexpr (cv_xd0(T - 107) > cv_xd0(T - 108)) cv_xd<cv_xd0(T - 108)>(c);
  if constexpr ((R == 3 || R == 4) && vm >= 0) {
    constexpr int f = (R - 3) * 8 + vm;
    asm volatile("buffer_load_dwordx4 a[%c0:%c1], v84, s[88:91], s96 offen offset:%2 sc1" ::"i"(128 + 4 * f), "i"(131 + 4 * f), "i"((f & 3) * 1024) : "memory");
    if constexpr ((f & 3) == 3) asm volatile("s_addk_i32 s96, %0" ::"i"(f == 7 ? 28672 : 4096));
  }
  if constexpr (R == 4 && Q == 35) asm volatile(FL_POLL ::: "memory");
  if constexpr (R >= 6 && vm >= 0)
    asm volatile("buffer_load_dwordx4 v%c0, s[88:91], s98 offen offset:%1 sc1 lds" ::"i"(86 + vm / 4), "i"((vm & 3) * 1024) : "memory");
  if constexpr (R == 6 && Q == 35) asm volatile("s_add_u32 s98, s98, 0x8000\n\ts_xor_b32 s79, s79, 0x8000");
  if constexpr (R == 7) cv_zs<cv_z0(Q)>(c, make_iseq<cv_z0(Q + 1) - cv_z0(Q)>{});
  __builtin_amdgcn_sched_barrier(0);
}
template <int R, int... Q> __device__ __forceinline__ void cv_step(CV& c, iseq<Q...>) { (cv_slot<R, Q>(c), ...); }
template <int... N> __device__ __forceinline__ void cv_rd0(CV& c, iseq<N...>) { (cv_rd<0, N>(c), ...); }
template <int FLAG> __device__ __forceinline__ void conv_wave(unsigned sz, int ck, float Dd, char* lw) {
  int lane = threadIdx.x & 63; asm volatile("" : "+v"(lane));
  const int l16 = lane & 15, fq = lane >> 4;
  u32x4c w[5]; unsigned w4[5];
#pragma unroll
  for (int r = 0; r < 5; ++r) {
    w[r] = *(const u32x4c*)(lw + r * 1024 + lane * 16);
    w4[r] = *(const unsigned*)(lw + r * 1024 + lane * 16 + 16);
  }
  d_store<NS_X>(); d_store<NS_X + 1>();
#pragma unroll
  for (int r = 0; r < 5; ++r)
    *(u32x4c*)(lw + 10304 + r * 1024 + lane * 16) = (u32x4c){__builtin_amdgcn_alignbit(w[r][1], w[r][0], 16), __builtin_amdgcn_alignbit(w[r][2], w[r][1], 16),
                                                             __builtin_amdgcn_alignbit(w[r][3], w[r][2], 16), __builtin_amdgcn_alignbit(w4[r], w[r][3], 16)};
  d_store<NS_X + 2>();
  CV c;
#pragma unroll
  for (int i = 0; i < 4; ++i) c.pa[i] = (unsigned)(size_t)lw + ((l16 + 1) & 1) * 10304 + 4 * ((8 * fq + l16 + 1) >> 1) + 1024 * i;
  c.pb = (unsigned)(size_t)lw + 5120 + (2032 - 16 * l16 + 8 * fq) * 2 - 512 * 7;
  c.vz = (l16 >> 1) * 4096 + (((l16 & 1) * 2 + (fq >> 1)) ^ (ck >> 1)) * 16 + (fq & 1) * 8;
  c.px = (unsigned)(size_t)lw + 5120 + 2044 * 2 - 512 * 7 - (16 * l16 + 4 * fq) * 2;
  c.sz = sz; c.D = Dd;
  asm volatile("" : "+v"(c.vz), "+v"(c.px), "+v"(c.D) :: "memory");
  asm volatile("" : "+s"(c.sz));
  cv_rd0(c, make_iseq<24>{});
  d_store<NS_X + 3>(); d_store<NS_X + 4>();
  __builtin_amdgcn_sched_barrier(0);
  static_for([&](auto R) __attribute__((always_inline)) {
    cv_step<R>(c, make_iseq<36>{});
    if constexpr (R == 2) {
      asm volatile("s_waitcnt vmcnt(0)\n\ts_barrier" ::: "memory");
      fl_set<FLAG>();
      sset<101>(c.sz);
    }
    if constexpr (R == 5) {
      asm volatile("s_waitcnt vmcnt(0)" ::: "memory");
      fl_check();
    }
    __builtin_amdgcn_sched_barrier(0);
  }, make_iseq<8>{});
  asm volatile("s_nop 7");
  cv_zs<cv_z0(36)>(c, make_iseq<88 - cv_z0(36)>{});
  __builtin_amdgcn_sched_barrier(0);
}

__device__ __forceinline__ void post_dma(int cg, int tb, const bf16* __restrict__ q, unsigned m0w) {
  int lane = threadIdx.x & 63; asm volatile("" : "+v"(lane));
  const __amdgpu_buffer_rsrc_t sq = __builtin_amdgcn_make_buffer_rsrc((void*)(q + (size_t)tb * 32 * DA + cg * 64), (short)0, 32 * DA * 2, 0x00020000);
  const unsigned vq = ((lane >> 3) * DA + (lane & 7) * 8) * 2;
  asm volatile("s_mov_b32 m0, %0\n\ts_nop 4\n\tbuffer_load_dwordx4 %1, %2, 0 offen lds\n\tbuffer_load_dwordx4 %1, %2, %3 offen offset:1024 lds\n\t"
               "buffer_load_dwordx4 %1, %2, %4 offen offset:2048 lds\n\tbuffer_load_dwordx4 %1, %2, %5 offen offset:3072 lds"
               ::"s"(m0w + 4096), "v"(vq), "s"(sq), "s"(8 * DA * 2 - 1024), "s"(16 * DA * 2 - 2048), "s"(24 * DA * 2 - 3072) : "memory");
}
__device__ __forceinline__ void post_math(int cg, int tb, bf16* __restrict__ out, const char* lw) {
  int lane = threadIdx.x & 63; asm volatile("" : "+v"(lane));
  const int tl = lane >> 3, ck = lane & 7;
  const __amdgpu_buffer_rsrc_t so = __builtin_amdgcn_make_buffer_rsrc((void*)(out + (size_t)tb * 32 * OUTW + cg * 64), (short)0, 32 * OUTW * 2 - 2 * DA, 0x00020000);
  const unsigned vo = (tl * OUTW + ck * 8) * 2;
#pragma unroll
  for (int i = 0; i < 4; ++i) {
    const u32x4c qv = *(const u32x4c*)(lw + 4096 + i * 1024 + lane * 16);
    const char* lz = lw + ck * 64 + 16 * (i ^ (ck >> 1)) + tl * 2;
    u32x4c o;
#pragma unroll
    for (int e = 0; e < 4; ++e) {
      const float z0 = bf2f(*(const unsigned short*)(lz + 2 * e * 512));
      const float z1 = bf2f(*(const unsigned short*)(lz + (2 * e + 1) * 512));
      o[e] = pk2(bfl(qv[e]) * z0, bfh(qv[e]) * z1);
    }
    __builtin_amdgcn_raw_buffer_store_b128(o, so, vo + i * 8 * OUTW * 2, 0, 17);
  }
}

#define DI __device__ __forceinline__
#define GAP() __builtin_amdgcn_sched_barrier(0)
typedef __attribute__((ext_vector_type(4))) float f32x4g;
typedef __attribute__((ext_vector_type(4))) unsigned u32x4g;
constexpr int LDW = NH * HD;
struct GM { unsigned aA[2], aB[2][8]; };
template <bool W, int J, int V> DI void g_dma() {
  if constexpr (J == 0) asm volatile("buffer_load_dwordx4 v%c0, s[%c1:%c2], 0 offen lds" ::"i"(V), "i"(W ? 60 : 56), "i"(W ? 63 : 59) : "memory");
  else asm volatile("buffer_load_dwordx4 v%c0, s[%c1:%c2], s%c3 offen lds" ::"i"(V), "i"(W ? 60 : 56), "i"(W ? 63 : 59), "i"((W ? 70 : 63) + J) : "memory");
}
template <int P, int KS, int I> DI void g_wread(GM& g) {
  constexpr int nf = I >> 1, hf = I & 1, r = 192 - 64 * P + 8 * nf + 4 * KS + 2 * hf;
  asm volatile("ds_read_b64_tr_b16 a[%c1:%c2], %0 offset:%c3" ::"v"(g.aB[hf][nf]), "i"(r), "i"(r + 1), "i"(P * 32768 + KS * 16384));
}
template <int P, int M, int KS> DI void g_aread(GM& g) {
  constexpr int r = 96 + 8 * (M & 1) + 4 * KS;
  asm volatile("ds_read_b128 v[%c1:%c2], %0 offset:%c3" ::"v"(g.aA[KS]), "i"(r), "i"(r + 3), "i"(P * 32768 + M * 2048));
}
constexpr float CS = 0.0625f * 1.4426950408889634f;
constexpr int NS_Q = 19;
constexpr int e_vm(int G) { return (G * 3) % 8 < 3 ? G * 3 / 8 : -1; }
constexpr int e_st(int MODE, int G) {
  const int q = G & 15, k3 = ((G >> 4) - 1) * 3 + (q == 0 ? 0 : q == 4 ? 1 : 2), k2 = NS_Q + (G >> 4) * 2 + (q == 4);
  if (MODE == 5) return e_vm(G) >= 32 ? e_vm(G) - 32 : -1;
  if (MODE == 3) return G >= 33 && (G - 33) % 5 == 0 && (G - 33) / 5 < NS_X ? (G - 33) / 5 : -1;
  if (MODE == 2) return G >= 30 && (q == 0 || q == 4 || q == 14) && k3 - 2 < NS_Q ? k3 - 2 : -1;
  if (MODE == 4) return (q == 0 || q == 4) && k2 < 32 ? k2 : -1;
  return -1;
}
constexpr int e_nst(int MODE, int G0, int G1) { int n = 0; for (int G = G0; G < G1; ++G) n += e_st(MODE, G) >= 0; return n; }
static_assert(e_nst(2, 0, 128) == NS_Q && e_nst(3, 30, 128) == NS_X && e_nst(5, 0, 128) == 16 && e_nst(4, 0, 104) == 32 - NS_Q);
constexpr int e_slot(int MODE, int G) { return MODE == 3 ? (G % 3 == 0 && G < 30 ? G / 3 : -1) : e_vm(G) >= 0 && e_vm(G) < 32 ? 10 + e_vm(G) : -1; }
struct ES { int op0[129], nop[129]; };
constexpr ES e_sched(bool TR, int MODE) {
  ES z{};
  const bool SCL = MODE == 2 || MODE == 4;
  const int per = SCL ? 16 : 8, o1 = 32 * per;
  int o = MODE == 4 ? e_sched(false, 2).op0[128] : 0;
  for (int G = 0; G <= 128; ++G) {
    const int q = G & 15;
    int cap = 2 - (q == 1 || q == 2 || q == 15);
    if (SCL) cap -= (q >= 3 && q < 11 && (q & 1)) + (q == 7 || q == 11) + 2 * (G == 70 || G == 74 || (G & 63) == 61);
    cap += MODE == 2 ? 4 + (q == 10 || q == 13) : MODE == 3 ? 1 : MODE == 5 ? 2 : 0;
    if (e_st(MODE, G) >= 0 || (SCL ? q == 8 || q == 12 : e_slot(MODE, G) >= 0) || (MODE == 4 && G < 4)) cap = 0;
    if (G == 128) cap = 10000;
    z.op0[G] = o;
    while (o < o1) {
      const int i = o / per, rdy = MODE == 4 ? 0 : (TR ? 32 * (i >> 3) + 16 + 2 * (i & 7) + 1 : 4 * i + 3) + 6;
      if ((G < rdy && G < 128) || cap-- <= 0) break;
      ++o; ++z.nop[G];
    }
  }
  return z;
}
template <bool TR, int MODE> constexpr ES ESCH = e_sched(TR, MODE);
static_assert(ESCH<false, 4>.op0[96] == 512 && ESCH<false, 4>.op0[0] >= 24 * 16);
template <bool TR, bool SCL, int O> DI void e_op() {
  constexpr int per = SCL ? 16 : 8, i = O / per, k = O % per, r = 128 + 4 * i, t = 114 + 4 * (i & 1), c = k - (per - 4);
  if constexpr (k < 4) asm volatile("v_accvgpr_read_b32 v%c0, a[%c1]" ::"i"(t + k), "i"(4 * i + k));
  else if constexpr (c < 0) asm volatile("v_mul_f32 v%c0, s83, v%c0" ::"i"(k < 8 ? r + (k ^ 6) : t + k - 8));
  else {
    constexpr int d = c == 0 ? r + 3 : c == 1 ? r + 2 : r + c - 2, x = c == 0 ? r + 2 : c == 1 ? r : t + 2 * (c - 2);
    asm volatile("v_cvt_pk_bf16_f32 v%c0, v%c1, v%c2" ::"i"(d), "i"(x), "i"(x + 1));
  }
}
template <bool TR, bool SCL, int B, int... Is> DI void e_ops(iseq<Is...>) { (e_op<TR, SCL, B + Is>(), ...); }
template <int S> DI void e_m0() {
  if constexpr (S == 0 || S == 1 || S == 5 || S == 9) asm volatile("s_add_u32 m0, s78, %0" ::"i"(S * 1024));
  if constexpr (S >= 26 && (S - 26) % 4 == 0) asm volatile("s_add_u32 m0, s79, %0" ::"i"(((S - 26) >> 3) * 32768 + ((S - 26) & 4) * 1024));
}
template <int S> DI void e_load() {
  if constexpr (S == 0) asm volatile("buffer_load_dwordx4 v122, s[56:59], 0 offen lds" ::: "memory");
  else if constexpr (S < 5) asm volatile("buffer_load_dwordx4 v84, s[56:59], s64 offen offset:%0 lds" ::"i"((S - 1) * 1024) : "memory");
  else if constexpr (S < 9) asm volatile("buffer_load_dwordx4 v84, s[60:63], 0 offen offset:%0 sc1 lds" ::"i"((S - 5) * 1024) : "memory");
  else if constexpr (S == 9) asm volatile("buffer_load_dwordx4 v84, s[60:63], s62 offen sc1 lds" ::: "memory");
  else if constexpr (S < 26) {
    constexpr int f = S - 10;
    asm volatile("buffer_load_dwordx4 a[%c0:%c1], v84, s[88:91], s96 offen offset:%2 sc1" ::"i"(128 + 4 * f), "i"(131 + 4 * f), "i"((f & 3) * 1024) : "memory");
    if constexpr ((f & 3) == 3) asm volatile("s_addk_i32 s96, %0" ::"i"(f == 7 ? 28672 : 4096));
  } else {
    constexpr int f = S - 26;
    asm volatile("buffer_load_dwordx4 v%c0, s[88:91], s98 offen offset:%1 sc1 lds" ::"i"(86 + ((f >> 2) & 1)), "i"((f & 3) * 1024) : "memory");
    if constexpr (f == 7) asm volatile("s_add_u32 s98, s98, s97");
    if constexpr (f == 15) asm volatile(FL_POLL ::: "memory");
  }
}
template <bool TR, int P, int H, int G, int MODE> DI void g_gap(GM& g) {
  constexpr int m = 4 * H + (G >> 4), q = G & 15, n = q >> 1, ks = q & 1, GG = 64 * H + G;
  constexpr bool inV = TR ? m & 1 : n & 1, END = MODE == 3 || MODE == 5;
  constexpr int a = 96 + 8 * (m & 1) + 4 * ks, b = 192 - 64 * P + 8 * n + 4 * ks, d = (inV ? 128 : 0) + 4 * (TR ? 8 * (m >> 1) + n : 4 * m + (n >> 1));
  constexpr int J = END ? -1 : q == 8 ? 2 * (G >> 4) : q == 12 ? 2 * (G >> 4) + 1 : -1, gj = (J & 3) ^ ((J & 3) >> 1);
  if constexpr (END) { if constexpr (q == 0 && GG > 0) asm volatile("s_waitcnt lgkmcnt(0)"); e_m0<e_slot(MODE, GG)>(); }
  else if constexpr (q == 0 && G > 0) asm volatile("s_waitcnt lgkmcnt(4)");
  if constexpr (J == 0) asm volatile("s_add_u32 m0, s78, %0" ::"i"((P ^ H ^ 1) * 32768 + (1 - H) * 8192));
  else if constexpr (J == 4) asm volatile("s_add_u32 m0, s79, %0" ::"i"((P ^ H) * 32768 + (1 - H) * 16384));
  else if constexpr (J > 0 && (J & 3) == 1) asm volatile("s_bitset1_b32 m0, 12");
  else if constexpr (J > 0 && (J & 3) == 2) asm volatile("s_bitset1_b32 m0, %0" ::"i"(J < 4 ? 14 : 13));
  else if constexpr (J > 0) asm volatile("s_bitset0_b32 m0, 12");
#define GMF(D, X, Y, C) asm volatile("v_mfma_f32_16x16x32_bf16 " D "[%c0:%c1], " X ", " Y ", " C ::"i"(d), "i"(d + 3), "i"(a), "i"(a + 3), "i"(b), "i"(b + 3))
#define GMF2(D, C) do { if constexpr (TR) GMF(D, "v[%c2:%c3]", "a[%c4:%c5]", C); else GMF(D, "a[%c4:%c5]", "v[%c2:%c3]", C); } while (0)
  if constexpr ((MODE == 1 || MODE == 4) && ks == 0) { if constexpr (inV) GMF2("v", "0"); else GMF2("a", "0"); }
  else if constexpr (inV) GMF2("v", "v[%c0:%c1]");
  else GMF2("a", "a[%c0:%c1]");
#undef GMF2
#undef GMF
  if constexpr ((q == 1 || q == 2) && !(END && m == 7)) g_aread<(m < 7 ? P : P ^ 1), (m + 1) & 7, q - 1>(g);
  if constexpr (!END && q >= 3 && q < 11 && (q & 1)) g_wread<P ^ 1, H, (G >> 4) * 4 + (q - 3) / 2>(g);
  if constexpr (MODE == 1 && GG < 7) {
    if constexpr (GG == 0) asm volatile("s_barrier" ::: "memory");
    if constexpr (GG == 2) asm volatile("s_add_u32 m0, s79, %0" ::"i"(P * 32768));
    if constexpr (GG >= 3) g_dma<true, (GG - 3) ^ ((GG - 3) >> 1), 91>();
    if constexpr (GG == 3 || GG == 4) asm volatile("s_bitset1_b32 m0, %0" ::"i"(9 + GG));
    if constexpr (GG == 5) asm volatile("s_bitset0_b32 m0, 12");
  }
  if constexpr (e_st(MODE, GG) >= 0) {
    static_assert(ESCH<TR, MODE>.op0[GG] >= (e_st(MODE, GG) + 1) * (END ? 8 : 16));
    d_store<e_st(MODE, GG)>();
  }
  if constexpr (J >= 0 && J < 4) g_dma<false, (1 - H) * 2 + (gj & 1) + (gj >> 1) * 4, 90>();
  else if constexpr (J >= 4) g_dma<true, (1 - H) * 4 + gj, 91>();
  if constexpr (MODE >= 2) e_ops<TR, !END, ESCH<TR, MODE>.op0[GG]>(make_iseq<ESCH<TR, MODE>.nop[GG]>{});
  if constexpr (END) {
    if constexpr (e_slot(MODE, GG) >= 0) e_load<e_slot(MODE, GG)>();
  } else {
    if constexpr (H == 1 && G == 6) asm volatile("v_lshl_add_u32 v90, s82, 7, v92\n\ts_and_b32 s82, s80, 15");
    if constexpr (H == 1 && G == 10) asm volatile("s_lshr_b64 s[80:81], s[80:81], 4\n\tv_lshl_add_u32 v91, s82, 19, v93");
    if constexpr (G == 61) {
      constexpr int nst = e_nst(MODE, H ? 29 : 0, GG + 1) + (MODE == 4 && !H ? e_nst(2, 93, 128) : 0);
      asm volatile("s_waitcnt vmcnt(%0) lgkmcnt(0)\n\ts_barrier" ::"i"(12 + nst) : "memory");
    }
  }
  if constexpr (END && GG == 127) {
    asm volatile("s_nop 7");
    e_ops<TR, false, ESCH<TR, MODE>.op0[128]>(make_iseq<ESCH<TR, MODE>.nop[128]>{});
  }
  GAP();
}
template <bool TR, int P, int H, int MODE, int... Gs> DI void g_half_(GM& g, iseq<Gs...>) { (g_gap<TR, P, H, Gs, MODE>(g), ...); }
template <bool TR, int P, int H, int MODE> DI void g_half(GM& g) { g_half_<TR, P, H, MODE>(g, make_iseq<64>{}); }
template <bool TR, int P, int MODE> DI void g_tile(GM& g) { g_half<TR, P, 0, MODE>(g); g_half<TR, P, 1, MODE>(g); }
template <int P, int... Is> DI void g_read0(GM& g, iseq<Is...>) {
  (g_wread<P, 0, Is>(g), ...); (g_wread<P, 1, Is>(g), ...);
  g_aread<P, 0, 0>(g); g_aread<P, 0, 1>(g);
}
DI void g_proW(unsigned g, unsigned mW) {
  asm volatile("v_lshl_add_u32 v91, %0, 18, v93\n\ts_mov_b32 m0, %1" ::"s"(g), "s"(mW));
  asm volatile("s_nop 0"); g_dma<true, 0, 91>();
  asm volatile("s_bitset1_b32 m0, 12\n\ts_nop 0"); g_dma<true, 1, 91>();
  asm volatile("s_bitset1_b32 m0, 13\n\ts_nop 0"); g_dma<true, 3, 91>();
  asm volatile("s_bitset0_b32 m0, 12\n\ts_nop 0"); g_dma<true, 2, 91>();
}
template <int PART = 3> DI void g_pro(unsigned i, unsigned mA, unsigned mW, unsigned lda) {
  i &= 3;
  if constexpr (PART & 1)
    asm volatile("v_add_u32 v90, %0, v92\n\tv_add_u32 v91, %1, v93\n\ts_mov_b32 m0, %2\n\ts_mov_b32 s54, %3" ::"s"(((i & 1) + (i >> 1) * 4) * 64 * lda),
                 "s"((i & 1) * 0x20000 + (i >> 1) * 256), "s"(mA + (i & 1) * 4096 + (i >> 1) * 16384), "s"(mW + i * 4096));
  if constexpr (PART == 1) return;
  g_dma<false, 0, 90>();
  asm volatile("s_bitset1_b32 m0, 13\n\ts_nop 0"); g_dma<false, 2, 90>();
  asm volatile("s_mov_b32 m0, s54\n\ts_nop 0"); g_dma<true, 0, 91>();
  asm volatile("s_bitset1_b32 m0, 14\n\ts_nop 0"); g_dma<true, 4, 91>();
  asm volatile("s_xor_b32 m0, s54, 0x8000\n\ts_nop 0\n\tbuffer_load_dwordx4 v91, s[60:63], s53 offen lds" ::: "memory", "scc");
}
DI void g_pro2(unsigned mA, unsigned mW) {
  asm volatile("v_add_u32 v90, 128, v92\n\ts_mov_b32 m0, %0" ::"s"(mA ^ 32768));
  asm volatile("s_nop 0"); g_dma<false, 0, 90>();
  asm volatile("s_bitset1_b32 m0, 12\n\ts_nop 0"); g_dma<false, 1, 90>();
  asm volatile("s_bitset1_b32 m0, 14\n\ts_nop 0"); g_dma<false, 5, 90>();
  asm volatile("s_bitset0_b32 m0, 12\n\ts_nop 0"); g_dma<false, 4, 90>();
  g_proW(3, mW ^ 49152);
}
template <bool TR, bool SCL, class H0, class H1> DI void g_loop(GM& g, H0&& early, H1&& late) {
  constexpr int nkt = SCL ? 7 : 16, P0 = SCL ? 0 : 1;
#pragma unroll 1
  for (int t = 1;; t += 2) {
    asm volatile("" : "+s"(t));
    g_half<TR, P0 ^ 1, 0, 0>(g);
    if (__builtin_expect(t == (SCL ? 5 : 13), 0)) { late(); GAP(); }
    g_half<TR, P0 ^ 1, 1, 0>(g);
    if (SCL && t + 2 >= nkt) break;
    g_half<TR, P0, 0, 0>(g);
    if (__builtin_expect(t == (SCL ? 3 : 1), 0)) { early(); GAP(); }
    if constexpr (!SCL) asm volatile("s_mov_b32 s78, s83");
    g_half<TR, P0, 1, 0>(g);
    if (!SCL && t + 3 >= nkt) break;
  }
}

typedef __attribute__((ext_vector_type(4))) unsigned u32x4;
typedef __attribute__((ext_vector_type(4))) float f32x4;

constexpr int R_S = 128, R_P = 192, R_CI = 224, R_E = 96;
constexpr int R_TS = 112, R_RS = 114, R_M = 116;
constexpr int R_PD = 118, R_THR = 120, R_TMP = 121;
constexpr int r_dk(int q) { return q < 6 ? 90 + q : 120 + q; }
constexpr int S_V = 96, S_T = 97, S_K = 98, S_I = 99, S_EV = 100;
struct AC {
  unsigned rare;
  unsigned tb, vs, o0, o1;
};
DI bool redo() { u64 t; asm volatile("s_mov_b64 %0, s[54:55]" : "=s"(t)); return t != 0; }
template <int X, int F> DI void kread() {
  constexpr int r = 192 + 4 * (F & 15);
  asm volatile("ds_read_b128 a[%c0:%c1], v84 offset:%c2" ::"i"(r), "i"(r + 3), "i"(X * 32768 + F * 1024));
}
template <int X, int F> DI void vread() {
  constexpr int r = 192 + 4 * (F & 15);
  asm volatile("ds_read_b128 a[%c0:%c1], v85 offset:%c2" ::"i"(r), "i"(r + 3), "i"(X * 32768 + (F & 1) * 16384 + (F >> 1) * 1024));
}
template <int X, int... Fs> DI void kreads(iseq<Fs...>) { (kread<X, Fs>(), ...); }
template <int X, int J> DI void dma_m0() {
  if constexpr (J == 0) asm volatile("s_mov_b32 m0, s%c0" ::"i"(78 + X));
  else if constexpr (J == 4) asm volatile("s_or_b32 m0, s%c0, 0x1000" ::"i"(78 + X));
  else if constexpr (J == 8) asm volatile("s_bitset0_b32 m0, 16");
  else if constexpr (J == 12) asm volatile("s_bitset0_b32 m0, 12");
}
template <int J> DI void dma_at() {
  if constexpr (J < 8)
    asm volatile("buffer_load_dwordx4 v%c0, s[88:91], s96 offen offset:%1 sc1 lds" ::"i"(86 + (J >= 4)), "i"((J & 3) * 1024) : "memory");
  else
    asm volatile("buffer_load_dwordx4 v%c0, s[92:95], s98 offen offset:%1 sc1 lds" ::"i"(r_dk(J - 8)), "i"((J & 3) * 1024) : "memory");
}
template <int Q> DI void dma4k() {
  asm volatile("s_xor_b32 m0, s79, %0\n\ts_add_u32 s48, s98, %1\n\tbuffer_load_dwordx4 v%c2, s[88:91], s48 offen sc1 lds\n\t"
               "buffer_load_dwordx4 v%c2, s[88:91], s48 offen offset:1024 sc1 lds\n\tbuffer_load_dwordx4 v%c2, s[88:91], s48 offen offset:2048 sc1 lds\n\t"
               "buffer_load_dwordx4 v%c2, s[88:91], s48 offen offset:3072 sc1 lds"
               ::"i"((Q >> 1) * 32768 + (Q & 1) * 4096), "i"((Q >> 1) * 32768), "i"(86 + (Q & 1)) : "memory", "scc");
}
DI void acc_zero() { static_for([](auto I) { asm volatile("v_accvgpr_write_b32 a[%c0], 0" ::"i"((int)I)); }, make_iseq<128>{}); }
#define SMV 0
struct SMS { int ex[128], op0[128], nop[128], last; };
constexpr SMS sm_sched() {
  SMS z{};
  int et[32] = {}, e = 0;
  for (int t = 0; t < 128; ++t) {
    const int g = t & 15;
    const bool x = t < 48 ? (g >= 8 && (g & 1) && g < 15) : t < 64 ? (g >= 9 && g < 15) : (g >= 8 && g < 14);
    z.ex[t] = x && e < 32 ? e : -1;
    if (z.ex[t] >= 0) et[e++] = t;
  }
  int o = 0;
  for (int t = 0; t < 128; ++t) {
    const int g = t & 15;
    int cap = z.ex[t] >= 0 || t < 8 || (t < 48 && (g == 7 || (g >= 8 && !(g & 1)))) || t == 56 || t == 63 || t == 111 ? 0 : 1;
    z.op0[t] = o; z.nop[t] = 0;
    while (cap > 0 && o < 48 && et[2 * (o / 3) + (o % 3 == 0 ? 0 : 1)] + 2 <= t) {
      const int el = 2 * (o / 3) + o % 3;
      if (!(o % 3 < 2 && (el == 0 || el == 4))) --cap;
      ++o; ++z.nop[t]; z.last = t;
    }
  }
  for (int k = 16; k < 32; ++k) if (z.op0[et[k]] < 3 * ((k - 16) / 2 + 1)) z.last = 1000;
  return z;
}
constexpr SMS SM = sm_sched();
static_assert(SM.last <= 117);
constexpr int p_reg(int Y, int k) { return R_P + 16 * Y + 4 * ((k >> 3) * 2 + ((k >> 1) & 1)) + ((k >> 2) & 1) * 2 + (k & 1); }
template <int Y, int O> DI void sm_op() {
  constexpr int k = O / 3, e = 2 * k + O % 3, ts = R_TS + ((e >> 2) & 1);
  if constexpr (O % 3 == 2) asm volatile("v_cvt_pk_bf16_f32 v%c0, v%c1, v%c2" ::"i"(p_reg(Y, k)), "i"(R_E + ((2 * k) & 15)), "i"(R_E + ((2 * k + 1) & 15)));
  else if constexpr (e == 1 || e == 5) asm volatile("v_add_f32_e32 v%c0, v%c1, v%c2" ::"i"(ts), "i"(R_E + e - 1), "i"(R_E + e));
  else if constexpr (e != 0 && e != 4) asm volatile("v_add_f32_e32 v%c0, v%c0, v%c1" ::"i"(ts), "i"(R_E + (e & 15)));
}
template <int Y, int T> DI void sm_gap(AC& c) {
  if constexpr (SM.ex[T] >= 0) asm volatile("v_exp_f32 v%c0, v%c1" ::"i"(R_E + (SM.ex[T] & 15)), "i"(R_S + 32 * Y + SM.ex[T]));
  if constexpr (SM.nop[T] > 0) sm_op<Y, SM.op0[T]>();
  if constexpr (SM.nop[T] > 1) sm_op<Y, SM.op0[T] + 1>();
  if constexpr (SM.nop[T] > 2) sm_op<Y, SM.op0[T] + 2>();
  if constexpr (T == SM.last + 1) asm volatile("v_add_f32_e32 v%c0, v%c1, v%c2" ::"i"(R_TMP), "i"(R_TS), "i"(R_TS + 1));
  if constexpr (T == SM.last + 2) asm volatile("v_cmp_nle_f32_e64 s[54:55], v%c0, v%c1" ::"i"(R_TMP), "i"(R_THR));
}
template <int X, int MODE, int G> DI void gapA(AC& c) {
  constexpr bool FIRST = MODE == 1 || MODE == 2, NOQK = MODE == 3;
  constexpr int Xp = X ^ 1, kb = G >> 4, g = G & 15, qb = g >> 3, s = g & 7;
  if constexpr (MODE == 3 && G < 48 && G % 3 == 0) {
    constexpr int f = G / 3;
    asm volatile("buffer_load_dwordx4 a[%c0:%c1], v84, s[88:91], s75 offen offset:%2 sc1" ::"i"(128 + 4 * f), "i"(131 + 4 * f), "i"((f & 3) * 1024) : "memory");
    if constexpr ((f & 3) == 3) asm volatile("s_addk_i32 s75, %0" ::"i"(f == 7 ? 28672 : 4096));
  }
  if constexpr (G == 48) asm volatile("s_waitcnt vmcnt(%0) lgkmcnt(0)\n\ts_barrier" ::"i"(MODE == 3 ? 16 : MODE == 2 ? 12 : MODE == 1 ? 7 : 0) : "memory");
  else if constexpr (g == 0 && !NOQK) asm volatile("s_waitcnt lgkmcnt(0)");
  if constexpr (!NOQK) {
    constexpr int r = 192 + 4 * ((kb & 1) * 8 + s), q = 128 + 4 * g;
    constexpr int d = R_S + 32 * X + 4 * (kb * 2 + qb), ci = s == 0 ? R_CI + 4 * (kb * 2 + qb) : d;
    if constexpr (FIRST && s == 0)
      asm volatile("v_mfma_f32_16x16x32_bf16 v[%c0:%c1], a[%c2:%c3], a[%c4:%c5], 0" ::"i"(d), "i"(d + 3), "i"(r), "i"(r + 3), "i"(q), "i"(q + 3));
    else
      asm volatile("v_mfma_f32_16x16x32_bf16 v[%c0:%c1], a[%c2:%c3], a[%c4:%c5], v[%c6:%c7]" ::"i"(d), "i"(d + 3), "i"(r), "i"(r + 3), "i"(q), "i"(q + 3),
                   "i"(ci), "i"(ci + 3));
    if constexpr (g < 8 && kb < 3) kread<X, (kb + 1) * 8 + g>();
    constexpr int S0 = MODE == 1 ? 0 : SMV ? 40 : 14, S1 = MODE == 1 ? 1 : SMV ? 41 : 30, S2 = SMV ? 42 : 45, S3 = SMV ? 43 : 46;
    if constexpr (G == S0) asm volatile("s_add_u32 s96, s97, 0x1000000");
    if constexpr (G == S1) dma_m0<X, 0>();
    if constexpr (G == S2) asm volatile("s_mov_b32 s97, s98");
    if constexpr (G == S3) asm volatile("s_add_u32 s98, s98, s99");
    if constexpr (MODE != 1 && G >= 56 && !(G & 1)) dma_at<(G - 56) / 2>();
    if constexpr (MODE == 1 && (G & 11) == 9) dma_at<(G >> 4) * 2 + ((G >> 2) & 1)>();
    if constexpr (MODE == 1 && G == 31) dma_m0<X, 4>();
    if constexpr (G == 57) asm volatile("s_lshr_b32 s100, s100, 1\n\ts_add_u32 s56, s56, 1");
    if constexpr (G == 59) asm volatile("s_getpc_b64 s[86:87]\nev_pc%c0:\n\ts_add_u32 s86, s86, ev_ret%c0-ev_pc%c0\n\ts_addc_u32 s87, s87, 0" ::"i"(X * 4 + MODE));
    if constexpr (G == 61 && !FIRST) asm volatile("s_or_b32 s57, s54, s55\n\ts_mov_b32 %0, s57\n\ts_bitcmp1_b32 s100, 0" : "=s"(c.rare));
    if constexpr (G == 61 && FIRST) asm volatile("s_bitcmp1_b32 s100, 0");
  } else if constexpr (SM.nop[64 + G] > 0) asm volatile("s_nop 0");
  if constexpr (!FIRST && g < 8 && kb == 3) vread<Xp, g>();
  if constexpr (MODE == 2 && (G & 3) == 0) d_store<16 + G / 4>();
  if constexpr (!FIRST) sm_gap<Xp, 64 + G>(c);
  if constexpr (G == 63 && !NOQK) asm volatile("s_cbranch_scc1 ev_entry\nev_ret%c0:" ::"i"(X * 4 + MODE) : "vcc", "memory");
  GAP();
}
template <int R> DI void rescale1(float alpha) {
  float t;
  asm volatile("v_accvgpr_read_b32 %0, a[%c2]\n\tv_mul_f32 %0, %0, %1\n\tv_accvgpr_write_b32 a[%c2], %0" : "=&v"(t) : "v"(alpha), "i"(R));
}
template <int... Rs> DI void rescale_all(const float (&alpha)[2], iseq<Rs...>) { (rescale1<Rs>(alpha[(Rs >> 2) & 1]), ...); }
template <bool MAX> DI float row_reduce(float v) {
  auto p = __builtin_amdgcn_permlane32_swap(__float_as_uint(v), __float_as_uint(v), false, false);
  v = MAX ? fmaxf(__uint_as_float(p[0]), __uint_as_float(p[1])) : __uint_as_float(p[0]) + __uint_as_float(p[1]);
  p = __builtin_amdgcn_permlane16_swap(__float_as_uint(v), __float_as_uint(v), false, false);
  return MAX ? fmaxf(__uint_as_float(p[0]), __uint_as_float(p[1])) : __uint_as_float(p[0]) + __uint_as_float(p[1]);
}
template <int Y, int QB> DI float row_max() {
  constexpr int r = R_S + 32 * Y + 4 * QB;
  float mx;
  asm volatile("v_max_f32 %0, v%c1, v%c2\n\tv_max3_f32 %0, %0, v%c3, v%c4" : "=&v"(mx) : "i"(r), "i"(r + 1), "i"(r + 2), "i"(r + 3));
  static_for([&](auto K) {
    constexpr int q = r + 8 * (K + 1);
    float t = mx;
    asm volatile("v_max3_f32 %0, %0, v%c1, v%c2\n\tv_max3_f32 %0, %0, v%c3, v%c4" : "+v"(t) : "i"(q), "i"(q + 1), "i"(q + 2), "i"(q + 3));
    mx = t;
  }, make_iseq<3>{});
  return row_reduce<true>(mx);
}
template <int R> DI void v_sub(float d) { asm volatile("v_sub_f32 v%c0, v%c0, %1" ::"i"(R), "v"(d)); }
template <int B> DI void s_shift(const float (&d)[2]) { static_for([&](auto E) { v_sub<B + E>(d[(E >> 2) & 1]); }, make_iseq<32>{}); }
template <bool CI> DI void m_shift(const float (&d)[2]) {
  if constexpr (CI) s_shift<R_CI>(d);
  v_sub<R_M>(-d[0]); v_sub<R_M + 1>(-d[1]);
}
DI void set_mask0() {
  const int mr = __float_as_int(vget<89>());
  static_for([&](auto I) {
    constexpr int e = (I >> 2) * 8 + (I & 3), k = (I >> 2) * 16 + (I & 3);
    const float x = vget<R_S + e>();
    vset<R_S + e>(k > mr ? -INFINITY : x);
  }, make_iseq<16>{});
}
template <int X, bool CI = true> DI void sm_redo() {
  constexpr int Xp = X ^ 1;
  float d[2], dt[2], alpha[2], ts[2] = {0.f, 0.f};
  static_for([&](auto Q) {
    constexpr int qb = Q;
    const float pd = vget<R_PD + qb>();
    d[qb] = fmaxf(row_max<Xp, qb>() - pd, 0.f);
    dt[qb] = d[qb] + pd;
    vset<R_PD + qb>(d[qb]);
    alpha[qb] = __builtin_amdgcn_exp2f(-d[qb]);
    vset<R_RS + qb>(vget<R_RS + qb>() * alpha[qb]);
  }, make_iseq<2>{});
  vset<R_THR>(d[0] > 0.f || d[1] > 0.f ? -1.f : 4096.f);
  m_shift<CI>(d);
  static_for([&](auto K) {
    constexpr int k = K, qb = (k >> 1) & 1;
    const float e0 = __builtin_amdgcn_exp2f(vget<R_S + 32 * Xp + 2 * k>() - dt[qb]), e1 = __builtin_amdgcn_exp2f(vget<R_S + 32 * Xp + 2 * k + 1>() - dt[qb]);
    ts[qb] += e0; ts[qb] += e1;
    vset<p_reg(Xp, k)>(__uint_as_float(pk2(e0, e1)));
  }, make_iseq<16>{});
  vset<R_TS>(ts[0]); vset<R_TS + 1>(ts[1]);
  rescale_all(alpha, make_iseq<128>{});
  asm volatile("s_nop 1" ::: "memory");
  GAP();
}
template <int X, int MODE, int... Gs> DI void phaseA_(AC& c, iseq<Gs...>) { (gapA<X, MODE, Gs>(c), ...); }
template <int X, int MODE> DI void phaseA(AC& c) { phaseA_<X, MODE>(c, make_iseq<64>{}); }

template <int X, bool SMX, int G, bool FIRST = false> DI void gapB(AC& c) {
  constexpr int Xp = X ^ 1, blk = G >> 4, g = G & 15;
  if constexpr (g == 0) asm volatile("s_waitcnt lgkmcnt(0)");
  constexpr int qb = (g >> 1) & 1, s = g & 1, r = 192 + 4 * ((blk & 1) * 8 + (g >> 2) * 2 + s), o = 4 * ((blk * 4 + (g >> 2)) * 2 + qb);
  constexpr int p = R_P + 16 * Xp + 4 * (s * 2 + qb);
  if constexpr (FIRST && s == 0)
    asm volatile("v_mfma_f32_16x16x32_bf16 a[%c0:%c1], a[%c2:%c3], v[%c4:%c5], 0" ::"i"(o), "i"(o + 3), "i"(r), "i"(r + 3), "i"(p), "i"(p + 3));
  else
  asm volatile("v_mfma_f32_16x16x32_bf16 a[%c0:%c1], a[%c2:%c3], v[%c4:%c5], a[%c0:%c1]" ::"i"(o), "i"(o + 3), "i"(r), "i"(r + 3), "i"(p), "i"(p + 3));
  if constexpr (g < 8) {
    if constexpr (blk < 3) vread<Xp, (blk + 1) * 8 + g>();
    else if constexpr (SMX) kread<Xp, g>();
  }
  if constexpr (SMX) {
    if constexpr (G == 1 || G == 2) asm volatile("v_add_f32_e32 v%c0, v%c0, v%c1" ::"i"(R_RS + G - 1), "i"(R_TS + G - 1));
    if constexpr (g == 7 && blk < 3) dma_m0<X, blk * 4 + 4>();
    if constexpr (g >= 8 && !(g & 1) && blk < 3) dma_at<blk * 4 + 4 + (g - 8) / 2>();
    if constexpr (G == 55) asm volatile("s_cmp_eq_u32 s56, s76");
    if constexpr (G == 3) asm volatile("s_lshr_b32 s77, s77, 1");
    if constexpr (G == 4) asm volatile("s_bitcmp1_b32 s77, 0");
    if constexpr (G == 5) asm volatile("s_cselect_b64 s[52:53], s[62:63], 0");
    if constexpr (G == 6) asm volatile(FL_ARM("s[52:53]"));
    if constexpr (G == 56) asm volatile(FL_POLL ::: "memory");
    sm_gap<X, G>(c);
  }
  GAP();
}
template <int X, bool FIRST, int... Gs> DI void phaseB_(AC& c, iseq<Gs...>) { (gapB<X, true, Gs, FIRST>(c), ...); }
template <int X, bool FIRST = false> DI void phaseB(AC& c) { phaseB_<X, FIRST>(c, make_iseq<64>{}); }
constexpr int sm_nex() { int n = 0; for (int t = 0; t < 64; ++t) n += SM.ex[t] >= 0; return n; }
template <int X> DI void phaseB0(unsigned mode, bool vh) {
  dma_m0<X, 4>();
  kreads<X ^ 1>(make_iseq<8>{});
  if (vh) { dma_at<4>(); dma_at<5>(); dma_at<6>(); dma_at<7>(); }
  dma_m0<X, 8>();
  asm volatile("s_nop 3");
  float d[2];
  if (mode & 2) {
    d[0] = vget<122>(); d[1] = vget<123>();
    dma_at<8>();
  } else {
    if constexpr (X == 0) if (__builtin_expect(mode & 4, 0)) set_mask0();
    d[0] = row_max<X, 0>(); GAP(); dma_at<8>();
    d[1] = row_max<X, 1>();
#pragma unroll
    for (int qb = 0; qb < 2; ++qb) if (!(d[qb] > -3.0e38f)) d[qb] = 0.f;
  }
  GAP();
  vset<R_M>(d[0]); vset<R_M + 1>(d[1]);
  dma_at<9>();
  const f32x2c nd[2] = {{-d[0], -d[0]}, {-d[1], -d[1]}};
  static_for([&](auto E) {
    constexpr int e = E;
    const f32x2c v = nd[(e >> 1) & 1];
    asm volatile("v_pk_add_f32 v[%c0:%c1], v[%c0:%c1], %4\n\tv_mov_b64 v[%c2:%c3], %4" ::"i"(R_S + 32 * X + 2 * e), "i"(R_S + 32 * X + 2 * e + 1), "i"(R_CI + 2 * e), "i"(R_CI + 2 * e + 1), "v"(v));
    if constexpr (e == 7 || e == 15) dma_at<10 + e / 8>();
  }, make_iseq<16>{});
  static_assert(SM.op0[64] >= 3 * ((sm_nex() - 15) / 2) && SM.op0[64] <= 24);
  static_for([&](auto E) {
    constexpr int e = E;
    if constexpr (e < sm_nex()) asm volatile("v_exp_f32 v%c0, v%c1" ::"i"(R_E + e), "i"(R_S + 32 * X + e));
    if constexpr (e == 2) dma_m0<X, 12>();
    if constexpr (e == 5 || e == 10 || e == 15) dma_at<11 + e / 5>();
  }, make_iseq<16>{});
  static_for([&](auto O) { sm_op<X, O>(); }, make_iseq<SM.op0[64]>{});
  dma_at<15>();
  static_for([&](auto E) { asm volatile("v_exp_f32 v%c0, v%c1" ::"i"(R_E + (int)E), "i"(R_S + 32 * X + 16 + (int)E)); }, make_iseq<(sm_nex() > 16 ? sm_nex() - 16 : 0)>{});
  asm volatile("buffer_store_dwordx2 v[%c0:%c1], v88, s[88:91], s76 offen sc1\n\ts_bitcmp1_b32 s77, 0\n\ts_cselect_b64 s[52:53], s[62:63], 0\n\t"
               FL_ARM("s[52:53]") "\n\t" FL_POLL ::"i"(R_M), "i"(R_M + 1) : "memory", "scc");
  GAP();
}
enum : int { T_Q0, T_K0, T_OQ, T_OG, T_FPREP, T_FQ, T_FG, T_FS, T_ZO, T_CNT, T_NPRE = 10, T_NST = 12, T_TAB = 14, T_SLAB = 16, T_PART, T_IT = 18, T_ITN = 23 };
#define MSK(R, K) "v_cmp_gt_i32 s[48:49], " #K ", v89\n\tv_cmp_gt_i32 s[50:51], " #K "+1, v89\n\tv_cmp_gt_i32 s[52:53], " #K "+2, v89\n\t" \
                  "v_cmp_gt_i32 s[54:55], " #K "+3, v89\n\tv_cndmask_b32_e64 v[" #R "], v[" #R "], v122, s[48:49]\n\t" \
                  "v_cndmask_b32_e64 v[" #R "+1], v[" #R "+1], v122, s[50:51]\n\tv_cndmask_b32_e64 v[" #R "+2], v[" #R "+2], v122, s[52:53]\n\t" \
                  "v_cndmask_b32_e64 v[" #R "+3], v[" #R "+3], v122, s[54:55]\n\t"
DI void ev_code(AC& c) {
  GAP();
  asm volatile(
    "s_getpc_b64 s[82:83]\n\ts_add_u32 s82, s82, 12\n\ts_addc_u32 s83, s83, 0\n\ts_branch 9f\nev_entry:\n\t"
    "s_bitcmp1_b32 s60, s56\n\ts_cbranch_scc1 3f\n\ts_bitcmp1_b32 s74, s56\n\ts_cbranch_scc1 2f\n\t"
    "s_bitcmp1_b32 s58, s56\n\ts_cselect_b64 vcc, -1, 0\n\tv_cndmask_b32 v121, -2.0, v81, vcc\n\t"
    "buffer_store_dwordx2 v[82:83], v121, s[88:91], s70 offen sc1\n\tbuffer_store_dwordx2 v[82:83], v121, s[88:91], s71 offen sc1\n\t"
    "v_cmp_eq_u64 vcc, s[84:85], v[124:125]\n\ts_andn2_b64 vcc, s[62:63], vcc\n\ts_cbranch_scc1 5f\n"
    "0:\n\ts_add_u32 s98, s98, s59\n\ts_mov_b32 s59, s61\n\ts_mov_b32 s61, s72\n\ts_mov_b64 s[62:63], s[64:65]\n\ts_mov_b64 s[64:65], s[66:67]\n\t"
    "s_mov_b64 s[66:67], s[68:69]\n\ts_cmp_eq_u32 s56, 1\n\ts_cbranch_scc1 ev_first\n\ts_setpc_b64 s[86:87]\n"
    "5:\n\t" FL_CHECK("s[62:63]") "\n\ts_branch 0b\n"
    "6:\n\t" FL_CHECK("s[62:63]") "\n\ts_setpc_b64 s[86:87]\n"
    "2:\n\ts_mov_b32 s98, 0\n\t"
    "v_lshrrev_b32 v90, 7, v86\n\tv_and_b32 v91, 6, v90\n\tv_lshlrev_b32 v91, 4, v91\n\tv_and_b32 v92, 0x70, v86\n\tv_xor_b32 v91, v91, v92\n\t"
    "s_movk_i32 s48, 896\n\tv_mad_u32_u24 v94, v90, s48, v91\n\ts_mov_b64 s[92:93], s[80:81]\n\ts_mov_b32 s94, 229376\n\t"
    "v_add_u32 v90, 28672, v94\n\tv_add_u32 v91, 34816, v94\n\tv_add_u32 v92, 40960, v94\n\tv_add_u32 v93, 47104, v94\n\t"
    "v_add_u32 v95, 6144, v94\n\tv_add_u32 v126, 12288, v94\n\tv_add_u32 v127, 18432, v94\n\t"
    "v_mov_b32 v122, 0xff800000\n\t" MSK(228, 0) MSK(236, 16) MSK(244, 32) MSK(252, 48)
    "v_cmp_eq_u64 vcc, s[84:85], v[124:125]\n\ts_andn2_b64 vcc, s[62:63], vcc\n\ts_cbranch_scc1 6b\n\ts_setpc_b64 s[86:87]\n"
    "3:\n\tv_mov_b32 v122, 0xff800000\n\t" MSK(224, 0) MSK(232, 16) MSK(240, 32) MSK(248, 48)
    "v_readlane_b32 s54, v78, %0\n\ts_add_u32 m0, s78, 0x10000\n\t"
    "s_bfe_u32 s53, s73, 0x80010\n\ts_mulk_i32 s53, 3584\n\ts_mov_b64 s[48:49], s[80:81]\n\ts_add_u32 s50, s53, 3584\n\t"
    "s_mov_b32 s51, 0x20000\n\t"
    "v_lshlrev_b32 v121, 3, v84\n\tbuffer_load_dword v121, s[48:51], s53 offen lds\n\t"
    "s_bitcmp0_b32 s73, 3\n\ts_cbranch_scc1 4f\n\t"
    "buffer_load_dwordx4 v84, s[88:91], s54 offen sc1 lds\n\tbuffer_load_dwordx4 v84, s[88:91], s54 offen offset:1024 sc1 lds\n\t"
    "buffer_load_dwordx4 v84, s[88:91], s54 offen offset:2048 sc1 lds\n\tbuffer_load_dwordx4 v84, s[88:91], s54 offen offset:3072 sc1 lds\n"
    "4:\n\ts_cmp_eq_u32 s56, 1\n\ts_cbranch_scc1 ev_first\n\ts_setpc_b64 s[86:87]\n"
    ::"i"((int)T_ZO) : "scc");
  asm volatile("ev_zero:");
  acc_zero();
  asm volatile("s_setpc_b64 s[86:87]\nev_first:\n\ts_cmp_lg_u32 s57, 0\n\ts_cbranch_scc1 ev_zero\n\ts_bitcmp0_b32 s73, 4\n\ts_cbranch_scc1 ev_first1");
  GAP();
  phaseB<0, true>(c);
  asm volatile("s_branch att_loop\nev_first1:");
  phaseB<1, true>(c);
  asm volatile("s_branch att_mid\n9:");
  GAP();
}
DI void ev_call() { asm volatile("s_swappc_b64 s[86:87], s[82:83]" ::: "vcc", "scc", "memory"); GAP(); }

#define ACLOB_ALL "a0","a1","a2","a3","a4","a5","a6","a7","a8","a9","a10","a11","a12","a13","a14","a15","a16","a17","a18","a19","a20","a21","a22","a23","a24","a25","a26","a27","a28","a29","a30","a31","a32","a33","a34","a35","a36","a37","a38","a39","a40","a41","a42","a43","a44","a45","a46","a47","a48","a49","a50","a51","a52","a53","a54","a55","a56","a57","a58","a59","a60","a61","a62","a63","a64","a65","a66","a67","a68","a69","a70","a71","a72","a73","a74","a75","a76","a77","a78","a79","a80","a81","a82","a83","a84","a85","a86","a87","a88","a89","a90","a91","a92","a93","a94","a95","a96","a97","a98","a99","a100","a101","a102","a103","a104","a105","a106","a107","a108","a109","a110","a111","a112","a113","a114","a115","a116","a117","a118","a119","a120","a121","a122","a123","a124","a125","a126","a127","a128","a129","a130","a131","a132","a133","a134","a135","a136","a137","a138","a139","a140","a141","a142","a143","a144","a145","a146","a147","a148","a149","a150","a151","a152","a153","a154","a155","a156","a157","a158","a159","a160","a161","a162","a163","a164","a165","a166","a167","a168","a169","a170","a171","a172","a173","a174","a175","a176","a177","a178","a179","a180","a181","a182","a183","a184","a185","a186","a187","a188","a189","a190","a191","a192","a193","a194","a195","a196","a197","a198","a199","a200","a201","a202","a203","a204","a205","a206","a207","a208","a209","a210","a211","a212","a213","a214","a215","a216","a217","a218","a219","a220","a221","a222","a223","a224","a225","a226","a227","a228","a229","a230","a231","a232","a233","a234","a235","a236","a237","a238","a239","a240","a241","a242","a243","a244","a245","a246","a247","a248","a249","a250","a251","a252","a253","a254","a255"

struct EP { float fa[2], fb[2]; };
template <int I, int E, int MODE> DI void epi_pair(const EP& p) {
  constexpr int qb = I & 1, r = 16 * (I >> 1) + 4 * qb + (E >> 1) * 8 + (E & 1) * 2, t = 96 + 2 * E, w = 128 + 4 * I + E;
  if constexpr (MODE == 2) asm volatile("v_accvgpr_read_b32 v106, a[%c0]" ::"i"(w));
  asm volatile("v_accvgpr_read_b32 v%c0, a[%c2]\n\tv_accvgpr_read_b32 v%c1, a[%c3]" ::"i"(t), "i"(t + 1), "i"(r), "i"(r + 1));
  if constexpr (MODE) asm volatile("v_mul_f32 v%c0, v%c0, %2\n\tv_mul_f32 v%c1, v%c1, %2" ::"i"(t), "i"(t + 1), "v"(p.fa[qb]));
  if constexpr (MODE == 2)
    asm volatile("v_lshlrev_b32 v104, 16, v106\n\tv_and_b32 v105, 0xffff0000, v106\n\tv_fmac_f32 v%c0, v104, %2\n\tv_fmac_f32 v%c1, v105, %2"
                 ::"i"(t), "i"(t + 1), "v"(p.fb[qb]));
  asm volatile("v_cvt_pk_bf16_f32 v%c0, v%c1, v%c2" ::"i"(w), "i"(t), "i"(t + 1));
}
template <int I> DI void epi_store() {
  asm volatile("buffer_store_dwordx4 v[%c0:%c1], v84, s[88:91], s101 offen offset:%2 sc1" ::"i"(128 + 4 * I), "i"(131 + 4 * I), "i"((I & 3) * 1024) : "memory");
  if constexpr ((I & 3) == 3) asm volatile("s_addk_i32 s101, 4096");
}
constexpr int R_TB = 232, R_VS = 236;
template <int HF, int R> DI void p_read() {
  constexpr int mb = R >> 4, r = 192 - 64 * HF + 2 * R;
  asm volatile("ds_read_b64_tr_b16 a[%c1:%c2], v%c0 offset:%c3" ::"i"(R_TB + 2 * HF + mb), "i"(r), "i"(r + 1), "i"((R & 15) * 2048));
}
template <int QB, int E> DI void p_cvt() {
  asm volatile("v_cvt_pk_bf16_f32 v%c0, v%c1, v%c2" ::"i"(208 + 4 * QB + E), "i"(192 + 8 * QB + 2 * E), "i"(193 + 8 * QB + 2 * E));
}
template <int QB, int PR> DI void p_store() {
  asm volatile("buffer_store_dwordx4 v[%c0:%c1], v%c2, s[88:91], s96 offen offset:%3 sc1" ::"i"(208 + 4 * QB), "i"(211 + 4 * QB), "i"(R_VS + QB),
               "i"(PR * 128) : "memory");
}
constexpr int p_dma(int HF, int G) { return G == 3 && !HF ? 0 : G == 11 ? 1 - HF : G == 15 ? 2 - HF : G == 19 ? 3 - HF : G == 27 && HF ? 3 : -1; }
template <int HF, bool LD, int G> DI void p_gap() {
  constexpr int ch = G >> 3, t = G & 7, a = 192 - 64 * HF + 32 * (ch & 1) + 4 * t, b = 128 + 4 * (2 * t + (ch >> 1)), d = 192 + 4 * ch;
  if constexpr (t == 0)
    asm volatile("v_mfma_f32_16x16x32_bf16 v[%c0:%c1], a[%c2:%c3], v[%c4:%c5], 0" ::"i"(d), "i"(d + 3), "i"(a), "i"(a + 3), "i"(b), "i"(b + 3));
  else
    asm volatile("v_mfma_f32_16x16x32_bf16 v[%c0:%c1], a[%c2:%c3], v[%c4:%c5], v[%c0:%c1]" ::"i"(d), "i"(d + 3), "i"(a), "i"(a + 3), "i"(b), "i"(b + 3));
  p_read<HF ^ 1, G>();
  if constexpr (G == 0) asm volatile("v_xor_b32 v%c0, s100, v%c0" ::"i"(R_TB + 2 * HF + 1));
  if constexpr (G == 17) asm volatile("v_xor_b32 v%c0, s100, v%c0" ::"i"(R_TB + 2 * (HF ^ 1)));
  if constexpr (G >= 4 && G <= 8 && G != 7) p_cvt<1, (G < 7 ? G - 4 : 3)>();
  if constexpr (G == 24 || (G == 7 && HF == 0)) asm volatile("s_waitcnt lgkmcnt(8)");
  if constexpr (G == 7 && HF == 1) asm volatile("s_waitcnt vmcnt(%0) lgkmcnt(8)\n\ts_barrier" ::"i"(LD ? 13 : 5) : "memory");
  if constexpr (G == 10) p_store<1, HF ^ 1>();
  if constexpr (G >= 20 && G < 24) p_cvt<0, G - 20>();
  if constexpr (G == 25) p_store<0, HF>();
  if constexpr (LD && p_dma(HF, G) >= 0) dma_at<(HF ? 8 : 12) + p_dma(HF, G)>();
  if constexpr (LD && HF == 0 && G == 2) asm volatile("s_bitset0_b32 m0, 12");
  if constexpr (HF == 0 && G == 13) asm volatile("s_add_u32 s96, s96, 0x8000");
  if constexpr (HF == 0 && G == 26) asm volatile("s_add_u32 s98, s98, 128");
  if constexpr (LD && HF == 0 && G == 27) asm volatile("s_cmp_lt_u32 s98, 0x380");
  if constexpr (HF == 1 && G == 1) asm volatile("s_xor_b32 s100, s100, 0x10000");
  if constexpr (LD && HF == 1 && G == 2) asm volatile("s_xor_b32 s97, s97, s100");
  if constexpr (LD && HF == 1 && G == 9) asm volatile("s_or_b32 m0, s97, 0x1000");
  if constexpr (!LD && HF == 1 && G == 30) asm volatile("s_cmp_lt_u32 s98, 0x500");
  GAP();
}
template <int HF, bool LD, int... Gs> DI void p_half(iseq<Gs...>) { (p_gap<HF, LD, Gs>(), ...); }
template <int G> DI void gapF(AC& c, const EP& p) {
  if constexpr (G < 64) gapB<0, false, G>(c);
  if constexpr (G >= 10) epi_pair<(G - 10) / 4, (G - 10) & 3, 0>(p);
  if constexpr (G >= 14 && ((G - 14) & 3) == 0) epi_store<(G - 14) / 4>();
  if constexpr (G == 73) epi_store<15>();
  GAP();
}
template <int... Gs> DI void phaseF(AC& c, const EP& p, iseq<Gs...>) { (gapF<Gs>(c, p), ...); }
template <int G> DI void gapAL(AC& c) {
  constexpr int kb = G >> 3, s = G & 7, r = 192 + 4 * (G & 15), q = 160 + 4 * s, d = R_S + 36 + 8 * kb, ci = s == 0 ? R_CI + 4 + 8 * kb : d;
  if constexpr (G == 0) asm volatile("s_add_u32 s96, s97, 0x1000000\n\ts_mov_b32 m0, s79\n\ts_waitcnt vmcnt(9) lgkmcnt(0)\n\ts_barrier" ::: "memory");
  if constexpr (G >= 8 && (G & 3) == 0) asm volatile("s_waitcnt lgkmcnt(4)");
  if constexpr (G == 29) asm volatile("s_barrier" ::: "memory");
  asm volatile("v_mfma_f32_16x16x32_bf16 v[%c0:%c1], a[%c2:%c3], a[%c4:%c5], v[%c6:%c7]" ::"i"(d), "i"(d + 3), "i"(r), "i"(r + 3), "i"(q), "i"(q + 3),
               "i"(ci), "i"(ci + 3));
  if constexpr (G < 24) kread<1, G + 8>();
  else vread<0, G - 24>();
  if constexpr (G % 3 == 1 && G < 24) dma_at<G / 3>();
  if constexpr (G == 11) asm volatile("s_bitset1_b32 m0, 12");
  if constexpr (G == 28) asm volatile("buffer_load_dwordx4 v[122:125], v88, s[88:91], s76 offen sc1" ::: "memory");
  if constexpr (G == 30) asm volatile("s_xor_b32 m0, s79, 0x11000\n\ts_movk_i32 s98, 128");
  sm_gap<0, 64 + 2 * G>(c); sm_gap<0, 65 + 2 * G>(c);
  GAP();
}
template <int... Gs> DI void phaseAL(AC& c, iseq<Gs...>) { (gapAL<Gs>(c), ...); }
struct BLS { int ex[65], op[65], vm[65], su[65]; };
constexpr BLS bl_sched(bool LD) {
  BLS z{};
  z.op[0] = 1;
  for (int t = 0; t < 64; ++t) {
    const int g = t & 15, o = z.op[t];
    const bool x = t == 6 || (t < 48 && g > 8 && g != 12 && g != 15);
    const bool v = LD ? (t & 3) == 0 : (t & 11) == 8;
    const bool p = t >= 15 && o < 24 && (t & 3) != 0 && t != 47 && !x && z.ex[t - 1] > 2 * (o / 3) + (o % 3 != 0);
    z.ex[t + 1] = z.ex[t] + x; z.vm[t + 1] = z.vm[t] + v; z.op[t + 1] = o + p;
    z.su[t + 1] = z.su[t] + (t >= 1 && z.su[t] < (LD ? 16 : 10) && g != 15 && !v && !x && (LD || t > 55 || (t > 3 && !p)));
  }
  return z;
}
template <bool LD> constexpr BLS BL = bl_sched(LD);
static_assert(BL<true>.ex[64] == 16 && BL<true>.op[61] == 24 && BL<true>.vm[64] == 16 && BL<false>.vm[64] == 8);
static_assert(BL<true>.su[56] == 16 && BL<false>.su[62] == 10);
template <bool LD, int G> DI void gapBL(AC& c) {
  if constexpr (G == 48) asm volatile("s_waitcnt vmcnt(%0) lgkmcnt(0)\n\ts_barrier" ::"i"(BL<LD>.vm[48]) : "memory");
  gapB<1, false, G>(c);
  if constexpr (G >= 48 && G < 56) vread<1, G - 48>();
  if constexpr (BL<LD>.vm[G + 1] > BL<LD>.vm[G]) {
    constexpr int k = BL<LD>.vm[G] + (LD ? 0 : 16), I = 2 * (k & 7) + ((k >> 3) & 1);
    if constexpr (k < 16) {
      asm volatile("buffer_load_dwordx4 a[%c0:%c1], v84, s[88:91], s75 offen offset:%2 sc1" ::"i"(128 + 4 * I), "i"(131 + 4 * I), "i"((I & 3) * 1024) : "memory");
      if constexpr (k & 1) asm volatile("s_addk_i32 s75, %0" ::"i"(k == 7 ? -12288 : 4096));
    } else dma_at<k - 8>();
    if constexpr (k == 19) asm volatile("s_bitset0_b32 m0, 12");
  }
  if constexpr (G == 1 || G == 2) asm volatile("v_add_f32_e32 v%c0, v%c0, v%c1" ::"i"(R_RS + G - 1), "i"(R_TS + G - 1));
  if constexpr (G == 3) asm volatile("v_mov_b32 v%c0, 0" ::"i"(R_TS));
  if constexpr (BL<LD>.ex[G + 1] > BL<LD>.ex[G]) {
    constexpr int j = BL<LD>.ex[G];
    asm volatile("v_exp_f32 v%c0, v%c1" ::"i"(R_E + j), "i"(R_S + 36 + 8 * (j >> 2) + (j & 3)));
  }
  if constexpr (BL<LD>.op[G + 1] > BL<LD>.op[G]) {
    constexpr int o = BL<LD>.op[G], k = o / 3, j = 2 * k + o % 3;
    if constexpr (o % 3 == 2) asm volatile("v_cvt_pk_bf16_f32 v%c0, v%c1, v%c2" ::"i"(p_reg(1, (k >> 1) * 4 + 2 + (k & 1))), "i"(R_E + 2 * k), "i"(R_E + 2 * k + 1));
    else asm volatile("v_add_f32_e32 v%c0, v%c1, v%c2" ::"i"(R_TS + 1), "i"(j == 1 ? R_E : R_TS + 1), "i"(R_E + j));
  }
  if constexpr (G == 61) asm volatile("v_cmp_nle_f32_e64 s[54:55], v%c0, v%c1" ::"i"(R_TS + 1), "i"(R_THR));
  if constexpr (BL<LD>.su[G + 1] > BL<LD>.su[G]) {
    constexpr int k = BL<LD>.su[G];
    if constexpr (k < 4) asm volatile("v_xor_b32 v%c0, %1, %2" ::"i"(R_TB + k), "i"(32 * k), "v"(c.tb));
    else if constexpr (k < 6) asm volatile("v_add_u32 v%c0, %1, %2" ::"i"(R_VS + k - 4), "i"(4096 * (k - 4)), "v"(c.vs));
    else if constexpr (k == 6) tload<96, T_SLAB>();
    else if constexpr (k == 7) tload<101, T_CNT>();
    else if constexpr (k == 8) asm volatile("v_mov_b64 v[240:241], 1");
    else if constexpr (k == 9) asm volatile("s_mov_b32 s97, s79\n\ts_mov_b32 s100, 0x8000");
    else if constexpr (k < 14) asm volatile("v_mov_b32 v%c0, %1" ::"i"(k < 12 ? 214 + k : 218 + k), "v"(k & 1 ? c.o1 : c.o0));
    else asm volatile("v_mov_b64 v[%c0:%c1], 0" ::"i"(198 + 2 * k), "i"(199 + 2 * k));
  }
  GAP();
}
template <bool LD, int... Gs> DI void phaseBL(AC& c, iseq<Gs...>) { (gapBL<LD, Gs>(c), ...); }
template <int MODE, int G> DI void gapFL(const EP& p) {
  constexpr int x = G - (MODE == 3 ? 4 : 0), J = MODE == 1 ? (G < 24 && G % 3 == 1 ? G / 3 : -1) : G < 27 && G % 5 != 0 && G % 5 != 3 ? G / 5 * 3 + (G % 5) / 2 : -1;
  if constexpr (G == 0 && MODE == 1) asm volatile("s_or_b32 m0, s78, 0x1000\n\ts_movk_i32 s98, 256\n\ts_waitcnt lgkmcnt(0)");
  if constexpr (G == 0 && MODE > 1) asm volatile("s_waitcnt vmcnt(8) lgkmcnt(0)" ::: "memory");
  if constexpr (G == 16 && MODE > 1) asm volatile("s_waitcnt vmcnt(9)" ::: "memory");
  if constexpr (G >= 8 && G < 32 && (G & 3) == 0) asm volatile("s_waitcnt lgkmcnt(%0)" ::"i"(G < 28 ? 4 : 0));
  if constexpr (G < 32) {
    constexpr int o = 8 * (G >> 1) + 4, r = 192 + 4 * (G & 15), q = R_P + 20 + 8 * (G & 1);
    asm volatile("v_mfma_f32_16x16x32_bf16 a[%c0:%c1], a[%c2:%c3], v[%c4:%c5], a[%c0:%c1]" ::"i"(o), "i"(o + 3), "i"(r), "i"(r + 3), "i"(q), "i"(q + 3));
  }
  if constexpr (G < 24) vread<1, G + 8>();
  if constexpr (G == 28) asm volatile("s_waitcnt vmcnt(8)\n\ts_barrier\n\ts_or_b32 m0, s79, 0x1000\n\ts_movk_i32 s98, 384" ::: "memory");
  if constexpr (G == 29 || G == 31) dma_at<8 + (G - 29) / 2>();
  if constexpr (G == 32 || G == 40) {
    static_for([&](auto I) { p_read<0, 2 * (G - 32) + I>(); }, make_iseq<16>{});
    dma_at<10 + (G - 32) / 8>();
  }
  if constexpr (J >= 0) dma_at<8 + (J & 7)>();
  if constexpr (x >= 0) epi_pair<2 * ((x >> 2) & 7) + (x >> 5), x & 3, MODE>(p);
  if constexpr (MODE == 3 && G < 32) {
    constexpr int db = G & 15, qb = G >> 4, o = 4 * (db * 2 + qb), a = 224 + 4 * (db & 1), b = 128 + 4 * ((db >> 1) * 2 + qb);
    asm volatile("v_mfma_f32_16x16x32_bf16 a[%c0:%c1], v[%c2:%c3], a[%c4:%c5], a[%c0:%c1]" ::"i"(o), "i"(o + 3), "i"(a), "i"(a + 3), "i"(b), "i"(b + 3));
  }
  if constexpr (J == 3 || J == 11) asm volatile("s_bitset0_b32 m0, 12");
  if constexpr (J == 7 && MODE > 1) asm volatile("s_or_b32 m0, s78, 0x1000\n\ts_movk_i32 s98, 256");
  if constexpr (x == 63) asm volatile("s_waitcnt vmcnt(4) lgkmcnt(0)" ::: "memory");
  GAP();
}
template <int MODE, int... Gs> DI void phaseFL_(const EP& p, iseq<Gs...>) { (gapFL<MODE, Gs>(p), ...); }
template <int MODE> DI void phaseFL(const EP& p) { GAP(); phaseFL_<MODE>(p, make_iseq<MODE == 3 ? 68 : 64>{}); }
template <int U, int K> DI void r_load(unsigned vo, unsigned so) {
  asm volatile("buffer_load_dwordx4 a[%c0:%c1], %2, s[88:91], %3 offen offset:%4 sc1" ::"i"(64 * U + 4 * K), "i"(64 * U + 4 * K + 3), "v"(vo),
               "s"(so + (K >> 1) * 0x40000 + (U >> 1) * 4096), "i"((K & 1) * 512 + (U & 1) * 2048) : "memory");
}
template <int U, int K> DI void r_sum() {
  constexpr int d = 128 + 8 * U + 4 * (K & 1), b = 64 * U + 4 * K;
  if constexpr (K < 2) asm volatile("v_mfma_f32_16x16x32_bf16 v[%c0:%c1], v[96:99], a[%c2:%c3], 0" ::"i"(d), "i"(d + 3), "i"(b), "i"(b + 3));
  else asm volatile("v_mfma_f32_16x16x32_bf16 v[%c0:%c1], v[96:99], a[%c2:%c3], v[%c0:%c1]" ::"i"(d), "i"(d + 3), "i"(b), "i"(b + 3));
}
template <int U> DI void r_sums() { static_for([&](auto K) { r_sum<U, K>(); }, make_iseq<16>{}); }
template <int U> DI void r_fin(unsigned vo, __amdgpu_buffer_rsrc_t srd, unsigned so) {
  static_for([&](auto E) { asm volatile("v_cvt_pk_bf16_f32 v%c0, v%c1, v%c2" ::"i"(128 + 8 * U + E), "i"(128 + 8 * U + 2 * E), "i"(129 + 8 * U + 2 * E)); }, make_iseq<4>{});
  asm volatile("s_nop 0\n\tbuffer_store_dwordx4 v[%c3:%c4], %0, %1, %2 offen sc0 sc1" ::"v"(vo), "s"(srd), "s"(so), "i"(128 + 8 * U), "i"(131 + 8 * U) : "memory");
}

#define CMAX 12
#define NSPL 5
#define NYT 15
#define NYM 0
struct TRow { unsigned v[64]; };
constexpr u64 kvmask(int m0, int m1) { u64 n = 0; for (int m = m0; m <= m1; ++m) n |= 0x101ull << m; return n; }
constexpr u64 k_order(bool isY, int rr) {
  const int cls = (rr + (rr >> 2)) & 3;
#define NCM 3
  u64 t = 0;
  int n = 0;
  auto put = [&](int k) { t |= (u64)k << (4 * n++); };
  if (isY) {
    for (int k = 1; k <= 4; ++k) put(NCM + 4 + (k & 3));
    for (int k = 1; k <= 4; ++k) put(NCM + 8 + (k & 3));
    for (int k = NCM + 12; k < 16; ++k) put(k);
    for (int k = NCM + 3; k >= 0; --k) put(k);
    return t;
  }
  for (int k = 0; k < NCM; ++k) put(k);
  for (int k = 0; k < 3; ++k) { put(NCM + 4 * k + cls); put(NCM + ((cls + k + 1) & 3)); }
  for (int k = 1; k < 4; ++k) put(NCM + 4 + ((cls + k) & 3));
  for (int k = 1; k < 4; ++k) put(NCM + 8 + ((cls + k) & 3));
  for (int k = NCM + 12; k < 16; ++k) put(NCM == 2 ? k ^ (cls & 1) : k);
  return t;
}
static_assert(NCM >= 3);
constexpr TRow t_row(int b, int wid) {
  TRow t{};
  const int j = b & 7, r = b >> 3, rr = r & 15, hsel = rr & 1, pj = rr >> 1, kv = pj & 1, h = j + 8 * hsel, u = hsel * 8 + j;
  const bool isY = r >= 16, noA = pj == 7, solo = pj >= NSPL;
  const int qbF = isY != solo ? 15 - pj : pj;
  const int mt = (rr >> 2) + (isY ? 4 : 0), g = j * 128, NY = NYT - ((NYM >> pj) & 1);
  const int nx0 = solo ? 0 : 32 - 2 * pj - NY, cx = nx0 < CMAX ? nx0 : CMAX, ex = nx0 - cx, qb0 = solo ? qbF : 15 - pj;
  const unsigned oQ = (unsigned)h << 20, oK = oQ + O_K, oP = O_P + (h * 8 + pj) * PSTR;
  if (wid == 4) {
    for (int l = 0; l < 64; ++l)
      t.v[l] = fo(l < 16 ? g + hsel * 16 + l : l == 16 ? g + 32 + hsel * 8 + (qb0 >> 1) : l == 17 ? g + 32 + hsel * 8 + (qbF >> 1) :
                  l == 26 ? g + 48 + hsel * 8 + pj : l < 32 ? g + 64 + hsel * 8 + pj :
                  l < 48 ? 2304 + pj * 16 + l - 32 : 2048 + pj * 16 + l - 48);
    return t;
  }
  auto put64 = [&](int i, u64 x) { t.v[i] = (unsigned)x; t.v[i + 1] = (unsigned)(x >> 32); };
  t.v[T_Q0] = oQ + (qb0 * 128 + wid * 16) * 512;
  t.v[T_K0] = oK + (isY ? cx : 0) * 32768;
  t.v[T_OQ] = oQ + pj * 131072 + (wid >> 1) * 65536 + (wid & 1) * 4096;
  t.v[T_OG] = oK + kv * TSB + mt * 131072 + (wid >> 1) * 65536 + (wid & 1) * (kv ? 8192 : 4096);
  t.v[T_FPREP] = fo(2048 + pj * 16 + u);
  t.v[T_FQ] = fo(g + 32 + hsel * 8 + pj);
  t.v[T_FG] = fo(g + hsel * 16 + kv * 8 + mt);
  t.v[T_FS] = fo(1024 + qbF * 16 + j * 2 + hsel);
  t.v[T_CNT] = fo(2560 + qbF * 16);
  t.v[T_ZO] = O_Z + pj * 64 * SEQ * 2 + (u * 4 + wid) * 4096;
  const u64 own = 0x1ull << (kv * 8 + mt), conv = noA ? 0 : 0xffffull << 32;
  put64(T_NPRE, 0x10000ull | (isY ? (1ull << (cx >> 2)) | (1ull << ((cx + 1) >> 2)) : noA ? 0 : 0xffffull << 48));
  put64(T_NST, isY ? (solo ? kvmask(0, (2 * qbF + 1) >> 2) : kvmask(cx >> 2, 3) | 1ull << 27) : kvmask(0, 0));
  put64(T_TAB, k_order(isY, rr));
  t.v[T_SLAB] = O_S + qbF * 0x200000 + j * 0x40000 + wid * 8192 + hsel * 1024;
  t.v[T_PART] = oP;
  const unsigned junk = fo(g + 96 + r), fconv = noA ? junk : fo(2304 + pj * 16 + u);
  for (int it = isY || solo; it < 2; ++it) {
    unsigned* p = t.v + T_IT + it * T_ITN;
    const int n = !it ? nx0 : isY && !solo ? NY : 2 * qbF + 2;
    u64 q[4] = {0, 0, 0, 0};
    unsigned jmp[3] = {0, 0, 0}, chk = 0;
    p[14] = junk;
    if (isY) {
      p[0] = 2; p[1] = 2; p[13] = t.v[T_FG];
      if (!solo) {
        chk = 1u << (14 - cx); p[0] |= chk; q[1] = kvmask(4, (31 - 2 * pj) >> 2) & ~own; jmp[1] = ex * 32768;
        chk |= 1u << (n - 2); q[2] = 1ull << 26;
      }
    } else if (it == 0) {
      chk = p[0] = 2u | (ex ? 1u << (cx - 2) : 0) | 1u << (n - 2);
      chk |= noA || qbF > 1 ? 0 : 1u << n;
      q[0] = kvmask(0, (cx - 1) >> 2);
      if (ex) { q[1] = kvmask(4, 4 + ((ex - 1) >> 2)); jmp[1] = (16 - cx) * 32768; }
      jmp[ex ? 2 : 1] = -(ex ? 16 + ex : cx) * 32768;
      q[ex ? 2 : 1] = 0x20000ull | kvmask(0, (2 * qbF + 1) >> 2);
      q[ex ? 3 : 2] = conv;
      p[1] = 2; p[13] = fconv; p[14] = fo(g + 64 + hsel * 8 + pj);
    } else if (solo) {
      chk = p[0] = 0x402; p[1] = 2; p[13] = fconv;
      q[0] = kvmask(0, 2); q[1] = kvmask(3, (2 * qbF + 1) >> 2) | conv;
    } else {
      p[0] = p[1] = n > 4 ? 2 : 1; p[13] = fo(g + 48 + hsel * 8 + pj);
      chk = noA ? 0 : p[0];
      q[0] = conv;
    }
    p[2] = jmp[0]; p[4] = jmp[1]; p[15] = jmp[2];
    p[3] = !it ? 0 : n > 2 ? 1u << (n - 3) : 1u << 30;
    for (int k = 0; k < 4; ++k) { p[5 + 2 * k] = (unsigned)q[k]; p[6 + 2 * k] = (unsigned)(q[k] >> 32); }
    p[16] = 32 | (isY && !solo ? 2 : 0) | (n == 2 ? 4 : 0) | (isY || noA ? 0 : 8) | (n & 1) << 4 | (n - 1 - it) << 8 | ((r >> 1) * 4 + wid) << 16 | h << 24 | qbF << 28;
    p[17] = !it ? 0 : n > 2 ? 1u << (n - 2) : 1u << 31;
    p[18] = it ? oP + wid * 16384 : oQ + (qbF * 128 + wid * 16) * 512;
    p[19] = oP + (it ? 1032 : 1024);
    p[20] = chk >> 1;
    p[21] = oK + (isY ? cx : 0) * 32768;
    p[22] = ((p[0] | p[3] | p[17]) & 0x3fffffffu) << 1;
  }
  return t;
}
struct TBlk { TRow row[5]; };
template <int B> constexpr TBlk t_blk = {{t_row(B, 0), t_row(B, 1), t_row(B, 2), t_row(B, 3), t_row(B, 4)}};
struct Tab { TBlk blk[256]; };
template <int... I> constexpr Tab t_make(iseq<I...>) { return Tab{{t_blk<I>...}}; }
__device__ const Tab g_tab = t_make(make_iseq<256>{});

template <int R> DI const bf16* sptr() { const bf16* p; asm volatile("s_mov_b64 %0, s[%c1:%c2]" : "=s"(p) : "i"(R), "i"(R + 1)); return p; }
template <class T, int I> DI T* arg_late() {
  unsigned lo, hi;
  asm volatile("v_readlane_b32 %0, v77, %2\n\tv_readlane_b32 %1, v77, %3\n\ts_nop 4" : "=s"(lo), "=s"(hi) : "i"(2 * I), "i"(2 * I + 1));
  return (T*)(((u64)hi << 32) | lo);
}
__global__ __launch_bounds__(256, 1) __attribute__((amdgpu_num_vgpr(77), amdgpu_num_sgpr(56))) void fused_kernel(
    const bf16* ckv_, const bf16* wuk_, const bf16* wuv_, const bf16* x_, const bf16* wq_, bf16* ws_, const bf16* ak_, const bf16* av_,
    const bf16* ash_, const bf16* assmk_, const bf16* assmD_, const bf16* wo_, const bf16* aq_, bf16* out_) {
  extern __shared__ __attribute__((aligned(16))) char lds[];
  const void* kap = (const void*)__builtin_amdgcn_kernarg_segment_ptr();
  asm volatile("at_code_begin:\n\ts_load_dwordx16 s[80:95], %0, 0\n\ts_load_dwordx2 s[96:97], %0, 0x40" ::"s"(kap) : ACLOB_ALL, "v255", "s101");
  asm volatile("s_getpc_b64 s[48:49]\nct_base:\n\ts_mov_b32 s50, pf_code_end-ct_base\n\ts_mov_b32 s51, 0x20000\n\ts_lshr_b32 s52, %1, 3\n\t"
               "v_and_b32 v116, 63, %0\n\tv_lshrrev_b32 v117, 6, %0\n\tv_lshl_add_u32 v116, v116, 7, v117\n\ts_lshl_b32 s52, s52, 9\n\t"
               "v_lshlrev_b32 v116, 7, v116\n\tbuffer_load_dword v117, v116, s[48:51], s52 offen" ::"v"(threadIdx.x), "s"(blockIdx.x) : "scc", "memory");
  GAP();
  const int tid = threadIdx.x, lane = tid & 63;
  const int wid = __builtin_amdgcn_readfirstlane(tid >> 6);
  const int b = blockIdx.x, j = b & 7, r = b >> 3, rr = r & 15, hsel = rr & 1, pj = rr >> 1, kv = pj & 1;
  const bool isY = r >= 16, doP = isY && pj != 7, doA = !isY && pj != 7;
  const int h = j + 8 * hsel, mt = (rr >> 2) + (isY ? 4 : 0), pu = (hsel * 8 + j) * 4 + wid;
  constexpr unsigned m0d = 131072 + 24576;
  const unsigned lA = isY ? 65536 : 0, lW = lA ^ 65536, po = isY ? 0 : 32768;
  unsigned mA = lA + wid * 1024 + po, mW = lW + wid * 1024 + po, ofA = isY ? pj * 256 * DA * 2 : mt * 256 * DC * 2, ofW = h * 512;
  const unsigned lda = isY ? DA : DC;
#define ROT 1
  unsigned g0 = (!ROT ? 0 : isY ? pj + 2 * hsel : rr + (rr >> 2)) & 3;
  {
    asm volatile("global_load_dword v78, %0, %2\n\tglobal_load_dword v79, %1, %2 offset:1024\n\tglobal_load_dword v77, %3, %4"
                 ::"v"(tid * 4), "v"(lane * 4), "s"(&g_tab.blk[b]), "v"((lane < 27 ? lane : 27) * 4), "s"(kap) : "memory");
    const unsigned ra = wid * 8 + (lane >> 3), sa = ((lane & 7) ^ (((wid & 1) * 4 + (lane >> 4)) & 7)) * 16;
    const unsigned vbA = ra * (lda * 2) + sa, vbB = (4 * wid + (lane >> 4)) * LDW * 2 + 16 * ((lane & 15) ^ (((lane >> 4) << 2) | wid));
    vsetu<92>(vbA); vsetu<93>(vbB); vsetu<94>(ra * (DC * 2) + sa);
    sset<65>(128 * lda);
    asm volatile("s_mov_b32 s74, 0x40000\n\ts_mov_b32 s53, 0x80000");
    sset<58>(256 * lda * 2); sset<62>((lda - 1) * LDW * 2 + 512);
    asm volatile("s_mov_b32 s59, 0x20000\n\ts_mov_b32 s63, 0x20000");
    g_pro<1>(g0, mA, mW, lda);
    asm volatile("" : "+s"(ofA), "+s"(ofW), "+s"(mA), "+s"(mW), "+s"(g0));
    GAP();
  }
  asm volatile("s_bitcmp1_b32 %0, 4\n\ts_waitcnt lgkmcnt(0)\n\ts_cselect_b64 s[98:99], s[84:85], s[82:83]\n\ts_bitcmp1_b32 %0, 7\n\t"
               "s_cselect_b64 s[60:61], s[88:89], s[98:99]\n\ts_cselect_b64 s[56:57], s[86:87], s[80:81]\n\t"
               "s_add_u32 s56, s56, %1\n\ts_addc_u32 s57, s57, 0\n\ts_add_u32 s60, s60, %2\n\ts_addc_u32 s61, s61, 0\n\t"
               "s_mov_b64 s[88:89], s[90:91]\n\ts_mov_b32 s90, %3\n\ts_mov_b32 s91, 0x20000" ::"s"(b), "s"(ofA), "s"(ofW), "i"(WSB) : "scc");
  KST(0, 0);
  AC c;
  {
    GM g;
    PR pr;
    const int ptb = hsel * 8 + j;
    if (doP) prep_load(pr, tid, pj, ptb, sptr<92>(), sptr<94>(), sptr<96>());
    g_pro<2>(g0, mA, mW, lda);
    g_pro(g0 + 1, mA, mW, lda);
    sset<64>(64 * lda); sset<67>(256 * lda); sset<68>(320 * lda);
    asm volatile("s_mov_b32 s71, 0x20000\n\ts_movk_i32 s72, 0x100\n\ts_mov_b32 s73, 0x20100");
    asm volatile("buffer_load_dwordx2 v[120:121], off, s[88:91], %0 sc1" ::"s"(fo(6144)) : "memory");
    GAP();
    {
      const int fr = lane & 15, fq = lane >> 4, q = (lane & 15) >> 2, p = lane & 3;
      vsetu<128>(lA + (wid >> 1) * 16384 + (fr >> 3) * 1024 + (fr & 7) * 128 + ((fq ^ (fr >> 1)) * 16));
      vsetu<129>(lW + (wid & 1) * 8192 + off_b(8 * fq + q, p >> 1) + 8 * (p & 1));
      vsetu<130>(lW + (wid & 1) * 8192 + off_b(8 * fq + 4 + q, p >> 1) + 8 * (p & 1));
      const unsigned one = fq != q ? 0u : (p & 1) ? 0x3f800000u : 0x3f80u;
      vsetu<131>((p & 2) ? 0u : one); vsetu<132>((p & 2) ? one : 0u);
      vsetu<133>((lane >> 2) * 128 + 16 * ((p >> 1) ^ ((lane >> 2) & 6)) + 8 * (p & 1));
      vsetu<134>((fr >> 3) * 2048 + (fq & 1) * 512 + (fq >> 1) * 256 + (fr & 7) * 16);
      vsetu<81>(tid == 0 ? 0u : 0x80000000u);
      vsetu<84>(lane * 16); vsetu<85>(lane * 16 + 65536); vsetu<86>(wid * 8192 + lane * 16); vsetu<87>(wid * 8192 + lane * 16 + 4096);
      vsetu<88>(65536 + wid * 256 + fr * 16); vsetu<89>(16 * wid + fr - 4 * fq);
      asm volatile("s_mov_b64 s[92:93], s[88:89]\n\ts_mov_b64 s[94:95], s[90:91]\n\ts_mov_b32 s75, 0x60000\n\t"
                   "s_mov_b32 s76, 0x40100\n\ts_mov_b32 s77, 0x60100\n\ts_mov_b32 s82, 2");
      asm volatile("s_add_u32 s86, s80, %0\n\ts_addc_u32 s87, s81, 0\n\ts_add_u32 s98, s98, %1\n\ts_addc_u32 s99, s99, 0"
                   ::"s"(mt * 256 * DC * 2), "s"(ofW) : "scc");
      sset<66>(192 * lda); sset<69>(384 * lda); sset<70>(448 * lda);
      sset<78>(lA + wid * 1024); sset<79>(lW + wid * 1024);
    }
    GAP();
    if (doP) {
      asm volatile("s_waitcnt vmcnt(11)" ::: "memory");
#pragma unroll
      for (int q = 0; q < 7; ++q) asm volatile("" : "+v"(pr.kk[q]));
#pragma unroll
      for (int q = 0; q < 4; ++q) asm volatile("" : "+v"(pr.vv[q]));
      static_for([&](auto Ic) __attribute__((always_inline)) {
        constexpr int I = Ic;
        if constexpr (I < 2) g_pro(g0 + 2 + I, mA, mW, lda);
        if constexpr (I == 2) g_pro2(mA, mW);
        GAP();
        prep_math<I>(pr, tid, lds + 131072);
        GAP();
      }, make_iseq<4>{});
    } else { g_pro(g0 + 2, mA, mW, lda); g_pro(g0 + 3, mA, mW, lda); g_pro2(mA, mW); }
    GAP();
    g.aA[0] = vxor<128, 0>(); g.aA[1] = vxor<128, 64>();
    static_for([&](auto I) { g.aB[I >> 3][I & 7] = vxor<129 + (I >> 3), 32 * (I & 7)>(); }, make_iseq<16>{});
    c.o0 = vxor<131, 0>(); c.o1 = vxor<132, 0>(); c.tb = vxor<133, 0>(); c.vs = vxor<134, 0>();
    asm volatile("v_add_u32 v91, 0x100000, v93\n\ts_waitcnt vmcnt(8) lgkmcnt(0)\n\ts_barrier" ::: "memory");
    if (isY) g_read0<0>(g, make_iseq<16>{});
    else g_read0<1>(g, make_iseq<16>{});
    GAP();
    asm volatile("v_readfirstlane_b32 s48, v120\n\tv_readfirstlane_b32 s49, v121\n\tv_readlane_b32 s80, v78, %0\n\tv_readlane_b32 s81, v78, %1\n\t"
                 "s_mov_b32 s50, 0x4c957f2d\n\ts_mul_i32 s84, s48, s50\n\ts_mul_hi_u32 s85, s48, s50\n\ts_mul_i32 s49, s49, s50\n\t"
                 "s_mul_i32 s48, s48, 0x5851f42d\n\ts_add_u32 s85, s85, s49\n\ts_add_u32 s85, s85, s48\n\ts_add_u32 s84, s84, 0xf767814f\n\t"
                 "s_addc_u32 s85, s85, 0x14057b7e\n\ts_lshr_b64 s[80:81], s[80:81], 12\n\tv_mov_b32 v82, s84\n\tv_mov_b32 v83, s85"
                 ::"i"((int)T_TAB), "i"(T_TAB + 1) : "scc");
    if (isY) asm volatile("s_mov_b64 s[80:81], 0x6543");
    if (doP) prep_store(tid, pj, ptb, (bf16*)sptr<88>() + O_X / 2, lds + 131072);
    asm volatile("s_waitcnt lgkmcnt(0)" ::: "memory");
    GAP();
    if (isY) {
      g_tile<false, 0, 1>(g);
      g_loop<false, true>(g, [&]() __attribute__((always_inline)) {
        tload<80, T_TAB>(); tload<81, T_TAB + 1>();
        fl_set<T_FPREP>();
        asm volatile("s_mov_b64 s[60:61], s[98:99]\n\ts_mov_b32 s62, %0" ::"i"((DC - 1) * LDW * 2 + 512));
      }, [&]() __attribute__((always_inline)) {
        asm volatile("s_mov_b64 s[56:57], s[86:87]\n\ts_mov_b32 s58, %0\n\tv_mov_b32 v92, v94" ::"i"(256 * DC * 2));
        static_for([&](auto J) { asm volatile("s_mov_b32 s%c0, %1" ::"i"(64 + J), "i"((J + 1) * 64 * DC)); }, make_iseq<7>{});
      });
      tload<101, T_OQ>(); sset<83>(__float_as_uint(CS));
      asm volatile("v_mov_b32 v112, v84\n\tv_add_u32 v113, 8192, v84");
      GAP();
      g_tile<false, 0, 2>(g);
      KST(0, 3);
    }
    auto kvtile = [&](auto TRc) __attribute__((always_inline)) {
      constexpr bool TR = TRc;
      if (isY) g_tile<TR, 1, 4>(g);
      else g_tile<TR, 1, 1>(g);
      asm volatile("s_mov_b32 s83, s78");
      g_loop<TR, false>(g, [&]() __attribute__((always_inline)) {
        asm volatile("v_readlane_b32 s49, v78, %1\n\tv_readlane_b32 s48, v78, %0\n\tv_and_b32 v122, 0xffffff00, v82\n\tv_mov_b32 v123, v83\n\t"
                     "s_and_b32 s49, s49, 0xf000000\n\ts_cselect_b32 s49, -2.0, 0\n\tv_or_b32 v121, s49, v81\n\ts_nop 0\n\t"
                     "buffer_store_dwordx2 v[122:123], v121, s[88:91], s48 offen sc1" ::"i"((int)T_CNT), "i"(T_IT + T_ITN + 16) : "memory", "scc");
        if (isY) fl_set<T_FQ>();
        else {
          int pv = pu; asm volatile("" : "+s"(pv));
          if (doA) post_dma(pj, pv, arg_late<const bf16, 12>(), 131072 + wid * 8192);
          else {
            const char* tp = arg_late<const char, 0>() + 1024 * DC * 2 + pv * 32768 + __float_as_uint(vget<84>()) * 8;
            asm volatile("s_mov_b32 m0, %0\n\ts_nop 0" ::"i"(m0d));
#pragma unroll
            for (int i = 0; i < 4; ++i) asm volatile("global_load_lds_dword %0, off" ::"v"(tp + i * 8192) : "memory");
          }
        }
      }, [&]() __attribute__((always_inline)) {
        asm volatile("s_mov_b32 s79, 0x28000\n\ts_mov_b32 s83, s79");
        fl_poll<T_NPRE>();
        if (!isY) {
          int pv = pu; asm volatile("" : "+s"(pv));
          if (doA) {
            const char* tp = arg_late<const char, 9>() + (size_t)(pj * 64 + pv) * (SEQ * 2) + __float_as_uint(vget<84>()) * 4;
            asm volatile("s_mov_b32 m0, %1\n\ts_nop 0\n\tglobal_load_lds_dword %0, off" ::"v"(tp), "i"(m0d) : "memory");
          } else {
            CODE_TOUCH("at_code_begin", "pf_code_end", (pv >> 5) * 8 + (pv & 3) * 2, 16u, 64, m0d);
            CODE_TOUCH("at_code_begin", "pf_code_end", (pv >> 5) * 8 + (pv & 3) * 2 + 1, 16u, 64, m0d);
          }
        }
      });
      tload<101, T_OG>(); tload<96, T_Q0>(); tload<98, T_K0>();
      fl_check();
      tload<50, T_NST>(); tload<51, T_NST + 1>();
      asm volatile("v_mov_b32 v112, v84\n\tv_add_u32 v113, %0, v84\n\tv_mov_b32 v90, v87\n\tv_mov_b32 v91, v87\n\tv_mov_b32 v92, v87\n\tv_mov_b32 v93, v87\n\t"
                   "v_mov_b32 v94, v86\n\tv_mov_b32 v95, v86\n\tv_mov_b32 v126, v86\n\tv_mov_b32 v127, v86\n\ts_mov_b32 s99, 0x8000\n\t" FL_ARM("s[50:51]")
                   ::"i"(TR ? 4096 : 8192));
      if (doA) {
        const int d = pj * 64 + pu;
        const bf16* dp = arg_late<const bf16, 10>() + d;
        asm volatile("global_load_ushort v123, %0, off" ::"v"(dp) : "memory");
        srd_set<56>(arg_late<const bf16, 9>() + (size_t)d * SEQ, SEQ * 2);
        srd_set<60>(arg_late<const char, 5>() + O_X + d * SEQ * 2, SEQ * 2);
        sset<78>(65536 + wid * 15488);
        asm volatile("s_movk_i32 s64, 992\n\tv_subrev_u32 v122, 32, v84");
        GAP();
        g_tile<TR, 0, 3>(g);
      } else {
        sset<79>(isY ? wid * 8192 : 0x30000);
        if (tget<T_IT + T_ITN + 16>() & 16) asm volatile("s_add_u32 s98, s98, 0x8000\n\ts_mov_b32 s97, 0xffff8000" ::: "scc");
        else asm volatile("s_mov_b32 s97, 0x8000");
        GAP();
        g_tile<TR, 0, 5>(g);
      }
    };
    if (kv) kvtile(std::true_type{});
    else kvtile(std::false_type{});
  }
  KST(0, 4);
  asm volatile("s_waitcnt vmcnt(16)" ::: "memory");
  const unsigned md0 = tget<T_IT + 16>(), md1 = tget<T_IT + T_ITN + 16>();
  sset<79>(wid * 8192 + (md0 & 16) * 2048);
  if (doA) {
    const float Dd = bfl(__float_as_uint(vget<123>()));
    KST(0, 5);
    conv_wave<T_FG>(O_Z + pj * 64 * SEQ * 2 + ((pu & 7) * 8 + (pu >> 3)) * 64, pu >> 3, Dd, lds + 65536 + wid * 15488);
    const char* tp = arg_late<const char, 11>() + (size_t)h * (HD * DA * 2) + ((pj * 4 + wid) * 64 + lane) * 128;
    asm volatile("s_mov_b32 m0, %1\n\ts_nop 0\n\tglobal_load_lds_dword %0, off\n\ts_waitcnt vmcnt(17)" ::"v"(tp), "i"(m0d) : "memory");
    KST(0, 6);
  } else if (!isY) {
    static_for([&](auto I) { d_store<16 + I>(); }, make_iseq<16>{});
    tload<98, T_K0>();
    asm volatile("s_waitcnt vmcnt(0)\n\ts_barrier" ::: "memory");
    fl_set<T_FG>();
    KST(0, 5);
    fl_check();
    CODE_TOUCH("att_begin", "pf_code_end", hsel * 4 + wid, 8u, 64, m0d);
    dma4k<0>(); dma4k<1>(); dma4k<2>(); dma4k<3>();
    asm volatile("s_waitcnt vmcnt(0)" ::: "memory");
    KST(0, 6);
  } else {
    tload<76, T_PART>();
    fl_check();
    asm volatile("buffer_load_dwordx2 v[122:123], v88, s[88:91], s76 offen offset:1024 sc1" ::: "memory");
  }
  sset<78>(65536 + wid * 8192); sset<79>(98304 + wid * 8192);
  { const bf16* wp = arg_late<const bf16, 11>() + ((md1 >> 24) & 15) * HD * DA; asm volatile("s_mov_b64 s[80:81], %0" ::"s"(wp)); }
  asm volatile("att_begin:");
  ev_code(c);
  asm volatile("s_waitcnt lgkmcnt(0)\n\ts_barrier" ::: "memory");
#pragma unroll 1
  for (int it = md0 ? 0 : 1; it < 2; ++it) {
    const unsigned mode = it ? md1 : md0;
    const bool odd = mode & 16;
    if (odd) kreads<1>(make_iseq<8>{});
    else kreads<0>(make_iseq<8>{});
    const unsigned oP = tget<T_PART>();
    if (it == 0) {
      static_for([&](auto I) { tload<57 + I, T_IT + I>(); }, make_iseq<21>{});
      tload<S_T, T_IT + 21>(); tload<S_EV, T_IT + 22>();
    } else {
      static_for([&](auto I) { tload<57 + I, T_IT + T_ITN + I>(); }, make_iseq<21>{});
      tload<S_T, T_IT + T_ITN + 21>(); tload<S_EV, T_IT + T_ITN + 22>();
    }
    asm volatile("s_add_u32 s98, s97, 0x8000\n\ts_mov_b32 s56, -1\n\tv_mov_b64 v[114:115], 0\n\tv_mov_b64 v[118:119], 0\n\t"
                 "v_mov_b32 v120, 0x45800000" ::: "scc");
    KST(0, 8 + 3 * (it && !isY));
    GAP();
#define STA(k)
#define STI()
#define LOOP_ENDS "s_bfe_u32 s76, s73, 0x80008"
    STA(0);
    if (odd) {
      if (isY) phaseA<1, 2>(c);
      else phaseA<1, 1>(c);
      STA(1);
      STA(2);
      phaseB0<1>(mode, isY);
      STA(3);
      asm volatile(LOOP_ENDS "\n\ts_branch att_mid" ::: "scc");
    } else {
      if (isY) phaseA<0, 2>(c);
      else phaseA<0, 1>(c);
      STA(1);
      if (__builtin_expect(mode & 4, 0)) { asm volatile("s_mov_b32 s56, 30"); ev_call(); asm volatile("s_mov_b32 s56, 31"); ev_call(); acc_zero(); }
      STA(2);
      phaseB0<0>(mode, isY);
      if (__builtin_expect(mode & 4, 0)) { ev_call(); asm volatile("s_branch att_done"); }
      STA(3);
    }
    GAP();
    asm volatile(LOOP_ENDS "\natt_loop:" ::: "scc");
    GAP();
    phaseA<1, 0>(c);
    STI();
    if (__builtin_expect(c.rare != 0, 0)) sm_redo<1>();
    phaseB<1>(c);
    asm volatile("s_cbranch_scc1 att_done\natt_mid:");
    phaseA<0, 0>(c);
    STI();
    if (__builtin_expect(c.rare != 0, 0)) sm_redo<0>();
    phaseB<0>(c);
    asm volatile("s_cbranch_scc0 att_loop\natt_done:");
    GAP();
    STA(4);
    if (it == 0) phaseA<0, 3>(c);
    else {
      sset<76>(oP);
      GAP();
      phaseAL(c, make_iseq<32>{});
      if (__builtin_expect(redo(), 0)) sm_redo<1, false>();
      if (mode & 2) phaseBL<true>(c, make_iseq<64>{});
      else phaseBL<false>(c, make_iseq<64>{});
    }
    if (__builtin_expect(redo(), 0)) {
      if (it) static_for([&](auto I) { vset<R_S + 32 + (I >> 2) * 8 + (I & 3)>(-INFINITY); }, make_iseq<16>{});
      asm volatile("s_nop 7");
      sm_redo<0, false>();
    }
    STA(5);
    KST(0, 9 + 3 * (it && !isY));
    float l[2];
    asm volatile("v_add_f32 %0, v%c2, v%c3\n\tv_add_f32 %1, v%c4, v%c5" : "=v"(l[0]), "=v"(l[1]) : "i"(R_RS), "i"(R_TS), "i"(R_RS + 1), "i"(R_TS + 1));
    const float cm[2] = {vget<R_M>(), vget<R_M + 1>()};
    l[0] = row_reduce<false>(l[0]); l[1] = row_reduce<false>(l[1]);
    EP p;
    if (it == 0) {
      const f32x4 ml = {cm[0], l[0], cm[1], l[1]};
      asm volatile("buffer_store_dwordx4 %0, v88, s[88:91], %1 offen sc1\n\ts_mov_b32 s101, %2" ::"v"(ml), "s"(oP), "s"(oP + wid * 16384) : "memory");
      phaseF(c, p, make_iseq<74>{});
      asm volatile(FL_POLL "\n\ts_waitcnt vmcnt(17)" ::: "memory");
    } else {
      if (!(mode & 2)) {
        p.fa[0] = __builtin_amdgcn_rcpf(l[0]); p.fa[1] = __builtin_amdgcn_rcpf(l[1]);
        phaseFL<1>(p);
      } else {
        const f32x4 ml = {vget<122>(), vget<123>(), vget<124>(), vget<125>()};
        if (__builtin_expect(__builtin_amdgcn_ballot_w64(ml[0] != cm[0] || ml[2] != cm[1]) == 0, 1)) {
          p.fa[0] = __builtin_amdgcn_rcpf(l[0] + ml[1]); p.fa[1] = __builtin_amdgcn_rcpf(l[1] + ml[3]);
          phaseFL<3>(p);
        } else {
#pragma unroll
          for (int e = 0; e < 2; ++e) {
            const float mm = fmaxf(cm[e], ml[2 * e]);
            const float wa = __builtin_amdgcn_exp2f(cm[e] - mm), wb = __builtin_amdgcn_exp2f(ml[2 * e] - mm);
            const float inv = __builtin_amdgcn_rcpf(l[e] * wa + ml[2 * e + 1] * wb);
            p.fa[e] = wa * inv; p.fb[e] = wb * inv;
          }
          phaseFL<2>(p);
        }
      }
    }
    KST(0, 10 + 3 * (it && !isY));
  }
  asm volatile(TICKET "\n\tv_xor_b32 v%c0, s100, v%c0\np_loop:" ::"i"(R_TB) : "memory");
  GAP();
  p_half<0, true>(make_iseq<32>{});
  asm volatile("s_cbranch_scc0 p_mid");
  p_half<1, true>(make_iseq<32>{});
  asm volatile("s_branch p_loop\np_loop2:");
  p_half<0, false>(make_iseq<32>{});
  asm volatile("p_mid:");
  p_half<1, false>(make_iseq<32>{});
  asm volatile("s_cbranch_scc1 p_loop2\n\ts_nop 5");
  GAP();
  p_cvt<1, 0>(); p_cvt<1, 1>(); p_cvt<1, 2>(); p_cvt<1, 3>();
  p_store<1, 1>();
  GAP();
  KST(0, 27);
  {
    constexpr unsigned rdy = 98304;
    int lz = threadIdx.x & 63; asm volatile("" : "+v"(lz));
    const int fr = lz & 15, fq = lz >> 4, qbF = md1 >> 28;
    if (wid == 0) {
#pragma unroll 1
      for (int q = 0; q < 0x100000; ++q) {
        unsigned lo, hi, tl, th;
        asm volatile("v_readfirstlane_b32 %0, v240\n\tv_readfirstlane_b32 %1, v241\n\ts_mov_b32 %2, s84\n\ts_mov_b32 %3, s85" : "=s"(lo), "=s"(hi), "=s"(tl), "=s"(th));
        if (__builtin_expect((((lo ^ tl) >> 8) | (hi ^ th)) == 0, 1)) break;
        asm volatile("s_sleep 4\n\tv_mov_b64 v[240:241], 1\n\t" TICKET "\n\ts_waitcnt vmcnt(0)" ::: "memory");
      }
      asm volatile("v_mov_b32 v241, 0\n\ts_mov_b64 exec, 1\n\tds_write_b64 %0, v[240:241]\n\ts_mov_b64 exec, -1" ::"v"(rdy) : "memory");
    }
    vsetu<79>(fo(1024) + qbF * 4352 + fr * 8);
    {
      const int jj = 4 * (fr >> 3) + (fr & 3);
      const unsigned one = ((fq ^ (fr >> 2)) & 1) ? 0u : (jj & 1) ? 0x3f800000u : 0x3f80u;
      static_for([&](auto I) { vsetu<96 + I>((jj >> 1) == I ? one : 0u); }, make_iseq<4>{});
    }
    unsigned vo = (fq >> 1) * 1024 + (fq & 1) * 256 + fr * 16;
    bf16* out = arg_late<bf16, 13>();
    __amdgpu_buffer_rsrc_t so_ = __builtin_amdgcn_make_buffer_rsrc((void*)out, (short)0, SEQ * OUTW * 2, 0x00020000);
    unsigned vw = ((qbF * 128 + (fr & 7)) * OUTW + DA + (fr >> 3) * 32 + fq * 8) * 2;
    asm volatile("" : "+s"(so_));
    asm volatile("s_waitcnt vmcnt(0) lgkmcnt(0)\n\ts_barrier" : "+v"(vo), "+v"(vw) :: "memory");
    fl_set<T_FS>();
    unsigned tk;
    asm volatile("ds_read_b32 %0, %1\n\ts_waitcnt lgkmcnt(0)" : "=v"(tk) : "v"(rdy) : "memory");
    tk = __builtin_amdgcn_readfirstlane(tk) & 255;
    KST(0, 30);
    if (doA) post_math(pj, pu, out, lds + 131072 + wid * 8192);
#define N3 0
#define N2 12
    constexpr int N1 = 28 - 3 * N3 - 2 * N2;
    static_assert(N1 >= 0 && N3 + N2 + N1 <= 16);
    const int nu = tk < N3 ? 3 : tk < N3 + N2 ? 2 : tk < N3 + N2 + N1 ? 1 : 0;
    const int u0 = 4 * (tk < N3 ? 3 * tk : tk < N3 + N2 ? N3 + 2 * tk : 2 * N3 + N2 + tk) + wid * nu;
    KST(0, 28);
    if (nu) {
      const unsigned so0 = O_S + qbF * 0x200000 + (16 + u0) * 2048, so[3] = {so0, so0, so0};
      unsigned pend = 0x5555u, cnt = 0x400000;
      while (true) {
        unsigned nw;
        if (wid == 3) {
          u64 m;
          const u64 ex = pend | (pend << 1);
          asm volatile(FL_ARM("%1") "\n\t" FL_POLL "\n\ts_waitcnt vmcnt(0)\n\tv_cmp_eq_u64 %0, s[84:85], v[124:125]" : "=s"(m) : "s"(ex) : "memory");
          nw = (unsigned)m & ((unsigned)m >> 1) & pend;
          if (nw) asm volatile("ds_write_b32 %0, %1 offset:4" ::"v"(rdy), "v"(~pend | nw) : "memory");
        } else {
          unsigned t;
          asm volatile("ds_read_b32 %0, %1 offset:4\n\ts_waitcnt lgkmcnt(0)" : "=v"(t) : "v"(rdy) : "memory");
          nw = __builtin_amdgcn_readfirstlane(t) & pend;
        }
        if (--cnt == 0) nw = pend;
        if (nw == 0) {
          if (wid == 3) asm volatile("s_sleep 4");
          else asm volatile("s_sleep 1");
          continue;
        }
        asm volatile("s_nop 4");
        static_for([&](auto S) {
          if ((nw >> (2 * S)) & 1) {

            r_load<0, 2 * S>(vo, so[0]); r_load<0, 2 * S + 1>(vo, so[0]);
            if (nu > 1) { r_load<1, 2 * S>(vo, so[1]); r_load<1, 2 * S + 1>(vo, so[1]); }
            if (nu > 2) { r_load<2, 2 * S>(vo, so[2]); r_load<2, 2 * S + 1>(vo, so[2]); }
          }
        }, make_iseq<8>{});
        pend &= ~nw;
        if (pend == 0) break;
      }
      auto oo = [&](int U) { return ((((U >> 1) & 1) * 64 + ((U >> 2) & 3) * 16 + (U & 1) * 8) * OUTW + (U >> 4) * 64) * 2; };
      unsigned o0 = oo(u0), o1 = oo(u0 + 1), o2 = oo(u0 + 2);
      asm volatile("s_waitcnt vmcnt(0)" : "+s"(o0), "+s"(o1), "+s"(o2), "+s"(so_) :: "memory");
      KST(0, 16);
      if (nu > 2) r_sums<2>();
      if (nu > 1) r_sums<1>();
      r_sums<0>();
      if (nu > 2) r_fin<2>(vw, so_, o2);
      if (nu > 1) r_fin<1>(vw, so_, o1);
      asm volatile("s_nop 7");
      r_fin<0>(vw, so_, o0);
      if (qbF == 15 && tk == 0) asm volatile(FL_SET("%0") ::"s"(fo(6144)) : "memory");
    }
    KST(0, 29);
  }
  asm volatile("pf_code_end:");
  KST(0, 1);
}

extern "C" void kernel_launch(void* const* d_in, const int* in_sizes, int n_in, void* d_out, int out_size, void* d_ws,
                              size_t ws_size, hipStream_t stream) {
  const bf16* Aq = (const bf16*)d_in[0];
  const bf16* Ak = (const bf16*)d_in[1];
  const bf16* Av = (const bf16*)d_in[2];
  const bf16* Ash = (const bf16*)d_in[3];
  const bf16* Assmk = (const bf16*)d_in[4];
  const bf16* AssmD = (const bf16*)d_in[5];
  const bf16* Bx = (const bf16*)d_in[6];
  const bf16* Bckv = (const bf16*)d_in[7];
  const bf16* Bwq = (const bf16*)d_in[8];
  const bf16* Bwuk = (const bf16*)d_in[9];
  const bf16* Bwuv = (const bf16*)d_in[10];
  const bf16* Bwo = (const bf16*)d_in[11];
  bf16* out = (bf16*)d_out;
  hipFuncSetAttribute((const void*)fused_kernel, hipFuncAttributeMaxDynamicSharedMemorySize, 163840);
  fused_kernel<<<256, 256, 163840, stream>>>(Bckv, Bwuk, Bwuv, Bx, Bwq, (bf16*)d_ws, Ak, Av, Ash, Assmk, AssmD, Bwo, Aq, out);
}
```

```cpp
#include <hip/hip_runtime.h>
#include <hip/hip_bf16.h>
#include <stdint.h>
#include <utility>

typedef __hip_bfloat16 bf16;
typedef __attribute__((ext_vector_type(4))) short bf16x4;
typedef __attribute__((ext_vector_type(2))) unsigned u32x2;

constexpr int SEQ = 2048;
constexpr int DA = 448;
constexpr int NH = 16;
constexpr int HD = 256;
constexpr int DC = 1024;
constexpr int OUTW = 896;
constexpr unsigned TSB = NH * SEQ * HD * 2, O_K = TSB, O_S = 3 * TSB, O_P = 5 * TSB, PSTR = 67584, O_F = O_P + 128 * PSTR,
                   O_X = O_F + 400 * 4352, O_Z = O_X + DA * SEQ * 2, WSB = O_Z + DA * SEQ * 2;
constexpr unsigned fo(int w) { return O_F + (w >> 4) * 4352 + (w & 15) * 8; }

#define KST(kid, which)

__device__ __forceinline__ float bf2f(unsigned short u) { return __uint_as_float(((unsigned)u) << 16); }
__device__ __forceinline__ unsigned short f2bf(float f) { return __bfloat16_as_ushort(__float2bfloat16(f)); }
__device__ __forceinline__ float rbf(float f) { return bf2f(f2bf(f)); }
typedef __attribute__((ext_vector_type(2))) __bf16 bf16x2n;
typedef __attribute__((ext_vector_type(2))) float f32x2n;
__device__ __forceinline__ unsigned pk2(float a, float b) {
  return __builtin_bit_cast(unsigned, __builtin_convertvector((f32x2n){a, b}, bf16x2n));
}

__device__ __forceinline__ int off_b(int row, int ch) { return 256 * row + 16 * (ch ^ (((row & 3) << 2) | ((row >> 2) & 3))); }

template <int R> __device__ __forceinline__ void sset(unsigned x) { asm volatile("s_mov_b32 s%c0, %1" ::"i"(R), "s"(x)); }
template <int R> __device__ __forceinline__ void vsetu(unsigned x) { asm volatile("v_mov_b32 v%c0, %1" ::"i"(R), "v"(x)); }
template <int R> __device__ __forceinline__ void srd_set(const void* p, unsigned n) {
  asm volatile("s_mov_b64 s[%c0:%c1], %4\n\ts_mov_b32 s%c2, %5\n\ts_mov_b32 s%c3, 0x20000" ::"i"(R), "i"(R + 1), "i"(R + 2), "i"(R + 3), "s"(p), "s"(n));
}
template <int R, int L> __device__ __forceinline__ void tload() { asm volatile("v_readlane_b32 s%c0, v78, %1" ::"i"(R), "i"(L)); }
template <int L> __device__ __forceinline__ unsigned tget() { unsigned t; asm volatile("v_readlane_b32 %0, v78, %1" : "=s"(t) : "i"(L)); return t; }
#define FL_SET(S) "buffer_store_dwordx2 v[82:83], v81, s[88:91], " S " offen sc1"
#define FL_ARM(N) "v_cndmask_b32_e64 v80, -2.0, v79, " N
#define FL_POLL "buffer_load_dwordx2 v[124:125], v80, s[88:91], 0 offen sc1"
#define FL_CHECK(N) "s_mov_b32 s48, 0x100000\n7:\n\tv_cmp_eq_u64 vcc, s[84:85], v[124:125]\n\ts_and_b64 vcc, vcc, " N "\n\ts_cmp_eq_u64 vcc, " N \
                    "\n\ts_cbranch_scc1 8f\n\ts_sleep 2\n\t" FL_POLL "\n\ts_waitcnt vmcnt(0)\n\ts_sub_u32 s48, s48, 1\n\ts_cbranch_scc0 7b\n8:"
template <int L> __device__ __forceinline__ void fl_set() {
  asm volatile("v_readlane_b32 s48, v78, %0\n\ts_nop 4\n\t" FL_SET("s48") ::"i"(L) : "memory");
}
template <int L> __device__ __forceinline__ void fl_poll() {
  asm volatile("v_readlane_b32 s50, v78, %0\n\tv_readlane_b32 s51, v78, %1\n\ts_nop 1\n\t" FL_ARM("s[50:51]") "\n\t" FL_POLL ::"i"(L), "i"(L + 1) : "memory");
}
#define TICKET "buffer_atomic_add_x2 v[240:241], v81, s[88:91], s101 offen sc0"
__device__ __forceinline__ void fl_check() { asm volatile(FL_CHECK("s[50:51]") ::: "vcc", "scc", "memory"); }
template <int R, int K> __device__ __forceinline__ unsigned vxor() { unsigned t; asm volatile("v_xor_b32 %0, %1, v%c2" : "=v"(t) : "i"(K), "i"(R)); return t; }
template <int R> __device__ __forceinline__ float vget() { float t; asm volatile("v_mov_b32 %0, v%c1" : "=v"(t) : "i"(R)); return t; }
template <int R> __device__ __forceinline__ void vset(float x) { asm volatile("v_mov_b32 v%c0, %1" ::"i"(R), "v"(x)); }
template <int R> __device__ __forceinline__ float acc_read1() { float t; asm volatile("v_accvgpr_read_b32 %0, a[%c1]" : "=v"(t) : "i"(R)); return t; }
template <class F, int... I> __device__ __forceinline__ void static_for(F&& f, std::integer_sequence<int, I...>) {
  (f(std::integral_constant<int, I>{}), ...);
}
template <int... Is> using iseq = std::integer_sequence<int, Is...>;
template <int N> using make_iseq = std::make_integer_sequence<int, N>;

typedef __attribute__((ext_vector_type(4))) unsigned u32x4c;
typedef __attribute__((ext_vector_type(4))) float f32x4c;
typedef __attribute__((ext_vector_type(2))) float f32x2c;
typedef unsigned long long u64;
struct PR { u32x4c kk[7], vv[4]; };
__device__ __forceinline__ void prep_load(PR& p, int tid, int cg, int tb, const bf16* __restrict__ k, const bf16* __restrict__ v,
                                          const bf16* __restrict__ shift_k) {
  const __amdgpu_buffer_rsrc_t sk = __builtin_amdgcn_make_buffer_rsrc((void*)k, (short)0, SEQ * DA * 2, 0x00020000);
  const __amdgpu_buffer_rsrc_t sv = __builtin_amdgcn_make_buffer_rsrc((void*)v, (short)0, SEQ * DA * 2, 0x00020000);
  const __amdgpu_buffer_rsrc_t sw = __builtin_amdgcn_make_buffer_rsrc((void*)(shift_k + cg * 256), (short)0, 512, 0x00020000);
  const int vo = ((tb * 128 + (tid >> 3) * 4) * DA + cg * 64 + (tid & 7) * 8) * 2;
#pragma unroll
  for (int q = 0; q < 7; ++q)
    asm volatile("buffer_load_dwordx4 %0, %1, %2, 0 offen" : "=v"(p.kk[q]) : "v"(vo + (q - 3) * DA * 2), "s"(sk) : "memory");
#pragma unroll
  for (int q = 0; q < 4; ++q)
    asm volatile("buffer_load_dwordx4 %0, %1, %2, 0 offen offset:%3" : "=v"(p.vv[q]) : "v"(vo), "s"(sv), "i"(q * DA * 2) : "memory");
#pragma unroll
  for (int q = 0; q < 4; ++q)
    asm volatile("buffer_load_dwordx4 a[%c0:%c1], %2, %3, 0 offen offset:%4" ::"i"(4 * q), "i"(4 * q + 3), "v"((tid & 7) * 64), "s"(sw), "i"(q * 16) : "memory");
}
__device__ __forceinline__ float bfl(unsigned u) { return __uint_as_float(u << 16); }
__device__ __forceinline__ float bfh(unsigned u) { return __uint_as_float(u & 0xffff0000u); }
__device__ __forceinline__ f32x2c rbf2(f32x2c a) {
  return (f32x2c){__uint_as_float(pk2(0.f, a[0])), __uint_as_float(pk2(0.f, a[1]))};
}
template <int I> __device__ __forceinline__ void prep_math(const PR& p, int tid, char* lx) {
  const int rp = tid >> 3, c = tid & 7;
  f32x2c wt[4], kf[7];
  const unsigned w[4] = {__float_as_uint(acc_read1<4 * I>()), __float_as_uint(acc_read1<4 * I + 1>()), __float_as_uint(acc_read1<4 * I + 2>()),
                         __float_as_uint(acc_read1<4 * I + 3>())};
#pragma unroll
  for (int t = 0; t < 4; ++t)
    wt[t] = (t & 1) ? (f32x2c){bfh(w[t >> 1]), bfh(w[2 + (t >> 1)])} : (f32x2c){bfl(w[t >> 1]), bfl(w[2 + (t >> 1)])};
#pragma unroll
  for (int q = 0; q < 7; ++q) kf[q] = (f32x2c){bfl(p.kk[q][I]), bfh(p.kk[q][I])};
  f32x2c x[4];
#pragma unroll
  for (int e = 0; e < 4; ++e) {
    f32x2c s = rbf2(kf[e] * wt[3]);
    s = rbf2(s + rbf2(kf[e + 1] * wt[2]));
    s = rbf2(s + rbf2(kf[e + 2] * wt[1]));
    s = rbf2(s + rbf2(kf[e + 3] * wt[0]));
    x[e] = s * (f32x2c){bfl(p.vv[e][I]), bfh(p.vv[e][I])};
  }
#pragma unroll
  for (int u = 0; u < 2; ++u)
    *(u32x2*)(lx + (c * 8 + 2 * I + u) * 256 + (((31 - rp) * 8) ^ (c << 4))) = (u32x2){pk2(x[3][u], x[2][u]), pk2(x[1][u], x[0][u])};
}
__device__ __forceinline__ void prep_store(int tid, int cg, int tb, bf16* __restrict__ xr, const char* lx) {
  const __amdgpu_buffer_rsrc_t sx = __builtin_amdgcn_make_buffer_rsrc((void*)(xr + (size_t)cg * 64 * SEQ), (short)0, 64 * SEQ * 2, 0x00020000);
#pragma unroll
  for (int rd = 0; rd < 4; ++rd) {
    const int idx = tid + 256 * rd, ch = idx >> 4, q = idx & 15;
    u32x4c w = *(const u32x4c*)(lx + ch * 256 + 16 * (q ^ (ch >> 3)));
    __builtin_amdgcn_raw_buffer_store_b128(w, sx, (ch * SEQ + (15 - tb) * 128 + q * 8) * 2, 0, 16);
  }
}

#define CODE_TOUCH(LB, LE, wx, NW, NL, m0v) do { unsigned blo_, bhi_, elo_; \
    asm volatile("s_getpc_b64 s[48:49]\n\ts_add_u32 s48, s48, " LB "@rel32@lo+4\n\ts_addc_u32 s49, s49, " LB "@rel32@hi+12\n\t" \
                 "s_mov_b32 %0, s48\n\ts_mov_b32 %1, s49\n\t" \
                 "s_getpc_b64 s[48:49]\n\ts_add_u32 s48, s48, " LE "@rel32@lo+4\n\ts_mov_b32 %2, s48" \
                 : "=s"(blo_), "=s"(bhi_), "=s"(elo_) :: "scc"); \
    const unsigned n_ = elo_ - (blo_ & ~127u); \
    const __amdgpu_buffer_rsrc_t srd_ = __builtin_amdgcn_make_buffer_rsrc((void*)(((unsigned long long)bhi_ << 32) | (blo_ & ~127u)), (short)0, n_ < 262144u ? n_ : 0u, 0x00020000); \
    const unsigned l_ = threadIdx.x & 63, vo_ = l_ < (NL) ? ((wx) + l_ * (NW)) * 128u : 0x80000000u; \
    asm volatile("s_mov_b32 m0, %0\n\ts_nop 4\n\tbuffer_load_dword %1, %2, 0 offen lds" ::"s"(m0v), "v"(vo_), "s"(srd_) : "memory"); } while (0)
template <int I> __device__ __forceinline__ void d_store() {
  asm volatile("buffer_store_dwordx4 v[%c0:%c1], v%c2, s[88:91], s101 offen offset:%3 sc1" ::"i"(128 + 4 * I), "i"(131 + 4 * I), "i"(112 + ((I >> 2) & 1)),
               "i"((I & 3) * 1024) : "memory");
  if constexpr ((I & 7) == 7) asm volatile("s_addk_i32 s101, 0x4000" ::: "scc");
}
constexpr int NS_X = 19;
struct CV { unsigned pa[4], pb, px, vz, sz; float D; };
constexpr int cv_J(bool up, int q) { int J = up ? 0 : 7; while (q > J) { q -= J + 1; J += up ? 1 : -1; } return J; }
constexpr int cv_i(bool up, int q) { int J = up ? 0 : 7; while (q > J) { q -= J + 1; J += up ? 1 : -1; } return q; }
template <int R, int N> __device__ __forceinline__ void cv_rd(CV& c) {
  if constexpr (N < 16) {
    constexpr int i = N >> 1, o = 16 * R + 128 * (i & 1) + 2 * (N & 1), r = 32 + 32 * (R & 1) + 2 * N;
    asm volatile("ds_read2_b32 a[%c1:%c2], %0 offset0:%c3 offset1:%c4" ::"v"(c.pa[i >> 1]), "i"(r), "i"(r + 1), "i"(o), "i"(o + 1));
  } else {
    constexpr int m = N - 16, r = 192 + 32 * (R & 1) + 4 * m;
    asm volatile("ds_read_b128 a[%c1:%c2], %0 offset:%c3" ::"v"(c.pb), "i"(r), "i"(r + 3), "i"(64 * R + 512 * (7 - m)));
  }
}
template <int O> __device__ __forceinline__ void cv_xd(CV& c) {
  constexpr int e = O & 31, x = 96 + ((e >> 1) ^ 1);
  if constexpr (O >= 64) asm volatile("v_cvt_pk_bf16_f32 v%c0, 0, v%c0" ::"i"(160 + e));
  else if constexpr (O >= 32) asm volatile("v_mul_f32 v%c0, v%c0, %1" ::"i"(160 + e), "v"(c.D));
  else if constexpr (O & 1) asm volatile("v_lshlrev_b32 v%c0, 16, v%c1" ::"i"(160 + e), "i"(x));
  else asm volatile("v_and_b32 v%c0, 0xffff0000, v%c1" ::"i"(160 + e), "i"(x));
}
template <int O> __device__ __forceinline__ void cv_z(CV& c) {
  constexpr int J = O / 11, k = O % 11, y = 128 + 4 * J;
  if constexpr (k < 4) asm volatile("v_cvt_pk_bf16_f32 v%c0, 0, v%c0" ::"i"(y + k));
  else if constexpr (k < 8) asm volatile("v_add_f32 v%c0, v%c0, v%c1" ::"i"(y + k - 4), "i"(y + 28 + k));
  else if constexpr (k < 10) asm volatile("v_cvt_pk_bf16_f32 v%c0, v%c1, v%c2" ::"i"(y + k - 8), "i"(y + 2 * k - 16), "i"(y + 2 * k - 15));
  else asm volatile("buffer_store_dwordx2 v[%c0:%c1], %2, s[88:91], s101 offen sc1\n\ts_add_u32 s101, s101, 0x8000" ::"i"(y), "i"(y + 1), "v"(c.vz) : "memory");
}
constexpr int cv_z0(int Q) {
  int o = 0;
  for (int q = 0; q < Q; ++q)
    for (int k = 0; k < (q % 4 == 1 ? 0 : 3); ++k) { const int J = o / 11; if ((J + 1) * (J + 2) / 2 - 1 + (J ? 4 : 7) <= q) ++o; }
  return o;
}
constexpr int cv_xd0(int T) { int o = 0; for (int t = 0; t < T; ++t) o += t % 4 != 1 || (t >= 72 && t < 108); return o < 96 ? o : 96; }
static_assert(cv_xd0(144) == 96);
template <int B, int... Is> __device__ __forceinline__ void cv_zs(CV& c, iseq<Is...>) { (cv_z<B + Is>(c), ...); }
template <int R, int Q> __device__ __forceinline__ void cv_slot(CV& c) {
  constexpr int i = cv_i(R == 7, Q), J = cv_J(R == 7, Q), d = 128 + 4 * J, a = 32 + 32 * (R & 1) + 4 * i, b = 192 + 32 * (R & 1) + 4 * (J - i);
  constexpr int T = 36 * R + Q, vm = Q % 4 == 1 && Q < 32 ? Q / 4 : -1;
  if constexpr (Q == 0) asm volatile("s_waitcnt lgkmcnt(0)");
  if constexpr (R >= 6 && vm >= 0 && vm % 4 == 0) asm volatile("s_add_u32 m0, s79, %0" ::"i"(vm * 1024));
  if constexpr (R == 0 && i == 0)
    asm volatile("v_mfma_f32_16x16x32_bf16 v[%c0:%c1], a[%c2:%c3], a[%c4:%c5], 0" ::"i"(d), "i"(d + 3), "i"(a), "i"(a + 3), "i"(b), "i"(b + 3));
  else
    asm volatile("v_mfma_f32_16x16x32_bf16 v[%c0:%c1], a[%c2:%c3], a[%c4:%c5], v[%c0:%c1]" ::"i"(d), "i"(d + 3), "i"(a), "i"(a + 3), "i"(b), "i"(b + 3));
  if constexpr (Q < 24 && R < 7) cv_rd<R + 1, Q>(c);
  if constexpr (T % 7 == 3 && NS_X + 5 + T / 7 < 32) d_store<NS_X + 5 + T / 7>();
  if constexpr (R == 2 && Q >= 24 && Q < 32)
    asm volatile("ds_read_b64 v[%c1:%c2], %0 offset:%c3" ::"v"(c.px), "i"(96 + 2 * (Q - 24)), "i"(97 + 2 * (Q - 24)), "i"(512 * (31 - Q)));
  if constexpr (R >= 3 && R < 7) if constexpr (cv_xd0(T - 107) > cv_xd0(T - 108)) cv_xd<cv_xd0(T - 108)>(c);
  if constexpr ((R == 3 || R == 4) && vm >= 0) {
    constexpr int f = (R - 3) * 8 + vm;
    asm volatile("buffer_load_dwordx4 a[%c0:%c1], v84, s[88:91], s96 offen offset:%2 sc1" ::"i"(128 + 4 * f), "i"(131 + 4 * f), "i"((f & 3) * 1024) : "memory");
    if constexpr ((f & 3) == 3) asm volatile("s_addk_i32 s96, %0" ::"i"(f == 7 ? 28672 : 4096));
  }
  if constexpr (R == 4 && Q == 35) asm volatile(FL_POLL ::: "memory");
  if constexpr (R >= 6 && vm >= 0)
    asm volatile("buffer_load_dwordx4 v%c0, s[88:91], s98 offen offset:%1 sc1 lds" ::"i"(86 + vm / 4), "i"((vm & 3) * 1024) : "memory");
  if constexpr (R == 6 && Q == 35) asm volatile("s_add_u32 s98, s98, 0x8000\n\ts_xor_b32 s79, s79, 0x8000");
  if constexpr (R == 7) cv_zs<cv_z0(Q)>(c, make_iseq<cv_z0(Q + 1) - cv_z0(Q)>{});
  __builtin_amdgcn_sched_barrier(0);
}
template <int R, int... Q> __device__ __forceinline__ void cv_step(CV& c, iseq<Q...>) { (cv_slot<R, Q>(c), ...); }
template <int... N> __device__ __forceinline__ void cv_rd0(CV& c, iseq<N...>) { (cv_rd<0, N>(c), ...); }
template <int FLAG> __device__ __forceinline__ void conv_wave(unsigned sz, int ck, float Dd, char* lw) {
  int lane = threadIdx.x & 63; asm volatile("" : "+v"(lane));
  const int l16 = lane & 15, fq = lane >> 4;
  u32x4c w[5]; unsigned w4[5];
#pragma unroll
  for (int r = 0; r < 5; ++r) {
    w[r] = *(const u32x4c*)(lw + r * 1024 + lane * 16);
    w4[r] = *(const unsigned*)(lw + r * 1024 + lane * 16 + 16);
  }
  d_store<NS_X>(); d_store<NS_X + 1>();
#pragma unroll
  for (int r = 0; r < 5; ++r)
    *(u32x4c*)(lw + 10304 + r * 1024 + lane * 16) = (u32x4c){__builtin_amdgcn_alignbit(w[r][1], w[r][0], 16), __builtin_amdgcn_alignbit(w[r][2], w[r][1], 16),
                                                             __builtin_amdgcn_alignbit(w[r][3], w[r][2], 16), __builtin_amdgcn_alignbit(w4[r], w[r][3], 16)};
  d_store<NS_X + 2>();
  CV c;
#pragma unroll
  for (int i = 0; i < 4; ++i) c.pa[i] = (unsigned)(size_t)lw + ((l16 + 1) & 1) * 10304 + 4 * ((8 * fq + l16 + 1) >> 1) + 1024 * i;
  c.pb = (unsigned)(size_t)lw + 5120 + (2032 - 16 * l16 + 8 * fq) * 2 - 512 * 7;
  c.vz = (l16 >> 1) * 4096 + (((l16 & 1) * 2 + (fq >> 1)) ^ (ck >> 1)) * 16 + (fq & 1) * 8;
  c.px = (unsigned)(size_t)lw + 5120 + 2044 * 2 - 512 * 7 - (16 * l16 + 4 * fq) * 2;
  c.sz = sz; c.D = Dd;
  asm volatile("" : "+v"(c.vz), "+v"(c.px), "+v"(c.D) :: "memory");
  asm volatile("" : "+s"(c.sz));
  cv_rd0(c, make_iseq<24>{});
  d_store<NS_X + 3>(); d_store<NS_X + 4>();
  __builtin_amdgcn_sched_barrier(0);
  static_for([&](auto R) __attribute__((always_inline)) {
    cv_step<R>(c, make_iseq<36>{});
    if constexpr (R == 2) {
      asm volatile("s_waitcnt vmcnt(0)\n\ts_barrier" ::: "memory");
      fl_set<FLAG>();
      sset<101>(c.sz);
    }
    if constexpr (R == 5) {
      asm volatile("s_waitcnt vmcnt(0)" ::: "memory");
      fl_check();
    }
    __builtin_amdgcn_sched_barrier(0);
  }, make_iseq<8>{});
  asm volatile("s_nop 7");
  cv_zs<cv_z0(36)>(c, make_iseq<88 - cv_z0(36)>{});
  __builtin_amdgcn_sched_barrier(0);
}

__device__ __forceinline__ void post_dma(int cg, int tb, const bf16* __restrict__ q, unsigned m0w) {
  int lane = threadIdx.x & 63; asm volatile("" : "+v"(lane));
  const __amdgpu_buffer_rsrc_t sq = __builtin_amdgcn_make_buffer_rsrc((void*)(q + (size_t)tb * 32 * DA + cg * 64), (short)0, 32 * DA * 2, 0x00020000);
  const unsigned vq = ((lane >> 3) * DA + (lane & 7) * 8) * 2;
  asm volatile("s_mov_b32 m0, %0\n\ts_nop 4\n\tbuffer_load_dwordx4 %1, %2, 0 offen lds\n\tbuffer_load_dwordx4 %1, %2, %3 offen offset:1024 lds\n\t"
               "buffer_load_dwordx4 %1, %2, %4 offen offset:2048 lds\n\tbuffer_load_dwordx4 %1, %2, %5 offen offset:3072 lds"
               ::"s"(m0w + 4096), "v"(vq), "s"(sq), "s"(8 * DA * 2 - 1024), "s"(16 * DA * 2 - 2048), "s"(24 * DA * 2 - 3072) : "memory");
}
__device__ __forceinline__ void post_math(int cg, int tb, bf16* __restrict__ out, const char* lw) {
  int lane = threadIdx.x & 63; asm volatile("" : "+v"(lane));
  const int tl = lane >> 3, ck = lane & 7;
  const __amdgpu_buffer_rsrc_t so = __builtin_amdgcn_make_buffer_rsrc((void*)(out + (size_t)tb * 32 * OUTW + cg * 64), (short)0, 32 * OUTW * 2 - 2 * DA, 0x00020000);
  const unsigned vo = (tl * OUTW + ck * 8) * 2;
#pragma unroll
  for (int i = 0; i < 4; ++i) {
    const u32x4c qv = *(const u32x4c*)(lw + 4096 + i * 1024 + lane * 16);
    const char* lz = lw + ck * 64 + 16 * (i ^ (ck >> 1)) + tl * 2;
    u32x4c o;
#pragma unroll
    for (int e = 0; e < 4; ++e) {
      const float z0 = bf2f(*(const unsigned short*)(lz + 2 * e * 512));
      const float z1 = bf2f(*(const unsigned short*)(lz + (2 * e + 1) * 512));
      o[e] = pk2(bfl(qv[e]) * z0, bfh(qv[e]) * z1);
    }
    __builtin_amdgcn_raw_buffer_store_b128(o, so, vo + i * 8 * OUTW * 2, 0, 17);
  }
}

#define DI __device__ __forceinline__
#define GAP() __builtin_amdgcn_sched_barrier(0)
typedef __attribute__((ext_vector_type(4))) float f32x4g;
typedef __attribute__((ext_vector_type(4))) unsigned u32x4g;
constexpr int LDW = NH * HD;
struct GM { unsigned aA[2], aB[2][8]; };
template <bool W, int J, int V> DI void g_dma() {
  if constexpr (J == 0) asm volatile("buffer_load_dwordx4 v%c0, s[%c1:%c2], 0 offen lds" ::"i"(V), "i"(W ? 60 : 56), "i"(W ? 63 : 59) : "memory");
  else asm volatile("buffer_load_dwordx4 v%c0, s[%c1:%c2], s%c3 offen lds" ::"i"(V), "i"(W ? 60 : 56), "i"(W ? 63 : 59), "i"((W ? 70 : 63) + J) : "memory");
}
template <int P, int KS, int I> DI void g_wread(GM& g) {
  constexpr int nf = I >> 1, hf = I & 1, r = 192 - 64 * P + 8 * nf + 4 * KS + 2 * hf;
  asm volatile("ds_read_b64_tr_b16 a[%c1:%c2], %0 offset:%c3" ::"v"(g.aB[hf][nf]), "i"(r), "i"(r + 1), "i"(P * 32768 + KS * 16384));
}
template <int P, int M, int KS> DI void g_aread(GM& g) {
  constexpr int r = 96 + 8 * (M & 1) + 4 * KS;
  asm volatile("ds_read_b128 v[%c1:%c2], %0 offset:%c3" ::"v"(g.aA[KS]), "i"(r), "i"(r + 3), "i"(P * 32768 + M * 2048));
}
constexpr float CS = 0.0625f * 1.4426950408889634f;
constexpr int NS_Q = 19;
constexpr int e_vm(int G) { return (G * 3) % 8 < 3 ? G * 3 / 8 : -1; }
constexpr int e_st(int MODE, int G) {
  const int q = G & 15, k3 = ((G >> 4) - 1) * 3 + (q == 0 ? 0 : q == 4 ? 1 : 2), k2 = NS_Q + (G >> 4) * 2 + (q == 4);
  if (MODE == 5) return e_vm(G) >= 32 ? e_vm(G) - 32 : -1;
  if (MODE == 3) return G >= 33 && (G - 33) % 5 == 0 && (G - 33) / 5 < NS_X ? (G - 33) / 5 : -1;
  if (MODE == 2) return G >= 30 && (q == 0 || q == 4 || q == 14) && k3 - 2 < NS_Q ? k3 - 2 : -1;
  if (MODE == 4) return (q == 0 || q == 4) && k2 < 32 ? k2 : -1;
  return -1;
}
constexpr int e_nst(int MODE, int G0, int G1) { int n = 0; for (int G = G0; G < G1; ++G) n += e_st(MODE, G) >= 0; return n; }
static_assert(e_nst(2, 0, 128) == NS_Q && e_nst(3, 30, 128) == NS_X && e_nst(5, 0, 128) == 16 && e_nst(4, 0, 104) == 32 - NS_Q);
constexpr int e_slot(int MODE, int G) { return MODE == 3 ? (G % 3 == 0 && G < 30 ? G / 3 : -1) : e_vm(G) >= 0 && e_vm(G) < 32 ? 10 + e_vm(G) : -1; }
struct ES { int op0[129], nop[129]; };
constexpr ES e_sched(bool TR, int MODE) {
  ES z{};
  const bool SCL = MODE == 2 || MODE == 4;
  const int per = SCL ? 16 : 8, o1 = 32 * per;
  int o = MODE == 4 ? e_sched(false, 2).op0[128] : 0;
  for (int G = 0; G <= 128; ++G) {
    const int q = G & 15;
    int cap = 2 - (q == 1 || q == 2 || q == 15);
    if (SCL) cap -= (q >= 3 && q < 11 && (q & 1)) + (q == 7 || q == 11) + 2 * (G == 70 || G == 74 || (G & 63) == 61);
    cap += MODE == 2 ? 4 + (q == 10 || q == 13) : MODE == 3 ? 1 : MODE == 5 ? 2 : 0;
    if (e_st(MODE, G) >= 0 || (SCL ? q == 8 || q == 12 : e_slot(MODE, G) >= 0) || (MODE == 4 && G < 4)) cap = 0;
    if (G == 128) cap = 10000;
    z.op0[G] = o;
    while (o < o1) {
      const int i = o / per, rdy = MODE == 4 ? 0 : (TR ? 32 * (i >> 3) + 16 + 2 * (i & 7) + 1 : 4 * i + 3) + 6;
      if ((G < rdy && G < 128) || cap-- <= 0) break;
      ++o; ++z.nop[G];
    }
  }
  return z;
}
template <bool TR, int MODE> constexpr ES ESCH = e_sched(TR, MODE);
static_assert(ESCH<false, 4>.op0[96] == 512 && ESCH<false, 4>.op0[0] >= 24 * 16);
template <bool TR, bool SCL, int O> DI void e_op() {
  constexpr int per = SCL ? 16 : 8, i = O / per, k = O % per, r = 128 + 4 * i, t = 114 + 4 * (i & 1), c = k - (per - 4);
  if constexpr (k < 4) asm volatile("v_accvgpr_read_b32 v%c0, a[%c1]" ::"i"(t + k), "i"(4 * i + k));
  else if constexpr (c < 0) asm volatile("v_mul_f32 v%c0, s83, v%c0" ::"i"(k < 8 ? r + (k ^ 6) : t + k - 8));
  else {
    constexpr int d = c == 0 ? r + 3 : c == 1 ? r + 2 : r + c - 2, x = c == 0 ? r + 2 : c == 1 ? r : t + 2 * (c - 2);
    asm volatile("v_cvt_pk_bf16_f32 v%c0, v%c1, v%c2" ::"i"(d), "i"(x), "i"(x + 1));
  }
}
template <bool TR, bool SCL, int B, int... Is> DI void e_ops(iseq<Is...>) { (e_op<TR, SCL, B + Is>(), ...); }
template <int S> DI void e_m0() {
  if constexpr (S == 0 || S == 1 || S == 5 || S == 9) asm volatile("s_add_u32 m0, s78, %0" ::"i"(S * 1024));
  if constexpr (S >= 26 && (S - 26) % 4 == 0) asm volatile("s_add_u32 m0, s79, %0" ::"i"(((S - 26) >> 3) * 32768 + ((S - 26) & 4) * 1024));
}
template <int S> DI void e_load() {
  if constexpr (S == 0) asm volatile("buffer_load_dwordx4 v122, s[56:59], 0 offen lds" ::: "memory");
  else if constexpr (S < 5) asm volatile("buffer_load_dwordx4 v84, s[56:59], s64 offen offset:%0 lds" ::"i"((S - 1) * 1024) : "memory");
  else if constexpr (S < 9) asm volatile("buffer_load_dwordx4 v84, s[60:63], 0 offen offset:%0 sc1 lds" ::"i"((S - 5) * 1024) : "memory");
  else if constexpr (S == 9) asm volatile("buffer_load_dwordx4 v84, s[60:63], s62 offen sc1 lds" ::: "memory");
  else if constexpr (S < 26) {
    constexpr int f = S - 10;
    asm volatile("buffer_load_dwordx4 a[%c0:%c1], v84, s[88:91], s96 offen offset:%2 sc1" ::"i"(128 + 4 * f), "i"(131 + 4 * f), "i"((f & 3) * 1024) : "memory");
    if constexpr ((f & 3) == 3) asm volatile("s_addk_i32 s96, %0" ::"i"(f == 7 ? 28672 : 4096));
  } else {
    constexpr int f = S - 26;
    asm volatile("buffer_load_dwordx4 v%c0, s[88:91], s98 offen offset:%1 sc1 lds" ::"i"(86 + ((f >> 2) & 1)), "i"((f & 3) * 1024) : "memory");
    if constexpr (f == 7) asm volatile("s_add_u32 s98, s98, s97");
    if constexpr (f == 15) asm volatile(FL_POLL ::: "memory");
  }
}
template <bool TR, int P, int H, int G, int MODE> DI void g_gap(GM& g) {
  constexpr int m = 4 * H + (G >> 4), q = G & 15, n = q >> 1, ks = q & 1, GG = 64 * H + G;
  constexpr bool inV = TR ? m & 1 : n & 1, END = MODE == 3 || MODE == 5;
  constexpr int a = 96 + 8 * (m & 1) + 4 * ks, b = 192 - 64 * P + 8 * n + 4 * ks, d = (inV ? 128 : 0) + 4 * (TR ? 8 * (m >> 1) + n : 4 * m + (n >> 1));
  constexpr int J = END ? -1 : q == 8 ? 2 * (G >> 4) : q == 12 ? 2 * (G >> 4) + 1 : -1, gj = (J & 3) ^ ((J & 3) >> 1);
  if constexpr (END) { if constexpr (q == 0 && GG > 0) asm volatile("s_waitcnt lgkmcnt(0)"); e_m0<e_slot(MODE, GG)>(); }
  else if constexpr (q == 0 && G > 0) asm volatile("s_waitcnt lgkmcnt(4)");
  if constexpr (J == 0) asm volatile("s_add_u32 m0, s78, %0" ::"i"((P ^ H ^ 1) * 32768 + (1 - H) * 8192));
  else if constexpr (J == 4) asm volatile("s_add_u32 m0, s79, %0" ::"i"((P ^ H) * 32768 + (1 - H) * 16384));
  else if constexpr (J > 0 && (J & 3) == 1) asm volatile("s_bitset1_b32 m0, 12");
  else if constexpr (J > 0 && (J & 3) == 2) asm volatile("s_bitset1_b32 m0, %0" ::"i"(J < 4 ? 14 : 13));
  else if constexpr (J > 0) asm volatile("s_bitset0_b32 m0, 12");
#define GMF(D, X, Y, C) asm volatile("v_mfma_f32_16x16x32_bf16 " D "[%c0:%c1], " X ", " Y ", " C ::"i"(d), "i"(d + 3), "i"(a), "i"(a + 3), "i"(b), "i"(b + 3))
#define GMF2(D, C) do { if constexpr (TR) GMF(D, "v[%c2:%c3]", "a[%c4:%c5]", C); else GMF(D, "a[%c4:%c5]", "v[%c2:%c3]", C); } while (0)
  if constexpr ((MODE == 1 || MODE == 4) && ks == 0) { if constexpr (inV) GMF2("v", "0"); else GMF2("a", "0"); }
  else if constexpr (inV) GMF2("v", "v[%c0:%c1]");
  else GMF2("a", "a[%c0:%c1]");
#undef GMF2
#undef GMF
  if constexpr ((q == 1 || q == 2) && !(END && m == 7)) g_aread<(m < 7 ? P : P ^ 1), (m + 1) & 7, q - 1>(g);
  if constexpr (!END && q >= 3 && q < 11 && (q & 1)) g_wread<P ^ 1, H, (G >> 4) * 4 + (q - 3) / 2>(g);
  if constexpr (MODE == 1 && GG < 7) {
    if constexpr (GG == 0) asm volatile("s_barrier" ::: "memory");
    if constexpr (GG == 2) asm volatile("s_add_u32 m0, s79, %0" ::"i"(P * 32768));
    if constexpr (GG >= 3) g_dma<true, (GG - 3) ^ ((GG - 3) >> 1), 91>();
    if constexpr (GG == 3 || GG == 4) asm volatile("s_bitset1_b32 m0, %0" ::"i"(9 + GG));
    if constexpr (GG == 5) asm volatile("s_bitset0_b32 m0, 12");
  }
  if constexpr (e_st(MODE, GG) >= 0) {
    static_assert(ESCH<TR, MODE>.op0[GG] >= (e_st(MODE, GG) + 1) * (END ? 8 : 16));
    d_store<e_st(MODE, GG)>();
  }
  if constexpr (J >= 0 && J < 4) g_dma<false, (1 - H) * 2 + (gj & 1) + (gj >> 1) * 4, 90>();
  else if constexpr (J >= 4) g_dma<true, (1 - H) * 4 + gj, 91>();
  if constexpr (MODE >= 2) e_ops<TR, !END, ESCH<TR, MODE>.op0[GG]>(make_iseq<ESCH<TR, MODE>.nop[GG]>{});
  if constexpr (END) {
    if constexpr (e_slot(MODE, GG) >= 0) e_load<e_slot(MODE, GG)>();
  } else {
    if constexpr (H == 1 && G == 6) asm volatile("v_lshl_add_u32 v90, s82, 7, v92\n\ts_and_b32 s82, s80, 15");
    if constexpr (H == 1 && G == 10) asm volatile("s_lshr_b64 s[80:81], s[80:81], 4\n\tv_lshl_add_u32 v91, s82, 19, v93");
    if constexpr (G == 61) {
      constexpr int nst = e_nst(MODE, H ? 29 : 0, GG + 1) + (MODE == 4 && !H ? e_nst(2, 93, 128) : 0);
      asm volatile("s_waitcnt vmcnt(%0) lgkmcnt(0)\n\ts_barrier" ::"i"(12 + nst) : "memory");
    }
  }
  if constexpr (END && GG == 127) {
    asm volatile("s_nop 7");
    e_ops<TR, false, ESCH<TR, MODE>.op0[128]>(make_iseq<ESCH<TR, MODE>.nop[128]>{});
  }
  GAP();
}
template <bool TR, int P, int H, int MODE, int... Gs> DI void g_half_(GM& g, iseq<Gs...>) { (g_gap<TR, P, H, Gs, MODE>(g), ...); }
template <bool TR, int P, int H, int MODE> DI void g_half(GM& g) { g_half_<TR, P, H, MODE>(g, make_iseq<64>{}); }
template <bool TR, int P, int MODE> DI void g_tile(GM& g) { g_half<TR, P, 0, MODE>(g); g_half<TR, P, 1, MODE>(g); }
template <int P, int... Is> DI void g_read0(GM& g, iseq<Is...>) {
  (g_wread<P, 0, Is>(g), ...); (g_wread<P, 1, Is>(g), ...);
  g_aread<P, 0, 0>(g); g_aread<P, 0, 1>(g);
}
DI void g_proW(unsigned g, unsigned mW) {
  asm volatile("v_lshl_add_u32 v91, %0, 18, v93\n\ts_mov_b32 m0, %1" ::"s"(g), "s"(mW));
  asm volatile("s_nop 0"); g_dma<true, 0, 91>();
  asm volatile("s_bitset1_b32 m0, 12\n\ts_nop 0"); g_dma<true, 1, 91>();
  asm volatile("s_bitset1_b32 m0, 13\n\ts_nop 0"); g_dma<true, 3, 91>();
  asm volatile("s_bitset0_b32 m0, 12\n\ts_nop 0"); g_dma<true, 2, 91>();
}
template <int PART = 3> DI void g_pro(unsigned i, unsigned mA, unsigned mW, unsigned lda) {
  i &= 3;
  if constexpr (PART & 1)
    asm volatile("v_add_u32 v90, %0, v92\n\tv_add_u32 v91, %1, v93\n\ts_mov_b32 m0, %2\n\ts_mov_b32 s54, %3" ::"s"(((i & 1) + (i >> 1) * 4) * 64 * lda),
                 "s"((i & 1) * 0x20000 + (i >> 1) * 256), "s"(mA + (i & 1) * 4096 + (i >> 1) * 16384), "s"(mW + i * 4096));
  if constexpr (PART == 1) return;
  g_dma<false, 0, 90>();
  asm volatile("s_bitset1_b32 m0, 13\n\ts_nop 0"); g_dma<false, 2, 90>();
  asm volatile("s_mov_b32 m0, s54\n\ts_nop 0"); g_dma<true, 0, 91>();
  asm volatile("s_bitset1_b32 m0, 14\n\ts_nop 0"); g_dma<true, 4, 91>();
  asm volatile("s_xor_b32 m0, s54, 0x8000\n\ts_nop 0\n\tbuffer_load_dwordx4 v91, s[60:63], s53 offen lds" ::: "memory", "scc");
}
DI void g_pro2(unsigned mA, unsigned mW) {
  asm volatile("v_add_u32 v90, 128, v92\n\ts_mov_b32 m0, %0" ::"s"(mA ^ 32768));
  asm volatile("s_nop 0"); g_dma<false, 0, 90>();
  asm volatile("s_bitset1_b32 m0, 12\n\ts_nop 0"); g_dma<false, 1, 90>();
  asm volatile("s_bitset1_b32 m0, 14\n\ts_nop 0"); g_dma<false, 5, 90>();
  asm volatile("s_bitset0_b32 m0, 12\n\ts_nop 0"); g_dma<false, 4, 90>();
  g_proW(3, mW ^ 49152);
}
template <bool TR, bool SCL, class H0, class H1> DI void g_loop(GM& g, H0&& early, H1&& late) {
  constexpr int nkt = SCL ? 7 : 16, P0 = SCL ? 0 : 1;
#pragma unroll 1
  for (int t = 1;; t += 2) {
    asm volatile("" : "+s"(t));
    g_half<TR, P0 ^ 1, 0, 0>(g);
    if (__builtin_expect(t == (SCL ? 5 : 13), 0)) { late(); GAP(); }
    g_half<TR, P0 ^ 1, 1, 0>(g);
    if (SCL && t + 2 >= nkt) break;
    g_half<TR, P0, 0, 0>(g);
    if (__builtin_expect(t == (SCL ? 3 : 1), 0)) { early(); GAP(); }
    if constexpr (!SCL) asm volatile("s_mov_b32 s78, s83");
    g_half<TR, P0, 1, 0>(g);
    if (!SCL && t + 3 >= nkt) break;
  }
}

typedef __attribute__((ext_vector_type(4))) unsigned u32x4;
typedef __attribute__((ext_vector_type(4))) float f32x4;

constexpr int R_S = 128, R_P = 192, R_CI = 224, R_E = 96;
constexpr int R_TS = 112, R_RS = 114, R_M = 116;
constexpr int R_PD = 118, R_THR = 120, R_TMP = 121;
constexpr int r_dk(int q) { return q < 6 ? 90 + q : 120 + q; }
constexpr int S_V = 96, S_T = 97, S_K = 98, S_I = 99, S_EV = 100;
struct AC {
  unsigned rare;
  unsigned tb, vs, o0, o1;
};
DI bool redo() { u64 t; asm volatile("s_mov_b64 %0, s[54:55]" : "=s"(t)); return t != 0; }
template <int X, int F> DI void kread() {
  constexpr int r = 192 + 4 * (F & 15);
  asm volatile("ds_read_b128 a[%c0:%c1], v84 offset:%c2" ::"i"(r), "i"(r + 3), "i"(X * 32768 + F * 1024));
}
template <int X, int F> DI void vread() {
  constexpr int r = 192 + 4 * (F & 15);
  asm volatile("ds_read_b128 a[%c0:%c1], v85 offset:%c2" ::"i"(r), "i"(r + 3), "i"(X * 32768 + (F & 1) * 16384 + (F >> 1) * 1024));
}
template <int X, int... Fs> DI void kreads(iseq<Fs...>) { (kread<X, Fs>(), ...); }
template <int X, int J> DI void dma_m0() {
  if constexpr (J == 0) asm volatile("s_mov_b32 m0, s%c0" ::"i"(78 + X));
  else if constexpr (J == 4) asm volatile("s_or_b32 m0, s%c0, 0x1000" ::"i"(78 + X));
  else if constexpr (J == 8) asm volatile("s_bitset0_b32 m0, 16");
  else if constexpr (J == 12) asm volatile("s_bitset0_b32 m0, 12");
}
template <int J> DI void dma_at() {
  if constexpr (J < 8)
    asm volatile("buffer_load_dwordx4 v%c0, s[88:91], s96 offen offset:%1 sc1 lds" ::"i"(86 + (J >= 4)), "i"((J & 3) * 1024) : "memory");
  else
    asm volatile("buffer_load_dwordx4 v%c0, s[92:95], s98 offen offset:%1 sc1 lds" ::"i"(r_dk(J - 8)), "i"((J & 3) * 1024) : "memory");
}
template <int Q> DI void dma4k() {
  asm volatile("s_xor_b32 m0, s79, %0\n\ts_add_u32 s48, s98, %1\n\tbuffer_load_dwordx4 v%c2, s[88:91], s48 offen sc1 lds\n\t"
               "buffer_load_dwordx4 v%c2, s[88:91], s48 offen offset:1024 sc1 lds\n\tbuffer_load_dwordx4 v%c2, s[88:91], s48 offen offset:2048 sc1 lds\n\t"
               "buffer_load_dwordx4 v%c2, s[88:91], s48 offen offset:3072 sc1 lds"
               ::"i"((Q >> 1) * 32768 + (Q & 1) * 4096), "i"((Q >> 1) * 32768), "i"(86 + (Q & 1)) : "memory", "scc");
}
DI void acc_zero() { static_for([](auto I) { asm volatile("v_accvgpr_write_b32 a[%c0], 0" ::"i"((int)I)); }, make_iseq<128>{}); }
#define SMV 0
struct SMS { int ex[128], op0[128], nop[128], last; };
constexpr SMS sm_sched() {
  SMS z{};
  int et[32] = {}, e = 0;
  for (int t = 0; t < 128; ++t) {
    const int g = t & 15;
    const bool x = t < 48 ? (g >= 8 && (g & 1) && g < 15) : t < 64 ? (g >= 9 && g < 15) : (g >= 8 && g < 14);
    z.ex[t] = x && e < 32 ? e : -1;
    if (z.ex[t] >= 0) et[e++] = t;
  }
  int o = 0;
  for (int t = 0; t < 128; ++t) {
    const int g = t & 15;
    int cap = z.ex[t] >= 0 || t < 8 || (t < 48 && (g == 7 || (g >= 8 && !(g & 1)))) || t == 56 || t == 63 || t == 111 ? 0 : 1;
    z.op0[t] = o; z.nop[t] = 0;
    while (cap > 0 && o < 48 && et[2 * (o / 3) + (o % 3 == 0 ? 0 : 1)] + 2 <= t) {
      const int el = 2 * (o / 3) + o % 3;
      if (!(o % 3 < 2 && (el == 0 || el == 4))) --cap;
      ++o; ++z.nop[t]; z.last = t;
    }
  }
  for (int k = 16; k < 32; ++k) if (z.op0[et[k]] < 3 * ((k - 16) / 2 + 1)) z.last = 1000;
  return z;
}
constexpr SMS SM = sm_sched();
static_assert(SM.last <= 117);
constexpr int p_reg(int Y, int k) { return R_P + 16 * Y + 4 * ((k >> 3) * 2 + ((k >> 1) & 1)) + ((k >> 2) & 1) * 2 + (k & 1); }
template <int Y, int O> DI void sm_op() {
  constexpr int k = O / 3, e = 2 * k + O % 3, ts = R_TS + ((e >> 2) & 1);
  if constexpr (O % 3 == 2) asm volatile("v_cvt_pk_bf16_f32 v%c0, v%c1, v%c2" ::"i"(p_reg(Y, k)), "i"(R_E + ((2 * k) & 15)), "i"(R_E + ((2 * k + 1) & 15)));
  else if constexpr (e == 1 || e == 5) asm volatile("v_add_f32_e32 v%c0, v%c1, v%c2" ::"i"(ts), "i"(R_E + e - 1), "i"(R_E + e));
  else if constexpr (e != 0 && e != 4) asm volatile("v_add_f32_e32 v%c0, v%c0, v%c1" ::"i"(ts), "i"(R_E + (e & 15)));
}
template <int Y, int T> DI void sm_gap(AC& c) {
  if constexpr (SM.ex[T] >= 0) asm volatile("v_exp_f32 v%c0, v%c1" ::"i"(R_E + (SM.ex[T] & 15)), "i"(R_S + 32 * Y + SM.ex[T]));
  if constexpr (SM.nop[T] > 0) sm_op<Y, SM.op0[T]>();
  if constexpr (SM.nop[T] > 1) sm_op<Y, SM.op0[T] + 1>();
  if constexpr (SM.nop[T] > 2) sm_op<Y, SM.op0[T] + 2>();
  if constexpr (T == SM.last + 1) asm volatile("v_add_f32_e32 v%c0, v%c1, v%c2" ::"i"(R_TMP), "i"(R_TS), "i"(R_TS + 1));
  if constexpr (T == SM.last + 2) asm volatile("v_cmp_nle_f32_e64 s[54:55], v%c0, v%c1" ::"i"(R_TMP), "i"(R_THR));
}
template <int X, int MODE, int G> DI void gapA(AC& c) {
  constexpr bool FIRST = MODE == 1 || MODE == 2, NOQK = MODE == 3;
  constexpr int Xp = X ^ 1, kb = G >> 4, g = G & 15, qb = g >> 3, s = g & 7;
  if constexpr (MODE == 3 && G < 48 && G % 3 == 0) {
    constexpr int f = G / 3;
    asm volatile("buffer_load_dwordx4 a[%c0:%c1], v84, s[88:91], s75 offen offset:%2 sc1" ::"i"(128 + 4 * f), "i"(131 + 4 * f), "i"((f & 3) * 1024) : "memory");
    if constexpr ((f & 3) == 3) asm volatile("s_addk_i32 s75, %0" ::"i"(f == 7 ? 28672 : 4096));
  }
  if constexpr (G == 48) asm volatile("s_waitcnt vmcnt(%0) lgkmcnt(0)\n\ts_barrier" ::"i"(MODE == 3 ? 16 : MODE == 2 ? 12 : MODE == 1 ? 7 : 0) : "memory");
  else if constexpr (g == 0 && !NOQK) asm volatile("s_waitcnt lgkmcnt(0)");
  if constexpr (!NOQK) {
    constexpr int r = 192 + 4 * ((kb & 1) * 8 + s), q = 128 + 4 * g;
    constexpr int d = R_S + 32 * X + 4 * (kb * 2 + qb), ci = s == 0 ? R_CI + 4 * (kb * 2 + qb) : d;
    if constexpr (FIRST && s == 0)
      asm volatile("v_mfma_f32_16x16x32_bf16 v[%c0:%c1], a[%c2:%c3], a[%c4:%c5], 0" ::"i"(d), "i"(d + 3), "i"(r), "i"(r + 3), "i"(q), "i"(q + 3));
    else
      asm volatile("v_mfma_f32_16x16x32_bf16 v[%c0:%c1], a[%c2:%c3], a[%c4:%c5], v[%c6:%c7]" ::"i"(d), "i"(d + 3), "i"(r), "i"(r + 3), "i"(q), "i"(q + 3),
                   "i"(ci), "i"(ci + 3));
    if constexpr (g < 8 && kb < 3) kread<X, (kb + 1) * 8 + g>();
    constexpr int S0 = MODE == 1 ? 0 : SMV ? 40 : 14, S1 = MODE == 1 ? 1 : SMV ? 41 : 30, S2 = SMV ? 42 : 45, S3 = SMV ? 43 : 46;
    if constexpr (G == S0) asm volatile("s_add_u32 s96, s97, 0x1000000");
    if constexpr (G == S1) dma_m0<X, 0>();
    if constexpr (G == S2) asm volatile("s_mov_b32 s97, s98");
    if constexpr (G == S3) asm volatile("s_add_u32 s98, s98, s99");
    if constexpr (MODE != 1 && G >= 56 && !(G & 1)) dma_at<(G - 56) / 2>();
    if constexpr (MODE == 1 && (G & 11) == 9) dma_at<(G >> 4) * 2 + ((G >> 2) & 1)>();
    if constexpr (MODE == 1 && G == 31) dma_m0<X, 4>();
    if constexpr (G == 57) asm volatile("s_lshr_b32 s100, s100, 1\n\ts_add_u32 s56, s56, 1");
    if constexpr (G == 59) asm volatile("s_getpc_b64 s[86:87]\nev_pc%c0:\n\ts_add_u32 s86, s86, ev_ret%c0-ev_pc%c0\n\ts_addc_u32 s87, s87, 0" ::"i"(X * 4 + MODE));
    if constexpr (G == 61 && !FIRST) asm volatile("s_or_b32 s57, s54, s55\n\ts_mov_b32 %0, s57\n\ts_bitcmp1_b32 s100, 0" : "=s"(c.rare));
    if constexpr (G == 61 && FIRST) asm volatile("s_bitcmp1_b32 s100, 0");
  } else if constexpr (SM.nop[64 + G] > 0) asm volatile("s_nop 0");
  if constexpr (!FIRST && g < 8 && kb == 3) vread<Xp, g>();
  if constexpr (MODE == 2 && (G & 3) == 0) d_store<16 + G / 4>();
  if constexpr (!FIRST) sm_gap<Xp, 64 + G>(c);
  if constexpr (G == 63 && !NOQK) asm volatile("s_cbranch_scc1 ev_entry\nev_ret%c0:" ::"i"(X * 4 + MODE) : "vcc", "memory");
  GAP();
}
template <int R> DI void rescale1(float alpha) {
  float t;
  asm volatile("v_accvgpr_read_b32 %0, a[%c2]\n\tv_mul_f32 %0, %0, %1\n\tv_accvgpr_write_b32 a[%c2], %0" : "=&v"(t) : "v"(alpha), "i"(R));
}
template <int... Rs> DI void rescale_all(const float (&alpha)[2], iseq<Rs...>) { (rescale1<Rs>(alpha[(Rs >> 2) & 1]), ...); }
template <bool MAX> DI float row_reduce(float v) {
  auto p = __builtin_amdgcn_permlane32_swap(__float_as_uint(v), __float_as_uint(v), false, false);
  v = MAX ? fmaxf(__uint_as_float(p[0]), __uint_as_float(p[1])) : __uint_as_float(p[0]) + __uint_as_float(p[1]);
  p = __builtin_amdgcn_permlane16_swap(__float_as_uint(v), __float_as_uint(v), false, false);
  return MAX ? fmaxf(__uint_as_float(p[0]), __uint_as_float(p[1])) : __uint_as_float(p[0]) + __uint_as_float(p[1]);
}
template <int Y, int QB> DI float row_max() {
  constexpr int r = R_S + 32 * Y + 4 * QB;
  float mx;
  asm volatile("v_max_f32 %0, v%c1, v%c2\n\tv_max3_f32 %0, %0, v%c3, v%c4" : "=&v"(mx) : "i"(r), "i"(r + 1), "i"(r + 2), "i"(r + 3));
  static_for([&](auto K) {
    constexpr int q = r + 8 * (K + 1);
    float t = mx;
    asm volatile("v_max3_f32 %0, %0, v%c1, v%c2\n\tv_max3_f32 %0, %0, v%c3, v%c4" : "+v"(t) : "i"(q), "i"(q + 1), "i"(q + 2), "i"(q + 3));
    mx = t;
  }, make_iseq<3>{});
  return row_reduce<true>(mx);
}
template <int R> DI void v_sub(float d) { asm volatile("v_sub_f32 v%c0, v%c0, %1" ::"i"(R), "v"(d)); }
template <int B> DI void s_shift(const float (&d)[2]) { static_for([&](auto E) { v_sub<B + E>(d[(E >> 2) & 1]); }, make_iseq<32>{}); }
template <bool CI> DI void m_shift(const float (&d)[2]) {
  if constexpr (CI) s_shift<R_CI>(d);
  v_sub<R_M>(-d[0]); v_sub<R_M + 1>(-d[1]);
}
#define MSK(R, K) "v_cmp_gt_i32 s[48:49], " #K ", v89\n\tv_cmp_gt_i32 s[50:51], " #K "+1, v89\n\tv_cmp_gt_i32 s[52:53], " #K "+2, v89\n\t" \
                  "v_cmp_gt_i32 s[54:55], " #K "+3, v89\n\tv_cndmask_b32_e64 v[" #R "], v[" #R "], v122, s[48:49]\n\t" \
                  "v_cndmask_b32_e64 v[" #R "+1], v[" #R "+1], v122, s[50:51]\n\tv_cndmask_b32_e64 v[" #R "+2], v[" #R "+2], v122, s[52:53]\n\t" \
                  "v_cndmask_b32_e64 v[" #R "+3], v[" #R "+3], v122, s[54:55]\n\t"
DI void set_mask0() { asm volatile("v_mov_b32 v122, 0xff800000\n\t" MSK(128, 0) MSK(136, 16) MSK(144, 32) MSK(152, 48)); }
template <int X, bool CI = true> DI void sm_redo() {
  constexpr int Xp = X ^ 1;
  float d[2], dt[2], alpha[2], ts[2] = {0.f, 0.f};
  static_for([&](auto Q) {
    constexpr int qb = Q;
    const float pd = vget<R_PD + qb>();
    d[qb] = fmaxf(row_max<Xp, qb>() - pd, 0.f);
    dt[qb] = d[qb] + pd;
    vset<R_PD + qb>(d[qb]);
    alpha[qb] = __builtin_amdgcn_exp2f(-d[qb]);
    vset<R_RS + qb>(vget<R_RS + qb>() * alpha[qb]);
  }, make_iseq<2>{});
  vset<R_THR>(d[0] > 0.f || d[1] > 0.f ? -1.f : 4096.f);
  m_shift<CI>(d);
  static_for([&](auto K) {
    constexpr int k = K, qb = (k >> 1) & 1;
    const float e0 = __builtin_amdgcn_exp2f(vget<R_S + 32 * Xp + 2 * k>() - dt[qb]), e1 = __builtin_amdgcn_exp2f(vget<R_S + 32 * Xp + 2 * k + 1>() - dt[qb]);
    ts[qb] += e0; ts[qb] += e1;
    vset<p_reg(Xp, k)>(__uint_as_float(pk2(e0, e1)));
  }, make_iseq<16>{});
  vset<R_TS>(ts[0]); vset<R_TS + 1>(ts[1]);
  rescale_all(alpha, make_iseq<128>{});
  asm volatile("s_nop 1" ::: "memory");
  GAP();
}
template <int X, int MODE, int... Gs> DI void phaseA_(AC& c, iseq<Gs...>) { (gapA<X, MODE, Gs>(c), ...); }
template <int X, int MODE> DI void phaseA(AC& c) { phaseA_<X, MODE>(c, make_iseq<64>{}); }

template <int X, bool SMX, int G, bool FIRST = false> DI void gapB(AC& c) {
  constexpr int Xp = X ^ 1, blk = G >> 4, g = G & 15;
  if constexpr (g == 0) asm volatile("s_waitcnt lgkmcnt(0)");
  constexpr int qb = (g >> 1) & 1, s = g & 1, r = 192 + 4 * ((blk & 1) * 8 + (g >> 2) * 2 + s), o = 4 * ((blk * 4 + (g >> 2)) * 2 + qb);
  constexpr int p = R_P + 16 * Xp + 4 * (s * 2 + qb);
  if constexpr (FIRST && s == 0)
    asm volatile("v_mfma_f32_16x16x32_bf16 a[%c0:%c1], a[%c2:%c3], v[%c4:%c5], 0" ::"i"(o), "i"(o + 3), "i"(r), "i"(r + 3), "i"(p), "i"(p + 3));
  else
  asm volatile("v_mfma_f32_16x16x32_bf16 a[%c0:%c1], a[%c2:%c3], v[%c4:%c5], a[%c0:%c1]" ::"i"(o), "i"(o + 3), "i"(r), "i"(r + 3), "i"(p), "i"(p + 3));
  if constexpr (g < 8) {
    if constexpr (blk < 3) vread<Xp, (blk + 1) * 8 + g>();
    else if constexpr (SMX) kread<Xp, g>();
  }
  if constexpr (SMX) {
    if constexpr (G == 1 || G == 2) asm volatile("v_add_f32_e32 v%c0, v%c0, v%c1" ::"i"(R_RS + G - 1), "i"(R_TS + G - 1));
    if constexpr (g == 7 && blk < 3) dma_m0<X, blk * 4 + 4>();
    if constexpr (g >= 8 && !(g & 1) && blk < 3) dma_at<blk * 4 + 4 + (g - 8) / 2>();
    if constexpr (G == 55) asm volatile("s_cmp_eq_u32 s56, s76");
    if constexpr (G == 3) asm volatile("s_lshr_b32 s77, s77, 1");
    if constexpr (G == 4) asm volatile("s_bitcmp1_b32 s77, 0");
    if constexpr (G == 5) asm volatile("s_cselect_b64 s[52:53], s[62:63], 0");
    if constexpr (G == 6) asm volatile(FL_ARM("s[52:53]"));
    if constexpr (G == 56) asm volatile(FL_POLL ::: "memory");
    sm_gap<X, G>(c);
  }
  GAP();
}
template <int X, bool FIRST, int... Gs> DI void phaseB_(AC& c, iseq<Gs...>) { (gapB<X, true, Gs, FIRST>(c), ...); }
template <int X, bool FIRST = false> DI void phaseB(AC& c) { phaseB_<X, FIRST>(c, make_iseq<64>{}); }
constexpr int sm_nex() { int n = 0; for (int t = 0; t < 64; ++t) n += SM.ex[t] >= 0; return n; }
template <int X> DI void phaseB0(unsigned mode, bool vh) {
  dma_m0<X, 4>();
  kreads<X ^ 1>(make_iseq<8>{});
  if (vh) { dma_at<4>(); dma_at<5>(); dma_at<6>(); dma_at<7>(); }
  dma_m0<X, 8>();
  asm volatile("s_nop 3");
  float d[2];
  if (mode & 2) {
    d[0] = vget<122>(); d[1] = vget<123>();
    dma_at<8>();
  } else {
    if constexpr (X == 0) if (__builtin_expect(mode & 4, 0)) set_mask0();
    d[0] = row_max<X, 0>(); GAP(); dma_at<8>();
    d[1] = row_max<X, 1>();
#pragma unroll
    for (int qb = 0; qb < 2; ++qb) if (!(d[qb] > -3.0e38f)) d[qb] = 0.f;
  }
  GAP();
  vset<R_M>(d[0]); vset<R_M + 1>(d[1]);
  dma_at<9>();
  const f32x2c nd[2] = {{-d[0], -d[0]}, {-d[1], -d[1]}};
  static_for([&](auto E) {
    constexpr int e = E;
    const f32x2c v = nd[(e >> 1) & 1];
    asm volatile("v_pk_add_f32 v[%c0:%c1], v[%c0:%c1], %4\n\tv_mov_b64 v[%c2:%c3], %4" ::"i"(R_S + 32 * X + 2 * e), "i"(R_S + 32 * X + 2 * e + 1), "i"(R_CI + 2 * e), "i"(R_CI + 2 * e + 1), "v"(v));
    if constexpr (e == 7 || e == 15) dma_at<10 + e / 8>();
  }, make_iseq<16>{});
  static_assert(SM.op0[64] >= 3 * ((sm_nex() - 15) / 2) && SM.op0[64] <= 24);
  static_for([&](auto E) {
    constexpr int e = E;
    if constexpr (e < sm_nex()) asm volatile("v_exp_f32 v%c0, v%c1" ::"i"(R_E + e), "i"(R_S + 32 * X + e));
    if constexpr (e == 2) dma_m0<X, 12>();
    if constexpr (e == 5 || e == 10 || e == 15) dma_at<11 + e / 5>();
  }, make_iseq<16>{});
  static_for([&](auto O) { sm_op<X, O>(); }, make_iseq<SM.op0[64]>{});
  dma_at<15>();
  static_for([&](auto E) { asm volatile("v_exp_f32 v%c0, v%c1" ::"i"(R_E + (int)E), "i"(R_S + 32 * X + 16 + (int)E)); }, make_iseq<(sm_nex() > 16 ? sm_nex() - 16 : 0)>{});
  asm volatile("buffer_store_dwordx2 v[%c0:%c1], v88, s[88:91], s76 offen sc1\n\ts_bitcmp1_b32 s77, 0\n\ts_cselect_b64 s[52:53], s[62:63], 0\n\t"
               FL_ARM("s[52:53]") "\n\t" FL_POLL ::"i"(R_M), "i"(R_M + 1) : "memory", "scc");
  GAP();
}
enum : int { T_Q0, T_K0, T_OQ, T_OG, T_FPREP, T_FQ, T_FG, T_FS, T_ZO, T_CNT, T_NPRE = 10, T_NST = 12, T_TAB = 14, T_SLAB = 16, T_PART, T_IT = 18, T_ITN = 23 };
DI void ev_code(AC& c) {
  GAP();
  asm volatile(
    "s_branch 9f\nev_entry:\n\t"
    "s_bitcmp1_b32 s60, s56\n\ts_cbranch_scc1 3f\n\ts_bitcmp1_b32 s74, s56\n\ts_cbranch_scc1 2f\n\t"
    "s_bitcmp1_b32 s58, s56\n\ts_cselect_b64 vcc, -1, 0\n\tv_cndmask_b32 v121, -2.0, v81, vcc\n\t"
    "buffer_store_dwordx2 v[82:83], v121, s[88:91], s70 offen sc1\n\tbuffer_store_dwordx2 v[82:83], v121, s[88:91], s71 offen sc1\n\t"
    "v_cmp_eq_u64 vcc, s[84:85], v[124:125]\n\ts_andn2_b64 vcc, s[62:63], vcc\n\ts_cbranch_scc1 5f\n"
    "0:\n\ts_add_u32 s98, s98, s59\n\ts_mov_b32 s59, s61\n\ts_mov_b32 s61, s72\n\ts_mov_b64 s[62:63], s[64:65]\n\ts_mov_b64 s[64:65], s[66:67]\n\t"
    "s_mov_b64 s[66:67], s[68:69]\n\ts_cmp_eq_u32 s56, 1\n\ts_cbranch_scc1 ev_first\n\ts_setpc_b64 s[86:87]\n"
    "5:\n\t" FL_CHECK("s[62:63]") "\n\ts_branch 0b\n"
    "6:\n\t" FL_CHECK("s[62:63]") "\n\ts_setpc_b64 s[86:87]\n"
    "2:\nev_mask:\n\tv_mov_b32 v122, 0xff800000\n\t" MSK(228, 0) MSK(236, 16) MSK(244, 32) MSK(252, 48)
    "ev_turn:\n\ts_mov_b32 s98, 0\n\t"
    "v_lshrrev_b32 v90, 7, v86\n\tv_and_b32 v91, 6, v90\n\tv_lshlrev_b32 v91, 4, v91\n\tv_and_b32 v92, 0x70, v86\n\tv_xor_b32 v91, v91, v92\n\t"
    "s_movk_i32 s48, 896\n\tv_mad_u32_u24 v94, v90, s48, v91\n\ts_mov_b64 s[92:93], s[80:81]\n\ts_mov_b32 s94, 229376\n\t"
    "v_add_u32 v90, 28672, v94\n\tv_add_u32 v91, 34816, v94\n\tv_add_u32 v92, 40960, v94\n\tv_add_u32 v93, 47104, v94\n\t"
    "v_add_u32 v95, 6144, v94\n\tv_add_u32 v126, 12288, v94\n\tv_add_u32 v127, 18432, v94\n\t"
    "v_cmp_eq_u64 vcc, s[84:85], v[124:125]\n\ts_andn2_b64 vcc, s[62:63], vcc\n\ts_cbranch_scc1 6b\n\ts_setpc_b64 s[86:87]\n"
    "3:\n\tv_mov_b32 v122, 0xff800000\n\t" MSK(224, 0) MSK(232, 16) MSK(240, 32) MSK(248, 48)
    "ev_post:\n\tv_readlane_b32 s54, v78, %0\n\ts_add_u32 m0, s78, 0x10000\n\t"
    "s_bfe_u32 s53, s73, 0x80010\n\ts_mulk_i32 s53, 3584\n\ts_mov_b64 s[48:49], s[80:81]\n\ts_add_u32 s50, s53, 3584\n\t"
    "s_mov_b32 s51, 0x20000\n\t"
    "v_lshlrev_b32 v121, 3, v84\n\tbuffer_load_dword v121, s[48:51], s53 offen lds\n\t"
    "s_bitcmp0_b32 s73, 3\n\ts_cbranch_scc1 4f\n\t"
    "buffer_load_dwordx4 v84, s[88:91], s54 offen sc1 lds\n\tbuffer_load_dwordx4 v84, s[88:91], s54 offen offset:1024 sc1 lds\n\t"
    "buffer_load_dwordx4 v84, s[88:91], s54 offen offset:2048 sc1 lds\n\tbuffer_load_dwordx4 v84, s[88:91], s54 offen offset:3072 sc1 lds\n"
    "4:\n\ts_cmp_eq_u32 s56, 1\n\ts_cbranch_scc1 ev_first\n\ts_setpc_b64 s[86:87]\n"
    ::"i"((int)T_ZO) : "scc");
  asm volatile("ev_zero:");
  acc_zero();
  asm volatile("s_setpc_b64 s[86:87]\nev_first:\n\ts_cmp_lg_u32 s57, 0\n\ts_cbranch_scc1 ev_zero\n\ts_bitcmp0_b32 s73, 4\n\ts_cbranch_scc1 ev_first1");
  GAP();
  phaseB<0, true>(c);
  asm volatile("s_branch att_loop\nev_first1:");
  phaseB<1, true>(c);
  asm volatile("s_branch att_mid\n9:");
  GAP();
}
#define EV_CALL(L) do { asm volatile("s_getpc_b64 s[86:87]\n\ts_add_u32 s86, s86, 12\n\ts_addc_u32 s87, s87, 0\n\ts_branch " L ::: "vcc", "scc", "memory"); GAP(); } while (0)

#define ACLOB_ALL "a0","a1","a2","a3","a4","a5","a6","a7","a8","a9","a10","a11","a12","a13","a14","a15","a16","a17","a18","a19","a20","a21","a22","a23","a24","a25","a26","a27","a28","a29","a30","a31","a32","a33","a34","a35","a36","a37","a38","a39","a40","a41","a42","a43","a44","a45","a46","a47","a48","a49","a50","a51","a52","a53","a54","a55","a56","a57","a58","a59","a60","a61","a62","a63","a64","a65","a66","a67","a68","a69","a70","a71","a72","a73","a74","a75","a76","a77","a78","a79","a80","a81","a82","a83","a84","a85","a86","a87","a88","a89","a90","a91","a92","a93","a94","a95","a96","a97","a98","a99","a100","a101","a102","a103","a104","a105","a106","a107","a108","a109","a110","a111","a112","a113","a114","a115","a116","a117","a118","a119","a120","a121","a122","a123","a124","a125","a126","a127","a128","a129","a130","a131","a132","a133","a134","a135","a136","a137","a138","a139","a140","a141","a142","a143","a144","a145","a146","a147","a148","a149","a150","a151","a152","a153","a154","a155","a156","a157","a158","a159","a160","a161","a162","a163","a164","a165","a166","a167","a168","a169","a170","a171","a172","a173","a174","a175","a176","a177","a178","a179","a180","a181","a182","a183","a184","a185","a186","a187","a188","a189","a190","a191","a192","a193","a194","a195","a196","a197","a198","a199","a200","a201","a202","a203","a204","a205","a206","a207","a208","a209","a210","a211","a212","a213","a214","a215","a216","a217","a218","a219","a220","a221","a222","a223","a224","a225","a226","a227","a228","a229","a230","a231","a232","a233","a234","a235","a236","a237","a238","a239","a240","a241","a242","a243","a244","a245","a246","a247","a248","a249","a250","a251","a252","a253","a254","a255"

struct EP { float fa[2], fb[2]; };
template <int I, int E, int MODE> DI void epi_pair(const EP& p) {
  constexpr int qb = I & 1, r = 16 * (I >> 1) + 4 * qb + (E >> 1) * 8 + (E & 1) * 2, t = 96 + 2 * E, w = 128 + 4 * I + E;
  if constexpr (MODE == 2) asm volatile("v_accvgpr_read_b32 v106, a[%c0]" ::"i"(w));
  asm volatile("v_accvgpr_read_b32 v%c0, a[%c2]\n\tv_accvgpr_read_b32 v%c1, a[%c3]" ::"i"(t), "i"(t + 1), "i"(r), "i"(r + 1));
  if constexpr (MODE) asm volatile("v_mul_f32 v%c0, v%c0, %2\n\tv_mul_f32 v%c1, v%c1, %2" ::"i"(t), "i"(t + 1), "v"(p.fa[qb]));
  if constexpr (MODE == 2)
    asm volatile("v_lshlrev_b32 v104, 16, v106\n\tv_and_b32 v105, 0xffff0000, v106\n\tv_fmac_f32 v%c0, v104, %2\n\tv_fmac_f32 v%c1, v105, %2"
                 ::"i"(t), "i"(t + 1), "v"(p.fb[qb]));
  asm volatile("v_cvt_pk_bf16_f32 v%c0, v%c1, v%c2" ::"i"(w), "i"(t), "i"(t + 1));
}
template <int I> DI void epi_store() {
  asm volatile("buffer_store_dwordx4 v[%c0:%c1], v84, s[88:91], s101 offen offset:%2 sc1" ::"i"(128 + 4 * I), "i"(131 + 4 * I), "i"((I & 3) * 1024) : "memory");
  if constexpr ((I & 3) == 3) asm volatile("s_addk_i32 s101, 4096");
}
constexpr int R_TB = 232, R_VS = 236;
template <int HF, int R> DI void p_read() {
  constexpr int mb = R >> 4, r = 192 - 64 * HF + 2 * R;
  asm volatile("ds_read_b64_tr_b16 a[%c1:%c2], v%c0 offset:%c3" ::"i"(R_TB + 2 * HF + mb), "i"(r), "i"(r + 1), "i"((R & 15) * 2048));
}
template <int QB, int E> DI void p_cvt() {
  asm volatile("v_cvt_pk_bf16_f32 v%c0, v%c1, v%c2" ::"i"(208 + 4 * QB + E), "i"(192 + 8 * QB + 2 * E), "i"(193 + 8 * QB + 2 * E));
}
template <int QB, int PR> DI void p_store() {
  asm volatile("buffer_store_dwordx4 v[%c0:%c1], v%c2, s[88:91], s96 offen offset:%3 sc1" ::"i"(208 + 4 * QB), "i"(211 + 4 * QB), "i"(R_VS + QB),
               "i"(PR * 128) : "memory");
}
constexpr int p_dma(int HF, int G) { return G == 3 && !HF ? 0 : G == 11 ? 1 - HF : G == 15 ? 2 - HF : G == 19 ? 3 - HF : G == 27 && HF ? 3 : -1; }
template <int HF, bool LD, int G> DI void p_gap() {
  constexpr int ch = G >> 3, t = G & 7, a = 192 - 64 * HF + 32 * (ch & 1) + 4 * t, b = 128 + 4 * (2 * t + (ch >> 1)), d = 192 + 4 * ch;
  if constexpr (t == 0)
    asm volatile("v_mfma_f32_16x16x32_bf16 v[%c0:%c1], a[%c2:%c3], v[%c4:%c5], 0" ::"i"(d), "i"(d + 3), "i"(a), "i"(a + 3), "i"(b), "i"(b + 3));
  else
    asm volatile("v_mfma_f32_16x16x32_bf16 v[%c0:%c1], a[%c2:%c3], v[%c4:%c5], v[%c0:%c1]" ::"i"(d), "i"(d + 3), "i"(a), "i"(a + 3), "i"(b), "i"(b + 3));
  p_read<HF ^ 1, G>();
  if constexpr (G == 0) asm volatile("v_xor_b32 v%c0, s100, v%c0" ::"i"(R_TB + 2 * HF + 1));
  if constexpr (G == 17) asm volatile("v_xor_b32 v%c0, s100, v%c0" ::"i"(R_TB + 2 * (HF ^ 1)));
  if constexpr (G >= 4 && G <= 8 && G != 7) p_cvt<1, (G < 7 ? G - 4 : 3)>();
  if constexpr (G == 24 || (G == 7 && HF == 0)) asm volatile("s_waitcnt lgkmcnt(8)");
  if constexpr (G == 7 && HF == 1) asm volatile("s_waitcnt vmcnt(%0) lgkmcnt(8)\n\ts_barrier" ::"i"(LD ? 13 : 5) : "memory");
  if constexpr (G == 10) p_store<1, HF ^ 1>();
  if constexpr (G >= 20 && G < 24) p_cvt<0, G - 20>();
  if constexpr (G == 25) p_store<0, HF>();
  if constexpr (LD && p_dma(HF, G) >= 0) dma_at<(HF ? 8 : 12) + p_dma(HF, G)>();
  if constexpr (LD && HF == 0 && G == 2) asm volatile("s_bitset0_b32 m0, 12");
  if constexpr (HF == 0 && G == 13) asm volatile("s_add_u32 s96, s96, 0x8000");
  if constexpr (HF == 0 && G == 26) asm volatile("s_add_u32 s98, s98, 128");
  if constexpr (LD && HF == 0 && G == 27) asm volatile("s_cmp_lt_u32 s98, 0x380");
  if constexpr (HF == 1 && G == 1) asm volatile("s_xor_b32 s100, s100, 0x10000");
  if constexpr (LD && HF == 1 && G == 2) asm volatile("s_xor_b32 s97, s97, s100");
  if constexpr (LD && HF == 1 && G == 9) asm volatile("s_or_b32 m0, s97, 0x1000");
  if constexpr (!LD && HF == 1 && G == 30) asm volatile("s_cmp_lt_u32 s98, 0x500");
  GAP();
}
template <int HF, bool LD, int... Gs> DI void p_half(iseq<Gs...>) { (p_gap<HF, LD, Gs>(), ...); }
template <int G> DI void gapF(AC& c, const EP& p) {
  if constexpr (G < 64) gapB<0, false, G>(c);
  if constexpr (G >= 10) epi_pair<(G - 10) / 4, (G - 10) & 3, 0>(p);
  if constexpr (G >= 14 && ((G - 14) & 3) == 0) epi_store<(G - 14) / 4>();
  if constexpr (G == 73) epi_store<15>();
  GAP();
}
template <int... Gs> DI void phaseF(AC& c, const EP& p, iseq<Gs...>) { (gapF<Gs>(c, p), ...); }
template <int G> DI void gapAL(AC& c) {
  constexpr int kb = G >> 3, s = G & 7, r = 192 + 4 * (G & 15), q = 160 + 4 * s, d = R_S + 36 + 8 * kb, ci = s == 0 ? R_CI + 4 + 8 * kb : d;
  if constexpr (G == 0) asm volatile("s_add_u32 s96, s97, 0x1000000\n\ts_mov_b32 m0, s79\n\ts_waitcnt vmcnt(9) lgkmcnt(0)\n\ts_barrier" ::: "memory");
  if constexpr (G >= 8 && (G & 3) == 0) asm volatile("s_waitcnt lgkmcnt(4)");
  if constexpr (G == 29) asm volatile("s_barrier" ::: "memory");
  asm volatile("v_mfma_f32_16x16x32_bf16 v[%c0:%c1], a[%c2:%c3], a[%c4:%c5], v[%c6:%c7]" ::"i"(d), "i"(d + 3), "i"(r), "i"(r + 3), "i"(q), "i"(q + 3),
               "i"(ci), "i"(ci + 3));
  if constexpr (G < 24) kread<1, G + 8>();
  else vread<0, G - 24>();
  if constexpr (G % 3 == 1 && G < 24) dma_at<G / 3>();
  if constexpr (G == 11) asm volatile("s_bitset1_b32 m0, 12");
  if constexpr (G == 28) asm volatile("buffer_load_dwordx4 v[122:125], v88, s[88:91], s76 offen sc1" ::: "memory");
  if constexpr (G == 30) asm volatile("s_xor_b32 m0, s79, 0x11000\n\ts_movk_i32 s98, 128");
  sm_gap<0, 64 + 2 * G>(c); sm_gap<0, 65 + 2 * G>(c);
  GAP();
}
template <int... Gs> DI void phaseAL(AC& c, iseq<Gs...>) { (gapAL<Gs>(c), ...); }
struct BLS { int ex[65], op[65], vm[65], su[65]; };
constexpr BLS bl_sched(bool LD) {
  BLS z{};
  z.op[0] = 1;
  for (int t = 0; t < 64; ++t) {
    const int g = t & 15, o = z.op[t];
    const bool x = t == 6 || (t < 48 && g > 8 && g != 12 && g != 15);
    const bool v = LD ? (t & 3) == 0 : (t & 11) == 8;
    const bool p = t >= 15 && o < 24 && (t & 3) != 0 && t != 47 && !x && z.ex[t - 1] > 2 * (o / 3) + (o % 3 != 0);
    z.ex[t + 1] = z.ex[t] + x; z.vm[t + 1] = z.vm[t] + v; z.op[t + 1] = o + p;
    z.su[t + 1] = z.su[t] + (t >= 1 && z.su[t] < (LD ? 16 : 10) && g != 15 && !v && !x && (LD || t > 55 || (t > 3 && !p)));
  }
  return z;
}
template <bool LD> constexpr BLS BL = bl_sched(LD);
static_assert(BL<true>.ex[64] == 16 && BL<true>.op[61] == 24 && BL<true>.vm[64] == 16 && BL<false>.vm[64] == 8);
static_assert(BL<true>.su[56] == 16 && BL<false>.su[62] == 10);
template <bool LD, int G> DI void gapBL(AC& c) {
  if constexpr (G == 48) asm volatile("s_waitcnt vmcnt(%0) lgkmcnt(0)\n\ts_barrier" ::"i"(BL<LD>.vm[48]) : "memory");
  gapB<1, false, G>(c);
  if constexpr (G >= 48 && G < 56) vread<1, G - 48>();
  if constexpr (BL<LD>.vm[G + 1] > BL<LD>.vm[G]) {
    constexpr int k = BL<LD>.vm[G] + (LD ? 0 : 16), I = 2 * (k & 7) + ((k >> 3) & 1);
    if constexpr (k < 16) {
      asm volatile("buffer_load_dwordx4 a[%c0:%c1], v84, s[88:91], s75 offen offset:%2 sc1" ::"i"(128 + 4 * I), "i"(131 + 4 * I), "i"((I & 3) * 1024) : "memory");
      if constexpr (k & 1) asm volatile("s_addk_i32 s75, %0" ::"i"(k == 7 ? -12288 : 4096));
    } else dma_at<k - 8>();
    if constexpr (k == 19) asm volatile("s_bitset0_b32 m0, 12");
  }
  if constexpr (G == 1 || G == 2) asm volatile("v_add_f32_e32 v%c0, v%c0, v%c1" ::"i"(R_RS + G - 1), "i"(R_TS + G - 1));
  if constexpr (G == 3) asm volatile("v_mov_b32 v%c0, 0" ::"i"(R_TS));
  if constexpr (BL<LD>.ex[G + 1] > BL<LD>.ex[G]) {
    constexpr int j = BL<LD>.ex[G];
    asm volatile("v_exp_f32 v%c0, v%c1" ::"i"(R_E + j), "i"(R_S + 36 + 8 * (j >> 2) + (j & 3)));
  }
  if constexpr (BL<LD>.op[G + 1] > BL<LD>.op[G]) {
    constexpr int o = BL<LD>.op[G], k = o / 3, j = 2 * k + o % 3;
    if constexpr (o % 3 == 2) asm volatile("v_cvt_pk_bf16_f32 v%c0, v%c1, v%c2" ::"i"(p_reg(1, (k >> 1) * 4 + 2 + (k & 1))), "i"(R_E + 2 * k), "i"(R_E + 2 * k + 1));
    else asm volatile("v_add_f32_e32 v%c0, v%c1, v%c2" ::"i"(R_TS + 1), "i"(j == 1 ? R_E : R_TS + 1), "i"(R_E + j));
  }
  if constexpr (G == 61) asm volatile("v_cmp_nle_f32_e64 s[54:55], v%c0, v%c1" ::"i"(R_TS + 1), "i"(R_THR));
  if constexpr (BL<LD>.su[G + 1] > BL<LD>.su[G]) {
    constexpr int k = BL<LD>.su[G];
    if constexpr (k < 4) asm volatile("v_xor_b32 v%c0, %1, %2" ::"i"(R_TB + k), "i"(32 * k), "v"(c.tb));
    else if constexpr (k < 6) asm volatile("v_add_u32 v%c0, %1, %2" ::"i"(R_VS + k - 4), "i"(4096 * (k - 4)), "v"(c.vs));
    else if constexpr (k == 6) tload<96, T_SLAB>();
    else if constexpr (k == 7) tload<101, T_CNT>();
    else if constexpr (k == 8) asm volatile("v_mov_b64 v[240:241], 1");
    else if constexpr (k == 9) asm volatile("s_mov_b32 s97, s79\n\ts_mov_b32 s100, 0x8000");
    else if constexpr (k < 14) asm volatile("v_mov_b32 v%c0, %1" ::"i"(k < 12 ? 214 + k : 218 + k), "v"(k & 1 ? c.o1 : c.o0));
    else asm volatile("v_mov_b64 v[%c0:%c1], 0" ::"i"(198 + 2 * k), "i"(199 + 2 * k));
  }
  GAP();
}
template <bool LD, int... Gs> DI void phaseBL(AC& c, iseq<Gs...>) { (gapBL<LD, Gs>(c), ...); }
template <int MODE, int G> DI void gapFL(const EP& p) {
  constexpr int x = G - (MODE == 3 ? 4 : 0), J = MODE == 1 ? (G < 24 && G % 3 == 1 ? G / 3 : -1) : G < 27 && G % 5 != 0 && G % 5 != 3 ? G / 5 * 3 + (G % 5) / 2 : -1;
  if constexpr (G == 0 && MODE == 1) asm volatile("s_or_b32 m0, s78, 0x1000\n\ts_movk_i32 s98, 256\n\ts_waitcnt lgkmcnt(0)");
  if constexpr (G == 0 && MODE > 1) asm volatile("s_waitcnt vmcnt(8) lgkmcnt(0)" ::: "memory");
  if constexpr (G == 16 && MODE > 1) asm volatile("s_waitcnt vmcnt(9)" ::: "memory");
  if constexpr (G >= 8 && G < 32 && (G & 3) == 0) asm volatile("s_waitcnt lgkmcnt(%0)" ::"i"(G < 28 ? 4 : 0));
  if constexpr (G < 32) {
    constexpr int o = 8 * (G >> 1) + 4, r = 192 + 4 * (G & 15), q = R_P + 20 + 8 * (G & 1);
    asm volatile("v_mfma_f32_16x16x32_bf16 a[%c0:%c1], a[%c2:%c3], v[%c4:%c5], a[%c0:%c1]" ::"i"(o), "i"(o + 3), "i"(r), "i"(r + 3), "i"(q), "i"(q + 3));
  }
  if constexpr (G < 24) vread<1, G + 8>();
  if constexpr (G == 28) asm volatile("s_waitcnt vmcnt(8)\n\ts_barrier\n\ts_or_b32 m0, s79, 0x1000\n\ts_movk_i32 s98, 384" ::: "memory");
  if constexpr (G == 29 || G == 31) dma_at<8 + (G - 29) / 2>();
  if constexpr (G == 32 || G == 40) {
    static_for([&](auto I) { p_read<0, 2 * (G - 32) + I>(); }, make_iseq<16>{});
    dma_at<10 + (G - 32) / 8>();
  }
  if constexpr (J >= 0) dma_at<8 + (J & 7)>();
  if constexpr (x >= 0) epi_pair<2 * ((x >> 2) & 7) + (x >> 5), x & 3, MODE>(p);
  if constexpr (MODE == 3 && G < 32) {
    constexpr int db = G & 15, qb = G >> 4, o = 4 * (db * 2 + qb), a = 224 + 4 * (db & 1), b = 128 + 4 * ((db >> 1) * 2 + qb);
    asm volatile("v_mfma_f32_16x16x32_bf16 a[%c0:%c1], v[%c2:%c3], a[%c4:%c5], a[%c0:%c1]" ::"i"(o), "i"(o + 3), "i"(a), "i"(a + 3), "i"(b), "i"(b + 3));
  }
  if constexpr (J == 3 || J == 11) asm volatile("s_bitset0_b32 m0, 12");
  if constexpr (J == 7 && MODE > 1) asm volatile("s_or_b32 m0, s78, 0x1000\n\ts_movk_i32 s98, 256");
  if constexpr (x == 63) asm volatile("s_waitcnt vmcnt(4) lgkmcnt(0)" ::: "memory");
  GAP();
}
template <int MODE, int... Gs> DI void phaseFL_(const EP& p, iseq<Gs...>) { (gapFL<MODE, Gs>(p), ...); }
template <int MODE> DI void phaseFL(const EP& p) { GAP(); phaseFL_<MODE>(p, make_iseq<MODE == 3 ? 68 : 64>{}); }
template <int U, int K> DI void r_load(unsigned vo, unsigned so) {
  asm volatile("buffer_load_dwordx4 a[%c0:%c1], %2, s[88:91], %3 offen offset:%4 sc1" ::"i"(64 * U + 4 * K), "i"(64 * U + 4 * K + 3), "v"(vo),
               "s"(so + (K >> 1) * 0x40000 + (U >> 1) * 4096), "i"((K & 1) * 512 + (U & 1) * 2048) : "memory");
}
template <int U, int K> DI void r_sum() {
  constexpr int d = 128 + 8 * U + 4 * (K & 1), b = 64 * U + 4 * K;
  if constexpr (K < 2) asm volatile("v_mfma_f32_16x16x32_bf16 v[%c0:%c1], v[96:99], a[%c2:%c3], 0" ::"i"(d), "i"(d + 3), "i"(b), "i"(b + 3));
  else asm volatile("v_mfma_f32_16x16x32_bf16 v[%c0:%c1], v[96:99], a[%c2:%c3], v[%c0:%c1]" ::"i"(d), "i"(d + 3), "i"(b), "i"(b + 3));
}
template <int U> DI void r_sums() { static_for([&](auto K) { r_sum<U, K>(); }, make_iseq<16>{}); }
template <int U> DI void r_fin(unsigned vo, __amdgpu_buffer_rsrc_t srd, unsigned so) {
  static_for([&](auto E) { asm volatile("v_cvt_pk_bf16_f32 v%c0, v%c1, v%c2" ::"i"(128 + 8 * U + E), "i"(128 + 8 * U + 2 * E), "i"(129 + 8 * U + 2 * E)); }, make_iseq<4>{});
  asm volatile("s_nop 0\n\tbuffer_store_dwordx4 v[%c3:%c4], %0, %1, %2 offen sc0 sc1" ::"v"(vo), "s"(srd), "s"(so), "i"(128 + 8 * U), "i"(131 + 8 * U) : "memory");
}

#define CMAX 12
#define NSPL 5
#define NYT 15
#define NYM 0
struct TRow { unsigned v[64]; };
constexpr u64 kvmask(int m0, int m1) { u64 n = 0; for (int m = m0; m <= m1; ++m) n |= 0x101ull << m; return n; }
constexpr u64 k_order(bool isY, int rr) {
  const int cls = (rr + (rr >> 2)) & 3;
#define NCM 3
  u64 t = 0;
  int n = 0;
  auto put = [&](int k) { t |= (u64)k << (4 * n++); };
  if (isY) {
    for (int k = 1; k <= 4; ++k) put(NCM + 4 + (k & 3));
    for (int k = 1; k <= 4; ++k) put(NCM + 8 + (k & 3));
    for (int k = NCM + 12; k < 16; ++k) put(k);
    for (int k = NCM + 3; k >= 0; --k) put(k);
    return t;
  }
  for (int k = 0; k < NCM; ++k) put(k);
  for (int k = 0; k < 3; ++k) { put(NCM + 4 * k + cls); put(NCM + ((cls + k + 1) & 3)); }
  for (int k = 1; k < 4; ++k) put(NCM + 4 + ((cls + k) & 3));
  for (int k = 1; k < 4; ++k) put(NCM + 8 + ((cls + k) & 3));
  for (int k = NCM + 12; k < 16; ++k) put(NCM == 2 ? k ^ (cls & 1) : k);
  return t;
}
static_assert(NCM >= 3);
constexpr TRow t_row(int b, int wid) {
  TRow t{};
  const int j = b & 7, r = b >> 3, rr = r & 15, hsel = rr & 1, pj = rr >> 1, kv = pj & 1, h = j + 8 * hsel, u = hsel * 8 + j;
  const bool isY = r >= 16, noA = pj == 7, solo = pj >= NSPL;
  const int qbF = isY != solo ? 15 - pj : pj;
  const int mt = (rr >> 2) + (isY ? 4 : 0), g = j * 128, NY = NYT - ((NYM >> pj) & 1) + ((NYM >> (8 + pj)) & 1);
  const int nx0 = solo ? 0 : 32 - 2 * pj - NY, cx = nx0 < CMAX ? nx0 : CMAX, ex = nx0 - cx, qb0 = solo ? qbF : 15 - pj;
  const unsigned oQ = (unsigned)h << 20, oK = oQ + O_K, oP = O_P + (h * 8 + pj) * PSTR;
  if (wid == 4) {
    for (int l = 0; l < 64; ++l)
      t.v[l] = fo(l < 16 ? g + hsel * 16 + l : l == 16 ? g + 32 + hsel * 8 + (qb0 >> 1) : l == 17 ? g + 32 + hsel * 8 + (qbF >> 1) :
                  l == 26 ? g + 48 + hsel * 8 + pj : l < 32 ? g + 64 + hsel * 8 + pj :
                  l < 48 ? 2304 + pj * 16 + l - 32 : 2048 + pj * 16 + l - 48);
    return t;
  }
  auto put64 = [&](int i, u64 x) { t.v[i] = (unsigned)x; t.v[i + 1] = (unsigned)(x >> 32); };
  t.v[T_Q0] = oQ + (qb0 * 128 + wid * 16) * 512;
  t.v[T_K0] = oK + (isY ? cx : 0) * 32768;
  t.v[T_OQ] = oQ + pj * 131072 + (wid >> 1) * 65536 + (wid & 1) * 4096;
  t.v[T_OG] = oK + kv * TSB + mt * 131072 + (wid >> 1) * 65536 + (wid & 1) * (kv ? 8192 : 4096);
  t.v[T_FPREP] = fo(2048 + pj * 16 + u);
  t.v[T_FQ] = fo(g + 32 + hsel * 8 + pj);
  t.v[T_FG] = fo(g + hsel * 16 + kv * 8 + mt);
  t.v[T_FS] = fo(1024 + qbF * 16 + j * 2 + hsel);
  t.v[T_CNT] = fo(2560 + qbF * 16);
  t.v[T_ZO] = O_Z + pj * 64 * SEQ * 2 + (u * 4 + wid) * 4096;
  const u64 own = 0x1ull << (kv * 8 + mt), conv = noA ? 0 : 0xffffull << 32;
  put64(T_NPRE, 0x10000ull | (isY ? (1ull << (cx >> 2)) | (1ull << ((cx + 1) >> 2)) : noA ? 0 : 0xffffull << 48));
  put64(T_NST, isY ? (solo ? kvmask(0, (2 * qbF + 1) >> 2) : kvmask(cx >> 2, 3) | 1ull << 27) : kvmask(0, 0));
  put64(T_TAB, k_order(isY, rr));
  t.v[T_SLAB] = O_S + qbF * 0x200000 + j * 0x40000 + wid * 8192 + hsel * 1024;
  t.v[T_PART] = oP;
  const unsigned junk = fo(g + 96 + r), fconv = noA ? junk : fo(2304 + pj * 16 + u);
  for (int it = isY || solo; it < 2; ++it) {
    unsigned* p = t.v + T_IT + it * T_ITN;
    const int n = !it ? nx0 : isY && !solo ? NY : 2 * qbF + 2;
    u64 q[4] = {0, 0, 0, 0};
    unsigned jmp[3] = {0, 0, 0}, chk = 0;
    p[14] = junk;
    if (isY) {
      p[0] = 2; p[1] = 2; p[13] = t.v[T_FG];
      if (!solo) {
        chk = 1u << (14 - cx); p[0] |= chk; q[1] = kvmask(4, (31 - 2 * pj) >> 2) & ~own; jmp[1] = ex * 32768;
        chk |= 1u << (n - 2); q[2] = 1ull << 26;
      }
    } else if (it == 0) {
      chk = p[0] = 2u | (ex ? 1u << (cx - 2) : 0) | 1u << (n - 2);
      chk |= noA || qbF > 1 ? 0 : 1u << n;
      q[0] = kvmask(0, (cx - 1) >> 2);
      if (ex) { q[1] = kvmask(4, 4 + ((ex - 1) >> 2)); jmp[1] = (16 - cx) * 32768; }
      jmp[ex ? 2 : 1] = -(ex ? 16 + ex : cx) * 32768;
      q[ex ? 2 : 1] = 0x20000ull | kvmask(0, (2 * qbF + 1) >> 2);
      q[ex ? 3 : 2] = conv;
      p[1] = 2; p[13] = fconv; p[14] = fo(g + 64 + hsel * 8 + pj);
    } else if (solo) {
      chk = p[0] = 0x402; p[1] = 2; p[13] = fconv;
      q[0] = kvmask(0, 2); q[1] = kvmask(3, (2 * qbF + 1) >> 2) | conv;
    } else {
      p[0] = p[1] = n > 4 ? 2 : 1; p[13] = fo(g + 48 + hsel * 8 + pj);
      chk = noA ? 0 : p[0];
      q[0] = conv;
    }
    p[2] = jmp[0]; p[4] = jmp[1]; p[15] = jmp[2];
    p[3] = it && n > 2 ? 1u << (n - 3) : 0;
    for (int k = 0; k < 4; ++k) { p[5 + 2 * k] = (unsigned)q[k]; p[6 + 2 * k] = (unsigned)(q[k] >> 32); }
    p[16] = 32 | (isY && !solo ? 2 : 0) | (n == 2 ? 4 : 0) | (isY || noA ? 0 : 8) | (n & 1) << 4 | (n - 1 - it) << 8 | ((r >> 1) * 4 + wid) << 16 | h << 24 | qbF << 28;
    p[17] = it && n > 2 ? 1u << (n - 2) : 0;
    p[18] = it ? oP + wid * 16384 : oQ + (qbF * 128 + wid * 16) * 512;
    p[19] = oP + (it ? 1032 : 1024);
    p[20] = chk >> 1;
    p[21] = oK + (isY ? cx : 0) * 32768;
    p[22] = (p[0] | p[3] | p[17]) << 1;
  }
  return t;
}
struct TBlk { TRow row[5]; };
template <int B> constexpr TBlk t_blk = {{t_row(B, 0), t_row(B, 1), t_row(B, 2), t_row(B, 3), t_row(B, 4)}};
struct Tab { TBlk blk[256]; };
template <int... I> constexpr Tab t_make(iseq<I...>) { return Tab{{t_blk<I>...}}; }
__device__ const Tab g_tab = t_make(make_iseq<256>{});

template <int R> DI const bf16* sptr() { const bf16* p; asm volatile("s_mov_b64 %0, s[%c1:%c2]" : "=s"(p) : "i"(R), "i"(R + 1)); return p; }
template <class T, int I> DI T* arg_late() {
  unsigned lo, hi;
  asm volatile("v_readlane_b32 %0, v77, %2\n\tv_readlane_b32 %1, v77, %3\n\ts_nop 4" : "=s"(lo), "=s"(hi) : "i"(2 * I), "i"(2 * I + 1));
  return (T*)(((u64)hi << 32) | lo);
}
__global__ __launch_bounds__(256, 1) __attribute__((amdgpu_num_vgpr(77), amdgpu_num_sgpr(56))) void fused_kernel(
    const bf16* ckv_, const bf16* wuk_, const bf16* wuv_, const bf16* x_, const bf16* wq_, bf16* ws_, const bf16* ak_, const bf16* av_,
    const bf16* ash_, const bf16* assmk_, const bf16* assmD_, const bf16* wo_, const bf16* aq_, bf16* out_) {
  extern __shared__ __attribute__((aligned(16))) char lds[];
  const void* kap = (const void*)__builtin_amdgcn_kernarg_segment_ptr();
  asm volatile("at_code_begin:\n\ts_load_dwordx16 s[80:95], %0, 0\n\ts_load_dwordx2 s[96:97], %0, 0x40" ::"s"(kap) : ACLOB_ALL, "v255", "s101");
  asm volatile("s_getpc_b64 s[48:49]\nct_base:\n\ts_mov_b32 s50, pf_code_end-ct_base\n\ts_mov_b32 s51, 0x20000\n\ts_lshr_b32 s52, %1, 3\n\t"
               "v_and_b32 v116, 63, %0\n\tv_lshrrev_b32 v117, 6, %0\n\tv_lshl_add_u32 v116, v116, 7, v117\n\ts_lshl_b32 s52, s52, 9\n\t"
               "v_lshlrev_b32 v116, 7, v116\n\tbuffer_load_dword v117, v116, s[48:51], s52 offen" ::"v"(threadIdx.x), "s"(blockIdx.x) : "scc", "memory");
  GAP();
  const int tid = threadIdx.x, lane = tid & 63;
  const int wid = __builtin_amdgcn_readfirstlane(tid >> 6);
  const int b = blockIdx.x, j = b & 7, r = b >> 3, rr = r & 15, hsel = rr & 1, pj = rr >> 1, kv = pj & 1;
  const bool isY = r >= 16, doP = isY && pj != 7, doA = !isY && pj != 7;
  const int h = j + 8 * hsel, mt = (rr >> 2) + (isY ? 4 : 0), pu = (hsel * 8 + j) * 4 + wid;
  constexpr unsigned m0d = 131072 + 24576;
  const unsigned lA = isY ? 65536 : 0, lW = lA ^ 65536, po = isY ? 0 : 32768;
  unsigned mA = lA + wid * 1024 + po, mW = lW + wid * 1024 + po, ofA = isY ? pj * 256 * DA * 2 : mt * 256 * DC * 2, ofW = h * 512;
  const unsigned lda = isY ? DA : DC;
#define ROT 1
  unsigned g0 = (!ROT ? 0 : isY ? pj + 2 * hsel : rr + (rr >> 2)) & 3;
  {
    asm volatile("global_load_dword v78, %0, %2\n\tglobal_load_dword v79, %1, %2 offset:1024\n\tglobal_load_dword v77, %3, %4"
                 ::"v"(tid * 4), "v"(lane * 4), "s"(&g_tab.blk[b]), "v"((lane < 27 ? lane : 27) * 4), "s"(kap) : "memory");
    const unsigned ra = wid * 8 + (lane >> 3), sa = ((lane & 7) ^ (((wid & 1) * 4 + (lane >> 4)) & 7)) * 16;
    const unsigned vbA = ra * (lda * 2) + sa, vbB = (4 * wid + (lane >> 4)) * LDW * 2 + 16 * ((lane & 15) ^ (((lane >> 4) << 2) | wid));
    vsetu<92>(vbA); vsetu<93>(vbB); vsetu<94>(ra * (DC * 2) + sa);
    sset<65>(128 * lda);
    asm volatile("s_mov_b32 s74, 0x40000\n\ts_mov_b32 s53, 0x80000");
    sset<58>(256 * lda * 2); sset<62>((lda - 1) * LDW * 2 + 512);
    asm volatile("s_mov_b32 s59, 0x20000\n\ts_mov_b32 s63, 0x20000");
    g_pro<1>(g0, mA, mW, lda);
    asm volatile("" : "+s"(ofA), "+s"(ofW), "+s"(mA), "+s"(mW), "+s"(g0));
    GAP();
  }
  asm volatile("s_bitcmp1_b32 %0, 4\n\ts_waitcnt lgkmcnt(0)\n\ts_cselect_b64 s[98:99], s[84:85], s[82:83]\n\ts_bitcmp1_b32 %0, 7\n\t"
               "s_cselect_b64 s[60:61], s[88:89], s[98:99]\n\ts_cselect_b64 s[56:57], s[86:87], s[80:81]\n\t"
               "s_add_u32 s56, s56, %1\n\ts_addc_u32 s57, s57, 0\n\ts_add_u32 s60, s60, %2\n\ts_addc_u32 s61, s61, 0\n\t"
               "s_mov_b64 s[88:89], s[90:91]\n\ts_mov_b32 s90, %3\n\ts_mov_b32 s91, 0x20000" ::"s"(b), "s"(ofA), "s"(ofW), "i"(WSB) : "scc");
  KST(0, 0);
  AC c;
  {
    GM g;
    PR pr;
    const int ptb = hsel * 8 + j;
    if (doP) prep_load(pr, tid, pj, ptb, sptr<92>(), sptr<94>(), sptr<96>());
    g_pro<2>(g0, mA, mW, lda);
    g_pro(g0 + 1, mA, mW, lda);
    sset<64>(64 * lda); sset<67>(256 * lda); sset<68>(320 * lda);
    asm volatile("s_mov_b32 s71, 0x20000\n\ts_movk_i32 s72, 0x100\n\ts_mov_b32 s73, 0x20100");
    asm volatile("buffer_load_dwordx2 v[120:121], off, s[88:91], %0 sc1" ::"s"(fo(6144)) : "memory");
    GAP();
    {
      const int fr = lane & 15, fq = lane >> 4, q = (lane & 15) >> 2, p = lane & 3;
      vsetu<128>(lA + (wid >> 1) * 16384 + (fr >> 3) * 1024 + (fr & 7) * 128 + ((fq ^ (fr >> 1)) * 16));
      vsetu<129>(lW + (wid & 1) * 8192 + off_b(8 * fq + q, p >> 1) + 8 * (p & 1));
      vsetu<130>(lW + (wid & 1) * 8192 + off_b(8 * fq + 4 + q, p >> 1) + 8 * (p & 1));
      const unsigned one = fq != q ? 0u : (p & 1) ? 0x3f800000u : 0x3f80u;
      vsetu<131>((p & 2) ? 0u : one); vsetu<132>((p & 2) ? one : 0u);
      vsetu<133>((lane >> 2) * 128 + 16 * ((p >> 1) ^ ((lane >> 2) & 6)) + 8 * (p & 1));
      vsetu<134>((fr >> 3) * 2048 + (fq & 1) * 512 + (fq >> 1) * 256 + (fr & 7) * 16);
      vsetu<81>(tid == 0 ? 0u : 0x80000000u);
      vsetu<84>(lane * 16); vsetu<85>(lane * 16 + 65536); vsetu<86>(wid * 8192 + lane * 16); vsetu<87>(wid * 8192 + lane * 16 + 4096);
      vsetu<88>(65536 + wid * 256 + fr * 16); vsetu<89>(16 * wid + fr - 4 * fq);
      asm volatile("s_mov_b64 s[92:93], s[88:89]\n\ts_mov_b64 s[94:95], s[90:91]\n\ts_mov_b32 s75, 0x60000\n\t"
                   "s_mov_b32 s76, 0x40100\n\ts_mov_b32 s77, 0x60100\n\ts_mov_b32 s82, 2");
      asm volatile("s_add_u32 s86, s80, %0\n\ts_addc_u32 s87, s81, 0\n\ts_add_u32 s98, s98, %1\n\ts_addc_u32 s99, s99, 0"
                   ::"s"(mt * 256 * DC * 2), "s"(ofW) : "scc");
      sset<66>(192 * lda); sset<69>(384 * lda); sset<70>(448 * lda);
      sset<78>(lA + wid * 1024); sset<79>(lW + wid * 1024);
    }
    GAP();
    if (doP) {
      asm volatile("s_waitcnt vmcnt(11)" ::: "memory");
#pragma unroll
      for (int q = 0; q < 7; ++q) asm volatile("" : "+v"(pr.kk[q]));
#pragma unroll
      for (int q = 0; q < 4; ++q) asm volatile("" : "+v"(pr.vv[q]));
      static_for([&](auto Ic) __attribute__((always_inline)) {
        constexpr int I = Ic;
        if constexpr (I < 2) g_pro(g0 + 2 + I, mA, mW, lda);
        if constexpr (I == 2) g_pro2(mA, mW);
        GAP();
        prep_math<I>(pr, tid, lds + 131072);
        GAP();
      }, make_iseq<4>{});
    } else { g_pro(g0 + 2, mA, mW, lda); g_pro(g0 + 3, mA, mW, lda); g_pro2(mA, mW); }
    GAP();
    g.aA[0] = vxor<128, 0>(); g.aA[1] = vxor<128, 64>();
    static_for([&](auto I) { g.aB[I >> 3][I & 7] = vxor<129 + (I >> 3), 32 * (I & 7)>(); }, make_iseq<16>{});
    c.o0 = vxor<131, 0>(); c.o1 = vxor<132, 0>(); c.tb = vxor<133, 0>(); c.vs = vxor<134, 0>();
    asm volatile("v_add_u32 v91, 0x100000, v93\n\ts_waitcnt vmcnt(8) lgkmcnt(0)\n\ts_barrier" ::: "memory");
    if (isY) g_read0<0>(g, make_iseq<16>{});
    else g_read0<1>(g, make_iseq<16>{});
    GAP();
    asm volatile("v_readfirstlane_b32 s48, v120\n\tv_readfirstlane_b32 s49, v121\n\tv_readlane_b32 s80, v78, %0\n\tv_readlane_b32 s81, v78, %1\n\t"
                 "s_mov_b32 s50, 0x4c957f2d\n\ts_mul_i32 s84, s48, s50\n\ts_mul_hi_u32 s85, s48, s50\n\ts_mul_i32 s49, s49, s50\n\t"
                 "s_mul_i32 s48, s48, 0x5851f42d\n\ts_add_u32 s85, s85, s49\n\ts_add_u32 s85, s85, s48\n\ts_add_u32 s84, s84, 0xf767814f\n\t"
                 "s_addc_u32 s85, s85, 0x14057b7e\n\ts_lshr_b64 s[80:81], s[80:81], 12\n\tv_mov_b32 v82, s84\n\tv_mov_b32 v83, s85"
                 ::"i"((int)T_TAB), "i"(T_TAB + 1) : "scc");
    if (isY) asm volatile("s_mov_b64 s[80:81], 0x6543");
    if (doP) prep_store(tid, pj, ptb, (bf16*)sptr<88>() + O_X / 2, lds + 131072);
    asm volatile("s_waitcnt lgkmcnt(0)" ::: "memory");
    GAP();
    if (isY) {
      g_tile<false, 0, 1>(g);
      g_loop<false, true>(g, [&]() __attribute__((always_inline)) {
        tload<80, T_TAB>(); tload<81, T_TAB + 1>();
        fl_set<T_FPREP>();
        asm volatile("s_mov_b64 s[60:61], s[98:99]\n\ts_mov_b32 s62, %0" ::"i"((DC - 1) * LDW * 2 + 512));
      }, [&]() __attribute__((always_inline)) {
        asm volatile("s_mov_b64 s[56:57], s[86:87]\n\ts_mov_b32 s58, %0\n\tv_mov_b32 v92, v94" ::"i"(256 * DC * 2));
        static_for([&](auto J) { asm volatile("s_mov_b32 s%c0, %1" ::"i"(64 + J), "i"((J + 1) * 64 * DC)); }, make_iseq<7>{});
      });
      tload<101, T_OQ>(); sset<83>(__float_as_uint(CS));
      asm volatile("v_mov_b32 v112, v84\n\tv_add_u32 v113, 8192, v84");
      GAP();
      g_tile<false, 0, 2>(g);
      KST(0, 3);
    }
    auto kvtile = [&](auto TRc) __attribute__((always_inline)) {
      constexpr bool TR = TRc;
      if (isY) g_tile<TR, 1, 4>(g);
      else g_tile<TR, 1, 1>(g);
      asm volatile("s_mov_b32 s83, s78");
      g_loop<TR, false>(g, [&]() __attribute__((always_inline)) {
        asm volatile("v_readlane_b32 s49, v78, %1\n\tv_readlane_b32 s48, v78, %0\n\tv_and_b32 v122, 0xffffff00, v82\n\tv_mov_b32 v123, v83\n\t"
                     "s_and_b32 s49, s49, 0xf000000\n\ts_cselect_b32 s49, -2.0, 0\n\tv_or_b32 v121, s49, v81\n\ts_nop 0\n\t"
                     "buffer_store_dwordx2 v[122:123], v121, s[88:91], s48 offen sc1" ::"i"((int)T_CNT), "i"(T_IT + T_ITN + 16) : "memory", "scc");
        if (isY) fl_set<T_FQ>();
        else {
          int pv = pu; asm volatile("" : "+s"(pv));
          if (doA) post_dma(pj, pv, arg_late<const bf16, 12>(), 131072 + wid * 8192);
          else {
            const char* tp = arg_late<const char, 0>() + 1024 * DC * 2 + pv * 32768 + __float_as_uint(vget<84>()) * 8;
            asm volatile("s_mov_b32 m0, %0\n\ts_nop 0" ::"i"(m0d));
#pragma unroll
            for (int i = 0; i < 4; ++i) asm volatile("global_load_lds_dword %0, off" ::"v"(tp + i * 8192) : "memory");
          }
        }
      }, [&]() __attribute__((always_inline)) {
        asm volatile("s_mov_b32 s79, 0x28000\n\ts_mov_b32 s83, s79");
        fl_poll<T_NPRE>();
        if (!isY) {
          int pv = pu; asm volatile("" : "+s"(pv));
          if (doA) {
            const char* tp = arg_late<const char, 9>() + (size_t)(pj * 64 + pv) * (SEQ * 2) + __float_as_uint(vget<84>()) * 4;
            asm volatile("s_mov_b32 m0, %1\n\ts_nop 0\n\tglobal_load_lds_dword %0, off" ::"v"(tp), "i"(m0d) : "memory");
          } else {
            CODE_TOUCH("at_code_begin", "pf_code_end", (pv >> 5) * 8 + (pv & 3) * 2, 16u, 64, m0d);
            CODE_TOUCH("at_code_begin", "pf_code_end", (pv >> 5) * 8 + (pv & 3) * 2 + 1, 16u, 64, m0d);
          }
        }
      });
      tload<101, T_OG>(); tload<96, T_Q0>(); tload<98, T_K0>();
      fl_check();
      tload<50, T_NST>(); tload<51, T_NST + 1>();
      asm volatile("v_mov_b32 v112, v84\n\tv_add_u32 v113, %0, v84\n\tv_mov_b32 v90, v87\n\tv_mov_b32 v91, v87\n\tv_mov_b32 v92, v87\n\tv_mov_b32 v93, v87\n\t"
                   "v_mov_b32 v94, v86\n\tv_mov_b32 v95, v86\n\tv_mov_b32 v126, v86\n\tv_mov_b32 v127, v86\n\ts_mov_b32 s99, 0x8000\n\t" FL_ARM("s[50:51]")
                   ::"i"(TR ? 4096 : 8192));
      if (doA) {
        const int d = pj * 64 + pu;
        const bf16* dp = arg_late<const bf16, 10>() + d;
        asm volatile("global_load_ushort v123, %0, off" ::"v"(dp) : "memory");
        srd_set<56>(arg_late<const bf16, 9>() + (size_t)d * SEQ, SEQ * 2);
        srd_set<60>(arg_late<const char, 5>() + O_X + d * SEQ * 2, SEQ * 2);
        sset<78>(65536 + wid * 15488);
        asm volatile("s_movk_i32 s64, 992\n\tv_subrev_u32 v122, 32, v84");
        GAP();
        g_tile<TR, 0, 3>(g);
      } else {
        sset<79>(isY ? wid * 8192 : 0x30000);
        if (tget<T_IT + T_ITN + 16>() & 16) asm volatile("s_add_u32 s98, s98, 0x8000\n\ts_mov_b32 s97, 0xffff8000" ::: "scc");
        else asm volatile("s_mov_b32 s97, 0x8000");
        GAP();
        g_tile<TR, 0, 5>(g);
      }
    };
    if (kv) kvtile(std::true_type{});
    else kvtile(std::false_type{});
  }
  KST(0, 4);
  asm volatile("s_waitcnt vmcnt(16)" ::: "memory");
  const unsigned md0 = tget<T_IT + 16>(), md1 = tget<T_IT + T_ITN + 16>();
  sset<79>(wid * 8192 + (md0 & 16) * 2048);
  if (doA) {
    const float Dd = bfl(__float_as_uint(vget<123>()));
    KST(0, 5);
    conv_wave<T_FG>(O_Z + pj * 64 * SEQ * 2 + ((pu & 7) * 8 + (pu >> 3)) * 64, pu >> 3, Dd, lds + 65536 + wid * 15488);
    const char* tp = arg_late<const char, 11>() + (size_t)h * (HD * DA * 2) + ((pj * 4 + wid) * 64 + lane) * 128;
    asm volatile("s_mov_b32 m0, %1\n\ts_nop 0\n\tglobal_load_lds_dword %0, off\n\ts_waitcnt vmcnt(17)" ::"v"(tp), "i"(m0d) : "memory");
    KST(0, 6);
  } else if (!isY) {
    static_for([&](auto I) { d_store<16 + I>(); }, make_iseq<16>{});
    tload<98, T_K0>();
    asm volatile("s_waitcnt vmcnt(0)\n\ts_barrier" ::: "memory");
    fl_set<T_FG>();
    KST(0, 5);
    fl_check();
    CODE_TOUCH("att_begin", "pf_code_end", hsel * 4 + wid, 8u, 64, m0d);
    dma4k<0>(); dma4k<1>(); dma4k<2>(); dma4k<3>();
    asm volatile("s_waitcnt vmcnt(0)" ::: "memory");
    KST(0, 6);
  } else {
    tload<76, T_PART>();
    fl_check();
    asm volatile("buffer_load_dwordx2 v[122:123], v88, s[88:91], s76 offen offset:1024 sc1" ::: "memory");
  }
  sset<78>(65536 + wid * 8192); sset<79>(98304 + wid * 8192);
  { const bf16* wp = arg_late<const bf16, 11>() + ((md1 >> 24) & 15) * HD * DA; asm volatile("s_mov_b64 s[80:81], %0" ::"s"(wp)); }
  asm volatile("att_begin:");
  ev_code(c);
  asm volatile("s_waitcnt lgkmcnt(0)\n\ts_barrier" ::: "memory");
#pragma unroll 1
  for (int it = md0 ? 0 : 1; it < 2; ++it) {
    const unsigned mode = it ? md1 : md0;
    const bool odd = mode & 16;
    if (odd) kreads<1>(make_iseq<8>{});
    else kreads<0>(make_iseq<8>{});
    const unsigned oP = tget<T_PART>();
    if (it == 0) {
      static_for([&](auto I) { tload<57 + I, T_IT + I>(); }, make_iseq<21>{});
      tload<S_T, T_IT + 21>(); tload<S_EV, T_IT + 22>();
    } else {
      static_for([&](auto I) { tload<57 + I, T_IT + T_ITN + I>(); }, make_iseq<21>{});
      tload<S_T, T_IT + T_ITN + 21>(); tload<S_EV, T_IT + T_ITN + 22>();
    }
    asm volatile("s_add_u32 s98, s97, 0x8000\n\ts_mov_b32 s56, -1\n\tv_mov_b64 v[114:115], 0\n\tv_mov_b64 v[118:119], 0\n\t"
                 "v_mov_b32 v120, 0x45800000" ::: "scc");
    KST(0, 8 + 3 * (it && !isY));
    GAP();
#define STA(k)
#define STI()
#define LOOP_ENDS "s_bfe_u32 s76, s73, 0x80008"
    STA(0);
    if (odd) {
      if (isY) phaseA<1, 2>(c);
      else phaseA<1, 1>(c);
      STA(1);
      STA(2);
      phaseB0<1>(mode, isY);
      STA(3);
      asm volatile(LOOP_ENDS "\n\ts_branch att_mid" ::: "scc");
    } else {
      if (isY) phaseA<0, 2>(c);
      else phaseA<0, 1>(c);
      STA(1);
      if (__builtin_expect(mode & 4, 0)) EV_CALL("ev_turn");
      STA(2);
      phaseB0<0>(mode, isY);
      if (__builtin_expect(mode & 4, 0)) { EV_CALL("ev_post"); EV_CALL("ev_mask"); acc_zero(); asm volatile("s_branch att_done"); }
      STA(3);
    }
    GAP();
    asm volatile(LOOP_ENDS "\natt_loop:" ::: "scc");
    GAP();
    phaseA<1, 0>(c);
    STI();
    if (__builtin_expect(c.rare != 0, 0)) sm_redo<1>();
    phaseB<1>(c);
    asm volatile("s_cbranch_scc1 att_done\natt_mid:");
    phaseA<0, 0>(c);
    STI();
    if (__builtin_expect(c.rare != 0, 0)) sm_redo<0>();
    phaseB<0>(c);
    asm volatile("s_cbranch_scc0 att_loop\natt_done:");
    GAP();
    STA(4);
    if (it == 0) phaseA<0, 3>(c);
    else {
      sset<76>(oP);
      GAP();
      phaseAL(c, make_iseq<32>{});
      if (__builtin_expect(redo(), 0)) sm_redo<1, false>();
      if (mode & 2) phaseBL<true>(c, make_iseq<64>{});
      else phaseBL<false>(c, make_iseq<64>{});
    }
    if (__builtin_expect(redo(), 0)) {
      if (it) static_for([&](auto I) { vset<R_S + 32 + (I >> 2) * 8 + (I & 3)>(-INFINITY); }, make_iseq<16>{});
      asm volatile("s_nop 7");
      sm_redo<0, false>();
    }
    STA(5);
    KST(0, 9 + 3 * (it && !isY));
    float l[2];
    asm volatile("v_add_f32 %0, v%c2, v%c3\n\tv_add_f32 %1, v%c4, v%c5" : "=v"(l[0]), "=v"(l[1]) : "i"(R_RS), "i"(R_TS), "i"(R_RS + 1), "i"(R_TS + 1));
    const float cm[2] = {vget<R_M>(), vget<R_M + 1>()};
    l[0] = row_reduce<false>(l[0]); l[1] = row_reduce<false>(l[1]);
    EP p;
    if (it == 0) {
      const f32x4 ml = {cm[0], l[0], cm[1], l[1]};
      asm volatile("buffer_store_dwordx4 %0, v88, s[88:91], %1 offen sc1\n\ts_mov_b32 s101, %2" ::"v"(ml), "s"(oP), "s"(oP + wid * 16384) : "memory");
      phaseF(c, p, make_iseq<74>{});
      asm volatile(FL_POLL "\n\ts_waitcnt vmcnt(17)" ::: "memory");
    } else {
      if (!(mode & 2)) {
        p.fa[0] = __builtin_amdgcn_rcpf(l[0]); p.fa[1] = __builtin_amdgcn_rcpf(l[1]);
        phaseFL<1>(p);
      } else {
        const f32x4 ml = {vget<122>(), vget<123>(), vget<124>(), vget<125>()};
        if (__builtin_expect(__builtin_amdgcn_ballot_w64(ml[0] != cm[0] || ml[2] != cm[1]) == 0, 1)) {
          p.fa[0] = __builtin_amdgcn_rcpf(l[0] + ml[1]); p.fa[1] = __builtin_amdgcn_rcpf(l[1] + ml[3]);
          phaseFL<3>(p);
        } else {
#pragma unroll
          for (int e = 0; e < 2; ++e) {
            const float mm = fmaxf(cm[e], ml[2 * e]);
            const float wa = __builtin_amdgcn_exp2f(cm[e] - mm), wb = __builtin_amdgcn_exp2f(ml[2 * e] - mm);
            const float inv = __builtin_amdgcn_rcpf(l[e] * wa + ml[2 * e + 1] * wb);
            p.fa[e] = wa * inv; p.fb[e] = wb * inv;
          }
          phaseFL<2>(p);
        }
      }
    }
    KST(0, 10 + 3 * (it && !isY));
  }
  asm volatile(TICKET "\n\tv_xor_b32 v%c0, s100, v%c0\np_loop:" ::"i"(R_TB) : "memory");
  GAP();
  p_half<0, true>(make_iseq<32>{});
  asm volatile("s_cbranch_scc0 p_mid");
  p_half<1, true>(make_iseq<32>{});
  asm volatile("s_branch p_loop\np_loop2:");
  p_half<0, false>(make_iseq<32>{});
  asm volatile("p_mid:");
  p_half<1, false>(make_iseq<32>{});
  asm volatile("s_cbranch_scc1 p_loop2\n\ts_nop 5");
  GAP();
  p_cvt<1, 0>(); p_cvt<1, 1>(); p_cvt<1, 2>(); p_cvt<1, 3>();
  p_store<1, 1>();
  GAP();
  KST(0, 27);
  {
    constexpr unsigned rdy = 98304;
    int lz = threadIdx.x & 63; asm volatile("" : "+v"(lz));
    const int fr = lz & 15, fq = lz >> 4, qbF = md1 >> 28;
    if (wid == 0) {
#pragma unroll 1
      for (int q = 0; q < 0x100000; ++q) {
        unsigned lo, hi, tl, th;
        asm volatile("v_readfirstlane_b32 %0, v240\n\tv_readfirstlane_b32 %1, v241\n\ts_mov_b32 %2, s84\n\ts_mov_b32 %3, s85" : "=s"(lo), "=s"(hi), "=s"(tl), "=s"(th));
        if (__builtin_expect((((lo ^ tl) >> 8) | (hi ^ th)) == 0, 1)) break;
        asm volatile("s_sleep 4\n\tv_mov_b64 v[240:241], 1\n\t" TICKET "\n\ts_waitcnt vmcnt(0)" ::: "memory");
      }
      asm volatile("v_mov_b32 v241, 0\n\ts_mov_b64 exec, 1\n\tds_write_b64 %0, v[240:241]\n\ts_mov_b64 exec, -1" ::"v"(rdy) : "memory");
    }
    vsetu<79>(fo(1024) + qbF * 4352 + fr * 8);
    {
      const int jj = 4 * (fr >> 3) + (fr & 3);
      const unsigned one = ((fq ^ (fr >> 2)) & 1) ? 0u : (jj & 1) ? 0x3f800000u : 0x3f80u;
      static_for([&](auto I) { vsetu<96 + I>((jj >> 1) == I ? one : 0u); }, make_iseq<4>{});
    }
    unsigned vo = (fq >> 1) * 1024 + (fq & 1) * 256 + fr * 16;
    bf16* out = arg_late<bf16, 13>();
    __amdgpu_buffer_rsrc_t so_ = __builtin_amdgcn_make_buffer_rsrc((void*)out, (short)0, SEQ * OUTW * 2, 0x00020000);
    unsigned vw = ((qbF * 128 + (fr & 7)) * OUTW + DA + (fr >> 3) * 32 + fq * 8) * 2;
    asm volatile("" : "+s"(so_));
    asm volatile("s_waitcnt vmcnt(0) lgkmcnt(0)\n\ts_barrier" : "+v"(vo), "+v"(vw) :: "memory");
    fl_set<T_FS>();
    unsigned tk;
    asm volatile("ds_read_b32 %0, %1\n\ts_waitcnt lgkmcnt(0)" : "=v"(tk) : "v"(rdy) : "memory");
    tk = __builtin_amdgcn_readfirstlane(tk) & 255;
    KST(0, 30);
    if (doA) post_math(pj, pu, out, lds + 131072 + wid * 8192);
#define N3 0
#define N2 12
    constexpr int N1 = 28 - 3 * N3 - 2 * N2;
    static_assert(N1 >= 0 && N3 + N2 + N1 <= 16);
    const int nu = tk < N3 ? 3 : tk < N3 + N2 ? 2 : tk < N3 + N2 + N1 ? 1 : 0;
    const int u0 = 4 * (tk < N3 ? 3 * tk : tk < N3 + N2 ? N3 + 2 * tk : 2 * N3 + N2 + tk) + wid * nu;
    KST(0, 28);
    if (nu) {
      const unsigned so0 = O_S + qbF * 0x200000 + (16 + u0) * 2048, so[3] = {so0, so0, so0};
      unsigned pend = 0x5555u, cnt = 0x400000;
      while (true) {
        unsigned nw;
        if (wid == 3) {
          u64 m;
          const u64 ex = pend | (pend << 1);
          asm volatile(FL_ARM("%1") "\n\t" FL_POLL "\n\ts_waitcnt vmcnt(0)\n\tv_cmp_eq_u64 %0, s[84:85], v[124:125]" : "=s"(m) : "s"(ex) : "memory");
          nw = (unsigned)m & ((unsigned)m >> 1) & pend;
          if (nw) asm volatile("ds_write_b32 %0, %1 offset:4" ::"v"(rdy), "v"(~pend | nw) : "memory");
        } else {
          unsigned t;
          asm volatile("ds_read_b32 %0, %1 offset:4\n\ts_waitcnt lgkmcnt(0)" : "=v"(t) : "v"(rdy) : "memory");
          nw = __builtin_amdgcn_readfirstlane(t) & pend;
        }
        if (--cnt == 0) nw = pend;
        if (nw == 0) {
          if (wid == 3) asm volatile("s_sleep 4");
          else asm volatile("s_sleep 1");
          continue;
        }
        asm volatile("s_nop 4");
        static_for([&](auto S) {
          if ((nw >> (2 * S)) & 1) {

            r_load<0, 2 * S>(vo, so[0]); r_load<0, 2 * S + 1>(vo, so[0]);
            if (nu > 1) { r_load<1, 2 * S>(vo, so[1]); r_load<1, 2 * S + 1>(vo, so[1]); }
            if (nu > 2) { r_load<2, 2 * S>(vo, so[2]); r_load<2, 2 * S + 1>(vo, so[2]); }
          }
        }, make_iseq<8>{});
        pend &= ~nw;
        if (pend == 0) break;
      }
      auto oo = [&](int U) { return ((((U >> 1) & 1) * 64 + ((U >> 2) & 3) * 16 + (U & 1) * 8) * OUTW + (U >> 4) * 64) * 2; };
      unsigned o0 = oo(u0), o1 = oo(u0 + 1), o2 = oo(u0 + 2);
      asm volatile("s_waitcnt vmcnt(0)" : "+s"(o0), "+s"(o1), "+s"(o2), "+s"(so_) :: "memory");
      KST(0, 16);
      if (nu > 2) r_sums<2>();
      if (nu > 1) r_sums<1>();
      r_sums<0>();
      if (nu > 2) r_fin<2>(vw, so_, o2);
      if (nu > 1) r_fin<1>(vw, so_, o1);
      asm volatile("s_nop 7");
      r_fin<0>(vw, so_, o0);
      if (qbF == 15 && tk == 0) asm volatile(FL_SET("%0") ::"s"(fo(6144)) : "memory");
    }
    KST(0, 29);
  }
  asm volatile("pf_code_end:");
  KST(0, 1);
}

extern "C" void kernel_launch(void* const* d_in, const int* in_sizes, int n_in, void* d_out, int out_size, void* d_ws,
                              size_t ws_size, hipStream_t stream) {
  const bf16* Aq = (const bf16*)d_in[0];
  const bf16* Ak = (const bf16*)d_in[1];
  const bf16* Av = (const bf16*)d_in[2];
  const bf16* Ash = (const bf16*)d_in[3];
  const bf16* Assmk = (const bf16*)d_in[4];
  const bf16* AssmD = (const bf16*)d_in[5];
  const bf16* Bx = (const bf16*)d_in[6];
  const bf16* Bckv = (const bf16*)d_in[7];
  const bf16* Bwq = (const bf16*)d_in[8];
  const bf16* Bwuk = (const bf16*)d_in[9];
  const bf16* Bwuv = (const bf16*)d_in[10];
  const bf16* Bwo = (const bf16*)d_in[11];
  bf16* out = (bf16*)d_out;
  hipFuncSetAttribute((const void*)fused_kernel, hipFuncAttributeMaxDynamicSharedMemorySize, 163840);
  fused_kernel<<<256, 256, 163840, stream>>>(Bckv, Bwuk, Bwuv, Bx, Bwq, (bf16*)d_ws, Ak, Av, Ash, Assmk, AssmD, Bwo, Aq, out);
}
```

```cpp
#include <hip/hip_runtime.h>
#include <hip/hip_bf16.h>
#include <stdint.h>
#include <utility>

typedef __hip_bfloat16 bf16;
typedef __attribute__((ext_vector_type(4))) short bf16x4;
typedef __attribute__((ext_vector_type(2))) unsigned u32x2;

constexpr int SEQ = 2048;
constexpr int DA = 448;
constexpr int NH = 16;
constexpr int HD = 256;
constexpr int DC = 1024;
constexpr int OUTW = 896;
constexpr unsigned TSB = NH * SEQ * HD * 2, O_K = TSB, O_S = 3 * TSB, O_P = 5 * TSB, PSTR = 67584, O_F = O_P + 128 * PSTR,
                   O_X = O_F + 400 * 4352, O_Z = O_X + DA * SEQ * 2, WSB = O_Z + DA * SEQ * 2;
constexpr unsigned fo(int w) { return O_F + (w >> 4) * 4352 + (w & 15) * 8; }

#define KST(kid, which)

__device__ __forceinline__ float bf2f(unsigned short u) { return __uint_as_float(((unsigned)u) << 16); }
__device__ __forceinline__ unsigned short f2bf(float f) { return __bfloat16_as_ushort(__float2bfloat16(f)); }
__device__ __forceinline__ float rbf(float f) { return bf2f(f2bf(f)); }
typedef __attribute__((ext_vector_type(2))) __bf16 bf16x2n;
typedef __attribute__((ext_vector_type(2))) float f32x2n;
__device__ __forceinline__ unsigned pk2(float a, float b) {
  return __builtin_bit_cast(unsigned, __builtin_convertvector((f32x2n){a, b}, bf16x2n));
}

__device__ __forceinline__ int off_b(int row, int ch) { return 256 * row + 16 * (ch ^ (((row & 3) << 2) | ((row >> 2) & 3))); }

template <int R> __device__ __forceinline__ void sset(unsigned x) { asm volatile("s_mov_b32 s%c0, %1" ::"i"(R), "s"(x)); }
template <int R> __device__ __forceinline__ void vsetu(unsigned x) { asm volatile("v_mov_b32 v%c0, %1" ::"i"(R), "v"(x)); }
template <int R> __device__ __forceinline__ void srd_set(const void* p, unsigned n) {
  asm volatile("s_mov_b64 s[%c0:%c1], %4\n\ts_mov_b32 s%c2, %5\n\ts_mov_b32 s%c3, 0x20000" ::"i"(R), "i"(R + 1), "i"(R + 2), "i"(R + 3), "s"(p), "s"(n));
}
template <int R, int L> __device__ __forceinline__ void tload() { asm volatile("v_readlane_b32 s%c0, v78, %1" ::"i"(R), "i"(L)); }
template <int L> __device__ __forceinline__ unsigned tget() { unsigned t; asm volatile("v_readlane_b32 %0, v78, %1" : "=s"(t) : "i"(L)); return t; }
#define FL_SET(S) "buffer_store_dwordx2 v[82:83], v81, s[88:91], " S " offen sc1"
#define FL_ARM(N) "v_cndmask_b32_e64 v80, -2.0, v79, " N
#define FL_POLL "buffer_load_dwordx2 v[124:125], v80, s[88:91], 0 offen sc1"
#define FL_CHECK(N) "s_mov_b32 s48, 0x100000\n7:\n\tv_cmp_eq_u64 vcc, s[84:85], v[124:125]\n\ts_and_b64 vcc, vcc, " N "\n\ts_cmp_eq_u64 vcc, " N \
                    "\n\ts_cbranch_scc1 8f\n\ts_sleep 2\n\t" FL_POLL "\n\ts_waitcnt vmcnt(0)\n\ts_sub_u32 s48, s48, 1\n\ts_cbranch_scc0 7b\n8:"
template <int L> __device__ __forceinline__ void fl_set() {
  asm volatile("v_readlane_b32 s48, v78, %0\n\ts_nop 4\n\t" FL_SET("s48") ::"i"(L) : "memory");
}
template <int L> __device__ __forceinline__ void fl_poll() {
  asm volatile("v_readlane_b32 s50, v78, %0\n\tv_readlane_b32 s51, v78, %1\n\ts_nop 1\n\t" FL_ARM("s[50:51]") "\n\t" FL_POLL ::"i"(L), "i"(L + 1) : "memory");
}
#define TICKET "buffer_atomic_add_x2 v[240:241], v81, s[88:91], s101 offen sc0"
__device__ __forceinline__ void fl_check() { asm volatile(FL_CHECK("s[50:51]") ::: "vcc", "scc", "memory"); }
template <int R, int K> __device__ __forceinline__ unsigned vxor() { unsigned t; asm volatile("v_xor_b32 %0, %1, v%c2" : "=v"(t) : "i"(K), "i"(R)); return t; }
template <int R> __device__ __forceinline__ float vget() { float t; asm volatile("v_mov_b32 %0, v%c1" : "=v"(t) : "i"(R)); return t; }
template <int R> __device__ __forceinline__ void vset(float x) { asm volatile("v_mov_b32 v%c0, %1" ::"i"(R), "v"(x)); }
template <int R> __device__ __forceinline__ float acc_read1() { float t; asm volatile("v_accvgpr_read_b32 %0, a[%c1]" : "=v"(t) : "i"(R)); return t; }
template <class F, int... I> __device__ __forceinline__ void static_for(F&& f, std::integer_sequence<int, I...>) {
  (f(std::integral_constant<int, I>{}), ...);
}
template <int... Is> using iseq = std::integer_sequence<int, Is...>;
template <int N> using make_iseq = std::make_integer_sequence<int, N>;

typedef __attribute__((ext_vector_type(4))) unsigned u32x4c;
typedef __attribute__((ext_vector_type(4))) float f32x4c;
typedef __attribute__((ext_vector_type(2))) float f32x2c;
typedef unsigned long long u64;
struct PR { u32x4c kk[7], vv[4]; };
__device__ __forceinline__ void prep_load(PR& p, int tid, int cg, int tb, const bf16* __restrict__ k, const bf16* __restrict__ v,
                                          const bf16* __restrict__ shift_k) {
  const __amdgpu_buffer_rsrc_t sk = __builtin_amdgcn_make_buffer_rsrc((void*)k, (short)0, SEQ * DA * 2, 0x00020000);
  const __amdgpu_buffer_rsrc_t sv = __builtin_amdgcn_make_buffer_rsrc((void*)v, (short)0, SEQ * DA * 2, 0x00020000);
  const __amdgpu_buffer_rsrc_t sw = __builtin_amdgcn_make_buffer_rsrc((void*)(shift_k + cg * 256), (short)0, 512, 0x00020000);
  const int vo = ((tb * 128 + (tid >> 3) * 4) * DA + cg * 64 + (tid & 7) * 8) * 2;
#pragma unroll
  for (int q = 0; q < 7; ++q)
    asm volatile("buffer_load_dwordx4 %0, %1, %2, 0 offen" : "=v"(p.kk[q]) : "v"(vo + (q - 3) * DA * 2), "s"(sk) : "memory");
#pragma unroll
  for (int q = 0; q < 4; ++q)
    asm volatile("buffer_load_dwordx4 %0, %1, %2, 0 offen offset:%3" : "=v"(p.vv[q]) : "v"(vo), "s"(sv), "i"(q * DA * 2) : "memory");
#pragma unroll
  for (int q = 0; q < 4; ++q)
    asm volatile("buffer_load_dwordx4 a[%c0:%c1], %2, %3, 0 offen offset:%4" ::"i"(4 * q), "i"(4 * q + 3), "v"((tid & 7) * 64), "s"(sw), "i"(q * 16) : "memory");
}
__device__ __forceinline__ float bfl(unsigned u) { return __uint_as_float(u << 16); }
__device__ __forceinline__ float bfh(unsigned u) { return __uint_as_float(u & 0xffff0000u); }
__device__ __forceinline__ f32x2c rbf2(f32x2c a) {
  return (f32x2c){__uint_as_float(pk2(0.f, a[0])), __uint_as_float(pk2(0.f, a[1]))};
}
template <int I> __device__ __forceinline__ void prep_math(const PR& p, int tid, char* lx) {
  const int rp = tid >> 3, c = tid & 7;
  f32x2c wt[4], kf[7];
  const unsigned w[4] = {__float_as_uint(acc_read1<4 * I>()), __float_as_uint(acc_read1<4 * I + 1>()), __float_as_uint(acc_read1<4 * I + 2>()),
                         __float_as_uint(acc_read1<4 * I + 3>())};
#pragma unroll
  for (int t = 0; t < 4; ++t)
    wt[t] = (t & 1) ? (f32x2c){bfh(w[t >> 1]), bfh(w[2 + (t >> 1)])} : (f32x2c){bfl(w[t >> 1]), bfl(w[2 + (t >> 1)])};
#pragma unroll
  for (int q = 0; q < 7; ++q) kf[q] = (f32x2c){bfl(p.kk[q][I]), bfh(p.kk[q][I])};
  f32x2c x[4];
#pragma unroll
  for (int e = 0; e < 4; ++e) {
    f32x2c s = rbf2(kf[e] * wt[3]);
    s = rbf2(s + rbf2(kf[e + 1] * wt[2]));
    s = rbf2(s + rbf2(kf[e + 2] * wt[1]));
    s = rbf2(s + rbf2(kf[e + 3] * wt[0]));
    x[e] = s * (f32x2c){bfl(p.vv[e][I]), bfh(p.vv[e][I])};
  }
#pragma unroll
  for (int u = 0; u < 2; ++u)
    *(u32x2*)(lx + (c * 8 + 2 * I + u) * 256 + (((31 - rp) * 8) ^ (c << 4))) = (u32x2){pk2(x[3][u], x[2][u]), pk2(x[1][u], x[0][u])};
}
__device__ __forceinline__ void prep_store(int tid, int cg, int tb, bf16* __restrict__ xr, const char* lx) {
  const __amdgpu_buffer_rsrc_t sx = __builtin_amdgcn_make_buffer_rsrc((void*)(xr + (size_t)cg * 64 * SEQ), (short)0, 64 * SEQ * 2, 0x00020000);
#pragma unroll
  for (int rd = 0; rd < 4; ++rd) {
    const int idx = tid + 256 * rd, ch = idx >> 4, q = idx & 15;
    u32x4c w = *(const u32x4c*)(lx + ch * 256 + 16 * (q ^ (ch >> 3)));
    __builtin_amdgcn_raw_buffer_store_b128(w, sx, (ch * SEQ + (15 - tb) * 128 + q * 8) * 2, 0, 16);
  }
}

#define CODE_TOUCH(LB, LE, wx, NW, NL, m0v) do { unsigned blo_, bhi_, elo_; \
    asm volatile("s_getpc_b64 s[48:49]\n\ts_add_u32 s48, s48, " LB "@rel32@lo+4\n\ts_addc_u32 s49, s49, " LB "@rel32@hi+12\n\t" \
                 "s_mov_b32 %0, s48\n\ts_mov_b32 %1, s49\n\t" \
                 "s_getpc_b64 s[48:49]\n\ts_add_u32 s48, s48, " LE "@rel32@lo+4\n\ts_mov_b32 %2, s48" \
                 : "=s"(blo_), "=s"(bhi_), "=s"(elo_) :: "scc"); \
    const unsigned n_ = elo_ - (blo_ & ~127u); \
    const __amdgpu_buffer_rsrc_t srd_ = __builtin_amdgcn_make_buffer_rsrc((void*)(((unsigned long long)bhi_ << 32) | (blo_ & ~127u)), (short)0, n_ < 262144u ? n_ : 0u, 0x00020000); \
    const unsigned l_ = threadIdx.x & 63, vo_ = l_ < (NL) ? ((wx) + l_ * (NW)) * 128u : 0x80000000u; \
    asm volatile("s_mov_b32 m0, %0\n\ts_nop 4\n\tbuffer_load_dword %1, %2, 0 offen lds" ::"s"(m0v), "v"(vo_), "s"(srd_) : "memory"); } while (0)
template <int I> __device__ __forceinline__ void d_store() {
  asm volatile("buffer_store_dwordx4 v[%c0:%c1], v%c2, s[88:91], s101 offen offset:%3 sc1" ::"i"(128 + 4 * I), "i"(131 + 4 * I), "i"(112 + ((I >> 2) & 1)),
               "i"((I & 3) * 1024) : "memory");
  if constexpr ((I & 7) == 7) asm volatile("s_addk_i32 s101, 0x4000" ::: "scc");
}
constexpr int NS_X = 19;
struct CV { unsigned pa[4], pb, px, vz, sz; float D; };
constexpr int cv_J(bool up, int q) { int J = up ? 0 : 7; while (q > J) { q -= J + 1; J += up ? 1 : -1; } return J; }
constexpr int cv_i(bool up, int q) { int J = up ? 0 : 7; while (q > J) { q -= J + 1; J += up ? 1 : -1; } return q; }
template <int R, int N> __device__ __forceinline__ void cv_rd(CV& c) {
  if constexpr (N < 16) {
    constexpr int i = N >> 1, o = 16 * R + 128 * (i & 1) + 2 * (N & 1), r = 32 + 32 * (R & 1) + 2 * N;
    asm volatile("ds_read2_b32 a[%c1:%c2], %0 offset0:%c3 offset1:%c4" ::"v"(c.pa[i >> 1]), "i"(r), "i"(r + 1), "i"(o), "i"(o + 1));
  } else {
    constexpr int m = N - 16, r = 192 + 32 * (R & 1) + 4 * m;
    asm volatile("ds_read_b128 a[%c1:%c2], %0 offset:%c3" ::"v"(c.pb), "i"(r), "i"(r + 3), "i"(64 * R + 512 * (7 - m)));
  }
}
template <int O> __device__ __forceinline__ void cv_xd(CV& c) {
  constexpr int e = O & 31, x = 96 + ((e >> 1) ^ 1);
  if constexpr (O >= 64) asm volatile("v_cvt_pk_bf16_f32 v%c0, 0, v%c0" ::"i"(160 + e));
  else if constexpr (O >= 32) asm volatile("v_mul_f32 v%c0, v%c0, %1" ::"i"(160 + e), "v"(c.D));
  else if constexpr (O & 1) asm volatile("v_lshlrev_b32 v%c0, 16, v%c1" ::"i"(160 + e), "i"(x));
  else asm volatile("v_and_b32 v%c0, 0xffff0000, v%c1" ::"i"(160 + e), "i"(x));
}
template <int O> __device__ __forceinline__ void cv_z(CV& c) {
  constexpr int J = O / 11, k = O % 11, y = 128 + 4 * J;
  if constexpr (k < 4) asm volatile("v_cvt_pk_bf16_f32 v%c0, 0, v%c0" ::"i"(y + k));
  else if constexpr (k < 8) asm volatile("v_add_f32 v%c0, v%c0, v%c1" ::"i"(y + k - 4), "i"(y + 28 + k));
  else if constexpr (k < 10) asm volatile("v_cvt_pk_bf16_f32 v%c0, v%c1, v%c2" ::"i"(y + k - 8), "i"(y + 2 * k - 16), "i"(y + 2 * k - 15));
  else asm volatile("buffer_store_dwordx2 v[%c0:%c1], %2, s[88:91], s101 offen sc1\n\ts_add_u32 s101, s101, 0x8000" ::"i"(y), "i"(y + 1), "v"(c.vz) : "memory");
}
constexpr int cv_z0(int Q) {
  int o = 0;
  for (int q = 0; q < Q; ++q)
    for (int k = 0; k < (q % 4 == 1 ? 0 : 3); ++k) { const int J = o / 11; if ((J + 1) * (J + 2) / 2 - 1 + (J ? 4 : 7) <= q) ++o; }
  return o;
}
constexpr int cv_xd0(int T) { int o = 0; for (int t = 0; t < T; ++t) o += t % 4 != 1 || (t >= 72 && t < 108); return o < 96 ? o : 96; }
static_assert(cv_xd0(144) == 96);
template <int B, int... Is> __device__ __forceinline__ void cv_zs(CV& c, iseq<Is...>) { (cv_z<B + Is>(c), ...); }
template <int R, int Q> __device__ __forceinline__ void cv_slot(CV& c) {
  constexpr int i = cv_i(R == 7, Q), J = cv_J(R == 7, Q), d = 128 + 4 * J, a = 32 + 32 * (R & 1) + 4 * i, b = 192 + 32 * (R & 1) + 4 * (J - i);
  constexpr int T = 36 * R + Q, vm = Q % 4 == 1 && Q < 32 ? Q / 4 : -1;
  if constexpr (Q == 0) asm volatile("s_waitcnt lgkmcnt(0)");
  if constexpr (R >= 6 && vm >= 0 && vm % 4 == 0) asm volatile("s_add_u32 m0, s79, %0" ::"i"(vm * 1024));
  if constexpr (R == 0 && i == 0)
    asm volatile("v_mfma_f32_16x16x32_bf16 v[%c0:%c1], a[%c2:%c3], a[%c4:%c5], 0" ::"i"(d), "i"(d + 3), "i"(a), "i"(a + 3), "i"(b), "i"(b + 3));
  else
    asm volatile("v_mfma_f32_16x16x32_bf16 v[%c0:%c1], a[%c2:%c3], a[%c4:%c5], v[%c0:%c1]" ::"i"(d), "i"(d + 3), "i"(a), "i"(a + 3), "i"(b), "i"(b + 3));
  if constexpr (Q < 24 && R < 7) cv_rd<R + 1, Q>(c);
  if constexpr (T % 7 == 3 && NS_X + 5 + T / 7 < 32) d_store<NS_X + 5 + T / 7>();
  if constexpr (R == 2 && Q >= 24 && Q < 32)
    asm volatile("ds_read_b64 v[%c1:%c2], %0 offset:%c3" ::"v"(c.px), "i"(96 + 2 * (Q - 24)), "i"(97 + 2 * (Q - 24)), "i"(512 * (31 - Q)));
  if constexpr (R >= 3 && R < 7) if constexpr (cv_xd0(T - 107) > cv_xd0(T - 108)) cv_xd<cv_xd0(T - 108)>(c);
  if constexpr ((R == 3 || R == 4) && vm >= 0) {
    constexpr int f = (R - 3) * 8 + vm;
    asm volatile("buffer_load_dwordx4 a[%c0:%c1], v84, s[88:91], s96 offen offset:%2 sc1" ::"i"(128 + 4 * f), "i"(131 + 4 * f), "i"((f & 3) * 1024) : "memory");
    if constexpr ((f & 3) == 3) asm volatile("s_addk_i32 s96, %0" ::"i"(f == 7 ? 28672 : 4096));
  }
  if constexpr (R == 4 && Q == 35) asm volatile(FL_POLL ::: "memory");
  if constexpr (R >= 6 && vm >= 0)
    asm volatile("buffer_load_dwordx4 v%c0, s[88:91], s98 offen offset:%1 sc1 lds" ::"i"(86 + vm / 4), "i"((vm & 3) * 1024) : "memory");
  if constexpr (R == 6 && Q == 35) asm volatile("s_add_u32 s98, s98, 0x8000\n\ts_xor_b32 s79, s79, 0x8000");
  if constexpr (R == 7) cv_zs<cv_z0(Q)>(c, make_iseq<cv_z0(Q + 1) - cv_z0(Q)>{});
  __builtin_amdgcn_sched_barrier(0);
}
template <int R, int... Q> __device__ __forceinline__ void cv_step(CV& c, iseq<Q...>) { (cv_slot<R, Q>(c), ...); }
template <int... N> __device__ __forceinline__ void cv_rd0(CV& c, iseq<N...>) { (cv_rd<0, N>(c), ...); }
template <int FLAG> __device__ __forceinline__ void conv_wave(unsigned sz, int ck, float Dd, char* lw) {
  int lane = threadIdx.x & 63; asm volatile("" : "+v"(lane));
  const int l16 = lane & 15, fq = lane >> 4;
  u32x4c w[5]; unsigned w4[5];
#pragma unroll
  for (int r = 0; r < 5; ++r) {
    w[r] = *(const u32x4c*)(lw + r * 1024 + lane * 16);
    w4[r] = *(const unsigned*)(lw + r * 1024 + lane * 16 + 16);
  }
  d_store<NS_X>(); d_store<NS_X + 1>();
#pragma unroll
  for (int r = 0; r < 5; ++r)
    *(u32x4c*)(lw + 10304 + r * 1024 + lane * 16) = (u32x4c){__builtin_amdgcn_alignbit(w[r][1], w[r][0], 16), __builtin_amdgcn_alignbit(w[r][2], w[r][1], 16),
                                                             __builtin_amdgcn_alignbit(w[r][3], w[r][2], 16), __builtin_amdgcn_alignbit(w4[r], w[r][3], 16)};
  d_store<NS_X + 2>();
  CV c;
#pragma unroll
  for (int i = 0; i < 4; ++i) c.pa[i] = (unsigned)(size_t)lw + ((l16 + 1) & 1) * 10304 + 4 * ((8 * fq + l16 + 1) >> 1) + 1024 * i;
  c.pb = (unsigned)(size_t)lw + 5120 + (2032 - 16 * l16 + 8 * fq) * 2 - 512 * 7;
  c.vz = (l16 >> 1) * 4096 + (((l16 & 1) * 2 + (fq >> 1)) ^ (ck >> 1)) * 16 + (fq & 1) * 8;
  c.px = (unsigned)(size_t)lw + 5120 + 2044 * 2 - 512 * 7 - (16 * l16 + 4 * fq) * 2;
  c.sz = sz; c.D = Dd;
  asm volatile("" : "+v"(c.vz), "+v"(c.px), "+v"(c.D) :: "memory");
  asm volatile("" : "+s"(c.sz));
  cv_rd0(c, make_iseq<24>{});
  d_store<NS_X + 3>(); d_store<NS_X + 4>();
  __builtin_amdgcn_sched_barrier(0);
  static_for([&](auto R) __attribute__((always_inline)) {
    cv_step<R>(c, make_iseq<36>{});
    if constexpr (R == 2) {
      asm volatile("s_waitcnt vmcnt(0)\n\ts_barrier" ::: "memory");
      fl_set<FLAG>();
      sset<101>(c.sz);
    }
    if constexpr (R == 5) {
      asm volatile("s_waitcnt vmcnt(0)" ::: "memory");
      fl_check();
    }
    __builtin_amdgcn_sched_barrier(0);
  }, make_iseq<8>{});
  asm volatile("s_nop 7");
  cv_zs<cv_z0(36)>(c, make_iseq<88 - cv_z0(36)>{});
  __builtin_amdgcn_sched_barrier(0);
}

__device__ __forceinline__ void post_dma(int cg, int tb, const bf16* __restrict__ q, unsigned m0w) {
  int lane = threadIdx.x & 63; asm volatile("" : "+v"(lane));
  const __amdgpu_buffer_rsrc_t sq = __builtin_amdgcn_make_buffer_rsrc((void*)(q + (size_t)tb * 32 * DA + cg * 64), (short)0, 32 * DA * 2, 0x00020000);
  const unsigned vq = ((lane >> 3) * DA + (lane & 7) * 8) * 2;
  asm volatile("s_mov_b32 m0, %0\n\ts_nop 4\n\tbuffer_load_dwordx4 %1, %2, 0 offen lds\n\tbuffer_load_dwordx4 %1, %2, %3 offen offset:1024 lds\n\t"
               "buffer_load_dwordx4 %1, %2, %4 offen offset:2048 lds\n\tbuffer_load_dwordx4 %1, %2, %5 offen offset:3072 lds"
               ::"s"(m0w + 4096), "v"(vq), "s"(sq), "s"(8 * DA * 2 - 1024), "s"(16 * DA * 2 - 2048), "s"(24 * DA * 2 - 3072) : "memory");
}
__device__ __forceinline__ void post_math(int cg, int tb, bf16* __restrict__ out, const char* lw) {
  int lane = threadIdx.x & 63; asm volatile("" : "+v"(lane));
  const int tl = lane >> 3, ck = lane & 7;
  const __amdgpu_buffer_rsrc_t so = __builtin_amdgcn_make_buffer_rsrc((void*)(out + (size_t)tb * 32 * OUTW + cg * 64), (short)0, 32 * OUTW * 2 - 2 * DA, 0x00020000);
  const unsigned vo = (tl * OUTW + ck * 8) * 2;
#pragma unroll
  for (int i = 0; i < 4; ++i) {
    const u32x4c qv = *(const u32x4c*)(lw + 4096 + i * 1024 + lane * 16);
    const char* lz = lw + ck * 64 + 16 * (i ^ (ck >> 1)) + tl * 2;
    u32x4c o;
#pragma unroll
    for (int e = 0; e < 4; ++e) {
      const float z0 = bf2f(*(const unsigned short*)(lz + 2 * e * 512));
      const float z1 = bf2f(*(const unsigned short*)(lz + (2 * e + 1) * 512));
      o[e] = pk2(bfl(qv[e]) * z0, bfh(qv[e]) * z1);
    }
    __builtin_amdgcn_raw_buffer_store_b128(o, so, vo + i * 8 * OUTW * 2, 0, 17);
  }
}

#define DI __device__ __forceinline__
#define GAP() __builtin_amdgcn_sched_barrier(0)
typedef __attribute__((ext_vector_type(4))) float f32x4g;
typedef __attribute__((ext_vector_type(4))) unsigned u32x4g;
constexpr int LDW = NH * HD;
struct GM { unsigned aA[2], aB[2][8]; };
template <bool W, int J, int V> DI void g_dma() {
  if constexpr (J == 0) asm volatile("buffer_load_dwordx4 v%c0, s[%c1:%c2], 0 offen lds" ::"i"(V), "i"(W ? 60 : 56), "i"(W ? 63 : 59) : "memory");
  else asm volatile("buffer_load_dwordx4 v%c0, s[%c1:%c2], s%c3 offen lds" ::"i"(V), "i"(W ? 60 : 56), "i"(W ? 63 : 59), "i"((W ? 70 : 63) + J) : "memory");
}
template <int P, int KS, int I> DI void g_wread(GM& g) {
  constexpr int nf = I >> 1, hf = I & 1, r = 192 - 64 * P + 8 * nf + 4 * KS + 2 * hf;
  asm volatile("ds_read_b64_tr_b16 a[%c1:%c2], %0 offset:%c3" ::"v"(g.aB[hf][nf]), "i"(r), "i"(r + 1), "i"(P * 32768 + KS * 16384));
}
template <int P, int M, int KS> DI void g_aread(GM& g) {
  constexpr int r = 96 + 8 * (M & 1) + 4 * KS;
  asm volatile("ds_read_b128 v[%c1:%c2], %0 offset:%c3" ::"v"(g.aA[KS]), "i"(r), "i"(r + 3), "i"(P * 32768 + M * 2048));
}
constexpr float CS = 0.0625f * 1.4426950408889634f;
constexpr int NS_Q = 19;
constexpr int e_vm(int G) { return (G * 3) % 8 < 3 ? G * 3 / 8 : -1; }
constexpr int e_st(int MODE, int G) {
  const int q = G & 15, k3 = ((G >> 4) - 1) * 3 + (q == 0 ? 0 : q == 4 ? 1 : 2), k2 = NS_Q + (G >> 4) * 2 + (q == 4);
  if (MODE == 5) return e_vm(G) >= 32 ? e_vm(G) - 32 : -1;
  if (MODE == 3) return G >= 33 && (G - 33) % 5 == 0 && (G - 33) / 5 < NS_X ? (G - 33) / 5 : -1;
  if (MODE == 2) return G >= 30 && (q == 0 || q == 4 || q == 14) && k3 - 2 < NS_Q ? k3 - 2 : -1;
  if (MODE == 4) return (q == 0 || q == 4) && k2 < 32 ? k2 : -1;
  return -1;
}
constexpr int e_nst(int MODE, int G0, int G1) { int n = 0; for (int G = G0; G < G1; ++G) n += e_st(MODE, G) >= 0; return n; }
static_assert(e_nst(2, 0, 128) == NS_Q && e_nst(3, 30, 128) == NS_X && e_nst(5, 0, 128) == 16 && e_nst(4, 0, 104) == 32 - NS_Q);
constexpr int e_slot(int MODE, int G) { return MODE == 3 ? (G % 3 == 0 && G < 30 ? G / 3 : -1) : e_vm(G) >= 0 && e_vm(G) < 32 ? 10 + e_vm(G) : -1; }
struct ES { int op0[129], nop[129]; };
constexpr ES e_sched(bool TR, int MODE) {
  ES z{};
  const bool SCL = MODE == 2 || MODE == 4;
  const int per = SCL ? 16 : 8, o1 = 32 * per;
  int o = MODE == 4 ? e_sched(false, 2).op0[128] : 0;
  for (int G = 0; G <= 128; ++G) {
    const int q = G & 15;
    int cap = 2 - (q == 1 || q == 2 || q == 15);
    if (SCL) cap -= (q >= 3 && q < 11 && (q & 1)) + (q == 7 || q == 11) + 2 * (G == 70 || G == 74 || (G & 63) == 61);
    cap += MODE == 2 ? 4 + (q == 10 || q == 13) : MODE == 3 ? 1 : MODE == 5 ? 2 : 0;
    if (e_st(MODE, G) >= 0 || (SCL ? q == 8 || q == 12 : e_slot(MODE, G) >= 0) || (MODE == 4 && G < 4)) cap = 0;
    if (G == 128) cap = 10000;
    z.op0[G] = o;
    while (o < o1) {
      const int i = o / per, rdy = MODE == 4 ? 0 : (TR ? 32 * (i >> 3) + 16 + 2 * (i & 7) + 1 : 4 * i + 3) + 6;
      if ((G < rdy && G < 128) || cap-- <= 0) break;
      ++o; ++z.nop[G];
    }
  }
  return z;
}
template <bool TR, int MODE> constexpr ES ESCH = e_sched(TR, MODE);
static_assert(ESCH<false, 4>.op0[96] == 512 && ESCH<false, 4>.op0[0] >= 24 * 16);
template <bool TR, bool SCL, int O> DI void e_op() {
  constexpr int per = SCL ? 16 : 8, i = O / per, k = O % per, r = 128 + 4 * i, t = 114 + 4 * (i & 1), c = k - (per - 4);
  if constexpr (k < 4) asm volatile("v_accvgpr_read_b32 v%c0, a[%c1]" ::"i"(t + k), "i"(4 * i + k));
  else if constexpr (c < 0) asm volatile("v_mul_f32 v%c0, s83, v%c0" ::"i"(k < 8 ? r + (k ^ 6) : t + k - 8));
  else {
    constexpr int d = c == 0 ? r + 3 : c == 1 ? r + 2 : r + c - 2, x = c == 0 ? r + 2 : c == 1 ? r : t + 2 * (c - 2);
    asm volatile("v_cvt_pk_bf16_f32 v%c0, v%c1, v%c2" ::"i"(d), "i"(x), "i"(x + 1));
  }
}
template <bool TR, bool SCL, int B, int... Is> DI void e_ops(iseq<Is...>) { (e_op<TR, SCL, B + Is>(), ...); }
template <int S> DI void e_m0() {
  if constexpr (S == 0 || S == 1 || S == 5 || S == 9) asm volatile("s_add_u32 m0, s78, %0" ::"i"(S * 1024));
  if constexpr (S >= 26 && (S - 26) % 4 == 0) asm volatile("s_add_u32 m0, s79, %0" ::"i"(((S - 26) >> 3) * 32768 + ((S - 26) & 4) * 1024));
}
template <int S> DI void e_load() {
  if constexpr (S == 0) asm volatile("buffer_load_dwordx4 v122, s[56:59], 0 offen lds" ::: "memory");
  else if constexpr (S < 5) asm volatile("buffer_load_dwordx4 v84, s[56:59], s64 offen offset:%0 lds" ::"i"((S - 1) * 1024) : "memory");
  else if constexpr (S < 9) asm volatile("buffer_load_dwordx4 v84, s[60:63], 0 offen offset:%0 sc1 lds" ::"i"((S - 5) * 1024) : "memory");
  else if constexpr (S == 9) asm volatile("buffer_load_dwordx4 v84, s[60:63], s62 offen sc1 lds" ::: "memory");
  else if constexpr (S < 26) {
    constexpr int f = S - 10;
    asm volatile("buffer_load_dwordx4 a[%c0:%c1], v84, s[88:91], s96 offen offset:%2 sc1" ::"i"(128 + 4 * f), "i"(131 + 4 * f), "i"((f & 3) * 1024) : "memory");
    if constexpr ((f & 3) == 3) asm volatile("s_addk_i32 s96, %0" ::"i"(f == 7 ? 28672 : 4096));
  } else {
    constexpr int f = S - 26;
    asm volatile("buffer_load_dwordx4 v%c0, s[88:91], s98 offen offset:%1 sc1 lds" ::"i"(86 + ((f >> 2) & 1)), "i"((f & 3) * 1024) : "memory");
    if constexpr (f == 7) asm volatile("s_add_u32 s98, s98, s97");
    if constexpr (f == 15) asm volatile(FL_POLL ::: "memory");
  }
}
template <bool TR, int P, int H, int G, int MODE> DI void g_gap(GM& g) {
  constexpr int m = 4 * H + (G >> 4), q = G & 15, n = q >> 1, ks = q & 1, GG = 64 * H + G;
  constexpr bool inV = TR ? m & 1 : n & 1, END = MODE == 3 || MODE == 5;
  constexpr int a = 96 + 8 * (m & 1) + 4 * ks, b = 192 - 64 * P + 8 * n + 4 * ks, d = (inV ? 128 : 0) + 4 * (TR ? 8 * (m >> 1) + n : 4 * m + (n >> 1));
  constexpr int J = END ? -1 : q == 8 ? 2 * (G >> 4) : q == 12 ? 2 * (G >> 4) + 1 : -1, gj = (J & 3) ^ ((J & 3) >> 1);
  if constexpr (END) { if constexpr (q == 0 && GG > 0) asm volatile("s_waitcnt lgkmcnt(0)"); e_m0<e_slot(MODE, GG)>(); }
  else if constexpr (q == 0 && G > 0) asm volatile("s_waitcnt lgkmcnt(4)");
  if constexpr (J == 0) asm volatile("s_add_u32 m0, s78, %0" ::"i"((P ^ H ^ 1) * 32768 + (1 - H) * 8192));
  else if constexpr (J == 4) asm volatile("s_add_u32 m0, s79, %0" ::"i"((P ^ H) * 32768 + (1 - H) * 16384));
  else if constexpr (J > 0 && (J & 3) == 1) asm volatile("s_bitset1_b32 m0, 12");
  else if constexpr (J > 0 && (J & 3) == 2) asm volatile("s_bitset1_b32 m0, %0" ::"i"(J < 4 ? 14 : 13));
  else if constexpr (J > 0) asm volatile("s_bitset0_b32 m0, 12");
#define GMF(D, X, Y, C) asm volatile("v_mfma_f32_16x16x32_bf16 " D "[%c0:%c1], " X ", " Y ", " C ::"i"(d), "i"(d + 3), "i"(a), "i"(a + 3), "i"(b), "i"(b + 3))
#define GMF2(D, C) do { if constexpr (TR) GMF(D, "v[%c2:%c3]", "a[%c4:%c5]", C); else GMF(D, "a[%c4:%c5]", "v[%c2:%c3]", C); } while (0)
  if constexpr ((MODE == 1 || MODE == 4) && ks == 0) { if constexpr (inV) GMF2("v", "0"); else GMF2("a", "0"); }
  else if constexpr (inV) GMF2("v", "v[%c0:%c1]");
  else GMF2("a", "a[%c0:%c1]");
#undef GMF2
#undef GMF
  if constexpr ((q == 1 || q == 2) && !(END && m == 7)) g_aread<(m < 7 ? P : P ^ 1), (m + 1) & 7, q - 1>(g);
  if constexpr (!END && q >= 3 && q < 11 && (q & 1)) g_wread<P ^ 1, H, (G >> 4) * 4 + (q - 3) / 2>(g);
  if constexpr (MODE == 1 && GG < 7) {
    if constexpr (GG == 0) asm volatile("s_barrier" ::: "memory");
    if constexpr (GG == 2) asm volatile("s_add_u32 m0, s79, %0" ::"i"(P * 32768));
    if constexpr (GG >= 3) g_dma<true, (GG - 3) ^ ((GG - 3) >> 1), 91>();
    if constexpr (GG == 3 || GG == 4) asm volatile("s_bitset1_b32 m0, %0" ::"i"(9 + GG));
    if constexpr (GG == 5) asm volatile("s_bitset0_b32 m0, 12");
  }
  if constexpr (e_st(MODE, GG) >= 0) {
    static_assert(ESCH<TR, MODE>.op0[GG] >= (e_st(MODE, GG) + 1) * (END ? 8 : 16));
    d_store<e_st(MODE, GG)>();
  }
  if constexpr (J >= 0 && J < 4) g_dma<false, (1 - H) * 2 + (gj & 1) + (gj >> 1) * 4, 90>();
  else if constexpr (J >= 4) g_dma<true, (1 - H) * 4 + gj, 91>();
  if constexpr (MODE >= 2) e_ops<TR, !END, ESCH<TR, MODE>.op0[GG]>(make_iseq<ESCH<TR, MODE>.nop[GG]>{});
  if constexpr (END) {
    if constexpr (e_slot(MODE, GG) >= 0) e_load<e_slot(MODE, GG)>();
  } else {
    if constexpr (H == 1 && G == 6) asm volatile("v_lshl_add_u32 v90, s82, 7, v92\n\ts_and_b32 s82, s80, 15");
    if constexpr (H == 1 && G == 10) asm volatile("s_lshr_b64 s[80:81], s[80:81], 4\n\tv_lshl_add_u32 v91, s82, 19, v93");
    if constexpr (G == 61) {
      constexpr int nst = e_nst(MODE, H ? 29 : 0, GG + 1) + (MODE == 4 && !H ? e_nst(2, 93, 128) : 0);
      asm volatile("s_waitcnt vmcnt(%0) lgkmcnt(0)\n\ts_barrier" ::"i"(12 + nst) : "memory");
    }
  }
  if constexpr (END && GG == 127) {
    asm volatile("s_nop 7");
    e_ops<TR, false, ESCH<TR, MODE>.op0[128]>(make_iseq<ESCH<TR, MODE>.nop[128]>{});
  }
  GAP();
}
template <bool TR, int P, int H, int MODE, int... Gs> DI void g_half_(GM& g, iseq<Gs...>) { (g_gap<TR, P, H, Gs, MODE>(g), ...); }
template <bool TR, int P, int H, int MODE> DI void g_half(GM& g) { g_half_<TR, P, H, MODE>(g, make_iseq<64>{}); }
template <bool TR, int P, int MODE> DI void g_tile(GM& g) { g_half<TR, P, 0, MODE>(g); g_half<TR, P, 1, MODE>(g); }
template <int P, int... Is> DI void g_read0(GM& g, iseq<Is...>) {
  (g_wread<P, 0, Is>(g), ...); (g_wread<P, 1, Is>(g), ...);
  g_aread<P, 0, 0>(g); g_aread<P, 0, 1>(g);
}
DI void g_proW(unsigned g, unsigned mW) {
  asm volatile("v_lshl_add_u32 v91, %0, 18, v93\n\ts_mov_b32 m0, %1" ::"s"(g), "s"(mW));
  asm volatile("s_nop 0"); g_dma<true, 0, 91>();
  asm volatile("s_bitset1_b32 m0, 12\n\ts_nop 0"); g_dma<true, 1, 91>();
  asm volatile("s_bitset1_b32 m0, 13\n\ts_nop 0"); g_dma<true, 3, 91>();
  asm volatile("s_bitset0_b32 m0, 12\n\ts_nop 0"); g_dma<true, 2, 91>();
}
template <int PART = 3> DI void g_pro(unsigned i, unsigned mA, unsigned mW, unsigned lda) {
  i &= 3;
  if constexpr (PART & 1)
    asm volatile("v_add_u32 v90, %0, v92\n\tv_add_u32 v91, %1, v93\n\ts_mov_b32 m0, %2\n\ts_mov_b32 s54, %3" ::"s"(((i & 1) + (i >> 1) * 4) * 64 * lda),
                 "s"((i & 1) * 0x20000 + (i >> 1) * 256), "s"(mA + (i & 1) * 4096 + (i >> 1) * 16384), "s"(mW + i * 4096));
  if constexpr (PART == 1) return;
  g_dma<false, 0, 90>();
  asm volatile("s_bitset1_b32 m0, 13\n\ts_nop 0"); g_dma<false, 2, 90>();
  asm volatile("s_mov_b32 m0, s54\n\ts_nop 0"); g_dma<true, 0, 91>();
  asm volatile("s_bitset1_b32 m0, 14\n\ts_nop 0"); g_dma<true, 4, 91>();
  asm volatile("s_xor_b32 m0, s54, 0x8000\n\ts_nop 0\n\tbuffer_load_dwordx4 v91, s[60:63], s53 offen lds" ::: "memory", "scc");
}
DI void g_pro2(unsigned mA, unsigned mW) {
  asm volatile("v_add_u32 v90, 128, v92\n\ts_mov_b32 m0, %0" ::"s"(mA ^ 32768));
  asm volatile("s_nop 0"); g_dma<false, 0, 90>();
  asm volatile("s_bitset1_b32 m0, 12\n\ts_nop 0"); g_dma<false, 1, 90>();
  asm volatile("s_bitset1_b32 m0, 14\n\ts_nop 0"); g_dma<false, 5, 90>();
  asm volatile("s_bitset0_b32 m0, 12\n\ts_nop 0"); g_dma<false, 4, 90>();
  g_proW(3, mW ^ 49152);
}
template <bool TR, bool SCL, class H0, class H1> DI void g_loop(GM& g, H0&& early, H1&& late) {
  constexpr int nkt = SCL ? 7 : 16, P0 = SCL ? 0 : 1;
#pragma unroll 1
  for (int t = 1;; t += 2) {
    asm volatile("" : "+s"(t));
    g_half<TR, P0 ^ 1, 0, 0>(g);
    if (__builtin_expect(t == (SCL ? 5 : 13), 0)) { late(); GAP(); }
    g_half<TR, P0 ^ 1, 1, 0>(g);
    if (SCL && t + 2 >= nkt) break;
    g_half<TR, P0, 0, 0>(g);
    if (__builtin_expect(t == (SCL ? 3 : 1), 0)) { early(); GAP(); }
    if constexpr (!SCL) asm volatile("s_mov_b32 s78, s83");
    g_half<TR, P0, 1, 0>(g);
    if (!SCL && t + 3 >= nkt) break;
  }
}

typedef __attribute__((ext_vector_type(4))) unsigned u32x4;
typedef __attribute__((ext_vector_type(4))) float f32x4;

constexpr int R_S = 128, R_P = 192, R_CI = 224, R_E = 96;
constexpr int R_TS = 112, R_RS = 114, R_M = 116;
constexpr int R_PD = 118, R_THR = 120, R_TMP = 121;
constexpr int r_dk(int q) { return q < 6 ? 90 + q : 120 + q; }
constexpr int S_V = 96, S_T = 97, S_K = 98, S_I = 99, S_EV = 100;
struct AC {
  unsigned rare;
  unsigned tb, vs, o0, o1;
};
DI bool redo() { u64 t; asm volatile("s_mov_b64 %0, s[54:55]" : "=s"(t)); return t != 0; }
template <int X, int F> DI void kread() {
  constexpr int r = 192 + 4 * (F & 15);
  asm volatile("ds_read_b128 a[%c0:%c1], v84 offset:%c2" ::"i"(r), "i"(r + 3), "i"(X * 32768 + F * 1024));
}
template <int X, int F> DI void vread() {
  constexpr int r = 192 + 4 * (F & 15);
  asm volatile("ds_read_b128 a[%c0:%c1], v85 offset:%c2" ::"i"(r), "i"(r + 3), "i"(X * 32768 + (F & 1) * 16384 + (F >> 1) * 1024));
}
template <int X, int... Fs> DI void kreads(iseq<Fs...>) { (kread<X, Fs>(), ...); }
template <int X, int J> DI void dma_m0() {
  if constexpr (J == 0) asm volatile("s_mov_b32 m0, s%c0" ::"i"(78 + X));
  else if constexpr (J == 4) asm volatile("s_or_b32 m0, s%c0, 0x1000" ::"i"(78 + X));
  else if constexpr (J == 8) asm volatile("s_bitset0_b32 m0, 16");
  else if constexpr (J == 12) asm volatile("s_bitset0_b32 m0, 12");
}
template <int J> DI void dma_at() {
  if constexpr (J < 8)
    asm volatile("buffer_load_dwordx4 v%c0, s[88:91], s96 offen offset:%1 sc1 lds" ::"i"(86 + (J >= 4)), "i"((J & 3) * 1024) : "memory");
  else
    asm volatile("buffer_load_dwordx4 v%c0, s[92:95], s98 offen offset:%1 sc1 lds" ::"i"(r_dk(J - 8)), "i"((J & 3) * 1024) : "memory");
}
template <int Q> DI void dma4k() {
  asm volatile("s_xor_b32 m0, s79, %0\n\ts_add_u32 s48, s98, %1\n\tbuffer_load_dwordx4 v%c2, s[88:91], s48 offen sc1 lds\n\t"
               "buffer_load_dwordx4 v%c2, s[88:91], s48 offen offset:1024 sc1 lds\n\tbuffer_load_dwordx4 v%c2, s[88:91], s48 offen offset:2048 sc1 lds\n\t"
               "buffer_load_dwordx4 v%c2, s[88:91], s48 offen offset:3072 sc1 lds"
               ::"i"((Q >> 1) * 32768 + (Q & 1) * 4096), "i"((Q >> 1) * 32768), "i"(86 + (Q & 1)) : "memory", "scc");
}
DI void acc_zero() { static_for([](auto I) { asm volatile("v_accvgpr_write_b32 a[%c0], 0" ::"i"((int)I)); }, make_iseq<128>{}); }
#define SMV 0
struct SMS { int ex[128], op0[128], nop[128], last; };
constexpr SMS sm_sched() {
  SMS z{};
  int et[32] = {}, e = 0;
  for (int t = 0; t < 128; ++t) {
    const int g = t & 15;
    const bool x = t < 48 ? (g >= 8 && (g & 1) && g < 15) : t < 64 ? (g >= 9 && g < 15) : (g >= 8 && g < 14);
    z.ex[t] = x && e < 32 ? e : -1;
    if (z.ex[t] >= 0) et[e++] = t;
  }
  int o = 0;
  for (int t = 0; t < 128; ++t) {
    const int g = t & 15;
    int cap = z.ex[t] >= 0 || t < 8 || (t < 48 && (g == 7 || (g >= 8 && !(g & 1)))) || t == 56 || t == 63 || t == 111 ? 0 : 1;
    z.op0[t] = o; z.nop[t] = 0;
    while (cap > 0 && o < 48 && et[2 * (o / 3) + (o % 3 == 0 ? 0 : 1)] + 2 <= t) {
      const int el = 2 * (o / 3) + o % 3;
      if (!(o % 3 < 2 && (el == 0 || el == 4))) --cap;
      ++o; ++z.nop[t]; z.last = t;
    }
  }
  for (int k = 16; k < 32; ++k) if (z.op0[et[k]] < 3 * ((k - 16) / 2 + 1)) z.last = 1000;
  return z;
}
constexpr SMS SM = sm_sched();
static_assert(SM.last <= 117);
constexpr int p_reg(int Y, int k) { return R_P + 16 * Y + 4 * ((k >> 3) * 2 + ((k >> 1) & 1)) + ((k >> 2) & 1) * 2 + (k & 1); }
template <int Y, int O> DI void sm_op() {
  constexpr int k = O / 3, e = 2 * k + O % 3, ts = R_TS + ((e >> 2) & 1);
  if constexpr (O % 3 == 2) asm volatile("v_cvt_pk_bf16_f32 v%c0, v%c1, v%c2" ::"i"(p_reg(Y, k)), "i"(R_E + ((2 * k) & 15)), "i"(R_E + ((2 * k + 1) & 15)));
  else if constexpr (e == 1 || e == 5) asm volatile("v_add_f32_e32 v%c0, v%c1, v%c2" ::"i"(ts), "i"(R_E + e - 1), "i"(R_E + e));
  else if constexpr (e != 0 && e != 4) asm volatile("v_add_f32_e32 v%c0, v%c0, v%c1" ::"i"(ts), "i"(R_E + (e & 15)));
}
template <int Y, int T> DI void sm_gap(AC& c) {
  if constexpr (SM.ex[T] >= 0) asm volatile("v_exp_f32 v%c0, v%c1" ::"i"(R_E + (SM.ex[T] & 15)), "i"(R_S + 32 * Y + SM.ex[T]));
  if constexpr (SM.nop[T] > 0) sm_op<Y, SM.op0[T]>();
  if constexpr (SM.nop[T] > 1) sm_op<Y, SM.op0[T] + 1>();
  if constexpr (SM.nop[T] > 2) sm_op<Y, SM.op0[T] + 2>();
  if constexpr (T == SM.last + 1) asm volatile("v_add_f32_e32 v%c0, v%c1, v%c2" ::"i"(R_TMP), "i"(R_TS), "i"(R_TS + 1));
  if constexpr (T == SM.last + 2) asm volatile("v_cmp_nle_f32_e64 s[54:55], v%c0, v%c1" ::"i"(R_TMP), "i"(R_THR));
}
template <int X, int MODE, int G> DI void gapA(AC& c) {
  constexpr bool FIRST = MODE == 1 || MODE == 2, NOQK = MODE == 3;
  constexpr int Xp = X ^ 1, kb = G >> 4, g = G & 15, qb = g >> 3, s = g & 7;
  if constexpr (MODE == 3 && G < 48 && G % 3 == 0) {
    constexpr int f = G / 3;
    asm volatile("buffer_load_dwordx4 a[%c0:%c1], v84, s[88:91], s75 offen offset:%2 sc1" ::"i"(128 + 4 * f), "i"(131 + 4 * f), "i"((f & 3) * 1024) : "memory");
    if constexpr ((f & 3) == 3) asm volatile("s_addk_i32 s75, %0" ::"i"(f == 7 ? 28672 : 4096));
  }
  if constexpr (G == 48) asm volatile("s_waitcnt vmcnt(%0) lgkmcnt(0)\n\ts_barrier" ::"i"(MODE == 3 ? 16 : MODE == 2 ? 12 : MODE == 1 ? 7 : 0) : "memory");
  else if constexpr (g == 0 && !NOQK) asm volatile("s_waitcnt lgkmcnt(0)");
  if constexpr (!NOQK) {
    constexpr int r = 192 + 4 * ((kb & 1) * 8 + s), q = 128 + 4 * g;
    constexpr int d = R_S + 32 * X + 4 * (kb * 2 + qb), ci = s == 0 ? R_CI + 4 * (kb * 2 + qb) : d;
    if constexpr (FIRST && s == 0)
      asm volatile("v_mfma_f32_16x16x32_bf16 v[%c0:%c1], a[%c2:%c3], a[%c4:%c5], 0" ::"i"(d), "i"(d + 3), "i"(r), "i"(r + 3), "i"(q), "i"(q + 3));
    else
      asm volatile("v_mfma_f32_16x16x32_bf16 v[%c0:%c1], a[%c2:%c3], a[%c4:%c5], v[%c6:%c7]" ::"i"(d), "i"(d + 3), "i"(r), "i"(r + 3), "i"(q), "i"(q + 3),
                   "i"(ci), "i"(ci + 3));
    if constexpr (g < 8 && kb < 3) kread<X, (kb + 1) * 8 + g>();
    constexpr int S0 = MODE == 1 ? 0 : SMV ? 40 : 14, S1 = MODE == 1 ? 1 : SMV ? 41 : 30, S2 = SMV ? 42 : 45, S3 = SMV ? 43 : 46;
    if constexpr (G == S0) asm volatile("s_add_u32 s96, s97, 0x1000000");
    if constexpr (G == S1) dma_m0<X, 0>();
    if constexpr (G == S2) asm volatile("s_mov_b32 s97, s98");
    if constexpr (G == S3) asm volatile("s_add_u32 s98, s98, s99");
    if constexpr (MODE != 1 && G >= 56 && !(G & 1)) dma_at<(G - 56) / 2>();
    if constexpr (MODE == 1 && (G & 11) == 9) dma_at<(G >> 4) * 2 + ((G >> 2) & 1)>();
    if constexpr (MODE == 1 && G == 31) dma_m0<X, 4>();
    if constexpr (G == 57) asm volatile("s_lshr_b32 s100, s100, 1\n\ts_add_u32 s56, s56, 1");
    if constexpr (G == 59) asm volatile("s_getpc_b64 s[86:87]\nev_pc%c0:\n\ts_add_u32 s86, s86, ev_ret%c0-ev_pc%c0\n\ts_addc_u32 s87, s87, 0" ::"i"(X * 4 + MODE));
    if constexpr (G == 61 && !FIRST) asm volatile("s_or_b32 s57, s54, s55\n\ts_mov_b32 %0, s57\n\ts_bitcmp1_b32 s100, 0" : "=s"(c.rare));
    if constexpr (G == 61 && FIRST) asm volatile("s_bitcmp1_b32 s100, 0");
  } else if constexpr (SM.nop[64 + G] > 0) asm volatile("s_nop 0");
  if constexpr (!FIRST && g < 8 && kb == 3) vread<Xp, g>();
  if constexpr (MODE == 2 && (G & 3) == 0) d_store<16 + G / 4>();
  if constexpr (!FIRST) sm_gap<Xp, 64 + G>(c);
  if constexpr (G == 63 && !NOQK) asm volatile("s_cbranch_scc1 ev_entry\nev_ret%c0:" ::"i"(X * 4 + MODE) : "vcc", "memory");
  GAP();
}
template <int R> DI void rescale1(float alpha) {
  float t;
  asm volatile("v_accvgpr_read_b32 %0, a[%c2]\n\tv_mul_f32 %0, %0, %1\n\tv_accvgpr_write_b32 a[%c2], %0" : "=&v"(t) : "v"(alpha), "i"(R));
}
template <int... Rs> DI void rescale_all(const float (&alpha)[2], iseq<Rs...>) { (rescale1<Rs>(alpha[(Rs >> 2) & 1]), ...); }
template <bool MAX> DI float row_reduce(float v) {
  auto p = __builtin_amdgcn_permlane32_swap(__float_as_uint(v), __float_as_uint(v), false, false);
  v = MAX ? fmaxf(__uint_as_float(p[0]), __uint_as_float(p[1])) : __uint_as_float(p[0]) + __uint_as_float(p[1]);
  p = __builtin_amdgcn_permlane16_swap(__float_as_uint(v), __float_as_uint(v), false, false);
  return MAX ? fmaxf(__uint_as_float(p[0]), __uint_as_float(p[1])) : __uint_as_float(p[0]) + __uint_as_float(p[1]);
}
template <int Y, int QB> DI float row_max() {
  constexpr int r = R_S + 32 * Y + 4 * QB;
  float mx;
  asm volatile("v_max_f32 %0, v%c1, v%c2\n\tv_max3_f32 %0, %0, v%c3, v%c4" : "=&v"(mx) : "i"(r), "i"(r + 1), "i"(r + 2), "i"(r + 3));
  static_for([&](auto K) {
    constexpr int q = r + 8 * (K + 1);
    float t = mx;
    asm volatile("v_max3_f32 %0, %0, v%c1, v%c2\n\tv_max3_f32 %0, %0, v%c3, v%c4" : "+v"(t) : "i"(q), "i"(q + 1), "i"(q + 2), "i"(q + 3));
    mx = t;
  }, make_iseq<3>{});
  return row_reduce<true>(mx);
}
template <int R> DI void v_sub(float d) { asm volatile("v_sub_f32 v%c0, v%c0, %1" ::"i"(R), "v"(d)); }
template <int B> DI void s_shift(const float (&d)[2]) { static_for([&](auto E) { v_sub<B + E>(d[(E >> 2) & 1]); }, make_iseq<32>{}); }
template <bool CI> DI void m_shift(const float (&d)[2]) {
  if constexpr (CI) s_shift<R_CI>(d);
  v_sub<R_M>(-d[0]); v_sub<R_M + 1>(-d[1]);
}
#define MSK(R, K) "v_cmp_gt_i32 s[48:49], " #K ", v89\n\tv_cmp_gt_i32 s[50:51], " #K "+1, v89\n\tv_cmp_gt_i32 s[52:53], " #K "+2, v89\n\t" \
                  "v_cmp_gt_i32 s[54:55], " #K "+3, v89\n\tv_cndmask_b32_e64 v[" #R "], v[" #R "], v122, s[48:49]\n\t" \
                  "v_cndmask_b32_e64 v[" #R "+1], v[" #R "+1], v122, s[50:51]\n\tv_cndmask_b32_e64 v[" #R "+2], v[" #R "+2], v122, s[52:53]\n\t" \
                  "v_cndmask_b32_e64 v[" #R "+3], v[" #R "+3], v122, s[54:55]\n\t"
DI void set_mask0() { asm volatile("v_mov_b32 v122, 0xff800000\n\t" MSK(128, 0) MSK(136, 16) MSK(144, 32) MSK(152, 48)); }
template <int X, bool CI = true> DI void sm_redo() {
  constexpr int Xp = X ^ 1;
  float d[2], dt[2], alpha[2], ts[2] = {0.f, 0.f};
  static_for([&](auto Q) {
    constexpr int qb = Q;
    const float pd = vget<R_PD + qb>();
    d[qb] = fmaxf(row_max<Xp, qb>() - pd, 0.f);
    dt[qb] = d[qb] + pd;
    vset<R_PD + qb>(d[qb]);
    alpha[qb] = __builtin_amdgcn_exp2f(-d[qb]);
    vset<R_RS + qb>(vget<R_RS + qb>() * alpha[qb]);
  }, make_iseq<2>{});
  vset<R_THR>(d[0] > 0.f || d[1] > 0.f ? -1.f : 4096.f);
  m_shift<CI>(d);
  static_for([&](auto K) {
    constexpr int k = K, qb = (k >> 1) & 1;
    const float e0 = __builtin_amdgcn_exp2f(vget<R_S + 32 * Xp + 2 * k>() - dt[qb]), e1 = __builtin_amdgcn_exp2f(vget<R_S + 32 * Xp + 2 * k + 1>() - dt[qb]);
    ts[qb] += e0; ts[qb] += e1;
    vset<p_reg(Xp, k)>(__uint_as_float(pk2(e0, e1)));
  }, make_iseq<16>{});
  vset<R_TS>(ts[0]); vset<R_TS + 1>(ts[1]);
  rescale_all(alpha, make_iseq<128>{});
  asm volatile("s_nop 1" ::: "memory");
  GAP();
}
template <int X, int MODE, int... Gs> DI void phaseA_(AC& c, iseq<Gs...>) { (gapA<X, MODE, Gs>(c), ...); }
template <int X, int MODE> DI void phaseA(AC& c) { phaseA_<X, MODE>(c, make_iseq<64>{}); }

template <int X, bool SMX, int G, bool FIRST = false> DI void gapB(AC& c) {
  constexpr int Xp = X ^ 1, blk = G >> 4, g = G & 15;
  if constexpr (g == 0) asm volatile("s_waitcnt lgkmcnt(0)");
  constexpr int qb = (g >> 1) & 1, s = g & 1, r = 192 + 4 * ((blk & 1) * 8 + (g >> 2) * 2 + s), o = 4 * ((blk * 4 + (g >> 2)) * 2 + qb);
  constexpr int p = R_P + 16 * Xp + 4 * (s * 2 + qb);
  if constexpr (FIRST && s == 0)
    asm volatile("v_mfma_f32_16x16x32_bf16 a[%c0:%c1], a[%c2:%c3], v[%c4:%c5], 0" ::"i"(o), "i"(o + 3), "i"(r), "i"(r + 3), "i"(p), "i"(p + 3));
  else
  asm volatile("v_mfma_f32_16x16x32_bf16 a[%c0:%c1], a[%c2:%c3], v[%c4:%c5], a[%c0:%c1]" ::"i"(o), "i"(o + 3), "i"(r), "i"(r + 3), "i"(p), "i"(p + 3));
  if constexpr (g < 8) {
    if constexpr (blk < 3) vread<Xp, (blk + 1) * 8 + g>();
    else if constexpr (SMX) kread<Xp, g>();
  }
  if constexpr (SMX) {
    if constexpr (G == 1 || G == 2) asm volatile("v_add_f32_e32 v%c0, v%c0, v%c1" ::"i"(R_RS + G - 1), "i"(R_TS + G - 1));
    if constexpr (g == 7 && blk < 3) dma_m0<X, blk * 4 + 4>();
    if constexpr (g >= 8 && !(g & 1) && blk < 3) dma_at<blk * 4 + 4 + (g - 8) / 2>();
    if constexpr (G == 55) asm volatile("s_cmp_eq_u32 s56, s76");
    if constexpr (G == 3) asm volatile("s_lshr_b32 s77, s77, 1");
    if constexpr (G == 4) asm volatile("s_bitcmp1_b32 s77, 0");
    if constexpr (G == 5) asm volatile("s_cselect_b64 s[52:53], s[62:63], 0");
    if constexpr (G == 6) asm volatile(FL_ARM("s[52:53]"));
    if constexpr (G == 56) asm volatile(FL_POLL ::: "memory");
    sm_gap<X, G>(c);
  }
  GAP();
}
template <int X, bool FIRST, int... Gs> DI void phaseB_(AC& c, iseq<Gs...>) { (gapB<X, true, Gs, FIRST>(c), ...); }
template <int X, bool FIRST = false> DI void phaseB(AC& c) { phaseB_<X, FIRST>(c, make_iseq<64>{}); }
constexpr int sm_nex() { int n = 0; for (int t = 0; t < 64; ++t) n += SM.ex[t] >= 0; return n; }
template <int X> DI void phaseB0(unsigned mode, bool vh) {
  dma_m0<X, 4>();
  kreads<X ^ 1>(make_iseq<8>{});
  if (vh) { dma_at<4>(); dma_at<5>(); dma_at<6>(); dma_at<7>(); }
  dma_m0<X, 8>();
  asm volatile("s_nop 3");
  float d[2];
  if (mode & 2) {
    d[0] = vget<122>(); d[1] = vget<123>();
    dma_at<8>();
  } else {
    if constexpr (X == 0) if (__builtin_expect(mode & 4, 0)) set_mask0();
    d[0] = row_max<X, 0>(); GAP(); dma_at<8>();
    d[1] = row_max<X, 1>();
#pragma unroll
    for (int qb = 0; qb < 2; ++qb) if (!(d[qb] > -3.0e38f)) d[qb] = 0.f;
  }
  GAP();
  vset<R_M>(d[0]); vset<R_M + 1>(d[1]);
  dma_at<9>();
  const f32x2c nd[2] = {{-d[0], -d[0]}, {-d[1], -d[1]}};
  static_for([&](auto E) {
    constexpr int e = E;
    const f32x2c v = nd[(e >> 1) & 1];
    asm volatile("v_pk_add_f32 v[%c0:%c1], v[%c0:%c1], %4\n\tv_mov_b64 v[%c2:%c3], %4" ::"i"(R_S + 32 * X + 2 * e), "i"(R_S + 32 * X + 2 * e + 1), "i"(R_CI + 2 * e), "i"(R_CI + 2 * e + 1), "v"(v));
    if constexpr (e == 7 || e == 15) dma_at<10 + e / 8>();
  }, make_iseq<16>{});
  static_assert(SM.op0[64] >= 3 * ((sm_nex() - 15) / 2) && SM.op0[64] <= 24);
  static_for([&](auto E) {
    constexpr int e = E;
    if constexpr (e < sm_nex()) asm volatile("v_exp_f32 v%c0, v%c1" ::"i"(R_E + e), "i"(R_S + 32 * X + e));
    if constexpr (e == 2) dma_m0<X, 12>();
    if constexpr (e == 5 || e == 10 || e == 15) dma_at<11 + e / 5>();
  }, make_iseq<16>{});
  static_for([&](auto O) { sm_op<X, O>(); }, make_iseq<SM.op0[64]>{});
  dma_at<15>();
  static_for([&](auto E) { asm volatile("v_exp_f32 v%c0, v%c1" ::"i"(R_E + (int)E), "i"(R_S + 32 * X + 16 + (int)E)); }, make_iseq<(sm_nex() > 16 ? sm_nex() - 16 : 0)>{});
  asm volatile("buffer_store_dwordx2 v[%c0:%c1], v88, s[88:91], s76 offen sc1\n\ts_bitcmp1_b32 s77, 0\n\ts_cselect_b64 s[52:53], s[62:63], 0\n\t"
               FL_ARM("s[52:53]") "\n\t" FL_POLL ::"i"(R_M), "i"(R_M + 1) : "memory", "scc");
  GAP();
}
enum : int { T_Q0, T_K0, T_OQ, T_OG, T_FPREP, T_FQ, T_FG, T_FS, T_ZO, T_CNT, T_NPRE = 10, T_NST = 12, T_TAB = 14, T_SLAB = 16, T_PART, T_IT = 18, T_ITN = 23 };
DI void ev_code(AC& c) {
  GAP();
  asm volatile(
    "s_branch 9f\nev_entry:\n\t"
    "s_bitcmp1_b32 s60, s56\n\ts_cbranch_scc1 3f\n\ts_bitcmp1_b32 s74, s56\n\ts_cbranch_scc1 2f\n\t"
    "s_bitcmp1_b32 s58, s56\n\ts_cselect_b64 vcc, -1, 0\n\tv_cndmask_b32 v121, -2.0, v81, vcc\n\t"
    "buffer_store_dwordx2 v[82:83], v121, s[88:91], s70 offen sc1\n\tbuffer_store_dwordx2 v[82:83], v121, s[88:91], s71 offen sc1\n\t"
    "v_cmp_eq_u64 vcc, s[84:85], v[124:125]\n\ts_andn2_b64 vcc, s[62:63], vcc\n\ts_cbranch_scc1 5f\n"
    "0:\n\ts_add_u32 s98, s98, s59\n\ts_mov_b32 s59, s61\n\ts_mov_b32 s61, s72\n\ts_mov_b64 s[62:63], s[64:65]\n\ts_mov_b64 s[64:65], s[66:67]\n\t"
    "s_mov_b64 s[66:67], s[68:69]\n\ts_cmp_eq_u32 s56, 1\n\ts_cbranch_scc1 ev_first\n\ts_setpc_b64 s[86:87]\n"
    "5:\n\t" FL_CHECK("s[62:63]") "\n\ts_branch 0b\n"
    "6:\n\t" FL_CHECK("s[62:63]") "\n\ts_setpc_b64 s[86:87]\n"
    "2:\nev_mask:\n\tv_mov_b32 v122, 0xff800000\n\t" MSK(228, 0) MSK(236, 16) MSK(244, 32) MSK(252, 48)
    "ev_turn:\n\ts_mov_b32 s98, 0\n\t"
    "v_lshrrev_b32 v90, 7, v86\n\tv_and_b32 v91, 6, v90\n\tv_lshlrev_b32 v91, 4, v91\n\tv_and_b32 v92, 0x70, v86\n\tv_xor_b32 v91, v91, v92\n\t"
    "s_movk_i32 s48, 896\n\tv_mad_u32_u24 v94, v90, s48, v91\n\ts_mov_b64 s[92:93], s[80:81]\n\ts_mov_b32 s94, 229376\n\t"
    "v_add_u32 v90, 28672, v94\n\tv_add_u32 v91, 34816, v94\n\tv_add_u32 v92, 40960, v94\n\tv_add_u32 v93, 47104, v94\n\t"
    "v_add_u32 v95, 6144, v94\n\tv_add_u32 v126, 12288, v94\n\tv_add_u32 v127, 18432, v94\n\t"
    "v_cmp_eq_u64 vcc, s[84:85], v[124:125]\n\ts_andn2_b64 vcc, s[62:63], vcc\n\ts_cbranch_scc1 6b\n\ts_setpc_b64 s[86:87]\n"
    "3:\n\tv_mov_b32 v122, 0xff800000\n\t" MSK(224, 0) MSK(232, 16) MSK(240, 32) MSK(248, 48)
    "ev_post:\n\tv_readlane_b32 s54, v78, %0\n\ts_add_u32 m0, s78, 0x10000\n\t"
    "s_bfe_u32 s53, s73, 0x80010\n\ts_mulk_i32 s53, 3584\n\ts_mov_b64 s[48:49], s[80:81]\n\ts_add_u32 s50, s53, 3584\n\t"
    "s_mov_b32 s51, 0x20000\n\t"
    "v_lshlrev_b32 v121, 3, v84\n\tbuffer_load_dword v121, s[48:51], s53 offen lds\n\t"
    "s_bitcmp0_b32 s73, 3\n\ts_cbranch_scc1 4f\n\t"
    "buffer_load_dwordx4 v84, s[88:91], s54 offen sc1 lds\n\tbuffer_load_dwordx4 v84, s[88:91], s54 offen offset:1024 sc1 lds\n\t"
    "buffer_load_dwordx4 v84, s[88:91], s54 offen offset:2048 sc1 lds\n\tbuffer_load_dwordx4 v84, s[88:91], s54 offen offset:3072 sc1 lds\n"
    "4:\n\ts_cmp_eq_u32 s56, 1\n\ts_cbranch_scc1 ev_first\n\ts_setpc_b64 s[86:87]\n"
    ::"i"((int)T_ZO) : "scc");
  asm volatile("ev_zero:");
  acc_zero();
  asm volatile("s_setpc_b64 s[86:87]\nev_first:\n\ts_cmp_lg_u32 s57, 0\n\ts_cbranch_scc1 ev_zero\n\ts_bitcmp0_b32 s73, 4\n\ts_cbranch_scc1 ev_first1");
  GAP();
  phaseB<0, true>(c);
  asm volatile("s_branch att_loop\nev_first1:");
  phaseB<1, true>(c);
  asm volatile("s_branch att_mid\n9:");
  GAP();
}
#define EV_CALL(L) do { asm volatile("s_getpc_b64 s[86:87]\n\ts_add_u32 s86, s86, 12\n\ts_addc_u32 s87, s87, 0\n\ts_branch " L ::: "vcc", "scc", "memory"); GAP(); } while (0)

#define ACLOB_ALL "a0","a1","a2","a3","a4","a5","a6","a7","a8","a9","a10","a11","a12","a13","a14","a15","a16","a17","a18","a19","a20","a21","a22","a23","a24","a25","a26","a27","a28","a29","a30","a31","a32","a33","a34","a35","a36","a37","a38","a39","a40","a41","a42","a43","a44","a45","a46","a47","a48","a49","a50","a51","a52","a53","a54","a55","a56","a57","a58","a59","a60","a61","a62","a63","a64","a65","a66","a67","a68","a69","a70","a71","a72","a73","a74","a75","a76","a77","a78","a79","a80","a81","a82","a83","a84","a85","a86","a87","a88","a89","a90","a91","a92","a93","a94","a95","a96","a97","a98","a99","a100","a101","a102","a103","a104","a105","a106","a107","a108","a109","a110","a111","a112","a113","a114","a115","a116","a117","a118","a119","a120","a121","a122","a123","a124","a125","a126","a127","a128","a129","a130","a131","a132","a133","a134","a135","a136","a137","a138","a139","a140","a141","a142","a143","a144","a145","a146","a147","a148","a149","a150","a151","a152","a153","a154","a155","a156","a157","a158","a159","a160","a161","a162","a163","a164","a165","a166","a167","a168","a169","a170","a171","a172","a173","a174","a175","a176","a177","a178","a179","a180","a181","a182","a183","a184","a185","a186","a187","a188","a189","a190","a191","a192","a193","a194","a195","a196","a197","a198","a199","a200","a201","a202","a203","a204","a205","a206","a207","a208","a209","a210","a211","a212","a213","a214","a215","a216","a217","a218","a219","a220","a221","a222","a223","a224","a225","a226","a227","a228","a229","a230","a231","a232","a233","a234","a235","a236","a237","a238","a239","a240","a241","a242","a243","a244","a245","a246","a247","a248","a249","a250","a251","a252","a253","a254","a255"

struct EP { float fa[2], fb[2]; };
template <int I, int E, int MODE> DI void epi_pair(const EP& p) {
  constexpr int qb = I & 1, r = 16 * (I >> 1) + 4 * qb + (E >> 1) * 8 + (E & 1) * 2, t = 96 + 2 * E, w = 128 + 4 * I + E;
  if constexpr (MODE == 2) asm volatile("v_accvgpr_read_b32 v106, a[%c0]" ::"i"(w));
  asm volatile("v_accvgpr_read_b32 v%c0, a[%c2]\n\tv_accvgpr_read_b32 v%c1, a[%c3]" ::"i"(t), "i"(t + 1), "i"(r), "i"(r + 1));
  if constexpr (MODE) asm volatile("v_mul_f32 v%c0, v%c0, %2\n\tv_mul_f32 v%c1, v%c1, %2" ::"i"(t), "i"(t + 1), "v"(p.fa[qb]));
  if constexpr (MODE == 2)
    asm volatile("v_lshlrev_b32 v104, 16, v106\n\tv_and_b32 v105, 0xffff0000, v106\n\tv_fmac_f32 v%c0, v104, %2\n\tv_fmac_f32 v%c1, v105, %2"
                 ::"i"(t), "i"(t + 1), "v"(p.fb[qb]));
  asm volatile("v_cvt_pk_bf16_f32 v%c0, v%c1, v%c2" ::"i"(w), "i"(t), "i"(t + 1));
}
template <int I> DI void epi_store() {
  asm volatile("buffer_store_dwordx4 v[%c0:%c1], v84, s[88:91], s101 offen offset:%2 sc1" ::"i"(128 + 4 * I), "i"(131 + 4 * I), "i"((I & 3) * 1024) : "memory");
  if constexpr ((I & 3) == 3) asm volatile("s_addk_i32 s101, 4096");
}
constexpr int R_TB = 232, R_VS = 236;
template <int HF, int R> DI void p_read() {
  constexpr int mb = R >> 4, r = 192 - 64 * HF + 2 * R;
  asm volatile("ds_read_b64_tr_b16 a[%c1:%c2], v%c0 offset:%c3" ::"i"(R_TB + 2 * HF + mb), "i"(r), "i"(r + 1), "i"((R & 15) * 2048));
}
template <int QB, int E> DI void p_cvt() {
  asm volatile("v_cvt_pk_bf16_f32 v%c0, v%c1, v%c2" ::"i"(208 + 4 * QB + E), "i"(192 + 8 * QB + 2 * E), "i"(193 + 8 * QB + 2 * E));
}
template <int QB, int PR> DI void p_store() {
  asm volatile("buffer_store_dwordx4 v[%c0:%c1], v%c2, s[88:91], s96 offen offset:%3 sc1" ::"i"(208 + 4 * QB), "i"(211 + 4 * QB), "i"(R_VS + QB),
               "i"(PR * 128) : "memory");
}
constexpr int p_dma(int HF, int G) { return G == 3 && !HF ? 0 : G == 11 ? 1 - HF : G == 15 ? 2 - HF : G == 19 ? 3 - HF : G == 27 && HF ? 3 : -1; }
template <int HF, bool LD, int G> DI void p_gap() {
  constexpr int ch = G >> 3, t = G & 7, a = 192 - 64 * HF + 32 * (ch & 1) + 4 * t, b = 128 + 4 * (2 * t + (ch >> 1)), d = 192 + 4 * ch;
  if constexpr (t == 0)
    asm volatile("v_mfma_f32_16x16x32_bf16 v[%c0:%c1], a[%c2:%c3], v[%c4:%c5], 0" ::"i"(d), "i"(d + 3), "i"(a), "i"(a + 3), "i"(b), "i"(b + 3));
  else
    asm volatile("v_mfma_f32_16x16x32_bf16 v[%c0:%c1], a[%c2:%c3], v[%c4:%c5], v[%c0:%c1]" ::"i"(d), "i"(d + 3), "i"(a), "i"(a + 3), "i"(b), "i"(b + 3));
  p_read<HF ^ 1, G>();
  if constexpr (G == 0) asm volatile("v_xor_b32 v%c0, s100, v%c0" ::"i"(R_TB + 2 * HF + 1));
  if constexpr (G == 17) asm volatile("v_xor_b32 v%c0, s100, v%c0" ::"i"(R_TB + 2 * (HF ^ 1)));
  if constexpr (G >= 4 && G <= 8 && G != 7) p_cvt<1, (G < 7 ? G - 4 : 3)>();
  if constexpr (G == 24 || (G == 7 && HF == 0)) asm volatile("s_waitcnt lgkmcnt(8)");
  if constexpr (G == 7 && HF == 1) asm volatile("s_waitcnt vmcnt(%0) lgkmcnt(8)\n\ts_barrier" ::"i"(LD ? 13 : 5) : "memory");
  if constexpr (G == 10) p_store<1, HF ^ 1>();
  if constexpr (G >= 20 && G < 24) p_cvt<0, G - 20>();
  if constexpr (G == 25) p_store<0, HF>();
  if constexpr (LD && p_dma(HF, G) >= 0) dma_at<(HF ? 8 : 12) + p_dma(HF, G)>();
  if constexpr (LD && HF == 0 && G == 2) asm volatile("s_bitset0_b32 m0, 12");
  if constexpr (HF == 0 && G == 13) asm volatile("s_add_u32 s96, s96, 0x8000");
  if constexpr (HF == 0 && G == 26) asm volatile("s_add_u32 s98, s98, 128");
  if constexpr (LD && HF == 0 && G == 27) asm volatile("s_cmp_lt_u32 s98, 0x380");
  if constexpr (HF == 1 && G == 1) asm volatile("s_xor_b32 s100, s100, 0x10000");
  if constexpr (LD && HF == 1 && G == 2) asm volatile("s_xor_b32 s97, s97, s100");
  if constexpr (LD && HF == 1 && G == 9) asm volatile("s_or_b32 m0, s97, 0x1000");
  if constexpr (!LD && HF == 1 && G == 30) asm volatile("s_cmp_lt_u32 s98, 0x500");
  GAP();
}
template <int HF, bool LD, int... Gs> DI void p_half(iseq<Gs...>) { (p_gap<HF, LD, Gs>(), ...); }
template <int G> DI void gapF(AC& c, const EP& p) {
  if constexpr (G < 64) gapB<0, false, G>(c);
  if constexpr (G >= 10) epi_pair<(G - 10) / 4, (G - 10) & 3, 0>(p);
  if constexpr (G >= 14 && ((G - 14) & 3) == 0) epi_store<(G - 14) / 4>();
  if constexpr (G == 73) epi_store<15>();
  GAP();
}
template <int... Gs> DI void phaseF(AC& c, const EP& p, iseq<Gs...>) { (gapF<Gs>(c, p), ...); }
template <int G> DI void gapAL(AC& c) {
  constexpr int kb = G >> 3, s = G & 7, r = 192 + 4 * (G & 15), q = 160 + 4 * s, d = R_S + 36 + 8 * kb, ci = s == 0 ? R_CI + 4 + 8 * kb : d;
  if constexpr (G == 0) asm volatile("s_add_u32 s96, s97, 0x1000000\n\ts_mov_b32 m0, s79\n\ts_waitcnt vmcnt(9) lgkmcnt(0)\n\ts_barrier" ::: "memory");
  if constexpr (G >= 8 && (G & 3) == 0) asm volatile("s_waitcnt lgkmcnt(4)");
  if constexpr (G == 29) asm volatile("s_barrier" ::: "memory");
  asm volatile("v_mfma_f32_16x16x32_bf16 v[%c0:%c1], a[%c2:%c3], a[%c4:%c5], v[%c6:%c7]" ::"i"(d), "i"(d + 3), "i"(r), "i"(r + 3), "i"(q), "i"(q + 3),
               "i"(ci), "i"(ci + 3));
  if constexpr (G < 24) kread<1, G + 8>();
  else vread<0, G - 24>();
  if constexpr (G % 3 == 1 && G < 24) dma_at<G / 3>();
  if constexpr (G == 11) asm volatile("s_bitset1_b32 m0, 12");
  if constexpr (G == 28) asm volatile("buffer_load_dwordx4 v[122:125], v88, s[88:91], s76 offen sc1" ::: "memory");
  if constexpr (G == 30) asm volatile("s_xor_b32 m0, s79, 0x11000\n\ts_movk_i32 s98, 128");
  sm_gap<0, 64 + 2 * G>(c); sm_gap<0, 65 + 2 * G>(c);
  GAP();
}
template <int... Gs> DI void phaseAL(AC& c, iseq<Gs...>) { (gapAL<Gs>(c), ...); }
struct BLS { int ex[65], op[65], vm[65], su[65]; };
constexpr BLS bl_sched(bool LD) {
  BLS z{};
  z.op[0] = 1;
  for (int t = 0; t < 64; ++t) {
    const int g = t & 15, o = z.op[t];
    const bool x = t == 6 || (t < 48 && g > 8 && g != 12 && g != 15);
    const bool v = LD ? (t & 3) == 0 : (t & 11) == 8;
    const bool p = t >= 15 && o < 24 && (t & 3) != 0 && t != 47 && !x && z.ex[t - 1] > 2 * (o / 3) + (o % 3 != 0);
    z.ex[t + 1] = z.ex[t] + x; z.vm[t + 1] = z.vm[t] + v; z.op[t + 1] = o + p;
    z.su[t + 1] = z.su[t] + (t >= 1 && z.su[t] < (LD ? 16 : 10) && g != 15 && !v && !x && (LD || t > 55 || (t > 3 && !p)));
  }
  return z;
}
template <bool LD> constexpr BLS BL = bl_sched(LD);
static_assert(BL<true>.ex[64] == 16 && BL<true>.op[61] == 24 && BL<true>.vm[64] == 16 && BL<false>.vm[64] == 8);
static_assert(BL<true>.su[56] == 16 && BL<false>.su[62] == 10);
template <bool LD, int G> DI void gapBL(AC& c) {
  if constexpr (G == 48) asm volatile("s_waitcnt vmcnt(%0) lgkmcnt(0)\n\ts_barrier" ::"i"(BL<LD>.vm[48]) : "memory");
  gapB<1, false, G>(c);
  if constexpr (G >= 48 && G < 56) vread<1, G - 48>();
  if constexpr (BL<LD>.vm[G + 1] > BL<LD>.vm[G]) {
    constexpr int k = BL<LD>.vm[G] + (LD ? 0 : 16), I = 2 * (k & 7) + ((k >> 3) & 1);
    if constexpr (k < 16) {
      asm volatile("buffer_load_dwordx4 a[%c0:%c1], v84, s[88:91], s75 offen offset:%2 sc1" ::"i"(128 + 4 * I), "i"(131 + 4 * I), "i"((I & 3) * 1024) : "memory");
      if constexpr (k & 1) asm volatile("s_addk_i32 s75, %0" ::"i"(k == 7 ? -12288 : 4096));
    } else dma_at<k - 8>();
    if constexpr (k == 19) asm volatile("s_bitset0_b32 m0, 12");
  }
  if constexpr (G == 1 || G == 2) asm volatile("v_add_f32_e32 v%c0, v%c0, v%c1" ::"i"(R_RS + G - 1), "i"(R_TS + G - 1));
  if constexpr (G == 3) asm volatile("v_mov_b32 v%c0, 0" ::"i"(R_TS));
  if constexpr (BL<LD>.ex[G + 1] > BL<LD>.ex[G]) {
    constexpr int j = BL<LD>.ex[G];
    asm volatile("v_exp_f32 v%c0, v%c1" ::"i"(R_E + j), "i"(R_S + 36 + 8 * (j >> 2) + (j & 3)));
  }
  if constexpr (BL<LD>.op[G + 1] > BL<LD>.op[G]) {
    constexpr int o = BL<LD>.op[G], k = o / 3, j = 2 * k + o % 3;
    if constexpr (o % 3 == 2) asm volatile("v_cvt_pk_bf16_f32 v%c0, v%c1, v%c2" ::"i"(p_reg(1, (k >> 1) * 4 + 2 + (k & 1))), "i"(R_E + 2 * k), "i"(R_E + 2 * k + 1));
    else asm volatile("v_add_f32_e32 v%c0, v%c1, v%c2" ::"i"(R_TS + 1), "i"(j == 1 ? R_E : R_TS + 1), "i"(R_E + j));
  }
  if constexpr (G == 61) asm volatile("v_cmp_nle_f32_e64 s[54:55], v%c0, v%c1" ::"i"(R_TS + 1), "i"(R_THR));
  if constexpr (BL<LD>.su[G + 1] > BL<LD>.su[G]) {
    constexpr int k = BL<LD>.su[G];
    if constexpr (k < 4) asm volatile("v_xor_b32 v%c0, %1, %2" ::"i"(R_TB + k), "i"(32 * k), "v"(c.tb));
    else if constexpr (k < 6) asm volatile("v_add_u32 v%c0, %1, %2" ::"i"(R_VS + k - 4), "i"(4096 * (k - 4)), "v"(c.vs));
    else if constexpr (k == 6) tload<96, T_SLAB>();
    else if constexpr (k == 7) tload<101, T_CNT>();
    else if constexpr (k == 8) asm volatile("v_mov_b64 v[240:241], 1");
    else if constexpr (k == 9) asm volatile("s_mov_b32 s97, s79\n\ts_mov_b32 s100, 0x8000");
    else if constexpr (k < 14) asm volatile("v_mov_b32 v%c0, %1" ::"i"(k < 12 ? 214 + k : 218 + k), "v"(k & 1 ? c.o1 : c.o0));
    else asm volatile("v_mov_b64 v[%c0:%c1], 0" ::"i"(198 + 2 * k), "i"(199 + 2 * k));
  }
  GAP();
}
template <bool LD, int... Gs> DI void phaseBL(AC& c, iseq<Gs...>) { (gapBL<LD, Gs>(c), ...); }
template <int MODE, int G> DI void gapFL(const EP& p) {
  constexpr int x = G - (MODE == 3 ? 4 : 0), J = MODE == 1 ? (G < 24 && G % 3 == 1 ? G / 3 : -1) : G < 27 && G % 5 != 0 && G % 5 != 3 ? G / 5 * 3 + (G % 5) / 2 : -1;
  if constexpr (G == 0 && MODE == 1) asm volatile("s_or_b32 m0, s78, 0x1000\n\ts_movk_i32 s98, 256\n\ts_waitcnt lgkmcnt(0)");
  if constexpr (G == 0 && MODE > 1) asm volatile("s_waitcnt vmcnt(8) lgkmcnt(0)" ::: "memory");
  if constexpr (G == 16 && MODE > 1) asm volatile("s_waitcnt vmcnt(9)" ::: "memory");
  if constexpr (G >= 8 && G < 32 && (G & 3) == 0) asm volatile("s_waitcnt lgkmcnt(%0)" ::"i"(G < 28 ? 4 : 0));
  if constexpr (G < 32) {
    constexpr int o = 8 * (G >> 1) + 4, r = 192 + 4 * (G & 15), q = R_P + 20 + 8 * (G & 1);
    asm volatile("v_mfma_f32_16x16x32_bf16 a[%c0:%c1], a[%c2:%c3], v[%c4:%c5], a[%c0:%c1]" ::"i"(o), "i"(o + 3), "i"(r), "i"(r + 3), "i"(q), "i"(q + 3));
  }
  if constexpr (G < 24) vread<1, G + 8>();
  if constexpr (G == 28) asm volatile("s_waitcnt vmcnt(8)\n\ts_barrier\n\ts_or_b32 m0, s79, 0x1000\n\ts_movk_i32 s98, 384" ::: "memory");
  if constexpr (G == 29 || G == 31) dma_at<8 + (G - 29) / 2>();
  if constexpr (G == 32 || G == 40) {
    static_for([&](auto I) { p_read<0, 2 * (G - 32) + I>(); }, make_iseq<16>{});
    dma_at<10 + (G - 32) / 8>();
  }
  if constexpr (J >= 0) dma_at<8 + (J & 7)>();
  if constexpr (x >= 0) epi_pair<2 * ((x >> 2) & 7) + (x >> 5), x & 3, MODE>(p);
  if constexpr (MODE == 3 && G < 32) {
    constexpr int db = G & 15, qb = G >> 4, o = 4 * (db * 2 + qb), a = 224 + 4 * (db & 1), b = 128 + 4 * ((db >> 1) * 2 + qb);
    asm volatile("v_mfma_f32_16x16x32_bf16 a[%c0:%c1], v[%c2:%c3], a[%c4:%c5], a[%c0:%c1]" ::"i"(o), "i"(o + 3), "i"(a), "i"(a + 3), "i"(b), "i"(b + 3));
  }
  if constexpr (J == 3 || J == 11) asm volatile("s_bitset0_b32 m0, 12");
  if constexpr (J == 7 && MODE > 1) asm volatile("s_or_b32 m0, s78, 0x1000\n\ts_movk_i32 s98, 256");
  if constexpr (x == 63) asm volatile("s_waitcnt vmcnt(4) lgkmcnt(0)" ::: "memory");
  GAP();
}
template <int MODE, int... Gs> DI void phaseFL_(const EP& p, iseq<Gs...>) { (gapFL<MODE, Gs>(p), ...); }
template <int MODE> DI void phaseFL(const EP& p) { GAP(); phaseFL_<MODE>(p, make_iseq<MODE == 3 ? 68 : 64>{}); }
template <int U, int K> DI void r_load(unsigned vo, unsigned so) {
  asm volatile("buffer_load_dwordx4 a[%c0:%c1], %2, s[88:91], %3 offen offset:%4 sc1" ::"i"(64 * U + 4 * K), "i"(64 * U + 4 * K + 3), "v"(vo),
               "s"(so + (K >> 1) * 0x40000 + (U >> 1) * 4096), "i"((K & 1) * 512 + (U & 1) * 2048) : "memory");
}
template <int U, int K> DI void r_sum() {
  constexpr int d = 128 + 8 * U + 4 * (K & 1), b = 64 * U + 4 * K;
  if constexpr (K < 2) asm volatile("v_mfma_f32_16x16x32_bf16 v[%c0:%c1], v[96:99], a[%c2:%c3], 0" ::"i"(d), "i"(d + 3), "i"(b), "i"(b + 3));
  else asm volatile("v_mfma_f32_16x16x32_bf16 v[%c0:%c1], v[96:99], a[%c2:%c3], v[%c0:%c1]" ::"i"(d), "i"(d + 3), "i"(b), "i"(b + 3));
}
template <int U> DI void r_sums() { static_for([&](auto K) { r_sum<U, K>(); }, make_iseq<16>{}); }
template <int U> DI void r_fin(unsigned vo, __amdgpu_buffer_rsrc_t srd, unsigned so) {
  static_for([&](auto E) { asm volatile("v_cvt_pk_bf16_f32 v%c0, v%c1, v%c2" ::"i"(128 + 8 * U + E), "i"(128 + 8 * U + 2 * E), "i"(129 + 8 * U + 2 * E)); }, make_iseq<4>{});
  asm volatile("s_nop 0\n\tbuffer_store_dwordx4 v[%c3:%c4], %0, %1, %2 offen sc0 sc1" ::"v"(vo), "s"(srd), "s"(so), "i"(128 + 8 * U), "i"(131 + 8 * U) : "memory");
}

#define CMAX 12
#define NSPL 5
#define NYT 15
#define NYM 0
struct TRow { unsigned v[64]; };
constexpr u64 kvmask(int m0, int m1) { u64 n = 0; for (int m = m0; m <= m1; ++m) n |= 0x101ull << m; return n; }
constexpr u64 k_order(bool isY, int rr) {
  const int cls = (rr + (rr >> 2)) & 3;
#define NCM 3
  u64 t = 0;
  int n = 0;
  auto put = [&](int k) { t |= (u64)k << (4 * n++); };
  if (isY) {
    for (int k = 1; k <= 4; ++k) put(NCM + 4 + (k & 3));
    for (int k = 1; k <= 4; ++k) put(NCM + 8 + (k & 3));
    for (int k = NCM + 12; k < 16; ++k) put(k);
    for (int k = NCM + 3; k >= 0; --k) put(k);
    return t;
  }
  for (int k = 0; k < NCM; ++k) put(k);
  for (int k = 0; k < 3; ++k) { put(NCM + 4 * k + cls); put(NCM + ((cls + k + 1) & 3)); }
  for (int k = 1; k < 4; ++k) put(NCM + 4 + ((cls + k) & 3));
  for (int k = 1; k < 4; ++k) put(NCM + 8 + ((cls + k) & 3));
  for (int k = NCM + 12; k < 16; ++k) put(NCM == 2 ? k ^ (cls & 1) : k);
  return t;
}
static_assert(NCM >= 3);
constexpr TRow t_row(int b, int wid) {
  TRow t{};
  const int j = b & 7, r = b >> 3, rr = r & 15, hsel = rr & 1, pj = rr >> 1, kv = pj & 1, h = j + 8 * hsel, u = hsel * 8 + j;
  const bool isY = r >= 16, noA = pj == 7, solo = pj >= NSPL;
  const int qbF = isY != solo ? 15 - pj : pj;
  const int mt = (rr >> 2) + (isY ? 4 : 0), g = j * 128, NY = NYT - ((NYM >> pj) & 1) + ((NYM >> (8 + pj)) & 1);
  const int nx0 = solo ? 0 : 32 - 2 * pj - NY, cx = nx0 < CMAX ? nx0 : CMAX, ex = nx0 - cx, qb0 = solo ? qbF : 15 - pj;
  const unsigned oQ = (unsigned)h << 20, oK = oQ + O_K, oP = O_P + (h * 8 + pj) * PSTR;
  if (wid == 4) {
    for (int l = 0; l < 64; ++l)
      t.v[l] = fo(l < 16 ? g + hsel * 16 + l : l == 16 ? g + 32 + hsel * 8 + (qb0 >> 1) : l == 17 ? g + 32 + hsel * 8 + (qbF >> 1) :
                  l == 26 ? g + 48 + hsel * 8 + pj : l < 32 ? g + 64 + hsel * 8 + pj :
                  l < 48 ? 2304 + pj * 16 + l - 32 : 2048 + pj * 16 + l - 48);
    return t;
  }
  auto put64 = [&](int i, u64 x) { t.v[i] = (unsigned)x; t.v[i + 1] = (unsigned)(x >> 32); };
  t.v[T_Q0] = oQ + (qb0 * 128 + wid * 16) * 512;
  t.v[T_K0] = oK + (isY ? cx : 0) * 32768;
  const int jq = (j & 6) | (pj & 1), tq = (j & 1) * 4 + (pj >> 1);
  t.v[T_OQ] = ((unsigned)(jq + 8 * hsel) << 20) + tq * 131072 + (wid >> 1) * 65536 + (wid & 1) * 4096;
  t.v[T_OG] = oK + kv * TSB + mt * 131072 + (wid >> 1) * 65536 + (wid & 1) * (kv ? 8192 : 4096);
  t.v[T_FPREP] = fo(2048 + pj * 16 + u);
  t.v[T_FQ] = fo(jq * 128 + 32 + hsel * 8 + tq);
  t.v[T_FG] = fo(g + hsel * 16 + kv * 8 + mt);
  t.v[T_FS] = fo(1024 + qbF * 16 + j * 2 + hsel);
  t.v[T_CNT] = fo(2560 + qbF * 16);
  t.v[T_ZO] = O_Z + pj * 64 * SEQ * 2 + (u * 4 + wid) * 4096;
  const u64 own = 0x1ull << (kv * 8 + mt), conv = noA ? 0 : 0xffffull << 32;
  put64(T_NPRE, 0x10000ull | (isY ? (1ull << (cx >> 2)) | (1ull << ((cx + 1) >> 2)) : noA ? 0 : 0xffffull << 48));
  put64(T_NST, isY ? (solo ? kvmask(0, (2 * qbF + 1) >> 2) : kvmask(cx >> 2, 3) | 1ull << 27) : kvmask(0, 0));
  put64(T_TAB, k_order(isY, rr));
  t.v[T_SLAB] = O_S + qbF * 0x200000 + j * 0x40000 + wid * 8192 + hsel * 1024;
  t.v[T_PART] = oP;
  const unsigned junk = fo(g + 96 + r), fconv = noA ? junk : fo(2304 + pj * 16 + u);
  for (int it = isY || solo; it < 2; ++it) {
    unsigned* p = t.v + T_IT + it * T_ITN;
    const int n = !it ? nx0 : isY && !solo ? NY : 2 * qbF + 2;
    u64 q[4] = {0, 0, 0, 0};
    unsigned jmp[3] = {0, 0, 0}, chk = 0;
    p[14] = junk;
    if (isY) {
      p[0] = 2; p[1] = 2; p[13] = t.v[T_FG];
      if (!solo) {
        chk = 1u << (14 - cx); p[0] |= chk; q[1] = kvmask(4, (31 - 2 * pj) >> 2) & ~own; jmp[1] = ex * 32768;
        chk |= 1u << (n - 2); q[2] = 1ull << 26;
      }
    } else if (it == 0) {
      chk = p[0] = 2u | (ex ? 1u << (cx - 2) : 0) | 1u << (n - 2);
      chk |= noA || qbF > 1 ? 0 : 1u << n;
      q[0] = kvmask(0, (cx - 1) >> 2);
      if (ex) { q[1] = kvmask(4, 4 + ((ex - 1) >> 2)); jmp[1] = (16 - cx) * 32768; }
      jmp[ex ? 2 : 1] = -(ex ? 16 + ex : cx) * 32768;
      q[ex ? 2 : 1] = 0x20000ull | kvmask(0, (2 * qbF + 1) >> 2);
      q[ex ? 3 : 2] = conv;
      p[1] = 2; p[13] = fconv; p[14] = fo(g + 64 + hsel * 8 + pj);
    } else if (solo) {
      chk = p[0] = 0x402; p[1] = 2; p[13] = fconv;
      q[0] = kvmask(0, 2); q[1] = kvmask(3, (2 * qbF + 1) >> 2) | conv;
    } else {
      p[0] = p[1] = n > 4 ? 2 : 1; p[13] = fo(g + 48 + hsel * 8 + pj);
      chk = noA ? 0 : p[0];
      q[0] = conv;
    }
    p[2] = jmp[0]; p[4] = jmp[1]; p[15] = jmp[2];
    p[3] = it && n > 2 ? 1u << (n - 3) : 0;
    for (int k = 0; k < 4; ++k) { p[5 + 2 * k] = (unsigned)q[k]; p[6 + 2 * k] = (unsigned)(q[k] >> 32); }
    p[16] = 32 | (isY && !solo ? 2 : 0) | (n == 2 ? 4 : 0) | (isY || noA ? 0 : 8) | (n & 1) << 4 | (n - 1 - it) << 8 | ((r >> 1) * 4 + wid) << 16 | h << 24 | qbF << 28;
    p[17] = it && n > 2 ? 1u << (n - 2) : 0;
    p[18] = it ? oP + wid * 16384 : oQ + (qbF * 128 + wid * 16) * 512;
    p[19] = oP + (it ? 1032 : 1024);
    p[20] = chk >> 1;
    p[21] = oK + (isY ? cx : 0) * 32768;
    p[22] = (p[0] | p[3] | p[17]) << 1;
  }
  return t;
}
struct TBlk { TRow row[5]; };
template <int B> constexpr TBlk t_blk = {{t_row(B, 0), t_row(B, 1), t_row(B, 2), t_row(B, 3), t_row(B, 4)}};
struct Tab { TBlk blk[256]; };
template <int... I> constexpr Tab t_make(iseq<I...>) { return Tab{{t_blk<I>...}}; }
__device__ const Tab g_tab = t_make(make_iseq<256>{});

template <int R> DI const bf16* sptr() { const bf16* p; asm volatile("s_mov_b64 %0, s[%c1:%c2]" : "=s"(p) : "i"(R), "i"(R + 1)); return p; }
template <class T, int I> DI T* arg_late() {
  unsigned lo, hi;
  asm volatile("v_readlane_b32 %0, v77, %2\n\tv_readlane_b32 %1, v77, %3\n\ts_nop 4" : "=s"(lo), "=s"(hi) : "i"(2 * I), "i"(2 * I + 1));
  return (T*)(((u64)hi << 32) | lo);
}
__global__ __launch_bounds__(256, 1) __attribute__((amdgpu_num_vgpr(77), amdgpu_num_sgpr(56))) void fused_kernel(
    const bf16* ckv_, const bf16* wuk_, const bf16* wuv_, const bf16* x_, const bf16* wq_, bf16* ws_, const bf16* ak_, const bf16* av_,
    const bf16* ash_, const bf16* assmk_, const bf16* assmD_, const bf16* wo_, const bf16* aq_, bf16* out_) {
  extern __shared__ __attribute__((aligned(16))) char lds[];
  const void* kap = (const void*)__builtin_amdgcn_kernarg_segment_ptr();
  asm volatile("at_code_begin:\n\ts_load_dwordx16 s[80:95], %0, 0\n\ts_load_dwordx2 s[96:97], %0, 0x40" ::"s"(kap) : ACLOB_ALL, "v255", "s101");
  asm volatile("s_getpc_b64 s[48:49]\nct_base:\n\ts_mov_b32 s50, pf_code_end-ct_base\n\ts_mov_b32 s51, 0x20000\n\ts_lshr_b32 s52, %1, 3\n\t"
               "v_and_b32 v116, 63, %0\n\tv_lshrrev_b32 v117, 6, %0\n\tv_lshl_add_u32 v116, v116, 7, v117\n\ts_lshl_b32 s52, s52, 9\n\t"
               "v_lshlrev_b32 v116, 7, v116\n\tbuffer_load_dword v117, v116, s[48:51], s52 offen" ::"v"(threadIdx.x), "s"(blockIdx.x) : "scc", "memory");
  GAP();
  const int tid = threadIdx.x, lane = tid & 63;
  const int wid = __builtin_amdgcn_readfirstlane(tid >> 6);
  const int b = blockIdx.x, j = b & 7, r = b >> 3, rr = r & 15, hsel = rr & 1, pj = rr >> 1, kv = pj & 1;
  const bool isY = r >= 16, doP = isY && pj != 7, doA = !isY && pj != 7;
  const int h = j + 8 * hsel, mt = (rr >> 2) + (isY ? 4 : 0), pu = (hsel * 8 + j) * 4 + wid;
  constexpr unsigned m0d = 131072 + 24576;
  const unsigned lA = isY ? 65536 : 0, lW = lA ^ 65536, po = isY ? 0 : 32768;
  unsigned mA = lA + wid * 1024 + po, mW = lW + wid * 1024 + po, ofA = isY ? ((j & 1) * 4 + (pj >> 1)) * 256 * DA * 2 : mt * 256 * DC * 2,
           ofW = (isY ? (h & 14) | (pj & 1) : h) * 512;
  const unsigned lda = isY ? DA : DC;
  unsigned g0 = (rr + (rr >> 2)) & 3;
  {
    asm volatile("global_load_dword v78, %0, %2\n\tglobal_load_dword v79, %1, %2 offset:1024\n\tglobal_load_dword v77, %3, %4"
                 ::"v"(tid * 4), "v"(lane * 4), "s"(&g_tab.blk[b]), "v"((lane < 27 ? lane : 27) * 4), "s"(kap) : "memory");
    const unsigned ra = wid * 8 + (lane >> 3), sa = ((lane & 7) ^ (((wid & 1) * 4 + (lane >> 4)) & 7)) * 16;
    const unsigned vbA = ra * (lda * 2) + sa, vbB = (4 * wid + (lane >> 4)) * LDW * 2 + 16 * ((lane & 15) ^ (((lane >> 4) << 2) | wid));
    vsetu<92>(vbA); vsetu<93>(vbB); vsetu<94>(ra * (DC * 2) + sa);
    sset<65>(128 * lda);
    asm volatile("s_mov_b32 s74, 0x40000\n\ts_mov_b32 s53, 0x80000");
    sset<58>(256 * lda * 2); sset<62>((lda - 1) * LDW * 2 + 512);
    asm volatile("s_mov_b32 s59, 0x20000\n\ts_mov_b32 s63, 0x20000");
    g_pro<1>(g0, mA, mW, lda);
    asm volatile("" : "+s"(ofA), "+s"(ofW), "+s"(mA), "+s"(mW), "+s"(g0));
    GAP();
  }
  asm volatile("s_bitcmp1_b32 %0, 4\n\ts_waitcnt lgkmcnt(0)\n\ts_cselect_b64 s[98:99], s[84:85], s[82:83]\n\ts_bitcmp1_b32 %0, 7\n\t"
               "s_cselect_b64 s[60:61], s[88:89], s[98:99]\n\ts_cselect_b64 s[56:57], s[86:87], s[80:81]\n\t"
               "s_add_u32 s56, s56, %1\n\ts_addc_u32 s57, s57, 0\n\ts_add_u32 s60, s60, %2\n\ts_addc_u32 s61, s61, 0\n\t"
               "s_mov_b64 s[88:89], s[90:91]\n\ts_mov_b32 s90, %3\n\ts_mov_b32 s91, 0x20000" ::"s"(b), "s"(ofA), "s"(ofW), "i"(WSB) : "scc");
  KST(0, 0);
  AC c;
  {
    GM g;
    PR pr;
    const int ptb = hsel * 8 + j;
    if (doP) prep_load(pr, tid, pj, ptb, sptr<92>(), sptr<94>(), sptr<96>());
    g_pro<2>(g0, mA, mW, lda);
    g_pro(g0 + 1, mA, mW, lda);
    sset<64>(64 * lda); sset<67>(256 * lda); sset<68>(320 * lda);
    asm volatile("s_mov_b32 s71, 0x20000\n\ts_movk_i32 s72, 0x100\n\ts_mov_b32 s73, 0x20100");
    asm volatile("buffer_load_dwordx2 v[120:121], off, s[88:91], %0 sc1" ::"s"(fo(6144)) : "memory");
    GAP();
    {
      const int fr = lane & 15, fq = lane >> 4, q = (lane & 15) >> 2, p = lane & 3;
      vsetu<128>(lA + (wid >> 1) * 16384 + (fr >> 3) * 1024 + (fr & 7) * 128 + ((fq ^ (fr >> 1)) * 16));
      vsetu<129>(lW + (wid & 1) * 8192 + off_b(8 * fq + q, p >> 1) + 8 * (p & 1));
      vsetu<130>(lW + (wid & 1) * 8192 + off_b(8 * fq + 4 + q, p >> 1) + 8 * (p & 1));
      const unsigned one = fq != q ? 0u : (p & 1) ? 0x3f800000u : 0x3f80u;
      vsetu<131>((p & 2) ? 0u : one); vsetu<132>((p & 2) ? one : 0u);
      vsetu<133>((lane >> 2) * 128 + 16 * ((p >> 1) ^ ((lane >> 2) & 6)) + 8 * (p & 1));
      vsetu<134>((fr >> 3) * 2048 + (fq & 1) * 512 + (fq >> 1) * 256 + (fr & 7) * 16);
      vsetu<81>(tid == 0 ? 0u : 0x80000000u);
      vsetu<84>(lane * 16); vsetu<85>(lane * 16 + 65536); vsetu<86>(wid * 8192 + lane * 16); vsetu<87>(wid * 8192 + lane * 16 + 4096);
      vsetu<88>(65536 + wid * 256 + fr * 16); vsetu<89>(16 * wid + fr - 4 * fq);
      asm volatile("s_mov_b64 s[92:93], s[88:89]\n\ts_mov_b64 s[94:95], s[90:91]\n\ts_mov_b32 s75, 0x60000\n\t"
                   "s_mov_b32 s76, 0x40100\n\ts_mov_b32 s77, 0x60100\n\ts_mov_b32 s82, 2");
      asm volatile("s_add_u32 s86, s80, %0\n\ts_addc_u32 s87, s81, 0\n\ts_add_u32 s98, s98, %1\n\ts_addc_u32 s99, s99, 0"
                   ::"s"(mt * 256 * DC * 2), "s"(h * 512) : "scc");
      sset<66>(192 * lda); sset<69>(384 * lda); sset<70>(448 * lda);
      sset<78>(lA + wid * 1024); sset<79>(lW + wid * 1024);
    }
    GAP();
    if (doP) {
      asm volatile("s_waitcnt vmcnt(11)" ::: "memory");
#pragma unroll
      for (int q = 0; q < 7; ++q) asm volatile("" : "+v"(pr.kk[q]));
#pragma unroll
      for (int q = 0; q < 4; ++q) asm volatile("" : "+v"(pr.vv[q]));
      static_for([&](auto Ic) __attribute__((always_inline)) {
        constexpr int I = Ic;
        if constexpr (I < 2) g_pro(g0 + 2 + I, mA, mW, lda);
        if constexpr (I == 2) g_pro2(mA, mW);
        GAP();
        prep_math<I>(pr, tid, lds + 131072);
        GAP();
      }, make_iseq<4>{});
    } else { g_pro(g0 + 2, mA, mW, lda); g_pro(g0 + 3, mA, mW, lda); g_pro2(mA, mW); }
    GAP();
    g.aA[0] = vxor<128, 0>(); g.aA[1] = vxor<128, 64>();
    static_for([&](auto I) { g.aB[I >> 3][I & 7] = vxor<129 + (I >> 3), 32 * (I & 7)>(); }, make_iseq<16>{});
    c.o0 = vxor<131, 0>(); c.o1 = vxor<132, 0>(); c.tb = vxor<133, 0>(); c.vs = vxor<134, 0>();
    asm volatile("v_add_u32 v91, 0x100000, v93\n\ts_waitcnt vmcnt(8) lgkmcnt(0)\n\ts_barrier" ::: "memory");
    if (isY) g_read0<0>(g, make_iseq<16>{});
    else g_read0<1>(g, make_iseq<16>{});
    GAP();
    asm volatile("v_readfirstlane_b32 s48, v120\n\tv_readfirstlane_b32 s49, v121\n\tv_readlane_b32 s80, v78, %0\n\tv_readlane_b32 s81, v78, %1\n\t"
                 "s_mov_b32 s50, 0x4c957f2d\n\ts_mul_i32 s84, s48, s50\n\ts_mul_hi_u32 s85, s48, s50\n\ts_mul_i32 s49, s49, s50\n\t"
                 "s_mul_i32 s48, s48, 0x5851f42d\n\ts_add_u32 s85, s85, s49\n\ts_add_u32 s85, s85, s48\n\ts_add_u32 s84, s84, 0xf767814f\n\t"
                 "s_addc_u32 s85, s85, 0x14057b7e\n\ts_lshr_b64 s[80:81], s[80:81], 12\n\tv_mov_b32 v82, s84\n\tv_mov_b32 v83, s85"
                 ::"i"((int)T_TAB), "i"(T_TAB + 1) : "scc");
    if (isY) asm volatile("s_mov_b64 s[80:81], 0x6543");
    if (doP) prep_store(tid, pj, ptb, (bf16*)sptr<88>() + O_X / 2, lds + 131072);
    asm volatile("s_waitcnt lgkmcnt(0)" ::: "memory");
    GAP();
    if (isY) {
      g_tile<false, 0, 1>(g);
      g_loop<false, true>(g, [&]() __attribute__((always_inline)) {
        tload<80, T_TAB>(); tload<81, T_TAB + 1>();
        fl_set<T_FPREP>();
        asm volatile("s_mov_b64 s[60:61], s[98:99]\n\ts_mov_b32 s62, %0" ::"i"((DC - 1) * LDW * 2 + 512));
      }, [&]() __attribute__((always_inline)) {
        asm volatile("s_mov_b64 s[56:57], s[86:87]\n\ts_mov_b32 s58, %0\n\tv_mov_b32 v92, v94" ::"i"(256 * DC * 2));
        static_for([&](auto J) { asm volatile("s_mov_b32 s%c0, %1" ::"i"(64 + J), "i"((J + 1) * 64 * DC)); }, make_iseq<7>{});
      });
      tload<101, T_OQ>(); sset<83>(__float_as_uint(CS));
      asm volatile("v_mov_b32 v112, v84\n\tv_add_u32 v113, 8192, v84");
      GAP();
      g_tile<false, 0, 2>(g);
      KST(0, 3);
    }
    auto kvtile = [&](auto TRc) __attribute__((always_inline)) {
      constexpr bool TR = TRc;
      if (isY) g_tile<TR, 1, 4>(g);
      else g_tile<TR, 1, 1>(g);
      asm volatile("s_mov_b32 s83, s78");
      g_loop<TR, false>(g, [&]() __attribute__((always_inline)) {
        asm volatile("v_readlane_b32 s49, v78, %1\n\tv_readlane_b32 s48, v78, %0\n\tv_and_b32 v122, 0xffffff00, v82\n\tv_mov_b32 v123, v83\n\t"
                     "s_and_b32 s49, s49, 0xf000000\n\ts_cselect_b32 s49, -2.0, 0\n\tv_or_b32 v121, s49, v81\n\ts_nop 0\n\t"
                     "buffer_store_dwordx2 v[122:123], v121, s[88:91], s48 offen sc1" ::"i"((int)T_CNT), "i"(T_IT + T_ITN + 16) : "memory", "scc");
        if (isY) fl_set<T_FQ>();
        else {
          int pv = pu; asm volatile("" : "+s"(pv));
          if (doA) post_dma(pj, pv, arg_late<const bf16, 12>(), 131072 + wid * 8192);
          else {
            const char* tp = arg_late<const char, 0>() + 1024 * DC * 2 + pv * 32768 + __float_as_uint(vget<84>()) * 8;
            asm volatile("s_mov_b32 m0, %0\n\ts_nop 0" ::"i"(m0d));
#pragma unroll
            for (int i = 0; i < 4; ++i) asm volatile("global_load_lds_dword %0, off" ::"v"(tp + i * 8192) : "memory");
          }
        }
      }, [&]() __attribute__((always_inline)) {
        asm volatile("s_mov_b32 s79, 0x28000\n\ts_mov_b32 s83, s79");
        fl_poll<T_NPRE>();
        if (!isY) {
          int pv = pu; asm volatile("" : "+s"(pv));
          if (doA) {
            const char* tp = arg_late<const char, 9>() + (size_t)(pj * 64 + pv) * (SEQ * 2) + __float_as_uint(vget<84>()) * 4;
            asm volatile("s_mov_b32 m0, %1\n\ts_nop 0\n\tglobal_load_lds_dword %0, off" ::"v"(tp), "i"(m0d) : "memory");
          } else {
            CODE_TOUCH("at_code_begin", "pf_code_end", (pv >> 5) * 8 + (pv & 3) * 2, 16u, 64, m0d);
            CODE_TOUCH("at_code_begin", "pf_code_end", (pv >> 5) * 8 + (pv & 3) * 2 + 1, 16u, 64, m0d);
          }
        }
      });
      tload<101, T_OG>(); tload<96, T_Q0>(); tload<98, T_K0>();
      fl_check();
      tload<50, T_NST>(); tload<51, T_NST + 1>();
      asm volatile("v_mov_b32 v112, v84\n\tv_add_u32 v113, %0, v84\n\tv_mov_b32 v90, v87\n\tv_mov_b32 v91, v87\n\tv_mov_b32 v92, v87\n\tv_mov_b32 v93, v87\n\t"
                   "v_mov_b32 v94, v86\n\tv_mov_b32 v95, v86\n\tv_mov_b32 v126, v86\n\tv_mov_b32 v127, v86\n\ts_mov_b32 s99, 0x8000\n\t" FL_ARM("s[50:51]")
                   ::"i"(TR ? 4096 : 8192));
      if (doA) {
        const int d = pj * 64 + pu;
        const bf16* dp = arg_late<const bf16, 10>() + d;
        asm volatile("global_load_ushort v123, %0, off" ::"v"(dp) : "memory");
        srd_set<56>(arg_late<const bf16, 9>() + (size_t)d * SEQ, SEQ * 2);
        srd_set<60>(arg_late<const char, 5>() + O_X + d * SEQ * 2, SEQ * 2);
        sset<78>(65536 + wid * 15488);
        asm volatile("s_movk_i32 s64, 992\n\tv_subrev_u32 v122, 32, v84");
        GAP();
        g_tile<TR, 0, 3>(g);
      } else {
        sset<79>(isY ? wid * 8192 : 0x30000);
        if (tget<T_IT + T_ITN + 16>() & 16) asm volatile("s_add_u32 s98, s98, 0x8000\n\ts_mov_b32 s97, 0xffff8000" ::: "scc");
        else asm volatile("s_mov_b32 s97, 0x8000");
        GAP();
        g_tile<TR, 0, 5>(g);
      }
    };
    if (kv) kvtile(std::true_type{});
    else kvtile(std::false_type{});
  }
  KST(0, 4);
  asm volatile("s_waitcnt vmcnt(16)" ::: "memory");
  const unsigned md0 = tget<T_IT + 16>(), md1 = tget<T_IT + T_ITN + 16>();
  sset<79>(wid * 8192 + (md0 & 16) * 2048);
  if (doA) {
    const float Dd = bfl(__float_as_uint(vget<123>()));
    KST(0, 5);
    conv_wave<T_FG>(O_Z + pj * 64 * SEQ * 2 + ((pu & 7) * 8 + (pu >> 3)) * 64, pu >> 3, Dd, lds + 65536 + wid * 15488);
    const char* tp = arg_late<const char, 11>() + (size_t)h * (HD * DA * 2) + ((pj * 4 + wid) * 64 + lane) * 128;
    asm volatile("s_mov_b32 m0, %1\n\ts_nop 0\n\tglobal_load_lds_dword %0, off\n\ts_waitcnt vmcnt(17)" ::"v"(tp), "i"(m0d) : "memory");
    KST(0, 6);
  } else if (!isY) {
    static_for([&](auto I) { d_store<16 + I>(); }, make_iseq<16>{});
    tload<98, T_K0>();
    asm volatile("s_waitcnt vmcnt(0)\n\ts_barrier" ::: "memory");
    fl_set<T_FG>();
    KST(0, 5);
    fl_check();
    CODE_TOUCH("att_begin", "pf_code_end", hsel * 4 + wid, 8u, 64, m0d);
    dma4k<0>(); dma4k<1>(); dma4k<2>(); dma4k<3>();
    asm volatile("s_waitcnt vmcnt(0)" ::: "memory");
    KST(0, 6);
  } else {
    tload<76, T_PART>();
    fl_check();
    asm volatile("buffer_load_dwordx2 v[122:123], v88, s[88:91], s76 offen offset:1024 sc1" ::: "memory");
  }
  sset<78>(65536 + wid * 8192); sset<79>(98304 + wid * 8192);
  { const bf16* wp = arg_late<const bf16, 11>() + ((md1 >> 24) & 15) * HD * DA; asm volatile("s_mov_b64 s[80:81], %0" ::"s"(wp)); }
  asm volatile("att_begin:");
  ev_code(c);
  asm volatile("s_waitcnt lgkmcnt(0)\n\ts_barrier" ::: "memory");
#pragma unroll 1
  for (int it = md0 ? 0 : 1; it < 2; ++it) {
    const unsigned mode = it ? md1 : md0;
    const bool odd = mode & 16;
    if (odd) kreads<1>(make_iseq<8>{});
    else kreads<0>(make_iseq<8>{});
    const unsigned oP = tget<T_PART>();
    if (it == 0) {
      static_for([&](auto I) { tload<57 + I, T_IT + I>(); }, make_iseq<21>{});
      tload<S_T, T_IT + 21>(); tload<S_EV, T_IT + 22>();
    } else {
      static_for([&](auto I) { tload<57 + I, T_IT + T_ITN + I>(); }, make_iseq<21>{});
      tload<S_T, T_IT + T_ITN + 21>(); tload<S_EV, T_IT + T_ITN + 22>();
    }
    asm volatile("s_add_u32 s98, s97, 0x8000\n\ts_mov_b32 s56, -1\n\tv_mov_b64 v[114:115], 0\n\tv_mov_b64 v[118:119], 0\n\t"
                 "v_mov_b32 v120, 0x45800000" ::: "scc");
    KST(0, 8 + 3 * (it && !isY));
    GAP();
#define STA(k)
#define STI()
#define LOOP_ENDS "s_bfe_u32 s76, s73, 0x80008"
    STA(0);
    if (odd) {
      if (isY) phaseA<1, 2>(c);
      else phaseA<1, 1>(c);
      STA(1);
      STA(2);
      phaseB0<1>(mode, isY);
      STA(3);
      asm volatile(LOOP_ENDS "\n\ts_branch att_mid" ::: "scc");
    } else {
      if (isY) phaseA<0, 2>(c);
      else phaseA<0, 1>(c);
      STA(1);
      if (__builtin_expect(mode & 4, 0)) EV_CALL("ev_turn");
      STA(2);
      phaseB0<0>(mode, isY);
      if (__builtin_expect(mode & 4, 0)) { EV_CALL("ev_post"); EV_CALL("ev_mask"); acc_zero(); asm volatile("s_branch att_done"); }
      STA(3);
    }
    GAP();
    asm volatile(LOOP_ENDS "\natt_loop:" ::: "scc");
    GAP();
    phaseA<1, 0>(c);
    STI();
    if (__builtin_expect(c.rare != 0, 0)) sm_redo<1>();
    phaseB<1>(c);
    asm volatile("s_cbranch_scc1 att_done\natt_mid:");
    phaseA<0, 0>(c);
    STI();
    if (__builtin_expect(c.rare != 0, 0)) sm_redo<0>();
    phaseB<0>(c);
    asm volatile("s_cbranch_scc0 att_loop\natt_done:");
    GAP();
    STA(4);
    if (it == 0) phaseA<0, 3>(c);
    else {
      sset<76>(oP);
      GAP();
      phaseAL(c, make_iseq<32>{});
      if (__builtin_expect(redo(), 0)) sm_redo<1, false>();
      if (mode & 2) phaseBL<true>(c, make_iseq<64>{});
      else phaseBL<false>(c, make_iseq<64>{});
    }
    if (__builtin_expect(redo(), 0)) {
      if (it) static_for([&](auto I) { vset<R_S + 32 + (I >> 2) * 8 + (I & 3)>(-INFINITY); }, make_iseq<16>{});
      asm volatile("s_nop 7");
      sm_redo<0, false>();
    }
    STA(5);
    KST(0, 9 + 3 * (it && !isY));
    float l[2];
    asm volatile("v_add_f32 %0, v%c2, v%c3\n\tv_add_f32 %1, v%c4, v%c5" : "=v"(l[0]), "=v"(l[1]) : "i"(R_RS), "i"(R_TS), "i"(R_RS + 1), "i"(R_TS + 1));
    const float cm[2] = {vget<R_M>(), vget<R_M + 1>()};
    l[0] = row_reduce<false>(l[0]); l[1] = row_reduce<false>(l[1]);
    EP p;
    if (it == 0) {
      const f32x4 ml = {cm[0], l[0], cm[1], l[1]};
      asm volatile("buffer_store_dwordx4 %0, v88, s[88:91], %1 offen sc1\n\ts_mov_b32 s101, %2" ::"v"(ml), "s"(oP), "s"(oP + wid * 16384) : "memory");
      phaseF(c, p, make_iseq<74>{});
      asm volatile(FL_POLL "\n\ts_waitcnt vmcnt(17)" ::: "memory");
    } else {
      if (!(mode & 2)) {
        p.fa[0] = __builtin_amdgcn_rcpf(l[0]); p.fa[1] = __builtin_amdgcn_rcpf(l[1]);
        phaseFL<1>(p);
      } else {
        const f32x4 ml = {vget<122>(), vget<123>(), vget<124>(), vget<125>()};
        if (__builtin_expect(__builtin_amdgcn_ballot_w64(ml[0] != cm[0] || ml[2] != cm[1]) == 0, 1)) {
          p.fa[0] = __builtin_amdgcn_rcpf(l[0] + ml[1]); p.fa[1] = __builtin_amdgcn_rcpf(l[1] + ml[3]);
          phaseFL<3>(p);
        } else {
#pragma unroll
          for (int e = 0; e < 2; ++e) {
            const float mm = fmaxf(cm[e], ml[2 * e]);
            const float wa = __builtin_amdgcn_exp2f(cm[e] - mm), wb = __builtin_amdgcn_exp2f(ml[2 * e] - mm);
            const float inv = __builtin_amdgcn_rcpf(l[e] * wa + ml[2 * e + 1] * wb);
            p.fa[e] = wa * inv; p.fb[e] = wb * inv;
          }
          phaseFL<2>(p);
        }
      }
    }
    KST(0, 10 + 3 * (it && !isY));
  }
  asm volatile(TICKET "\n\tv_xor_b32 v%c0, s100, v%c0\np_loop:" ::"i"(R_TB) : "memory");
  GAP();
  p_half<0, true>(make_iseq<32>{});
  asm volatile("s_cbranch_scc0 p_mid");
  p_half<1, true>(make_iseq<32>{});
  asm volatile("s_branch p_loop\np_loop2:");
  p_half<0, false>(make_iseq<32>{});
  asm volatile("p_mid:");
  p_half<1, false>(make_iseq<32>{});
  asm volatile("s_cbranch_scc1 p_loop2\n\ts_nop 5");
  GAP();
  p_cvt<1, 0>(); p_cvt<1, 1>(); p_cvt<1, 2>(); p_cvt<1, 3>();
  p_store<1, 1>();
  GAP();
  KST(0, 27);
  {
    constexpr unsigned rdy = 98304;
    int lz = threadIdx.x & 63; asm volatile("" : "+v"(lz));
    const int fr = lz & 15, fq = lz >> 4, qbF = md1 >> 28;
    if (wid == 0) {
#pragma unroll 1
      for (int q = 0; q < 0x100000; ++q) {
        unsigned lo, hi, tl, th;
        asm volatile("v_readfirstlane_b32 %0, v240\n\tv_readfirstlane_b32 %1, v241\n\ts_mov_b32 %2, s84\n\ts_mov_b32 %3, s85" : "=s"(lo), "=s"(hi), "=s"(tl), "=s"(th));
        if (__builtin_expect((((lo ^ tl) >> 8) | (hi ^ th)) == 0, 1)) break;
        asm volatile("s_sleep 4\n\tv_mov_b64 v[240:241], 1\n\t" TICKET "\n\ts_waitcnt vmcnt(0)" ::: "memory");
      }
      asm volatile("v_mov_b32 v241, 0\n\ts_mov_b64 exec, 1\n\tds_write_b64 %0, v[240:241]\n\ts_mov_b64 exec, -1" ::"v"(rdy) : "memory");
    }
    vsetu<79>(fo(1024) + qbF * 4352 + fr * 8);
    {
      const int jj = 4 * (fr >> 3) + (fr & 3);
      const unsigned one = ((fq ^ (fr >> 2)) & 1) ? 0u : (jj & 1) ? 0x3f800000u : 0x3f80u;
      static_for([&](auto I) { vsetu<96 + I>((jj >> 1) == I ? one : 0u); }, make_iseq<4>{});
    }
    unsigned vo = (fq >> 1) * 1024 + (fq & 1) * 256 + fr * 16;
    bf16* out = arg_late<bf16, 13>();
    __amdgpu_buffer_rsrc_t so_ = __builtin_amdgcn_make_buffer_rsrc((void*)out, (short)0, SEQ * OUTW * 2, 0x00020000);
    unsigned vw = ((qbF * 128 + (fr & 7)) * OUTW + DA + (fr >> 3) * 32 + fq * 8) * 2;
    asm volatile("" : "+s"(so_));
    asm volatile("s_waitcnt vmcnt(0) lgkmcnt(0)\n\ts_barrier" : "+v"(vo), "+v"(vw) :: "memory");
    fl_set<T_FS>();
    unsigned tk;
    asm volatile("ds_read_b32 %0, %1\n\ts_waitcnt lgkmcnt(0)" : "=v"(tk) : "v"(rdy) : "memory");
    tk = __builtin_amdgcn_readfirstlane(tk) & 255;
    KST(0, 30);
    if (doA) post_math(pj, pu, out, lds + 131072 + wid * 8192);
#define N3 0
#define N2 12
    constexpr int N1 = 28 - 3 * N3 - 2 * N2;
    static_assert(N1 >= 0 && N3 + N2 + N1 <= 16);
    const int nu = tk < N3 ? 3 : tk < N3 + N2 ? 2 : tk < N3 + N2 + N1 ? 1 : 0;
    const int u0 = 4 * (tk < N3 ? 3 * tk : tk < N3 + N2 ? N3 + 2 * tk : 2 * N3 + N2 + tk) + wid * nu;
    KST(0, 28);
    if (nu) {
      const unsigned so0 = O_S + qbF * 0x200000 + (16 + u0) * 2048, so[3] = {so0, so0, so0};
      unsigned pend = 0x5555u, cnt = 0x400000;
      while (true) {
        unsigned nw;
        if (wid == 3) {
          u64 m;
          const u64 ex = pend | (pend << 1);
          asm volatile(FL_ARM("%1") "\n\t" FL_POLL "\n\ts_waitcnt vmcnt(0)\n\tv_cmp_eq_u64 %0, s[84:85], v[124:125]" : "=s"(m) : "s"(ex) : "memory");
          nw = (unsigned)m & ((unsigned)m >> 1) & pend;
          if (nw) asm volatile("ds_write_b32 %0, %1 offset:4" ::"v"(rdy), "v"(~pend | nw) : "memory");
        } else {
          unsigned t;
          asm volatile("ds_read_b32 %0, %1 offset:4\n\ts_waitcnt lgkmcnt(0)" : "=v"(t) : "v"(rdy) : "memory");
          nw = __builtin_amdgcn_readfirstlane(t) & pend;
        }
        if (--cnt == 0) nw = pend;
        if (nw == 0) {
          if (wid == 3) asm volatile("s_sleep 4");
          else asm volatile("s_sleep 1");
          continue;
        }
        asm volatile("s_nop 4");
        static_for([&](auto S) {
          if ((nw >> (2 * S)) & 1) {

            r_load<0, 2 * S>(vo, so[0]); r_load<0, 2 * S + 1>(vo, so[0]);
            if (nu > 1) { r_load<1, 2 * S>(vo, so[1]); r_load<1, 2 * S + 1>(vo, so[1]); }
            if (nu > 2) { r_load<2, 2 * S>(vo, so[2]); r_load<2, 2 * S + 1>(vo, so[2]); }
          }
        }, make_iseq<8>{});
        pend &= ~nw;
        if (pend == 0) break;
      }
      auto oo = [&](int U) { return ((((U >> 1) & 1) * 64 + ((U >> 2) & 3) * 16 + (U & 1) * 8) * OUTW + (U >> 4) * 64) * 2; };
      unsigned o0 = oo(u0), o1 = oo(u0 + 1), o2 = oo(u0 + 2);
      asm volatile("s_waitcnt vmcnt(0)" : "+s"(o0), "+s"(o1), "+s"(o2), "+s"(so_) :: "memory");
      KST(0, 16);
      if (nu > 2) r_sums<2>();
      if (nu > 1) r_sums<1>();
      r_sums<0>();
      if (nu > 2) r_fin<2>(vw, so_, o2);
      if (nu > 1) r_fin<1>(vw, so_, o1);
      asm volatile("s_nop 7");
      r_fin<0>(vw, so_, o0);
      if (qbF == 15 && tk == 0) asm volatile(FL_SET("%0") ::"s"(fo(6144)) : "memory");
    }
    KST(0, 29);
  }
  asm volatile("pf_code_end:");
  KST(0, 1);
}

extern "C" void kernel_launch(void* const* d_in, const int* in_sizes, int n_in, void* d_out, int out_size, void* d_ws,
                              size_t ws_size, hipStream_t stream) {
  const bf16* Aq = (const bf16*)d_in[0];
  const bf16* Ak = (const bf16*)d_in[1];
  const bf16* Av = (const bf16*)d_in[2];
  const bf16* Ash = (const bf16*)d_in[3];
  const bf16* Assmk = (const bf16*)d_in[4];
  const bf16* AssmD = (const bf16*)d_in[5];
  const bf16* Bx = (const bf16*)d_in[6];
  const bf16* Bckv = (const bf16*)d_in[7];
  const bf16* Bwq = (const bf16*)d_in[8];
  const bf16* Bwuk = (const bf16*)d_in[9];
  const bf16* Bwuv = (const bf16*)d_in[10];
  const bf16* Bwo = (const bf16*)d_in[11];
  bf16* out = (bf16*)d_out;
  hipFuncSetAttribute((const void*)fused_kernel, hipFuncAttributeMaxDynamicSharedMemorySize, 163840);
  fused_kernel<<<256, 256, 163840, stream>>>(Bckv, Bwuk, Bwuv, Bx, Bwq, (bf16*)d_ws, Ak, Av, Ash, Assmk, AssmD, Bwo, Aq, out);
}
```

```cpp
#include <hip/hip_runtime.h>
#include <hip/hip_bf16.h>
#include <stdint.h>
#include <utility>

typedef __hip_bfloat16 bf16;
typedef __attribute__((ext_vector_type(4))) short bf16x4;
typedef __attribute__((ext_vector_type(2))) unsigned u32x2;

constexpr int SEQ = 2048;
constexpr int DA = 448;
constexpr int NH = 16;
constexpr int HD = 256;
constexpr int DC = 1024;
constexpr int OUTW = 896;
constexpr unsigned TSB = NH * SEQ * HD * 2, O_K = TSB, O_S = 3 * TSB, O_P = 5 * TSB, PSTR = 67584, O_F = O_P + 128 * PSTR,
                   O_X = O_F + 400 * 4352, O_Z = O_X + DA * SEQ * 2, WSB = O_Z + DA * SEQ * 2;
constexpr unsigned fo(int w) { return O_F + (w >> 4) * 4352 + (w & 15) * 8; }

#define KST(kid, which)

__device__ __forceinline__ float bf2f(unsigned short u) { return __uint_as_float(((unsigned)u) << 16); }
__device__ __forceinline__ unsigned short f2bf(float f) { return __bfloat16_as_ushort(__float2bfloat16(f)); }
__device__ __forceinline__ float rbf(float f) { return bf2f(f2bf(f)); }
typedef __attribute__((ext_vector_type(2))) __bf16 bf16x2n;
typedef __attribute__((ext_vector_type(2))) float f32x2n;
__device__ __forceinline__ unsigned pk2(float a, float b) {
  return __builtin_bit_cast(unsigned, __builtin_convertvector((f32x2n){a, b}, bf16x2n));
}

__device__ __forceinline__ int off_b(int row, int ch) { return 256 * row + 16 * (ch ^ (((row & 3) << 2) | ((row >> 2) & 3))); }

template <int R> __device__ __forceinline__ void sset(unsigned x) { asm volatile("s_mov_b32 s%c0, %1" ::"i"(R), "s"(x)); }
template <int R> __device__ __forceinline__ void vsetu(unsigned x) { asm volatile("v_mov_b32 v%c0, %1" ::"i"(R), "v"(x)); }
template <int R> __device__ __forceinline__ void srd_set(const void* p, unsigned n) {
  asm volatile("s_mov_b64 s[%c0:%c1], %4\n\ts_mov_b32 s%c2, %5\n\ts_mov_b32 s%c3, 0x20000" ::"i"(R), "i"(R + 1), "i"(R + 2), "i"(R + 3), "s"(p), "s"(n));
}
template <int R, int L> __device__ __forceinline__ void tload() { asm volatile("v_readlane_b32 s%c0, v78, %1" ::"i"(R), "i"(L)); }
template <int L> __device__ __forceinline__ unsigned tget() { unsigned t; asm volatile("v_readlane_b32 %0, v78, %1" : "=s"(t) : "i"(L)); return t; }
#define FL_SET(S) "buffer_store_dwordx2 v[82:83], v81, s[88:91], " S " offen sc1"
#define FL_ARM(N) "v_cndmask_b32_e64 v80, -2.0, v79, " N
#define FL_POLL "buffer_load_dwordx2 v[124:125], v80, s[88:91], 0 offen sc1"
#define FL_CHECK(N) "s_mov_b32 s48, 0x100000\n7:\n\tv_cmp_eq_u64 vcc, s[84:85], v[124:125]\n\ts_and_b64 vcc, vcc, " N "\n\ts_cmp_eq_u64 vcc, " N \
                    "\n\ts_cbranch_scc1 8f\n\ts_sleep 2\n\t" FL_POLL "\n\ts_waitcnt vmcnt(0)\n\ts_sub_u32 s48, s48, 1\n\ts_cbranch_scc0 7b\n8:"
template <int L> __device__ __forceinline__ void fl_set() {
  asm volatile("v_readlane_b32 s48, v78, %0\n\ts_nop 4\n\t" FL_SET("s48") ::"i"(L) : "memory");
}
template <int L> __device__ __forceinline__ void fl_poll() {
  asm volatile("v_readlane_b32 s50, v78, %0\n\tv_readlane_b32 s51, v78, %1\n\ts_nop 1\n\t" FL_ARM("s[50:51]") "\n\t" FL_POLL ::"i"(L), "i"(L + 1) : "memory");
}
#define TICKET "buffer_atomic_add_x2 v[240:241], v81, s[88:91], s101 offen sc0"
__device__ __forceinline__ void fl_check() { asm volatile(FL_CHECK("s[50:51]") ::: "vcc", "scc", "memory"); }
template <int R, int K> __device__ __forceinline__ unsigned vxor() { unsigned t; asm volatile("v_xor_b32 %0, %1, v%c2" : "=v"(t) : "i"(K), "i"(R)); return t; }
template <int R> __device__ __forceinline__ float vget() { float t; asm volatile("v_mov_b32 %0, v%c1" : "=v"(t) : "i"(R)); return t; }
template <int R> __device__ __forceinline__ void vset(float x) { asm volatile("v_mov_b32 v%c0, %1" ::"i"(R), "v"(x)); }
template <int R> __device__ __forceinline__ float acc_read1() { float t; asm volatile("v_accvgpr_read_b32 %0, a[%c1]" : "=v"(t) : "i"(R)); return t; }
template <class F, int... I> __device__ __forceinline__ void static_for(F&& f, std::integer_sequence<int, I...>) {
  (f(std::integral_constant<int, I>{}), ...);
}
template <int... Is> using iseq = std::integer_sequence<int, Is...>;
template <int N> using make_iseq = std::make_integer_sequence<int, N>;

typedef __attribute__((ext_vector_type(4))) unsigned u32x4c;
typedef __attribute__((ext_vector_type(4))) float f32x4c;
typedef __attribute__((ext_vector_type(2))) float f32x2c;
typedef unsigned long long u64;
struct PR { u32x4c kk[7], vv[4]; };
__device__ __forceinline__ void prep_load(PR& p, int tid, int cg, int tb, const bf16* __restrict__ k, const bf16* __restrict__ v,
                                          const bf16* __restrict__ shift_k) {
  const __amdgpu_buffer_rsrc_t sk = __builtin_amdgcn_make_buffer_rsrc((void*)k, (short)0, SEQ * DA * 2, 0x00020000);
  const __amdgpu_buffer_rsrc_t sv = __builtin_amdgcn_make_buffer_rsrc((void*)v, (short)0, SEQ * DA * 2, 0x00020000);
  const __amdgpu_buffer_rsrc_t sw = __builtin_amdgcn_make_buffer_rsrc((void*)(shift_k + cg * 256), (short)0, 512, 0x00020000);
  const int vo = ((tb * 128 + (tid >> 3) * 4) * DA + cg * 64 + (tid & 7) * 8) * 2;
#pragma unroll
  for (int q = 0; q < 7; ++q)
    asm volatile("buffer_load_dwordx4 %0, %1, %2, 0 offen" : "=v"(p.kk[q]) : "v"(vo + (q - 3) * DA * 2), "s"(sk) : "memory");
#pragma unroll
  for (int q = 0; q < 4; ++q)
    asm volatile("buffer_load_dwordx4 %0, %1, %2, 0 offen offset:%3" : "=v"(p.vv[q]) : "v"(vo), "s"(sv), "i"(q * DA * 2) : "memory");
#pragma unroll
  for (int q = 0; q < 4; ++q)
    asm volatile("buffer_load_dwordx4 a[%c0:%c1], %2, %3, 0 offen offset:%4" ::"i"(4 * q), "i"(4 * q + 3), "v"((tid & 7) * 64), "s"(sw), "i"(q * 16) : "memory");
}
__device__ __forceinline__ float bfl(unsigned u) { return __uint_as_float(u << 16); }
__device__ __forceinline__ float bfh(unsigned u) { return __uint_as_float(u & 0xffff0000u); }
__device__ __forceinline__ f32x2c rbf2(f32x2c a) {
  return (f32x2c){__uint_as_float(pk2(0.f, a[0])), __uint_as_float(pk2(0.f, a[1]))};
}
template <int I> __device__ __forceinline__ void prep_math(const PR& p, int tid, char* lx) {
  const int rp = tid >> 3, c = tid & 7;
  f32x2c wt[4], kf[7];
  const unsigned w[4] = {__float_as_uint(acc_read1<4 * I>()), __float_as_uint(acc_read1<4 * I + 1>()), __float_as_uint(acc_read1<4 * I + 2>()),
                         __float_as_uint(acc_read1<4 * I + 3>())};
#pragma unroll
  for (int t = 0; t < 4; ++t)
    wt[t] = (t & 1) ? (f32x2c){bfh(w[t >> 1]), bfh(w[2 + (t >> 1)])} : (f32x2c){bfl(w[t >> 1]), bfl(w[2 + (t >> 1)])};
#pragma unroll
  for (int q = 0; q < 7; ++q) kf[q] = (f32x2c){bfl(p.kk[q][I]), bfh(p.kk[q][I])};
  f32x2c x[4];
#pragma unroll
  for (int e = 0; e < 4; ++e) {
    f32x2c s = rbf2(kf[e] * wt[3]);
    s = rbf2(s + rbf2(kf[e + 1] * wt[2]));
    s = rbf2(s + rbf2(kf[e + 2] * wt[1]));
    s = rbf2(s + rbf2(kf[e + 3] * wt[0]));
    x[e] = s * (f32x2c){bfl(p.vv[e][I]), bfh(p.vv[e][I])};
  }
#pragma unroll
  for (int u = 0; u < 2; ++u)
    *(u32x2*)(lx + (c * 8 + 2 * I + u) * 256 + (((31 - rp) * 8) ^ (c << 4))) = (u32x2){pk2(x[3][u], x[2][u]), pk2(x[1][u], x[0][u])};
}
__device__ __forceinline__ void prep_store(int tid, int cg, int tb, bf16* __restrict__ xr, const char* lx) {
  const __amdgpu_buffer_rsrc_t sx = __builtin_amdgcn_make_buffer_rsrc((void*)(xr + (size_t)cg * 64 * SEQ), (short)0, 64 * SEQ * 2, 0x00020000);
#pragma unroll
  for (int rd = 0; rd < 4; ++rd) {
    const int idx = tid + 256 * rd, ch = idx >> 4, q = idx & 15;
    u32x4c w = *(const u32x4c*)(lx + ch * 256 + 16 * (q ^ (ch >> 3)));
    __builtin_amdgcn_raw_buffer_store_b128(w, sx, (ch * SEQ + (15 - tb) * 128 + q * 8) * 2, 0, 16);
  }
}

#define CODE_TOUCH(LB, LE, wx, NW, NL, m0v) do { unsigned blo_, bhi_, elo_; \
    asm volatile("s_getpc_b64 s[48:49]\n\ts_add_u32 s48, s48, " LB "@rel32@lo+4\n\ts_addc_u32 s49, s49, " LB "@rel32@hi+12\n\t" \
                 "s_mov_b32 %0, s48\n\ts_mov_b32 %1, s49\n\t" \
                 "s_getpc_b64 s[48:49]\n\ts_add_u32 s48, s48, " LE "@rel32@lo+4\n\ts_mov_b32 %2, s48" \
                 : "=s"(blo_), "=s"(bhi_), "=s"(elo_) :: "scc"); \
    const unsigned n_ = elo_ - (blo_ & ~127u); \
    const __amdgpu_buffer_rsrc_t srd_ = __builtin_amdgcn_make_buffer_rsrc((void*)(((unsigned long long)bhi_ << 32) | (blo_ & ~127u)), (short)0, n_ < 262144u ? n_ : 0u, 0x00020000); \
    const unsigned l_ = threadIdx.x & 63, vo_ = l_ < (NL) ? ((wx) + l_ * (NW)) * 128u : 0x80000000u; \
    asm volatile("s_mov_b32 m0, %0\n\ts_nop 4\n\tbuffer_load_dword %1, %2, 0 offen lds" ::"s"(m0v), "v"(vo_), "s"(srd_) : "memory"); } while (0)
template <int I> __device__ __forceinline__ void d_store() {
  asm volatile("buffer_store_dwordx4 v[%c0:%c1], v%c2, s[88:91], s101 offen offset:%3 sc1" ::"i"(128 + 4 * I), "i"(131 + 4 * I), "i"(112 + ((I >> 2) & 1)),
               "i"((I & 3) * 1024) : "memory");
  if constexpr ((I & 7) == 7) asm volatile("s_addk_i32 s101, 0x4000" ::: "scc");
}
constexpr int NS_X = 19;
struct CV { unsigned pa[4], pb, px, vz, sz; float D; };
constexpr int cv_J(bool up, int q) { int J = up ? 0 : 7; while (q > J) { q -= J + 1; J += up ? 1 : -1; } return J; }
constexpr int cv_i(bool up, int q) { int J = up ? 0 : 7; while (q > J) { q -= J + 1; J += up ? 1 : -1; } return q; }
template <int R, int N> __device__ __forceinline__ void cv_rd(CV& c) {
  if constexpr (N < 16) {
    constexpr int i = N >> 1, o = 16 * R + 128 * (i & 1) + 2 * (N & 1), r = 32 + 32 * (R & 1) + 2 * N;
    asm volatile("ds_read2_b32 a[%c1:%c2], %0 offset0:%c3 offset1:%c4" ::"v"(c.pa[i >> 1]), "i"(r), "i"(r + 1), "i"(o), "i"(o + 1));
  } else {
    constexpr int m = N - 16, r = 192 + 32 * (R & 1) + 4 * m;
    asm volatile("ds_read_b128 a[%c1:%c2], %0 offset:%c3" ::"v"(c.pb), "i"(r), "i"(r + 3), "i"(64 * R + 512 * (7 - m)));
  }
}
template <int O> __device__ __forceinline__ void cv_xd(CV& c) {
  constexpr int e = O & 31, x = 96 + ((e >> 1) ^ 1);
  if constexpr (O >= 64) asm volatile("v_cvt_pk_bf16_f32 v%c0, 0, v%c0" ::"i"(160 + e));
  else if constexpr (O >= 32) asm volatile("v_mul_f32 v%c0, v%c0, %1" ::"i"(160 + e), "v"(c.D));
  else if constexpr (O & 1) asm volatile("v_lshlrev_b32 v%c0, 16, v%c1" ::"i"(160 + e), "i"(x));
  else asm volatile("v_and_b32 v%c0, 0xffff0000, v%c1" ::"i"(160 + e), "i"(x));
}
template <int O> __device__ __forceinline__ void cv_z(CV& c) {
  constexpr int J = O / 11, k = O % 11, y = 128 + 4 * J;
  if constexpr (k < 4) asm volatile("v_cvt_pk_bf16_f32 v%c0, 0, v%c0" ::"i"(y + k));
  else if constexpr (k < 8) asm volatile("v_add_f32 v%c0, v%c0, v%c1" ::"i"(y + k - 4), "i"(y + 28 + k));
  else if constexpr (k < 10) asm volatile("v_cvt_pk_bf16_f32 v%c0, v%c1, v%c2" ::"i"(y + k - 8), "i"(y + 2 * k - 16), "i"(y + 2 * k - 15));
  else asm volatile("buffer_store_dwordx2 v[%c0:%c1], %2, s[88:91], s101 offen sc1\n\ts_add_u32 s101, s101, 0x8000" ::"i"(y), "i"(y + 1), "v"(c.vz) : "memory");
}
constexpr int cv_z0(int Q) {
  int o = 0;
  for (int q = 0; q < Q; ++q)
    for (int k = 0; k < (q % 4 == 1 ? 0 : 3); ++k) { const int J = o / 11; if ((J + 1) * (J + 2) / 2 - 1 + (J ? 4 : 7) <= q) ++o; }
  return o;
}
constexpr int cv_xd0(int T) { int o = 0; for (int t = 0; t < T; ++t) o += t % 4 != 1 || (t >= 72 && t < 108); return o < 96 ? o : 96; }
static_assert(cv_xd0(144) == 96);
template <int B, int... Is> __device__ __forceinline__ void cv_zs(CV& c, iseq<Is...>) { (cv_z<B + Is>(c), ...); }
template <int R, int Q> __device__ __forceinline__ void cv_slot(CV& c) {
  constexpr int i = cv_i(R == 7, Q), J = cv_J(R == 7, Q), d = 128 + 4 * J, a = 32 + 32 * (R & 1) + 4 * i, b = 192 + 32 * (R & 1) + 4 * (J - i);
  constexpr int T = 36 * R + Q, vm = Q % 4 == 1 && Q < 32 ? Q / 4 : -1;
  if constexpr (Q == 0) asm volatile("s_waitcnt lgkmcnt(0)");
  if constexpr (R >= 6 && vm >= 0 && vm % 4 == 0) asm volatile("s_add_u32 m0, s79, %0" ::"i"(vm * 1024));
  if constexpr (R == 0 && i == 0)
    asm volatile("v_mfma_f32_16x16x32_bf16 v[%c0:%c1], a[%c2:%c3], a[%c4:%c5], 0" ::"i"(d), "i"(d + 3), "i"(a), "i"(a + 3), "i"(b), "i"(b + 3));
  else
    asm volatile("v_mfma_f32_16x16x32_bf16 v[%c0:%c1], a[%c2:%c3], a[%c4:%c5], v[%c0:%c1]" ::"i"(d), "i"(d + 3), "i"(a), "i"(a + 3), "i"(b), "i"(b + 3));
  if constexpr (Q < 24 && R < 7) cv_rd<R + 1, Q>(c);
  if constexpr (T % 7 == 3 && NS_X + 5 + T / 7 < 32) d_store<NS_X + 5 + T / 7>();
  if constexpr (R == 2 && Q >= 24 && Q < 32)
    asm volatile("ds_read_b64 v[%c1:%c2], %0 offset:%c3" ::"v"(c.px), "i"(96 + 2 * (Q - 24)), "i"(97 + 2 * (Q - 24)), "i"(512 * (31 - Q)));
  if constexpr (R >= 3 && R < 7) if constexpr (cv_xd0(T - 107) > cv_xd0(T - 108)) cv_xd<cv_xd0(T - 108)>(c);
  if constexpr ((R == 3 || R == 4) && vm >= 0) {
    constexpr int f = (R - 3) * 8 + vm;
    asm volatile("buffer_load_dwordx4 a[%c0:%c1], v84, s[88:91], s96 offen offset:%2 sc1" ::"i"(128 + 4 * f), "i"(131 + 4 * f), "i"((f & 3) * 1024) : "memory");
    if constexpr ((f & 3) == 3) asm volatile("s_addk_i32 s96, %0" ::"i"(f == 7 ? 28672 : 4096));
  }
  if constexpr (R == 4 && Q == 35) asm volatile(FL_POLL ::: "memory");
  if constexpr (R >= 6 && vm >= 0)
    asm volatile("buffer_load_dwordx4 v%c0, s[88:91], s98 offen offset:%1 sc1 lds" ::"i"(86 + vm / 4), "i"((vm & 3) * 1024) : "memory");
  if constexpr (R == 6 && Q == 35) asm volatile("s_add_u32 s98, s98, 0x8000\n\ts_xor_b32 s79, s79, 0x8000");
  if constexpr (R == 7) cv_zs<cv_z0(Q)>(c, make_iseq<cv_z0(Q + 1) - cv_z0(Q)>{});
  __builtin_amdgcn_sched_barrier(0);
}
template <int R, int... Q> __device__ __forceinline__ void cv_step(CV& c, iseq<Q...>) { (cv_slot<R, Q>(c), ...); }
template <int... N> __device__ __forceinline__ void cv_rd0(CV& c, iseq<N...>) { (cv_rd<0, N>(c), ...); }
template <int FLAG> __device__ __forceinline__ void conv_wave(unsigned sz, int ck, float Dd, char* lw) {
  int lane = threadIdx.x & 63; asm volatile("" : "+v"(lane));
  const int l16 = lane & 15, fq = lane >> 4;
  u32x4c w[5]; unsigned w4[5];
#pragma unroll
  for (int r = 0; r < 5; ++r) {
    w[r] = *(const u32x4c*)(lw + r * 1024 + lane * 16);
    w4[r] = *(const unsigned*)(lw + r * 1024 + lane * 16 + 16);
  }
  d_store<NS_X>(); d_store<NS_X + 1>();
#pragma unroll
  for (int r = 0; r < 5; ++r)
    *(u32x4c*)(lw + 10304 + r * 1024 + lane * 16) = (u32x4c){__builtin_amdgcn_alignbit(w[r][1], w[r][0], 16), __builtin_amdgcn_alignbit(w[r][2], w[r][1], 16),
                                                             __builtin_amdgcn_alignbit(w[r][3], w[r][2], 16), __builtin_amdgcn_alignbit(w4[r], w[r][3], 16)};
  d_store<NS_X + 2>();
  CV c;
#pragma unroll
  for (int i = 0; i < 4; ++i) c.pa[i] = (unsigned)(size_t)lw + ((l16 + 1) & 1) * 10304 + 4 * ((8 * fq + l16 + 1) >> 1) + 1024 * i;
  c.pb = (unsigned)(size_t)lw + 5120 + (2032 - 16 * l16 + 8 * fq) * 2 - 512 * 7;
  c.vz = (l16 >> 1) * 4096 + (((l16 & 1) * 2 + (fq >> 1)) ^ (ck >> 1)) * 16 + (fq & 1) * 8;
  c.px = (unsigned)(size_t)lw + 5120 + 2044 * 2 - 512 * 7 - (16 * l16 + 4 * fq) * 2;
  c.sz = sz; c.D = Dd;
  asm volatile("" : "+v"(c.vz), "+v"(c.px), "+v"(c.D) :: "memory");
  asm volatile("" : "+s"(c.sz));
  cv_rd0(c, make_iseq<24>{});
  d_store<NS_X + 3>(); d_store<NS_X + 4>();
  __builtin_amdgcn_sched_barrier(0);
  static_for([&](auto R) __attribute__((always_inline)) {
    cv_step<R>(c, make_iseq<36>{});
    if constexpr (R == 2) {
      asm volatile("s_waitcnt vmcnt(0)\n\ts_barrier" ::: "memory");
      fl_set<FLAG>();
      sset<101>(c.sz);
    }
    if constexpr (R == 5) {
      asm volatile("s_waitcnt vmcnt(0)" ::: "memory");
      fl_check();
    }
    __builtin_amdgcn_sched_barrier(0);
  }, make_iseq<8>{});
  asm volatile("s_nop 7");
  cv_zs<cv_z0(36)>(c, make_iseq<88 - cv_z0(36)>{});
  __builtin_amdgcn_sched_barrier(0);
}

__device__ __forceinline__ void post_dma(int cg, int tb, const bf16* __restrict__ q, unsigned m0w) {
  int lane = threadIdx.x & 63; asm volatile("" : "+v"(lane));
  const __amdgpu_buffer_rsrc_t sq = __builtin_amdgcn_make_buffer_rsrc((void*)(q + (size_t)tb * 32 * DA + cg * 64), (short)0, 32 * DA * 2, 0x00020000);
  const unsigned vq = ((lane >> 3) * DA + (lane & 7) * 8) * 2;
  asm volatile("s_mov_b32 m0, %0\n\ts_nop 4\n\tbuffer_load_dwordx4 %1, %2, 0 offen lds\n\tbuffer_load_dwordx4 %1, %2, %3 offen offset:1024 lds\n\t"
               "buffer_load_dwordx4 %1, %2, %4 offen offset:2048 lds\n\tbuffer_load_dwordx4 %1, %2, %5 offen offset:3072 lds"
               ::"s"(m0w + 4096), "v"(vq), "s"(sq), "s"(8 * DA * 2 - 1024), "s"(16 * DA * 2 - 2048), "s"(24 * DA * 2 - 3072) : "memory");
}
__device__ __forceinline__ void post_math(int cg, int tb, bf16* __restrict__ out, const char* lw) {
  int lane = threadIdx.x & 63; asm volatile("" : "+v"(lane));
  const int tl = lane >> 3, ck = lane & 7;
  const __amdgpu_buffer_rsrc_t so = __builtin_amdgcn_make_buffer_rsrc((void*)(out + (size_t)tb * 32 * OUTW + cg * 64), (short)0, 32 * OUTW * 2 - 2 * DA, 0x00020000);
  const unsigned vo = (tl * OUTW + ck * 8) * 2;
#pragma unroll
  for (int i = 0; i < 4; ++i) {
    const u32x4c qv = *(const u32x4c*)(lw + 4096 + i * 1024 + lane * 16);
    const char* lz = lw + ck * 64 + 16 * (i ^ (ck >> 1)) + tl * 2;
    u32x4c o;
#pragma unroll
    for (int e = 0; e < 4; ++e) {
      const float z0 = bf2f(*(const unsigned short*)(lz + 2 * e * 512));
      const float z1 = bf2f(*(const unsigned short*)(lz + (2 * e + 1) * 512));
      o[e] = pk2(bfl(qv[e]) * z0, bfh(qv[e]) * z1);
    }
    __builtin_amdgcn_raw_buffer_store_b128(o, so, vo + i * 8 * OUTW * 2, 0, 17);
  }
}

#define DI __device__ __forceinline__
#define GAP() __builtin_amdgcn_sched_barrier(0)
typedef __attribute__((ext_vector_type(4))) float f32x4g;
typedef __attribute__((ext_vector_type(4))) unsigned u32x4g;
constexpr int LDW = NH * HD;
struct GM { unsigned aA[2], aB[2][8]; };
template <bool W, int J, int V> DI void g_dma() {
  if constexpr (J == 0) asm volatile("buffer_load_dwordx4 v%c0, s[%c1:%c2], 0 offen lds" ::"i"(V), "i"(W ? 60 : 56), "i"(W ? 63 : 59) : "memory");
  else asm volatile("buffer_load_dwordx4 v%c0, s[%c1:%c2], s%c3 offen lds" ::"i"(V), "i"(W ? 60 : 56), "i"(W ? 63 : 59), "i"((W ? 70 : 63) + J) : "memory");
}
template <int P, int KS, int I> DI void g_wread(GM& g) {
  constexpr int nf = I >> 1, hf = I & 1, r = 192 - 64 * P + 8 * nf + 4 * KS + 2 * hf;
  asm volatile("ds_read_b64_tr_b16 a[%c1:%c2], %0 offset:%c3" ::"v"(g.aB[hf][nf]), "i"(r), "i"(r + 1), "i"(P * 32768 + KS * 16384));
}
template <int P, int M, int KS> DI void g_aread(GM& g) {
  constexpr int r = 96 + 8 * (M & 1) + 4 * KS;
  asm volatile("ds_read_b128 v[%c1:%c2], %0 offset:%c3" ::"v"(g.aA[KS]), "i"(r), "i"(r + 3), "i"(P * 32768 + M * 2048));
}
constexpr float CS = 0.0625f * 1.4426950408889634f;
constexpr int NS_Q = 19;
constexpr int e_vm(int G) { return (G * 3) % 8 < 3 ? G * 3 / 8 : -1; }
constexpr int e_st(int MODE, int G) {
  const int q = G & 15, k3 = ((G >> 4) - 1) * 3 + (q == 0 ? 0 : q == 4 ? 1 : 2), k2 = NS_Q + (G >> 4) * 2 + (q == 4);
  if (MODE == 5) return e_vm(G) >= 32 ? e_vm(G) - 32 : -1;
  if (MODE == 3) return G >= 33 && (G - 33) % 5 == 0 && (G - 33) / 5 < NS_X ? (G - 33) / 5 : -1;
  if (MODE == 2) return G >= 30 && (q == 0 || q == 4 || q == 14) && k3 - 2 < NS_Q ? k3 - 2 : -1;
  if (MODE == 4) return (q == 0 || q == 4) && k2 < 32 ? k2 : -1;
  return -1;
}
constexpr int e_nst(int MODE, int G0, int G1) { int n = 0; for (int G = G0; G < G1; ++G) n += e_st(MODE, G) >= 0; return n; }
static_assert(e_nst(2, 0, 128) == NS_Q && e_nst(3, 30, 128) == NS_X && e_nst(5, 0, 128) == 16 && e_nst(4, 0, 104) == 32 - NS_Q);
constexpr int e_slot(int MODE, int G) { return MODE == 3 ? (G % 3 == 0 && G < 30 ? G / 3 : -1) : e_vm(G) >= 0 && e_vm(G) < 32 ? 10 + e_vm(G) : -1; }
struct ES { int op0[129], nop[129]; };
constexpr ES e_sched(bool TR, int MODE) {
  ES z{};
  const bool SCL = MODE == 2 || MODE == 4;
  const int per = SCL ? 16 : 8, o1 = 32 * per;
  int o = MODE == 4 ? e_sched(false, 2).op0[128] : 0;
  for (int G = 0; G <= 128; ++G) {
    const int q = G & 15;
    int cap = 2 - (q == 1 || q == 2 || q == 15);
    if (SCL) cap -= (q >= 3 && q < 11 && (q & 1)) + (q == 7 || q == 11) + 2 * (G == 70 || G == 74 || (G & 63) == 61);
    cap += MODE == 2 ? 4 + (q == 10 || q == 13) : MODE == 3 ? 1 : MODE == 5 ? 2 : 0;
    if (e_st(MODE, G) >= 0 || (SCL ? q == 8 || q == 12 : e_slot(MODE, G) >= 0) || (MODE == 4 && G < 4)) cap = 0;
    if (G == 128) cap = 10000;
    z.op0[G] = o;
    while (o < o1) {
      const int i = o / per, rdy = MODE == 4 ? 0 : (TR ? 32 * (i >> 3) + 16 + 2 * (i & 7) + 1 : 4 * i + 3) + 6;
      if ((G < rdy && G < 128) || cap-- <= 0) break;
      ++o; ++z.nop[G];
    }
  }
  return z;
}
template <bool TR, int MODE> constexpr ES ESCH = e_sched(TR, MODE);
static_assert(ESCH<false, 4>.op0[96] == 512 && ESCH<false, 4>.op0[0] >= 24 * 16);
template <bool TR, bool SCL, int O> DI void e_op() {
  constexpr int per = SCL ? 16 : 8, i = O / per, k = O % per, r = 128 + 4 * i, t = 114 + 4 * (i & 1), c = k - (per - 4);
  if constexpr (k < 4) asm volatile("v_accvgpr_read_b32 v%c0, a[%c1]" ::"i"(t + k), "i"(4 * i + k));
  else if constexpr (c < 0) asm volatile("v_mul_f32 v%c0, s83, v%c0" ::"i"(k < 8 ? r + (k ^ 6) : t + k - 8));
  else {
    constexpr int d = c == 0 ? r + 3 : c == 1 ? r + 2 : r + c - 2, x = c == 0 ? r + 2 : c == 1 ? r : t + 2 * (c - 2);
    asm volatile("v_cvt_pk_bf16_f32 v%c0, v%c1, v%c2" ::"i"(d), "i"(x), "i"(x + 1));
  }
}
template <bool TR, bool SCL, int B, int... Is> DI void e_ops(iseq<Is...>) { (e_op<TR, SCL, B + Is>(), ...); }
template <int S> DI void e_m0() {
  if constexpr (S == 0 || S == 1 || S == 5 || S == 9) asm volatile("s_add_u32 m0, s78, %0" ::"i"(S * 1024));
  if constexpr (S >= 26 && (S - 26) % 4 == 0) asm volatile("s_add_u32 m0, s79, %0" ::"i"(((S - 26) >> 3) * 32768 + ((S - 26) & 4) * 1024));
}
template <int S> DI void e_load() {
  if constexpr (S == 0) asm volatile("buffer_load_dwordx4 v122, s[56:59], 0 offen lds" ::: "memory");
  else if constexpr (S < 5) asm volatile("buffer_load_dwordx4 v84, s[56:59], s64 offen offset:%0 lds" ::"i"((S - 1) * 1024) : "memory");
  else if constexpr (S < 9) asm volatile("buffer_load_dwordx4 v84, s[60:63], 0 offen offset:%0 sc1 lds" ::"i"((S - 5) * 1024) : "memory");
  else if constexpr (S == 9) asm volatile("buffer_load_dwordx4 v84, s[60:63], s62 offen sc1 lds" ::: "memory");
  else if constexpr (S < 26) {
    constexpr int f = S - 10;
    asm volatile("buffer_load_dwordx4 a[%c0:%c1], v84, s[88:91], s96 offen offset:%2 sc1" ::"i"(128 + 4 * f), "i"(131 + 4 * f), "i"((f & 3) * 1024) : "memory");
    if constexpr ((f & 3) == 3) asm volatile("s_addk_i32 s96, %0" ::"i"(f == 7 ? 28672 : 4096));
  } else {
    constexpr int f = S - 26;
    asm volatile("buffer_load_dwordx4 v%c0, s[88:91], s98 offen offset:%1 sc1 lds" ::"i"(86 + ((f >> 2) & 1)), "i"((f & 3) * 1024) : "memory");
    if constexpr (f == 7) asm volatile("s_add_u32 s98, s98, s97");
    if constexpr (f == 15) asm volatile(FL_POLL ::: "memory");
  }
}
template <bool TR, int P, int H, int G, int MODE> DI void g_gap(GM& g) {
  constexpr int m = 4 * H + (G >> 4), q = G & 15, n = q >> 1, ks = q & 1, GG = 64 * H + G;
  constexpr bool inV = TR ? m & 1 : n & 1, END = MODE == 3 || MODE == 5;
  constexpr int a = 96 + 8 * (m & 1) + 4 * ks, b = 192 - 64 * P + 8 * n + 4 * ks, d = (inV ? 128 : 0) + 4 * (TR ? 8 * (m >> 1) + n : 4 * m + (n >> 1));
  constexpr int J = END ? -1 : q == 8 ? 2 * (G >> 4) : q == 12 ? 2 * (G >> 4) + 1 : -1, gj = (J & 3) ^ ((J & 3) >> 1);
  if constexpr (END) { if constexpr (q == 0 && GG > 0) asm volatile("s_waitcnt lgkmcnt(0)"); e_m0<e_slot(MODE, GG)>(); }
  else if constexpr (q == 0 && G > 0) asm volatile("s_waitcnt lgkmcnt(4)");
  if constexpr (J == 0) asm volatile("s_add_u32 m0, s78, %0" ::"i"((P ^ H ^ 1) * 32768 + (1 - H) * 8192));
  else if constexpr (J == 4) asm volatile("s_add_u32 m0, s79, %0" ::"i"((P ^ H) * 32768 + (1 - H) * 16384));
  else if constexpr (J > 0 && (J & 3) == 1) asm volatile("s_bitset1_b32 m0, 12");
  else if constexpr (J > 0 && (J & 3) == 2) asm volatile("s_bitset1_b32 m0, %0" ::"i"(J < 4 ? 14 : 13));
  else if constexpr (J > 0) asm volatile("s_bitset0_b32 m0, 12");
#define GMF(D, X, Y, C) asm volatile("v_mfma_f32_16x16x32_bf16 " D "[%c0:%c1], " X ", " Y ", " C ::"i"(d), "i"(d + 3), "i"(a), "i"(a + 3), "i"(b), "i"(b + 3))
#define GMF2(D, C) do { if constexpr (TR) GMF(D, "v[%c2:%c3]", "a[%c4:%c5]", C); else GMF(D, "a[%c4:%c5]", "v[%c2:%c3]", C); } while (0)
  if constexpr ((MODE == 1 || MODE == 4) && ks == 0) { if constexpr (inV) GMF2("v", "0"); else GMF2("a", "0"); }
  else if constexpr (inV) GMF2("v", "v[%c0:%c1]");
  else GMF2("a", "a[%c0:%c1]");
#undef GMF2
#undef GMF
  if constexpr ((q == 1 || q == 2) && !(END && m == 7)) g_aread<(m < 7 ? P : P ^ 1), (m + 1) & 7, q - 1>(g);
  if constexpr (!END && q >= 3 && q < 11 && (q & 1)) g_wread<P ^ 1, H, (G >> 4) * 4 + (q - 3) / 2>(g);
  if constexpr (MODE == 1 && GG < 7) {
    if constexpr (GG == 0) asm volatile("s_barrier" ::: "memory");
    if constexpr (GG == 2) asm volatile("s_add_u32 m0, s79, %0" ::"i"(P * 32768));
    if constexpr (GG >= 3) g_dma<true, (GG - 3) ^ ((GG - 3) >> 1), 91>();
    if constexpr (GG == 3 || GG == 4) asm volatile("s_bitset1_b32 m0, %0" ::"i"(9 + GG));
    if constexpr (GG == 5) asm volatile("s_bitset0_b32 m0, 12");
  }
  if constexpr (e_st(MODE, GG) >= 0) {
    static_assert(ESCH<TR, MODE>.op0[GG] >= (e_st(MODE, GG) + 1) * (END ? 8 : 16));
    d_store<e_st(MODE, GG)>();
  }
  if constexpr (J >= 0 && J < 4) g_dma<false, (1 - H) * 2 + (gj & 1) + (gj >> 1) * 4, 90>();
  else if constexpr (J >= 4) g_dma<true, (1 - H) * 4 + gj, 91>();
  if constexpr (MODE >= 2) e_ops<TR, !END, ESCH<TR, MODE>.op0[GG]>(make_iseq<ESCH<TR, MODE>.nop[GG]>{});
  if constexpr (END) {
    if constexpr (e_slot(MODE, GG) >= 0) e_load<e_slot(MODE, GG)>();
  } else {
    if constexpr (H == 1 && G == 6) asm volatile("v_lshl_add_u32 v90, s82, 7, v92\n\ts_and_b32 s82, s80, 15");
    if constexpr (H == 1 && G == 10) asm volatile("s_lshr_b64 s[80:81], s[80:81], 4\n\tv_lshl_add_u32 v91, s82, 19, v93");
    if constexpr (G == 61) {
      constexpr int nst = e_nst(MODE, H ? 29 : 0, GG + 1) + (MODE == 4 && !H ? e_nst(2, 93, 128) : 0);
      asm volatile("s_waitcnt vmcnt(%0) lgkmcnt(0)\n\ts_barrier" ::"i"(12 + nst) : "memory");
    }
  }
  if constexpr (END && GG == 127) {
    asm volatile("s_nop 7");
    e_ops<TR, false, ESCH<TR, MODE>.op0[128]>(make_iseq<ESCH<TR, MODE>.nop[128]>{});
  }
  GAP();
}
template <bool TR, int P, int H, int MODE, int... Gs> DI void g_half_(GM& g, iseq<Gs...>) { (g_gap<TR, P, H, Gs, MODE>(g), ...); }
template <bool TR, int P, int H, int MODE> DI void g_half(GM& g) { g_half_<TR, P, H, MODE>(g, make_iseq<64>{}); }
template <bool TR, int P, int MODE> DI void g_tile(GM& g) { g_half<TR, P, 0, MODE>(g); g_half<TR, P, 1, MODE>(g); }
template <int P, int... Is> DI void g_read0(GM& g, iseq<Is...>) {
  (g_wread<P, 0, Is>(g), ...); (g_wread<P, 1, Is>(g), ...);
  g_aread<P, 0, 0>(g); g_aread<P, 0, 1>(g);
}
DI void g_proW(unsigned g, unsigned mW) {
  asm volatile("v_lshl_add_u32 v91, %0, 18, v93\n\ts_mov_b32 m0, %1" ::"s"(g), "s"(mW));
  asm volatile("s_nop 0"); g_dma<true, 0, 91>();
  asm volatile("s_bitset1_b32 m0, 12\n\ts_nop 0"); g_dma<true, 1, 91>();
  asm volatile("s_bitset1_b32 m0, 13\n\ts_nop 0"); g_dma<true, 3, 91>();
  asm volatile("s_bitset0_b32 m0, 12\n\ts_nop 0"); g_dma<true, 2, 91>();
}
template <int PART = 3> DI void g_pro(unsigned i, unsigned mA, unsigned mW, unsigned lda) {
  i &= 3;
  if constexpr (PART & 1)
    asm volatile("v_add_u32 v90, %0, v92\n\tv_add_u32 v91, %1, v93\n\ts_mov_b32 m0, %2\n\ts_mov_b32 s54, %3" ::"s"(((i & 1) + (i >> 1) * 4) * 64 * lda),
                 "s"((i & 1) * 0x20000 + (i >> 1) * 256), "s"(mA + (i & 1) * 4096 + (i >> 1) * 16384), "s"(mW + i * 4096));
  if constexpr (PART == 1) return;
  g_dma<false, 0, 90>();
  asm volatile("s_bitset1_b32 m0, 13\n\ts_nop 0"); g_dma<false, 2, 90>();
  asm volatile("s_mov_b32 m0, s54\n\ts_nop 0"); g_dma<true, 0, 91>();
  asm volatile("s_bitset1_b32 m0, 14\n\ts_nop 0"); g_dma<true, 4, 91>();
  asm volatile("s_xor_b32 m0, s54, 0x8000\n\ts_nop 0\n\tbuffer_load_dwordx4 v91, s[60:63], s53 offen lds" ::: "memory", "scc");
}
DI void g_pro2(unsigned mA, unsigned mW) {
  asm volatile("v_add_u32 v90, 128, v92\n\ts_mov_b32 m0, %0" ::"s"(mA ^ 32768));
  asm volatile("s_nop 0"); g_dma<false, 0, 90>();
  asm volatile("s_bitset1_b32 m0, 12\n\ts_nop 0"); g_dma<false, 1, 90>();
  asm volatile("s_bitset1_b32 m0, 14\n\ts_nop 0"); g_dma<false, 5, 90>();
  asm volatile("s_bitset0_b32 m0, 12\n\ts_nop 0"); g_dma<false, 4, 90>();
  g_proW(3, mW ^ 49152);
}
template <bool TR, bool SCL, class H0, class H1> DI void g_loop(GM& g, H0&& early, H1&& late) {
  constexpr int nkt = SCL ? 7 : 16, P0 = SCL ? 0 : 1;
#pragma unroll 1
  for (int t = 1;; t += 2) {
    asm volatile("" : "+s"(t));
    g_half<TR, P0 ^ 1, 0, 0>(g);
    if (__builtin_expect(t == (SCL ? 5 : 13), 0)) { late(); GAP(); }
    g_half<TR, P0 ^ 1, 1, 0>(g);
    if (SCL && t + 2 >= nkt) break;
    g_half<TR, P0, 0, 0>(g);
    if (__builtin_expect(t == (SCL ? 3 : 1), 0)) { early(); GAP(); }
    if constexpr (!SCL) asm volatile("s_mov_b32 s78, s83");
    g_half<TR, P0, 1, 0>(g);
    if (!SCL && t + 3 >= nkt) break;
  }
}

typedef __attribute__((ext_vector_type(4))) unsigned u32x4;
typedef __attribute__((ext_vector_type(4))) float f32x4;

constexpr int R_S = 128, R_P = 192, R_CI = 224, R_E = 96;
constexpr int R_TS = 112, R_RS = 114, R_M = 116;
constexpr int R_PD = 118, R_THR = 120, R_TMP = 121;
constexpr int r_dk(int q) { return q < 6 ? 90 + q : 120 + q; }
constexpr int S_V = 96, S_T = 97, S_K = 98, S_I = 99, S_EV = 100;
struct AC {
  unsigned rare;
  unsigned tb, vs, o0, o1;
};
DI bool redo() { u64 t; asm volatile("s_mov_b64 %0, s[54:55]" : "=s"(t)); return t != 0; }
template <int X, int F> DI void kread() {
  constexpr int r = 192 + 4 * (F & 15);
  asm volatile("ds_read_b128 a[%c0:%c1], v84 offset:%c2" ::"i"(r), "i"(r + 3), "i"(X * 32768 + F * 1024));
}
template <int X, int F> DI void vread() {
  constexpr int r = 192 + 4 * (F & 15);
  asm volatile("ds_read_b128 a[%c0:%c1], v85 offset:%c2" ::"i"(r), "i"(r + 3), "i"(X * 32768 + (F & 1) * 16384 + (F >> 1) * 1024));
}
template <int X, int... Fs> DI void kreads(iseq<Fs...>) { (kread<X, Fs>(), ...); }
template <int X, int J> DI void dma_m0() {
  if constexpr (J == 0) asm volatile("s_mov_b32 m0, s%c0" ::"i"(78 + X));
  else if constexpr (J == 4) asm volatile("s_or_b32 m0, s%c0, 0x1000" ::"i"(78 + X));
  else if constexpr (J == 8) asm volatile("s_bitset0_b32 m0, 16");
  else if constexpr (J == 12) asm volatile("s_bitset0_b32 m0, 12");
}
template <int J> DI void dma_at() {
  if constexpr (J < 8)
    asm volatile("buffer_load_dwordx4 v%c0, s[88:91], s96 offen offset:%1 sc1 lds" ::"i"(86 + (J >= 4)), "i"((J & 3) * 1024) : "memory");
  else
    asm volatile("buffer_load_dwordx4 v%c0, s[92:95], s98 offen offset:%1 sc1 lds" ::"i"(r_dk(J - 8)), "i"((J & 3) * 1024) : "memory");
}
template <int Q> DI void dma4k() {
  asm volatile("s_xor_b32 m0, s79, %0\n\ts_add_u32 s48, s98, %1\n\tbuffer_load_dwordx4 v%c2, s[88:91], s48 offen sc1 lds\n\t"
               "buffer_load_dwordx4 v%c2, s[88:91], s48 offen offset:1024 sc1 lds\n\tbuffer_load_dwordx4 v%c2, s[88:91], s48 offen offset:2048 sc1 lds\n\t"
               "buffer_load_dwordx4 v%c2, s[88:91], s48 offen offset:3072 sc1 lds"
               ::"i"((Q >> 1) * 32768 + (Q & 1) * 4096), "i"((Q >> 1) * 32768), "i"(86 + (Q & 1)) : "memory", "scc");
}
DI void acc_zero() { static_for([](auto I) { asm volatile("v_accvgpr_write_b32 a[%c0], 0" ::"i"((int)I)); }, make_iseq<128>{}); }
#define SMV 0
struct SMS { int ex[128], op0[128], nop[128], last; };
constexpr SMS sm_sched() {
  SMS z{};
  int et[32] = {}, e = 0;
  for (int t = 0; t < 128; ++t) {
    const int g = t & 15;
    const bool x = t < 48 ? (g >= 8 && (g & 1) && g < 15) : t < 64 ? (g >= 9 && g < 15) : (g >= 8 && g < 14);
    z.ex[t] = x && e < 32 ? e : -1;
    if (z.ex[t] >= 0) et[e++] = t;
  }
  int o = 0;
  for (int t = 0; t < 128; ++t) {
    const int g = t & 15;
    int cap = z.ex[t] >= 0 || t < 8 || (t < 48 && (g == 7 || (g >= 8 && !(g & 1)))) || t == 56 || t == 63 || t == 111 ? 0 : 1;
    z.op0[t] = o; z.nop[t] = 0;
    while (cap > 0 && o < 48 && et[2 * (o / 3) + (o % 3 == 0 ? 0 : 1)] + 2 <= t) {
      const int el = 2 * (o / 3) + o % 3;
      if (!(o % 3 < 2 && (el == 0 || el == 4))) --cap;
      ++o; ++z.nop[t]; z.last = t;
    }
  }
  for (int k = 16; k < 32; ++k) if (z.op0[et[k]] < 3 * ((k - 16) / 2 + 1)) z.last = 1000;
  return z;
}
constexpr SMS SM = sm_sched();
static_assert(SM.last <= 117);
constexpr int p_reg(int Y, int k) { return R_P + 16 * Y + 4 * ((k >> 3) * 2 + ((k >> 1) & 1)) + ((k >> 2) & 1) * 2 + (k & 1); }
template <int Y, int O> DI void sm_op() {
  constexpr int k = O / 3, e = 2 * k + O % 3, ts = R_TS + ((e >> 2) & 1);
  if constexpr (O % 3 == 2) asm volatile("v_cvt_pk_bf16_f32 v%c0, v%c1, v%c2" ::"i"(p_reg(Y, k)), "i"(R_E + ((2 * k) & 15)), "i"(R_E + ((2 * k + 1) & 15)));
  else if constexpr (e == 1 || e == 5) asm volatile("v_add_f32_e32 v%c0, v%c1, v%c2" ::"i"(ts), "i"(R_E + e - 1), "i"(R_E + e));
  else if constexpr (e != 0 && e != 4) asm volatile("v_add_f32_e32 v%c0, v%c0, v%c1" ::"i"(ts), "i"(R_E + (e & 15)));
}
template <int Y, int T> DI void sm_gap(AC& c) {
  if constexpr (SM.ex[T] >= 0) asm volatile("v_exp_f32 v%c0, v%c1" ::"i"(R_E + (SM.ex[T] & 15)), "i"(R_S + 32 * Y + SM.ex[T]));
  if constexpr (SM.nop[T] > 0) sm_op<Y, SM.op0[T]>();
  if constexpr (SM.nop[T] > 1) sm_op<Y, SM.op0[T] + 1>();
  if constexpr (SM.nop[T] > 2) sm_op<Y, SM.op0[T] + 2>();
  if constexpr (T == SM.last + 1) asm volatile("v_add_f32_e32 v%c0, v%c1, v%c2" ::"i"(R_TMP), "i"(R_TS), "i"(R_TS + 1));
  if constexpr (T == SM.last + 2) asm volatile("v_cmp_nle_f32_e64 s[54:55], v%c0, v%c1" ::"i"(R_TMP), "i"(R_THR));
}
template <int X, int MODE, int G> DI void gapA(AC& c) {
  constexpr bool FIRST = MODE == 1 || MODE == 2, NOQK = MODE == 3;
  constexpr int Xp = X ^ 1, kb = G >> 4, g = G & 15, qb = g >> 3, s = g & 7;
  if constexpr (MODE == 3 && G < 48 && G % 3 == 0) {
    constexpr int f = G / 3;
    asm volatile("buffer_load_dwordx4 a[%c0:%c1], v84, s[88:91], s75 offen offset:%2 sc1" ::"i"(128 + 4 * f), "i"(131 + 4 * f), "i"((f & 3) * 1024) : "memory");
    if constexpr ((f & 3) == 3) asm volatile("s_addk_i32 s75, %0" ::"i"(f == 7 ? 28672 : 4096));
  }
  if constexpr (G == 48) asm volatile("s_waitcnt vmcnt(%0) lgkmcnt(0)\n\ts_barrier" ::"i"(MODE == 3 ? 16 : MODE == 2 ? 12 : MODE == 1 ? 7 : 0) : "memory");
  else if constexpr (g == 0 && !NOQK) asm volatile("s_waitcnt lgkmcnt(0)");
  if constexpr (!NOQK) {
    constexpr int r = 192 + 4 * ((kb & 1) * 8 + s), q = 128 + 4 * g;
    constexpr int d = R_S + 32 * X + 4 * (kb * 2 + qb), ci = s == 0 ? R_CI + 4 * (kb * 2 + qb) : d;
    if constexpr (FIRST && s == 0)
      asm volatile("v_mfma_f32_16x16x32_bf16 v[%c0:%c1], a[%c2:%c3], a[%c4:%c5], 0" ::"i"(d), "i"(d + 3), "i"(r), "i"(r + 3), "i"(q), "i"(q + 3));
    else
      asm volatile("v_mfma_f32_16x16x32_bf16 v[%c0:%c1], a[%c2:%c3], a[%c4:%c5], v[%c6:%c7]" ::"i"(d), "i"(d + 3), "i"(r), "i"(r + 3), "i"(q), "i"(q + 3),
                   "i"(ci), "i"(ci + 3));
    if constexpr (g < 8 && kb < 3) kread<X, (kb + 1) * 8 + g>();
    constexpr int S0 = MODE == 1 ? 0 : SMV ? 40 : 14, S1 = MODE == 1 ? 1 : SMV ? 41 : 30, S2 = SMV ? 42 : 45, S3 = SMV ? 43 : 46;
    if constexpr (G == S0) asm volatile("s_add_u32 s96, s97, 0x1000000");
    if constexpr (G == S1) dma_m0<X, 0>();
    if constexpr (G == S2) asm volatile("s_mov_b32 s97, s98");
    if constexpr (G == S3) asm volatile("s_add_u32 s98, s98, s99");
    if constexpr (MODE != 1 && G >= 56 && !(G & 1)) dma_at<(G - 56) / 2>();
    if constexpr (MODE == 1 && (G & 11) == 9) dma_at<(G >> 4) * 2 + ((G >> 2) & 1)>();
    if constexpr (MODE == 1 && G == 31) dma_m0<X, 4>();
    if constexpr (G == 57) asm volatile("s_lshr_b32 s100, s100, 1\n\ts_add_u32 s56, s56, 1");
    if constexpr (G == 59) asm volatile("s_getpc_b64 s[86:87]\nev_pc%c0:\n\ts_add_u32 s86, s86, ev_ret%c0-ev_pc%c0\n\ts_addc_u32 s87, s87, 0" ::"i"(X * 4 + MODE));
    if constexpr (G == 61 && !FIRST) asm volatile("s_or_b32 s57, s54, s55\n\ts_mov_b32 %0, s57\n\ts_bitcmp1_b32 s100, 0" : "=s"(c.rare));
    if constexpr (G == 61 && FIRST) asm volatile("s_bitcmp1_b32 s100, 0");
  } else if constexpr (SM.nop[64 + G] > 0) asm volatile("s_nop 0");
  if constexpr (!FIRST && g < 8 && kb == 3) vread<Xp, g>();
  if constexpr (MODE == 2 && (G & 3) == 0) d_store<16 + G / 4>();
  if constexpr (!FIRST) sm_gap<Xp, 64 + G>(c);
  if constexpr (G == 63 && !NOQK) asm volatile("s_cbranch_scc1 ev_entry\nev_ret%c0:" ::"i"(X * 4 + MODE) : "vcc", "memory");
  GAP();
}
template <int R> DI void rescale1(float alpha) {
  float t;
  asm volatile("v_accvgpr_read_b32 %0, a[%c2]\n\tv_mul_f32 %0, %0, %1\n\tv_accvgpr_write_b32 a[%c2], %0" : "=&v"(t) : "v"(alpha), "i"(R));
}
template <int... Rs> DI void rescale_all(const float (&alpha)[2], iseq<Rs...>) { (rescale1<Rs>(alpha[(Rs >> 2) & 1]), ...); }
template <bool MAX> DI float row_reduce(float v) {
  auto p = __builtin_amdgcn_permlane32_swap(__float_as_uint(v), __float_as_uint(v), false, false);
  v = MAX ? fmaxf(__uint_as_float(p[0]), __uint_as_float(p[1])) : __uint_as_float(p[0]) + __uint_as_float(p[1]);
  p = __builtin_amdgcn_permlane16_swap(__float_as_uint(v), __float_as_uint(v), false, false);
  return MAX ? fmaxf(__uint_as_float(p[0]), __uint_as_float(p[1])) : __uint_as_float(p[0]) + __uint_as_float(p[1]);
}
template <int Y, int QB> DI float row_max() {
  constexpr int r = R_S + 32 * Y + 4 * QB;
  float mx;
  asm volatile("v_max_f32 %0, v%c1, v%c2\n\tv_max3_f32 %0, %0, v%c3, v%c4" : "=&v"(mx) : "i"(r), "i"(r + 1), "i"(r + 2), "i"(r + 3));
  static_for([&](auto K) {
    constexpr int q = r + 8 * (K + 1);
    float t = mx;
    asm volatile("v_max3_f32 %0, %0, v%c1, v%c2\n\tv_max3_f32 %0, %0, v%c3, v%c4" : "+v"(t) : "i"(q), "i"(q + 1), "i"(q + 2), "i"(q + 3));
    mx = t;
  }, make_iseq<3>{});
  return row_reduce<true>(mx);
}
template <int R> DI void v_sub(float d) { asm volatile("v_sub_f32 v%c0, v%c0, %1" ::"i"(R), "v"(d)); }
template <int B> DI void s_shift(const float (&d)[2]) { static_for([&](auto E) { v_sub<B + E>(d[(E >> 2) & 1]); }, make_iseq<32>{}); }
template <bool CI> DI void m_shift(const float (&d)[2]) {
  if constexpr (CI) s_shift<R_CI>(d);
  v_sub<R_M>(-d[0]); v_sub<R_M + 1>(-d[1]);
}
#define MSK(R, K) "v_cmp_gt_i32 s[48:49], " #K ", v89\n\tv_cmp_gt_i32 s[50:51], " #K "+1, v89\n\tv_cmp_gt_i32 s[52:53], " #K "+2, v89\n\t" \
                  "v_cmp_gt_i32 s[54:55], " #K "+3, v89\n\tv_cndmask_b32_e64 v[" #R "], v[" #R "], v122, s[48:49]\n\t" \
                  "v_cndmask_b32_e64 v[" #R "+1], v[" #R "+1], v122, s[50:51]\n\tv_cndmask_b32_e64 v[" #R "+2], v[" #R "+2], v122, s[52:53]\n\t" \
                  "v_cndmask_b32_e64 v[" #R "+3], v[" #R "+3], v122, s[54:55]\n\t"
DI void set_mask0() { asm volatile("v_mov_b32 v122, 0xff800000\n\t" MSK(128, 0) MSK(136, 16) MSK(144, 32) MSK(152, 48)); }
template <int X, bool CI = true> DI void sm_redo() {
  constexpr int Xp = X ^ 1;
  float d[2], dt[2], alpha[2], ts[2] = {0.f, 0.f};
  static_for([&](auto Q) {
    constexpr int qb = Q;
    const float pd = vget<R_PD + qb>();
    d[qb] = fmaxf(row_max<Xp, qb>() - pd, 0.f);
    dt[qb] = d[qb] + pd;
    vset<R_PD + qb>(d[qb]);
    alpha[qb] = __builtin_amdgcn_exp2f(-d[qb]);
    vset<R_RS + qb>(vget<R_RS + qb>() * alpha[qb]);
  }, make_iseq<2>{});
  vset<R_THR>(d[0] > 0.f || d[1] > 0.f ? -1.f : 4096.f);
  m_shift<CI>(d);
  static_for([&](auto K) {
    constexpr int k = K, qb = (k >> 1) & 1;
    const float e0 = __builtin_amdgcn_exp2f(vget<R_S + 32 * Xp + 2 * k>() - dt[qb]), e1 = __builtin_amdgcn_exp2f(vget<R_S + 32 * Xp + 2 * k + 1>() - dt[qb]);
    ts[qb] += e0; ts[qb] += e1;
    vset<p_reg(Xp, k)>(__uint_as_float(pk2(e0, e1)));
  }, make_iseq<16>{});
  vset<R_TS>(ts[0]); vset<R_TS + 1>(ts[1]);
  rescale_all(alpha, make_iseq<128>{});
  asm volatile("s_nop 1" ::: "memory");
  GAP();
}
template <int X, int MODE, int... Gs> DI void phaseA_(AC& c, iseq<Gs...>) { (gapA<X, MODE, Gs>(c), ...); }
template <int X, int MODE> DI void phaseA(AC& c) { phaseA_<X, MODE>(c, make_iseq<64>{}); }

template <int X, bool SMX, int G, bool FIRST = false> DI void gapB(AC& c) {
  constexpr int Xp = X ^ 1, blk = G >> 4, g = G & 15;
  if constexpr (g == 0) asm volatile("s_waitcnt lgkmcnt(0)");
  constexpr int qb = (g >> 1) & 1, s = g & 1, r = 192 + 4 * ((blk & 1) * 8 + (g >> 2) * 2 + s), o = 4 * ((blk * 4 + (g >> 2)) * 2 + qb);
  constexpr int p = R_P + 16 * Xp + 4 * (s * 2 + qb);
  if constexpr (FIRST && s == 0)
    asm volatile("v_mfma_f32_16x16x32_bf16 a[%c0:%c1], a[%c2:%c3], v[%c4:%c5], 0" ::"i"(o), "i"(o + 3), "i"(r), "i"(r + 3), "i"(p), "i"(p + 3));
  else
  asm volatile("v_mfma_f32_16x16x32_bf16 a[%c0:%c1], a[%c2:%c3], v[%c4:%c5], a[%c0:%c1]" ::"i"(o), "i"(o + 3), "i"(r), "i"(r + 3), "i"(p), "i"(p + 3));
  if constexpr (g < 8) {
    if constexpr (blk < 3) vread<Xp, (blk + 1) * 8 + g>();
    else if constexpr (SMX) kread<Xp, g>();
  }
  if constexpr (SMX) {
    if constexpr (G == 1 || G == 2) asm volatile("v_add_f32_e32 v%c0, v%c0, v%c1" ::"i"(R_RS + G - 1), "i"(R_TS + G - 1));
    if constexpr (g == 7 && blk < 3) dma_m0<X, blk * 4 + 4>();
    if constexpr (g >= 8 && !(g & 1) && blk < 3) dma_at<blk * 4 + 4 + (g - 8) / 2>();
    if constexpr (G == 55) asm volatile("s_cmp_eq_u32 s56, s76");
    if constexpr (G == 3) asm volatile("s_lshr_b32 s77, s77, 1");
    if constexpr (G == 4) asm volatile("s_bitcmp1_b32 s77, 0");
    if constexpr (G == 5) asm volatile("s_cselect_b64 s[52:53], s[62:63], 0");
    if constexpr (G == 6) asm volatile(FL_ARM("s[52:53]"));
    if constexpr (G == 56) asm volatile(FL_POLL ::: "memory");
    sm_gap<X, G>(c);
  }
  GAP();
}
template <int X, bool FIRST, int... Gs> DI void phaseB_(AC& c, iseq<Gs...>) { (gapB<X, true, Gs, FIRST>(c), ...); }
template <int X, bool FIRST = false> DI void phaseB(AC& c) { phaseB_<X, FIRST>(c, make_iseq<64>{}); }
constexpr int sm_nex() { int n = 0; for (int t = 0; t < 64; ++t) n += SM.ex[t] >= 0; return n; }
template <int X> DI void phaseB0(unsigned mode, bool vh) {
  dma_m0<X, 4>();
  kreads<X ^ 1>(make_iseq<8>{});
  if (vh) { dma_at<4>(); dma_at<5>(); dma_at<6>(); dma_at<7>(); }
  dma_m0<X, 8>();
  asm volatile("s_nop 3");
  float d[2];
  if (mode & 2) {
    d[0] = vget<122>(); d[1] = vget<123>();
    dma_at<8>();
  } else {
    if constexpr (X == 0) if (__builtin_expect(mode & 4, 0)) set_mask0();
    d[0] = row_max<X, 0>(); GAP(); dma_at<8>();
    d[1] = row_max<X, 1>();
#pragma unroll
    for (int qb = 0; qb < 2; ++qb) if (!(d[qb] > -3.0e38f)) d[qb] = 0.f;
  }
  GAP();
  vset<R_M>(d[0]); vset<R_M + 1>(d[1]);
  dma_at<9>();
  const f32x2c nd[2] = {{-d[0], -d[0]}, {-d[1], -d[1]}};
  static_for([&](auto E) {
    constexpr int e = E;
    const f32x2c v = nd[(e >> 1) & 1];
    asm volatile("v_pk_add_f32 v[%c0:%c1], v[%c0:%c1], %4\n\tv_mov_b64 v[%c2:%c3], %4" ::"i"(R_S + 32 * X + 2 * e), "i"(R_S + 32 * X + 2 * e + 1), "i"(R_CI + 2 * e), "i"(R_CI + 2 * e + 1), "v"(v));
    if constexpr (e == 7 || e == 15) dma_at<10 + e / 8>();
  }, make_iseq<16>{});
  static_assert(SM.op0[64] >= 3 * ((sm_nex() - 15) / 2) && SM.op0[64] <= 24);
  static_for([&](auto E) {
    constexpr int e = E;
    if constexpr (e < sm_nex()) asm volatile("v_exp_f32 v%c0, v%c1" ::"i"(R_E + e), "i"(R_S + 32 * X + e));
    if constexpr (e == 2) dma_m0<X, 12>();
    if constexpr (e == 5 || e == 10 || e == 15) dma_at<11 + e / 5>();
  }, make_iseq<16>{});
  static_for([&](auto O) { sm_op<X, O>(); }, make_iseq<SM.op0[64]>{});
  dma_at<15>();
  static_for([&](auto E) { asm volatile("v_exp_f32 v%c0, v%c1" ::"i"(R_E + (int)E), "i"(R_S + 32 * X + 16 + (int)E)); }, make_iseq<(sm_nex() > 16 ? sm_nex() - 16 : 0)>{});
  asm volatile("buffer_store_dwordx2 v[%c0:%c1], v88, s[88:91], s76 offen sc1\n\ts_bitcmp1_b32 s77, 0\n\ts_cselect_b64 s[52:53], s[62:63], 0\n\t"
               FL_ARM("s[52:53]") "\n\t" FL_POLL ::"i"(R_M), "i"(R_M + 1) : "memory", "scc");
  GAP();
}
enum : int { T_Q0, T_K0, T_OQ, T_OG, T_FPREP, T_FQ, T_FG, T_FS, T_ZO, T_CNT, T_NPRE = 10, T_NST = 12, T_TAB = 14, T_SLAB = 16, T_PART, T_IT = 18, T_ITN = 23 };
DI void ev_code(AC& c) {
  GAP();
  asm volatile(
    "s_branch 9f\nev_entry:\n\t"
    "s_bitcmp1_b32 s60, s56\n\ts_cbranch_scc1 3f\n\ts_bitcmp1_b32 s74, s56\n\ts_cbranch_scc1 2f\n\t"
    "s_bitcmp1_b32 s58, s56\n\ts_cselect_b64 vcc, -1, 0\n\tv_cndmask_b32 v121, -2.0, v81, vcc\n\t"
    "buffer_store_dwordx2 v[82:83], v121, s[88:91], s70 offen sc1\n\tbuffer_store_dwordx2 v[82:83], v121, s[88:91], s71 offen sc1\n\t"
    "v_cmp_eq_u64 vcc, s[84:85], v[124:125]\n\ts_andn2_b64 vcc, s[62:63], vcc\n\ts_cbranch_scc1 5f\n"
    "0:\n\ts_add_u32 s98, s98, s59\n\ts_mov_b32 s59, s61\n\ts_mov_b32 s61, s72\n\ts_mov_b64 s[62:63], s[64:65]\n\ts_mov_b64 s[64:65], s[66:67]\n\t"
    "s_mov_b64 s[66:67], s[68:69]\n\ts_cmp_eq_u32 s56, 1\n\ts_cbranch_scc1 ev_first\n\ts_setpc_b64 s[86:87]\n"
    "5:\n\t" FL_CHECK("s[62:63]") "\n\ts_branch 0b\n"
    "6:\n\t" FL_CHECK("s[62:63]") "\n\ts_setpc_b64 s[86:87]\n"
    "2:\nev_mask:\n\tv_mov_b32 v122, 0xff800000\n\t" MSK(228, 0) MSK(236, 16) MSK(244, 32) MSK(252, 48)
    "ev_turn:\n\ts_mov_b32 s98, 0\n\t"
    "v_lshrrev_b32 v90, 7, v86\n\tv_and_b32 v91, 6, v90\n\tv_lshlrev_b32 v91, 4, v91\n\tv_and_b32 v92, 0x70, v86\n\tv_xor_b32 v91, v91, v92\n\t"
    "s_movk_i32 s48, 896\n\tv_mad_u32_u24 v94, v90, s48, v91\n\ts_mov_b64 s[92:93], s[80:81]\n\ts_mov_b32 s94, 229376\n\t"
    "v_add_u32 v90, 28672, v94\n\tv_add_u32 v91, 34816, v94\n\tv_add_u32 v92, 40960, v94\n\tv_add_u32 v93, 47104, v94\n\t"
    "v_add_u32 v95, 6144, v94\n\tv_add_u32 v126, 12288, v94\n\tv_add_u32 v127, 18432, v94\n\t"
    "v_cmp_eq_u64 vcc, s[84:85], v[124:125]\n\ts_andn2_b64 vcc, s[62:63], vcc\n\ts_cbranch_scc1 6b\n\ts_setpc_b64 s[86:87]\n"
    "3:\n\tv_mov_b32 v122, 0xff800000\n\t" MSK(224, 0) MSK(232, 16) MSK(240, 32) MSK(248, 48)
    "ev_post:\n\tv_readlane_b32 s54, v78, %0\n\ts_add_u32 m0, s78, 0x10000\n\t"
    "s_bfe_u32 s53, s73, 0x80010\n\ts_mulk_i32 s53, 3584\n\ts_mov_b64 s[48:49], s[80:81]\n\ts_add_u32 s50, s53, 3584\n\t"
    "s_mov_b32 s51, 0x20000\n\t"
    "v_lshlrev_b32 v121, 3, v84\n\tbuffer_load_dword v121, s[48:51], s53 offen lds\n\t"
    "s_bitcmp0_b32 s73, 3\n\ts_cbranch_scc1 4f\n\t"
    "buffer_load_dwordx4 v84, s[88:91], s54 offen sc1 lds\n\tbuffer_load_dwordx4 v84, s[88:91], s54 offen offset:1024 sc1 lds\n\t"
    "buffer_load_dwordx4 v84, s[88:91], s54 offen offset:2048 sc1 lds\n\tbuffer_load_dwordx4 v84, s[88:91], s54 offen offset:3072 sc1 lds\n"
    "4:\n\ts_cmp_eq_u32 s56, 1\n\ts_cbranch_scc1 ev_first\n\ts_setpc_b64 s[86:87]\n"
    ::"i"((int)T_ZO) : "scc");
  asm volatile("ev_zero:");
  acc_zero();
  asm volatile("s_setpc_b64 s[86:87]\nev_first:\n\ts_cmp_lg_u32 s57, 0\n\ts_cbranch_scc1 ev_zero\n\ts_bitcmp0_b32 s73, 4\n\ts_cbranch_scc1 ev_first1");
  GAP();
  phaseB<0, true>(c);
  asm volatile("s_branch att_loop\nev_first1:");
  phaseB<1, true>(c);
  asm volatile("s_branch att_mid\n9:");
  GAP();
}
#define EV_CALL(L) do { asm volatile("s_getpc_b64 s[86:87]\n\ts_add_u32 s86, s86, 12\n\ts_addc_u32 s87, s87, 0\n\ts_branch " L ::: "vcc", "scc", "memory"); GAP(); } while (0)

#define ACLOB_ALL "a0","a1","a2","a3","a4","a5","a6","a7","a8","a9","a10","a11","a12","a13","a14","a15","a16","a17","a18","a19","a20","a21","a22","a23","a24","a25","a26","a27","a28","a29","a30","a31","a32","a33","a34","a35","a36","a37","a38","a39","a40","a41","a42","a43","a44","a45","a46","a47","a48","a49","a50","a51","a52","a53","a54","a55","a56","a57","a58","a59","a60","a61","a62","a63","a64","a65","a66","a67","a68","a69","a70","a71","a72","a73","a74","a75","a76","a77","a78","a79","a80","a81","a82","a83","a84","a85","a86","a87","a88","a89","a90","a91","a92","a93","a94","a95","a96","a97","a98","a99","a100","a101","a102","a103","a104","a105","a106","a107","a108","a109","a110","a111","a112","a113","a114","a115","a116","a117","a118","a119","a120","a121","a122","a123","a124","a125","a126","a127","a128","a129","a130","a131","a132","a133","a134","a135","a136","a137","a138","a139","a140","a141","a142","a143","a144","a145","a146","a147","a148","a149","a150","a151","a152","a153","a154","a155","a156","a157","a158","a159","a160","a161","a162","a163","a164","a165","a166","a167","a168","a169","a170","a171","a172","a173","a174","a175","a176","a177","a178","a179","a180","a181","a182","a183","a184","a185","a186","a187","a188","a189","a190","a191","a192","a193","a194","a195","a196","a197","a198","a199","a200","a201","a202","a203","a204","a205","a206","a207","a208","a209","a210","a211","a212","a213","a214","a215","a216","a217","a218","a219","a220","a221","a222","a223","a224","a225","a226","a227","a228","a229","a230","a231","a232","a233","a234","a235","a236","a237","a238","a239","a240","a241","a242","a243","a244","a245","a246","a247","a248","a249","a250","a251","a252","a253","a254","a255"

struct EP { float fa[2], fb[2]; };
template <int I, int E, int MODE> DI void epi_pair(const EP& p) {
  constexpr int qb = I & 1, r = 16 * (I >> 1) + 4 * qb + (E >> 1) * 8 + (E & 1) * 2, t = 96 + 2 * E, w = 128 + 4 * I + E;
  if constexpr (MODE == 2) asm volatile("v_accvgpr_read_b32 v106, a[%c0]" ::"i"(w));
  asm volatile("v_accvgpr_read_b32 v%c0, a[%c2]\n\tv_accvgpr_read_b32 v%c1, a[%c3]" ::"i"(t), "i"(t + 1), "i"(r), "i"(r + 1));
  if constexpr (MODE) asm volatile("v_mul_f32 v%c0, v%c0, %2\n\tv_mul_f32 v%c1, v%c1, %2" ::"i"(t), "i"(t + 1), "v"(p.fa[qb]));
  if constexpr (MODE == 2)
    asm volatile("v_lshlrev_b32 v104, 16, v106\n\tv_and_b32 v105, 0xffff0000, v106\n\tv_fmac_f32 v%c0, v104, %2\n\tv_fmac_f32 v%c1, v105, %2"
                 ::"i"(t), "i"(t + 1), "v"(p.fb[qb]));
  asm volatile("v_cvt_pk_bf16_f32 v%c0, v%c1, v%c2" ::"i"(w), "i"(t), "i"(t + 1));
}
template <int I> DI void epi_store() {
  asm volatile("buffer_store_dwordx4 v[%c0:%c1], v84, s[88:91], s101 offen offset:%2 sc1" ::"i"(128 + 4 * I), "i"(131 + 4 * I), "i"((I & 3) * 1024) : "memory");
  if constexpr ((I & 3) == 3) asm volatile("s_addk_i32 s101, 4096");
}
constexpr int R_TB = 232, R_VS = 236;
template <int HF, int R> DI void p_read() {
  constexpr int mb = R >> 4, r = 192 - 64 * HF + 2 * R;
  asm volatile("ds_read_b64_tr_b16 a[%c1:%c2], v%c0 offset:%c3" ::"i"(R_TB + 2 * HF + mb), "i"(r), "i"(r + 1), "i"((R & 15) * 2048));
}
template <int QB, int E> DI void p_cvt() {
  asm volatile("v_cvt_pk_bf16_f32 v%c0, v%c1, v%c2" ::"i"(208 + 4 * QB + E), "i"(192 + 8 * QB + 2 * E), "i"(193 + 8 * QB + 2 * E));
}
template <int QB, int PR> DI void p_store() {
  asm volatile("buffer_store_dwordx4 v[%c0:%c1], v%c2, s[88:91], s96 offen offset:%3 sc1" ::"i"(208 + 4 * QB), "i"(211 + 4 * QB), "i"(R_VS + QB),
               "i"(PR * 128) : "memory");
}
constexpr int p_dma(int HF, int G) { return G == 3 && !HF ? 0 : G == 11 ? 1 - HF : G == 15 ? 2 - HF : G == 19 ? 3 - HF : G == 27 && HF ? 3 : -1; }
template <int HF, bool LD, int G> DI void p_gap() {
  constexpr int ch = G >> 3, t = G & 7, a = 192 - 64 * HF + 32 * (ch & 1) + 4 * t, b = 128 + 4 * (2 * t + (ch >> 1)), d = 192 + 4 * ch;
  if constexpr (t == 0)
    asm volatile("v_mfma_f32_16x16x32_bf16 v[%c0:%c1], a[%c2:%c3], v[%c4:%c5], 0" ::"i"(d), "i"(d + 3), "i"(a), "i"(a + 3), "i"(b), "i"(b + 3));
  else
    asm volatile("v_mfma_f32_16x16x32_bf16 v[%c0:%c1], a[%c2:%c3], v[%c4:%c5], v[%c0:%c1]" ::"i"(d), "i"(d + 3), "i"(a), "i"(a + 3), "i"(b), "i"(b + 3));
  p_read<HF ^ 1, G>();
  if constexpr (G == 0) asm volatile("v_xor_b32 v%c0, s100, v%c0" ::"i"(R_TB + 2 * HF + 1));
  if constexpr (G == 17) asm volatile("v_xor_b32 v%c0, s100, v%c0" ::"i"(R_TB + 2 * (HF ^ 1)));
  if constexpr (G >= 4 && G <= 8 && G != 7) p_cvt<1, (G < 7 ? G - 4 : 3)>();
  if constexpr (G == 24 || (G == 7 && HF == 0)) asm volatile("s_waitcnt lgkmcnt(8)");
  if constexpr (G == 7 && HF == 1) asm volatile("s_waitcnt vmcnt(%0) lgkmcnt(8)\n\ts_barrier" ::"i"(LD ? 13 : 5) : "memory");
  if constexpr (G == 10) p_store<1, HF ^ 1>();
  if constexpr (G >= 20 && G < 24) p_cvt<0, G - 20>();
  if constexpr (G == 25) p_store<0, HF>();
  if constexpr (LD && p_dma(HF, G) >= 0) dma_at<(HF ? 8 : 12) + p_dma(HF, G)>();
  if constexpr (LD && HF == 0 && G == 2) asm volatile("s_bitset0_b32 m0, 12");
  if constexpr (HF == 0 && G == 13) asm volatile("s_add_u32 s96, s96, 0x8000");
  if constexpr (HF == 0 && G == 26) asm volatile("s_add_u32 s98, s98, 128");
  if constexpr (LD && HF == 0 && G == 27) asm volatile("s_cmp_lt_u32 s98, 0x380");
  if constexpr (HF == 1 && G == 1) asm volatile("s_xor_b32 s100, s100, 0x10000");
  if constexpr (LD && HF == 1 && G == 2) asm volatile("s_xor_b32 s97, s97, s100");
  if constexpr (LD && HF == 1 && G == 9) asm volatile("s_or_b32 m0, s97, 0x1000");
  if constexpr (!LD && HF == 1 && G == 30) asm volatile("s_cmp_lt_u32 s98, 0x500");
  GAP();
}
template <int HF, bool LD, int... Gs> DI void p_half(iseq<Gs...>) { (p_gap<HF, LD, Gs>(), ...); }
template <int G> DI void gapF(AC& c, const EP& p) {
  if constexpr (G < 64) gapB<0, false, G>(c);
  if constexpr (G >= 10) epi_pair<(G - 10) / 4, (G - 10) & 3, 0>(p);
  if constexpr (G >= 14 && ((G - 14) & 3) == 0) epi_store<(G - 14) / 4>();
  if constexpr (G == 73) epi_store<15>();
  GAP();
}
template <int... Gs> DI void phaseF(AC& c, const EP& p, iseq<Gs...>) { (gapF<Gs>(c, p), ...); }
template <int I> DI void pr_load() {
  asm volatile("buffer_load_dwordx4 a[%c0:%c1], v84, s[88:91], s75 offen offset:%2 sc1" ::"i"(128 + 4 * I), "i"(131 + 4 * I), "i"((I & 3) * 1024) : "memory");
  if constexpr ((I & 3) == 3) asm volatile("s_addk_i32 s75, 4096");
}
template <bool LD, int G> DI void gapAL(AC& c) {
  constexpr int kb = G >> 3, s = G & 7, r = 192 + 4 * (G & 15), q = 160 + 4 * s, d = R_S + 36 + 8 * kb, ci = s == 0 ? R_CI + 4 + 8 * kb : d;
  if constexpr (G == 0) asm volatile("s_add_u32 s96, s97, 0x1000000\n\ts_mov_b32 m0, s79\n\ts_waitcnt vmcnt(9) lgkmcnt(0)\n\ts_barrier" ::: "memory");
  if constexpr (G >= 8 && (G & 3) == 0) asm volatile("s_waitcnt lgkmcnt(4)");
  if constexpr (G == 29) asm volatile("s_barrier" ::: "memory");
  asm volatile("v_mfma_f32_16x16x32_bf16 v[%c0:%c1], a[%c2:%c3], a[%c4:%c5], v[%c6:%c7]" ::"i"(d), "i"(d + 3), "i"(r), "i"(r + 3), "i"(q), "i"(q + 3),
               "i"(ci), "i"(ci + 3));
  if constexpr (G < 24) kread<1, G + 8>();
  else vread<0, G - 24>();
  constexpr int VS = LD ? 2 : 3;
  if constexpr (G % VS == 1 && G < 8 * VS) dma_at<G / VS>();
  if constexpr (G == 3 * VS + 2) asm volatile("s_bitset1_b32 m0, 12");
  if constexpr (LD && G == 16) asm volatile("buffer_load_dwordx4 v[122:125], v88, s[88:91], s76 offen sc1" ::: "memory");
  if constexpr (LD && G > 16 && (G & 1)) pr_load<(G - 17) / 2>();
  if constexpr (G == 30) asm volatile("s_xor_b32 m0, s79, 0x11000\n\ts_movk_i32 s98, 128");
  sm_gap<0, 64 + 2 * G>(c); sm_gap<0, 65 + 2 * G>(c);
  GAP();
}
template <bool LD, int... Gs> DI void phaseAL(AC& c, iseq<Gs...>) { (gapAL<LD, Gs>(c), ...); }
struct BLS { int ex[65], op[65], vm[65], su[65]; };
constexpr BLS bl_sched(bool LD) {
  BLS z{};
  z.op[0] = 1;
  for (int t = 0; t < 64; ++t) {
    const int g = t & 15, o = z.op[t];
    const bool x = t == 6 || (t < 48 && g > 8 && g != 12 && g != 15);
    const bool v = (t & 11) == 8;
    const bool p = t >= 15 && o < 24 && (t & 3) != 0 && t != 47 && !x && z.ex[t - 1] > 2 * (o / 3) + (o % 3 != 0);
    z.ex[t + 1] = z.ex[t] + x; z.vm[t + 1] = z.vm[t] + v; z.op[t + 1] = o + p;
    z.su[t + 1] = z.su[t] + (t >= 1 && z.su[t] < (LD ? 16 : 10) && g != 15 && !v && !x && (LD || t > 55 || (t > 3 && !p)));
  }
  return z;
}
template <bool LD> constexpr BLS BL = bl_sched(LD);
static_assert(BL<true>.ex[64] == 16 && BL<true>.op[61] == 24 && BL<true>.vm[64] == 8 && BL<false>.vm[64] == 8);
static_assert(BL<true>.su[56] == 16 && BL<false>.su[62] == 10);
template <bool LD, int G> DI void gapBL(AC& c) {
  if constexpr (G == 48) asm volatile("s_waitcnt vmcnt(%0) lgkmcnt(0)\n\ts_barrier" ::"i"(BL<LD>.vm[48] + (LD ? 8 : 0)) : "memory");
  gapB<1, false, G>(c);
  if constexpr (G >= 48 && G < 56) vread<1, G - 48>();
  if constexpr (BL<LD>.vm[G + 1] > BL<LD>.vm[G]) {
    constexpr int k = BL<LD>.vm[G];
    if constexpr (LD) pr_load<8 + k>();
    else dma_at<8 + k>();
    if constexpr (!LD && k == 3) asm volatile("s_bitset0_b32 m0, 12");
  }
  if constexpr (G == 1 || G == 2) asm volatile("v_add_f32_e32 v%c0, v%c0, v%c1" ::"i"(R_RS + G - 1), "i"(R_TS + G - 1));
  if constexpr (G == 3) asm volatile("v_mov_b32 v%c0, 0" ::"i"(R_TS));
  if constexpr (BL<LD>.ex[G + 1] > BL<LD>.ex[G]) {
    constexpr int j = BL<LD>.ex[G];
    asm volatile("v_exp_f32 v%c0, v%c1" ::"i"(R_E + j), "i"(R_S + 36 + 8 * (j >> 2) + (j & 3)));
  }
  if constexpr (BL<LD>.op[G + 1] > BL<LD>.op[G]) {
    constexpr int o = BL<LD>.op[G], k = o / 3, j = 2 * k + o % 3;
    if constexpr (o % 3 == 2) asm volatile("v_cvt_pk_bf16_f32 v%c0, v%c1, v%c2" ::"i"(p_reg(1, (k >> 1) * 4 + 2 + (k & 1))), "i"(R_E + 2 * k), "i"(R_E + 2 * k + 1));
    else asm volatile("v_add_f32_e32 v%c0, v%c1, v%c2" ::"i"(R_TS + 1), "i"(j == 1 ? R_E : R_TS + 1), "i"(R_E + j));
  }
  if constexpr (G == 61) asm volatile("v_cmp_nle_f32_e64 s[54:55], v%c0, v%c1" ::"i"(R_TS + 1), "i"(R_THR));
  if constexpr (BL<LD>.su[G + 1] > BL<LD>.su[G]) {
    constexpr int k = BL<LD>.su[G];
    if constexpr (k < 4) asm volatile("v_xor_b32 v%c0, %1, %2" ::"i"(R_TB + k), "i"(32 * k), "v"(c.tb));
    else if constexpr (k < 6) asm volatile("v_add_u32 v%c0, %1, %2" ::"i"(R_VS + k - 4), "i"(4096 * (k - 4)), "v"(c.vs));
    else if constexpr (k == 6) tload<96, T_SLAB>();
    else if constexpr (k == 7) tload<101, T_CNT>();
    else if constexpr (k == 8) asm volatile("v_mov_b64 v[240:241], 1");
    else if constexpr (k == 9) asm volatile("s_mov_b32 s97, s79\n\ts_mov_b32 s100, 0x8000");
    else if constexpr (k < 14) asm volatile("v_mov_b32 v%c0, %1" ::"i"(k < 12 ? 214 + k : 218 + k), "v"(k & 1 ? c.o1 : c.o0));
    else asm volatile("v_mov_b64 v[%c0:%c1], 0" ::"i"(198 + 2 * k), "i"(199 + 2 * k));
  }
  GAP();
}
template <bool LD, int... Gs> DI void phaseBL(AC& c, iseq<Gs...>) { (gapBL<LD, Gs>(c), ...); }
template <int MODE, int G> DI void gapFL(const EP& p) {
  constexpr int x = G - (MODE == 3 ? 4 : 0), J = MODE == 1 ? (G < 24 && G % 3 == 1 ? G / 3 : -1) : G < 27 && G % 5 != 0 && G % 5 != 3 ? G / 5 * 3 + (G % 5) / 2 : -1;
  if constexpr (G == 0 && MODE == 1) asm volatile("s_or_b32 m0, s78, 0x1000\n\ts_movk_i32 s98, 256\n\ts_waitcnt lgkmcnt(0)");
  if constexpr (G == 0 && MODE > 1) asm volatile("s_waitcnt vmcnt(8) lgkmcnt(0)" ::: "memory");
  if constexpr (G == 8 && MODE > 1) asm volatile("s_waitcnt vmcnt(5)" ::: "memory");
  if constexpr (G >= 8 && G < 32 && (G & 3) == 0) asm volatile("s_waitcnt lgkmcnt(%0)" ::"i"(G < 28 ? 4 : 0));
  if constexpr (G < 32) {
    constexpr int o = 8 * (G >> 1) + 4, r = 192 + 4 * (G & 15), q = R_P + 20 + 8 * (G & 1);
    asm volatile("v_mfma_f32_16x16x32_bf16 a[%c0:%c1], a[%c2:%c3], v[%c4:%c5], a[%c0:%c1]" ::"i"(o), "i"(o + 3), "i"(r), "i"(r + 3), "i"(q), "i"(q + 3));
  }
  if constexpr (G < 24) vread<1, G + 8>();
  if constexpr (G == 28) asm volatile("s_waitcnt vmcnt(8)\n\ts_barrier\n\ts_or_b32 m0, s79, 0x1000\n\ts_movk_i32 s98, 384" ::: "memory");
  if constexpr (G == 29 || G == 31) dma_at<8 + (G - 29) / 2>();
  if constexpr (G == 32 || G == 40) {
    static_for([&](auto I) { p_read<0, 2 * (G - 32) + I>(); }, make_iseq<16>{});
    dma_at<10 + (G - 32) / 8>();
  }
  if constexpr (J >= 0) dma_at<8 + (J & 7)>();
  if constexpr (x >= 0) epi_pair<2 * ((x >> 2) & 7) + (x >> 5), x & 3, MODE>(p);
  if constexpr (MODE == 3 && G < 32) {
    constexpr int db = G & 15, qb = G >> 4, o = 4 * (db * 2 + qb), a = 224 + 4 * (db & 1), b = 128 + 4 * ((db >> 1) * 2 + qb);
    asm volatile("v_mfma_f32_16x16x32_bf16 a[%c0:%c1], v[%c2:%c3], a[%c4:%c5], a[%c0:%c1]" ::"i"(o), "i"(o + 3), "i"(a), "i"(a + 3), "i"(b), "i"(b + 3));
  }
  if constexpr (J == 3 || J == 11) asm volatile("s_bitset0_b32 m0, 12");
  if constexpr (J == 7 && MODE > 1) asm volatile("s_or_b32 m0, s78, 0x1000\n\ts_movk_i32 s98, 256");
  if constexpr (x == 63) asm volatile("s_waitcnt vmcnt(4) lgkmcnt(0)" ::: "memory");
  GAP();
}
template <int MODE, int... Gs> DI void phaseFL_(const EP& p, iseq<Gs...>) { (gapFL<MODE, Gs>(p), ...); }
template <int MODE> DI void phaseFL(const EP& p) { GAP(); phaseFL_<MODE>(p, make_iseq<MODE == 3 ? 68 : 64>{}); }
template <int U, int K> DI void r_load(unsigned vo, unsigned so) {
  asm volatile("buffer_load_dwordx4 a[%c0:%c1], %2, s[88:91], %3 offen offset:%4 sc1" ::"i"(64 * U + 4 * K), "i"(64 * U + 4 * K + 3), "v"(vo),
               "s"(so + (K >> 1) * 0x40000 + (U >> 1) * 4096), "i"((K & 1) * 512 + (U & 1) * 2048) : "memory");
}
template <int U, int K> DI void r_sum() {
  constexpr int d = 128 + 8 * U + 4 * (K & 1), b = 64 * U + 4 * K;
  if constexpr (K < 2) asm volatile("v_mfma_f32_16x16x32_bf16 v[%c0:%c1], v[96:99], a[%c2:%c3], 0" ::"i"(d), "i"(d + 3), "i"(b), "i"(b + 3));
  else asm volatile("v_mfma_f32_16x16x32_bf16 v[%c0:%c1], v[96:99], a[%c2:%c3], v[%c0:%c1]" ::"i"(d), "i"(d + 3), "i"(b), "i"(b + 3));
}
template <int U> DI void r_sums() { static_for([&](auto K) { r_sum<U, K>(); }, make_iseq<16>{}); }
template <int U> DI void r_fin(unsigned vo, __amdgpu_buffer_rsrc_t srd, unsigned so) {
  static_for([&](auto E) { asm volatile("v_cvt_pk_bf16_f32 v%c0, v%c1, v%c2" ::"i"(128 + 8 * U + E), "i"(128 + 8 * U + 2 * E), "i"(129 + 8 * U + 2 * E)); }, make_iseq<4>{});
  asm volatile("s_nop 0\n\tbuffer_store_dwordx4 v[%c3:%c4], %0, %1, %2 offen sc0 sc1" ::"v"(vo), "s"(srd), "s"(so), "i"(128 + 8 * U), "i"(131 + 8 * U) : "memory");
}

#define CMAX 12
#define NSPL 5
#define NYT 15
#define NYM 0
struct TRow { unsigned v[64]; };
constexpr u64 kvmask(int m0, int m1) { u64 n = 0; for (int m = m0; m <= m1; ++m) n |= 0x101ull << m; return n; }
constexpr u64 k_order(bool isY, int rr) {
  const int cls = (rr + (rr >> 2)) & 3;
#define NCM 3
  u64 t = 0;
  int n = 0;
  auto put = [&](int k) { t |= (u64)k << (4 * n++); };
  if (isY) {
    for (int k = 1; k <= 4; ++k) put(NCM + 4 + (k & 3));
    for (int k = 1; k <= 4; ++k) put(NCM + 8 + (k & 3));
    for (int k = NCM + 12; k < 16; ++k) put(k);
    for (int k = NCM + 3; k >= 0; --k) put(k);
    return t;
  }
  for (int k = 0; k < NCM; ++k) put(k);
  for (int k = 0; k < 3; ++k) { put(NCM + 4 * k + cls); put(NCM + ((cls + k + 1) & 3)); }
  for (int k = 1; k < 4; ++k) put(NCM + 4 + ((cls + k) & 3));
  for (int k = 1; k < 4; ++k) put(NCM + 8 + ((cls + k) & 3));
  for (int k = NCM + 12; k < 16; ++k) put(NCM == 2 ? k ^ (cls & 1) : k);
  return t;
}
static_assert(NCM >= 3);
constexpr TRow t_row(int b, int wid) {
  TRow t{};
  const int j = b & 7, r = b >> 3, rr = r & 15, hsel = rr & 1, pj = rr >> 1, kv = pj & 1, h = j + 8 * hsel, u = hsel * 8 + j;
  const bool isY = r >= 16, noA = pj == 7, solo = pj >= NSPL;
  const int qbF = isY != solo ? 15 - pj : pj;
  const int mt = (rr >> 2) + (isY ? 4 : 0), g = j * 128, NY = NYT - ((NYM >> pj) & 1) + ((NYM >> (8 + pj)) & 1);
  const int nx0 = solo ? 0 : 32 - 2 * pj - NY, cx = nx0 < CMAX ? nx0 : CMAX, ex = nx0 - cx, qb0 = solo ? qbF : 15 - pj;
  const unsigned oQ = (unsigned)h << 20, oK = oQ + O_K, oP = O_P + (h * 8 + pj) * PSTR;
  if (wid == 4) {
    for (int l = 0; l < 64; ++l)
      t.v[l] = fo(l < 16 ? g + hsel * 16 + l : l == 16 ? g + 32 + hsel * 8 + (qb0 >> 1) : l == 17 ? g + 32 + hsel * 8 + (qbF >> 1) :
                  l == 26 ? g + 48 + hsel * 8 + pj : l < 32 ? g + 64 + hsel * 8 + pj :
                  l < 48 ? 2304 + pj * 16 + l - 32 : 2048 + pj * 16 + l - 48);
    return t;
  }
  auto put64 = [&](int i, u64 x) { t.v[i] = (unsigned)x; t.v[i + 1] = (unsigned)(x >> 32); };
  t.v[T_Q0] = oQ + (qb0 * 128 + wid * 16) * 512;
  t.v[T_K0] = oK + (isY ? cx : 0) * 32768;
  const int jq = (j & 6) | (pj & 1), tq = (j & 1) * 4 + (pj >> 1);
  t.v[T_OQ] = ((unsigned)(jq + 8 * hsel) << 20) + tq * 131072 + (wid >> 1) * 65536 + (wid & 1) * 4096;
  t.v[T_OG] = oK + kv * TSB + mt * 131072 + (wid >> 1) * 65536 + (wid & 1) * (kv ? 8192 : 4096);
  t.v[T_FPREP] = fo(2048 + pj * 16 + u);
  t.v[T_FQ] = fo(jq * 128 + 32 + hsel * 8 + tq);
  t.v[T_FG] = fo(g + hsel * 16 + kv * 8 + mt);
  t.v[T_FS] = fo(1024 + qbF * 16 + j * 2 + hsel);
  t.v[T_CNT] = fo(2560 + qbF * 16);
  t.v[T_ZO] = O_Z + pj * 64 * SEQ * 2 + (u * 4 + wid) * 4096;
  const u64 own = 0x1ull << (kv * 8 + mt), conv = noA ? 0 : 0xffffull << 32;
  put64(T_NPRE, 0x10000ull | (isY ? (1ull << (cx >> 2)) | (1ull << ((cx + 1) >> 2)) : noA ? 0 : 0xffffull << 48));
  put64(T_NST, isY ? (solo ? kvmask(0, (2 * qbF + 1) >> 2) : kvmask(cx >> 2, 3) | 1ull << 27) : kvmask(0, 0));
  put64(T_TAB, k_order(isY, rr));
  t.v[T_SLAB] = O_S + qbF * 0x200000 + j * 0x40000 + wid * 8192 + hsel * 1024;
  t.v[T_PART] = oP;
  const unsigned junk = fo(g + 96 + r), fconv = noA ? junk : fo(2304 + pj * 16 + u);
  for (int it = isY || solo; it < 2; ++it) {
    unsigned* p = t.v + T_IT + it * T_ITN;
    const int n = !it ? nx0 : isY && !solo ? NY : 2 * qbF + 2;
    u64 q[4] = {0, 0, 0, 0};
    unsigned jmp[3] = {0, 0, 0}, chk = 0;
    p[14] = junk;
    if (isY) {
      p[0] = 2; p[1] = 2; p[13] = t.v[T_FG];
      if (!solo) {
        chk = 1u << (14 - cx); p[0] |= chk; q[1] = kvmask(4, (31 - 2 * pj) >> 2) & ~own; jmp[1] = ex * 32768;
        chk |= 1u << (n - 2); q[2] = 1ull << 26;
      }
    } else if (it == 0) {
      chk = p[0] = 2u | (ex ? 1u << (cx - 2) : 0) | 1u << (n - 2);
      chk |= noA || qbF > 1 ? 0 : 1u << n;
      q[0] = kvmask(0, (cx - 1) >> 2);
      if (ex) { q[1] = kvmask(4, 4 + ((ex - 1) >> 2)); jmp[1] = (16 - cx) * 32768; }
      jmp[ex ? 2 : 1] = -(ex ? 16 + ex : cx) * 32768;
      q[ex ? 2 : 1] = 0x20000ull | kvmask(0, (2 * qbF + 1) >> 2);
      q[ex ? 3 : 2] = conv;
      p[1] = 2; p[13] = fconv; p[14] = fo(g + 64 + hsel * 8 + pj);
    } else if (solo) {
      chk = p[0] = 0x402; p[1] = 2; p[13] = fconv;
      q[0] = kvmask(0, 2); q[1] = kvmask(3, (2 * qbF + 1) >> 2) | conv;
    } else {
      p[0] = p[1] = n > 4 ? 2 : 1; p[13] = fo(g + 48 + hsel * 8 + pj);
      chk = noA ? 0 : p[0];
      q[0] = conv;
    }
    p[2] = jmp[0]; p[4] = jmp[1]; p[15] = jmp[2];
    p[3] = it && n > 2 ? 1u << (n - 3) : 0;
    for (int k = 0; k < 4; ++k) { p[5 + 2 * k] = (unsigned)q[k]; p[6 + 2 * k] = (unsigned)(q[k] >> 32); }
    p[16] = 32 | (isY && !solo ? 2 : 0) | (n == 2 ? 4 : 0) | (isY || noA ? 0 : 8) | (n & 1) << 4 | (n - 1 - it) << 8 | ((r >> 1) * 4 + wid) << 16 | h << 24 | qbF << 28;
    p[17] = it && n > 2 ? 1u << (n - 2) : 0;
    p[18] = it ? oP + wid * 16384 : oQ + (qbF * 128 + wid * 16) * 512;
    p[19] = oP + (it ? 1032 : 1024);
    p[20] = chk >> 1;
    p[21] = oK + (isY ? cx : 0) * 32768;
    p[22] = (p[0] | p[3] | p[17]) << 1;
  }
  return t;
}
struct TBlk { TRow row[5]; };
template <int B> constexpr TBlk t_blk = {{t_row(B, 0), t_row(B, 1), t_row(B, 2), t_row(B, 3), t_row(B, 4)}};
struct Tab { TBlk blk[256]; };
template <int... I> constexpr Tab t_make(iseq<I...>) { return Tab{{t_blk<I>...}}; }
__device__ const Tab g_tab = t_make(make_iseq<256>{});

template <int R> DI const bf16* sptr() { const bf16* p; asm volatile("s_mov_b64 %0, s[%c1:%c2]" : "=s"(p) : "i"(R), "i"(R + 1)); return p; }
template <class T, int I> DI T* arg_late() {
  unsigned lo, hi;
  asm volatile("v_readlane_b32 %0, v77, %2\n\tv_readlane_b32 %1, v77, %3\n\ts_nop 4" : "=s"(lo), "=s"(hi) : "i"(2 * I), "i"(2 * I + 1));
  return (T*)(((u64)hi << 32) | lo);
}
__global__ __launch_bounds__(256, 1) __attribute__((amdgpu_num_vgpr(77), amdgpu_num_sgpr(56))) void fused_kernel(
    const bf16* ckv_, const bf16* wuk_, const bf16* wuv_, const bf16* x_, const bf16* wq_, bf16* ws_, const bf16* ak_, const bf16* av_,
    const bf16* ash_, const bf16* assmk_, const bf16* assmD_, const bf16* wo_, const bf16* aq_, bf16* out_) {
  extern __shared__ __attribute__((aligned(16))) char lds[];
  const void* kap = (const void*)__builtin_amdgcn_kernarg_segment_ptr();
  asm volatile("at_code_begin:\n\ts_load_dwordx16 s[80:95], %0, 0\n\ts_load_dwordx2 s[96:97], %0, 0x40" ::"s"(kap) : ACLOB_ALL, "v255", "s101");
  asm volatile("s_getpc_b64 s[48:49]\nct_base:\n\ts_mov_b32 s50, pf_code_end-ct_base\n\ts_mov_b32 s51, 0x20000\n\ts_lshr_b32 s52, %1, 3\n\t"
               "v_and_b32 v116, 63, %0\n\tv_lshrrev_b32 v117, 6, %0\n\tv_lshl_add_u32 v116, v116, 7, v117\n\ts_lshl_b32 s52, s52, 9\n\t"
               "v_lshlrev_b32 v116, 7, v116\n\tbuffer_load_dword v117, v116, s[48:51], s52 offen" ::"v"(threadIdx.x), "s"(blockIdx.x) : "scc", "memory");
  GAP();
  const int tid = threadIdx.x, lane = tid & 63;
  const int wid = __builtin_amdgcn_readfirstlane(tid >> 6);
  const int b = blockIdx.x, j = b & 7, r = b >> 3, rr = r & 15, hsel = rr & 1, pj = rr >> 1, kv = pj & 1;
  const bool isY = r >= 16, doP = isY && pj != 7, doA = !isY && pj != 7;
  const int h = j + 8 * hsel, mt = (rr >> 2) + (isY ? 4 : 0), pu = (hsel * 8 + j) * 4 + wid;
  constexpr unsigned m0d = 131072 + 24576;
  const unsigned lA = isY ? 65536 : 0, lW = lA ^ 65536, po = isY ? 0 : 32768;
  unsigned mA = lA + wid * 1024 + po, mW = lW + wid * 1024 + po, ofA = isY ? ((j & 1) * 4 + (pj >> 1)) * 256 * DA * 2 : mt * 256 * DC * 2,
           ofW = (isY ? (h & 14) | (pj & 1) : h) * 512;
  const unsigned lda = isY ? DA : DC;
  unsigned g0 = (rr + (rr >> 2)) & 3;
  {
    asm volatile("global_load_dword v78, %0, %2\n\tglobal_load_dword v79, %1, %2 offset:1024\n\tglobal_load_dword v77, %3, %4"
                 ::"v"(tid * 4), "v"(lane * 4), "s"(&g_tab.blk[b]), "v"((lane < 27 ? lane : 27) * 4), "s"(kap) : "memory");
    const unsigned ra = wid * 8 + (lane >> 3), sa = ((lane & 7) ^ (((wid & 1) * 4 + (lane >> 4)) & 7)) * 16;
    const unsigned vbA = ra * (lda * 2) + sa, vbB = (4 * wid + (lane >> 4)) * LDW * 2 + 16 * ((lane & 15) ^ (((lane >> 4) << 2) | wid));
    vsetu<92>(vbA); vsetu<93>(vbB); vsetu<94>(ra * (DC * 2) + sa);
    sset<65>(128 * lda);
    asm volatile("s_mov_b32 s74, 0x40000\n\ts_mov_b32 s53, 0x80000");
    sset<58>(256 * lda * 2); sset<62>((lda - 1) * LDW * 2 + 512);
    asm volatile("s_mov_b32 s59, 0x20000\n\ts_mov_b32 s63, 0x20000");
    g_pro<1>(g0, mA, mW, lda);
    asm volatile("" : "+s"(ofA), "+s"(ofW), "+s"(mA), "+s"(mW), "+s"(g0));
    GAP();
  }
  asm volatile("s_bitcmp1_b32 %0, 4\n\ts_waitcnt lgkmcnt(0)\n\ts_cselect_b64 s[98:99], s[84:85], s[82:83]\n\ts_bitcmp1_b32 %0, 7\n\t"
               "s_cselect_b64 s[60:61], s[88:89], s[98:99]\n\ts_cselect_b64 s[56:57], s[86:87], s[80:81]\n\t"
               "s_add_u32 s56, s56, %1\n\ts_addc_u32 s57, s57, 0\n\ts_add_u32 s60, s60, %2\n\ts_addc_u32 s61, s61, 0\n\t"
               "s_mov_b64 s[88:89], s[90:91]\n\ts_mov_b32 s90, %3\n\ts_mov_b32 s91, 0x20000" ::"s"(b), "s"(ofA), "s"(ofW), "i"(WSB) : "scc");
  KST(0, 0);
  AC c;
  {
    GM g;
    PR pr;
    const int ptb = hsel * 8 + j;
    if (doP) prep_load(pr, tid, pj, ptb, sptr<92>(), sptr<94>(), sptr<96>());
    g_pro<2>(g0, mA, mW, lda);
    g_pro(g0 + 1, mA, mW, lda);
    sset<64>(64 * lda); sset<67>(256 * lda); sset<68>(320 * lda);
    asm volatile("s_mov_b32 s71, 0x20000\n\ts_movk_i32 s72, 0x100\n\ts_mov_b32 s73, 0x20100");
    asm volatile("buffer_load_dwordx2 v[120:121], off, s[88:91], %0 sc1" ::"s"(fo(6144)) : "memory");
    GAP();
    {
      const int fr = lane & 15, fq = lane >> 4, q = (lane & 15) >> 2, p = lane & 3;
      vsetu<128>(lA + (wid >> 1) * 16384 + (fr >> 3) * 1024 + (fr & 7) * 128 + ((fq ^ (fr >> 1)) * 16));
      vsetu<129>(lW + (wid & 1) * 8192 + off_b(8 * fq + q, p >> 1) + 8 * (p & 1));
      vsetu<130>(lW + (wid & 1) * 8192 + off_b(8 * fq + 4 + q, p >> 1) + 8 * (p & 1));
      const unsigned one = fq != q ? 0u : (p & 1) ? 0x3f800000u : 0x3f80u;
      vsetu<131>((p & 2) ? 0u : one); vsetu<132>((p & 2) ? one : 0u);
      vsetu<133>((lane >> 2) * 128 + 16 * ((p >> 1) ^ ((lane >> 2) & 6)) + 8 * (p & 1));
      vsetu<134>((fr >> 3) * 2048 + (fq & 1) * 512 + (fq >> 1) * 256 + (fr & 7) * 16);
      vsetu<81>(tid == 0 ? 0u : 0x80000000u);
      vsetu<84>(lane * 16); vsetu<85>(lane * 16 + 65536); vsetu<86>(wid * 8192 + lane * 16); vsetu<87>(wid * 8192 + lane * 16 + 4096);
      vsetu<88>(65536 + wid * 256 + fr * 16); vsetu<89>(16 * wid + fr - 4 * fq);
      asm volatile("s_mov_b64 s[92:93], s[88:89]\n\ts_mov_b64 s[94:95], s[90:91]\n\ts_mov_b32 s75, 0x60000\n\t"
                   "s_mov_b32 s76, 0x40100\n\ts_mov_b32 s77, 0x60100\n\ts_mov_b32 s82, 2");
      asm volatile("s_add_u32 s86, s80, %0\n\ts_addc_u32 s87, s81, 0\n\ts_add_u32 s98, s98, %1\n\ts_addc_u32 s99, s99, 0"
                   ::"s"(mt * 256 * DC * 2), "s"(h * 512) : "scc");
      sset<66>(192 * lda); sset<69>(384 * lda); sset<70>(448 * lda);
      sset<78>(lA + wid * 1024); sset<79>(lW + wid * 1024);
    }
    GAP();
    if (doP) {
      asm volatile("s_waitcnt vmcnt(11)" ::: "memory");
#pragma unroll
      for (int q = 0; q < 7; ++q) asm volatile("" : "+v"(pr.kk[q]));
#pragma unroll
      for (int q = 0; q < 4; ++q) asm volatile("" : "+v"(pr.vv[q]));
      static_for([&](auto Ic) __attribute__((always_inline)) {
        constexpr int I = Ic;
        if constexpr (I < 2) g_pro(g0 + 2 + I, mA, mW, lda);
        if constexpr (I == 2) g_pro2(mA, mW);
        GAP();
        prep_math<I>(pr, tid, lds + 131072);
        GAP();
      }, make_iseq<4>{});
    } else { g_pro(g0 + 2, mA, mW, lda); g_pro(g0 + 3, mA, mW, lda); g_pro2(mA, mW); }
    GAP();
    g.aA[0] = vxor<128, 0>(); g.aA[1] = vxor<128, 64>();
    static_for([&](auto I) { g.aB[I >> 3][I & 7] = vxor<129 + (I >> 3), 32 * (I & 7)>(); }, make_iseq<16>{});
    c.o0 = vxor<131, 0>(); c.o1 = vxor<132, 0>(); c.tb = vxor<133, 0>(); c.vs = vxor<134, 0>();
    asm volatile("v_add_u32 v91, 0x100000, v93\n\ts_waitcnt vmcnt(8) lgkmcnt(0)\n\ts_barrier" ::: "memory");
    if (isY) g_read0<0>(g, make_iseq<16>{});
    else g_read0<1>(g, make_iseq<16>{});
    GAP();
    asm volatile("v_readfirstlane_b32 s48, v120\n\tv_readfirstlane_b32 s49, v121\n\tv_readlane_b32 s80, v78, %0\n\tv_readlane_b32 s81, v78, %1\n\t"
                 "s_mov_b32 s50, 0x4c957f2d\n\ts_mul_i32 s84, s48, s50\n\ts_mul_hi_u32 s85, s48, s50\n\ts_mul_i32 s49, s49, s50\n\t"
                 "s_mul_i32 s48, s48, 0x5851f42d\n\ts_add_u32 s85, s85, s49\n\ts_add_u32 s85, s85, s48\n\ts_add_u32 s84, s84, 0xf767814f\n\t"
                 "s_addc_u32 s85, s85, 0x14057b7e\n\ts_lshr_b64 s[80:81], s[80:81], 12\n\tv_mov_b32 v82, s84\n\tv_mov_b32 v83, s85"
                 ::"i"((int)T_TAB), "i"(T_TAB + 1) : "scc");
    if (isY) asm volatile("s_mov_b64 s[80:81], 0x6543");
    if (doP) prep_store(tid, pj, ptb, (bf16*)sptr<88>() + O_X / 2, lds + 131072);
    asm volatile("s_waitcnt lgkmcnt(0)" ::: "memory");
    GAP();
    if (isY) {
      g_tile<false, 0, 1>(g);
      g_loop<false, true>(g, [&]() __attribute__((always_inline)) {
        tload<80, T_TAB>(); tload<81, T_TAB + 1>();
        fl_set<T_FPREP>();
        asm volatile("s_mov_b64 s[60:61], s[98:99]\n\ts_mov_b32 s62, %0" ::"i"((DC - 1) * LDW * 2 + 512));
      }, [&]() __attribute__((always_inline)) {
        asm volatile("s_mov_b64 s[56:57], s[86:87]\n\ts_mov_b32 s58, %0\n\tv_mov_b32 v92, v94" ::"i"(256 * DC * 2));
        static_for([&](auto J) { asm volatile("s_mov_b32 s%c0, %1" ::"i"(64 + J), "i"((J + 1) * 64 * DC)); }, make_iseq<7>{});
      });
      tload<101, T_OQ>(); sset<83>(__float_as_uint(CS));
      asm volatile("v_mov_b32 v112, v84\n\tv_add_u32 v113, 8192, v84");
      GAP();
      g_tile<false, 0, 2>(g);
      KST(0, 3);
    }
    auto kvtile = [&](auto TRc) __attribute__((always_inline)) {
      constexpr bool TR = TRc;
      if (isY) g_tile<TR, 1, 4>(g);
      else g_tile<TR, 1, 1>(g);
      asm volatile("s_mov_b32 s83, s78");
      g_loop<TR, false>(g, [&]() __attribute__((always_inline)) {
        asm volatile("v_readlane_b32 s49, v78, %1\n\tv_readlane_b32 s48, v78, %0\n\tv_and_b32 v122, 0xffffff00, v82\n\tv_mov_b32 v123, v83\n\t"
                     "s_and_b32 s49, s49, 0xf000000\n\ts_cselect_b32 s49, -2.0, 0\n\tv_or_b32 v121, s49, v81\n\ts_nop 0\n\t"
                     "buffer_store_dwordx2 v[122:123], v121, s[88:91], s48 offen sc1" ::"i"((int)T_CNT), "i"(T_IT + T_ITN + 16) : "memory", "scc");
        if (isY) fl_set<T_FQ>();
        else {
          int pv = pu; asm volatile("" : "+s"(pv));
          if (doA) post_dma(pj, pv, arg_late<const bf16, 12>(), 131072 + wid * 8192);
          else {
            const char* tp = arg_late<const char, 0>() + 1024 * DC * 2 + pv * 32768 + __float_as_uint(vget<84>()) * 8;
            asm volatile("s_mov_b32 m0, %0\n\ts_nop 0" ::"i"(m0d));
#pragma unroll
            for (int i = 0; i < 4; ++i) asm volatile("global_load_lds_dword %0, off" ::"v"(tp + i * 8192) : "memory");
          }
        }
      }, [&]() __attribute__((always_inline)) {
        asm volatile("s_mov_b32 s79, 0x28000\n\ts_mov_b32 s83, s79");
        fl_poll<T_NPRE>();
        if (!isY) {
          int pv = pu; asm volatile("" : "+s"(pv));
          if (doA) {
            const char* tp = arg_late<const char, 9>() + (size_t)(pj * 64 + pv) * (SEQ * 2) + __float_as_uint(vget<84>()) * 4;
            asm volatile("s_mov_b32 m0, %1\n\ts_nop 0\n\tglobal_load_lds_dword %0, off" ::"v"(tp), "i"(m0d) : "memory");
          } else {
            CODE_TOUCH("at_code_begin", "pf_code_end", (pv >> 5) * 8 + (pv & 3) * 2, 16u, 64, m0d);
            CODE_TOUCH("at_code_begin", "pf_code_end", (pv >> 5) * 8 + (pv & 3) * 2 + 1, 16u, 64, m0d);
          }
        }
      });
      tload<101, T_OG>(); tload<96, T_Q0>(); tload<98, T_K0>();
      fl_check();
      tload<50, T_NST>(); tload<51, T_NST + 1>();
      asm volatile("v_mov_b32 v112, v84\n\tv_add_u32 v113, %0, v84\n\tv_mov_b32 v90, v87\n\tv_mov_b32 v91, v87\n\tv_mov_b32 v92, v87\n\tv_mov_b32 v93, v87\n\t"
                   "v_mov_b32 v94, v86\n\tv_mov_b32 v95, v86\n\tv_mov_b32 v126, v86\n\tv_mov_b32 v127, v86\n\ts_mov_b32 s99, 0x8000\n\t" FL_ARM("s[50:51]")
                   ::"i"(TR ? 4096 : 8192));
      if (doA) {
        const int d = pj * 64 + pu;
        const bf16* dp = arg_late<const bf16, 10>() + d;
        asm volatile("global_load_ushort v123, %0, off" ::"v"(dp) : "memory");
        srd_set<56>(arg_late<const bf16, 9>() + (size_t)d * SEQ, SEQ * 2);
        srd_set<60>(arg_late<const char, 5>() + O_X + d * SEQ * 2, SEQ * 2);
        sset<78>(65536 + wid * 15488);
        asm volatile("s_movk_i32 s64, 992\n\tv_subrev_u32 v122, 32, v84");
        GAP();
        g_tile<TR, 0, 3>(g);
      } else {
        sset<79>(isY ? wid * 8192 : 0x30000);
        if (tget<T_IT + T_ITN + 16>() & 16) asm volatile("s_add_u32 s98, s98, 0x8000\n\ts_mov_b32 s97, 0xffff8000" ::: "scc");
        else asm volatile("s_mov_b32 s97, 0x8000");
        GAP();
        g_tile<TR, 0, 5>(g);
      }
    };
    if (kv) kvtile(std::true_type{});
    else kvtile(std::false_type{});
  }
  KST(0, 4);
  asm volatile("s_waitcnt vmcnt(16)" ::: "memory");
  const unsigned md0 = tget<T_IT + 16>(), md1 = tget<T_IT + T_ITN + 16>();
  sset<79>(wid * 8192 + (md0 & 16) * 2048);
  if (doA) {
    const float Dd = bfl(__float_as_uint(vget<123>()));
    KST(0, 5);
    conv_wave<T_FG>(O_Z + pj * 64 * SEQ * 2 + ((pu & 7) * 8 + (pu >> 3)) * 64, pu >> 3, Dd, lds + 65536 + wid * 15488);
    const char* tp = arg_late<const char, 11>() + (size_t)h * (HD * DA * 2) + ((pj * 4 + wid) * 64 + lane) * 128;
    asm volatile("s_mov_b32 m0, %1\n\ts_nop 0\n\tglobal_load_lds_dword %0, off\n\ts_waitcnt vmcnt(17)" ::"v"(tp), "i"(m0d) : "memory");
    KST(0, 6);
  } else if (!isY) {
    static_for([&](auto I) { d_store<16 + I>(); }, make_iseq<16>{});
    tload<98, T_K0>();
    asm volatile("s_waitcnt vmcnt(0)\n\ts_barrier" ::: "memory");
    fl_set<T_FG>();
    KST(0, 5);
    fl_check();
    CODE_TOUCH("att_begin", "pf_code_end", hsel * 4 + wid, 8u, 64, m0d);
    dma4k<0>(); dma4k<1>(); dma4k<2>(); dma4k<3>();
    asm volatile("s_waitcnt vmcnt(0)" ::: "memory");
    KST(0, 6);
  } else {
    tload<76, T_PART>();
    fl_check();
    asm volatile("buffer_load_dwordx2 v[122:123], v88, s[88:91], s76 offen offset:1024 sc1" ::: "memory");
  }
  sset<78>(65536 + wid * 8192); sset<79>(98304 + wid * 8192);
  { const bf16* wp = arg_late<const bf16, 11>() + ((md1 >> 24) & 15) * HD * DA; asm volatile("s_mov_b64 s[80:81], %0" ::"s"(wp)); }
  asm volatile("att_begin:");
  ev_code(c);
  asm volatile("s_waitcnt lgkmcnt(0)\n\ts_barrier" ::: "memory");
#pragma unroll 1
  for (int it = md0 ? 0 : 1; it < 2; ++it) {
    const unsigned mode = it ? md1 : md0;
    const bool odd = mode & 16;
    if (odd) kreads<1>(make_iseq<8>{});
    else kreads<0>(make_iseq<8>{});
    const unsigned oP = tget<T_PART>();
    if (it == 0) {
      static_for([&](auto I) { tload<57 + I, T_IT + I>(); }, make_iseq<21>{});
      tload<S_T, T_IT + 21>(); tload<S_EV, T_IT + 22>();
    } else {
      static_for([&](auto I) { tload<57 + I, T_IT + T_ITN + I>(); }, make_iseq<21>{});
      tload<S_T, T_IT + T_ITN + 21>(); tload<S_EV, T_IT + T_ITN + 22>();
    }
    asm volatile("s_add_u32 s98, s97, 0x8000\n\ts_mov_b32 s56, -1\n\tv_mov_b64 v[114:115], 0\n\tv_mov_b64 v[118:119], 0\n\t"
                 "v_mov_b32 v120, 0x45800000" ::: "scc");
    KST(0, 8 + 3 * (it && !isY));
    GAP();
#define STA(k)
#define STI()
#define LOOP_ENDS "s_bfe_u32 s76, s73, 0x80008"
    STA(0);
    if (odd) {
      if (isY) phaseA<1, 2>(c);
      else phaseA<1, 1>(c);
      STA(1);
      STA(2);
      phaseB0<1>(mode, isY);
      STA(3);
      asm volatile(LOOP_ENDS "\n\ts_branch att_mid" ::: "scc");
    } else {
      if (isY) phaseA<0, 2>(c);
      else phaseA<0, 1>(c);
      STA(1);
      if (__builtin_expect(mode & 4, 0)) EV_CALL("ev_turn");
      STA(2);
      phaseB0<0>(mode, isY);
      if (__builtin_expect(mode & 4, 0)) { EV_CALL("ev_post"); EV_CALL("ev_mask"); acc_zero(); asm volatile("s_branch att_done"); }
      STA(3);
    }
    GAP();
    asm volatile(LOOP_ENDS "\natt_loop:" ::: "scc");
    GAP();
    phaseA<1, 0>(c);
    STI();
    if (__builtin_expect(c.rare != 0, 0)) sm_redo<1>();
    phaseB<1>(c);
    asm volatile("s_cbranch_scc1 att_done\natt_mid:");
    phaseA<0, 0>(c);
    STI();
    if (__builtin_expect(c.rare != 0, 0)) sm_redo<0>();
    phaseB<0>(c);
    asm volatile("s_cbranch_scc0 att_loop\natt_done:");
    GAP();
    STA(4);
    if (it == 0) phaseA<0, 3>(c);
    else {
      sset<76>(oP);
      GAP();
      if (mode & 2) phaseAL<true>(c, make_iseq<32>{});
      else phaseAL<false>(c, make_iseq<32>{});
      if (__builtin_expect(redo(), 0)) sm_redo<1, false>();
      if (mode & 2) phaseBL<true>(c, make_iseq<64>{});
      else phaseBL<false>(c, make_iseq<64>{});
    }
    if (__builtin_expect(redo(), 0)) {
      if (it) static_for([&](auto I) { vset<R_S + 32 + (I >> 2) * 8 + (I & 3)>(-INFINITY); }, make_iseq<16>{});
      asm volatile("s_nop 7");
      sm_redo<0, false>();
    }
    STA(5);
    KST(0, 9 + 3 * (it && !isY));
    float l[2];
    asm volatile("v_add_f32 %0, v%c2, v%c3\n\tv_add_f32 %1, v%c4, v%c5" : "=v"(l[0]), "=v"(l[1]) : "i"(R_RS), "i"(R_TS), "i"(R_RS + 1), "i"(R_TS + 1));
    const float cm[2] = {vget<R_M>(), vget<R_M + 1>()};
    l[0] = row_reduce<false>(l[0]); l[1] = row_reduce<false>(l[1]);
    EP p;
    if (it == 0) {
      const f32x4 ml = {cm[0], l[0], cm[1], l[1]};
      asm volatile("buffer_store_dwordx4 %0, v88, s[88:91], %1 offen sc1\n\ts_mov_b32 s101, %2" ::"v"(ml), "s"(oP), "s"(oP + wid * 16384) : "memory");
      phaseF(c, p, make_iseq<74>{});
      asm volatile(FL_POLL "\n\ts_waitcnt vmcnt(17)" ::: "memory");
    } else {
      if (!(mode & 2)) {
        p.fa[0] = __builtin_amdgcn_rcpf(l[0]); p.fa[1] = __builtin_amdgcn_rcpf(l[1]);
        phaseFL<1>(p);
      } else {
        const f32x4 ml = {vget<122>(), vget<123>(), vget<124>(), vget<125>()};
        if (__builtin_expect(__builtin_amdgcn_ballot_w64(ml[0] != cm[0] || ml[2] != cm[1]) == 0, 1)) {
          p.fa[0] = __builtin_amdgcn_rcpf(l[0] + ml[1]); p.fa[1] = __builtin_amdgcn_rcpf(l[1] + ml[3]);
          phaseFL<3>(p);
        } else {
#pragma unroll
          for (int e = 0; e < 2; ++e) {
            const float mm = fmaxf(cm[e], ml[2 * e]);
            const float wa = __builtin_amdgcn_exp2f(cm[e] - mm), wb = __builtin_amdgcn_exp2f(ml[2 * e] - mm);
            const float inv = __builtin_amdgcn_rcpf(l[e] * wa + ml[2 * e + 1] * wb);
            p.fa[e] = wa * inv; p.fb[e] = wb * inv;
          }
          phaseFL<2>(p);
        }
      }
    }
    KST(0, 10 + 3 * (it && !isY));
  }
  asm volatile(TICKET "\n\tv_xor_b32 v%c0, s100, v%c0\np_loop:" ::"i"(R_TB) : "memory");
  GAP();
  p_half<0, true>(make_iseq<32>{});
  asm volatile("s_cbranch_scc0 p_mid");
  p_half<1, true>(make_iseq<32>{});
  asm volatile("s_branch p_loop\np_loop2:");
  p_half<0, false>(make_iseq<32>{});
  asm volatile("p_mid:");
  p_half<1, false>(make_iseq<32>{});
  asm volatile("s_cbranch_scc1 p_loop2\n\ts_nop 5");
  GAP();
  p_cvt<1, 0>(); p_cvt<1, 1>(); p_cvt<1, 2>(); p_cvt<1, 3>();
  p_store<1, 1>();
  GAP();
  KST(0, 27);
  {
    constexpr unsigned rdy = 98304;
    int lz = threadIdx.x & 63; asm volatile("" : "+v"(lz));
    const int fr = lz & 15, fq = lz >> 4, qbF = md1 >> 28;
    if (wid == 0) {
#pragma unroll 1
      for (int q = 0; q < 0x100000; ++q) {
        unsigned lo, hi, tl, th;
        asm volatile("v_readfirstlane_b32 %0, v240\n\tv_readfirstlane_b32 %1, v241\n\ts_mov_b32 %2, s84\n\ts_mov_b32 %3, s85" : "=s"(lo), "=s"(hi), "=s"(tl), "=s"(th));
        if (__builtin_expect((((lo ^ tl) >> 8) | (hi ^ th)) == 0, 1)) break;
        asm volatile("s_sleep 4\n\tv_mov_b64 v[240:241], 1\n\t" TICKET "\n\ts_waitcnt vmcnt(0)" ::: "memory");
      }
      asm volatile("v_mov_b32 v241, 0\n\ts_mov_b64 exec, 1\n\tds_write_b64 %0, v[240:241]\n\ts_mov_b64 exec, -1" ::"v"(rdy) : "memory");
    }
    vsetu<79>(fo(1024) + qbF * 4352 + fr * 8);
    {
      const int jj = 4 * (fr >> 3) + (fr & 3);
      const unsigned one = ((fq ^ (fr >> 2)) & 1) ? 0u : (jj & 1) ? 0x3f800000u : 0x3f80u;
      static_for([&](auto I) { vsetu<96 + I>((jj >> 1) == I ? one : 0u); }, make_iseq<4>{});
    }
    unsigned vo = (fq >> 1) * 1024 + (fq & 1) * 256 + fr * 16;
    bf16* out = arg_late<bf16, 13>();
    __amdgpu_buffer_rsrc_t so_ = __builtin_amdgcn_make_buffer_rsrc((void*)out, (short)0, SEQ * OUTW * 2, 0x00020000);
    unsigned vw = ((qbF * 128 + (fr & 7)) * OUTW + DA + (fr >> 3) * 32 + fq * 8) * 2;
    asm volatile("" : "+s"(so_));
    asm volatile("s_waitcnt vmcnt(0) lgkmcnt(0)\n\ts_barrier" : "+v"(vo), "+v"(vw) :: "memory");
    fl_set<T_FS>();
    unsigned tk;
    asm volatile("ds_read_b32 %0, %1\n\ts_waitcnt lgkmcnt(0)" : "=v"(tk) : "v"(rdy) : "memory");
    tk = __builtin_amdgcn_readfirstlane(tk) & 255;
    KST(0, 30);
    if (doA) post_math(pj, pu, out, lds + 131072 + wid * 8192);
#define N3 0
#define N2 12
    constexpr int N1 = 28 - 3 * N3 - 2 * N2;
    static_assert(N1 >= 0 && N3 + N2 + N1 <= 16);
    const int nu = tk < N3 ? 3 : tk < N3 + N2 ? 2 : tk < N3 + N2 + N1 ? 1 : 0;
    const int u0 = 4 * (tk < N3 ? 3 * tk : tk < N3 + N2 ? N3 + 2 * tk : 2 * N3 + N2 + tk) + wid * nu;
    KST(0, 28);
    if (nu) {
      const unsigned so0 = O_S + qbF * 0x200000 + (16 + u0) * 2048, so[3] = {so0, so0, so0};
      unsigned pend = 0x5555u, cnt = 0x400000;
      while (true) {
        unsigned nw;
        if (wid == 3) {
          u64 m;
          const u64 ex = pend | (pend << 1);
          asm volatile(FL_ARM("%1") "\n\t" FL_POLL "\n\ts_waitcnt vmcnt(0)\n\tv_cmp_eq_u64 %0, s[84:85], v[124:125]" : "=s"(m) : "s"(ex) : "memory");
          nw = (unsigned)m & ((unsigned)m >> 1) & pend;
          if (nw) asm volatile("ds_write_b32 %0, %1 offset:4" ::"v"(rdy), "v"(~pend | nw) : "memory");
        } else {
          unsigned t;
          asm volatile("ds_read_b32 %0, %1 offset:4\n\ts_waitcnt lgkmcnt(0)" : "=v"(t) : "v"(rdy) : "memory");
          nw = __builtin_amdgcn_readfirstlane(t) & pend;
        }
        if (--cnt == 0) nw = pend;
        if (nw == 0) {
          if (wid == 3) asm volatile("s_sleep 4");
          else asm volatile("s_sleep 1");
          continue;
        }
        asm volatile("s_nop 4");
        static_for([&](auto S) {
          if ((nw >> (2 * S)) & 1) {

            r_load<0, 2 * S>(vo, so[0]); r_load<0, 2 * S + 1>(vo, so[0]);
            if (nu > 1) { r_load<1, 2 * S>(vo, so[1]); r_load<1, 2 * S + 1>(vo, so[1]); }
            if (nu > 2) { r_load<2, 2 * S>(vo, so[2]); r_load<2, 2 * S + 1>(vo, so[2]); }
          }
        }, make_iseq<8>{});
        pend &= ~nw;
        if (pend == 0) break;
      }
      auto oo = [&](int U) { return ((((U >> 1) & 1) * 64 + ((U >> 2) & 3) * 16 + (U & 1) * 8) * OUTW + (U >> 4) * 64) * 2; };
      unsigned o0 = oo(u0), o1 = oo(u0 + 1), o2 = oo(u0 + 2);
      asm volatile("s_waitcnt vmcnt(0)" : "+s"(o0), "+s"(o1), "+s"(o2), "+s"(so_) :: "memory");
      KST(0, 16);
      if (nu > 2) r_sums<2>();
      if (nu > 1) r_sums<1>();
      r_sums<0>();
      if (nu > 2) r_fin<2>(vw, so_, o2);
      if (nu > 1) r_fin<1>(vw, so_, o1);
      asm volatile("s_nop 7");
      r_fin<0>(vw, so_, o0);
      if (qbF == 15 && tk == 0) asm volatile(FL_SET("%0") ::"s"(fo(6144)) : "memory");
    }
    KST(0, 29);
  }
  asm volatile("pf_code_end:");
  KST(0, 1);
}

extern "C" void kernel_launch(void* const* d_in, const int* in_sizes, int n_in, void* d_out, int out_size, void* d_ws,
                              size_t ws_size, hipStream_t stream) {
  const bf16* Aq = (const bf16*)d_in[0];
  const bf16* Ak = (const bf16*)d_in[1];
  const bf16* Av = (const bf16*)d_in[2];
  const bf16* Ash = (const bf16*)d_in[3];
  const bf16* Assmk = (const bf16*)d_in[4];
  const bf16* AssmD = (const bf16*)d_in[5];
  const bf16* Bx = (const bf16*)d_in[6];
  const bf16* Bckv = (const bf16*)d_in[7];
  const bf16* Bwq = (const bf16*)d_in[8];
  const bf16* Bwuk = (const bf16*)d_in[9];
  const bf16* Bwuv = (const bf16*)d_in[10];
  const bf16* Bwo = (const bf16*)d_in[11];
  bf16* out = (bf16*)d_out;
  hipFuncSetAttribute((const void*)fused_kernel, hipFuncAttributeMaxDynamicSharedMemorySize, 163840);
  fused_kernel<<<256, 256, 163840, stream>>>(Bckv, Bwuk, Bwuv, Bx, Bwq, (bf16*)d_ws, Ak, Av, Ash, Assmk, AssmD, Bwo, Aq, out);
}
```
